```python
import torch

def _softmax(x, axis=-1):
    return torch.softmax(x, dim=axis)

def _gelu(x):
    return torch.nn.functional.gelu(x, approximate='tanh')

def _silu(x):
    return torch.nn.functional.silu(x)

def _sigmoid(x):
    return torch.sigmoid(x)

def _layernorm(x, axis=-1, eps=1e-05):
    m = x.mean(dim=axis, keepdim=True)
    v = x.var(dim=axis, keepdim=True, unbiased=False)
    return (x - m) / torch.sqrt(v + eps)

def _scatter_add(tgt, idx, src, axis=0):
    return tgt.clone().index_add_(axis, idx, src)

def _segment_sum(src, seg, n):
    out = torch.zeros((n,) + src.shape[1:], dtype=src.dtype)
    return out.index_add_(0, seg, src)

def _pad(x, pw):
    flat = tuple((v for pair in reversed(pw) for v in pair))
    return torch.nn.functional.pad(x, flat)
kernel_name = 'interleave_layers__memory_aug_recurrent_mem_tr__softmax_full_mha__prompt'

def setup_inputs():

    _w = lambda a: (lambda t: t.to(torch.bfloat16) if t.is_floating_point() else t)(torch.as_tensor(a))

    def _A_setup():
        B, S, Dm = (1, 2048, 640)
        H, Dh = (32, 192)
        M, seg = (8, 32)
        seg = min(seg, S)
        S = S // seg * seg
        return {'x': _w((lambda *s: torch.randn(*s, dtype=torch.bfloat16) * 0.05)(B, S, Dm)), 'mem0': _w((lambda *s: torch.randn(*s, dtype=torch.bfloat16) * 0.8)(B, M, Dm)), 'Wq': _w((lambda *s: torch.randn(*s, dtype=torch.bfloat16) * 0.8)(Dm, H, Dh) * Dm ** (-0.5)), 'Wk': _w((lambda *s: torch.randn(*s, dtype=torch.bfloat16) * 0.8)(Dm, H, Dh) * Dm ** (-0.5)), 'Wv': _w((lambda *s: torch.randn(*s, dtype=torch.bfloat16) * 0.8)(Dm, H, Dh) * Dm ** (-0.5)), 'Wo': _w((lambda *s: torch.randn(*s, dtype=torch.bfloat16) * 0.8)(H, Dh, Dm) * (H * Dh) ** (-0.5)), 'seg_len': seg}
    _A_in = _A_setup()

    def _B_setup():
        B, S, D = (1, 2048, 640)
        return {'x': _w((lambda *s: torch.randn(*s, dtype=torch.bfloat16) * 0.05)(B, S, D)), 'wqkv': _w((lambda *s: torch.randn(*s, dtype=torch.bfloat16) * 0.05)(D, 3 * D)), 'wo': _w((lambda *s: torch.randn(*s, dtype=torch.bfloat16) * 0.05)(D, D))}
    _B_in = _B_setup()
    return {'A_x': _A_in['x'], 'A_mem0': _A_in['mem0'], 'A_Wq': _A_in['Wq'], 'A_Wk': _A_in['Wk'], 'A_Wv': _A_in['Wv'], 'A_Wo': _A_in['Wo'], 'A_seg_len': _A_in['seg_len'], 'B_x': _B_in['x'], 'B_wqkv': _B_in['wqkv'], 'B_wo': _B_in['wo']}

def reference(A_x, A_mem0, A_Wq, A_Wk, A_Wv, A_Wo, A_seg_len, B_x, B_wqkv, B_wo):

    def _A_ref(x, mem0, Wq, Wk, Wv, Wo, seg_len):
        B, S, Dm = x.shape
        H, Dh = (Wq.shape[1], Wq.shape[2])
        M = mem0.shape[1]
        scale = Dh ** (-0.5)
        n_seg = S // seg_len
        L = M + seg_len
        idx = torch.arange(L)
        mask = (idx[:, None] < M) | (idx[:, None] >= idx[None, :])
        mem = mem0
        outs = []
        for s in range(n_seg):
            xs = x[:, s * seg_len:(s + 1) * seg_len, :].clone()
            h = torch.concatenate([mem, xs], axis=1)
            q = torch.einsum('btd,dhe->bhte', h, Wq)
            k = torch.einsum('btd,dhe->bhte', h, Wk)
            v = torch.einsum('btd,dhe->bhte', h, Wv)
            scores = torch.einsum('bhqe,bhke->bhqk', q, k) * scale
            scores = torch.where(mask[None, None], scores, -1000000000.0)
            p = _softmax(scores, axis=-1)
            o = torch.einsum('bhqk,bhke->bhqe', p, v)
            o = torch.einsum('bhte,hed->btd', o, Wo)
            mem = o[:, :M, :].clone()
            outs.append(o[:, M:, :])
        return torch.concatenate(outs, axis=1)

    def _B_ref(x, wqkv, wo):
        B, S, D = x.shape
        H = 32
        Dh = D // H
        qkv = (lambda x, *d: x.permute(*d))((x @ wqkv).reshape(B, S, 3, H, Dh), 2, 0, 3, 1, 4)
        q, k, v = (qkv[0], qkv[1], qkv[2])
        scale = Dh ** (-0.5)
        scores = torch.einsum('bhqd,bhkd->bhqk', q, k) * scale
        p = _softmax(scores, axis=-1)
        o = torch.einsum('bhqk,bhkd->bhqd', p, v)
        return (lambda x, *d: x.permute(*d))(o, 0, 2, 1, 3).reshape(B, S, D) @ wo
    x = A_x
    x = x + _A_ref(_layernorm(x, axis=-1), A_mem0, A_Wq, A_Wk, A_Wv, A_Wo, A_seg_len)
    x = x + _B_ref(_layernorm(x, axis=-1), B_wqkv, B_wo)
    x = x + _A_ref(_layernorm(x, axis=-1), A_mem0, A_Wq, A_Wk, A_Wv, A_Wo, A_seg_len)
    x = x + _B_ref(_layernorm(x, axis=-1), B_wqkv, B_wo)
    return x
```

```cpp
#include <hip/hip_runtime.h>
#include <hip/hip_bf16.h>
#include <stdint.h>
#include <type_traits>

typedef unsigned short u16;
typedef unsigned long long u64;
typedef __attribute__((ext_vector_type(8))) short bf16x8;
typedef __attribute__((ext_vector_type(16))) short bf16x16;
typedef __attribute__((ext_vector_type(4))) float f32x4;
typedef __attribute__((ext_vector_type(2))) float f32x2;
typedef __attribute__((ext_vector_type(16))) float f32x16;
typedef __attribute__((ext_vector_type(4))) unsigned u32x4;
typedef __attribute__((ext_vector_type(2))) unsigned u32x2;
#define GAS __attribute__((address_space(1)))
#define LAS __attribute__((address_space(3)))
typedef GAS unsigned long long gu64;
#define BAR() do { __builtin_amdgcn_s_waitcnt(0xC07F); asm volatile("s_waitcnt lgkmcnt(0)\n\ts_barrier" ::: "memory"); } while (0)

constexpr int S_ = 2048, DM = 640;
constexpr int NH = 32, DH = 192, HD = NH * DH;
constexpr int MEM = 8, SEG = 32, NSEG = S_ / SEG;
constexpr int BH = 32, BD = 20;

constexpr size_t al256(size_t x) { return (x + 255) & ~(size_t)255; }
constexpr int LAG3 = 1, LAG4 = 3, LAG5 = 6;
#define SR_ 8
constexpr int SR = SR_;
constexpr unsigned SCT_STEP = 131072, OFF_SCM = SR * SCT_STEP, SCM_STEP = 32768;
constexpr int ACC_NB = 192;
constexpr size_t ACC_STEP = 65536;
constexpr unsigned ACC_TS = ACC_NB * 256;
constexpr size_t ACC_LAYER = SR * ACC_STEP;
constexpr size_t OFF_ACC = 3 * 524288;
static_assert(SR == 8, "regions");
__device__ __forceinline__ unsigned ts_piece(int i) { return ACC_TS + (unsigned)(i < 64 ? i * 256 : 128); }
constexpr size_t OFF_LNX = OFF_SCM + SR * SCM_STEP;
constexpr size_t OFF_RDY = OFF_LNX + 65536;
constexpr size_t PG = 2097152;
#define RING_ 4
constexpr int RING = RING_;
constexpr size_t TS_SLOT = (size_t)NH * 8 * 2560, PT_SLOT = (size_t)NH * 2560, OP_SLOT = (size_t)256 * 2560;
__device__ __forceinline__ unsigned ts_tok(int h, int j) { return (unsigned)((h & 15) * 32768 + (j >> 2) * 16384 + (j & 3) * 2048); }
__device__ __forceinline__ unsigned ts_mem(int h, int j) { return (unsigned)(524288 + ((h & 15) >> 1) * 16384 + (h & 1) * 4096 + j * 512); }
__device__ __forceinline__ unsigned pt_tok(int h) { return (unsigned)(((h & 15) >> 2) * 16384 + (h & 3) * 2048); }
__device__ __forceinline__ unsigned pt_mem(int h) { return (unsigned)(65536 + (h & 15) * 512); }
constexpr size_t OFF_X = 2 * PG + TS_SLOT + 3 * PT_SLOT, X_BYTES = 7 * OP_SLOT + 3 * PT_SLOT;
constexpr size_t OFF_OP = OFF_X + TS_SLOT + 3 * PT_SLOT;
constexpr size_t COMM_BYTES = OFF_X + X_BYTES + RING * OP_SLOT;
__device__ __forceinline__ unsigned near_half(unsigned ck, unsigned o) { return o + ((ck >> (o >= 2 * PG ? 8 : 4)) & 8192u); }
__device__ __forceinline__ unsigned ts_slot(unsigned ck, int sl) {
  return near_half(ck, sl == NSEG - 1 ? (unsigned)OFF_X : ((sl & 3) == 3 ? (unsigned)(2 * PG) : (unsigned)PG + (unsigned)(sl & 3) * (unsigned)TS_SLOT));
}
__device__ __forceinline__ unsigned pt_slot(unsigned ck, int sl) {
  return near_half(ck, sl >= NSEG - 3 ? (unsigned)(OFF_X + TS_SLOT) + (unsigned)(sl - (NSEG - 3)) * (unsigned)PT_SLOT
                                      : ((sl & 3) == 0 ? (unsigned)(PG + 3 * TS_SLOT) : (unsigned)(2 * PG + TS_SLOT - PT_SLOT) + (unsigned)(sl & 3) * (unsigned)PT_SLOT));
}
__device__ __forceinline__ unsigned op_slot(int sl) { return (unsigned)OFF_OP + (unsigned)(sl >= NSEG - 6 ? sl - (NSEG - 6) : 6 + sl % RING) * (unsigned)OP_SLOT; }
static_assert(RING == 4 && OFF_OP <= 3 * PG && OFF_RDY + 2048 <= OFF_ACC && OFF_ACC + ACC_LAYER == PG && TS_SLOT % 16384 == 0 && PT_SLOT % 16384 == 0, "pages");
static_assert(TS_SLOT == OP_SLOT && RING >= 4 && NSEG % SR == 0 && SR >= 5 && X_BYTES % 4096 == 0, "slots");
constexpr size_t OFF_WB = al256(COMM_BYTES);
constexpr size_t OFF_BNRM = OFF_WB + (size_t)256 * 100 * 1024;
constexpr size_t OFF_LAT = OFF_BNRM + 1024;
constexpr size_t OFF_XNRM = OFF_LAT + 2048;
constexpr size_t OFF_WQKVBT = OFF_XNRM + al256(S_ * 16);
constexpr size_t OFF_WOBT = OFF_WQKVBT + al256((size_t)3 * DM * DM * 2);
constexpr size_t OFF_XN = OFF_WOBT + al256((size_t)DM * DM * 2);
constexpr int QST = BD;
constexpr size_t OFF_XA = OFF_XN + al256((size_t)S_ * DM * 2);
constexpr size_t OFF_XB = OFF_XA + al256((size_t)S_ * DM * 2);
constexpr size_t OFF_QB = OFF_XB + al256((size_t)S_ * DM * 2);
constexpr int KVT = 2624;
constexpr size_t OFF_KV = OFF_QB + al256((size_t)BH * S_ * QST * 2);
constexpr size_t OFF_OB = OFF_KV + al256((size_t)BH * 64 * KVT);
constexpr size_t OFF_DBG = OFF_OB + al256((size_t)S_ * DM * 2);
constexpr size_t WS_TOTAL = OFF_DBG + (size_t)2 * 64 * 8 * DM * 2;
#define WS_AL 2097152
#define WS_OFF 0
static_assert(WS_TOTAL + WS_AL + ((size_t)WS_OFF << 20) <= ((size_t)256 << 20), "workspace budget");

#if defined(__HIP_DEVICE_COMPILE__)
#pragma clang attribute push(__attribute__((target("no-packed-fp32-ops"))), apply_to = function)
#endif
__device__ __forceinline__ float u2f(unsigned v) { return __builtin_bit_cast(float, v); }
__device__ __forceinline__ unsigned f2u(float v) { return __builtin_bit_cast(unsigned, v); }
__device__ __forceinline__ float bf2f(unsigned v) { return __builtin_bit_cast(float, v << 16); }
__device__ __forceinline__ unsigned f2bf(float f) { __bf16 b = (__bf16)f; return (unsigned)__builtin_bit_cast(u16, b); }
__device__ __forceinline__ float rbf(float f) { return bf2f(f2bf(f)); }
typedef __attribute__((ext_vector_type(2))) __bf16 hbf16x2;
__device__ __forceinline__ unsigned pack2(float lo, float hi) { f32x2 v = {lo, hi}; hbf16x2 b = __builtin_convertvector(v, hbf16x2); return __builtin_bit_cast(unsigned, b); }
__device__ __forceinline__ f32x4 mfma32(bf16x8 a, bf16x8 b, f32x4 c) { return __builtin_amdgcn_mfma_f32_16x16x32_bf16(a, b, c, 0, 0, 0); }
typedef __attribute__((ext_vector_type(8))) __bf16 hbf16x8;
typedef __attribute__((ext_vector_type(16))) __bf16 hbf16x16;
__device__ __forceinline__ f32x4 smfma64(bf16x8 a, bf16x16 b, f32x4 c, int idx) {
  return __builtin_amdgcn_smfmac_f32_16x16x64_bf16(__builtin_bit_cast(hbf16x8, a), __builtin_bit_cast(hbf16x16, b), c, idx, 0, 0);
}
__device__ __forceinline__ bf16x16 cat16(bf16x8 a, bf16x8 b) { return __builtin_shufflevector(a, b, 0, 1, 2, 3, 4, 5, 6, 7, 8, 9, 10, 11, 12, 13, 14, 15); }
__device__ __forceinline__ bf16x8 half8(bf16x16 v, int hi) { return hi ? __builtin_shufflevector(v, v, 8, 9, 10, 11, 12, 13, 14, 15) : __builtin_shufflevector(v, v, 0, 1, 2, 3, 4, 5, 6, 7); }
__device__ __forceinline__ bf16x8 mk8(unsigned a, unsigned b, unsigned c, unsigned d) { u32x4 v = {a, b, c, d}; return __builtin_bit_cast(bf16x8, v); }
__device__ __forceinline__ unsigned dpp_ror8(unsigned v) { return (unsigned)__builtin_amdgcn_mov_dpp((int)v, 0x128, 0xf, 0xf, true); }
__device__ __forceinline__ float fmx(float a, float b) { return __builtin_elementwise_maximum(a, b); }

__device__ __forceinline__ int imax(int a, int b) { return a > b ? a : b; }
__device__ __forceinline__ int imin(int a, int b) { return a < b ? a : b; }
__device__ __forceinline__ unsigned umin(unsigned a, unsigned b) { return a < b ? a : b; }
__device__ __forceinline__ bool wave_all(bool p) { return __builtin_amdgcn_ballot_w64(!p) == 0ull; }
__device__ __forceinline__ float x16mx(float x) { unsigned u = __builtin_bit_cast(unsigned, x); auto r = __builtin_amdgcn_permlane16_swap(u, u, false, false); return fmx(u2f(r[0]), u2f(r[1])); }
__device__ __forceinline__ float x32mx(float x) { unsigned u = __builtin_bit_cast(unsigned, x); auto r = __builtin_amdgcn_permlane32_swap(u, u, false, false); return fmx(u2f(r[0]), u2f(r[1])); }
__device__ __forceinline__ float x16sum(float x) { unsigned u = __builtin_bit_cast(unsigned, x); auto r = __builtin_amdgcn_permlane16_swap(u, u, false, false); return u2f(r[0]) + u2f(r[1]); }
__device__ __forceinline__ float x32sum(float x) { unsigned u = __builtin_bit_cast(unsigned, x); auto r = __builtin_amdgcn_permlane32_swap(u, u, false, false); return u2f(r[0]) + u2f(r[1]); }
template <int CTRL> __device__ __forceinline__ float dppf(float v) { return u2f((unsigned)__builtin_amdgcn_mov_dpp((int)f2u(v), CTRL, 0xf, 0xf, true)); }
#define OWN_PF 1
template <int BYTES> __device__ __forceinline__ void own_code(int tid, unsigned* sink) {
  if (!OWN_PF || __builtin_amdgcn_readfirstlane(tid >> 6) * 4096 >= BYTES) return;
  const char* pc; unsigned keep;
  asm volatile("s_getpc_b64 %0" : "=s"(pc));
  asm volatile("s_mov_b32 %0, m0\n\ts_mov_b32 m0, %3\n\ts_nop 0\n\tglobal_load_lds_dword %1, %2\n\ts_mov_b32 m0, %0"
               : "=&s"(keep) : "v"(tid * 64 < BYTES ? tid * 64 : 0), "s"(pc), "s"((unsigned)(uintptr_t)(LAS unsigned*)sink) : "memory");
}
constexpr int DPP_XOR1 = 0xB1, DPP_XOR2 = 0x4E, DPP_ROR4 = 0x124, DPP_ROR8 = 0x128;
#if defined(__HIP_DEVICE_COMPILE__)
#pragma clang attribute pop
#endif

typedef short v4i16_t __attribute__((ext_vector_type(4)));
__device__ __forceinline__ bf16x8 tr_frag(const char* lo_, const char* hi_) {
  const v4i16_t lo = __builtin_amdgcn_ds_read_tr16_b64_v4i16((LAS v4i16_t*)lo_), hi = __builtin_amdgcn_ds_read_tr16_b64_v4i16((LAS v4i16_t*)hi_);
  return (bf16x8){lo[0], lo[1], lo[2], lo[3], hi[0], hi[1], hi[2], hi[3]};
}
__device__ __forceinline__ int rec_wg(int gp, int m) { return (((gp & 1) * 16 + m) << 3) | (gp >> 1); }
__device__ __forceinline__ float wsum(float v) {
  v += dppf<DPP_XOR1>(v); v += dppf<DPP_XOR2>(v); v += dppf<DPP_ROR4>(v); v += dppf<DPP_ROR8>(v);
  return x32sum(x16sum(v));
}
__device__ __forceinline__ float ln_row(int lane, u32x4 (&xo)[2]) {
  const bool two = lane < 16;
  float v[16];
#pragma unroll
  for (int hf = 0; hf < 2; ++hf)
#pragma unroll
    for (int i = 0; i < 4; ++i) { const unsigned u = xo[hf][i]; v[hf * 8 + 2 * i] = bf2f(u & 0xffff); v[hf * 8 + 2 * i + 1] = bf2f(u >> 16); }
  float s0 = 0.f, s1 = 0.f;
#pragma unroll
  for (int i = 0; i < 8; ++i) { s0 += v[i]; s1 += v[8 + i]; }
  const float mean = wsum(two ? s0 + s1 : s0) * (1.0f / DM);
  s0 = s1 = 0.f;
#pragma unroll
  for (int i = 0; i < 8; ++i) { const float d0 = v[i] - mean, d1 = v[8 + i] - mean; s0 += d0 * d0; s1 += d1 * d1; }
  const float var = rbf(wsum(two ? s0 + s1 : s0) * (1.0f / DM));
  const float mb = rbf(mean);
  const float rden = 1.0f / rbf(sqrtf(rbf(var + 1e-5f)));
  float n2[2] = {0.f, 0.f};
#pragma unroll
  for (int hf = 0; hf < 2; ++hf) {
    float y[8];
#pragma unroll
    for (int i = 0; i < 8; ++i) { y[i] = rbf(rbf(v[hf * 8 + i] - mb) * rden); n2[hf] += y[i] * y[i]; }
    xo[hf] = (u32x4){pack2(y[0], y[1]), pack2(y[2], y[3]), pack2(y[4], y[5]), pack2(y[6], y[7])};
  }
  return wsum(two ? n2[0] + n2[1] : n2[0]);
}

#define PROT(k)
#define WARM_CODE 1
#define PRB_N0 0
#define PRB_N1 3
#define PRB_LOG(i, v)
#define PLD(p) __builtin_nontemporal_load((const u32x4*)(p))
struct ProArgs { const u16* x; const u16* wq; const u16* wk; const u16* wv; const u16* wo; const u16* wqkvb; const u16* wob; char* ws; };
__global__ __launch_bounds__(640, 3) void prologue_kernel(ProArgs a) {
  __shared__ __attribute__((aligned(16))) char img[192 * 320];
  __shared__ __attribute__((aligned(16))) char slots[10 * 6144];
  __shared__ float red[10][160];
  __shared__ unsigned latw[8];
  const int b = blockIdx.x, tid = threadIdx.x, lane = tid & 63, v = __builtin_amdgcn_readfirstlane(tid >> 6), c = lane & 15, g = lane >> 4;
  const int h = (b & 7) * 4 + ((b >> 3) & 3), dh = (b >> 5) & 1, cq = b >> 6, ks = dh * 10 + v;
  char* const slot = slots + v * 6144;
  PROT(0);
  __shared__ unsigned sink[64];
  own_code<9600>(tid, sink);
  const unsigned zo = v < 8 ? (unsigned)(((b >> 6) * 512 + ((b & 7) * 2 + (v >> 2)) * 32 + ((b >> 3) & 7) * 4 + (v & 3)) * 1024) : (unsigned)(((v - 7) * 2048 + (b & 7) * 32 + ((b >> 3) & 7) * 4 + (b >> 6)) * 1024);
  __builtin_amdgcn_raw_buffer_store_b128((u32x4){0u, 0u, 0u, 0u}, __builtin_amdgcn_make_buffer_rsrc(a.ws, 0, (int)(3 * PG), 0x00020000), zo + lane * 16, 0, 16);
  __builtin_amdgcn_sched_barrier(0);
  u32x4 wa[2][6], wb[6], tr[4], xo[2];
  const int xrow = b * 8 + (v & 7);
  xo[0] = *(const u32x4*)(a.x + (size_t)xrow * DM + lane * 8);
  xo[1] = *(const u32x4*)(a.x + (size_t)xrow * DM + 512 + c * 8);
  const int job = imin(b + 256 * (9 - v), 800);
  const int jn = job < 600 ? job / 20 : (job % 200) / 20, jk = job % 20;
  {
    const u16* src = job < 600 ? a.wqkvb + (size_t)(32 * jk) * 3 * DM : a.wob + (size_t)(32 * jk) * DM;
#pragma unroll
    for (int i = 0; i < 4; ++i) tr[i] = *(const u32x4*)(src + (size_t)(i * 8 + (lane >> 3)) * (job < 600 ? 3 * DM : DM) + jn * 64 + (lane & 7) * 8);
  }
#pragma unroll
  for (int i = 0; i < 6; ++i) {
    const int idx = (v * 6 + i) * 64 + lane, row = idx / 20;
    wb[i] = PLD(a.wo + (size_t)(h * DH + row) * DM + cq * 160 + (idx - row * 20) * 8);
  }
#pragma unroll
  for (int i = 0; i < 6; ++i) {
    const int idx = i * 64 + lane, r = idx / 24;
#pragma unroll
    for (int u = 0; u < 2; ++u) wa[u][i] = PLD(a.wv + (size_t)(32 * ks + 8 * (r >> 2) + (r & 3) + 4 * u) * HD + h * DH + (idx - r * 24) * 8);
  }
  unsigned pf[2] = {0u, 0u};
  auto prefetch = [&]() {
#pragma unroll
    for (int i = 0; i < 2; ++i) pf[i] = *(const unsigned*)((i ? a.wq : a.wk) + ((size_t)(b * 10 + v) * 48 + imin(lane, 47)) * 32);
  };
  prefetch();
  __builtin_amdgcn_sched_barrier(0);
  unsigned lat = ~0u;
  auto probe = [&](int n0, int n) {
    const char* p = a.ws + zo;
#pragma unroll 1
    for (int i = n0; i < n0 + n; ++i) {
      u64 t0, t1; unsigned d;
      asm volatile("s_memtime %0\n\ts_waitcnt lgkmcnt(0)\n\ts_load_dword %2, %3, %4\n\ts_waitcnt lgkmcnt(0)\n\ts_memtime %1\n\ts_waitcnt lgkmcnt(0)"
                   : "=&s"(t0), "=&s"(t1), "=&s"(d) : "s"(p), "s"((i & 3) * 256 + (i >> 2) * 128) : "memory");
      PRB_LOG(i, (unsigned)t1 - (unsigned)t0);
      lat = umin(lat, (unsigned)t1 - (unsigned)t0);
    }
  };
  if (PRB_N0) { asm volatile("s_waitcnt vmcnt(26)" ::: "memory"); probe(0, PRB_N0); }
  __builtin_amdgcn_sched_barrier(0);
  const float xn2 = u2f(__builtin_amdgcn_readfirstlane(f2u(ln_row(lane, xo))));
#pragma unroll
  for (int i = 0; i < 4; ++i) *(u32x4*)(slot + (i * 8 + (lane >> 3)) * 144 + (lane & 7) * 16) = tr[i];
  __builtin_amdgcn_fence(__ATOMIC_RELEASE, "wavefront"); __builtin_amdgcn_wave_barrier();
  bf16x8 tf[4];
#pragma unroll
  for (int t = 0; t < 4; ++t) {
    const char* bp = slot + (8 * g + (c >> 2)) * 144 + t * 32 + (c & 3) * 8;
    tf[t] = tr_frag(bp, bp + 4 * 144);
  }
  __builtin_amdgcn_fence(__ATOMIC_RELEASE, "wavefront"); __builtin_amdgcn_wave_barrier();
  __builtin_amdgcn_sched_barrier(0);
#pragma unroll
  for (int i = 0; i < 6; ++i) {
    const int idx = (v * 6 + i) * 64 + lane, row = idx / 20;
    *(u32x4*)(img + row * 320 + (((idx - row * 20) ^ ((row >> 2) & 2)) * 16)) = wb[i];
  }
  bf16x8 A[2][6];
#pragma unroll
  for (int u = 0; u < 2; ++u) {
#pragma unroll
    for (int i = 0; i < 6; ++i) {
      const int idx = i * 64 + lane, r = idx / 24;
      *(u32x4*)(slot + r * 384 + (((idx - r * 24) ^ (r >> 1)) * 16)) = wa[u][i];
    }
    __builtin_amdgcn_fence(__ATOMIC_RELEASE, "wavefront"); __builtin_amdgcn_wave_barrier();
#pragma unroll
    for (int e = 0; e < 6; ++e) A[u][e] = *(const bf16x8*)(slot + c * 384 + (((4 * e + g) ^ (c >> 1)) * 16));
    __builtin_amdgcn_fence(__ATOMIC_RELEASE, "wavefront"); __builtin_amdgcn_wave_barrier();
  }
  BAR();
  PROT(1);
  asm volatile("" :: "v"(pf[0]), "v"(pf[1]));
  if (v < 8) {
    u16* xn = (u16*)(a.ws + OFF_XN) + (((size_t)(xrow >> 4) * 20 + (lane >> 2)) * 64 + (lane & 3) * 16 + (xrow & 15)) * 8;
    *(u32x4*)xn = xo[0];
    if (lane < 16) *(u32x4*)(xn + 16 * 512) = xo[1];
    if (lane < 4) ((float*)(a.ws + OFF_XNRM))[xrow * 4 + lane] = lane == 0 ? xn2 : 0.f;
  }
  if (job < 800) {
    u16* dst = (u16*)(a.ws + (job < 600 ? OFF_WQKVBT : OFF_WOBT)) + ((size_t)jn * 80 + jk) * 512 + lane * 8;
#pragma unroll
    for (int t = 0; t < 4; ++t) *(bf16x8*)(dst + (size_t)t * 20 * 512) = tf[t];
  }
  const int gp = h >> 1, hh = h & 1;
  const __amdgpu_buffer_rsrc_t rwb = __builtin_amdgcn_make_buffer_rsrc(a.ws + OFF_WB, 0, 256 * 100 * 1024, 0x00020000);
#pragma unroll
  for (int n = 0; n < 10; ++n) {
    const int off = n * 32 + 8 * (c & 3), T = cq * 10 + n;
    const char* bp = img + (8 * g + (c >> 2)) * 320 + (off ^ ((g & 1) << 5));
    f32x4 ac[2] = {{0.f, 0.f, 0.f, 0.f}, {0.f, 0.f, 0.f, 0.f}};
#pragma unroll
    for (int e = 0; e < 6; ++e) {
      const bf16x8 bf = tr_frag(bp + e * 32 * 320, bp + (e * 32 + 4) * 320);
      ac[0] = mfma32(A[0][e], bf, ac[0]); ac[1] = mfma32(A[1][e], bf, ac[1]);
    }
    const u32x4 o = {pack2(ac[0][0], ac[0][1]), pack2(ac[0][2], ac[0][3]), pack2(ac[1][0], ac[1][1]), pack2(ac[1][2], ac[1][3])};
    const unsigned oo = T < 32 ? (unsigned)((rec_wg(gp, T & 15) * 100 + (hh * 3 + (T >> 4)) * 20 + ks) * 1024 + lane * 16)
                               : (unsigned)((rec_wg(gp, 2 * (T - 32) + (c >> 3)) * 100 + 40 + ks) * 1024 + (g * 16 + hh * 8 + (c & 7)) * 16);
    __builtin_amdgcn_raw_buffer_store_b128(o, rwb, oo, 0, 0);
    float n2 = 0.f;
#pragma unroll
    for (int r = 0; r < 4; ++r) n2 += ac[0][r] * ac[0][r] + ac[1][r] * ac[1][r];
    n2 = x32sum(x16sum(n2));
    if (g == 0) red[v][n * 16 + c] = n2;
  }
  PROT(2);
  probe(PRB_N0, PRB_N1);
  if (v < 8) latw[v] = lat;
  else if (b < 64 && lane == 0) ((unsigned*)(a.ws + OFF_LAT))[(v - 4) * 64 + b] = umin(lat, 60000u);
  if (WARM_CODE && v == 7) {
    const char* pc; int d0, d1;
    asm("s_getpc_b64 %0\n\ts_mov_b32 %1, _Z10rec_kernel7RecArgs@rel32@lo+4\n\ts_mov_b32 %2, _Z11proj_kernelPKtS0_S0_Pt@rel32@lo+12" : "=s"(pc), "=s"(d0), "=s"(d1));
    const char *c0 = pc + d0, *c1 = pc + d1;
    const long o = ((long)(b >> 3) * 64 + lane) * 64;
    if (o < c1 - c0) asm volatile("" :: "v"(*(const volatile GAS unsigned*)(c0 + o)));
  }
  if (v == 9 && lane < 32) {
    const int q = b * 32 + lane;
    *(u32x4*)(a.ws + OFF_KV + (size_t)(q >> 2) * KVT + 1280 + (q & 3) * 336 + 320) = (u32x4){0x3f803f80u, 0x3f803f80u, 0x3f803f80u, 0x3f803f80u};
  }
  BAR();
  if (v == 1 && lane == 0) ((unsigned*)(a.ws + OFF_LAT))[b] = umin(umin(umin(latw[0], latw[4]) + umin(latw[1], latw[5]), 30000u) + umin(umin(latw[2], latw[6]) + umin(latw[3], latw[7]), 30000u), 60000u);
  if (v == 0) {
    float mx = 0.f;
#pragma unroll
    for (int k = 0; k < 3; ++k) {
      const int col = k * 64 + lane;
      float sm = 0.f;
#pragma unroll
      for (int i = 0; i < 10; ++i) sm += red[i][col < 160 ? col : 0];
      mx = fmx(mx, sm);
    }
    mx = fmx(mx, dppf<DPP_XOR1>(mx)); mx = fmx(mx, dppf<DPP_XOR2>(mx)); mx = fmx(mx, dppf<DPP_ROR4>(mx)); mx = fmx(mx, dppf<DPP_ROR8>(mx));
    mx = x32mx(x16mx(mx));
    if (lane == 0) ((float*)(a.ws + OFF_BNRM))[(cq * 32 + h) * 2 + dh] = mx;
  }
  PROT(3);
}

struct RecArgs {
  const u16* mem0; const u16* wq; const u16* wk; const u16* xold; u16* xnew; char* ws;
  unsigned layer; unsigned pad;
};
#define RLX_AGENT __ATOMIC_RELAXED, __HIP_MEMORY_SCOPE_AGENT
constexpr unsigned SPIN_CAP = 1u << 20;
#define TSTAMP(k, v)
#define ARRIVE(k)
#define ARRIVE_OUT()
#define ARRIVE_DECL()
#define LOGV(k, v)
#define LOGF(k, v)
#define LOGP(k)
#define LOGQ(k)
#define LOGF2(k)
#define LOGD(k)
#define LOGT(k, v)
#define STAMP_DECL()
#define STAMP(k)
#define STAMPV(k, v)
#define STAMP_OUT()
#define REC_D1 480
#define SCH_V 19
#define SCH_VMIN 0
#define SCH_DMIN 4
#define SCH_PEN 170
#define SCH_VMIN2 (-4)
#define SCH_PEN2 40
#define SCH_GU 1
#define SCH_GD 2
#define SCH_UP 6
#define SCH_D0 212
#define SCH_FIRST 120
#define EXP_NODMA 0
#define BG_HOLD 300
#define QBATCH 0
#define WPF0 53
#define BG_HOLD4 420
#define D1_W1 20
#define F3_D 1200
#define T2W 2
#define AT_ASM 1
#define PM_Z 1
#define P2_X 1
#define ST_KS 14
#define ST_KS0 ST_KS
#define ST_KS1 ST_KS
#define PADR 0x6666
#define PADT 0x000
#define CODE_PAD(n) asm volatile(".p2align 6\n.rept %0\ns_nop 0\n.endr" :: "n"(n))
#define ST_X 290
#define ST_ALL 4
#define CPF_S 61
#define FR_D 700
#define FR_PACE 6
#define END_E60 0
#define END_E1 1300
#define END_E2 5200
#define END_E62 2500
#define END_E3 9800
#define END_E4 14500
#define EARLY_N 9
#define EARLY_D1 120
#define EARLY_V 8
#define EARLY_V1 30
#define F3 8
#define F3A 6
#define F3A_D 400
#define T4_D 300
#define T4_P 1
#define P2_W 1
#define PS_SL 1
#define F2B 10
#define F0C 3
#define F2C 3
#define W2_P 3
#define SCH_W 1
#define SP_SL 0
#define D1_M 300
#define D1_MIN 480
#define D1_C 96
#define D1_G 2
#define KP0 0
#define KP1 0
#define XPF 1
#define W1_D 0
#define F4 4
#define F5 0
#define F67 0
#define KP2 0
#define REC_THR 32.0f
#define SCH_DRAIN 40
#define PL_X 0
#define FC(k)
#define PIN_V(x) asm volatile("" : "+v"(x))
#define PIN_S(x) asm volatile("" : "+s"(x))
#define VM_DRAIN() do { asm volatile("s_waitcnt vmcnt(0)" ::: "memory"); __builtin_amdgcn_s_waitcnt(0x0F70); } while (0)
#define SC1_LOAD(off) __builtin_amdgcn_raw_buffer_load_b128(rs, (off), 0, 16)
#define SC1_LOADS(off, soff) __builtin_amdgcn_raw_buffer_load_b128(rs, (off), (soff), 16)
#define SC1_STORE8S(v, off, soff) __builtin_amdgcn_raw_buffer_store_b64((v), rs, (off), (soff), 16)
#define SC1_LOAD8(off) __builtin_amdgcn_raw_buffer_load_b64(rs, (off), 0, 16)
#define SC1_STORE(v, off) __builtin_amdgcn_raw_buffer_store_b128((v), rs, (off), 0, 16)
#define SC1_STORE8(v, off) __builtin_amdgcn_raw_buffer_store_b64((v), rs, (off), 0, 16)
#if defined(__HIP_DEVICE_COMPILE__)
#pragma clang attribute push(__attribute__((target("no-packed-fp32-ops"))), apply_to = function)
#endif
__device__ __forceinline__ unsigned now32() { return (unsigned)__builtin_amdgcn_s_memrealtime(); }
__device__ __forceinline__ void wait_until(unsigned t) { for (;;) { const int d = (int)(t - now32()); if (d <= 0) break; if (d > 8) __builtin_amdgcn_s_sleep(1); } }
__device__ __forceinline__ unsigned cyc32() { return (unsigned)__builtin_amdgcn_s_memtime(); }
__device__ __forceinline__ void wait_cyc(unsigned t) { for (;;) { const int d = (int)(t - cyc32()); if (d <= 0) break; if (d > 200) __builtin_amdgcn_s_sleep(1); } }
#define WAIT_ASM(clk, far_) "1:\n\t" clk " vcc\n\ts_waitcnt lgkmcnt(0)\n\ts_sub_i32 %0, %1, vcc_lo\n\ts_cmp_lt_i32 %0, " far_ "\n\ts_cbranch_scc1 3f\n\ts_sleep 1\n\ts_branch 1b\n" \
                            "2:\n\t" clk " vcc\n\ts_waitcnt lgkmcnt(0)\n\ts_sub_i32 %0, %1, vcc_lo\n3:\n\ts_cmp_gt_i32 %0, 0\n\ts_cbranch_scc1 2b"
__device__ __forceinline__ int spin_until(unsigned t) { int d; asm volatile(WAIT_ASM("s_memrealtime", "9") : "=&s"(d) : "s"(t) : "vcc", "scc", "memory"); return d; }
__device__ __forceinline__ int spin_cyc(unsigned t) { int d; asm volatile(WAIT_ASM("s_memtime", "200") : "=&s"(d) : "s"(t) : "vcc", "scc", "memory"); return d; }
template <int OFF, int SL> __device__ __forceinline__ void lds_spin(unsigned lds0, unsigned want) {
  unsigned v;
  asm volatile("1:\n\tds_read_b32 %0, %1 offset:%3\n\t.if %4\n\ts_sleep %4\n\t.endif\n\ts_waitcnt lgkmcnt(0)\n\tv_cmp_gt_u32 vcc, %2, %0\n\ts_cbranch_vccnz 1b"
               : "=&v"(v) : "v"(lds0), "s"(want), "n"(OFF), "n"(SL) : "vcc", "memory");
}
template <int CTRL> __device__ __forceinline__ int dppi(int v) { return __builtin_amdgcn_mov_dpp(v, CTRL, 0xf, 0xf, true); }
__device__ __forceinline__ int wave_imax(int l) {
  l = imax(l, dppi<DPP_XOR1>(l)); l = imax(l, dppi<DPP_XOR2>(l)); l = imax(l, dppi<DPP_ROR4>(l)); l = imax(l, dppi<DPP_ROR8>(l));
  return imax(imax(__builtin_amdgcn_readlane(l, 0), __builtin_amdgcn_readlane(l, 16)), imax(__builtin_amdgcn_readlane(l, 32), __builtin_amdgcn_readlane(l, 48)));
}
__device__ __forceinline__ void glds16(const void* gsrc, unsigned lds_dst) {
  unsigned keep;
  asm volatile("s_mov_b32 %0, m0\n\ts_mov_b32 m0, %2\n\ts_nop 0\n\tglobal_load_lds_dwordx4 %1, off\n\ts_mov_b32 m0, %0" : "=&s"(keep) : "v"(gsrc), "s"(lds_dst) : "memory");
}
__device__ __forceinline__ void glds16_sc1(const void* gsrc, unsigned lds_dst) {
  unsigned keep;
  asm volatile("s_mov_b32 %0, m0\n\ts_mov_b32 m0, %2\n\ts_nop 0\n\tglobal_load_lds_dwordx4 %1, off sc1\n\ts_mov_b32 m0, %0" : "=&s"(keep) : "v"(gsrc), "s"(lds_dst) : "memory");
}
__device__ __forceinline__ unsigned acc_off(int G, int row) {
  return (unsigned)(G < 128 ? ((G & 31) | ((G & 32) << 1) | ((G & 64) >> 1)) * 256 + row * 16 : (2 * G - 128 + (row >> 2)) * 256 + (row & 3) * 16);
}
__device__ __forceinline__ unsigned nz(unsigned v) { return v > 1u ? v : 1u; }
__device__ __forceinline__ unsigned pack2n(float lo, float hi) { return nz(pack2(lo, hi)); }
__device__ __forceinline__ unsigned min4(u32x4 v) { return umin(umin(v[0], v[1]), umin(v[2], v[3])); }
__device__ __forceinline__ bf16x8 as8(u32x4 v) { return __builtin_bit_cast(bf16x8, v); }

constexpr int NV = 7, NKQ = 2;
constexpr int L_RED1 = 0;
constexpr int L_BM = L_RED1 + 8 * 1536;
constexpr int L_STG = L_BM + 10240;
constexpr int L_ST = L_STG + 2048 + 2560;
constexpr int L_MISC = L_ST + 512;
constexpr int L_KX = L_MISC + 128;
constexpr int L_QX = L_KX + NKQ * 2048;
constexpr int L_XN = L_QX + NKQ * 2048;
constexpr int L_VOX = L_XN + 81920;
constexpr int L_VOM = L_VOX + NV * 5120;
constexpr int L_BYTES = L_VOM + NV * 1280;
constexpr int S_IMG = L_XN + 40960;
static_assert(S_IMG + 61440 <= L_VOM, "start-up image");
static_assert(L_BYTES <= 160 * 1024, "LDS budget");

__device__ __forceinline__ bf16x8 wqk_frag(const char* lds, int t, int ks, int lane) {
  const int c = lane & 15, g = lane >> 4, p = c & 3;
  const char* q = lds + S_IMG + (32 * ks + 8 * g + (c >> 2)) * 96 + 8 * p + (t == 0 ? 0 : (t == 1 ? 48 : (p < 2 ? 32 : 64)));
  const v4i16_t lo = __builtin_amdgcn_ds_read_tr16_b64_v4i16((LAS v4i16_t*)q), hi = __builtin_amdgcn_ds_read_tr16_b64_v4i16((LAS v4i16_t*)(q + 4 * 96));
  return (bf16x8){lo[0], lo[1], lo[2], lo[3], hi[0], hi[1], hi[2], hi[3]};
}
__device__ __forceinline__ bf16x8 sel_frag(int c, int base) {
  bf16x8 r;
#pragma unroll
  for (int i = 0; i < 8; ++i) r[i] = (c == base + i) ? (short)0x3f80 : (short)0;
  return r;
}
template <class F> __device__ __forceinline__ bf16x8 qk_frag(const char* lds, int g, int c, F mid) {
  const char* p4 = lds + L_RED1 + c * 112 + g * 16;
  const char* p2 = lds + L_RED1 + c * 112 + 64 + g * 8;
  f32x4 a0 = *(const f32x4*)(p4), a1 = *(const f32x4*)(p4 + 1792), a2 = *(const f32x4*)(p4 + 3584), a3 = *(const f32x4*)(p4 + 5376);
  f32x2 b0 = *(const f32x2*)(p2), b1 = *(const f32x2*)(p2 + 1792), b2 = *(const f32x2*)(p2 + 3584), b3 = *(const f32x2*)(p2 + 5376);
  mid();
  f32x4 va = (a0 + a1) + (a2 + a3);
  f32x2 vb = (b0 + b1) + (b2 + b3);
  return mk8(pack2(va[0], va[1]), pack2(va[2], va[3]), pack2(vb[0], vb[1]), 0u);
}
__device__ __forceinline__ void x_fetch(const u16* src, unsigned lds0, int seg, int p, int lane) {
  if (EXP_NODMA) return;
  glds16_sc1(src + (size_t)p * 512 + lane * 8, lds0 + L_XN + (seg & 1) * 40960 + p * 1024);
}
template <int N> __device__ __forceinline__ void x_fetch_n(const u16* src, unsigned lds0, int seg, int p0, unsigned voff) {
  if (EXP_NODMA) return;
#pragma unroll
  for (int i = 0; i < N; i += 4) {
    unsigned keep;
    asm volatile("s_mov_b32 %0, m0\n\ts_mov_b32 m0, %3\n\ts_nop 0\n\tglobal_load_lds_dwordx4 %1, %2 sc1\n\t"
                 ".if %4 > 1\n\tglobal_load_lds_dwordx4 %1, %2 offset:1024 sc1\n\t.endif\n\t.if %4 > 2\n\tglobal_load_lds_dwordx4 %1, %2 offset:2048 sc1\n\t.endif\n\t"
                 ".if %4 > 3\n\tglobal_load_lds_dwordx4 %1, %2 offset:3072 sc1\n\t.endif\n\ts_mov_b32 m0, %0"
                 : "=&s"(keep) : "v"(voff), "s"(src + (size_t)(p0 + i) * 512), "s"(lds0 + L_XN + (seg & 1) * 40960 + (p0 + i) * 1024), "n"(N - i < 4 ? N - i : 4) : "memory");
  }
}
__device__ __forceinline__ int qkv_row(int b) { return 2 * (b & 7) + ((b >> 3) >= 15); }
__device__ __forceinline__ const u16* x_src(const RecArgs& a, int seg, int b) {
  return seg < NSEG ? (const u16*)(a.ws + OFF_XN) + (size_t)seg * 32 * DM : a.xnew + (size_t)(qkv_row(b) + 16 * (seg - NSEG)) * 32 * DM;
}
template <int N>
__device__ __forceinline__ void img_fetch(const RecArgs& a, unsigned lds0, int h, int j, int part, int lane) {
#pragma unroll
  for (int i = 0; i < N; ++i) {
    const int n = part * N + i, idx = n * 64 + lane, row = idx / 6, pc = idx - row * 6;
    if (FR_PACE && N == 20) asm volatile("s_waitcnt vmcnt(%0)" :: "n"(FR_PACE) : "memory");
    glds16((pc < 3 ? a.wq : a.wk - 24) + (size_t)row * HD + h * DH + j * 24 + pc * 8, lds0 + S_IMG + n * 1024);
  }
}
constexpr int P2B = F4 + F5 + 2 * F67, P0C = P2B + F2B, F3C = 40 - F3A - F3 - P0C - 2 * F0C - F2C;
static_assert(F3C >= 0, "fetch plan");
template <int OFF> __device__ __forceinline__ void lds_inc(unsigned lds0) {
  asm volatile("s_mov_b64 exec, 1\n\tds_add_u32 %0, %1 offset:%2\n\ts_mov_b64 exec, -1" :: "v"(lds0), "v"(1u), "n"(OFF) : "memory");
}
__device__ __forceinline__ void lds_arrive(unsigned* p, int lane) {
  asm volatile("s_waitcnt lgkmcnt(0)" ::: "memory");
  if (lane == 0) __hip_atomic_fetch_add(p, 1u, __ATOMIC_RELAXED, __HIP_MEMORY_SCOPE_WORKGROUP);
}
__device__ __forceinline__ void lds_wait(unsigned* p, unsigned want) {
  while (__hip_atomic_load(p, __ATOMIC_RELAXED, __HIP_MEMORY_SCOPE_WORKGROUP) < want) __builtin_amdgcn_s_sleep(1);
  asm volatile("" ::: "memory");
}
constexpr float KEXP = 0.07216878364870322f * 1.4426950408889634f;
#define FRESH_LANE() int lane = lane0; asm volatile("" : "+v"(lane)); const int c = lane & 15, g = lane >> 4; (void)c; (void)g

struct OutTask {
  u32x4 y[4]; u32x2 xo, out;
  bf16x8 sel;
  unsigned t5x, t5f, t5o;
  __device__ __forceinline__ void init(int b, int lane) {
    const int um = b / 10, uq = (b / 5) & 1, up = b % 5, tok = lane & 15, k = up < 4 ? (up >> 1) * 256 + um * 16 + (up & 1) * 8 : 512 + um * 8, q4 = ((lane >> 4) & 1) * 4;
    t5x = (unsigned)((uq * 16 + tok) * DM + k + q4);
    t5f = (unsigned)(((uq * 20 + (k >> 5)) * 64 + ((k & 31) >> 3) * 16 + tok) * 8 + q4);
    t5o = (unsigned)(((um * 2 + uq) * 16 + (lane >> 4)) * 1280 + up * 256 + tok * 16);
    if (b >= 160) { t5x = t5f = 0u; t5o = 0x7ffffff0u; }
    sel = sel_frag(tok, 0);
    PIN_V(sel);
  }
  static __device__ __forceinline__ bool ok(int sl, int b) { return sl >= 0 && b < 160; }
  __device__ __forceinline__ void load(const RecArgs& a, const __amdgpu_buffer_rsrc_t rs, int sl, int b, int part = 3) {
    const unsigned o = t5o | (sl < 0 ? 0x7ff00000u : 0u);
    sl = imax(sl, 0);
#pragma unroll
    for (int k = 0; k < 4; ++k) if (part & 1) y[k] = SC1_LOADS(o, op_slot(sl) + k * 5120);
    if (part & 2) xo = __builtin_bit_cast(u32x2, __hip_atomic_load((const gu64*)(a.xold + (size_t)sl * 32 * DM + (a.layer ? t5f : t5x)), RLX_AGENT));
  }
  __device__ __forceinline__ bool ready(int b) { return b >= 160 || wave_all(umin(umin(min4(y[0]), min4(y[1])), umin(min4(y[2]), min4(y[3]))) != 0u); }
  __device__ __forceinline__ void run(int lane) {
    f32x4 d = {0.f, 0.f, 0.f, 0.f};
#pragma unroll
    for (int k = 0; k < 4; ++k) d = mfma32(sel, as8(y[k]), d);
#pragma unroll
    for (int i = 0; i < 2; ++i) {
      const unsigned r = pack2(d[2 * i], d[2 * i + 1]), x = xo[i];
      out[i] = pack2(u2f(x << 16) + u2f(r << 16), u2f(x & 0xffff0000u) + u2f(r & 0xffff0000u));
    }
    PIN_V(out);
  }
  __device__ __forceinline__ void store(const RecArgs& a, int sl, int b, int lane) {
    if (ok(sl, b) && lane < 32) __hip_atomic_store((gu64*)(a.xnew + (size_t)sl * 32 * DM + t5f), (u64)out[0] | ((u64)out[1] << 32), RLX_AGENT);
  }
};

struct PvTask {
  u32x4 y[2]; u32x2 xo[2];
  __device__ __forceinline__ void load(const __amdgpu_buffer_rsrc_t rs, unsigned ck, int sl, int gp, int qt, int lane) {
    const unsigned o = pt_slot(ck, sl);
#pragma unroll
    for (int k = 0; k < 2; ++k) {
      y[k] = SC1_LOAD(o + pt_tok(2 * gp) + k * 2048 + qt * 1024 + lane * 16);
      xo[k] = SC1_LOAD8(o + pt_mem(2 * gp) + k * 512 + qt * 256 + (lane & 31) * 8);
    }
  }
  __device__ __forceinline__ bool ready() { return wave_all(umin(umin(min4(y[0]), min4(y[1])), umin(umin(xo[0][0], xo[0][1]), umin(xo[1][0], xo[1][1]))) != 0u); }
  __device__ __forceinline__ void run(char* lds, const __amdgpu_buffer_rsrc_t rs, int sl, int gp, int m, int qt, int lane, bool direct) {
    const f32x4 Z4 = {0.f, 0.f, 0.f, 0.f};
    PIN_V(y[0]); PIN_V(y[1]); PIN_V(xo[0]); PIN_V(xo[1]);
    const int sv = (sl + 2 * NV) % NV;
    const char* vp = lds + L_VOX + sv * 5120 + lane * 16;
    const char* mp = lds + L_VOM + sv * 1280 + (lane & 31) * 8;
    bf16x8 pm[2], vm[5], vx[5];
#pragma unroll
    for (int t = 0; t < 5; ++t) { vx[t] = *(const bf16x8*)(vp + t * 1024); const u32x2 v = *(const u32x2*)(mp + t * 256); vm[t] = mk8(v[0], v[1], 0u, 0u); }
#pragma unroll
    for (int k = 0; k < 2; ++k) pm[k] = mk8(lane < 32 ? xo[k][0] : 0u, lane < 32 ? xo[k][1] : 0u, 0u, 0u);
    __builtin_amdgcn_sched_barrier(0);
    f32x4 o[4];
#pragma unroll
    for (int k = 0; k < 2; ++k) { o[k] = mfma32(vx[k], as8(y[0]), Z4); o[2 + k] = mfma32(vx[2], as8(y[k]), Z4); }
#pragma unroll
    for (int k = 0; k < 2; ++k) { o[k] = mfma32(vm[k], pm[0], o[k]); o[2 + k] = mfma32(vm[2], pm[k], o[2 + k]); }
#pragma unroll
    for (int k = 0; k < 2; ++k) o[k] = mfma32(vx[3 + k], as8(y[1]), o[k]);
#pragma unroll
    for (int k = 0; k < 2; ++k) o[k] = mfma32(vm[3 + k], pm[1], o[k]);
    float oc[4];
#pragma unroll
    for (int r = 0; r < 4; ++r) {
      auto sw = __builtin_amdgcn_permlane32_swap(f2u(o[3][r]), f2u(o[3][r]), false, false);
      oc[r] = o[2][r] + u2f(sw[1]);
    }
    const u32x2 v0 = {pack2n(o[0][0], o[0][1]), pack2n(o[0][2], o[0][3])}, v1 = {pack2n(o[1][0], o[1][1]), pack2n(o[1][2], o[1][3])};
    const u32x2 v2 = {pack2n(oc[0], oc[1]), pack2n(oc[2], oc[3])};
    const unsigned po = (unsigned)(((lane >> 5) * 16 + (lane & 15)) * 16 + ((lane >> 4) & 1) * 8);
    if (direct) {
      const unsigned oo = op_slot(sl) + (unsigned)((m * 32 + qt * 16 + gp) * 1280) + po;
      SC1_STORE8(v0, oo); SC1_STORE8(v1, oo + 512);
      if (lane < 32) SC1_STORE8(v2, oo + 1024);
    } else {
      char* sp = lds + L_STG + 2048 + qt * 1280 + po;
      *(u32x2*)(sp) = v0; *(u32x2*)(sp + 512) = v1;
      if (lane < 32) *(u32x2*)(sp + 1024) = v2;
    }
  }
};

struct PvTask2 {
  u32x4 y[2][2], pa[2], pb[2];
  unsigned lo, lm, vr, ma, mz, so, sz;
  __device__ __forceinline__ void init(int gp, int lane) {
    const int hi = lane >> 5;
    lo = pt_tok(2 * gp) + (unsigned)(lane * 16); lm = pt_mem(2 * gp + hi) + (unsigned)((lane & 31) * 8);
    vr = (unsigned)(L_VOX + 2048 + (lane ^ 8) * 16);
    ma = (unsigned)(L_VOM + hi * 768 + (lane & 31) * 8); mz = (unsigned)(L_VOM + 512 + ((lane & 31) ^ (hi * 8)) * 8);
    so = (unsigned)(L_STG + 2048 + (hi * 16 + (lane & 15)) * 16 + ((lane >> 4) & 1) * 8);
    sz = hi ? (unsigned)(L_RED1 + 8704 + lane * 8) : so + 1024;
    PIN_V(lo); PIN_V(lm); PIN_V(vr); PIN_V(ma); PIN_V(mz); PIN_V(so); PIN_V(sz);
#pragma unroll
    for (int q = 0; q < 2; ++q) { pa[q] = pb[q] = y[q][0] = y[q][1] = (u32x4){0u, 0u, 0u, 0u}; PIN_V(pa[q]); PIN_V(pb[q]); }
  }
  __device__ __forceinline__ void load(const __amdgpu_buffer_rsrc_t rs, unsigned ck, int sl) {
    const unsigned o = sl < 0 ? 0x7ff00000u : pt_slot(ck, sl);
#pragma unroll
    for (int q = 0; q < 2; ++q) {
      y[q][0] = SC1_LOAD(lo + o + q * 1024); y[q][1] = SC1_LOAD(lo + o + 2048 + q * 1024);
      const u32x2 p = SC1_LOAD8(lm + o + q * 256);
      pa[q][0] = p[0]; pa[q][1] = p[1];
    }
  }
  __device__ __forceinline__ void run(char* lds, int sv, int lane) {
    const f32x4 Z4 = {0.f, 0.f, 0.f, 0.f};
    const char* vp = lds + L_VOX + sv * 5120 + lane * 16;
    bf16x8 vx[6];
#pragma unroll
    for (int t = 0; t < 5; ++t) vx[t] = *(const bf16x8*)(vp + t * 1024);
    vx[5] = *(const bf16x8*)(lds + vr + sv * 5120);
    const u32x2 a0 = *(const u32x2*)(lds + ma + sv * 1280), a1 = *(const u32x2*)(lds + ma + sv * 1280 + 256);
    const u32x2 z0 = *(const u32x2*)(lds + mz + sv * 1280), z1 = *(const u32x2*)(lds + mz + sv * 1280 + 256);
    const bf16x8 vm = mk8(a0[0], a0[1], a1[0], a1[1]), vz = mk8(z0[0], z0[1], z1[0], z1[1]);
#pragma unroll
    for (int q = 0; q < 2; ++q) { PIN_V(pa[q]); pb[q][2] = pa[q][0]; pb[q][3] = pa[q][1]; PIN_V(pb[q]); }
    __builtin_amdgcn_sched_barrier(0);
    f32x4 o[2][3];
#pragma unroll
    for (int q = 0; q < 2; ++q) {
#pragma unroll
      for (int t = 0; t < 3; ++t) o[q][t] = mfma32(vx[t], as8(y[q][0]), Z4);
#pragma unroll
      for (int t = 0; t < 3; ++t) o[q][t] = mfma32(vx[3 + t], as8(y[q][1]), o[q][t]);
      o[q][0] = mfma32(vm, as8(pa[q]), o[q][0]); o[q][1] = mfma32(vm, as8(pb[q]), o[q][1]); o[q][2] = mfma32(vz, as8(pa[q]), o[q][2]);
    }
#pragma unroll
    for (int q = 0; q < 2; ++q) {
#pragma unroll
      for (int t = 0; t < 3; ++t)
        *(u32x2*)(lds + (t < 2 ? so + t * 512 : sz) + q * 1280) = (u32x2){pack2n(o[q][t][0], o[q][t][1]), pack2n(o[q][t][2], o[q][t][3])};
    }
  }
};

__device__ __forceinline__ void t2_tok(char* lds, int seg, int lane) {
  const f32x4 Z4 = {0.f, 0.f, 0.f, 0.f};
  const char* kp = lds + L_KX + (seg & 1) * 2048 + lane * 16;
  const char* qp = lds + L_QX + (seg & 1) * 2048 + lane * 16;
  const bf16x8 k0 = *(const bf16x8*)(kp), k1 = *(const bf16x8*)(kp + 1024);
#pragma unroll
  for (int qt = 0; qt < 2; ++qt) {
    const bf16x8 q = *(const bf16x8*)(qp + qt * 1024);
    const f32x4 d1 = mfma32(k0, q, Z4), d2 = mfma32(k1, q, Z4);
    *(u32x4*)(lds + L_STG + qt * 1024 + lane * 16) = (u32x4){pack2n(d1[0], d1[1]), pack2n(d1[2], d1[3]), pack2n(d2[0], d2[1]), pack2n(d2[2], d2[3])};
  }
}

__device__ __forceinline__ void touch(const char* base, unsigned off, unsigned lds0) {
  unsigned keep;
  asm volatile("s_mov_b32 %0, m0\n\ts_mov_b32 m0, %3\n\ts_nop 0\n\tglobal_load_lds_dword %1, %2 sc1\n\ts_mov_b32 m0, %0" : "=&s"(keep) : "v"(off), "s"(base), "s"(lds0 + L_RED1 + 8192) : "memory");
}
__device__ __forceinline__ void code_touch(int b, int lane, unsigned lds0) {
  const char* pc; int d0, d1;
  asm volatile("s_getpc_b64 %0\n\ts_mov_b32 %1, _Z10rec_kernel7RecArgs@rel32@lo+4\n\ts_mov_b32 %2, _Z11attb_kernelPKtS0_Pt@rel32@lo+12" : "=s"(pc), "=s"(d0), "=s"(d1));
  const int o = ((b >> 3) * 64 + lane) * 64;
  touch(pc + d0, (unsigned)(o < d1 - d0 ? o : 0), lds0);
}
__device__ __forceinline__ unsigned zero_base(int b, int h, int j, int ln) {
  return ln < 32 ? (unsigned)((h & 3) * 32768 + j * 512 + ln * 16) : ln < 40 ? (unsigned)((h & 2) * 1024 + j * 256 + (ln & 4) * 32 + (h & 1) * 64 + (ln & 3) * 16)
                 : (unsigned)OFF_ACC + (b < ACC_NB ? (unsigned)(b * 256 + (ln - 40) * 16) : ts_piece((b - ACC_NB) * 8 + ln - 40));
}
__device__ __forceinline__ unsigned sct_slot(unsigned ck, unsigned sl) { return sl * SCT_STEP + ((ck >> (sl & 4u)) & 7u) * 4096u; }
__device__ __forceinline__ unsigned scm_slot(unsigned ck, unsigned sl) { return OFF_SCM + sl * SCM_STEP + ((ck >> 8) & 7u) * 4096u; }
__device__ __forceinline__ int ck_rot(int x) { return ((x & 1) << 2) | (x >> 1); }
struct Place {
  unsigned t[8];
  __device__ __forceinline__ void load(const char* ws, int lane) {
#pragma unroll
    for (int x = 0; x < 8; ++x) t[x] = *(const unsigned*)(ws + OFF_LAT + ((imin(lane >> 3, 5) * 8 + (ck_rot(x) ^ (lane & 7))) * 8 + x) * 4);
  }
  __device__ __forceinline__ unsigned get(int b, int lane) {
    int c = (int)((((t[0] + t[1]) + (t[2] + t[3])) + ((t[4] + t[5]) + (t[6] + t[7]))) * 8u) + (lane & 7);
    c = imin(c, dppi<DPP_XOR1>(c)); c = imin(c, dppi<DPP_XOR2>(c)); c = imin(c, dppi<0x141>(c));
    unsigned k = 0u;
#pragma unroll
    for (int r = 0; r < 6; ++r) k |= (unsigned)(__builtin_amdgcn_readlane(c, 8 * r) & 7) << (4 * r);
    return (k ^ PL_X) ^ (unsigned)ck_rot(b & 7) * 0x111111u;
  }
};

struct Sched {
  u32x4 tsv, rec;
  __device__ __forceinline__ void load(const __amdgpu_buffer_rsrc_t rs, int s, int lane) {
    const unsigned o = (unsigned)OFF_ACC + (unsigned)((s + SR - 1) % SR) * (unsigned)ACC_STEP + ACC_TS;
    tsv = SC1_LOADS(lane * 256, o); rec = SC1_LOADS(128, o);
  }
  __device__ __forceinline__ unsigned run(const RecArgs& a, const __amdgpu_buffer_rsrc_t rs, int s, int b, int lane) {
    constexpr int w = 3; (void)w;
    if (__builtin_expect(!wave_all(min4(tsv) != 0u), 0)) {
      int it = s ? 39 : 0;
      do {
        asm volatile("" ::: "memory");
        load(rs, s, lane);
        if (wave_all(min4(tsv) != 0u)) break;
        __builtin_amdgcn_s_sleep(ST_ALL);
      } while (++it < 40);
    }
    const unsigned ref = now32();
    int l = -100000;
#pragma unroll
    for (int i = 0; i < 4; ++i) l = imax(l, tsv[i] ? (int)(tsv[i] - ref) : -100000);
    l = wave_imax(l);
    const unsigned L = ref + (unsigned)l;
    unsigned acur = __builtin_amdgcn_readfirstlane(rec[0]), aprev = __builtin_amdgcn_readfirstlane(rec[1]);
    unsigned r2 = __builtin_amdgcn_readfirstlane(rec[2]), r3 = __builtin_amdgcn_readfirstlane(rec[3]);
    PIN_S(r2); PIN_S(r3);
    int D8 = acur ? (int)r2 : 8 * SCH_D0;
    unsigned hist = acur ? r3 : 0u;
    const bool late = (acur == 0u) | ((int)(L + SCH_VMIN - acur) > 0);
    if (late && b == 0) FC(3);
    int up = hist ? 8 * SCH_UP : 24 * SCH_UP;
    up = acur ? up : 0;
    const int d8 = 8 * imin(imax((int)(L - aprev), 1), 4000);
    int Du = D8 + ((d8 - D8) >> SCH_GU), Dd = D8 - ((D8 - d8) >> SCH_GD), Dl = D8 + up;
    PIN_S(Du); PIN_S(Dd); PIN_S(Dl);
    int Dm = d8 > D8 ? Du : Dd;
    Dm = hist < 2u ? d8 : Dm;
    unsigned hm = umin(hist + 1u, 2u);
    PIN_S(Dm); PIN_S(hm);
    Dm = aprev ? Dm : D8; hm = aprev ? hm : hist;
    D8 = late ? Dl : Dm; hist = late ? hist : hm;
    const int D = (D8 + 4) >> 3;
    unsigned bl = L + SCH_V + ((int)(L + SCH_VMIN2 - acur) <= 0 ? SCH_PEN2 : SCH_PEN), b0 = L + SCH_FIRST, a0 = ST_X ? (L + ST_X) | 1u : 0u;
    PIN_S(bl); PIN_S(b0); PIN_S(a0);
    bl = late ? bl : acur;
    bl = acur ? bl : b0;
    unsigned anext = (bl + (unsigned)D + SCH_V + (s == 1 ? EARLY_V1 : (s < EARLY_N ? EARLY_V : 0))) | 1u;
    PIN_S(anext);
    anext = s == 0 ? a0 : anext;
    anext = l < -50000 ? 0u : anext;
    rec = (u32x4){anext, late ? 0u : acur, (unsigned)D8, hist};
    if (__builtin_expect(b == 0, 0)) __builtin_amdgcn_raw_buffer_store_b128(rec, rs, lane == 0 ? 0 : 0x7ffffff0, (unsigned)OFF_ACC + (unsigned)(s % SR) * (unsigned)ACC_STEP + ACC_TS + 128u, 16);
    LOGV(0, anext); LOGV(1, late); LOGV(2, D); LOGV(3, L - aprev); LOGV(4, acur - L); LOGV(5, aprev);
    return anext;
  }
};

template <int w>
__device__ __forceinline__ void token_waves(const RecArgs& a, char* lds, const __amdgpu_buffer_rsrc_t rs) {
  const int lane0 = __builtin_amdgcn_workitem_id_x() & 63, b = __builtin_amdgcn_workgroup_id_x(), wid = w + 4; (void)wid;
  const int gp = (b & 7) * 2 + (b >> 7), m = (b >> 3) & 15, h0 = 2 * gp, h = h0 + (m >> 3), j = m & 7;
  float* const misc = (float*)(lds + L_MISC);
  const f32x4 Z4 = {0.f, 0.f, 0.f, 0.f};
  const unsigned lds0 = (unsigned)(uintptr_t)(LAS char*)lds;
  unsigned* const cnt = (unsigned*)(misc + 6);
  const float* const xnrm = (const float*)(a.ws + OFF_XNRM);
  bf16x16 W[2][10];
  auto btile = [&](int i) { return w == 1 ? 0 : (w == 2 ? 1 + i : 3 + i); };
  if (w == 1) code_touch(b, lane0, lds0);
  Place pl;
  if (w == 3) pl.load(a.ws, lane0);
  if (a.layer) {
    if (w != 0) img_fetch<20>(a, lds0, h, j, w - 1, lane0);
  } else {
    if (w == 0) img_fetch<12>(a, lds0, h, j, 4, lane0);
#pragma unroll
    for (int i = 0; i < 10; ++i) x_fetch(x_src(a, 0, b), lds0, 0, 10 * w + i, lane0);
  }
#pragma unroll
  for (int i = 0; i < 2; ++i)
#pragma unroll
    for (int p = 0; p < 10; ++p)
      if (w >= 2 || (w == 1 && i == 1)) {
        const char* wp = a.ws + OFF_WB + ((size_t)(b * 5 + btile(i)) * 20 + 2 * p) * 1024 + lane0 * 16;
        if (FR_PACE && a.layer) asm volatile("s_waitcnt vmcnt(%0)" :: "n"(FR_PACE) : "memory");
        W[i][p] = cat16(*(const bf16x8*)wp, *(const bf16x8*)(wp + 1024));
      }
  f32x4 bn = Z4;
  if (w == 0) {
    bn = *(const f32x4*)(a.ws + OFF_BNRM + lane0 * 16);
    if (!a.layer) asm volatile("s_waitcnt vmcnt(0)" ::: "memory");
  } else asm volatile("s_waitcnt vmcnt(%0)" :: "n"(w == 1 ? 20 : 40) : "memory");
  unsigned ck = 0u;
  if (w == 3) { ck = pl.get(b, lane0); *(unsigned*)(misc + 11) = ck; }
  BAR();
#pragma unroll
  for (int p = 0; p < 10; ++p) {
    if (w < 2) W[0][p] = cat16(wqk_frag(lds, 2 * w, 2 * p, lane0), wqk_frag(lds, 2 * w, 2 * p + 1, lane0));
    if (w == 0) W[1][p] = cat16(wqk_frag(lds, 1, 2 * p, lane0), wqk_frag(lds, 1, 2 * p + 1, lane0));
  }
  float cb2 = 0.f;
  if (w == 0) {
    float n = __builtin_amdgcn_sqrtf(bn[0] + bn[1]) + __builtin_amdgcn_sqrtf(bn[2] + bn[3]);
    n += dppf<DPP_XOR1>(n); n += dppf<DPP_XOR2>(n); n += dppf<DPP_ROR4>(n); n += dppf<DPP_ROR8>(n);
    n = x32mx(x16mx(n));
    cb2 = u2f(__builtin_amdgcn_readfirstlane(f2u(1.15f * n * n)));
  }
  if (w >= 2) {
#pragma unroll
    for (int i = 0; i < 4; ++i) *(unsigned*)(lds + L_KX + (w - 2) * 4096 + i * 1024 + lane0 * 16 + 12) = 0u;
  }
  OutTask ot;
  const int zl = lane0 - 16;
  const bool zon = w == 3 && zl >= 0 && (zl < 40 || b < ACC_NB + 9);
  unsigned zo = zon ? zero_base(b, h, j, zl) + (zl < 32 ? sct_slot(ck, 0u) : (zl < 40 ? scm_slot(ck, 0u) : 0u)) : 0x7ffffff0u;
  unsigned zm = zon ? (zl < 32 ? SCT_STEP + 1u : (zl < 40 ? SCM_STEP : (unsigned)ACC_STEP)) : 0u;
  const unsigned zx = ((ck ^ (ck >> 4)) & 7u) * 4096u;
  if (w == 3) { PIN_V(zo); PIN_V(zm); }
  auto st_out = [&](int s, int lane) {
    if (w < 2) return;
    const int sl = s - 2 - LAG4;
    const char* sp = lds + L_STG + 2048 + (w & 1) * 1280;
    const u32x4 v0 = *(const u32x4*)(sp + lane * 16), v1 = *(const u32x4*)(sp + 1024 + (lane & 15) * 16), z = {0u, 0u, 0u, 0u};
    const unsigned oo = (sl < 0 ? 0x7ff00000u : op_slot(sl) + (unsigned)((m * 32 + (w & 1) * 16 + gp) * 1280)) + (unsigned)(lane * 16);
    if (w == 2) {
      ot.store(a, s - 2 - LAG5, b, lane);
      SC1_STORE(v0, oo);
      SC1_STORE(v1, lane < 16 ? oo + 1024 : 0x7ffffff0u);
    } else {
      const u32x4 t0 = *(const u32x4*)(lds + L_STG + lane * 16), t1 = *(const u32x4*)(lds + L_STG + 1024 + lane * 16);
      const unsigned zs = (unsigned)(s + 2) % SR;
      unsigned za = (zo + (s < 0 ? 1024u : zs) * (zm & ~1u)) ^ ((zm & 1u) * (zs & 4u ? zx : 0u));
      za = lane < 16 ? oo + 1024 : za;
      PIN_V(za);
      SC1_STORE(v0, oo);
      u32x4 vz;
#pragma unroll
      for (int i = 0; i < 4; ++i) { vz[i] = lane < 16 ? v1[i] : z[i]; PIN_V(vz[i]); }
      SC1_STORE(vz, za);
      const unsigned ots = (s < 0 ? 0x7ff00000u : ts_slot(ck, s) + ts_tok(h, j)) + (unsigned)(lane * 16);
      SC1_STORE(t0, ots);
      SC1_STORE(t1, ots + 1024);
      const unsigned xo_ = (unsigned)((s - 8) * 1024 + lane * 16);
      if (__builtin_expect((unsigned)(s - 8) < 19u, 0)) SC1_STORE(z, xo_ < X_BYTES / 256 ? (unsigned)(OFF_X + b * (X_BYTES / 256)) + xo_ : 0x7ffffff0u);
    }
  };
  STAMP_DECL(); ARRIVE_DECL();
  auto mma_seg = [&](int seg, int sv, int lane, auto pause) {
    const int c = lane & 15, g = lane >> 4;
    const char* xp = lds + L_XN + (seg & 1) * 40960 + lane * 16;
    f32x4 acc[2][2] = {{Z4, Z4}, {Z4, Z4}};
    bf16x8 x[4][2];
#pragma unroll
    for (int p = 0; p < 3; ++p) { x[p][0] = *(const bf16x8*)(xp + p * 1024); x[p][1] = *(const bf16x8*)(xp + (20 + p) * 1024); }
    unsigned eo[2];
#pragma unroll
    for (int ks = 0; ks < 20; ++ks) {
      if (ks == (w == 0 ? KP0 : (w == 1 ? KP1 : KP2))) { pause(); __builtin_amdgcn_sched_barrier(0); }
      if (ks == 17) {
#pragma unroll
        for (int i = 0; i < 2; ++i) {
          eo[i] = (unsigned)(w == 0 ? (i ? L_KX : L_QX) + (seg % NKQ) * 2048 + lane * 16
                                    : (w == 1 && i == 0 ? ((g >> 1) ? L_KX : L_QX) + (seg % NKQ) * 2048 + (2 * (g & 1) * 16 + c) * 16 + 8 : L_VOX + sv * 5120 + btile(i) * 1024 + lane * 16));
          if (i == 0 || w == 1) PIN_V(eo[i]);
        }
        if (w == 0) eo[1] = eo[0] + (L_KX - L_QX);
        if (w >= 2) eo[1] = eo[0] + 1024;
      }
      __builtin_amdgcn_sched_barrier(0);
#pragma unroll
      for (int q = 0; q < 4; ++q) {
        const int tt = 1 - (q >> 1), i = q & 1;
        const bool isqk = w == 0 || (w == 1 && i == 0);
        const bf16x8 wf = half8(W[i][ks >> 1], ks & 1);
        acc[i][tt] = isqk ? mfma32(wf, x[ks & 3][tt], acc[i][tt]) : mfma32(x[ks & 3][tt], wf, acc[i][tt]);
        __builtin_amdgcn_sched_barrier(0);
        if (q < 2 && ks + 3 < 20) x[(ks + 3) & 3][q] = *(const bf16x8*)(xp + (q * 20 + ks + 3) * 1024);
        __builtin_amdgcn_sched_barrier(0);
      }
    }
#pragma unroll
    for (int i = 0; i < 2; ++i) {
      if (w + i == 1) { const float ks_ = (w == 0 || (g >> 1)) ? KEXP : 1.0f; acc[i][0] *= ks_; acc[i][1] *= ks_; }
      const u32x4 v = {pack2(acc[i][0][0], acc[i][0][1]), pack2(acc[i][0][2], acc[i][0][3]), pack2(acc[i][1][0], acc[i][1][1]), pack2(acc[i][1][2], acc[i][1][3])};
      char* p = lds + eo[i];
      if (w == 0) { *(u32x2*)(p) = (u32x2){v[0], v[1]}; *(u32x2*)(p + 1024) = (u32x2){v[2], v[3]}; }
      else if (w == 1 && i == 0) { *(unsigned*)(p) = v[0]; *(unsigned*)(p + 256) = v[1]; *(unsigned*)(p + 1024) = v[2]; *(unsigned*)(p + 1280) = v[3]; }
      else *(u32x4*)p = v;
    }
  };
  auto vo_mem = [&](int s, int sv, unsigned t0, int lane, auto last) {
    const int c = lane & 15, g = lane >> 4;
    unsigned vp = (unsigned)(L_VOM + sv * 1280 + (btile(0) * 32 + (w == 1 ? lane & 31 : lane)) * 8);
    float inv = misc[4 + (s & 1)], xmax = misc[s & 3];
    unsigned mp = (unsigned)(L_BM + lane * 16);
    int sidx = (lane & 8) ? 0xEEEE : 0x4444;
    f32x4 acc[2] = {Z4, Z4};
    PIN_V(inv); PIN_V(mp);
    if (w == 0) PIN_V(xmax);
    else { PIN_V(vp); PIN_V(sidx); PIN_V(acc[1]); }
    if (w >= 2) PIN_V(acc[0]);
    __builtin_amdgcn_s_setprio(2);
    spin_cyc(t0);
    STAMP(5);
    bf16x8 x[10];
#pragma unroll
    for (int p = 0; p < 10; ++p) x[p] = *(const bf16x8*)(lds + mp + p * 1024);
    __builtin_amdgcn_sched_barrier(0);
#pragma unroll
    for (int p = 0; p < 10; ++p) {
      if (w == 0) acc[0] = mfma32(x[p], x[p], acc[0]);
#pragma unroll
      for (int i = (w == 1); i < 2 && w != 0; ++i) acc[i] = smfma64(x[p], W[i][p], acc[i], sidx);
      __builtin_amdgcn_sched_barrier(0);
    }
    STAMP(7);
    if (w != 0) {
      float v[4];
#pragma unroll
      for (int r = 0; r < 4; ++r) {
        const auto sw = __builtin_amdgcn_permlane32_swap(f2u(acc[w == 1][r]), f2u(acc[1][r]), false, false);
        v[r] = (u2f(sw[0]) + u2f(sw[1])) * inv;
      }
      *(u32x2*)(lds + vp) = (u32x2){pack2(v[0], v[1]), pack2(v[2], v[3])};
      lds_inc<L_MISC + 28>(lds0);
    } else {
      const f32x4 gr = acc[0];
      const int r = c & 3;
      float n2 = (r & 1) ? gr[1] : gr[0], n3 = (r & 1) ? gr[3] : gr[2];
      PIN_V(n2); PIN_V(n3);
      n2 = (r & 2) ? n3 : n2;
      n2 = (c >> 2) == g ? (n2 * inv) * inv : 0.f;
      n2 = x32sum(n2 + dppf<DPP_ROR8>(n2));
      n2 = fmx(n2, dppf<DPP_XOR1>(n2)); n2 = fmx(n2, dppf<DPP_XOR2>(n2)); n2 = fmx(n2, dppf<DPP_ROR4>(n2)); n2 = fmx(n2, dppf<DPP_ROR8>(n2));
      n2 = x16mx(n2);
      const float yb = __builtin_amdgcn_sqrtf(cb2 * fmx(n2, xmax));
      int e = (int)((f2u(yb) >> 23) & 0xffu) + 1;
      e = e < 32 ? 32 : e;
      const float scl = u2f((unsigned)(279 - e) << 23), thr = (scl > 0x1p-14f && scl < 0x1p66f) ? REC_THR : -1.0f;
      misc[4 + ((s + 1) & 1)] = u2f((unsigned)(e - 31) << 23); *(f32x2*)(misc + 8) = (f32x2){scl * 64.0f, thr};
    }
    STAMP(8);
    __builtin_amdgcn_s_setprio(0);
    if (last == 0) {
      if (w >= 2) { VM_DRAIN(); lds_inc<L_MISC + 112>(lds0); }
      if (w != 1) lds_inc<L_MISC + 52>(lds0);
    } else if (w == 2) { VM_DRAIN(); lds_inc<L_MISC + 28>(lds0); }
  };
  unsigned* const cntq = (unsigned*)(misc + 12);
  unsigned* const cntt = (unsigned*)(misc + 26 + (w & 1));
  unsigned* const cntp = (unsigned*)(misc + 23);
  unsigned* const cntx2 = (unsigned*)(misc + 28);
  float xn4 = 0.f;
  auto late = [&](int s, int lane) {
    if (w == 2 || (w == 0 && F4) || (w == 1 && F5)) lds_spin<L_MISC + 48, 1>(lds0, 2u * (unsigned)imax(s + 1, 0));
    if (w == 2) {
      ot.load(a, rs, s - LAG5, b);
      xn4 = u2f(__hip_atomic_load((const GAS unsigned*)xnrm + (imax(s + 1, 0) * 32 + (lane & 31)) * 4, RLX_AGENT));
    }
    if (w >= 2) return;
    x_fetch_n<w == 0 ? F4 : F5>(x_src(a, s + 3, b), lds0, s + 3, w == 0 ? 0 : F4, lane * 16);
    if (w == 0 && __builtin_expect(s >= WPF0 && s < WPF0 + 10, 0))
      touch(a.ws + OFF_WQKVBT + (size_t)(b >> 3) * 76800 + (s - WPF0) * 7680, imin(lane, 59) * 128, lds0);
    if (w == 1 && __builtin_expect(s == CPF_S, 0)) code_touch(b, lane, lds0);
    lds_spin<L_MISC + 104 + 4 * (w & 1), PS_SL>(lds0, (unsigned)imax(s + 1, 0));
  };
  auto xn_put = [&](int s) {
    float xn2 = xn4;
    xn2 = fmx(xn2, dppf<DPP_XOR1>(xn2)); xn2 = fmx(xn2, dppf<DPP_XOR2>(xn2));
    xn2 = fmx(xn2, dppf<DPP_ROR4>(xn2)); xn2 = fmx(xn2, dppf<DPP_ROR8>(xn2));
    misc[s & 3] = x16mx(xn2);
  };
  if (w == 2) { ot.init(b, lane0); ot.out = (u32x2){0u, 0u}; }
  VM_DRAIN();
  lds_arrive(cnt, lane0);
  lds_wait(cnt, 8u);
  Sched sch;
  int sv = NV - 1;
  CODE_PAD((PADT >> (4 * w)) & 15);
#pragma unroll 1
  for (int s = -1; s < NSEG - 1; ++s) {
    const int sv1 = sv == NV - 1 ? 0 : sv + 1;
    {
      FRESH_LANE();
      STAMP(0);
      if (F67) lds_spin<L_MISC + 112, 1>(lds0, 2u * (unsigned)imax(s, 0));
      mma_seg(s + 1, sv1, lane, [&]() { STAMP(1); late(s - 1, lane); STAMP(9); });
      STAMP(2);
      __builtin_amdgcn_sched_barrier(0);
      VM_DRAIN();
      u32x2 keep;
      if (w == 2) { keep = ot.out; ot.run(lane); xn_put(s); const u32x2 t = ot.out; ot.out = keep; keep = t; }
      if (w >= 2) { lds_spin<L_MISC + 92, 1>(lds0, 4u * (unsigned)imax(s, 0)); if (W1_D) spin_cyc(cyc32() + W1_D); }
      STAMP(10);
      st_out(s, lane);
      if (w == 2) ot.out = keep;
      __builtin_amdgcn_sched_barrier(0);
      if (w == 2 && XPF) touch(a.ws + OFF_XN + (size_t)imin(imax(s + 3, s < 2 ? 0 : 5), NSEG - 1) * (s < 2 ? 0 : 40960) + (b >> 3) * 1280, imin(lane, 9) * 128, lds0);
      if (w >= 2 && s > 0) x_fetch_n<F67>(x_src(a, s + 2, b), lds0, s + 2, F4 + F5 + (w - 2) * F67, lane * 16);
      STAMP(3);
    }
    BAR();
    {
      FRESH_LANE();
      STAMP(4);
      const unsigned tb1 = cyc32();
      if (__builtin_expect(s >= 0, 1)) {
        if (w == SCH_W) { sch.load(rs, s, lane); __builtin_amdgcn_sched_barrier(0); }
        vo_mem(s, sv, tb1 + (s > 0 ? (w == 0 ? BG_HOLD4 : BG_HOLD) : 0), lane, std::integral_constant<int, 0>{});
        STAMP(6);
        if (w == SCH_W) { *(unsigned*)(misc + 10) = sch.run(a, rs, s, b, lane); lds_inc<L_MISC + 88>(lds0); }
        STAMP(11);
        ARRIVE(0);
      } else {
        if (w == 1) t2_tok(lds, 0, lane);
        BAR();
      }
    }
    ARRIVE(1);
    ARRIVE_OUT();
    sv = sv1;
  }
  STAMP_OUT();
  late(NSEG - 2, lane0);
  if (w >= 2) {
    lds_wait(cntp, 4u * (NSEG - 1));
    if (w == 2) { const u32x2 keep = ot.out; ot.run(lane0); ot.store(a, NSEG - 2 - LAG5, b, lane0); ot.out = keep; }
    __builtin_amdgcn_sched_barrier(0);
    PvTask pv;
    pv.load(rs, __builtin_amdgcn_readfirstlane(*(const unsigned*)(misc + 11)), NSEG - 2 - LAG4, gp, w & 1, lane0);
    st_out(NSEG - 1, lane0);
    pv.run(lds, rs, NSEG - 2 - LAG4, gp, m, w & 1, lane0, true);
    x_fetch_n<F67>(x_src(a, NSEG + 1, b), lds0, NSEG + 1, F4 + F5 + (w - 2) * F67, lane0 * 16);
  }
  BAR();
  if (w != 0) vo_mem(NSEG - 1, (NSEG - 1) % NV, cyc32(), lane0, std::integral_constant<int, 1>{});
}

__device__ __forceinline__ void qkv_tail(const RecArgs& a, char* lds, const int w) {
  const int lane0 = __builtin_amdgcn_workitem_id_x() & 63, b = __builtin_amdgcn_workgroup_id_x();
  const int r = qkv_row(b), cb = (b >> 3) - 15 * (r & 1);
  if (cb >= 15) return;
  const unsigned lds0 = (unsigned)(uintptr_t)(LAS char*)lds;
  unsigned* const stage = (unsigned*)(lds + L_MISC) + 17;
  unsigned* const cnty = stage + 1;
  const f32x4 Z4 = {0.f, 0.f, 0.f, 0.f};
  auto fetch = [&](int i) {
    FRESH_LANE();
#pragma unroll
    for (int p = 0; p < 10; ++p) x_fetch(x_src(a, NSEG + i, b), lds0, i, 10 * w + p, lane);
  };
  unsigned want = 0;
  auto sync = [&]() { VM_DRAIN(); want += 4u; lds_inc<L_MISC + 72>(lds0); while (__hip_atomic_load(cnty, __ATOMIC_RELAXED, __HIP_MEMORY_SCOPE_WORKGROUP) < want) { } asm volatile("" ::: "memory"); };
  LOGQ(0);
  bf16x8 W[2][20];
#pragma unroll
  for (int k = 0; k < 40; ++k) {
    if (QBATCH) asm volatile("s_waitcnt vmcnt(%0)" :: "n"(QBATCH) : "memory");
    W[k & 1][k >> 1] = *(const bf16x8*)(a.ws + OFF_WQKVBT + ((size_t)(cb * 8 + 2 * w + (k & 1)) * 20 + (k >> 1)) * 1024 + lane0 * 16);
    __builtin_amdgcn_sched_barrier(0);
  }
  bf16x8 ones = mk8(0x3f803f80u, 0x3f803f80u, 0x3f803f80u, 0x3f803f80u);
  PIN_V(ones);
  const __amdgpu_buffer_rsrc_t ro = __builtin_amdgcn_make_buffer_rsrc(a.ws + OFF_QB, 0, (int)(OFF_OB - OFF_QB), 0x00020000);
  const int t0 = w & 1, col0 = (cb % 5) * 128 + w * 32;
  auto body = [&](auto SW, auto SQ) {
    unsigned vo[2][2];
    {
      FRESH_LANE();
#pragma unroll
      for (int i = 0; i < 2; ++i)
#pragma unroll
        for (int tt = 0; tt < 2; ++tt) {
          const int tq = tt ^ t0, tk = tq * 16 + c, cc = col0 + i * 16 + (SW ? 4 * g : c), hd = cc / BD, e = cc - hd * BD;
          const int kv = (int)(OFF_KV - OFF_QB) + hd * 64 * KVT;
          vo[i][tt] = (unsigned)(!SW ? kv + 1280 + tq * 672 + ((g & 1) * 21 + e) * 16 + (g >> 1) * 8
                                     : (cb < 5 ? (hd * S_ + tk) * 2 * QST + e * 2 : kv + (e < 16 ? (tk + 32 * (e >> 3)) * 16 + (e & 7) * 2 : 1024 + tk * 8)));
          PIN_V(vo[i][tt]);
        }
    }
    const float qs = cb < 5 ? 0.22360679774997896f * 1.4426950408889634f : 1.0f;
    f32x4 cs[2] = {Z4, Z4};
    LOGQ(1);
#pragma unroll 1
    for (int it = 0; it < 4; ++it) {
      const char* xp0 = lds + L_XN + (it & 1) * 40960 + t0 * 20480 + lane0 * 16;
      const char* xp1 = lds + L_XN + (it & 1) * 40960 + (1 - t0) * 20480 + lane0 * 16;
      f32x4 acc[2][2] = {{Z4, Z4}, {Z4, Z4}}, st = Z4;
      bf16x8 x[4][2];
#pragma unroll
      for (int p = 0; p < 3; ++p) { x[p][0] = *(const bf16x8*)(xp0 + p * 1024); x[p][1] = *(const bf16x8*)(xp1 + p * 1024); }
#pragma unroll
      for (int ks = 0; ks < 20; ++ks) {
        if (ks + 3 < 20) { x[(ks + 3) & 3][0] = *(const bf16x8*)(xp0 + (ks + 3) * 1024); x[(ks + 3) & 3][1] = *(const bf16x8*)(xp1 + (ks + 3) * 1024); }
        st = mfma32(SQ ? x[ks & 3][0] : ones, x[ks & 3][0], st);
#pragma unroll
        for (int tt = 0; tt < 2; ++tt)
#pragma unroll
          for (int i = 0; i < 2; ++i) acc[i][tt] = SW ? mfma32(W[i][ks], x[ks & 3][tt], acc[i][tt]) : mfma32(x[ks & 3][tt], W[i][ks], acc[i][tt]);
        __builtin_amdgcn_sched_barrier(0);
      }
      LOGQ(2 + 3 * it);
      FRESH_LANE();
      float* const sp = (float*)(lds + L_ST) + (it & 1) * 64;
      if (SQ) { if ((c >> 2) == g) sp[32 + t0 * 16 + c] = (c & 3) == 0 ? st[0] : ((c & 3) == 1 ? st[1] : ((c & 3) == 2 ? st[2] : st[3])); }
      else if (g == 0) sp[t0 * 16 + c] = st[0];
      if (it == 0) {
#pragma unroll
        for (int ks = 0; ks < 20; ++ks)
#pragma unroll
          for (int i = 0; i < 2; ++i) cs[i] = SW ? mfma32(W[i][ks], ones, cs[i]) : mfma32(ones, W[i][ks], cs[i]);
      }
      sync();
      LOGQ(3 + 3 * it);
      if (it == 0) fetch(2);
      const bool late = r + 48 >= 55;
      if (it == 1 && !late) fetch(3);
      const bool now = it == 2 && late && __builtin_amdgcn_readfirstlane(*(volatile unsigned*)stage) != 0u;
      if (now) fetch(3);
      const unsigned so = (unsigned)((r + 16 * it) * (cb < 5 ? 64 * QST : KVT));
      auto stat = [&](float sm, float sq, float& mean, float& rstd) {
        mean = sm * (1.0f / DM);
        rstd = __builtin_amdgcn_rsqf(__builtin_fmaxf(sq * (1.0f / DM) - mean * mean, 0.f) + 1e-5f);
      };
#pragma unroll
      for (int tt = 0; tt < 2; ++tt) {
        const int tq = tt ^ t0;
        float mean[4], rstd[4];
        if (SW) {
          stat(sp[tq * 16 + c], sp[32 + tq * 16 + c], mean[0], rstd[0]);
          rstd[0] *= qs;
        } else {
          const f32x4 sm = *(const f32x4*)(sp + tq * 16 + 4 * g), sq = *(const f32x4*)(sp + 32 + tq * 16 + 4 * g);
#pragma unroll
          for (int k = 0; k < 4; ++k) stat(sm[k], sq[k], mean[k], rstd[k]);
        }
#pragma unroll
        for (int i = 0; i < 2; ++i) {
          float v[4];
#pragma unroll
          for (int k = 0; k < 4; ++k) v[k] = (acc[i][tt][k] - mean[SW ? 0 : k] * cs[i][SW ? k : 0]) * rstd[SW ? 0 : k];
          __builtin_amdgcn_raw_buffer_store_b64((u32x2){pack2(v[0], v[1]), pack2(v[2], v[3])}, ro, vo[i][tt], so, 0);
        }
      }
      LOGQ(4 + 3 * it);
      if (it == 2 && late) {
        if (!now) { lds_wait(stage, 1u); fetch(3); }
        sync();
      }
    }
  };
  typedef std::integral_constant<bool, false> F_; typedef std::integral_constant<bool, true> T_;
  if (cb < 10) { if (w >> 1) body(T_{}, T_{}); else body(T_{}, F_{}); }
  else { if (w >> 1) body(F_{}, T_{}); else body(F_{}, F_{}); }
}

struct TokTask {
  u32x4 y[2]; u32x2 ym; u32x4 res;
  unsigned l0, l1, lm;
  static __device__ __forceinline__ bool ok(int sl) { return sl >= 0 && sl < NSEG; }
  __device__ __forceinline__ void init(int h, int j, int lane) {
    const int c = lane & 15, g = lane >> 4, qc = 4 * (j & 3) + (c >> 2);
    l0 = ts_tok(h, g) + (unsigned)((j >> 2) * 1024 + ((c & 3) * 16 + qc) * 16); l1 = l0 + 16384u;
    lm = ts_mem(h, g + 4 * (c & 1)) + (unsigned)((j >> 2) * 256 + (((c >> 1) & 1) * 16 + qc) * 8);
    res = (u32x4){0u, 0u, 0u, 0u};
    PIN_V(l0); PIN_V(l1); PIN_V(lm);
  }
  __device__ __forceinline__ void load(const __amdgpu_buffer_rsrc_t rs, unsigned o) {
    y[0] = SC1_LOADS(l0, o); y[1] = SC1_LOADS(l1, o);
    ym = __builtin_amdgcn_raw_buffer_load_b64(rs, lm, o, 16);
  }
  __device__ __forceinline__ bool ready() { return wave_all(umin(umin(min4(y[0]), min4(y[1])), umin(ym[0], ym[1])) != 0u); }
  __device__ __forceinline__ void run(int j, int lane, const bf16x8 SelLo) {
    const int c = lane & 15, g = lane >> 4;
    const f32x4 Z4 = {0.f, 0.f, 0.f, 0.f};
    const f32x4 d1 = mfma32(SelLo, as8(y[1]), mfma32(SelLo, as8(y[0]), Z4));
    const f32x4 d0 = mfma32(SelLo, mk8(ym[0], ym[1], 0u, 0u), Z4);
    const int qi = 4 * j + (c >> 2), k0 = 16 * g + 4 * (c & 3);
    float t[4], mv[4];
    float mx = -INFINITY;
#pragma unroll
    for (int r = 0; r < 4; ++r) {
      t[r] = (g < 2 && k0 + r <= qi) ? d1[r] : -INFINITY;
      const float mt = d0[r] + dppf<DPP_XOR1>(d0[r]);
      mv[r] = (g == 0 && (c & 1) == 0) ? mt : -INFINITY;
      mx = fmx(mx, fmx(t[r], mv[r]));
    }
    mx = fmx(mx, dppf<DPP_XOR1>(mx)); mx = fmx(mx, dppf<DPP_XOR2>(mx));
    mx = x16mx(mx);
    float sum = 0.f;
#pragma unroll
    for (int r = 0; r < 4; ++r) {
      t[r] = __builtin_amdgcn_exp2f(t[r] - mx); mv[r] = __builtin_amdgcn_exp2f(mv[r] - mx);
      sum += t[r] + mv[r];
    }
    sum += dppf<DPP_XOR1>(sum); sum += dppf<DPP_XOR2>(sum);
    sum = x16sum(sum);
    const float rs_ = __builtin_amdgcn_rcpf(sum);
    res = (u32x4){pack2n(t[0] * rs_, t[1] * rs_), pack2n(t[2] * rs_, t[3] * rs_), pack2n(mv[0] * rs_, mv[1] * rs_), pack2n(mv[2] * rs_, mv[3] * rs_)};
    PIN_V(res);
  }
  __device__ __forceinline__ void store(const __amdgpu_buffer_rsrc_t rs, unsigned ck, int sl, int h, int j, int lane) {
    const int c = lane & 15, g = lane >> 4, qi = 4 * j + (c >> 2);
    const unsigned o = pt_slot(ck, sl);
    SC1_STORE8(((u32x2){res[0], res[1]}), ok(sl) && g < 2 ? o + pt_tok(h) + (qi >> 4) * 1024 + ((c & 3) * 16 + (qi & 15)) * 16 + g * 8 : 0x7ffffff0u);
    SC1_STORE8(((u32x2){res[2], res[3]}), ok(sl) && g == 0 && (c & 1) == 0 ? o + pt_mem(h) + (qi >> 4) * 256 + (((c >> 1) & 1) * 16 + (qi & 15)) * 8 : 0x7ffffff0u);
  }
};

__device__ __forceinline__ void t2_mem(const bf16x8 fr, const bf16x8 q0, const bf16x8 q1, const __amdgpu_buffer_rsrc_t rs, unsigned o, int h, int j, float inv, int lane) {
  const f32x4 Z4 = {0.f, 0.f, 0.f, 0.f};
  const f32x4 d0 = mfma32(fr, q0, Z4) * inv, d1 = mfma32(fr, q1, Z4) * inv;
  const unsigned a0 = pack2n(d0[0], d0[1]), a1 = pack2n(d0[2], d0[3]), b0 = pack2n(d1[0], d1[1]), b1 = pack2n(d1[2], d1[3]);
  const auto r0 = __builtin_amdgcn_permlane32_swap(a0, b0, false, false), r1 = __builtin_amdgcn_permlane32_swap(a1, b1, false, false);
  SC1_STORE8S(((u32x2){r0[1], r1[1]}), ts_mem(h, j) + lane * 8, o);
}

template <int w>
__device__ __forceinline__ void rec_waves(const RecArgs& a, char* lds, const __amdgpu_buffer_rsrc_t rs) {
  const int lane = __builtin_amdgcn_workitem_id_x() & 63, b = __builtin_amdgcn_workgroup_id_x(), c = lane & 15, g = lane >> 4, wid = w; (void)wid;
  const int gp = (b & 7) * 2 + (b >> 7), m = (b >> 3) & 15, h0 = 2 * gp, h = h0 + (m >> 3), j = m & 7;
  float* const misc = (float*)(lds + L_MISC);
  const f32x4 Z4 = {0.f, 0.f, 0.f, 0.f};
  LOGP(0);
  __builtin_amdgcn_s_setprio(3);
  const unsigned lds0 = (unsigned)(uintptr_t)(LAS char*)lds;
  if (!a.layer) img_fetch<12>(a, lds0, h, j, w, lane);
  constexpr int NC = w < 2 ? 2 : 3;
  const int far = ((b >> 2) & 1) ^ 1;
  auto chunk = [&](int f) { return f == 0 ? 4 * far + w : (w == 2 && f == 1 ? 8 + far : (w == 3 && f == 2 ? 9 - far : 4 - 4 * far + w)); };
  bf16x16 Wqk[3][NC];
  u32x4 bm0[NC][2];
#pragma unroll
  for (int f = 0; f < 2 * NC; ++f) bm0[f >> 1][f & 1] = *(const u32x4*)(a.mem0 + (size_t)(c & 7) * DM + 64 * chunk(f >> 1) + 16 * g + 8 * (f & 1));
  if (!a.layer) asm volatile("s_waitcnt vmcnt(0)" ::: "memory");
  BAR();
  LOGP(9);
  const unsigned ck = __builtin_amdgcn_readfirstlane(*(const unsigned*)(misc + 11));
  const unsigned flip = (ck ^ (unsigned)(b << 11)) & 8192u;
#pragma unroll
  for (int t = 0; t < 3; ++t)
#pragma unroll
    for (int f = 0; f < NC; ++f) Wqk[t][f] = cat16(wqk_frag(lds, t, 2 * chunk(f), lane), wqk_frag(lds, t, 2 * chunk(f) + 1, lane));
  if (!a.layer) lds_arrive((unsigned*)(misc + 6), lane);
  else {
    if (w == 0) {
      for (unsigned spins = 0; spins < SPIN_CAP; ++spins) {
        if (__builtin_amdgcn_raw_buffer_load_b32(rs, (b & 7) * 256, (unsigned)OFF_RDY, 16) >= 16u) break;
        __builtin_amdgcn_s_sleep(4);
        asm volatile("" ::: "memory");
      }
      lds_inc<L_MISC + 84>(lds0);
    }
    lds_wait((unsigned*)(misc + 21), 2u);
#pragma unroll
    for (int f = 0; f < 2 * NC; ++f) PIN_V(bm0[f >> 1][f & 1]);
#pragma unroll
    for (int i = 0; i < 10; ++i) x_fetch(x_src(a, 0, b), lds0, 0, 10 * w + i, lane);
  }
  const bf16x8 SelLo = sel_frag(c, 0), SelHi = sel_frag(c, 8);
  bf16x8 ONES = mk8(0x3f803f80u, 0x3f803f80u, 0x3f803f80u, 0x3f803f80u);
  if (w < 2) PIN_V(ONES);
  bool dead = false;
  float inv = 1.0f;
  unsigned* const cnt2 = (unsigned*)(misc + 7);
  unsigned* const cntv = (unsigned*)(misc + 13);
  unsigned l2off[2 * NC];
#pragma unroll
  for (int f = 0; f < 2 * NC; ++f) { l2off[f] = acc_off(16 * chunk(f >> 1) + 4 * g + 2 * (f & 1) + (c >> 3), c & 7) ^ flip; PIN_V(l2off[f]); }
  int sidx = (c >> 3) ? 0xEEEE : 0x4444;
  PIN_V(sidx);
  const unsigned p1off = (unsigned)((w == 0 ? (h & 2) * 1024 + j * 256 + (h & 1) * 64 : (h & 3) * 32768 + j * 512 + (g >> 1) * 256 + (w - 1) * 64) + (c & 4) * 32 + (c & 3) * 16 + (g & 1) * 8);
  unsigned p1v = (c < 8) && (w != 0 || g >= 2) ? p1off : 0x7ffffff0u, p1cur = 0x7ffffff0u;
  PIN_V(p1v); PIN_V(p1cur);
  const int hd = (c >> 2) & 1, mr = hd ? 3 - (c & 3) : (c & 3);
  const unsigned l1off = (unsigned)(((h0 + hd) & 3) * 32768 + g * 512 + (w & 1) * 128 + (c >> 3) * 64 + mr * 16);
  const unsigned l1offm = (unsigned)((h0 & 2) * 1024 + (g + 4 * (c >> 3)) * 256 + (w & 1) * 128 + hd * 64 + mr * 16);
  const int plane = (lane & ~6) | ((lane & 2) << 1) | ((lane & 4) >> 1);
  unsigned acco = c < 8 ? (acc_off(64 * far + m * 4 + g, 4 * (w & 1) + mr) + (unsigned)(hd * 8)) ^ flip : 0x7ffffff0u;
  unsigned acco2 = c >= 8 ? (acc_off(64 - 64 * far + m * 4 + g, 4 * (w & 1) + mr) + (unsigned)(hd * 8)) ^ flip
                          : (c < 4 ? (acc_off(128 + m * 2 + (g & 1), 4 * (w & 1) + mr) + (unsigned)((g >> 1) * 8)) ^ flip : 0x7ffffff0u);
  if (w < 2) { PIN_V(acco); PIN_V(acco2); }
  unsigned bmo[NC];
#pragma unroll
  for (int f = 0; f < NC; ++f) { bmo[f] = (unsigned)(chunk(f) * 1024 + lane * 16); PIN_V(bmo[f]); }
  bf16x8 bm[NC];
  auto bm_put = [&](int f) { *(bf16x8*)(lds + L_BM + bmo[f]) = bm[f]; __builtin_amdgcn_sched_barrier(0); };
  bf16x8 kx;
  f32x4 acc[3] = {Z4, Z4, Z4};
  float* const redp = (float*)(lds + L_RED1 + w * 1792) + 4 * g * 28 + c;
  float* const redp2 = (float*)(lds + L_RED1 + w * 1792) + ((c >> 3) * 8 + 4 * (g & 1) + 2 * (g >> 1)) * 28 + 16 + (c & 7);
  auto red_put = [&]() {
    auto hs = [&](float x, float y) { const auto sw = __builtin_amdgcn_permlane32_swap(f2u(x), f2u(y), false, false); return u2f(sw[0]) + u2f(sw[1]); };
#pragma unroll
    for (int i = 0; i < 4; ++i) redp[i * 28] = hs(acc[0][i], acc[1][i]);
#pragma unroll
    for (int i = 0; i < 2; ++i) redp2[i * 28] = hs(acc[2][i], acc[2][i + 2]);
  };
#pragma unroll
  for (int f = 0; f < NC; ++f) {
    const u32x4 u = bm0[f][0], v = bm0[f][1];
    bm[f] = c < 8 ? mk8(u[0], u[2], v[0], v[2]) : mk8(u[1], u[3], v[1], v[3]);
    bm_put(f);
#pragma unroll
    for (int t = 0; t < 3; ++t) acc[t] = smfma64(bm[f], Wqk[t][f], acc[t], sidx);
  }
  red_put();
  VM_DRAIN();
  if (a.layer) lds_arrive((unsigned*)(misc + 6), lane);
  lds_wait((unsigned*)(misc + 6), 8u);
#pragma unroll
  for (int i = 0; i < 10; ++i) x_fetch(x_src(a, 1, b), lds0, 1, 10 * w + i, lane);
  LOGP(1);
  BAR();
  STAMP_DECL(); ARRIVE_DECL();
  bf16x8 fr, qx[2];
  auto kq_get = [&](int seg) {
    if (w == 1 || w == 2) kx = *(const bf16x8*)(lds + L_KX + (seg % NKQ) * 2048 + (w - 1) * 1024 + lane * 16);
    if (w == T2W || (w == 3 && seg == NSEG - 1)) { qx[0] = *(const bf16x8*)(lds + L_QX + (seg % NKQ) * 2048 + lane * 16); qx[1] = *(const bf16x8*)(lds + L_QX + (seg % NKQ) * 2048 + 1024 + lane * 16); }
  };
  auto pub1 = [&](unsigned off, unsigned o1, auto mid) {
    fr = qk_frag(lds, g, c, mid);
    const f32x4 sc = mfma32(w == 0 ? fr : kx, fr, Z4);
    const u32x2 v = {nz(pack2(sc[0], sc[1])), nz(pack2(sc[2], sc[3]))};
    SC1_STORE8S(v, off, o1);
  };
  VM_DRAIN();
  kq_get(0);
  unsigned o1 = sct_slot(ck, 0u), o1m = scm_slot(ck, 0u);
  if (w < 3) pub1(p1v, w == 0 ? o1m : o1, []() {});
  const unsigned tst = now32() | 1u;
  if (w == 3 && ST_X) __builtin_amdgcn_raw_buffer_store_b32(tst, rs, lane == 0 ? (b >> 2) * 256 + (b & 3) * 4 : 0x7ffffff0, (unsigned)(OFF_ACC + (SR - 1) * ACC_STEP) + ACC_TS, 16);
  LOGP(2);
  BAR();
  LOGP(3);
#pragma unroll
  for (int i = 0; i < 10; ++i) x_fetch(x_src(a, 2, b), lds0, 2, 10 * w + i, lane);
  VM_DRAIN();
  unsigned o2 = (unsigned)OFF_ACC, o2p = 0u, tw = 0u;
  asm volatile("" : "+s"(o2p));
  unsigned tc0 = 0u, tstar = cyc32();
  int dmin = 0;
  int e1 = 0;
  unsigned offs = 1u;
  TokTask tk;
  OutTask ot;
  PvTask pv;
  PvTask2 pv2;
  if (w == 3) pv2.init(gp, lane);
  if (w == 2) tk.init(h, j, lane);
  unsigned o3 = ts_slot(ck, 3), o3p = 0u;
  u32x4 pmz = {0u, 0u, 0u, 0u}, vom[5];
  PIN_V(pmz);
#pragma unroll
  for (int k = 0; k < 5; ++k) { vom[k] = pmz; if (w < 2) PIN_V(vom[k]); }
  unsigned tso = lane == 0 ? (unsigned)((b >> 2) * 256 + (b & 3) * 4) : 0x7ffffff0u;
  if (w == 3) PIN_V(tso);
  float minf = g >= 2 ? -INFINITY : 0.f;
  if (w < 2) PIN_V(minf);
  int sv = NV - 1;
  bf16x8 vox[5];
  unsigned vma[3], need2, needv;
  u32x4 slo, shi;
  float k1, va0, va1, vb0, vb1;
  const u16* xsrc;
  auto prep = [&](int s) {
    o1 = sct_slot(ck, s % SR); o1m = scm_slot(ck, s % SR); o2 = (unsigned)OFF_ACC + (unsigned)(s % SR) * (unsigned)ACC_STEP;
    xsrc = x_src(a, s + 3, b);
    PIN_S(o1); PIN_S(o1m); PIN_S(o2); PIN_S(xsrc);
    if (w == 2) { o3p = o3; o3 = ts_slot(ck, s); PIN_S(o3); PIN_S(o3p); }
    sv = sv == NV - 1 ? 0 : sv + 1;
    PIN_S(sv);
    if (w != 3) kq_get(s);
    if (w >= 2) return;
#pragma unroll
    for (int k = 0; k < 3; ++k) {
      const int t = k == 0 ? 2 : (k == 1 ? far : 1 - far);
      const char* p = lds + L_VOX + sv * 5120 + t * 1024 + plane * 16;
      vox[k == 0 ? 0 : 2 * k - 1] = *(const bf16x8*)p;
      if (k) vox[2 * k] = *(const bf16x8*)(p + 3072);
      vma[k] = (unsigned)(L_VOM + sv * 1280 + t * 256 + (plane & 31) * 8);
      PIN_V(vma[k]);
    }
    slo = __builtin_bit_cast(u32x4, SelLo); shi = __builtin_bit_cast(u32x4, SelHi);
#pragma unroll
    for (int i = 0; i < 4; ++i) { slo[i] = ((slo[i] >> 7) & 0x10001u) * (f2u(inv) >> 16); shi[i] = ((shi[i] >> 7) & 0x10001u) * (f2u(inv) >> 16); }
    k1 = KEXP * inv;
    va0 = 32.0f; va1 = 0.f; vb0 = 32.0f; vb1 = 0.f;
    need2 = 3u * (unsigned)(s + 1); needv = 5u * (unsigned)(s + 1) - 1u;
    PIN_V(k1); PIN_V(slo); PIN_V(shi); PIN_V(va0); PIN_V(va1); PIN_V(vb0); PIN_V(vb1); PIN_S(need2); PIN_S(needv);
    asm volatile("" : "+v"(vox[0]), "+v"(vox[1]), "+v"(vox[2]), "+v"(vox[3]), "+v"(vox[4]));
  };
  auto rows = [&](int s) {
    const int dl = spin_until(tw);
    if (w < 2) tc0 = cyc32();
    __builtin_amdgcn_sched_barrier(0);
    STAMP(1);
    unsigned spins = 0;
    auto pass = [&](bool first) {
      u32x4 x[2 * NC];
#pragma unroll
      for (int f = 0; f < 2 * NC; ++f) x[f] = SC1_LOADS(l2off[f], o2p);
      __builtin_amdgcn_sched_barrier(0);
      if (first) lds_inc<L_MISC + 92>(lds0);
      if (w >= 2 && first && P2_W) lds_spin<L_MISC + 92, 0>(lds0, 4u * (unsigned)s);
      if (w == 2 && first) tk.store(rs, ck, s - 1 - LAG3, h, j, lane);
      if (w == 3 && first) pv2.load(rs, ck, s - 1 - LAG4);
      if (first) { prep(s); p1cur = p1v; PIN_V(p1cur); offs = dl > -8 ? offs : 1u; PIN_S(offs); }
      if (first && w < 2) {
        tstar = tc0 + (unsigned)(e1 + D1_M + (w == 1 ? D1_W1 : 0));
        dmin = offs == 0u ? D1_MIN : REC_D1;
        dmin = s < EARLY_N ? REC_D1 + EARLY_D1 : dmin;
        PIN_S(tstar); PIN_S(dmin);
      }
      __builtin_amdgcn_sched_barrier(0);
      unsigned ok = 32u;
#pragma unroll
      for (int t = 0; t < 3; ++t) { acc[t] = Z4; PIN_V(acc[t]); }
      __builtin_amdgcn_sched_barrier(0);
      float fv[8 * NC];
      unsigned p[4 * NC];
      auto op = [&](int n) {
        const int f = n / 12, i = n % 12;
        if (f >= NC) {
          const int k = n - 12 * NC;
          if (k < 2 * NC) { ok &= x[k][0] & x[k][2]; PIN_V(ok); }
        } else if (i < 8) { fv[8 * f + i] = (float)(int)x[2 * f + (i >> 2)][i & 3]; PIN_V(fv[8 * f + i]); }
        else { p[4 * f + i - 8] = pack2(fv[8 * f + 2 * (i - 8)], fv[8 * f + 2 * (i - 8) + 1]); PIN_V(p[4 * f + i - 8]); }
        __builtin_amdgcn_sched_barrier(0);
      };
      constexpr int N0 = 12 + 3 * (NC - 1) + P2_X;
#pragma unroll
      for (int n = 0; n < N0; ++n) op(n);
#pragma unroll
      for (int f = 0; f < NC; ++f) {
        bm[f] = mk8(p[4 * f], p[4 * f + 1], p[4 * f + 2], p[4 * f + 3]);
#pragma unroll
        for (int t = 0; t < 3; ++t) {
          acc[t] = smfma64(bm[f], Wqk[t][f], acc[t], sidx);
          __builtin_amdgcn_sched_barrier(0);
#pragma unroll
          for (int n = 0; n < 3; ++n) op(N0 + 9 * f + 3 * t + n);
        }
      }
      const bool r = wave_all(ok != 0u);
      __builtin_amdgcn_sched_barrier(0);
      red_put();
#pragma unroll
      for (int f = 0; f < NC; ++f) bm_put(f);
      return r;
    };
    if (__builtin_expect(!pass(true), 0)) {
      FC(0);
      do {
        if (dead || ++spins > SPIN_CAP) { dead = true; break; }
        asm volatile("" ::: "memory");
      } while (!pass(false));
    }
    STAMPV(12, spins); LOGF(1, spins);
    TSTAMP(2, now32()); TSTAMP(3, spins);
  };
  prep(0);
  CODE_PAD((PADR >> (4 * w)) & 15);
#pragma unroll 1
  for (int s = 0; s < NSEG - 1; ++s) {
    if (__builtin_expect(o2p != 0u, 1)) rows(s);
    STAMP(2);
    BAR();
    STAMP(3);
    if (w < 3) pub1(p1cur, w == 0 ? o1m : o1, [&]() { if (w == 2) { __builtin_amdgcn_sched_barrier(0); tk.load(rs, o3p); __builtin_amdgcn_sched_barrier(0); } });
    const unsigned tp1 = cyc32();
    __builtin_amdgcn_sched_barrier(0);
    if (w == 2) __builtin_amdgcn_s_setprio(W2_P);
    if (w == T2W) t2_mem(fr, qx[0], qx[1], rs, o3, h, j, inv * KEXP, lane);
    TSTAMP(0, now32());
    STAMP(4);
    __builtin_amdgcn_sched_barrier(0);
    unsigned tpub = 0;
    if (w == 2) {
      STAMP(5);
      t2_tok(lds, s + 1, lane);
      __builtin_amdgcn_sched_barrier(0);
      STAMP(6);
      ARRIVE(0);
      VM_DRAIN();
      lds_inc<L_MISC + 52>(lds0);
      if (F2B) { lds_spin<L_MISC + 48, 1>(lds0, 2u * (unsigned)(s + 1)); x_fetch_n<F2B>(xsrc, lds0, s + 3, P2B, lane * 16); }
      PIN_V(tk.y[0]); PIN_V(tk.y[1]); PIN_V(tk.ym);
      tk.run(j, lane, SelLo);
      __builtin_amdgcn_s_setprio(3);
    }
    if (w == 3) {
      VM_DRAIN();
      lds_inc<L_MISC + 52>(lds0);
      if (F3A) { spin_cyc(tp1 + F3A_D); x_fetch_n<F3A>(xsrc, lds0, s + 3, 40 - F3C - F3 - F3A, lane * 16); }
      __builtin_amdgcn_s_setprio(T4_P);
      if (!F3A || T4_D > F3A_D) spin_cyc(tp1 + T4_D);
      pv2.run(lds, sv < 1 + LAG4 ? sv + NV - 1 - LAG4 : sv - 1 - LAG4, lane);
      STAMP(11);
      __builtin_amdgcn_s_setprio(3);
      STAMP(5);
      ARRIVE(0);
      if (F3) { lds_spin<L_MISC + 48, 1>(lds0, 2u * (unsigned)(s + 1)); x_fetch_n<F3>(xsrc, lds0, s + 3, 40 - F3C - F3, lane * 16); }
      STAMP(6);
    }
    if (w < 2) {
      int dq = imin((int)(tstar - tp1), REC_D1);
      PIN_S(dq);
      spin_cyc(tp1 + (unsigned)imax(dq, dmin));
      STAMP(5);
      u32x4 y[5];
      y[4] = SC1_LOADS(l1offm, o1m);
#pragma unroll
      for (int k = 0; k < 4; ++k) { __builtin_amdgcn_sched_barrier(0); y[k] = SC1_LOADS(l1off + (k & 1) * 2048 + (k >> 1) * 256, o1); }
      __builtin_amdgcn_sched_barrier(0);
      lds_inc<L_MISC + 48>(lds0);
      {
        int e0 = (int)(tp1 - tc0), en = imax(e0 - e1, -D1_C);
        PIN_S(en);
        en = e1 + (imin(en, D1_C) >> D1_G);
        PIN_S(en); PIN_S(e0);
        en = offs == 0u ? en : e1;
        e1 = s < EARLY_N ? e0 : en;
        PIN_S(e1);
      }
      STAMP(6);
      ARRIVE(0);
      f32x2 st;
      unsigned drn;
      auto vom_get = [&](int k) {
        const u32x2 v = *(const u32x2*)(lds + vma[(k + 1) >> 1] + (k && !(k & 1) ? 768 : 0));
        vom[k][0] = v[0]; vom[k][1] = v[1];
        __builtin_amdgcn_sched_barrier(0);
      };
      lds_spin<L_MISC + 28, 0>(lds0, need2);
#pragma unroll
      for (int k = 0; k < 5; ++k) vom_get(k);
      auto st_get = [&]() { st = __builtin_bit_cast(f32x2, __hip_atomic_load((u64*)(misc + 8), __ATOMIC_RELAXED, __HIP_MEMORY_SCOPE_WORKGROUP)); };
      STAMP(11);
      __builtin_amdgcn_sched_barrier(0);
      {
        unsigned spins = 0;
        const bool lo = c < 8;
        f32x4 d1, o[5], sm;
        float m0, m1, chk;
        unsigned mn;
        auto pv_mem = [&](float a0, float a1) {
          const unsigned mm = pack2(__builtin_amdgcn_exp2f(a0), __builtin_amdgcn_exp2f(a1));
          pmz[0] = mm; pmz[1] = dpp_ror8(mm);
          PIN_V(pmz);
          return __builtin_bit_cast(bf16x8, pmz);
        };
        auto sum8 = [&]() {
          const f32x4 d0 = mfma32(as8(slo), as8(y[4]), Z4);
          mn = umin(umin(y[4][0], y[4][1]), y[4][2]);
          PIN_V(mn);
          __builtin_amdgcn_sched_barrier(0);
          d1 = mfma32(as8(slo), as8(y[0]), Z4);
          mn = umin(umin(umin(umin(mn, y[4][3]), y[0][0]), y[0][1]), y[0][2]);
          PIN_V(mn);
          __builtin_amdgcn_sched_barrier(0);
          d1 = mfma32(as8(slo), as8(y[1]), d1);
          __builtin_amdgcn_sched_barrier(0);
          m0 = (lo ? d0[0] : d0[2]) + dppf<DPP_ROR8>(lo ? d0[2] : d0[0]);
          m1 = (lo ? d0[1] : d0[3]) + dppf<DPP_ROR8>(lo ? d0[3] : d0[1]);
          m0 = __builtin_fmaf(m0, k1, minf); m1 = __builtin_fmaf(m1, k1, minf);
          PIN_V(m0); PIN_V(m1);
          __builtin_amdgcn_sched_barrier(0);
          d1 = mfma32(as8(shi), as8(y[2]), d1);
          __builtin_amdgcn_sched_barrier(0);
          float e0 = __builtin_amdgcn_exp2f(m0), e1 = __builtin_amdgcn_exp2f(m1);
          PIN_V(e0); PIN_V(e1);
          __builtin_amdgcn_sched_barrier(0);
          d1 = mfma32(as8(shi), as8(y[3]), d1);
          __builtin_amdgcn_sched_barrier(0);
          pmz[0] = pack2(e0, e1);
          PIN_V(pmz);
          mn = umin(umin(umin(umin(mn, y[0][3]), y[1][0]), y[1][1]), y[1][2]);
          PIN_V(mn);
          __builtin_amdgcn_sched_barrier(0);
          pmz[1] = dpp_ror8(pmz[0]);
          PIN_V(pmz);
          __builtin_amdgcn_sched_barrier(0);
          mn = umin(umin(umin(umin(mn, y[1][3]), y[2][0]), y[2][1]), y[2][2]);
          PIN_V(mn);
          asm volatile("" : "+v"(vom[0]), "+v"(vom[1]), "+v"(vom[2]), "+v"(vom[3]), "+v"(vom[4]));
          __builtin_amdgcn_sched_barrier(0);
          const bf16x8 pm = __builtin_bit_cast(bf16x8, pmz);
          sm = mfma32(ONES, pm, Z4);
          __builtin_amdgcn_sched_barrier(0);
          drn = __hip_atomic_load(cntv, __ATOMIC_RELAXED, __HIP_MEMORY_SCOPE_WORKGROUP);
          asm volatile("" ::: "memory");
          st_get();
          __builtin_amdgcn_sched_barrier(0);
          return pm;
        };
        auto p_tok = [&](const f32x4 a, const bf16x8 pm) {
          float e[4];
#pragma unroll
          for (int i = 0; i < 4; ++i) {
            e[i] = __builtin_amdgcn_exp2f(a[i]);
            __builtin_amdgcn_sched_barrier(0);
            if (i == 1 || i == 3) o[1 + (i >> 1)] = mfma32(as8(vom[1 + (i >> 1)]), pm, Z4);
            __builtin_amdgcn_sched_barrier(0);
          }
          const unsigned t01 = pack2(e[0], e[1]), t23 = pack2(e[2], e[3]);
          __builtin_amdgcn_sched_barrier(0);
          o[3] = mfma32(as8(vom[3]), pm, Z4);
          __builtin_amdgcn_sched_barrier(0);
          const bf16x8 r = mk8(t01, t23, dpp_ror8(t01), dpp_ror8(t23));
          __builtin_amdgcn_sched_barrier(0);
          o[4] = mfma32(as8(vom[4]), pm, Z4);
          o[0] = mfma32(as8(vom[0]), pm, Z4);
          __builtin_amdgcn_sched_barrier(0);
          return r;
        };
        auto chk_a = [&]() { chk = fmx(fmx(__builtin_fabsf(d1[0]), __builtin_fabsf(d1[1])), __builtin_fabsf(d1[2])); };
        auto chk_b = [&]() { chk = fmx(fmx(chk, __builtin_fabsf(d1[3])), g < 2 ? fmx(__builtin_fabsf(m0), __builtin_fabsf(m1)) : 0.f); };
        bf16x8 pm = sum8();
        bf16x8 pt = p_tok(d1, pm);
        sm = mfma32(ONES, pt, sm);
        __builtin_amdgcn_sched_barrier(0);
        mn = umin(umin(umin(umin(mn, y[2][3]), y[3][0]), y[3][1]), y[3][2]);
        __builtin_amdgcn_sched_barrier(0);
        o[1] = mfma32(vox[1], pt, o[1]);
        __builtin_amdgcn_sched_barrier(0);
        mn = umin(umin(mn, y[3][3]), __builtin_elementwise_sub_sat(drn, needv));
        __builtin_amdgcn_sched_barrier(0);
        o[2] = mfma32(vox[2], pt, o[2]);
        __builtin_amdgcn_sched_barrier(0);
        u64 bad = __builtin_amdgcn_ballot_w64(mn == 0u);
        chk_a();
        __builtin_amdgcn_sched_barrier(0);
        float rs_ = __builtin_amdgcn_rcpf(sm[0]);
        __builtin_amdgcn_sched_barrier(0);
        o[3] = mfma32(vox[3], pt, o[3]);
        __builtin_amdgcn_sched_barrier(0);
        rs_ *= st[0];
        chk_b();
        __builtin_amdgcn_sched_barrier(0);
        o[4] = mfma32(vox[4], pt, o[4]);
        __builtin_amdgcn_sched_barrier(0);
        float rm = dppf<0x141>(rs_);
        bad |= __builtin_amdgcn_ballot_w64(!(chk <= st[1]));
        __builtin_amdgcn_sched_barrier(0);
        o[0] = mfma32(vox[0], pt, o[0]);
        __builtin_amdgcn_sched_barrier(0);
        const bool good = bad == 0u;
        if (__builtin_expect(!good, 0)) {
          mn = umin(umin(umin(min4(y[0]), min4(y[1])), umin(min4(y[2]), min4(y[3]))), min4(y[4]));
          if (!wave_all(mn != 0u) && !dead) {
            FC(1);
            do {
              if (++spins > SPIN_CAP) { dead = true; break; }
              asm volatile("" ::: "memory");
#pragma unroll
              for (int k = 0; k < 4; ++k) y[k] = SC1_LOADS(l1off + (k & 1) * 2048 + (k >> 1) * 256, o1);
              y[4] = SC1_LOADS(l1offm, o1m);
              mn = umin(umin(umin(min4(y[0]), min4(y[1])), umin(min4(y[2]), min4(y[3]))), min4(y[4]));
            } while (!wave_all(mn != 0u));
          } else FC(2);
          while (__hip_atomic_load(cntv, __ATOMIC_RELAXED, __HIP_MEMORY_SCOPE_WORKGROUP) <= needv) { }
          asm volatile("" ::: "memory");
          pm = sum8();
          chk_a(); chk_b();
          float mx = 0.f;
          if (!wave_all(chk <= st[1])) {
            mx = fmx(fmx(fmx(d1[0], d1[1]), d1[2]), fmx(fmx(d1[3], m0), m1));
            mx = fmx(mx, dppf<DPP_ROR8>(mx));
            mx = x32mx(x16mx(mx));
            pm = pv_mem(m0 - mx, m1 - mx);
            sm = mfma32(ONES, pm, Z4);
          }
          pt = p_tok(d1 - mx, pm);
          sm = mfma32(ONES, pt, sm);
#pragma unroll
          for (int k = 0; k < 5; ++k) o[k] = mfma32(vox[k], pt, o[k]);
          __builtin_amdgcn_sched_barrier(0);
#pragma unroll
          for (int k = 0; k < 12; ++k) __builtin_amdgcn_s_setprio(3);
          __builtin_amdgcn_sched_barrier(0);
          rs_ = __builtin_amdgcn_rcpf(sm[0]) * st[0];
          rm = dppf<0x141>(rs_);
          PIN_V(rs_); PIN_V(rm);
          __builtin_amdgcn_sched_barrier(0);
        }
        STAMP(7); STAMPV(13, spins); LOGF(0, spins);
        {
          STAMP(0);
          __builtin_amdgcn_sched_barrier(0);
#define FM(d, x, r, ctrl) "v_fmac_f32_dpp " d ", " x ", " r " " ctrl "\n\t"
#define ID_ "quad_perm:[0,1,2,3] row_mask:0xf bank_mask:"
#define HM_ "row_half_mirror row_mask:0xf bank_mask:"
#define R8_ "row_ror:8 row_mask:0xf bank_mask:"
#define MI_ "row_mirror row_mask:0xf bank_mask:"
          auto add = [&](float v0, float v1, unsigned off) {
            const u32x2 v = {((unsigned)(int)v0 & ~63u) | 2u, (unsigned)(int)v1};
            asm volatile("buffer_atomic_add_x2 %0, %1, %2, %3 offen" :: "v"(v), "v"(off), "s"(rs), "s"(o2) : "memory");
            __builtin_amdgcn_sched_barrier(0);
          };
          asm volatile(FM("%0", "%4", "%2", ID_ "0x1") FM("%1", "%5", "%2", ID_ "0x1") FM("%0", "%6", "%3", HM_ "0x2") FM("%1", "%7", "%3", HM_ "0x2")
                       FM("%0", "%8", "%3", HM_ "0x1") FM("%1", "%9", "%3", HM_ "0x1") FM("%0", "%10", "%2", ID_ "0x2") FM("%1", "%11", "%2", ID_ "0x2")
                       : "+v"(va0), "+v"(va1) : "v"(rs_), "v"(rm), "v"(o[1][0]), "v"(o[1][1]), "v"(o[1][2]), "v"(o[1][3]), "v"(o[2][0]), "v"(o[2][1]), "v"(o[2][2]), "v"(o[2][3]));
          __builtin_amdgcn_sched_barrier(0);
          add(va0, va1, acco);
          const auto z0 = __builtin_amdgcn_permlane32_swap(f2u(o[0][0]), f2u(o[0][2]), false, false), z1 = __builtin_amdgcn_permlane32_swap(f2u(o[0][1]), f2u(o[0][3]), false, false);
          asm volatile(FM("%0", "%4", "%2", R8_ "0x4") FM("%1", "%5", "%2", R8_ "0x4") FM("%0", "%6", "%3", MI_ "0x8") FM("%1", "%7", "%3", MI_ "0x8")
                       FM("%0", "%8", "%3", MI_ "0x4") FM("%1", "%9", "%3", MI_ "0x4") FM("%0", "%10", "%2", R8_ "0x8") FM("%1", "%11", "%2", R8_ "0x8")
                       FM("%0", "%12", "%2", ID_ "0x1") FM("%1", "%13", "%2", ID_ "0x1") FM("%0", "%14", "%3", HM_ "0x1") FM("%1", "%15", "%3", HM_ "0x1")
                       : "+v"(vb0), "+v"(vb1) : "v"(rs_), "v"(rm), "v"(o[3][0]), "v"(o[3][1]), "v"(o[3][2]), "v"(o[3][3]), "v"(o[4][0]), "v"(o[4][1]), "v"(o[4][2]), "v"(o[4][3]),
                         "v"(z0[0]), "v"(z1[0]), "v"(z0[1]), "v"(z1[1]));
          __builtin_amdgcn_sched_barrier(0);
          add(vb0, vb1, acco2);
          lds_inc<L_MISC + 104 + 4 * (w & 1)>(lds0);
          STAMP(10);
          tpub = now32();
          *(unsigned*)(misc + 14 + w) = tpub;
          lds_inc<L_MISC + 88>(lds0);
        }
      }
    }
    STAMP(8);
    ARRIVE(1);
    o2p = o2;
    const unsigned o2t = o2 + ACC_TS;
    unsigned an, iv;
    u64 tp2;
    for (;;) {
      const unsigned n = __hip_atomic_load((unsigned*)(misc + 22), __ATOMIC_RELAXED, __HIP_MEMORY_SCOPE_WORKGROUP);
      asm volatile("" ::: "memory");
      an = __hip_atomic_load((unsigned*)(misc + 10), __ATOMIC_RELAXED, __HIP_MEMORY_SCOPE_WORKGROUP);
      iv = __hip_atomic_load((unsigned*)(misc + 4 + ((s + 1) & 1)), __ATOMIC_RELAXED, __HIP_MEMORY_SCOPE_WORKGROUP);
      tp2 = __hip_atomic_load((u64*)(misc + 14), __ATOMIC_RELAXED, __HIP_MEMORY_SCOPE_WORKGROUP);
      asm volatile("" : "+v"(an), "+v"(iv), "+v"(tp2));
      if (__builtin_amdgcn_readfirstlane(n) >= 3u * (unsigned)(s + 1)) break;
      if (SP_SL && w >= 2) __builtin_amdgcn_s_sleep(1);
    }
    PIN_S(o2p);
    STAMP(9);
    {
      const unsigned t0 = __builtin_amdgcn_readfirstlane((unsigned)tp2), t1 = __builtin_amdgcn_readfirstlane((unsigned)(tp2 >> 32));
      tpub = (int)(t0 - t1) > 0 ? t0 : t1;
    }
    if (w == 3) __builtin_amdgcn_raw_buffer_store_b32(tpub | 1u, rs, tso, o2t, 16);
    x_fetch_n<w == 3 ? F3C : (w == 2 ? F2C : F0C)>(xsrc, lds0, s + 3, w == 3 ? 40 - F3C : P0C + w * F0C, lane * 16);
    LOGT(0, tpub); LOGT(1, tb2_);
    TSTAMP(1, tpub);
    ARRIVE_OUT();
    inv = u2f(__builtin_amdgcn_readfirstlane(iv));
    an = __builtin_amdgcn_readfirstlane(an);
    const int ahead = (int)(an - tpub);
    tw = tpub + (unsigned)(an == 0u || ahead > 1000 ? SCH_FIRST : imax(ahead, SCH_DMIN));
    offs = tw - an;
  }
  rows(NSEG - 1);
  STAMP_OUT();
  LOGP(4);
  static_assert(LAG3 == 1 && LAG4 == 3 && LAG5 == 6, "drain schedule");
  unsigned* const cntx = (unsigned*)(misc + 16);
  auto poll = [&](auto load, auto ready) {
    load();
    for (unsigned spins = 0; !ready() && !dead;) {
      if (++spins > SPIN_CAP) dead = true;
      asm volatile("" ::: "memory");
      load();
    }
  };
  auto mark = [&](int k) { if (w == 0) *(volatile unsigned*)(misc + k) = cyc32() | 1u; };
  auto after = [&](int k, unsigned d) { lds_wait((unsigned*)(misc + k), 1u); wait_cyc(__builtin_amdgcn_readfirstlane(*(volatile unsigned*)(misc + k)) + d); };
  const int qt = w >> 1;
  auto t4 = [&](int sl) { poll([&]() { pv.load(rs, ck, sl, gp, qt, lane); }, [&]() { return pv.ready(); }); pv.run(lds, rs, sl, gp, m, qt, lane, true); };
  auto t5 = [&](int sl, int part) { poll([&]() { ot.load(a, rs, sl, b, part); }, [&]() { return ot.ready(b); }); ot.run(lane); ot.store(a, sl, b, lane); };
  BAR();
  const unsigned tp1 = cyc32();
  if (w == 2) tk.load(rs, ts_slot(ck, NSEG - 2));
  if (w == 3) { kq_get(NSEG - 1); t2_mem(qk_frag(lds, g, c, []() {}), qx[0], qx[1], rs, ts_slot(ck, NSEG - 1), h, j, inv * KEXP, lane); }
  if (w == 0) tk.init(h, j, lane);
  LOGD(0);
  if (w == 0) {
    ot.init(b, lane);
    wait_cyc(tp1 + END_E1);
    poll([&]() { tk.load(rs, ts_slot(ck, NSEG - 1)); }, [&]() { return tk.ready(); });
    LOGD(1);
  }
  if (!(w & 1)) {
    tk.run(j, lane, SelLo); tk.store(rs, ck, NSEG - 1 - qt, h, j, lane);
    mark(24);
    LOGD(2);
    if (w == 2) ot.init(b, lane);
    ot.load(a, rs, NSEG - 4 + w, b, 2);
    lds_wait(cnt2, 3u * NSEG + 1u);
    after(24, 600); wait_cyc(tp1 + END_E2);
    t4(NSEG - 1);
    mark(25);
    LOGD(3);
  } else {
    ot.init(b, lane);
#pragma unroll 1
    for (int i = 0; i < 3; ++i) {
      wait_cyc(tp1 + (i == 0 ? END_E60 : (i == 1 ? 900 : END_E62)));
      t4(NSEG - 4 + i);
      if (i == 1 || (i == 2 && w == 1)) t5(i == 1 ? NSEG - 7 + qt : NSEG - 5, 3);
    }
    ot.load(a, rs, NSEG - 4 + w, b, 2);
    LOGP(5);
  }
  after(25, 600); wait_cyc(tp1 + END_E3);
  t5(NSEG - 4 + w, 1);
  LOGD(4);
  VM_DRAIN();
  if (w != 0) lds_inc<L_MISC + 64>(lds0);
  else {
    lds_wait(cntx, 3u);
    LOGD(5);
    if (lane == 0) __hip_atomic_fetch_add((gu64*)((GAS char*)(a.ws + o2) + acc_off(m * 4, 0)), (u64)1, RLX_AGENT);
    wait_until(now32() + SCH_DRAIN);
    wait_cyc(tp1 + END_E4);
    unsigned x;
    poll([&]() { x = __builtin_amdgcn_raw_buffer_load_b32(rs, acc_off(c * 4, 0), o2, 16); }, [&]() { return wave_all((x & 16u) != 0u); });
    lds_inc<L_MISC + 68>(lds0);
    LOGD(6);
  }
  LOGP(8);
}

__device__ __forceinline__ void proj_front(const RecArgs& a, char* lds, const __amdgpu_buffer_rsrc_t rs, const int v) {
  const int lane = __builtin_amdgcn_workitem_id_x() & 63, b = __builtin_amdgcn_workgroup_id_x(), c = lane & 15, g = lane >> 4;
  const int rb = (b & 7) + 8 * (b >> 5), cq = (b >> 3) & 3;
  const unsigned lds0 = (unsigned)(uintptr_t)(LAS char*)lds;
  unsigned* const cnt = (unsigned*)(lds + L_MISC) + 20;
  LOGF2(0);
  float* const red = (float*)(lds + L_RED1);
  float* const fin = red + 640;
  const f32x4 Z4 = {0.f, 0.f, 0.f, 0.f};
#pragma unroll
  for (int i = 0; i < 8; ++i) x_fetch((const u16*)(a.ws + OFF_OB) + (size_t)rb * 32 * DM, lds0, 0, 8 * v + i, lane);
  bf16x8 W[2][20];
#pragma unroll
  for (int k = 0; k < 40; ++k) W[k & 1][k >> 1] = *(const bf16x8*)(a.ws + OFF_WOBT + ((size_t)(cq * 10 + 2 * v + (k & 1)) * 20 + (k >> 1)) * 1024 + lane * 16);
  const unsigned fo = (unsigned)(rb * 40960 + (cq * 5 + v) * 1024 + ((g >> 1) * 16 + c) * 16), f8 = fo + (g & 1) * 8, f16 = fo + (g & 1) * 20480;
  u32x2 xo[2][2];
#pragma unroll
  for (int k = 0; k < 4; ++k)
    xo[k >> 1][k & 1] = __builtin_bit_cast(u32x2, __hip_atomic_load((const gu64*)((const char*)a.xnew + f8 + (k >> 1) * 512 + (k & 1) * 20480), RLX_AGENT));
  asm volatile("s_waitcnt vmcnt(44)" ::: "memory");
  lds_inc<L_MISC + 80>(lds0);
  lds_wait(cnt, 5u);
  LOGF2(1);
  f32x4 acc[2][2] = {{Z4, Z4}, {Z4, Z4}};
#pragma unroll
  for (int ks = 0; ks < 20; ++ks)
#pragma unroll
    for (int t = 0; t < 2; ++t) {
      const bf16x8 x = *(const bf16x8*)(lds + L_XN + (t * 20 + ks) * 1024 + lane * 16);
#pragma unroll
      for (int i = 0; i < 2; ++i) acc[i][t] = mfma32(W[i][ks], x, acc[i][t]);
    }
  u32x2 xp[2][2];
  PIN_V(acc[1][1]);
  LOGF2(2);
#pragma unroll
  for (int k = 0; k < 4; ++k) {
    const int i = k >> 1, t = k & 1;
#pragma unroll
    for (int q = 0; q < 2; ++q) {
      const unsigned r = pack2(acc[i][t][2 * q], acc[i][t][2 * q + 1]), x = xo[i][t][q];
      xp[i][t][q] = pack2(u2f(x << 16) + u2f(r << 16), u2f(x & 0xffff0000u) + u2f(r & 0xffff0000u));
      acc[i][t][2 * q] = u2f(xp[i][t][q] << 16); acc[i][t][2 * q + 1] = u2f(xp[i][t][q] & 0xffff0000u);
    }
    const float ml = x32sum(x16sum((acc[i][t][0] + acc[i][t][1]) + (acc[i][t][2] + acc[i][t][3]))) * (1.0f / 16.0f);
    float q = 0.f;
#pragma unroll
    for (int r = 0; r < 4; ++r) { const float d = acc[i][t][r] - ml; q += d * d; }
    q = x32sum(x16sum(q));
    if (g == 0) { red[(2 * v + i) * 32 + t * 16 + c] = ml; red[320 + (2 * v + i) * 32 + t * 16 + c] = q; }
  }
  lds_inc<L_MISC + 80>(lds0);
  if (v == 0) {
    lds_wait(cnt, 10u);
    LOGF2(3);
    float mw[10], ml = 0.f, m2 = 0.f;
#pragma unroll
    for (int i = 0; i < 10; ++i) { mw[i] = red[i * 32 + (lane & 31)]; ml += mw[i]; m2 += red[320 + i * 32 + (lane & 31)]; }
    ml *= 0.1f;
#pragma unroll
    for (int i = 0; i < 10; ++i) m2 += 16.0f * (mw[i] - ml) * (mw[i] - ml);
    const unsigned xs = (unsigned)OFF_LNX + rb * 1024;
    if (lane < 32) SC1_STORE8S(((u32x2){f2u(ml), nz(f2u(m2))}), (cq * 32 + lane) * 8, xs);
    wait_cyc(cyc32() + FR_D);
    u32x2 x0, x1;
    for (unsigned spins = 0; spins < SPIN_CAP; ++spins) {
      x0 = __builtin_amdgcn_raw_buffer_load_b64(rs, lane * 8, xs, 16); x1 = __builtin_amdgcn_raw_buffer_load_b64(rs, 512 + lane * 8, xs, 16);
      if (wave_all(x0[1] != 0u && x1[1] != 0u)) break;
      asm volatile("" ::: "memory");
    }
    const float m0 = u2f(x0[0]), m1 = u2f(x1[0]);
    const float mean = x32sum(m0 + m1) * 0.25f;
    const float M2 = x32sum((u2f(x0[1]) + u2f(x1[1])) + 160.0f * ((m0 - mean) * (m0 - mean) + (m1 - mean) * (m1 - mean)));
    const float mb = rbf(mean), den = rbf(__builtin_sqrtf(rbf(rbf(M2 * (1.0f / DM)) + 1e-5f)));
    if (lane < 32) {
      fin[lane] = mb; fin[32 + lane] = 1.0f / den;
      if (cq == 0) __hip_atomic_store((GAS unsigned*)(a.ws + OFF_XNRM) + (rb * 32 + lane) * 4, f2u(1.02f * (M2 + DM * (mean - mb) * (mean - mb)) / (den * den)), RLX_AGENT);
    }
    lds_inc<L_MISC + 84>(lds0);
  }
  lds_wait(cnt + 1, 1u);
  LOGF2(4);
  const __amdgpu_buffer_rsrc_t rx = __builtin_amdgcn_make_buffer_rsrc(a.ws + OFF_XN, 0, (int)(OFF_QB - OFF_XN), 0x00020000);
  auto put = [&](const u32x2 p0, const u32x2 p1, unsigned so) {
    const auto r0 = __builtin_amdgcn_permlane16_swap(p0[0], p1[0], false, false), r1 = __builtin_amdgcn_permlane16_swap(p0[1], p1[1], false, false);
    __builtin_amdgcn_raw_buffer_store_b128((u32x4){r0[0], r1[0], r0[1], r1[1]}, rx, f16, so, 16);
  };
#pragma unroll
  for (int i = 0; i < 2; ++i) {
    u32x2 y[2];
#pragma unroll
    for (int t = 0; t < 2; ++t) {
      const float mb = fin[t * 16 + c], rden = fin[32 + t * 16 + c];
      y[t] = (u32x2){pack2(rbf(acc[i][t][0] - mb) * rden, rbf(acc[i][t][1] - mb) * rden), pack2(rbf(acc[i][t][2] - mb) * rden, rbf(acc[i][t][3] - mb) * rden)};
    }
    put(y[0], y[1], i * 512);
  }
  VM_DRAIN();
  lds_inc<L_MISC + 80>(lds0);
#pragma unroll
  for (int i = 0; i < 2; ++i) put(xp[i][0], xp[i][1], (unsigned)(OFF_XB - OFF_XN) + i * 512);
  if (v == 0) {
    lds_wait(cnt, 15u);
    if (rb < 4 && lane < 8) __hip_atomic_fetch_add((GAS unsigned*)(a.ws + OFF_RDY) + lane * 64, 1u, RLX_AGENT);
  }
  LOGF2(5);
}

__global__ __launch_bounds__(512, 2) void rec_kernel(RecArgs a) {
  __shared__ __attribute__((aligned(16))) char lds[L_BYTES];
  const int tid = __builtin_amdgcn_workitem_id_x(), wid = __builtin_amdgcn_readfirstlane(tid >> 6), w = wid & 3;
  const __amdgpu_buffer_rsrc_t rs = __builtin_amdgcn_make_buffer_rsrc(a.ws, 0, (int)COMM_BYTES, 0x00020000);
  if (tid < 32) ((float*)(lds + L_MISC))[tid] = (tid == 4 || tid == 5) ? 1.0f : 0.0f;
  BAR();
  if (a.layer && wid < 5) proj_front(a, lds, rs, wid);
  if (wid < 4) {
    if (w == 0) rec_waves<0>(a, lds, rs);
    else if (w == 1) rec_waves<1>(a, lds, rs);
    else if (w == 2) rec_waves<2>(a, lds, rs);
    else rec_waves<3>(a, lds, rs);
  } else {
    if (w == 0) token_waves<0>(a, lds, rs);
    else if (w == 1) token_waves<1>(a, lds, rs);
    else if (w == 2) token_waves<2>(a, lds, rs);
    else token_waves<3>(a, lds, rs);
    qkv_tail(a, lds, w);
  }
}

#if defined(__HIP_DEVICE_COMPILE__)
#pragma clang attribute pop
#endif

constexpr int KQ = S_ / 4;
#define ATT_P1 6
#define ATT_P2 10
#define ATT_P3 14
#define ATT_THR 6.0f
#define ATT_NQ 4
#define ATT_LMAX 1.0e12f
#if defined(__HIP_DEVICE_COMPILE__)
#pragma clang attribute push(__attribute__((target("no-packed-fp32-ops"))), apply_to = function)
#endif
__global__ __launch_bounds__(256 * ATT_NQ, 4) void attb_kernel(const u16* __restrict__ Qp, const u16* __restrict__ KV, u16* __restrict__ O) {
  __shared__ float mrg_[ATT_NQ][3][2][13][64];
  const int tid = __builtin_amdgcn_workitem_id_x(), bid = __builtin_amdgcn_workgroup_id_x();
  __shared__ unsigned sink[64];
  own_code<4800>(tid, sink);
  const int lane = tid & 63, wid = __builtin_amdgcn_readfirstlane(tid >> 6), kh = wid / ATT_NQ, r = lane & 31, hh = lane >> 5;
  const int hd = (bid & 7) * 4 + ((bid >> 3) & 3), qb = (bid >> 5) * ATT_NQ + wid % ATT_NQ;
  float (*mrg)[2][13][64] = mrg_[wid % ATT_NQ];
  const int q0 = qb * 64;
  bf16x8 qa[2]; u32x4 qb2[2];
#pragma unroll
  for (int qt = 0; qt < 2; ++qt) {
    { const u32x2* qp = (const u32x2*)(Qp + ((size_t)hd * S_ + q0 + qt * 32 + r) * QST + 8 * hh); const u32x2 q0_ = qp[0], q1_ = qp[1]; qa[qt] = mk8(q0_[0], q0_[1], q1_[0], q1_[1]); }
    const u32x2 t = *(const u32x2*)(Qp + ((size_t)hd * S_ + q0 + qt * 32 + r) * QST + 16);
    qb2[qt] = (u32x4){hh ? 0u : t[0], hh ? 0u : t[1], 0u, 0u};
  }
  const __amdgpu_buffer_rsrc_t rk = __builtin_amdgcn_make_buffer_rsrc((void*)(KV + ((size_t)hd * 64 + kh * 16) * (KVT / 2)), 0, 16 * KVT, 0x00020000);
  unsigned ok1 = lane * 16, ok2 = hh ? 1u << 30 : 1024 + r * 8, ov = r > 20 ? 1u << 30 : 1280 + (hh * 21 + r) * 16;
  f32x16 o[2];
  float m[2];
  const f32x16 Z16 = {0.f, 0.f, 0.f, 0.f, 0.f, 0.f, 0.f, 0.f, 0.f, 0.f, 0.f, 0.f, 0.f, 0.f, 0.f, 0.f};
  struct KvT { u32x4 k1, v0, v1; u32x2 k2; };
  auto fetch = [&](int t) {
    KvT x;
    __builtin_amdgcn_sched_barrier(0);
    x.k1 = __builtin_amdgcn_raw_buffer_load_b128(rk, ok1, t * KVT, 0); x.k2 = __builtin_amdgcn_raw_buffer_load_b64(rk, ok2, t * KVT, 0);
    x.v0 = __builtin_amdgcn_raw_buffer_load_b128(rk, ov, t * KVT, 0); x.v1 = __builtin_amdgcn_raw_buffer_load_b128(rk, ov + 672, t * KVT, 0);
    __builtin_amdgcn_sched_barrier(0);
    return x;
  };
  auto tile = [&](const KvT& x, auto MODE) {
    const bf16x8 ckb = mk8(x.k2[0], x.k2[1], 0x3f803f80u, 0x00003f80u);
    f32x16 sa[2];
#pragma unroll
    for (int qt = 0; qt < 2; ++qt) {
      sa[qt] = __builtin_amdgcn_mfma_f32_32x32x16_bf16(as8(x.k1), qa[qt], Z16, 0, 0, 0);
      sa[qt] = __builtin_amdgcn_mfma_f32_32x32x16_bf16(ckb, as8(qb2[qt]), sa[qt], 0, 0, 0);
    }
#pragma unroll
    for (int qt = 0; qt < 2; ++qt) {
      if (MODE != 0) {
        float mx = fmx(fmx(sa[qt][0], sa[qt][1]), sa[qt][2]);
#pragma unroll
        for (int i = 3; i < 15; i += 2) mx = fmx(fmx(mx, sa[qt][i]), sa[qt][i + 1]);
        mx = fmx(mx, sa[qt][15]);
        if (MODE == 1 || !wave_all(mx <= ATT_THR)) {
          mx = x32mx(mx);
          if (MODE != 1) mx = fmx(mx, 0.f);
          const float mn = m[qt] + mx;
          const unsigned h1 = f2bf(mn); const float r1 = mn - bf2f(h1);
          const unsigned h2 = f2bf(r1); const float r2 = r1 - bf2f(h2);
          const unsigned h3 = f2bf(r2);
          const float me = (bf2f(h1) + bf2f(h2)) + bf2f(h3);
          const float dm = me - m[qt];
          m[qt] = me;
          if (hh == 0) { qb2[qt][2] = (h1 | (h2 << 16)) ^ 0x80008000u; qb2[qt][3] = h3 ^ 0x8000u; }
          const float alpha = __builtin_amdgcn_exp2f(-dm);
#pragma unroll
          for (int i = 0; i < 16; ++i) { if (MODE != 1) o[qt][i] *= alpha; sa[qt][i] -= dm; }
        }
      }
      float p[16];
#pragma unroll
      for (int i = 0; i < 16; ++i) p[i] = __builtin_amdgcn_exp2f(sa[qt][i]);
      bf16x8 p0 = mk8(pack2(p[0], p[1]), pack2(p[2], p[3]), pack2(p[4], p[5]), pack2(p[6], p[7]));
      bf16x8 p1 = mk8(pack2(p[8], p[9]), pack2(p[10], p[11]), pack2(p[12], p[13]), pack2(p[14], p[15]));
      o[qt] = __builtin_amdgcn_mfma_f32_32x32x16_bf16(as8(x.v0), p0, MODE == 1 ? Z16 : o[qt], 0, 0, 0);
      o[qt] = __builtin_amdgcn_mfma_f32_32x32x16_bf16(as8(x.v1), p1, o[qt], 0, 0, 0);
    }
  };
  typedef std::integral_constant<int, 0> I0; typedef std::integral_constant<int, 1> I1; typedef std::integral_constant<int, 2> I2;
  auto pass = [&](auto MODE) {
    asm volatile("" : "+v"(ok1), "+v"(ok2), "+v"(ov) :: "memory");
    m[0] = m[1] = 0.f;
    qb2[0][2] = qb2[0][3] = qb2[1][2] = qb2[1][3] = 0u;
    KvT xa = fetch(0), xb = fetch(1);
    tile(xa, I1{});
    xa = fetch(2);
    tile(xb, MODE);
#pragma unroll 1
    for (int t = 2; t < KQ / 32; t += 2) {
      if (t == ATT_P1) __builtin_amdgcn_s_setprio(2);
      if (t == ATT_P2) __builtin_amdgcn_s_setprio(1);
      if (t == ATT_P3) __builtin_amdgcn_s_setprio(0);
      xb = fetch(t + 1);
      tile(xa, MODE);
      xa = fetch(t + 2);
      tile(xb, MODE);
    }
  };
  if (ATT_P1) __builtin_amdgcn_s_setprio(3);
  pass(I0{});
  {
    float big = 0.f;
#pragma unroll
    for (int qt = 0; qt < 2; ++qt) {
      big = fmx(big, hh ? o[qt][8] * (3.0e38f / ATT_LMAX) : __builtin_fabsf(o[qt][8]));
#pragma unroll
      for (int i = 0; i < 12; ++i) if (i != 8) big = fmx(big, (i > 8 && hh) ? 0.f : __builtin_fabsf(o[qt][i]));
    }
    if (__builtin_expect(!wave_all(big <= 3.0e38f), 0)) pass(I2{});
  }
  if (kh > 0) {
#pragma unroll
    for (int qt = 0; qt < 2; ++qt) {
#pragma unroll
      for (int i = 0; i < 12; ++i) mrg[kh - 1][qt][i][lane] = o[qt][i];
      mrg[kh - 1][qt][12][lane] = m[qt];
    }
  }
  asm volatile("s_waitcnt lgkmcnt(0)\n\ts_barrier" ::: "memory");
  if (kh == 0) {
#pragma unroll
    for (int qt = 0; qt < 2; ++qt) {
      float m1 = mrg[0][qt][12][lane], m2 = mrg[1][qt][12][lane], m3 = mrg[2][qt][12][lane];
      float mn = fmx(fmx(m[qt], m1), fmx(m2, m3));
      float a0 = __builtin_amdgcn_exp2f(m[qt] - mn), a1 = __builtin_amdgcn_exp2f(m1 - mn);
      float a2 = __builtin_amdgcn_exp2f(m2 - mn), a3 = __builtin_amdgcn_exp2f(m3 - mn);
      float v[12];
#pragma unroll
      for (int i = 0; i < 12; ++i) v[i] = (o[qt][i] * a0 + mrg[0][qt][i][lane] * a1) + (mrg[1][qt][i][lane] * a2 + mrg[2][qt][i][lane] * a3);
      auto lr = __builtin_amdgcn_permlane32_swap(f2u(v[8]), f2u(v[8]), false, false);
      float inv = __builtin_amdgcn_rcpf(u2f(lr[1]));
      const int row = q0 + qt * 32 + r;
      auto oaddr = [&](int k) { return O + (((size_t)(row >> 4) * 20 + (k >> 5)) * 64 + ((k & 31) >> 3) * 16 + (row & 15)) * 8 + (k & 7); };
      const int k0 = hd * BD + 4 * hh;
      *(u32x2*)oaddr(k0) = (u32x2){pack2(v[0] * inv, v[1] * inv), pack2(v[2] * inv, v[3] * inv)};
      *(u32x2*)oaddr(k0 + 8) = (u32x2){pack2(v[4] * inv, v[5] * inv), pack2(v[6] * inv, v[7] * inv)};
      if (hh == 0) *(u32x2*)oaddr(k0 + 16) = (u32x2){pack2(v[8] * inv, v[9] * inv), pack2(v[10] * inv, v[11] * inv)};
    }
  }
}
#if defined(__HIP_DEVICE_COMPILE__)
#pragma clang attribute pop
#endif

__global__ __launch_bounds__(640, 3) void proj_kernel(const u16* __restrict__ Of, const u16* __restrict__ Wf, const u16* __restrict__ xold,
                                                      u16* __restrict__ xnew) {
  __shared__ __attribute__((aligned(16))) char ob[40960];
  const int tid = threadIdx.x, lane = tid & 63, w = __builtin_amdgcn_readfirstlane(tid >> 6), g = lane >> 4, c = lane & 15;
  __shared__ unsigned sink[64];
  own_code<1500>(tid, sink);
  const int rb = (blockIdx.x & 7) + 8 * (blockIdx.x >> 5), cb = (blockIdx.x >> 3) & 3;
  const int col = cb * 160 + w * 16 + 4 * g;
  const u16* wp = Wf + (size_t)(cb * 10 + w) * 20 * 512 + lane * 8;
  u32x4 st[4];
#pragma unroll
  for (int i = 0; i < 4; ++i) st[i] = *(const u32x4*)(Of + (size_t)rb * 40 * 512 + (i * 640 + tid) * 8);
  bf16x8 wf[20];
#pragma unroll
  for (int ks = 0; ks < 20; ++ks) wf[ks] = *(const bf16x8*)(wp + (size_t)ks * 512);
  u32x2 xo[2];
#pragma unroll
  for (int t = 0; t < 2; ++t) xo[t] = *(const u32x2*)(xold + (((size_t)(rb * 2 + t) * 20 + (col >> 5)) * 64 + ((col & 31) >> 3) * 16 + c) * 8 + (col & 7));
#pragma unroll
  for (int i = 0; i < 4; ++i) *(u32x4*)(ob + (i * 640 + tid) * 16) = st[i];
  BAR();
  f32x4 acc[2] = {{0.f, 0.f, 0.f, 0.f}, {0.f, 0.f, 0.f, 0.f}};
#pragma unroll
  for (int ks = 0; ks < 20; ++ks)
#pragma unroll
    for (int t = 0; t < 2; ++t) acc[t] = mfma32(wf[ks], *(const bf16x8*)(ob + (t * 20 + ks) * 1024 + lane * 16), acc[t]);
#pragma unroll
  for (int t = 0; t < 2; ++t) {
    const unsigned r01 = pack2(acc[t][0], acc[t][1]), r23 = pack2(acc[t][2], acc[t][3]);
    const unsigned p01 = pack2(u2f(xo[t][0] << 16) + u2f(r01 << 16), u2f(xo[t][0] & 0xffff0000u) + u2f(r01 & 0xffff0000u));
    const unsigned p23 = pack2(u2f(xo[t][1] << 16) + u2f(r23 << 16), u2f(xo[t][1] & 0xffff0000u) + u2f(r23 & 0xffff0000u));
    *(u32x2*)(xnew + (size_t)(rb * 32 + t * 16 + c) * DM + col) = (u32x2){p01, p23};
  }
}

extern "C" void kernel_launch(void* const* d_in, const int* in_sizes, int n_in, void* d_out, int out_size, void* d_ws,
                              size_t ws_size, hipStream_t stream) {
  const u16* A_x = (const u16*)d_in[0];
  const u16* A_mem0 = (const u16*)d_in[1];
  const u16* A_Wq = (const u16*)d_in[2];
  const u16* A_Wk = (const u16*)d_in[3];
  const u16* A_Wv = (const u16*)d_in[4];
  const u16* A_Wo = (const u16*)d_in[5];
  const u16* B_wqkv = (const u16*)d_in[8];
  const u16* B_wo = (const u16*)d_in[9];
  char* ws = (char*)(((uintptr_t)d_ws + ((size_t)WS_OFF << 20) + WS_AL - 1) & ~(uintptr_t)(WS_AL - 1));
  u16* xa = (u16*)(ws + OFF_XA); u16* Ob = (u16*)(ws + OFF_OB);

  prologue_kernel<<<256, 640, 0, stream>>>(ProArgs{A_x, A_Wq, A_Wk, A_Wv, A_Wo, B_wqkv, B_wo, ws});
  for (int layer = 0; layer < 2; ++layer) {
    rec_kernel<<<256, 512, 0, stream>>>(RecArgs{A_mem0, A_Wq, A_Wk, layer ? (const u16*)(ws + OFF_XB) : A_x, xa, ws, (unsigned)layer, 0u});
    attb_kernel<<<BH * 32 / ATT_NQ, 256 * ATT_NQ, 0, stream>>>((const u16*)(ws + OFF_QB), (const u16*)(ws + OFF_KV), Ob);
  }
  proj_kernel<<<256, 640, 0, stream>>>(Ob, (const u16*)(ws + OFF_WOBT), xa, (u16*)d_out);
}
```

```cpp
#include <hip/hip_runtime.h>
#include <hip/hip_bf16.h>
#include <stdint.h>
#include <type_traits>

typedef unsigned short u16;
typedef unsigned long long u64;
typedef __attribute__((ext_vector_type(8))) short bf16x8;
typedef __attribute__((ext_vector_type(16))) short bf16x16;
typedef __attribute__((ext_vector_type(4))) float f32x4;
typedef __attribute__((ext_vector_type(2))) float f32x2;
typedef __attribute__((ext_vector_type(16))) float f32x16;
typedef __attribute__((ext_vector_type(4))) unsigned u32x4;
typedef __attribute__((ext_vector_type(2))) unsigned u32x2;
#define GAS __attribute__((address_space(1)))
#define LAS __attribute__((address_space(3)))
typedef GAS unsigned long long gu64;
#define BAR() do { __builtin_amdgcn_s_waitcnt(0xC07F); asm volatile("s_waitcnt lgkmcnt(0)\n\ts_barrier" ::: "memory"); } while (0)

constexpr int S_ = 2048, DM = 640;
constexpr int NH = 32, DH = 192, HD = NH * DH;
constexpr int MEM = 8, SEG = 32, NSEG = S_ / SEG;
constexpr int BH = 32, BD = 20;

constexpr size_t al256(size_t x) { return (x + 255) & ~(size_t)255; }
constexpr int LAG3 = 2, LAG4 = 3, LAG5 = 6;
#define SR_ 8
constexpr int SR = SR_;
constexpr unsigned SCT_STEP = 131072, OFF_SCM = SR * SCT_STEP, SCM_STEP = 32768;
constexpr int ACC_NB = 192;
constexpr size_t ACC_STEP = 65536;
constexpr unsigned ACC_TS = ACC_NB * 256;
constexpr size_t ACC_LAYER = SR * ACC_STEP;
constexpr size_t OFF_ACC = 3 * 524288;
static_assert(SR == 8, "regions");
__device__ __forceinline__ unsigned ts_piece(int i) { return ACC_TS + (unsigned)(i < 64 ? i * 256 : 128); }
constexpr size_t OFF_LNX = OFF_SCM + SR * SCM_STEP;
constexpr size_t OFF_RDY = OFF_LNX + 65536;
constexpr size_t PG = 2097152;
#define RING_ 4
constexpr int RING = RING_;
constexpr size_t TS_SLOT = (size_t)NH * 8 * 2560, PT_SLOT = (size_t)NH * 2560, OP_SLOT = (size_t)256 * 2560;
__device__ __forceinline__ unsigned ts_tok(int h, int j) { return (unsigned)((h & 15) * 32768 + (j >> 2) * 16384 + (j & 3) * 2048); }
__device__ __forceinline__ unsigned ts_mem(int h, int j) { return (unsigned)(524288 + ((h & 15) >> 1) * 16384 + (h & 1) * 4096 + j * 512); }
__device__ __forceinline__ unsigned pt_tok(int h) { return (unsigned)(((h & 15) >> 2) * 16384 + (h & 3) * 2048); }
__device__ __forceinline__ unsigned pt_mem(int h) { return (unsigned)(65536 + (h & 15) * 512); }
constexpr size_t OFF_X = 2 * PG + TS_SLOT + 3 * PT_SLOT, X_BYTES = 7 * OP_SLOT + 5 * PT_SLOT;
constexpr size_t OFF_OP = OFF_X + TS_SLOT + 5 * PT_SLOT;
constexpr size_t COMM_BYTES = OFF_X + X_BYTES + RING * OP_SLOT;
__device__ __forceinline__ unsigned near_half(unsigned ck, unsigned o) { return o + ((ck >> (o >= 2 * PG ? 8 : 4)) & 8192u); }
__device__ __forceinline__ unsigned ts_slot(unsigned ck, int sl) {
  return near_half(ck, sl == NSEG - 1 ? (unsigned)OFF_X : ((sl & 3) == 3 ? (unsigned)(2 * PG) : (unsigned)PG + (unsigned)(sl & 3) * (unsigned)TS_SLOT));
}
__device__ __forceinline__ unsigned pt_slot(unsigned ck, int sl) {
  return near_half(ck, sl >= NSEG - 5 ? (unsigned)(OFF_X + TS_SLOT) + (unsigned)(sl - (NSEG - 5)) * (unsigned)PT_SLOT
                                      : ((sl & 3) == 0 ? (unsigned)(PG + 3 * TS_SLOT) : (unsigned)(2 * PG + TS_SLOT - PT_SLOT) + (unsigned)(sl & 3) * (unsigned)PT_SLOT));
}
__device__ __forceinline__ unsigned op_slot(int sl) { return (unsigned)OFF_OP + (unsigned)(sl >= NSEG - 6 ? sl - (NSEG - 6) : 6 + sl % RING) * (unsigned)OP_SLOT; }
static_assert(RING == 4 && OFF_OP <= 3 * PG && OFF_RDY + 2048 <= OFF_ACC && OFF_ACC + ACC_LAYER == PG && TS_SLOT % 16384 == 0 && PT_SLOT % 16384 == 0, "pages");
static_assert(TS_SLOT == OP_SLOT && RING >= 4 && NSEG % SR == 0 && SR >= 5 && X_BYTES % 4096 == 0 && X_BYTES <= 256 * 20 * 1024, "slots");
constexpr size_t OFF_WB = al256(COMM_BYTES);
constexpr size_t OFF_BNRM = OFF_WB + (size_t)256 * 100 * 1024;
constexpr size_t OFF_LAT = OFF_BNRM + 1024;
constexpr size_t OFF_XNRM = OFF_LAT + 2048;
constexpr size_t OFF_WQKVBT = OFF_XNRM + al256(S_ * 16);
constexpr size_t OFF_WOBT = OFF_WQKVBT + al256((size_t)3 * DM * DM * 2);
constexpr size_t OFF_XN = OFF_WOBT + al256((size_t)DM * DM * 2);
constexpr int QST = BD;
constexpr size_t OFF_XA = OFF_XN + al256((size_t)S_ * DM * 2);
constexpr size_t OFF_XB = OFF_XA + al256((size_t)S_ * DM * 2);
constexpr size_t OFF_QB = OFF_XB + al256((size_t)S_ * DM * 2);
constexpr int KVT = 2624;
constexpr size_t OFF_KV = OFF_QB + al256((size_t)BH * S_ * QST * 2);
constexpr size_t OFF_OB = OFF_KV + al256((size_t)BH * 64 * KVT);
constexpr size_t OFF_DBG = OFF_OB + al256((size_t)S_ * DM * 2);
constexpr size_t WS_TOTAL = OFF_DBG + (size_t)2 * 64 * 8 * DM * 2;
#define WS_AL 2097152
#define WS_OFF 0
static_assert(WS_TOTAL + WS_AL + ((size_t)WS_OFF << 20) <= ((size_t)256 << 20), "workspace budget");

#if defined(__HIP_DEVICE_COMPILE__)
#pragma clang attribute push(__attribute__((target("no-packed-fp32-ops"))), apply_to = function)
#endif
__device__ __forceinline__ float u2f(unsigned v) { return __builtin_bit_cast(float, v); }
__device__ __forceinline__ unsigned f2u(float v) { return __builtin_bit_cast(unsigned, v); }
__device__ __forceinline__ float bf2f(unsigned v) { return __builtin_bit_cast(float, v << 16); }
__device__ __forceinline__ unsigned f2bf(float f) { __bf16 b = (__bf16)f; return (unsigned)__builtin_bit_cast(u16, b); }
__device__ __forceinline__ float rbf(float f) { return bf2f(f2bf(f)); }
typedef __attribute__((ext_vector_type(2))) __bf16 hbf16x2;
__device__ __forceinline__ unsigned pack2(float lo, float hi) { f32x2 v = {lo, hi}; hbf16x2 b = __builtin_convertvector(v, hbf16x2); return __builtin_bit_cast(unsigned, b); }
__device__ __forceinline__ f32x4 mfma32(bf16x8 a, bf16x8 b, f32x4 c) { return __builtin_amdgcn_mfma_f32_16x16x32_bf16(a, b, c, 0, 0, 0); }
typedef __attribute__((ext_vector_type(8))) __bf16 hbf16x8;
typedef __attribute__((ext_vector_type(16))) __bf16 hbf16x16;
__device__ __forceinline__ f32x4 smfma64(bf16x8 a, bf16x16 b, f32x4 c, int idx) {
  return __builtin_amdgcn_smfmac_f32_16x16x64_bf16(__builtin_bit_cast(hbf16x8, a), __builtin_bit_cast(hbf16x16, b), c, idx, 0, 0);
}
__device__ __forceinline__ bf16x16 cat16(bf16x8 a, bf16x8 b) { return __builtin_shufflevector(a, b, 0, 1, 2, 3, 4, 5, 6, 7, 8, 9, 10, 11, 12, 13, 14, 15); }
__device__ __forceinline__ bf16x8 half8(bf16x16 v, int hi) { return hi ? __builtin_shufflevector(v, v, 8, 9, 10, 11, 12, 13, 14, 15) : __builtin_shufflevector(v, v, 0, 1, 2, 3, 4, 5, 6, 7); }
__device__ __forceinline__ bf16x8 mk8(unsigned a, unsigned b, unsigned c, unsigned d) { u32x4 v = {a, b, c, d}; return __builtin_bit_cast(bf16x8, v); }
__device__ __forceinline__ unsigned dpp_ror8(unsigned v) { return (unsigned)__builtin_amdgcn_mov_dpp((int)v, 0x128, 0xf, 0xf, true); }
__device__ __forceinline__ float fmx(float a, float b) { return __builtin_elementwise_maximum(a, b); }

__device__ __forceinline__ int imax(int a, int b) { return a > b ? a : b; }
__device__ __forceinline__ int imin(int a, int b) { return a < b ? a : b; }
__device__ __forceinline__ unsigned umin(unsigned a, unsigned b) { return a < b ? a : b; }
__device__ __forceinline__ bool wave_all(bool p) { return __builtin_amdgcn_ballot_w64(!p) == 0ull; }
__device__ __forceinline__ float x16mx(float x) { unsigned u = __builtin_bit_cast(unsigned, x); auto r = __builtin_amdgcn_permlane16_swap(u, u, false, false); return fmx(u2f(r[0]), u2f(r[1])); }
__device__ __forceinline__ float x32mx(float x) { unsigned u = __builtin_bit_cast(unsigned, x); auto r = __builtin_amdgcn_permlane32_swap(u, u, false, false); return fmx(u2f(r[0]), u2f(r[1])); }
__device__ __forceinline__ float x16sum(float x) { unsigned u = __builtin_bit_cast(unsigned, x); auto r = __builtin_amdgcn_permlane16_swap(u, u, false, false); return u2f(r[0]) + u2f(r[1]); }
__device__ __forceinline__ float x32sum(float x) { unsigned u = __builtin_bit_cast(unsigned, x); auto r = __builtin_amdgcn_permlane32_swap(u, u, false, false); return u2f(r[0]) + u2f(r[1]); }
template <int CTRL> __device__ __forceinline__ float dppf(float v) { return u2f((unsigned)__builtin_amdgcn_mov_dpp((int)f2u(v), CTRL, 0xf, 0xf, true)); }
#define OWN_PF 1
template <int BYTES> __device__ __forceinline__ void own_code(int tid, unsigned* sink) {
  if (!OWN_PF || __builtin_amdgcn_readfirstlane(tid >> 6) * 4096 >= BYTES) return;
  const char* pc; unsigned keep;
  asm volatile("s_getpc_b64 %0" : "=s"(pc));
  asm volatile("s_mov_b32 %0, m0\n\ts_mov_b32 m0, %3\n\ts_nop 0\n\tglobal_load_lds_dword %1, %2\n\ts_mov_b32 m0, %0"
               : "=&s"(keep) : "v"(tid * 64 < BYTES ? tid * 64 : 0), "s"(pc), "s"((unsigned)(uintptr_t)(LAS unsigned*)sink) : "memory");
}
constexpr int DPP_XOR1 = 0xB1, DPP_XOR2 = 0x4E, DPP_ROR4 = 0x124, DPP_ROR8 = 0x128;
#if defined(__HIP_DEVICE_COMPILE__)
#pragma clang attribute pop
#endif

typedef short v4i16_t __attribute__((ext_vector_type(4)));
__device__ __forceinline__ bf16x8 tr_frag(const char* lo_, const char* hi_) {
  const v4i16_t lo = __builtin_amdgcn_ds_read_tr16_b64_v4i16((LAS v4i16_t*)lo_), hi = __builtin_amdgcn_ds_read_tr16_b64_v4i16((LAS v4i16_t*)hi_);
  return (bf16x8){lo[0], lo[1], lo[2], lo[3], hi[0], hi[1], hi[2], hi[3]};
}
__device__ __forceinline__ int rec_wg(int gp, int m) { return (((gp & 1) * 16 + m) << 3) | (gp >> 1); }
__device__ __forceinline__ float wsum(float v) {
  v += dppf<DPP_XOR1>(v); v += dppf<DPP_XOR2>(v); v += dppf<DPP_ROR4>(v); v += dppf<DPP_ROR8>(v);
  return x32sum(x16sum(v));
}
__device__ __forceinline__ float ln_row(int lane, u32x4 (&xo)[2]) {
  const bool two = lane < 16;
  float v[16];
#pragma unroll
  for (int hf = 0; hf < 2; ++hf)
#pragma unroll
    for (int i = 0; i < 4; ++i) { const unsigned u = xo[hf][i]; v[hf * 8 + 2 * i] = bf2f(u & 0xffff); v[hf * 8 + 2 * i + 1] = bf2f(u >> 16); }
  float s0 = 0.f, s1 = 0.f;
#pragma unroll
  for (int i = 0; i < 8; ++i) { s0 += v[i]; s1 += v[8 + i]; }
  const float mean = wsum(two ? s0 + s1 : s0) * (1.0f / DM);
  s0 = s1 = 0.f;
#pragma unroll
  for (int i = 0; i < 8; ++i) { const float d0 = v[i] - mean, d1 = v[8 + i] - mean; s0 += d0 * d0; s1 += d1 * d1; }
  const float var = rbf(wsum(two ? s0 + s1 : s0) * (1.0f / DM));
  const float mb = rbf(mean);
  const float rden = 1.0f / rbf(sqrtf(rbf(var + 1e-5f)));
  float n2[2] = {0.f, 0.f};
#pragma unroll
  for (int hf = 0; hf < 2; ++hf) {
    float y[8];
#pragma unroll
    for (int i = 0; i < 8; ++i) { y[i] = rbf(rbf(v[hf * 8 + i] - mb) * rden); n2[hf] += y[i] * y[i]; }
    xo[hf] = (u32x4){pack2(y[0], y[1]), pack2(y[2], y[3]), pack2(y[4], y[5]), pack2(y[6], y[7])};
  }
  return wsum(two ? n2[0] + n2[1] : n2[0]);
}

#define PROT(k)
#define WARM_CODE 1
#define PRB_N0 0
#define PRB_N1 3
#define PRB_LOG(i, v)
#define PLD(p) __builtin_nontemporal_load((const u32x4*)(p))
struct ProArgs { const u16* x; const u16* wq; const u16* wk; const u16* wv; const u16* wo; const u16* wqkvb; const u16* wob; char* ws; };
__global__ __launch_bounds__(640, 3) void prologue_kernel(ProArgs a) {
  __shared__ __attribute__((aligned(16))) char img[192 * 320];
  __shared__ __attribute__((aligned(16))) char slots[10 * 6144];
  __shared__ float red[10][160];
  __shared__ unsigned latw[8];
  const int b = blockIdx.x, tid = threadIdx.x, lane = tid & 63, v = __builtin_amdgcn_readfirstlane(tid >> 6), c = lane & 15, g = lane >> 4;
  const int h = (b & 7) * 4 + ((b >> 3) & 3), dh = (b >> 5) & 1, cq = b >> 6, ks = dh * 10 + v;
  char* const slot = slots + v * 6144;
  PROT(0);
  __shared__ unsigned sink[64];
  own_code<9600>(tid, sink);
  const unsigned zo = v < 8 ? (unsigned)(((b >> 6) * 512 + ((b & 7) * 2 + (v >> 2)) * 32 + ((b >> 3) & 7) * 4 + (v & 3)) * 1024) : (unsigned)(((v - 7) * 2048 + (b & 7) * 32 + ((b >> 3) & 7) * 4 + (b >> 6)) * 1024);
  __builtin_amdgcn_raw_buffer_store_b128((u32x4){0u, 0u, 0u, 0u}, __builtin_amdgcn_make_buffer_rsrc(a.ws, 0, (int)(3 * PG), 0x00020000), zo + lane * 16, 0, 16);
  __builtin_amdgcn_sched_barrier(0);
  u32x4 wa[2][6], wb[6], tr[4], xo[2];
  const int xrow = b * 8 + (v & 7);
  xo[0] = *(const u32x4*)(a.x + (size_t)xrow * DM + lane * 8);
  xo[1] = *(const u32x4*)(a.x + (size_t)xrow * DM + 512 + c * 8);
  const int job = imin(b + 256 * (9 - v), 800);
  const int jn = job < 600 ? job / 20 : (job % 200) / 20, jk = job % 20;
  {
    const u16* src = job < 600 ? a.wqkvb + (size_t)(32 * jk) * 3 * DM : a.wob + (size_t)(32 * jk) * DM;
#pragma unroll
    for (int i = 0; i < 4; ++i) tr[i] = *(const u32x4*)(src + (size_t)(i * 8 + (lane >> 3)) * (job < 600 ? 3 * DM : DM) + jn * 64 + (lane & 7) * 8);
  }
#pragma unroll
  for (int i = 0; i < 6; ++i) {
    const int idx = (v * 6 + i) * 64 + lane, row = idx / 20;
    wb[i] = PLD(a.wo + (size_t)(h * DH + row) * DM + cq * 160 + (idx - row * 20) * 8);
  }
#pragma unroll
  for (int i = 0; i < 6; ++i) {
    const int idx = i * 64 + lane, r = idx / 24;
#pragma unroll
    for (int u = 0; u < 2; ++u) wa[u][i] = PLD(a.wv + (size_t)(32 * ks + 8 * (r >> 2) + (r & 3) + 4 * u) * HD + h * DH + (idx - r * 24) * 8);
  }
  unsigned pf[2] = {0u, 0u};
  auto prefetch = [&]() {
#pragma unroll
    for (int i = 0; i < 2; ++i) pf[i] = *(const unsigned*)((i ? a.wq : a.wk) + ((size_t)(b * 10 + v) * 48 + imin(lane, 47)) * 32);
  };
  prefetch();
  __builtin_amdgcn_sched_barrier(0);
  unsigned lat = ~0u;
  auto probe = [&](int n0, int n) {
    const char* p = a.ws + zo;
#pragma unroll 1
    for (int i = n0; i < n0 + n; ++i) {
      u64 t0, t1; unsigned d;
      asm volatile("s_memtime %0\n\ts_waitcnt lgkmcnt(0)\n\ts_load_dword %2, %3, %4\n\ts_waitcnt lgkmcnt(0)\n\ts_memtime %1\n\ts_waitcnt lgkmcnt(0)"
                   : "=&s"(t0), "=&s"(t1), "=&s"(d) : "s"(p), "s"((i & 3) * 256 + (i >> 2) * 128) : "memory");
      PRB_LOG(i, (unsigned)t1 - (unsigned)t0);
      lat = umin(lat, (unsigned)t1 - (unsigned)t0);
    }
  };
  if (PRB_N0) { asm volatile("s_waitcnt vmcnt(26)" ::: "memory"); probe(0, PRB_N0); }
  __builtin_amdgcn_sched_barrier(0);
  const float xn2 = u2f(__builtin_amdgcn_readfirstlane(f2u(ln_row(lane, xo))));
#pragma unroll
  for (int i = 0; i < 4; ++i) *(u32x4*)(slot + (i * 8 + (lane >> 3)) * 144 + (lane & 7) * 16) = tr[i];
  __builtin_amdgcn_fence(__ATOMIC_RELEASE, "wavefront"); __builtin_amdgcn_wave_barrier();
  bf16x8 tf[4];
#pragma unroll
  for (int t = 0; t < 4; ++t) {
    const char* bp = slot + (8 * g + (c >> 2)) * 144 + t * 32 + (c & 3) * 8;
    tf[t] = tr_frag(bp, bp + 4 * 144);
  }
  __builtin_amdgcn_fence(__ATOMIC_RELEASE, "wavefront"); __builtin_amdgcn_wave_barrier();
  __builtin_amdgcn_sched_barrier(0);
#pragma unroll
  for (int i = 0; i < 6; ++i) {
    const int idx = (v * 6 + i) * 64 + lane, row = idx / 20;
    *(u32x4*)(img + row * 320 + (((idx - row * 20) ^ ((row >> 2) & 2)) * 16)) = wb[i];
  }
  bf16x8 A[2][6];
#pragma unroll
  for (int u = 0; u < 2; ++u) {
#pragma unroll
    for (int i = 0; i < 6; ++i) {
      const int idx = i * 64 + lane, r = idx / 24;
      *(u32x4*)(slot + r * 384 + (((idx - r * 24) ^ (r >> 1)) * 16)) = wa[u][i];
    }
    __builtin_amdgcn_fence(__ATOMIC_RELEASE, "wavefront"); __builtin_amdgcn_wave_barrier();
#pragma unroll
    for (int e = 0; e < 6; ++e) A[u][e] = *(const bf16x8*)(slot + c * 384 + (((4 * e + g) ^ (c >> 1)) * 16));
    __builtin_amdgcn_fence(__ATOMIC_RELEASE, "wavefront"); __builtin_amdgcn_wave_barrier();
  }
  BAR();
  PROT(1);
  asm volatile("" :: "v"(pf[0]), "v"(pf[1]));
  if (v < 8) {
    u16* xn = (u16*)(a.ws + OFF_XN) + (((size_t)(xrow >> 4) * 20 + (lane >> 2)) * 64 + (lane & 3) * 16 + (xrow & 15)) * 8;
    *(u32x4*)xn = xo[0];
    if (lane < 16) *(u32x4*)(xn + 16 * 512) = xo[1];
    if (lane < 4) ((float*)(a.ws + OFF_XNRM))[xrow * 4 + lane] = lane == 0 ? xn2 : 0.f;
  }
  if (job < 800) {
    u16* dst = (u16*)(a.ws + (job < 600 ? OFF_WQKVBT : OFF_WOBT)) + ((size_t)jn * 80 + jk) * 512 + lane * 8;
#pragma unroll
    for (int t = 0; t < 4; ++t) *(bf16x8*)(dst + (size_t)t * 20 * 512) = tf[t];
  }
  const int gp = h >> 1, hh = h & 1;
  const __amdgpu_buffer_rsrc_t rwb = __builtin_amdgcn_make_buffer_rsrc(a.ws + OFF_WB, 0, 256 * 100 * 1024, 0x00020000);
#pragma unroll
  for (int n = 0; n < 10; ++n) {
    const int off = n * 32 + 8 * (c & 3), T = cq * 10 + n;
    const char* bp = img + (8 * g + (c >> 2)) * 320 + (off ^ ((g & 1) << 5));
    f32x4 ac[2] = {{0.f, 0.f, 0.f, 0.f}, {0.f, 0.f, 0.f, 0.f}};
#pragma unroll
    for (int e = 0; e < 6; ++e) {
      const bf16x8 bf = tr_frag(bp + e * 32 * 320, bp + (e * 32 + 4) * 320);
      ac[0] = mfma32(A[0][e], bf, ac[0]); ac[1] = mfma32(A[1][e], bf, ac[1]);
    }
    const u32x4 o = {pack2(ac[0][0], ac[0][1]), pack2(ac[0][2], ac[0][3]), pack2(ac[1][0], ac[1][1]), pack2(ac[1][2], ac[1][3])};
    const unsigned oo = T < 32 ? (unsigned)((rec_wg(gp, T & 15) * 100 + (hh * 3 + (T >> 4)) * 20 + ks) * 1024 + lane * 16)
                               : (unsigned)((rec_wg(gp, 2 * (T - 32) + (c >> 3)) * 100 + 40 + ks) * 1024 + (g * 16 + hh * 8 + (c & 7)) * 16);
    __builtin_amdgcn_raw_buffer_store_b128(o, rwb, oo, 0, 0);
    float n2 = 0.f;
#pragma unroll
    for (int r = 0; r < 4; ++r) n2 += ac[0][r] * ac[0][r] + ac[1][r] * ac[1][r];
    n2 = x32sum(x16sum(n2));
    if (g == 0) red[v][n * 16 + c] = n2;
  }
  PROT(2);
  probe(PRB_N0, PRB_N1);
  if (v < 8) latw[v] = lat;
  else if (b < 64 && lane == 0) ((unsigned*)(a.ws + OFF_LAT))[(v - 4) * 64 + b] = umin(lat, 60000u);
  if (WARM_CODE && v == 7) {
    const char* pc; int d0, d1;
    asm("s_getpc_b64 %0\n\ts_mov_b32 %1, _Z10rec_kernel7RecArgs@rel32@lo+4\n\ts_mov_b32 %2, _Z11proj_kernelPKtS0_S0_Pt@rel32@lo+12" : "=s"(pc), "=s"(d0), "=s"(d1));
    const char *c0 = pc + d0, *c1 = pc + d1;
    const long o = ((long)(b >> 3) * 64 + lane) * 64;
    if (o < c1 - c0) asm volatile("" :: "v"(*(const volatile GAS unsigned*)(c0 + o)));
  }
  if (v == 9 && lane < 32) {
    const int q = b * 32 + lane;
    *(u32x4*)(a.ws + OFF_KV + (size_t)(q >> 2) * KVT + 1280 + (q & 3) * 336 + 320) = (u32x4){0x3f803f80u, 0x3f803f80u, 0x3f803f80u, 0x3f803f80u};
  }
  BAR();
  if (v == 1 && lane == 0) ((unsigned*)(a.ws + OFF_LAT))[b] = umin(umin(umin(latw[0], latw[4]) + umin(latw[1], latw[5]), 30000u) + umin(umin(latw[2], latw[6]) + umin(latw[3], latw[7]), 30000u), 60000u);
  if (v == 0) {
    float mx = 0.f;
#pragma unroll
    for (int k = 0; k < 3; ++k) {
      const int col = k * 64 + lane;
      float sm = 0.f;
#pragma unroll
      for (int i = 0; i < 10; ++i) sm += red[i][col < 160 ? col : 0];
      mx = fmx(mx, sm);
    }
    mx = fmx(mx, dppf<DPP_XOR1>(mx)); mx = fmx(mx, dppf<DPP_XOR2>(mx)); mx = fmx(mx, dppf<DPP_ROR4>(mx)); mx = fmx(mx, dppf<DPP_ROR8>(mx));
    mx = x32mx(x16mx(mx));
    if (lane == 0) ((float*)(a.ws + OFF_BNRM))[(cq * 32 + h) * 2 + dh] = mx;
  }
  PROT(3);
}

struct RecArgs {
  const u16* mem0; const u16* wq; const u16* wk; const u16* xold; u16* xnew; char* ws;
  unsigned layer; unsigned pad;
};
#define RLX_AGENT __ATOMIC_RELAXED, __HIP_MEMORY_SCOPE_AGENT
constexpr unsigned SPIN_CAP = 1u << 20;
#define TSTAMP(k, v)
#define ARRIVE(k)
#define ARRIVE_OUT()
#define ARRIVE_DECL()
#define LOGV(k, v)
#define LOGF(k, v)
#define LOGP(k)
#define LOGQ(k)
#define LOGF2(k)
#define LOGD(k)
#define LOGT(k, v)
#define STAMP_DECL()
#define STAMP(k)
#define STAMPV(k, v)
#define STAMP_OUT()
#define REC_D1 520
#define SCH_V 19
#define SCH_VMIN 0
#define SCH_DMIN 4
#define SCH_PEN 170
#define SCH_VMIN2 (-4)
#define SCH_PEN2 40
#define SCH_GU 1
#define SCH_GD 2
#define SCH_UP 6
#define SCH_D0 212
#define SCH_FIRST 120
#define EXP_NODMA 0
#define BG_HOLD 300
#define QBATCH 0
#define WPF0 53
#define BG_HOLD4 330
#define D1_W1 -20
#define F3_D 1200
#define T2W 2
#define AT_ASM 1
#define PM_Z 1
#define P2_X 1
#define ST_KS 14
#define ST_KS0 ST_KS
#define ST_KS1 ST_KS
#define PADR 0x6666
#define PADT 0x000
#define CODE_PAD(n) asm volatile(".p2align 6\n.rept %0\ns_nop 0\n.endr" :: "n"(n))
#define ST_X 290
#define ST_ALL 4
#define CPF_S 61
#define FR_D 700
#define FR_PACE 6
#define END_E60 0
#define END_E1 1300
#define END_E2 5200
#define END_E62 2500
#define END_E3 9800
#define END_E4 14500
#define EARLY_N 9
#define EARLY_D1 120
#define EARLY_V 8
#define EARLY_V1 30
#define F3 8
#define F3A 6
#define F3A_D 400
#define T4_D 300
#define T4_P 1
#define P2_W 1
#define PS_SL 1
#define F2B 6
#define F0C 0
#define F2C 0
#define W2_P 3
#define SCH_W 1
#define SP_SL 0
#define D1_M 400
#define D1_MIN 200
#define D1_C 96
#define D1_G 2
#define BG_HOLD6 0
#define W2_D 150
#define END_E61 1500
#define D1_LATE 200
#define P2_G0 10
#define P2_G2 6
#define P2_G3 6
#define SCH_RL 0
#define SCH_P 3
#define MMA_P 0
#define SP_P 0
#define F6 0
#define F7 8
#define KP0 0
#define KP1 0
#define XPF 1
#define W1_D 0
#define F4 12
#define F5 0
#define F67 0
#define KP2 0
#define REC_THR 32.0f
#define SCH_DRAIN 40
#define PL_X 0
#define FC(k)
#define PIN_V(x) asm volatile("" : "+v"(x))
#define PIN_S(x) asm volatile("" : "+s"(x))
#define VM_DRAIN() do { asm volatile("s_waitcnt vmcnt(0)" ::: "memory"); __builtin_amdgcn_s_waitcnt(0x0F70); } while (0)
#define SC1_LOAD(off) __builtin_amdgcn_raw_buffer_load_b128(rs, (off), 0, 16)
#define SC1_LOADS(off, soff) __builtin_amdgcn_raw_buffer_load_b128(rs, (off), (soff), 16)
#define SC1_STORE8S(v, off, soff) __builtin_amdgcn_raw_buffer_store_b64((v), rs, (off), (soff), 16)
#define SC1_LOAD8(off) __builtin_amdgcn_raw_buffer_load_b64(rs, (off), 0, 16)
#define SC1_STORE(v, off) __builtin_amdgcn_raw_buffer_store_b128((v), rs, (off), 0, 16)
#define SC1_STORE8(v, off) __builtin_amdgcn_raw_buffer_store_b64((v), rs, (off), 0, 16)
#if defined(__HIP_DEVICE_COMPILE__)
#pragma clang attribute push(__attribute__((target("no-packed-fp32-ops"))), apply_to = function)
#endif
__device__ __forceinline__ unsigned now32() { return (unsigned)__builtin_amdgcn_s_memrealtime(); }
__device__ __forceinline__ void wait_until(unsigned t) { for (;;) { const int d = (int)(t - now32()); if (d <= 0) break; if (d > 8) __builtin_amdgcn_s_sleep(1); } }
__device__ __forceinline__ unsigned cyc32() { return (unsigned)__builtin_amdgcn_s_memtime(); }
__device__ __forceinline__ void wait_cyc(unsigned t) { for (;;) { const int d = (int)(t - cyc32()); if (d <= 0) break; if (d > 200) __builtin_amdgcn_s_sleep(1); } }
#define WAIT_ASM(clk, far_) "1:\n\t" clk " vcc\n\ts_waitcnt lgkmcnt(0)\n\ts_sub_i32 %0, %1, vcc_lo\n\ts_cmp_lt_i32 %0, " far_ "\n\ts_cbranch_scc1 3f\n\ts_sleep 1\n\ts_branch 1b\n" \
                            "2:\n\t" clk " vcc\n\ts_waitcnt lgkmcnt(0)\n\ts_sub_i32 %0, %1, vcc_lo\n3:\n\ts_cmp_gt_i32 %0, 0\n\ts_cbranch_scc1 2b"
__device__ __forceinline__ int spin_until(unsigned t) { int d; asm volatile(WAIT_ASM("s_memrealtime", "9") : "=&s"(d) : "s"(t) : "vcc", "scc", "memory"); return d; }
__device__ __forceinline__ int spin_cyc(unsigned t) { int d; asm volatile(WAIT_ASM("s_memtime", "200") : "=&s"(d) : "s"(t) : "vcc", "scc", "memory"); return d; }
template <int OFF, int SL> __device__ __forceinline__ void lds_spin(unsigned lds0, unsigned want) {
  unsigned v;
  asm volatile("1:\n\tds_read_b32 %0, %1 offset:%3\n\t.if %4\n\ts_sleep %4\n\t.endif\n\ts_waitcnt lgkmcnt(0)\n\tv_cmp_gt_u32 vcc, %2, %0\n\ts_cbranch_vccnz 1b"
               : "=&v"(v) : "v"(lds0), "s"(want), "n"(OFF), "n"(SL) : "vcc", "memory");
}
template <int CTRL> __device__ __forceinline__ int dppi(int v) { return __builtin_amdgcn_mov_dpp(v, CTRL, 0xf, 0xf, true); }
__device__ __forceinline__ int wave_imax(int l) {
  l = imax(l, dppi<DPP_XOR1>(l)); l = imax(l, dppi<DPP_XOR2>(l)); l = imax(l, dppi<DPP_ROR4>(l)); l = imax(l, dppi<DPP_ROR8>(l));
  int a = imax(__builtin_amdgcn_readlane(l, 0), __builtin_amdgcn_readlane(l, 16)), b = imax(__builtin_amdgcn_readlane(l, 32), __builtin_amdgcn_readlane(l, 48));
  PIN_S(a); PIN_S(b);
  return imax(a, b);
}
__device__ __forceinline__ void glds16(const void* gsrc, unsigned lds_dst) {
  unsigned keep;
  asm volatile("s_mov_b32 %0, m0\n\ts_mov_b32 m0, %2\n\ts_nop 0\n\tglobal_load_lds_dwordx4 %1, off\n\ts_mov_b32 m0, %0" : "=&s"(keep) : "v"(gsrc), "s"(lds_dst) : "memory");
}
__device__ __forceinline__ void glds16_sc1(const void* gsrc, unsigned lds_dst) {
  unsigned keep;
  asm volatile("s_mov_b32 %0, m0\n\ts_mov_b32 m0, %2\n\ts_nop 0\n\tglobal_load_lds_dwordx4 %1, off sc1\n\ts_mov_b32 m0, %0" : "=&s"(keep) : "v"(gsrc), "s"(lds_dst) : "memory");
}
__device__ __forceinline__ unsigned acc_off(int G, int row) {
  return (unsigned)(G < 128 ? ((G & 31) | ((G & 32) << 1) | ((G & 64) >> 1)) * 256 + row * 16 : (2 * G - 128 + (row >> 2)) * 256 + (row & 3) * 16);
}
__device__ __forceinline__ unsigned nz(unsigned v) { return v > 1u ? v : 1u; }
__device__ __forceinline__ unsigned pack2n(float lo, float hi) { return nz(pack2(lo, hi)); }
__device__ __forceinline__ unsigned min4(u32x4 v) { return umin(umin(v[0], v[1]), umin(v[2], v[3])); }
__device__ __forceinline__ bf16x8 as8(u32x4 v) { return __builtin_bit_cast(bf16x8, v); }

constexpr int NV = 7, NKQ = 2;
constexpr int L_RED1 = 0;
constexpr int L_BM = L_RED1 + 8 * 1536;
constexpr int L_STG = L_BM + 10240;
constexpr int L_ST = L_STG + 2048 + 2560;
constexpr int L_MISC = L_ST + 512;
constexpr int L_KX = L_MISC + 128;
constexpr int L_QX = L_KX + NKQ * 2048;
constexpr int L_XN = L_QX + NKQ * 2048;
constexpr int L_VOX = L_XN + 81920;
constexpr int L_VOM = L_VOX + NV * 5120;
constexpr int L_BYTES = L_VOM + NV * 1280;
constexpr int S_IMG = L_XN + 40960;
static_assert(S_IMG + 61440 <= L_VOM, "start-up image");
static_assert(L_BYTES <= 160 * 1024, "LDS budget");

__device__ __forceinline__ bf16x8 wqk_frag(const char* lds, int t, int ks, int lane) {
  const int c = lane & 15, g = lane >> 4, p = c & 3;
  const char* q = lds + S_IMG + (32 * ks + 8 * g + (c >> 2)) * 96 + 8 * p + (t == 0 ? 0 : (t == 1 ? 48 : (p < 2 ? 32 : 64)));
  const v4i16_t lo = __builtin_amdgcn_ds_read_tr16_b64_v4i16((LAS v4i16_t*)q), hi = __builtin_amdgcn_ds_read_tr16_b64_v4i16((LAS v4i16_t*)(q + 4 * 96));
  return (bf16x8){lo[0], lo[1], lo[2], lo[3], hi[0], hi[1], hi[2], hi[3]};
}
__device__ __forceinline__ bf16x8 sel_frag(int c, int base) {
  bf16x8 r;
#pragma unroll
  for (int i = 0; i < 8; ++i) r[i] = (c == base + i) ? (short)0x3f80 : (short)0;
  return r;
}
__device__ __forceinline__ bf16x8 qk_frag(const char* lds, int g, int c) {
  const char* p4 = lds + L_RED1 + c * 112 + g * 16;
  const char* p2 = lds + L_RED1 + c * 112 + 64 + g * 8;
  f32x4 a0 = *(const f32x4*)(p4), a1 = *(const f32x4*)(p4 + 1792), a2 = *(const f32x4*)(p4 + 3584), a3 = *(const f32x4*)(p4 + 5376);
  f32x2 b0 = *(const f32x2*)(p2), b1 = *(const f32x2*)(p2 + 1792), b2 = *(const f32x2*)(p2 + 3584), b3 = *(const f32x2*)(p2 + 5376);
  f32x4 va = (a0 + a1) + (a2 + a3);
  f32x2 vb = (b0 + b1) + (b2 + b3);
  return mk8(pack2(va[0], va[1]), pack2(va[2], va[3]), pack2(vb[0], vb[1]), 0u);
}
__device__ __forceinline__ void x_fetch(const u16* src, unsigned lds0, int seg, int p, int lane) {
  if (EXP_NODMA) return;
  glds16_sc1(src + (size_t)p * 512 + lane * 8, lds0 + L_XN + (seg & 1) * 40960 + p * 1024);
}
template <int N> __device__ __forceinline__ void x_fetch_n(const u16* src, unsigned lds0, int seg, int p0, unsigned voff) {
  if (EXP_NODMA) return;
#pragma unroll
  for (int i = 0; i < N; i += 4) {
    unsigned keep;
    asm volatile("s_mov_b32 %0, m0\n\ts_mov_b32 m0, %3\n\ts_nop 0\n\tglobal_load_lds_dwordx4 %1, %2 sc1\n\t"
                 ".if %4 > 1\n\tglobal_load_lds_dwordx4 %1, %2 offset:1024 sc1\n\t.endif\n\t.if %4 > 2\n\tglobal_load_lds_dwordx4 %1, %2 offset:2048 sc1\n\t.endif\n\t"
                 ".if %4 > 3\n\tglobal_load_lds_dwordx4 %1, %2 offset:3072 sc1\n\t.endif\n\ts_mov_b32 m0, %0"
                 : "=&s"(keep) : "v"(voff), "s"(src + (size_t)(p0 + i) * 512), "s"(lds0 + L_XN + (seg & 1) * 40960 + (p0 + i) * 1024), "n"(N - i < 4 ? N - i : 4) : "memory");
  }
}
__device__ __forceinline__ int qkv_row(int b) { return 2 * (b & 7) + ((b >> 3) >= 15); }
__device__ __forceinline__ const u16* x_src(const RecArgs& a, int seg, int b) {
  return seg < NSEG ? (const u16*)(a.ws + OFF_XN) + (size_t)seg * 32 * DM : a.xnew + (size_t)(qkv_row(b) + 16 * (seg - NSEG)) * 32 * DM;
}
template <int N>
__device__ __forceinline__ void img_fetch(const RecArgs& a, unsigned lds0, int h, int j, int part, int lane) {
#pragma unroll
  for (int i = 0; i < N; ++i) {
    const int n = part * N + i, idx = n * 64 + lane, row = idx / 6, pc = idx - row * 6;
    if (FR_PACE && N == 20) asm volatile("s_waitcnt vmcnt(%0)" :: "n"(FR_PACE) : "memory");
    glds16((pc < 3 ? a.wq : a.wk - 24) + (size_t)row * HD + h * DH + j * 24 + pc * 8, lds0 + S_IMG + n * 1024);
  }
}
constexpr int P2B = F4 + F5 + 2 * F67 + F6 + F7, P0C = P2B + F2B, F3C = 40 - F3A - F3 - P0C - 2 * F0C - F2C;
static_assert(F3C >= 0, "fetch plan");
template <int OFF> __device__ __forceinline__ void lds_inc(unsigned lds0) {
  asm volatile("s_mov_b64 exec, 1\n\tds_add_u32 %0, %1 offset:%2\n\ts_mov_b64 exec, -1" :: "v"(lds0), "v"(1u), "n"(OFF) : "memory");
}
__device__ __forceinline__ void lds_arrive(unsigned* p, int lane) {
  asm volatile("s_waitcnt lgkmcnt(0)" ::: "memory");
  if (lane == 0) __hip_atomic_fetch_add(p, 1u, __ATOMIC_RELAXED, __HIP_MEMORY_SCOPE_WORKGROUP);
}
__device__ __forceinline__ void lds_wait(unsigned* p, unsigned want) {
  while (__hip_atomic_load(p, __ATOMIC_RELAXED, __HIP_MEMORY_SCOPE_WORKGROUP) < want) __builtin_amdgcn_s_sleep(1);
  asm volatile("" ::: "memory");
}
constexpr float KEXP = 0.07216878364870322f * 1.4426950408889634f;
#define FRESH_LANE() int lane = lane0; asm volatile("" : "+v"(lane)); const int c = lane & 15, g = lane >> 4; (void)c; (void)g

struct OutTask {
  u32x4 y[4]; u32x2 xo, out;
  bf16x8 sel;
  unsigned t5x, t5f, t5o;
  __device__ __forceinline__ void init(int b, int lane) {
    const int um = b / 10, uq = (b / 5) & 1, up = b % 5, tok = lane & 15, k = up < 4 ? (up >> 1) * 256 + um * 16 + (up & 1) * 8 : 512 + um * 8, q4 = ((lane >> 4) & 1) * 4;
    t5x = (unsigned)((uq * 16 + tok) * DM + k + q4);
    t5f = (unsigned)(((uq * 20 + (k >> 5)) * 64 + ((k & 31) >> 3) * 16 + tok) * 8 + q4);
    t5o = (unsigned)(((um * 2 + uq) * 16 + (lane >> 4)) * 1280 + up * 256 + tok * 16);
    if (b >= 160) { t5x = t5f = 0u; t5o = 0x7ffffff0u; }
    sel = sel_frag(tok, 0);
    PIN_V(sel);
  }
  static __device__ __forceinline__ bool ok(int sl, int b) { return sl >= 0 && b < 160; }
  __device__ __forceinline__ void load(const RecArgs& a, const __amdgpu_buffer_rsrc_t rs, int sl, int b, int part = 3) {
    const unsigned o = t5o | (sl < 0 ? 0x7ff00000u : 0u);
    sl = imax(sl, 0);
#pragma unroll
    for (int k = 0; k < 4; ++k) if (part & 1) y[k] = SC1_LOADS(o, op_slot(sl) + k * 5120);
    if (part & 2) xo = __builtin_bit_cast(u32x2, __hip_atomic_load((const gu64*)(a.xold + (size_t)sl * 32 * DM + (a.layer ? t5f : t5x)), RLX_AGENT));
  }
  __device__ __forceinline__ bool ready(int b) { return b >= 160 || wave_all(umin(umin(min4(y[0]), min4(y[1])), umin(min4(y[2]), min4(y[3]))) != 0u); }
  __device__ __forceinline__ void run(int lane) {
    f32x4 d = {0.f, 0.f, 0.f, 0.f};
#pragma unroll
    for (int k = 0; k < 4; ++k) d = mfma32(sel, as8(y[k]), d);
#pragma unroll
    for (int i = 0; i < 2; ++i) {
      const unsigned r = pack2(d[2 * i], d[2 * i + 1]), x = xo[i];
      out[i] = pack2(u2f(x << 16) + u2f(r << 16), u2f(x & 0xffff0000u) + u2f(r & 0xffff0000u));
    }
    PIN_V(out);
  }
  __device__ __forceinline__ void store(const RecArgs& a, int sl, int b, int lane) {
    if (ok(sl, b) && lane < 32) __hip_atomic_store((gu64*)(a.xnew + (size_t)sl * 32 * DM + t5f), (u64)out[0] | ((u64)out[1] << 32), RLX_AGENT);
  }
};

struct PvTask {
  u32x4 y[2]; u32x2 xo[2];
  __device__ __forceinline__ void load(const __amdgpu_buffer_rsrc_t rs, unsigned ck, int sl, int gp, int qt, int lane) {
    const unsigned o = pt_slot(ck, sl);
#pragma unroll
    for (int k = 0; k < 2; ++k) {
      y[k] = SC1_LOAD(o + pt_tok(2 * gp) + k * 2048 + qt * 1024 + lane * 16);
      xo[k] = SC1_LOAD8(o + pt_mem(2 * gp) + k * 512 + qt * 256 + (lane & 31) * 8);
    }
  }
  __device__ __forceinline__ bool ready() { return wave_all(umin(umin(min4(y[0]), min4(y[1])), umin(umin(xo[0][0], xo[0][1]), umin(xo[1][0], xo[1][1]))) != 0u); }
  __device__ __forceinline__ void run(char* lds, const __amdgpu_buffer_rsrc_t rs, int sl, int gp, int m, int qt, int lane, bool direct) {
    const f32x4 Z4 = {0.f, 0.f, 0.f, 0.f};
    PIN_V(y[0]); PIN_V(y[1]); PIN_V(xo[0]); PIN_V(xo[1]);
    const int sv = (sl + 2 * NV) % NV;
    const char* vp = lds + L_VOX + sv * 5120 + lane * 16;
    const char* mp = lds + L_VOM + sv * 1280 + (lane & 31) * 8;
    bf16x8 pm[2], vm[5], vx[5];
#pragma unroll
    for (int t = 0; t < 5; ++t) { vx[t] = *(const bf16x8*)(vp + t * 1024); const u32x2 v = *(const u32x2*)(mp + t * 256); vm[t] = mk8(v[0], v[1], 0u, 0u); }
#pragma unroll
    for (int k = 0; k < 2; ++k) pm[k] = mk8(lane < 32 ? xo[k][0] : 0u, lane < 32 ? xo[k][1] : 0u, 0u, 0u);
    __builtin_amdgcn_sched_barrier(0);
    f32x4 o[4];
#pragma unroll
    for (int k = 0; k < 2; ++k) { o[k] = mfma32(vx[k], as8(y[0]), Z4); o[2 + k] = mfma32(vx[2], as8(y[k]), Z4); }
#pragma unroll
    for (int k = 0; k < 2; ++k) { o[k] = mfma32(vm[k], pm[0], o[k]); o[2 + k] = mfma32(vm[2], pm[k], o[2 + k]); }
#pragma unroll
    for (int k = 0; k < 2; ++k) o[k] = mfma32(vx[3 + k], as8(y[1]), o[k]);
#pragma unroll
    for (int k = 0; k < 2; ++k) o[k] = mfma32(vm[3 + k], pm[1], o[k]);
    float oc[4];
#pragma unroll
    for (int r = 0; r < 4; ++r) {
      auto sw = __builtin_amdgcn_permlane32_swap(f2u(o[3][r]), f2u(o[3][r]), false, false);
      oc[r] = o[2][r] + u2f(sw[1]);
    }
    const u32x2 v0 = {pack2n(o[0][0], o[0][1]), pack2n(o[0][2], o[0][3])}, v1 = {pack2n(o[1][0], o[1][1]), pack2n(o[1][2], o[1][3])};
    const u32x2 v2 = {pack2n(oc[0], oc[1]), pack2n(oc[2], oc[3])};
    const unsigned po = (unsigned)(((lane >> 5) * 16 + (lane & 15)) * 16 + ((lane >> 4) & 1) * 8);
    if (direct) {
      const unsigned oo = op_slot(sl) + (unsigned)((m * 32 + qt * 16 + gp) * 1280) + po;
      SC1_STORE8(v0, oo); SC1_STORE8(v1, oo + 512);
      if (lane < 32) SC1_STORE8(v2, oo + 1024);
    } else {
      char* sp = lds + L_STG + 2048 + qt * 1280 + po;
      *(u32x2*)(sp) = v0; *(u32x2*)(sp + 512) = v1;
      if (lane < 32) *(u32x2*)(sp + 1024) = v2;
    }
  }
};

struct PvTask2 {
  u32x4 y[2][2], pa[2], pb[2];
  unsigned lo, lm, vr, ma, mz, so, sz;
  __device__ __forceinline__ void init(int gp, int lane) {
    const int hi = lane >> 5;
    lo = pt_tok(2 * gp) + (unsigned)(lane * 16); lm = pt_mem(2 * gp + hi) + (unsigned)((lane & 31) * 8);
    vr = (unsigned)(L_VOX + 2048 + (lane ^ 8) * 16);
    ma = (unsigned)(L_VOM + hi * 768 + (lane & 31) * 8); mz = (unsigned)(L_VOM + 512 + ((lane & 31) ^ (hi * 8)) * 8);
    so = (unsigned)(L_STG + 2048 + (hi * 16 + (lane & 15)) * 16 + ((lane >> 4) & 1) * 8);
    sz = hi ? (unsigned)(L_RED1 + 8704 + lane * 8) : so + 1024;
    PIN_V(lo); PIN_V(lm); PIN_V(vr); PIN_V(ma); PIN_V(mz); PIN_V(so); PIN_V(sz);
#pragma unroll
    for (int q = 0; q < 2; ++q) { pa[q] = pb[q] = y[q][0] = y[q][1] = (u32x4){0u, 0u, 0u, 0u}; PIN_V(pa[q]); PIN_V(pb[q]); }
  }
  __device__ __forceinline__ void load(const __amdgpu_buffer_rsrc_t rs, unsigned o) {
#pragma unroll
    for (int q = 0; q < 2; ++q) {
      y[q][0] = SC1_LOADS(lo + q * 1024, o); y[q][1] = SC1_LOADS(lo + 2048 + q * 1024, o);
      const u32x2 p = __builtin_amdgcn_raw_buffer_load_b64(rs, lm + q * 256, o, 16);
      pa[q][0] = p[0]; pa[q][1] = p[1];
    }
  }
  __device__ __forceinline__ void run(char* lds, int sv, int lane) {
    const f32x4 Z4 = {0.f, 0.f, 0.f, 0.f};
    const char* vp = lds + L_VOX + sv * 5120 + lane * 16;
    bf16x8 vx[6];
#pragma unroll
    for (int t = 0; t < 5; ++t) vx[t] = *(const bf16x8*)(vp + t * 1024);
    vx[5] = *(const bf16x8*)(lds + vr + sv * 5120);
    const u32x2 a0 = *(const u32x2*)(lds + ma + sv * 1280), a1 = *(const u32x2*)(lds + ma + sv * 1280 + 256);
    const u32x2 z0 = *(const u32x2*)(lds + mz + sv * 1280), z1 = *(const u32x2*)(lds + mz + sv * 1280 + 256);
    const bf16x8 vm = mk8(a0[0], a0[1], a1[0], a1[1]), vz = mk8(z0[0], z0[1], z1[0], z1[1]);
#pragma unroll
    for (int q = 0; q < 2; ++q) { PIN_V(pa[q]); pb[q][2] = pa[q][0]; pb[q][3] = pa[q][1]; PIN_V(pb[q]); }
    __builtin_amdgcn_sched_barrier(0);
    f32x4 o[2][3];
#pragma unroll
    for (int q = 0; q < 2; ++q) {
#pragma unroll
      for (int t = 0; t < 3; ++t) o[q][t] = mfma32(vx[t], as8(y[q][0]), Z4);
#pragma unroll
      for (int t = 0; t < 3; ++t) o[q][t] = mfma32(vx[3 + t], as8(y[q][1]), o[q][t]);
      o[q][0] = mfma32(vm, as8(pa[q]), o[q][0]); o[q][1] = mfma32(vm, as8(pb[q]), o[q][1]); o[q][2] = mfma32(vz, as8(pa[q]), o[q][2]);
    }
#pragma unroll
    for (int q = 0; q < 2; ++q) {
#pragma unroll
      for (int t = 0; t < 3; ++t)
        *(u32x2*)(lds + (t < 2 ? so + t * 512 : sz) + q * 1280) = (u32x2){pack2n(o[q][t][0], o[q][t][1]), pack2n(o[q][t][2], o[q][t][3])};
    }
  }
};

template <class F> __device__ __forceinline__ void t2_tok(char* lds, const __amdgpu_buffer_rsrc_t rs, unsigned off, unsigned o, int seg, int lane, F mid) {
  const f32x4 Z4 = {0.f, 0.f, 0.f, 0.f};
  const char* kp = lds + L_KX + (seg & 1) * 2048 + lane * 16;
  const char* qp = lds + L_QX + (seg & 1) * 2048 + lane * 16;
  const bf16x8 k0 = *(const bf16x8*)(kp), k1 = *(const bf16x8*)(kp + 1024);
  const bf16x8 q[2] = {*(const bf16x8*)(qp), *(const bf16x8*)(qp + 1024)};
  mid();
#pragma unroll
  for (int qt = 0; qt < 2; ++qt) {
    const f32x4 d1 = mfma32(k0, q[qt], Z4), d2 = mfma32(k1, q[qt], Z4);
    __builtin_amdgcn_raw_buffer_store_b128((u32x4){pack2n(d1[0], d1[1]), pack2n(d1[2], d1[3]), pack2n(d2[0], d2[1]), pack2n(d2[2], d2[3])}, rs, off + qt * 1024, o, 16);
  }
}

__device__ __forceinline__ void touch(const char* base, unsigned off, unsigned lds0) {
  unsigned keep;
  asm volatile("s_mov_b32 %0, m0\n\ts_mov_b32 m0, %3\n\ts_nop 0\n\tglobal_load_lds_dword %1, %2 sc1\n\ts_mov_b32 m0, %0" : "=&s"(keep) : "v"(off), "s"(base), "s"(lds0 + L_RED1 + 8192) : "memory");
}
__device__ __forceinline__ void code_touch(int b, int lane, unsigned lds0) {
  const char* pc; int d0, d1;
  asm volatile("s_getpc_b64 %0\n\ts_mov_b32 %1, _Z10rec_kernel7RecArgs@rel32@lo+4\n\ts_mov_b32 %2, _Z11attb_kernelPKtS0_Pt@rel32@lo+12" : "=s"(pc), "=s"(d0), "=s"(d1));
  const int o = ((b >> 3) * 64 + lane) * 64;
  touch(pc + d0, (unsigned)(o < d1 - d0 ? o : 0), lds0);
}
__device__ __forceinline__ unsigned zero_base(int b, int h, int j, int ln) {
  return ln < 32 ? (unsigned)((h & 3) * 32768 + j * 512 + ln * 16) : ln < 40 ? (unsigned)((h & 2) * 1024 + j * 256 + (ln & 4) * 32 + (h & 1) * 64 + (ln & 3) * 16)
                 : (unsigned)OFF_ACC + (b < ACC_NB ? (unsigned)(b * 256 + (ln - 40) * 16) : ts_piece((b - ACC_NB) * 8 + ln - 40));
}
__device__ __forceinline__ unsigned sct_slot(unsigned ck, unsigned sl) { return sl * SCT_STEP + ((ck >> (sl & 4u)) & 7u) * 4096u; }
__device__ __forceinline__ unsigned scm_slot(unsigned ck, unsigned sl) { return OFF_SCM + sl * SCM_STEP + ((ck >> 8) & 7u) * 4096u; }
__device__ __forceinline__ int ck_rot(int x) { return ((x & 1) << 2) | (x >> 1); }
struct Place {
  unsigned t[8];
  __device__ __forceinline__ void load(const char* ws, int lane) {
#pragma unroll
    for (int x = 0; x < 8; ++x) t[x] = *(const unsigned*)(ws + OFF_LAT + ((imin(lane >> 3, 5) * 8 + (ck_rot(x) ^ (lane & 7))) * 8 + x) * 4);
  }
  __device__ __forceinline__ unsigned get(int b, int lane) {
    int c = (int)((((t[0] + t[1]) + (t[2] + t[3])) + ((t[4] + t[5]) + (t[6] + t[7]))) * 8u) + (lane & 7);
    c = imin(c, dppi<DPP_XOR1>(c)); c = imin(c, dppi<DPP_XOR2>(c)); c = imin(c, dppi<0x141>(c));
    unsigned k = 0u;
#pragma unroll
    for (int r = 0; r < 6; ++r) k |= (unsigned)(__builtin_amdgcn_readlane(c, 8 * r) & 7) << (4 * r);
    return (k ^ PL_X) ^ (unsigned)ck_rot(b & 7) * 0x111111u;
  }
};

struct Sched {
  u32x4 tsv, rec;
  __device__ __forceinline__ void load(const __amdgpu_buffer_rsrc_t rs, int s, int lane) {
    const unsigned o = (unsigned)OFF_ACC + (unsigned)((s + SR - 1) % SR) * (unsigned)ACC_STEP + ACC_TS;
    tsv = SC1_LOADS(lane * 256, o); rec = SC1_LOADS(128, o);
  }
  __device__ __forceinline__ unsigned run(const RecArgs& a, const __amdgpu_buffer_rsrc_t rs, int s, int b, int lane) {
    constexpr int w = 3; (void)w;
    if (__builtin_expect((SCH_RL || s == 0) && !wave_all(min4(tsv) != 0u), 0)) {
      int it = s ? 39 : 0;
      do {
        asm volatile("" ::: "memory");
        load(rs, s, lane);
        if (wave_all(min4(tsv) != 0u)) break;
        __builtin_amdgcn_s_sleep(ST_ALL);
      } while (++it < 40);
    }
    const unsigned ref = now32();
    int l = -100000;
#pragma unroll
    for (int i = 0; i < 4; ++i) l = imax(l, tsv[i] ? (int)(tsv[i] - ref) : -100000);
    l = wave_imax(l);
    const unsigned L = ref + (unsigned)l;
    unsigned acur = __builtin_amdgcn_readfirstlane(rec[0]), aprev = __builtin_amdgcn_readfirstlane(rec[1]);
    unsigned r2 = __builtin_amdgcn_readfirstlane(rec[2]), r3 = __builtin_amdgcn_readfirstlane(rec[3]);
    PIN_S(r2); PIN_S(r3);
    int D8 = acur ? (int)r2 : 8 * SCH_D0;
    unsigned hist = acur ? r3 : 0u;
    const bool late = (acur == 0u) | ((int)(L + SCH_VMIN - acur) > 0);
    if (late && b == 0) FC(3);
    int up = hist ? 8 * SCH_UP : 24 * SCH_UP;
    up = acur ? up : 0;
    const int d8 = 8 * imin(imax((int)(L - aprev), 1), 4000);
    int Du = D8 + ((d8 - D8) >> SCH_GU), Dd = D8 - ((D8 - d8) >> SCH_GD), Dl = D8 + up;
    PIN_S(Du); PIN_S(Dd); PIN_S(Dl);
    int Dm = d8 > D8 ? Du : Dd;
    Dm = hist < 2u ? d8 : Dm;
    unsigned hm = umin(hist + 1u, 2u);
    PIN_S(Dm); PIN_S(hm);
    Dm = aprev ? Dm : D8; hm = aprev ? hm : hist;
    D8 = late ? Dl : Dm; hist = late ? hist : hm;
    const int D = (D8 + 4) >> 3;
    unsigned bl = L + SCH_V + ((int)(L + SCH_VMIN2 - acur) <= 0 ? SCH_PEN2 : SCH_PEN), b0 = L + SCH_FIRST, a0 = ST_X ? (L + ST_X) | 1u : 0u;
    PIN_S(bl); PIN_S(b0); PIN_S(a0);
    bl = late ? bl : acur;
    bl = acur ? bl : b0;
    unsigned anext = (bl + (unsigned)D + SCH_V + (s == 1 ? EARLY_V1 : (s < EARLY_N ? EARLY_V : 0))) | 1u;
    PIN_S(anext);
    anext = s == 0 ? a0 : anext;
    anext = l < -50000 ? 0u : anext;
    rec = (u32x4){anext, late ? 0u : acur, (unsigned)D8, hist};
    if (__builtin_expect(b == 0, 0)) __builtin_amdgcn_raw_buffer_store_b128(rec, rs, lane == 0 ? 0 : 0x7ffffff0, (unsigned)OFF_ACC + (unsigned)(s % SR) * (unsigned)ACC_STEP + ACC_TS + 128u, 16);
    LOGV(0, anext); LOGV(1, late); LOGV(2, D); LOGV(3, L - aprev); LOGV(4, acur - L); LOGV(5, aprev);
    return anext;
  }
};

template <int w>
__device__ __forceinline__ void token_waves(const RecArgs& a, char* lds, const __amdgpu_buffer_rsrc_t rs) {
  const int lane0 = __builtin_amdgcn_workitem_id_x() & 63, b = __builtin_amdgcn_workgroup_id_x(), wid = w + 4; (void)wid;
  const int gp = (b & 7) * 2 + (b >> 7), m = (b >> 3) & 15, h0 = 2 * gp, h = h0 + (m >> 3), j = m & 7;
  float* const misc = (float*)(lds + L_MISC);
  const f32x4 Z4 = {0.f, 0.f, 0.f, 0.f};
  const unsigned lds0 = (unsigned)(uintptr_t)(LAS char*)lds;
  unsigned* const cnt = (unsigned*)(misc + 6);
  const float* const xnrm = (const float*)(a.ws + OFF_XNRM);
  bf16x16 W[2][10];
  auto btile = [&](int i) { return w == 1 ? 0 : (w == 2 ? 1 + i : 3 + i); };
  if (w == 1) code_touch(b, lane0, lds0);
  Place pl;
  if (w == 3) pl.load(a.ws, lane0);
  if (a.layer) {
    if (w != 0) img_fetch<20>(a, lds0, h, j, w - 1, lane0);
  } else {
    if (w == 0) img_fetch<12>(a, lds0, h, j, 4, lane0);
#pragma unroll
    for (int i = 0; i < 10; ++i) x_fetch(x_src(a, 0, b), lds0, 0, 10 * w + i, lane0);
  }
#pragma unroll
  for (int i = 0; i < 2; ++i)
#pragma unroll
    for (int p = 0; p < 10; ++p)
      if (w >= 2 || (w == 1 && i == 1)) {
        const char* wp = a.ws + OFF_WB + ((size_t)(b * 5 + btile(i)) * 20 + 2 * p) * 1024 + lane0 * 16;
        if (FR_PACE && a.layer) asm volatile("s_waitcnt vmcnt(%0)" :: "n"(FR_PACE) : "memory");
        W[i][p] = cat16(*(const bf16x8*)wp, *(const bf16x8*)(wp + 1024));
      }
  f32x4 bn = Z4;
  if (w == 0) {
    bn = *(const f32x4*)(a.ws + OFF_BNRM + lane0 * 16);
    if (!a.layer) asm volatile("s_waitcnt vmcnt(0)" ::: "memory");
  } else asm volatile("s_waitcnt vmcnt(%0)" :: "n"(w == 1 ? 20 : 40) : "memory");
  unsigned ck = 0u;
  if (w == 3) { ck = pl.get(b, lane0); *(unsigned*)(misc + 11) = ck; }
  BAR();
#pragma unroll
  for (int p = 0; p < 10; ++p) {
    if (w < 2) W[0][p] = cat16(wqk_frag(lds, 2 * w, 2 * p, lane0), wqk_frag(lds, 2 * w, 2 * p + 1, lane0));
    if (w == 0) W[1][p] = cat16(wqk_frag(lds, 1, 2 * p, lane0), wqk_frag(lds, 1, 2 * p + 1, lane0));
  }
  float cb2 = 0.f;
  if (w == 0) {
    float n = __builtin_amdgcn_sqrtf(bn[0] + bn[1]) + __builtin_amdgcn_sqrtf(bn[2] + bn[3]);
    n += dppf<DPP_XOR1>(n); n += dppf<DPP_XOR2>(n); n += dppf<DPP_ROR4>(n); n += dppf<DPP_ROR8>(n);
    n = x32mx(x16mx(n));
    cb2 = u2f(__builtin_amdgcn_readfirstlane(f2u(1.15f * n * n)));
  }
  const float cm2 = u2f((f2u(cb2) & 0x7fffffu) | 0x40000000u);
  const int ek = imax((int)((f2u(cb2) >> 23) & 0xffu), 1) - 252;
  if (w >= 2) {
#pragma unroll
    for (int i = 0; i < 4; ++i) *(unsigned*)(lds + L_KX + (w - 2) * 4096 + i * 1024 + lane0 * 16 + 12) = 0u;
  }
  OutTask ot;
  const int zl = lane0 - 16;
  const bool zon = w == 3 && zl >= 0 && (zl < 40 || b < ACC_NB + 9);
  unsigned zo = zon ? zero_base(b, h, j, zl) + (zl < 32 ? sct_slot(ck, 0u) : (zl < 40 ? scm_slot(ck, 0u) : 0u)) : 0x7ffffff0u;
  unsigned zm = zon ? (zl < 32 ? SCT_STEP + 1u : (zl < 40 ? SCM_STEP : (unsigned)ACC_STEP)) : 0u;
  const unsigned zx = ((ck ^ (ck >> 4)) & 7u) * 4096u;
  if (w == 3) { PIN_V(zo); PIN_V(zm); }
  auto st_out = [&](int s, int lane) {
    if (w < 2) return;
    const int sl = s - 2 - LAG4;
    const char* sp = lds + L_STG + 2048 + (w & 1) * 1280;
    const u32x4 v0 = *(const u32x4*)(sp + lane * 16), v1 = *(const u32x4*)(sp + 1024 + (lane & 15) * 16), z = {0u, 0u, 0u, 0u};
    const unsigned oo = (sl < 0 ? 0x7ff00000u : op_slot(sl) + (unsigned)((m * 32 + (w & 1) * 16 + gp) * 1280)) + (unsigned)(lane * 16);
    if (w == 2) {
      ot.store(a, s - 2 - LAG5, b, lane);
      SC1_STORE(v0, oo);
      SC1_STORE(v1, lane < 16 ? oo + 1024 : 0x7ffffff0u);
    } else {
      const unsigned zs = (unsigned)(s + 2) % SR;
      unsigned za = (zo + (s < 0 ? 1024u : zs) * (zm & ~1u)) ^ ((zm & 1u) * (zs & 4u ? zx : 0u));
      za = lane < 16 ? oo + 1024 : za;
      PIN_V(za);
      SC1_STORE(v0, oo);
      u32x4 vz;
#pragma unroll
      for (int i = 0; i < 4; ++i) { vz[i] = lane < 16 ? v1[i] : z[i]; PIN_V(vz[i]); }
      SC1_STORE(vz, za);
      const unsigned xo_ = (unsigned)((s - 8) * 1024 + lane * 16);
      if (__builtin_expect((unsigned)(s - 8) < 20u, 0)) SC1_STORE(z, xo_ < X_BYTES / 256 ? (unsigned)(OFF_X + b * (X_BYTES / 256)) + xo_ : 0x7ffffff0u);
    }
  };
  STAMP_DECL(); ARRIVE_DECL();
  auto mma_seg = [&](int seg, int sv, int lane, auto pause) {
    const int c = lane & 15, g = lane >> 4;
    const char* xp = lds + L_XN + (seg & 1) * 40960 + lane * 16;
    f32x4 acc[2][2] = {{Z4, Z4}, {Z4, Z4}};
    bf16x8 x[4][2];
#pragma unroll
    for (int p = 0; p < 3; ++p) { x[p][0] = *(const bf16x8*)(xp + p * 1024); x[p][1] = *(const bf16x8*)(xp + (20 + p) * 1024); }
    unsigned eo[2];
#pragma unroll
    for (int ks = 0; ks < 20; ++ks) {
      if (ks == (w == 0 ? KP0 : (w == 1 ? KP1 : KP2))) { pause(); __builtin_amdgcn_sched_barrier(0); }
      if (ks == 17) {
#pragma unroll
        for (int i = 0; i < 2; ++i) {
          eo[i] = (unsigned)(w == 0 ? (i ? L_KX : L_QX) + (seg % NKQ) * 2048 + lane * 16
                                    : (w == 1 && i == 0 ? ((g >> 1) ? L_KX : L_QX) + (seg % NKQ) * 2048 + (2 * (g & 1) * 16 + c) * 16 + 8 : L_VOX + sv * 5120 + btile(i) * 1024 + lane * 16));
          if (i == 0 || w == 1) PIN_V(eo[i]);
        }
        if (w == 0) eo[1] = eo[0] + (L_KX - L_QX);
        if (w >= 2) eo[1] = eo[0] + 1024;
      }
      __builtin_amdgcn_sched_barrier(0);
#pragma unroll
      for (int q = 0; q < 4; ++q) {
        const int tt = 1 - (q >> 1), i = q & 1;
        const bool isqk = w == 0 || (w == 1 && i == 0);
        const bf16x8 wf = half8(W[i][ks >> 1], ks & 1);
        acc[i][tt] = isqk ? mfma32(wf, x[ks & 3][tt], acc[i][tt]) : mfma32(x[ks & 3][tt], wf, acc[i][tt]);
        __builtin_amdgcn_sched_barrier(0);
        if (q < 2 && ks + 3 < 20) x[(ks + 3) & 3][q] = *(const bf16x8*)(xp + (q * 20 + ks + 3) * 1024);
        __builtin_amdgcn_sched_barrier(0);
      }
    }
#pragma unroll
    for (int i = 0; i < 2; ++i) {
      if (w + i == 1) { const float ks_ = (w == 0 || (g >> 1)) ? KEXP : 1.0f; acc[i][0] *= ks_; acc[i][1] *= ks_; }
      const u32x4 v = {pack2(acc[i][0][0], acc[i][0][1]), pack2(acc[i][0][2], acc[i][0][3]), pack2(acc[i][1][0], acc[i][1][1]), pack2(acc[i][1][2], acc[i][1][3])};
      char* p = lds + eo[i];
      if (w == 0) { *(u32x2*)(p) = (u32x2){v[0], v[1]}; *(u32x2*)(p + 1024) = (u32x2){v[2], v[3]}; }
      else if (w == 1 && i == 0) { *(unsigned*)(p) = v[0]; *(unsigned*)(p + 256) = v[1]; *(unsigned*)(p + 1024) = v[2]; *(unsigned*)(p + 1280) = v[3]; }
      else *(u32x4*)p = v;
    }
  };
  auto vo_mem = [&](int s, int sv, unsigned t0, int lane, auto last) {
    unsigned vp = (unsigned)(L_VOM + sv * 1280 + (btile(0) * 32 + (w == 1 ? lane & 31 : lane)) * 8);
    float inv = misc[4 + (s & 1)], xmax = misc[s & 3];
    unsigned mp = (unsigned)(L_BM + lane * 16);
    int sidx = (lane & 8) ? 0xEEEE : 0x4444;
    f32x4 acc[2] = {Z4, Z4};
    int ex = 0, ei = 0;
    unsigned ip = (unsigned)(L_MISC + 16 + ((s + 1) & 1) * 4);
    PIN_V(inv); PIN_V(mp);
    if (w != 0) { PIN_V(vp); PIN_V(sidx); PIN_V(acc[1]); }
    if (w >= 2) PIN_V(acc[0]);
    __builtin_amdgcn_s_setprio(2);
    if (w >= 2 && BG_HOLD6) spin_cyc(t0);
    bf16x8 x[10];
#pragma unroll
    for (int p = 0; p < 10; ++p) x[p] = *(const bf16x8*)(lds + mp + p * 1024);
    __builtin_amdgcn_sched_barrier(0);
    if (w == 0) {
      ex = imax((int)((__builtin_amdgcn_readfirstlane(f2u(__builtin_amdgcn_sqrtf(cb2 * xmax))) >> 23) & 0xffu) + 1, 32);
      ei = (int)(__builtin_amdgcn_readfirstlane(f2u(inv)) >> 23);
      PIN_S(ex); PIN_S(ei); PIN_V(ip);
    }
    if (w < 2) spin_cyc(t0);
    STAMP(5);
    __builtin_amdgcn_sched_barrier(0);
#pragma unroll
    for (int p = 0; p < 10; ++p) {
      if (w == 0) acc[0] = mfma32(x[p], x[p], acc[0]);
#pragma unroll
      for (int i = (w == 1); i < 2 && w != 0; ++i) acc[i] = smfma64(x[p], W[i][p], acc[i], sidx);
      __builtin_amdgcn_sched_barrier(0);
    }
    STAMP(7);
    if (w != 0) {
      float v[4];
#pragma unroll
      for (int r = 0; r < 4; ++r) {
        const auto sw = __builtin_amdgcn_permlane32_swap(f2u(acc[w == 1][r]), f2u(acc[1][r]), false, false);
        v[r] = (u2f(sw[0]) + u2f(sw[1])) * inv;
      }
      *(u32x2*)(lds + vp) = (u32x2){pack2(v[0], v[1]), pack2(v[2], v[3])};
    } else {
      const int t = (int)f2u(fmx(fmx(acc[0][0], acc[0][1]), fmx(acc[0][2], acc[0][3])) * cm2);
      const int et = wave_imax(t) >> 23, em = ((et + ek) >> 1) + ei;
      int e = imax(et == 255 ? 256 : em, ex);
      PIN_S(e);
      e = imin(e, 256);
      *(unsigned*)(lds + ip) = (unsigned)(e - 31) << 23;
      *(u32x2*)(misc + 8) = (u32x2){(unsigned)(285 - e) << 23, (unsigned)(e - 87) < 79u ? f2u(REC_THR) : f2u(-1.0f)};
    }
    if (last == 0 && w >= 2) asm volatile("s_waitcnt vmcnt(%0)" :: "n"(w == SCH_W ? 2 : 0) : "memory");
    lds_inc<L_MISC + 28>(lds0);
    STAMP(8);
    if (last) __builtin_amdgcn_s_setprio(0);
    if (last == 0) { if (w >= 2 && F67) lds_inc<L_MISC + 112>(lds0); }
    else if (w == 2) { VM_DRAIN(); lds_inc<L_MISC + 28>(lds0); }
  };
  unsigned* const cntq = (unsigned*)(misc + 12);
  unsigned* const cntt = (unsigned*)(misc + 26 + (w & 1));
  unsigned* const cntp = (unsigned*)(misc + 23);
  unsigned* const cntx2 = (unsigned*)(misc + 28);
  float xn4 = 0.f;
  auto late = [&](int s, int lane) {
    if (w == 2 || (w == 0 && F4) || (w == 1 && F5) || (w == 3 && F7)) lds_spin<L_MISC + 48, 1>(lds0, 2u * (unsigned)imax(s + 1, 0));
    if (w == 2) {
      ot.load(a, rs, s - LAG5, b);
      xn4 = u2f(__hip_atomic_load((const GAS unsigned*)xnrm + (imax(s + 1, 0) * 32 + (lane & 31)) * 4, RLX_AGENT));
    }
    if (w >= 2) { x_fetch_n<w == 2 ? F6 : F7>(x_src(a, s + 3, b), lds0, s + 3, F4 + F5 + 2 * F67 + (w == 3 ? F6 : 0), lane * 16); return; }
    x_fetch_n<w == 0 ? F4 : F5>(x_src(a, s + 3, b), lds0, s + 3, w == 0 ? 0 : F4, lane * 16);
    if (w == 0 && __builtin_expect(s >= WPF0 && s < WPF0 + 10, 0))
      touch(a.ws + OFF_WQKVBT + (size_t)(b >> 3) * 76800 + (s - WPF0) * 7680, imin(lane, 59) * 128, lds0);
    if (w == 1 && __builtin_expect(s == CPF_S, 0)) code_touch(b, lane, lds0);
    lds_spin<L_MISC + 104 + 4 * (w & 1), PS_SL>(lds0, (unsigned)imax(s + 1, 0));
  };
  auto xn_put = [&](int s) {
    float xn2 = xn4;
    xn2 = fmx(xn2, dppf<DPP_XOR1>(xn2)); xn2 = fmx(xn2, dppf<DPP_XOR2>(xn2));
    xn2 = fmx(xn2, dppf<DPP_ROR4>(xn2)); xn2 = fmx(xn2, dppf<DPP_ROR8>(xn2));
    misc[s & 3] = x16mx(xn2);
  };
  if (w == 2) { ot.init(b, lane0); ot.out = (u32x2){0u, 0u}; }
  VM_DRAIN();
  lds_arrive(cnt, lane0);
  lds_wait(cnt, 8u);
  Sched sch;
  int sv = NV - 1;
  CODE_PAD((PADT >> (4 * w)) & 15);
#pragma unroll 1
  for (int s = -1; s < NSEG - 1; ++s) {
    const int sv1 = sv == NV - 1 ? 0 : sv + 1;
    {
      FRESH_LANE();
      STAMP(0);
      if (F67) lds_spin<L_MISC + 112, 1>(lds0, 2u * (unsigned)imax(s, 0));
      mma_seg(s + 1, sv1, lane, [&]() { STAMP(1); late(s - 1, lane); STAMP(9); });
      STAMP(2);
      __builtin_amdgcn_sched_barrier(0);
      VM_DRAIN();
      u32x2 keep;
      if (w == 2) { keep = ot.out; ot.run(lane); xn_put(s); const u32x2 t = ot.out; ot.out = keep; keep = t; }
      if (w >= 2) { lds_spin<L_MISC + 92, 1>(lds0, 4u * (unsigned)imax(s, 0)); if (W1_D) spin_cyc(cyc32() + W1_D); }
      STAMP(10);
      st_out(s, lane);
      if (w == 2) ot.out = keep;
      __builtin_amdgcn_sched_barrier(0);
      if (w == 2 && XPF) touch(a.ws + OFF_XN + (size_t)imin(imax(s + 3, s < 2 ? 0 : 5), NSEG - 1) * (s < 2 ? 0 : 40960) + (b >> 3) * 1280, imin(lane, 9) * 128, lds0);
      if (w >= 2 && s > 0) x_fetch_n<F67>(x_src(a, s + 2, b), lds0, s + 2, F4 + F5 + (w - 2) * F67, lane * 16);
      STAMP(3);
    }
    BAR();
    {
      FRESH_LANE();
      STAMP(4);
      const unsigned tb1 = cyc32();
      if (__builtin_expect(s >= 0, 1)) {
        if (w == SCH_W) { sch.load(rs, s, lane); __builtin_amdgcn_sched_barrier(0); }
        vo_mem(s, sv, tb1 + (s > 0 ? (w == 0 ? BG_HOLD4 : (w == 1 ? BG_HOLD : BG_HOLD6)) : 0), lane, std::integral_constant<int, 0>{});
        STAMP(6);
        if (w == SCH_W) {
          __builtin_amdgcn_s_setprio(SCH_P);
          *(u32x2*)(misc + 24) = (u32x2){sch.run(a, rs, s, b, lane), (unsigned)(s + 1)};
        }
        __builtin_amdgcn_s_setprio(MMA_P);
        STAMP(11);
        ARRIVE(0);
      } else {
        BAR();
      }
    }
    ARRIVE(1);
    ARRIVE_OUT();
    sv = sv1;
  }
  STAMP_OUT();
  late(NSEG - 2, lane0);
  if (w >= 2) {
    lds_wait(cntp, 4u * (NSEG - 1));
    if (w == 2) { const u32x2 keep = ot.out; ot.run(lane0); ot.store(a, NSEG - 2 - LAG5, b, lane0); ot.out = keep; }
    __builtin_amdgcn_sched_barrier(0);
    PvTask pv;
    const unsigned ck2 = __builtin_amdgcn_readfirstlane(*(const unsigned*)(misc + 11));
    pv.load(rs, ck2, NSEG - 2 - LAG4, gp, w & 1, lane0);
    st_out(NSEG - 1, lane0);
    for (unsigned spins = 0; !pv.ready() && spins < SPIN_CAP; ++spins) { asm volatile("" ::: "memory"); pv.load(rs, ck2, NSEG - 2 - LAG4, gp, w & 1, lane0); }
    pv.run(lds, rs, NSEG - 2 - LAG4, gp, m, w & 1, lane0, true);
    x_fetch_n<F67>(x_src(a, NSEG + 1, b), lds0, NSEG + 1, F4 + F5 + (w - 2) * F67, lane0 * 16);
  }
  BAR();
  if (w != 0) vo_mem(NSEG - 1, (NSEG - 1) % NV, cyc32(), lane0, std::integral_constant<int, 1>{});
}

__device__ __forceinline__ void qkv_tail(const RecArgs& a, char* lds, const int w) {
  const int lane0 = __builtin_amdgcn_workitem_id_x() & 63, b = __builtin_amdgcn_workgroup_id_x();
  const int r = qkv_row(b), cb = (b >> 3) - 15 * (r & 1);
  if (cb >= 15) return;
  const unsigned lds0 = (unsigned)(uintptr_t)(LAS char*)lds;
  unsigned* const stage = (unsigned*)(lds + L_MISC) + 17;
  unsigned* const cnty = stage + 1;
  const f32x4 Z4 = {0.f, 0.f, 0.f, 0.f};
  auto fetch = [&](int i) {
    FRESH_LANE();
#pragma unroll
    for (int p = 0; p < 10; ++p) x_fetch(x_src(a, NSEG + i, b), lds0, i, 10 * w + p, lane);
  };
  unsigned want = 0;
  auto sync = [&]() { VM_DRAIN(); want += 4u; lds_inc<L_MISC + 72>(lds0); while (__hip_atomic_load(cnty, __ATOMIC_RELAXED, __HIP_MEMORY_SCOPE_WORKGROUP) < want) { } asm volatile("" ::: "memory"); };
  LOGQ(0);
  bf16x8 W[2][20];
#pragma unroll
  for (int k = 0; k < 40; ++k) {
    if (QBATCH) asm volatile("s_waitcnt vmcnt(%0)" :: "n"(QBATCH) : "memory");
    W[k & 1][k >> 1] = *(const bf16x8*)(a.ws + OFF_WQKVBT + ((size_t)(cb * 8 + 2 * w + (k & 1)) * 20 + (k >> 1)) * 1024 + lane0 * 16);
    __builtin_amdgcn_sched_barrier(0);
  }
  bf16x8 ones = mk8(0x3f803f80u, 0x3f803f80u, 0x3f803f80u, 0x3f803f80u);
  PIN_V(ones);
  const __amdgpu_buffer_rsrc_t ro = __builtin_amdgcn_make_buffer_rsrc(a.ws + OFF_QB, 0, (int)(OFF_OB - OFF_QB), 0x00020000);
  const int t0 = w & 1, col0 = (cb % 5) * 128 + w * 32;
  auto body = [&](auto SW, auto SQ) {
    unsigned vo[2][2];
    {
      FRESH_LANE();
#pragma unroll
      for (int i = 0; i < 2; ++i)
#pragma unroll
        for (int tt = 0; tt < 2; ++tt) {
          const int tq = tt ^ t0, tk = tq * 16 + c, cc = col0 + i * 16 + (SW ? 4 * g : c), hd = cc / BD, e = cc - hd * BD;
          const int kv = (int)(OFF_KV - OFF_QB) + hd * 64 * KVT;
          vo[i][tt] = (unsigned)(!SW ? kv + 1280 + tq * 672 + ((g & 1) * 21 + e) * 16 + (g >> 1) * 8
                                     : (cb < 5 ? (hd * S_ + tk) * 2 * QST + e * 2 : kv + (e < 16 ? (tk + 32 * (e >> 3)) * 16 + (e & 7) * 2 : 1024 + tk * 8)));
          PIN_V(vo[i][tt]);
        }
    }
    const float qs = cb < 5 ? 0.22360679774997896f * 1.4426950408889634f : 1.0f;
    f32x4 cs[2] = {Z4, Z4};
    LOGQ(1);
#pragma unroll 1
    for (int it = 0; it < 4; ++it) {
      const char* xp0 = lds + L_XN + (it & 1) * 40960 + t0 * 20480 + lane0 * 16;
      const char* xp1 = lds + L_XN + (it & 1) * 40960 + (1 - t0) * 20480 + lane0 * 16;
      f32x4 acc[2][2] = {{Z4, Z4}, {Z4, Z4}}, st = Z4;
      bf16x8 x[4][2];
#pragma unroll
      for (int p = 0; p < 3; ++p) { x[p][0] = *(const bf16x8*)(xp0 + p * 1024); x[p][1] = *(const bf16x8*)(xp1 + p * 1024); }
#pragma unroll
      for (int ks = 0; ks < 20; ++ks) {
        if (ks + 3 < 20) { x[(ks + 3) & 3][0] = *(const bf16x8*)(xp0 + (ks + 3) * 1024); x[(ks + 3) & 3][1] = *(const bf16x8*)(xp1 + (ks + 3) * 1024); }
        st = mfma32(SQ ? x[ks & 3][0] : ones, x[ks & 3][0], st);
#pragma unroll
        for (int tt = 0; tt < 2; ++tt)
#pragma unroll
          for (int i = 0; i < 2; ++i) acc[i][tt] = SW ? mfma32(W[i][ks], x[ks & 3][tt], acc[i][tt]) : mfma32(x[ks & 3][tt], W[i][ks], acc[i][tt]);
        __builtin_amdgcn_sched_barrier(0);
      }
      LOGQ(2 + 3 * it);
      FRESH_LANE();
      float* const sp = (float*)(lds + L_ST) + (it & 1) * 64;
      if (SQ) { if ((c >> 2) == g) sp[32 + t0 * 16 + c] = (c & 3) == 0 ? st[0] : ((c & 3) == 1 ? st[1] : ((c & 3) == 2 ? st[2] : st[3])); }
      else if (g == 0) sp[t0 * 16 + c] = st[0];
      if (it == 0) {
#pragma unroll
        for (int ks = 0; ks < 20; ++ks)
#pragma unroll
          for (int i = 0; i < 2; ++i) cs[i] = SW ? mfma32(W[i][ks], ones, cs[i]) : mfma32(ones, W[i][ks], cs[i]);
      }
      sync();
      LOGQ(3 + 3 * it);
      if (it == 0) fetch(2);
      const bool late = r + 48 >= 55;
      if (it == 1 && !late) fetch(3);
      const bool now = it == 2 && late && __builtin_amdgcn_readfirstlane(*(volatile unsigned*)stage) != 0u;
      if (now) fetch(3);
      const unsigned so = (unsigned)((r + 16 * it) * (cb < 5 ? 64 * QST : KVT));
      auto stat = [&](float sm, float sq, float& mean, float& rstd) {
        mean = sm * (1.0f / DM);
        rstd = __builtin_amdgcn_rsqf(__builtin_fmaxf(sq * (1.0f / DM) - mean * mean, 0.f) + 1e-5f);
      };
#pragma unroll
      for (int tt = 0; tt < 2; ++tt) {
        const int tq = tt ^ t0;
        float mean[4], rstd[4];
        if (SW) {
          stat(sp[tq * 16 + c], sp[32 + tq * 16 + c], mean[0], rstd[0]);
          rstd[0] *= qs;
        } else {
          const f32x4 sm = *(const f32x4*)(sp + tq * 16 + 4 * g), sq = *(const f32x4*)(sp + 32 + tq * 16 + 4 * g);
#pragma unroll
          for (int k = 0; k < 4; ++k) stat(sm[k], sq[k], mean[k], rstd[k]);
        }
#pragma unroll
        for (int i = 0; i < 2; ++i) {
          float v[4];
#pragma unroll
          for (int k = 0; k < 4; ++k) v[k] = (acc[i][tt][k] - mean[SW ? 0 : k] * cs[i][SW ? k : 0]) * rstd[SW ? 0 : k];
          __builtin_amdgcn_raw_buffer_store_b64((u32x2){pack2(v[0], v[1]), pack2(v[2], v[3])}, ro, vo[i][tt], so, 0);
        }
      }
      LOGQ(4 + 3 * it);
      if (it == 2 && late) {
        if (!now) { lds_wait(stage, 1u); fetch(3); }
        sync();
      }
    }
  };
  typedef std::integral_constant<bool, false> F_; typedef std::integral_constant<bool, true> T_;
  if (cb < 10) { if (w >> 1) body(T_{}, T_{}); else body(T_{}, F_{}); }
  else { if (w >> 1) body(F_{}, T_{}); else body(F_{}, F_{}); }
}

struct TokTask {
  u32x4 y[2]; u32x2 ym; u32x4 res;
  unsigned l0, l1, lm, s0, s1;
  static __device__ __forceinline__ bool ok(int sl) { return sl >= 0 && sl < NSEG; }
  __device__ __forceinline__ void init(int h, int j, int lane) {
    const int c = lane & 15, g = lane >> 4, qc = 4 * (j & 3) + (c >> 2);
    l0 = ts_tok(h, g) + (unsigned)((j >> 2) * 1024 + ((c & 3) * 16 + qc) * 16); l1 = l0 + 16384u;
    lm = ts_mem(h, g + 4 * (c & 1)) + (unsigned)((j >> 2) * 256 + (((c >> 1) & 1) * 16 + qc) * 8);
    res = (u32x4){0u, 0u, 0u, 0u};
    const int qi = 4 * j + (c >> 2);
    s0 = g < 2 ? pt_tok(h) + (unsigned)((qi >> 4) * 1024 + ((c & 3) * 16 + (qi & 15)) * 16 + g * 8) : 0x7ffffff0u;
    s1 = g == 0 && (c & 1) == 0 ? pt_mem(h) + (unsigned)((qi >> 4) * 256 + (((c >> 1) & 1) * 16 + (qi & 15)) * 8) : 0x7ffffff0u;
    PIN_V(l0); PIN_V(l1); PIN_V(lm); PIN_V(s0); PIN_V(s1);
  }
  __device__ __forceinline__ void load(const __amdgpu_buffer_rsrc_t rs, unsigned o) {
    y[0] = SC1_LOADS(l0, o); y[1] = SC1_LOADS(l1, o);
    ym = __builtin_amdgcn_raw_buffer_load_b64(rs, lm, o, 16);
  }
  __device__ __forceinline__ bool ready() { return wave_all(umin(umin(min4(y[0]), min4(y[1])), umin(ym[0], ym[1])) != 0u); }
  __device__ __forceinline__ void run(int j, int lane, const bf16x8 SelLo) {
    const int c = lane & 15, g = lane >> 4;
    const f32x4 Z4 = {0.f, 0.f, 0.f, 0.f};
    const f32x4 d1 = mfma32(SelLo, as8(y[1]), mfma32(SelLo, as8(y[0]), Z4));
    const f32x4 d0 = mfma32(SelLo, mk8(ym[0], ym[1], 0u, 0u), Z4);
    const int qi = 4 * j + (c >> 2), k0 = 16 * g + 4 * (c & 3);
    float t[4], mv[4];
    float mx = -INFINITY;
#pragma unroll
    for (int r = 0; r < 4; ++r) {
      t[r] = (g < 2 && k0 + r <= qi) ? d1[r] : -INFINITY;
      const float mt = d0[r] + dppf<DPP_XOR1>(d0[r]);
      mv[r] = (g == 0 && (c & 1) == 0) ? mt : -INFINITY;
      mx = fmx(mx, fmx(t[r], mv[r]));
    }
    mx = fmx(mx, dppf<DPP_XOR1>(mx)); mx = fmx(mx, dppf<DPP_XOR2>(mx));
    mx = x16mx(mx);
    float sum = 0.f;
#pragma unroll
    for (int r = 0; r < 4; ++r) {
      t[r] = __builtin_amdgcn_exp2f(t[r] - mx); mv[r] = __builtin_amdgcn_exp2f(mv[r] - mx);
      sum += t[r] + mv[r];
    }
    sum += dppf<DPP_XOR1>(sum); sum += dppf<DPP_XOR2>(sum);
    sum = x16sum(sum);
    const float rs_ = __builtin_amdgcn_rcpf(sum);
    res = (u32x4){pack2n(t[0] * rs_, t[1] * rs_), pack2n(t[2] * rs_, t[3] * rs_), pack2n(mv[0] * rs_, mv[1] * rs_), pack2n(mv[2] * rs_, mv[3] * rs_)};
    PIN_V(res);
  }
  __device__ __forceinline__ void store(const __amdgpu_buffer_rsrc_t rs, unsigned o, unsigned no = 0u) {
    SC1_STORE8S(((u32x2){res[0], res[1]}), s0 | no, o);
    SC1_STORE8S(((u32x2){res[2], res[3]}), s1 | no, o);
  }
};

__device__ __forceinline__ void t2_mem(const bf16x8 fr, const bf16x8 q0, const bf16x8 q1, const __amdgpu_buffer_rsrc_t rs, unsigned o, int h, int j, float inv, int lane) {
  const f32x4 Z4 = {0.f, 0.f, 0.f, 0.f};
  const f32x4 d0 = mfma32(fr, q0, Z4) * inv, d1 = mfma32(fr, q1, Z4) * inv;
  const unsigned a0 = pack2n(d0[0], d0[1]), a1 = pack2n(d0[2], d0[3]), b0 = pack2n(d1[0], d1[1]), b1 = pack2n(d1[2], d1[3]);
  const auto r0 = __builtin_amdgcn_permlane32_swap(a0, b0, false, false), r1 = __builtin_amdgcn_permlane32_swap(a1, b1, false, false);
  SC1_STORE8S(((u32x2){r0[1], r1[1]}), ts_mem(h, j) + lane * 8, o);
}

template <int w>
__device__ __forceinline__ void rec_waves(const RecArgs& a, char* lds, const __amdgpu_buffer_rsrc_t rs) {
  const int lane = __builtin_amdgcn_workitem_id_x() & 63, b = __builtin_amdgcn_workgroup_id_x(), c = lane & 15, g = lane >> 4, wid = w; (void)wid;
  const int gp = (b & 7) * 2 + (b >> 7), m = (b >> 3) & 15, h0 = 2 * gp, h = h0 + (m >> 3), j = m & 7;
  float* const misc = (float*)(lds + L_MISC);
  const f32x4 Z4 = {0.f, 0.f, 0.f, 0.f};
  LOGP(0);
  __builtin_amdgcn_s_setprio(3);
  const unsigned lds0 = (unsigned)(uintptr_t)(LAS char*)lds;
  if (!a.layer) img_fetch<12>(a, lds0, h, j, w, lane);
  constexpr int NC = w < 2 ? 2 : 3;
  const int far = ((b >> 2) & 1) ^ 1;
  auto chunk = [&](int f) { return f == 0 ? 4 * far + w : (w == 2 && f == 1 ? 8 + far : (w == 3 && f == 2 ? 9 - far : 4 - 4 * far + w)); };
  bf16x16 Wqk[3][NC];
  u32x4 bm0[NC][2];
#pragma unroll
  for (int f = 0; f < 2 * NC; ++f) bm0[f >> 1][f & 1] = *(const u32x4*)(a.mem0 + (size_t)(c & 7) * DM + 64 * chunk(f >> 1) + 16 * g + 8 * (f & 1));
  if (!a.layer) asm volatile("s_waitcnt vmcnt(0)" ::: "memory");
  BAR();
  LOGP(9);
  const unsigned ck = __builtin_amdgcn_readfirstlane(*(const unsigned*)(misc + 11));
  const unsigned flip = (ck ^ (unsigned)(b << 11)) & 8192u;
#pragma unroll
  for (int t = 0; t < 3; ++t)
#pragma unroll
    for (int f = 0; f < NC; ++f) Wqk[t][f] = cat16(wqk_frag(lds, t, 2 * chunk(f), lane), wqk_frag(lds, t, 2 * chunk(f) + 1, lane));
  if (!a.layer) lds_arrive((unsigned*)(misc + 6), lane);
  else {
    if (w == 0) {
      for (unsigned spins = 0; spins < SPIN_CAP; ++spins) {
        if (__builtin_amdgcn_raw_buffer_load_b32(rs, (b & 7) * 256, (unsigned)OFF_RDY, 16) >= 16u) break;
        __builtin_amdgcn_s_sleep(4);
        asm volatile("" ::: "memory");
      }
      lds_inc<L_MISC + 84>(lds0);
    }
    lds_wait((unsigned*)(misc + 21), 2u);
#pragma unroll
    for (int f = 0; f < 2 * NC; ++f) PIN_V(bm0[f >> 1][f & 1]);
#pragma unroll
    for (int i = 0; i < 10; ++i) x_fetch(x_src(a, 0, b), lds0, 0, 10 * w + i, lane);
  }
  const bf16x8 SelLo = sel_frag(c, 0), SelHi = sel_frag(c, 8);
  bf16x8 ONES = mk8(0x3f803f80u, 0x3f803f80u, 0x3f803f80u, 0x3f803f80u);
  if (w < 2) PIN_V(ONES);
  bool dead = false;
  float inv = 1.0f;
  unsigned* const cnt2 = (unsigned*)(misc + 7);
  unsigned l2off[2 * NC];
#pragma unroll
  for (int f = 0; f < 2 * NC; ++f) { l2off[f] = acc_off(16 * chunk(f >> 1) + 4 * g + 2 * (f & 1) + (c >> 3), c & 7) ^ flip; PIN_V(l2off[f]); }
  int sidx = (c >> 3) ? 0xEEEE : 0x4444;
  PIN_V(sidx);
  const unsigned p1off = (unsigned)((w == 0 ? (h & 2) * 1024 + j * 256 + (h & 1) * 64 + (g & 1) * 8 : (h & 3) * 32768 + j * 512 + (g >> 1) * 256 + (g & 1) * 64) + (c & 4) * 32 + (c & 3) * 16);
  unsigned p1v = (c < 8) && (w != 0 || g >= 2) ? p1off : 0x7ffffff0u, p1cur = 0x7ffffff0u;
  PIN_V(p1v); PIN_V(p1cur);
  const int hd = (c >> 2) & 1, mr = hd ? 3 - (c & 3) : (c & 3);
  const unsigned l1off = (unsigned)(((h0 + hd) & 3) * 32768 + g * 512 + (w & 1) * 128 + (c >> 3) * 64 + mr * 16);
  const unsigned l1offm = (unsigned)((h0 & 2) * 1024 + (g + 4 * (c >> 3)) * 256 + (w & 1) * 128 + hd * 64 + mr * 16);
  const int plane = (lane & ~6) | ((lane & 2) << 1) | ((lane & 4) >> 1);
  unsigned acco = c < 8 ? (acc_off(64 * far + m * 4 + g, 4 * (w & 1) + mr) + (unsigned)(hd * 8)) ^ flip : 0x7ffffff0u;
  unsigned acco2 = c >= 8 ? (acc_off(64 - 64 * far + m * 4 + g, 4 * (w & 1) + mr) + (unsigned)(hd * 8)) ^ flip
                          : (c < 4 ? (acc_off(128 + m * 2 + (g & 1), 4 * (w & 1) + mr) + (unsigned)((g >> 1) * 8)) ^ flip : 0x7ffffff0u);
  if (w < 2) { PIN_V(acco); PIN_V(acco2); }
  unsigned bmo[NC];
#pragma unroll
  for (int f = 0; f < NC; ++f) { bmo[f] = (unsigned)(chunk(f) * 1024 + lane * 16); PIN_V(bmo[f]); }
  bf16x8 bm[NC];
  auto bm_put = [&](int f) { *(bf16x8*)(lds + L_BM + bmo[f]) = bm[f]; __builtin_amdgcn_sched_barrier(0); };
  f32x4 acc[3] = {Z4, Z4, Z4};
  const int rt2 = ((c >> 3) * 8 + 4 * (g & 1)) * 28 + 16 + (c & 7);
  unsigned redo[3] = {(unsigned)(4 * g * 28 + c), (unsigned)(g < 2 ? (4 * g + 2) * 28 + c : rt2), (unsigned)(g < 2 ? (4 * g + 10) * 28 + c : rt2 + 56)};
#pragma unroll
  for (int k = 0; k < 3; ++k) { redo[k] = L_RED1 + w * 1792 + redo[k] * 4; PIN_V(redo[k]); }
  auto red_put = [&]() {
    auto hs = [&](float x, float y) { const auto sw = __builtin_amdgcn_permlane32_swap(f2u(x), f2u(y), false, false); return u2f(sw[0]) + u2f(sw[1]); };
#pragma unroll
    for (int k = 0; k < 3; ++k) {
#pragma unroll
      for (int i = 0; i < 2; ++i) *(float*)(lds + redo[k] + i * 112) = k == 0 ? hs(acc[0][i], acc[1][i]) : hs(acc[k - 1][2 + i], acc[2][2 * k - 2 + i]);
      __builtin_amdgcn_sched_barrier(0);
    }
  };
#pragma unroll
  for (int f = 0; f < NC; ++f) {
    const u32x4 u = bm0[f][0], v = bm0[f][1];
    bm[f] = c < 8 ? mk8(u[0], u[2], v[0], v[2]) : mk8(u[1], u[3], v[1], v[3]);
    bm_put(f);
#pragma unroll
    for (int t = 0; t < 3; ++t) acc[t] = smfma64(bm[f], Wqk[t][f], acc[t], sidx);
  }
  red_put();
  VM_DRAIN();
  if (a.layer) lds_arrive((unsigned*)(misc + 6), lane);
  lds_wait((unsigned*)(misc + 6), 8u);
#pragma unroll
  for (int i = 0; i < 10; ++i) x_fetch(x_src(a, 1, b), lds0, 1, 10 * w + i, lane);
  LOGP(1);
  BAR();
  STAMP_DECL(); ARRIVE_DECL();
  bf16x8 fr, qx[2];
  auto kq_get = [&](int seg) {
    const char* p = lds + (w == 1 ? L_KX : L_QX) + (seg % NKQ) * 2048 + lane * 16;
    if (w == 1 || w == 2 || (w == 3 && seg == NSEG - 1)) { qx[0] = *(const bf16x8*)p; qx[1] = *(const bf16x8*)(p + 1024); }
  };
  auto pub1 = [&](unsigned off, unsigned o1) {
    fr = qk_frag(lds, g, c);
    const f32x4 sc = mfma32(w == 0 ? fr : qx[0], fr, Z4);
    if (w == 0) SC1_STORE8S(((u32x2){pack2n(sc[0], sc[1]), pack2n(sc[2], sc[3])}), off, o1);
    else {
      const f32x4 s2 = mfma32(qx[1], fr, Z4);
      __builtin_amdgcn_raw_buffer_store_b128((u32x4){pack2n(sc[0], sc[1]), pack2n(sc[2], sc[3]), pack2n(s2[0], s2[1]), pack2n(s2[2], s2[3])}, rs, off, o1, 16);
    }
  };
  VM_DRAIN();
  kq_get(0);
  unsigned o1 = sct_slot(ck, 0u), o1m = scm_slot(ck, 0u);
  unsigned t2o = ts_tok(h, j) + (unsigned)(lane * 16);
  if (w == 3) { PIN_V(t2o); t2_tok(lds, rs, t2o, ts_slot(ck, 0), 0, lane, []() {}); }
  if (w < 2) pub1(p1v, w == 0 ? o1m : o1);
  const unsigned tst = now32() | 1u;
  if (w == 3 && ST_X) __builtin_amdgcn_raw_buffer_store_b32(tst, rs, lane == 0 ? (b >> 2) * 256 + (b & 3) * 4 : 0x7ffffff0, (unsigned)(OFF_ACC + (SR - 1) * ACC_STEP) + ACC_TS, 16);
  LOGP(2);
  BAR();
  LOGP(3);
#pragma unroll
  for (int i = 0; i < 10; ++i) x_fetch(x_src(a, 2, b), lds0, 2, 10 * w + i, lane);
  VM_DRAIN();
  unsigned o2 = (unsigned)OFF_ACC, o2p = 0u, tw = 0u;
  asm volatile("" : "+s"(o2p));
  unsigned tc0 = 0u, tstar = 0u;
  int dmin = 0, dmax = 0;
  int e1 = 0;
  unsigned offs = 100u;
  TokTask tk;
  OutTask ot;
  PvTask pv;
  PvTask2 pv2;
  if (w == 3) pv2.init(gp, lane);
  if (w == 2) tk.init(h, j, lane);
  unsigned o4 = 0u;
  unsigned o3, o3p;
  u32x4 pmz = {0u, 0u, 0u, 0u}, vom[5];
  PIN_V(pmz);
#pragma unroll
  for (int k = 0; k < 5; ++k) { vom[k] = pmz; if (w < 2) PIN_V(vom[k]); }
  unsigned tso = lane == 0 ? ACC_TS + (unsigned)((b >> 2) * 256 + (b & 3) * 4) : 0x7ffffff0u;
  if (w < 2) PIN_V(tso);
  float minf = g >= 2 ? -INFINITY : 0.f;
  if (w < 2) PIN_V(minf);
  int sv = NV - 1;
  bf16x8 vox[5];
  unsigned vma[3], need2;
  u32x4 slo, shi;
  float k1, va0, va1, vb0, vb1;
  const u16* xsrc;
  auto prep = [&](int s) {
    o1 = sct_slot(ck, s % SR); o1m = scm_slot(ck, s % SR); o2 = (unsigned)OFF_ACC + (unsigned)(s % SR) * (unsigned)ACC_STEP;
    xsrc = x_src(a, s + 3, b);
    PIN_S(o2);
    if (w < 2) { PIN_S(o1); PIN_S(o1m); if (F0C) PIN_S(xsrc); }
    if (w == 2) { o3p = ts_slot(ck, (s - LAG3) & 3); o3 = ts_slot(ck, s & 3); PIN_S(o3p); }
    if (w == 3) { o3 = pt_slot(ck, (s - 1 - LAG4) & 3); PIN_S(o3); }
    sv = sv == NV - 1 ? 0 : sv + 1;
    if (w < 2) PIN_S(sv);
    inv = u2f(__builtin_amdgcn_readfirstlane(f2u(misc[4 + (s & 1)])));
    if (w != 3) kq_get(s);
    need2 = 5u * (unsigned)(s + 1) + 2u * ((unsigned)(s + 2) / NSEG);
    if (w < 2) PIN_S(need2);
  };
  auto prep2 = [&]() {
#pragma unroll
    for (int k = 0; k < 3; ++k) {
      const int t = k == 0 ? 2 : (k == 1 ? far : 1 - far);
      const char* p = lds + L_VOX + sv * 5120 + t * 1024 + (plane & 47) * 16 + (g & 1) * 8;
      auto rd = [&](const char* q) { const u32x2 a0 = *(const u32x2*)q, a1 = *(const u32x2*)(q + 256); return mk8(a0[0], a0[1], a1[0], a1[1]); };
      vox[k == 0 ? 0 : 2 * k - 1] = rd(p);
      if (k) vox[2 * k] = rd(p + 3072);
      vma[k] = (unsigned)(L_VOM + sv * 1280 + t * 256 + (plane & 31) * 8);
      PIN_V(vma[k]);
    }
    slo = __builtin_bit_cast(u32x4, SelLo); shi = __builtin_bit_cast(u32x4, SelHi);
#pragma unroll
    for (int i = 0; i < 4; ++i) { slo[i] = ((slo[i] >> 7) & 0x10001u) * (f2u(inv) >> 16); shi[i] = ((shi[i] >> 7) & 0x10001u) * (f2u(inv) >> 16); }
    k1 = KEXP * inv;
    va0 = 32.0f; va1 = 0.f; vb0 = 32.0f; vb1 = 0.f;
    PIN_V(k1); PIN_V(slo); PIN_V(shi); PIN_V(va0); PIN_V(va1); PIN_V(vb0); PIN_V(vb1);
    __builtin_amdgcn_sched_barrier(0);
  };
  auto rows = [&](int s) {
    const int dl = spin_until(tw);
    if (w < 2) tc0 = cyc32();
    __builtin_amdgcn_sched_barrier(0);
    STAMP(1);
    unsigned spins = 0;
    auto pass = [&](bool first) {
      u32x4 x[2 * NC];
#pragma unroll
      for (int f = 0; f < NC; ++f) {
        if (f) asm volatile(".rept %0\n\ts_nop 3\n\t.endr" :: "n"(w < 2 ? P2_G0 : (f == 1 ? P2_G2 : P2_G3)));
        x[2 * f] = SC1_LOADS(l2off[2 * f], o2p); x[2 * f + 1] = SC1_LOADS(l2off[2 * f + 1], o2p);
        __builtin_amdgcn_sched_barrier(0);
      }
      if (first) lds_inc<L_MISC + 92>(lds0);
      if (w == 2 && first && P2_W) lds_spin<L_MISC + 92, 0>(lds0, 4u * (unsigned)s);
      if (w == 2 && first) tk.store(rs, o4, s > LAG3 ? 0u : 0x7ff00000u);
      if (w == 3 && first) t2_tok(lds, rs, t2o, o4, s, lane, [&]() { lds_spin<L_MISC + 92, 0>(lds0, 4u * (unsigned)s); });
      if (first) prep(s);
      if (first && w < 2) {
        p1cur = p1v; offs = (offs - (unsigned)dl) & ~3u;
        PIN_V(p1cur); PIN_S(offs);
        tstar = tc0 + (unsigned)(e1 + D1_M + (w == 1 ? D1_W1 : 0)) - offs * 20u;
        dmin = offs < 40u ? D1_MIN : REC_D1;
        dmin = s < EARLY_N ? REC_D1 + EARLY_D1 : dmin;
        dmax = REC_D1;
        PIN_S(tstar); PIN_S(dmin); PIN_S(dmax);
      }
      __builtin_amdgcn_sched_barrier(0);
      unsigned ok = 32u;
#pragma unroll
      for (int t = 0; t < 3; ++t) { acc[t] = Z4; PIN_V(acc[t]); }
      __builtin_amdgcn_sched_barrier(0);
      bool r = true;
#pragma unroll
      for (int f = 0; f < NC; ++f) {
        unsigned p[4];
#pragma unroll
        for (int k = 0; k < 2; ++k) {
          const u32x4 v = x[2 * f + k];
          p[2 * k] = pack2((float)(int)v[0], (float)(int)v[1]); p[2 * k + 1] = pack2((float)(int)v[2], (float)(int)v[3]);
          asm volatile("" : "+v"(p[2 * k]), "+v"(p[2 * k + 1]));
          __builtin_amdgcn_sched_barrier(0);
        }
        ok = (ok & x[2 * f][0] & x[2 * f][2]) & (x[2 * f + 1][0] & x[2 * f + 1][2]);
        PIN_V(ok);
        __builtin_amdgcn_sched_barrier(0);
        bm[f] = mk8(p[0], p[1], p[2], p[3]);
        acc[0] = smfma64(bm[f], Wqk[0][f], acc[0], sidx);
        __builtin_amdgcn_sched_barrier(0);
        bm_put(f);
        acc[1] = smfma64(bm[f], Wqk[1][f], acc[1], sidx);
        __builtin_amdgcn_sched_barrier(0);
        if (f == NC - 1) { u64 bad = __builtin_amdgcn_ballot_w64(ok == 0u); PIN_S(bad); r = bad == 0ull; }
        __builtin_amdgcn_sched_barrier(0);
        acc[2] = smfma64(bm[f], Wqk[2][f], acc[2], sidx);
        __builtin_amdgcn_sched_barrier(0);
      }
      red_put();
      return r;
    };
    if (__builtin_expect(!pass(true), 0)) {
      FC(0);
      do {
        if (dead || ++spins > SPIN_CAP) { dead = true; break; }
        asm volatile("" ::: "memory");
      } while (!pass(false));
    }
    STAMPV(12, spins); LOGF(1, spins);
    TSTAMP(2, now32()); TSTAMP(3, spins);
  };
  prep(0);
  CODE_PAD((PADR >> (4 * w)) & 15);
#pragma unroll 1
  for (int s = 0; s < NSEG - 1; ++s) {
    if (__builtin_expect(o2p != 0u, 1)) rows(s);
    STAMP(2);
    BAR();
    STAMP(3);
    if (w < 2) pub1(p1cur, w == 0 ? o1m : o1);
    if (w == 2) tk.load(rs, o3p);
    if (w == 3) pv2.load(rs, o3);
    const unsigned tp1 = cyc32();
    __builtin_amdgcn_sched_barrier(0);
    if (w == 2) {
      __builtin_amdgcn_s_setprio(W2_P);
      spin_cyc(tp1 + W2_D);
      fr = qk_frag(lds, g, c);
      t2_mem(fr, qx[0], qx[1], rs, o3, h, j, inv * KEXP, lane);
      __builtin_amdgcn_sched_barrier(0);
      asm volatile("s_waitcnt vmcnt(1)" ::: "memory");
      lds_inc<L_MISC + 28>(lds0);
    }
    if (w >= 2 && __builtin_expect(s == NSEG - 2, 0)) { VM_DRAIN(); lds_inc<L_MISC + 28>(lds0); }
    TSTAMP(0, now32());
    STAMP(4);
    __builtin_amdgcn_sched_barrier(0);
    unsigned tpub = 0;
    u32x2 sw;
    if (w == 2) {
      STAMP(5);
      __builtin_amdgcn_sched_barrier(0);
      STAMP(6);
      ARRIVE(0);
      tk.run(j, lane, SelLo);
      if (F2B) { lds_spin<L_MISC + 48, 1>(lds0, 2u * (unsigned)(s + 1)); x_fetch_n<F2B>(xsrc, lds0, s + 3, P2B, lane * 16); }
      o4 = pt_slot(ck, imax(s - LAG3, 0));
      PIN_S(o4);
      __builtin_amdgcn_s_setprio(SP_P);
    }
    if (w == 3) {
      if (F3A) { spin_cyc(tp1 + F3A_D); x_fetch_n<F3A>(xsrc, lds0, s + 3, 40 - F3C - F3 - F3A, lane * 16); }
      __builtin_amdgcn_s_setprio(T4_P);
      if (!F3A || T4_D > F3A_D) spin_cyc(tp1 + T4_D);
      pv2.run(lds, sv < 1 + LAG4 ? sv + NV - 1 - LAG4 : sv - 1 - LAG4, lane);
      STAMP(11);
      o4 = ts_slot(ck, s + 1);
      PIN_S(o4);
      __builtin_amdgcn_s_setprio(SP_P);
      STAMP(5);
      ARRIVE(0);
      if (F3) { lds_spin<L_MISC + 48, 1>(lds0, 2u * (unsigned)(s + 1)); x_fetch_n<F3>(xsrc, lds0, s + 3, 40 - F3C - F3, lane * 16); }
      STAMP(6);
    }
    if (w < 2) {
      prep2();
      int dq = imin((int)(tstar - tp1), dmax);
      dq = dq < -D1_LATE ? dmax : dq;
      PIN_S(dq);
      spin_cyc(tp1 + (unsigned)imax(dq, dmin));
      asm volatile("" : "+v"(vox[0]), "+v"(vox[1]), "+v"(vox[2]), "+v"(vox[3]), "+v"(vox[4]));
      STAMP(5);
      u32x4 y[5];
      y[4] = SC1_LOADS(l1offm, o1m);
#pragma unroll
      for (int k = 0; k < 4; ++k) { __builtin_amdgcn_sched_barrier(0); y[k] = SC1_LOADS(l1off + (k & 1) * 2048 + (k >> 1) * 256, o1); }
      __builtin_amdgcn_sched_barrier(0);
      lds_inc<L_MISC + 48>(lds0);
      {
        int e0 = (int)(tp1 - tc0), en = imax(e0 - e1, -D1_C);
        PIN_S(en);
        en = e1 + (imin(en, D1_C) >> D1_G);
        PIN_S(en); PIN_S(e0);
        en = offs < 40u ? en : e1;
        e1 = s < EARLY_N ? e0 : en;
        PIN_S(e1);
      }
      STAMP(6);
      ARRIVE(0);
      f32x2 st;
      auto vom_get = [&](int k) {
        const u32x2 v = *(const u32x2*)(lds + vma[(k + 1) >> 1] + (k && !(k & 1) ? 768 : 0));
        vom[k][0] = v[0]; vom[k][1] = v[1];
        __builtin_amdgcn_sched_barrier(0);
      };
      lds_spin<L_MISC + 28, 0>(lds0, need2);
#pragma unroll
      for (int k = 0; k < 5; ++k) vom_get(k);
      st = *(const f32x2*)(misc + 8);
      STAMP(11);
      __builtin_amdgcn_sched_barrier(0);
      {
        unsigned spins = 0;
        const bool lo = c < 8;
        f32x4 d1, o[5], sm;
        float m0, m1, chk;
        unsigned mn;
        auto pv_mem = [&](float a0, float a1) {
          const unsigned mm = pack2(__builtin_amdgcn_exp2f(a0), __builtin_amdgcn_exp2f(a1));
          pmz[0] = mm; pmz[1] = dpp_ror8(mm);
          PIN_V(pmz);
          return __builtin_bit_cast(bf16x8, pmz);
        };
        auto sum8 = [&]() {
          const f32x4 d0 = mfma32(as8(slo), as8(y[4]), Z4);
          mn = umin(umin(y[4][0], y[4][1]), y[4][2]);
          PIN_V(mn);
          __builtin_amdgcn_sched_barrier(0);
          d1 = mfma32(as8(slo), as8(y[0]), Z4);
          mn = umin(umin(umin(umin(mn, y[4][3]), y[0][0]), y[0][1]), y[0][2]);
          PIN_V(mn);
          __builtin_amdgcn_sched_barrier(0);
          d1 = mfma32(as8(slo), as8(y[1]), d1);
          __builtin_amdgcn_sched_barrier(0);
          m0 = (lo ? d0[0] : d0[2]) + dppf<DPP_ROR8>(lo ? d0[2] : d0[0]);
          m1 = (lo ? d0[1] : d0[3]) + dppf<DPP_ROR8>(lo ? d0[3] : d0[1]);
          m0 = __builtin_fmaf(m0, k1, minf); m1 = __builtin_fmaf(m1, k1, minf);
          PIN_V(m0); PIN_V(m1);
          __builtin_amdgcn_sched_barrier(0);
          d1 = mfma32(as8(shi), as8(y[2]), d1);
          __builtin_amdgcn_sched_barrier(0);
          float e0 = __builtin_amdgcn_exp2f(m0), e1 = __builtin_amdgcn_exp2f(m1);
          PIN_V(e0); PIN_V(e1);
          __builtin_amdgcn_sched_barrier(0);
          d1 = mfma32(as8(shi), as8(y[3]), d1);
          __builtin_amdgcn_sched_barrier(0);
          pmz[0] = pack2(e0, e1);
          PIN_V(pmz);
          mn = umin(umin(umin(umin(mn, y[0][3]), y[1][0]), y[1][1]), y[1][2]);
          PIN_V(mn);
          __builtin_amdgcn_sched_barrier(0);
          pmz[1] = dpp_ror8(pmz[0]);
          PIN_V(pmz);
          __builtin_amdgcn_sched_barrier(0);
          mn = umin(umin(umin(umin(mn, y[1][3]), y[2][0]), y[2][1]), y[2][2]);
          PIN_V(mn);
          asm volatile("" : "+v"(vom[0]), "+v"(vom[1]), "+v"(vom[2]), "+v"(vom[3]), "+v"(vom[4]), "+v"(st));
          __builtin_amdgcn_sched_barrier(0);
          const bf16x8 pm = __builtin_bit_cast(bf16x8, pmz);
          sm = mfma32(ONES, pm, Z4);
          __builtin_amdgcn_sched_barrier(0);
          return pm;
        };
        auto p_tok = [&](const f32x4 a, const bf16x8 pm) {
          float e[4];
#pragma unroll
          for (int i = 0; i < 4; ++i) {
            e[i] = __builtin_amdgcn_exp2f(a[i]);
            __builtin_amdgcn_sched_barrier(0);
            if (i == 1 || i == 3) o[1 + (i >> 1)] = mfma32(as8(vom[1 + (i >> 1)]), pm, Z4);
            __builtin_amdgcn_sched_barrier(0);
          }
          const unsigned t01 = pack2(e[0], e[1]), t23 = pack2(e[2], e[3]);
          __builtin_amdgcn_sched_barrier(0);
          o[3] = mfma32(as8(vom[3]), pm, Z4);
          __builtin_amdgcn_sched_barrier(0);
          const bf16x8 r = mk8(t01, t23, dpp_ror8(t01), dpp_ror8(t23));
          __builtin_amdgcn_sched_barrier(0);
          o[4] = mfma32(as8(vom[4]), pm, Z4);
          o[0] = mfma32(as8(vom[0]), pm, Z4);
          __builtin_amdgcn_sched_barrier(0);
          return r;
        };
        auto chk_a = [&]() { chk = fmx(fmx(__builtin_fabsf(d1[0]), __builtin_fabsf(d1[1])), __builtin_fabsf(d1[2])); };
        auto chk_b = [&]() { chk = fmx(fmx(chk, __builtin_fabsf(d1[3])), g < 2 ? fmx(__builtin_fabsf(m0), __builtin_fabsf(m1)) : 0.f); };
        bf16x8 pm = sum8();
        bf16x8 pt = p_tok(d1, pm);
        sm = mfma32(ONES, pt, sm);
        __builtin_amdgcn_sched_barrier(0);
        mn = umin(umin(umin(umin(mn, y[2][3]), y[3][0]), y[3][1]), y[3][2]);
        __builtin_amdgcn_sched_barrier(0);
        o[1] = mfma32(vox[1], pt, o[1]);
        __builtin_amdgcn_sched_barrier(0);
        mn = umin(mn, y[3][3]);
        __builtin_amdgcn_sched_barrier(0);
        o[2] = mfma32(vox[2], pt, o[2]);
        __builtin_amdgcn_sched_barrier(0);
        u64 bad = __builtin_amdgcn_ballot_w64(mn == 0u);
        chk_a();
        __builtin_amdgcn_sched_barrier(0);
        float rs_ = __builtin_amdgcn_rcpf(sm[0]);
        __builtin_amdgcn_sched_barrier(0);
        o[3] = mfma32(vox[3], pt, o[3]);
        __builtin_amdgcn_sched_barrier(0);
        rs_ *= st[0];
        chk_b();
        __builtin_amdgcn_sched_barrier(0);
        o[4] = mfma32(vox[4], pt, o[4]);
        __builtin_amdgcn_sched_barrier(0);
        float rm = dppf<0x141>(rs_);
        bad |= __builtin_amdgcn_ballot_w64(!(chk <= st[1]));
        __builtin_amdgcn_sched_barrier(0);
        o[0] = mfma32(vox[0], pt, o[0]);
        __builtin_amdgcn_sched_barrier(0);
        const bool good = bad == 0u;
        if (__builtin_expect(!good, 0)) {
          mn = umin(umin(umin(min4(y[0]), min4(y[1])), umin(min4(y[2]), min4(y[3]))), min4(y[4]));
          if (!wave_all(mn != 0u) && !dead) {
            FC(1);
            do {
              if (++spins > SPIN_CAP) { dead = true; break; }
              asm volatile("" ::: "memory");
#pragma unroll
              for (int k = 0; k < 4; ++k) y[k] = SC1_LOADS(l1off + (k & 1) * 2048 + (k >> 1) * 256, o1);
              y[4] = SC1_LOADS(l1offm, o1m);
              mn = umin(umin(umin(min4(y[0]), min4(y[1])), umin(min4(y[2]), min4(y[3]))), min4(y[4]));
            } while (!wave_all(mn != 0u));
          } else FC(2);
          pm = sum8();
          chk_a(); chk_b();
          float mx = 0.f;
          if (!wave_all(chk <= st[1])) {
            mx = fmx(fmx(fmx(d1[0], d1[1]), d1[2]), fmx(fmx(d1[3], m0), m1));
            mx = fmx(mx, dppf<DPP_ROR8>(mx));
            mx = x32mx(x16mx(mx));
            pm = pv_mem(m0 - mx, m1 - mx);
            sm = mfma32(ONES, pm, Z4);
          }
          pt = p_tok(d1 - mx, pm);
          sm = mfma32(ONES, pt, sm);
#pragma unroll
          for (int k = 0; k < 5; ++k) o[k] = mfma32(vox[k], pt, o[k]);
          __builtin_amdgcn_sched_barrier(0);
#pragma unroll
          for (int k = 0; k < 12; ++k) __builtin_amdgcn_s_setprio(3);
          __builtin_amdgcn_sched_barrier(0);
          rs_ = __builtin_amdgcn_rcpf(sm[0]) * st[0];
          rm = dppf<0x141>(rs_);
          PIN_V(rs_); PIN_V(rm);
          __builtin_amdgcn_sched_barrier(0);
        }
        STAMP(7); STAMPV(13, spins); LOGF(0, spins);
        {
          STAMP(0);
          __builtin_amdgcn_sched_barrier(0);
#define FM(d, x, r, ctrl) "v_fmac_f32_dpp " d ", " x ", " r " " ctrl "\n\t"
#define ID_ "quad_perm:[0,1,2,3] row_mask:0xf bank_mask:"
#define HM_ "row_half_mirror row_mask:0xf bank_mask:"
#define R8_ "row_ror:8 row_mask:0xf bank_mask:"
#define MI_ "row_mirror row_mask:0xf bank_mask:"
          auto add = [&](float v0, float v1, unsigned off) {
            const u32x2 v = {((unsigned)(int)v0 & ~63u) | 2u, (unsigned)(int)v1};
            asm volatile("buffer_atomic_add_x2 %0, %1, %2, %3 offen" :: "v"(v), "v"(off), "s"(rs), "s"(o2) : "memory");
            __builtin_amdgcn_sched_barrier(0);
          };
          asm volatile(FM("%0", "%4", "%2", ID_ "0x1") FM("%1", "%5", "%2", ID_ "0x1") FM("%0", "%6", "%3", HM_ "0x2") FM("%1", "%7", "%3", HM_ "0x2")
                       FM("%0", "%8", "%3", HM_ "0x1") FM("%1", "%9", "%3", HM_ "0x1") FM("%0", "%10", "%2", ID_ "0x2") FM("%1", "%11", "%2", ID_ "0x2")
                       : "+v"(va0), "+v"(va1) : "v"(rs_), "v"(rm), "v"(o[1][0]), "v"(o[1][1]), "v"(o[1][2]), "v"(o[1][3]), "v"(o[2][0]), "v"(o[2][1]), "v"(o[2][2]), "v"(o[2][3]));
          __builtin_amdgcn_sched_barrier(0);
          add(va0, va1, acco);
          sw = __builtin_bit_cast(u32x2, __hip_atomic_load((u64*)(misc + 24), __ATOMIC_RELAXED, __HIP_MEMORY_SCOPE_WORKGROUP));
          __builtin_amdgcn_sched_barrier(0);
          const auto z0 = __builtin_amdgcn_permlane32_swap(f2u(o[0][0]), f2u(o[0][2]), false, false), z1 = __builtin_amdgcn_permlane32_swap(f2u(o[0][1]), f2u(o[0][3]), false, false);
          asm volatile(FM("%0", "%4", "%2", R8_ "0x4") FM("%1", "%5", "%2", R8_ "0x4") FM("%0", "%6", "%3", MI_ "0x8") FM("%1", "%7", "%3", MI_ "0x8")
                       FM("%0", "%8", "%3", MI_ "0x4") FM("%1", "%9", "%3", MI_ "0x4") FM("%0", "%10", "%2", R8_ "0x8") FM("%1", "%11", "%2", R8_ "0x8")
                       FM("%0", "%12", "%2", ID_ "0x1") FM("%1", "%13", "%2", ID_ "0x1") FM("%0", "%14", "%3", HM_ "0x1") FM("%1", "%15", "%3", HM_ "0x1")
                       : "+v"(vb0), "+v"(vb1) : "v"(rs_), "v"(rm), "v"(o[3][0]), "v"(o[3][1]), "v"(o[3][2]), "v"(o[3][3]), "v"(o[4][0]), "v"(o[4][1]), "v"(o[4][2]), "v"(o[4][3]),
                         "v"(z0[0]), "v"(z1[0]), "v"(z0[1]), "v"(z1[1]));
          __builtin_amdgcn_sched_barrier(0);
          add(vb0, vb1, acco2);
          tpub = now32();
          lds_inc<L_MISC + 104 + 4 * (w & 1)>(lds0);
          STAMP(10);
          asm volatile("buffer_atomic_umax %0, %1, %2, %3 offen" :: "v"(tpub | 1u), "v"(tso), "s"(rs), "s"(o2) : "memory");
        }
      }
    }
    STAMP(8);
    ARRIVE(1);
    o2p = o2;
    PIN_S(o2p);
    if (w < 2) {
      if (__builtin_expect(__builtin_amdgcn_readfirstlane(sw[1]) <= (unsigned)s, 0)) {
        lds_spin<L_MISC + 100, 0>(lds0, (unsigned)s + 1u);
        sw[0] = __hip_atomic_load((unsigned*)(misc + 24), __ATOMIC_RELAXED, __HIP_MEMORY_SCOPE_WORKGROUP);
      }
    } else {
      u32x4 q;
      asm volatile("1:\n\tds_read_b128 %0, %1 offset:%3\n\t.if %4\n\ts_sleep 1\n\t.endif\n\ts_waitcnt lgkmcnt(0)\n\tv_min3_u32 v201, v201, v202, v203\n\tv_cmp_ge_u32 vcc, %2, v201\n\ts_cbranch_vccnz 1b"
                   : "=&{v[200:203]}"(q) : "v"(lds0), "s"(s), "n"(L_MISC + 96), "n"(SP_SL) : "vcc", "memory");
      __builtin_amdgcn_s_setprio(3);
      tpub = now32();
      sw[0] = q[0];
    }
    STAMP(9);
    x_fetch_n<w == 3 ? F3C : (w == 2 ? F2C : F0C)>(xsrc, lds0, s + 3, w == 3 ? 40 - F3C : P0C + w * F0C, lane * 16);
    LOGT(0, tpub); LOGT(1, tb2_);
    TSTAMP(1, tpub);
    ARRIVE_OUT();
    const unsigned an = __builtin_amdgcn_readfirstlane(sw[0]);
    const int ahead = (int)(an - tpub);
    tw = tpub + (unsigned)(an == 0u || ahead > 1000 ? SCH_FIRST : imax(ahead, w < 2 ? SCH_DMIN : 0));
    offs = tw - an;
  }
  rows(NSEG - 1);
  STAMP_OUT();
  LOGP(4);
  static_assert(LAG3 == 2 && LAG4 == 3 && LAG5 == 6, "drain schedule");
  unsigned* const cntx = (unsigned*)(misc + 16);
  auto poll = [&](auto load, auto ready) {
    load();
    for (unsigned spins = 0; !ready() && !dead;) {
      if (++spins > SPIN_CAP) dead = true;
      asm volatile("" ::: "memory");
      load();
    }
  };
  auto mark = [&](int k) { if (w == 0) *(volatile unsigned*)(misc + k) = cyc32() | 1u; };
  auto after = [&](int k, unsigned d) { lds_wait((unsigned*)(misc + k), 1u); wait_cyc(__builtin_amdgcn_readfirstlane(*(volatile unsigned*)(misc + k)) + d); };
  const int qt = w >> 1;
  auto t4 = [&](int sl) { poll([&]() { pv.load(rs, ck, sl, gp, qt, lane); }, [&]() { return pv.ready(); }); pv.run(lds, rs, sl, gp, m, qt, lane, true); };
  auto t5 = [&](int sl, int part) { poll([&]() { ot.load(a, rs, sl, b, part); }, [&]() { return ot.ready(b); }); ot.run(lane); ot.store(a, sl, b, lane); };
  BAR();
  const unsigned tp1 = cyc32();
  if (w == 2) {
    TokTask t2 = tk;
    t2.load(rs, ts_slot(ck, NSEG - 3)); tk.load(rs, ts_slot(ck, NSEG - 2));
    t2.run(j, lane, SelLo); t2.store(rs, pt_slot(ck, NSEG - 3));
  }
  if (w == 3) { kq_get(NSEG - 1); t2_mem(qk_frag(lds, g, c), qx[0], qx[1], rs, ts_slot(ck, NSEG - 1), h, j, inv * KEXP, lane); }
  if (w == 0) tk.init(h, j, lane);
  LOGD(0);
  if (w == 0) {
    ot.init(b, lane);
    wait_cyc(tp1 + END_E1);
    poll([&]() { tk.load(rs, ts_slot(ck, NSEG - 1)); }, [&]() { return tk.ready(); });
    LOGD(1);
  }
  if (!(w & 1)) {
    tk.run(j, lane, SelLo); tk.store(rs, pt_slot(ck, NSEG - 1 - qt));
    mark(14);
    LOGD(2);
    if (w == 2) ot.init(b, lane);
    ot.load(a, rs, NSEG - 4 + w, b, 2);
    lds_wait(cnt2, 5u * NSEG + 1u);
    after(14, 600); wait_cyc(tp1 + END_E2);
    t4(NSEG - 1);
    mark(15);
    LOGD(3);
  } else {
    ot.init(b, lane);
#pragma unroll 1
    for (int i = 0; i < 3; ++i) {
      wait_cyc(tp1 + (i == 0 ? END_E60 : (i == 1 ? END_E61 : END_E62)));
      t4(NSEG - 4 + i);
      if (i == 1 || (i == 2 && w == 1)) t5(i == 1 ? NSEG - 7 + qt : NSEG - 5, 3);
    }
    ot.load(a, rs, NSEG - 4 + w, b, 2);
    LOGP(5);
  }
  after(15, 600); wait_cyc(tp1 + END_E3);
  t5(NSEG - 4 + w, 1);
  LOGD(4);
  VM_DRAIN();
  if (w != 0) lds_inc<L_MISC + 64>(lds0);
  else {
    lds_wait(cntx, 3u);
    LOGD(5);
    if (lane == 0) __hip_atomic_fetch_add((gu64*)((GAS char*)(a.ws + o2) + acc_off(m * 4, 0)), (u64)1, RLX_AGENT);
    wait_until(now32() + SCH_DRAIN);
    wait_cyc(tp1 + END_E4);
    unsigned x;
    poll([&]() { x = __builtin_amdgcn_raw_buffer_load_b32(rs, acc_off(c * 4, 0), o2, 16); }, [&]() { return wave_all((x & 16u) != 0u); });
    lds_inc<L_MISC + 68>(lds0);
    LOGD(6);
  }
  LOGP(8);
}

__device__ __forceinline__ void proj_front(const RecArgs& a, char* lds, const __amdgpu_buffer_rsrc_t rs, const int v) {
  const int lane = __builtin_amdgcn_workitem_id_x() & 63, b = __builtin_amdgcn_workgroup_id_x(), c = lane & 15, g = lane >> 4;
  const int rb = (b & 7) + 8 * (b >> 5), cq = (b >> 3) & 3;
  const unsigned lds0 = (unsigned)(uintptr_t)(LAS char*)lds;
  unsigned* const cnt = (unsigned*)(lds + L_MISC) + 20;
  LOGF2(0);
  float* const red = (float*)(lds + L_RED1);
  float* const fin = red + 640;
  const f32x4 Z4 = {0.f, 0.f, 0.f, 0.f};
#pragma unroll
  for (int i = 0; i < 8; ++i) x_fetch((const u16*)(a.ws + OFF_OB) + (size_t)rb * 32 * DM, lds0, 0, 8 * v + i, lane);
  bf16x8 W[2][20];
#pragma unroll
  for (int k = 0; k < 40; ++k) W[k & 1][k >> 1] = *(const bf16x8*)(a.ws + OFF_WOBT + ((size_t)(cq * 10 + 2 * v + (k & 1)) * 20 + (k >> 1)) * 1024 + lane * 16);
  const unsigned fo = (unsigned)(rb * 40960 + (cq * 5 + v) * 1024 + ((g >> 1) * 16 + c) * 16), f8 = fo + (g & 1) * 8, f16 = fo + (g & 1) * 20480;
  u32x2 xo[2][2];
#pragma unroll
  for (int k = 0; k < 4; ++k)
    xo[k >> 1][k & 1] = __builtin_bit_cast(u32x2, __hip_atomic_load((const gu64*)((const char*)a.xnew + f8 + (k >> 1) * 512 + (k & 1) * 20480), RLX_AGENT));
  asm volatile("s_waitcnt vmcnt(44)" ::: "memory");
  lds_inc<L_MISC + 80>(lds0);
  lds_wait(cnt, 5u);
  LOGF2(1);
  f32x4 acc[2][2] = {{Z4, Z4}, {Z4, Z4}};
#pragma unroll
  for (int ks = 0; ks < 20; ++ks)
#pragma unroll
    for (int t = 0; t < 2; ++t) {
      const bf16x8 x = *(const bf16x8*)(lds + L_XN + (t * 20 + ks) * 1024 + lane * 16);
#pragma unroll
      for (int i = 0; i < 2; ++i) acc[i][t] = mfma32(W[i][ks], x, acc[i][t]);
    }
  u32x2 xp[2][2];
  PIN_V(acc[1][1]);
  LOGF2(2);
#pragma unroll
  for (int k = 0; k < 4; ++k) {
    const int i = k >> 1, t = k & 1;
#pragma unroll
    for (int q = 0; q < 2; ++q) {
      const unsigned r = pack2(acc[i][t][2 * q], acc[i][t][2 * q + 1]), x = xo[i][t][q];
      xp[i][t][q] = pack2(u2f(x << 16) + u2f(r << 16), u2f(x & 0xffff0000u) + u2f(r & 0xffff0000u));
      acc[i][t][2 * q] = u2f(xp[i][t][q] << 16); acc[i][t][2 * q + 1] = u2f(xp[i][t][q] & 0xffff0000u);
    }
    const float ml = x32sum(x16sum((acc[i][t][0] + acc[i][t][1]) + (acc[i][t][2] + acc[i][t][3]))) * (1.0f / 16.0f);
    float q = 0.f;
#pragma unroll
    for (int r = 0; r < 4; ++r) { const float d = acc[i][t][r] - ml; q += d * d; }
    q = x32sum(x16sum(q));
    if (g == 0) { red[(2 * v + i) * 32 + t * 16 + c] = ml; red[320 + (2 * v + i) * 32 + t * 16 + c] = q; }
  }
  lds_inc<L_MISC + 80>(lds0);
  if (v == 0) {
    lds_wait(cnt, 10u);
    LOGF2(3);
    float mw[10], ml = 0.f, m2 = 0.f;
#pragma unroll
    for (int i = 0; i < 10; ++i) { mw[i] = red[i * 32 + (lane & 31)]; ml += mw[i]; m2 += red[320 + i * 32 + (lane & 31)]; }
    ml *= 0.1f;
#pragma unroll
    for (int i = 0; i < 10; ++i) m2 += 16.0f * (mw[i] - ml) * (mw[i] - ml);
    const unsigned xs = (unsigned)OFF_LNX + rb * 1024;
    if (lane < 32) SC1_STORE8S(((u32x2){f2u(ml), nz(f2u(m2))}), (cq * 32 + lane) * 8, xs);
    wait_cyc(cyc32() + FR_D);
    u32x2 x0, x1;
    for (unsigned spins = 0; spins < SPIN_CAP; ++spins) {
      x0 = __builtin_amdgcn_raw_buffer_load_b64(rs, lane * 8, xs, 16); x1 = __builtin_amdgcn_raw_buffer_load_b64(rs, 512 + lane * 8, xs, 16);
      if (wave_all(x0[1] != 0u && x1[1] != 0u)) break;
      asm volatile("" ::: "memory");
    }
    const float m0 = u2f(x0[0]), m1 = u2f(x1[0]);
    const float mean = x32sum(m0 + m1) * 0.25f;
    const float M2 = x32sum((u2f(x0[1]) + u2f(x1[1])) + 160.0f * ((m0 - mean) * (m0 - mean) + (m1 - mean) * (m1 - mean)));
    const float mb = rbf(mean), den = rbf(__builtin_sqrtf(rbf(rbf(M2 * (1.0f / DM)) + 1e-5f)));
    if (lane < 32) {
      fin[lane] = mb; fin[32 + lane] = 1.0f / den;
      if (cq == 0) __hip_atomic_store((GAS unsigned*)(a.ws + OFF_XNRM) + (rb * 32 + lane) * 4, f2u(1.02f * (M2 + DM * (mean - mb) * (mean - mb)) / (den * den)), RLX_AGENT);
    }
    lds_inc<L_MISC + 84>(lds0);
  }
  lds_wait(cnt + 1, 1u);
  LOGF2(4);
  const __amdgpu_buffer_rsrc_t rx = __builtin_amdgcn_make_buffer_rsrc(a.ws + OFF_XN, 0, (int)(OFF_QB - OFF_XN), 0x00020000);
  auto put = [&](const u32x2 p0, const u32x2 p1, unsigned so) {
    const auto r0 = __builtin_amdgcn_permlane16_swap(p0[0], p1[0], false, false), r1 = __builtin_amdgcn_permlane16_swap(p0[1], p1[1], false, false);
    __builtin_amdgcn_raw_buffer_store_b128((u32x4){r0[0], r1[0], r0[1], r1[1]}, rx, f16, so, 16);
  };
#pragma unroll
  for (int i = 0; i < 2; ++i) {
    u32x2 y[2];
#pragma unroll
    for (int t = 0; t < 2; ++t) {
      const float mb = fin[t * 16 + c], rden = fin[32 + t * 16 + c];
      y[t] = (u32x2){pack2(rbf(acc[i][t][0] - mb) * rden, rbf(acc[i][t][1] - mb) * rden), pack2(rbf(acc[i][t][2] - mb) * rden, rbf(acc[i][t][3] - mb) * rden)};
    }
    put(y[0], y[1], i * 512);
  }
  VM_DRAIN();
  lds_inc<L_MISC + 80>(lds0);
#pragma unroll
  for (int i = 0; i < 2; ++i) put(xp[i][0], xp[i][1], (unsigned)(OFF_XB - OFF_XN) + i * 512);
  if (v == 0) {
    lds_wait(cnt, 15u);
    if (rb < 4 && lane < 8) __hip_atomic_fetch_add((GAS unsigned*)(a.ws + OFF_RDY) + lane * 64, 1u, RLX_AGENT);
  }
  LOGF2(5);
}

__global__ __launch_bounds__(512, 2) void rec_kernel(RecArgs a) {
  __shared__ __attribute__((aligned(16))) char lds[L_BYTES];
  const int tid = __builtin_amdgcn_workitem_id_x(), wid = __builtin_amdgcn_readfirstlane(tid >> 6), w = wid & 3;
  const __amdgpu_buffer_rsrc_t rs = __builtin_amdgcn_make_buffer_rsrc(a.ws, 0, (int)COMM_BYTES, 0x00020000);
  if (tid < 32) ((float*)(lds + L_MISC))[tid] = (tid == 4 || tid == 5) ? 1.0f : 0.0f;
  BAR();
  if (a.layer && wid < 5) proj_front(a, lds, rs, wid);
  if (wid < 4) {
    if (w == 0) rec_waves<0>(a, lds, rs);
    else if (w == 1) rec_waves<1>(a, lds, rs);
    else if (w == 2) rec_waves<2>(a, lds, rs);
    else rec_waves<3>(a, lds, rs);
  } else {
    if (w == 0) token_waves<0>(a, lds, rs);
    else if (w == 1) token_waves<1>(a, lds, rs);
    else if (w == 2) token_waves<2>(a, lds, rs);
    else token_waves<3>(a, lds, rs);
    qkv_tail(a, lds, w);
  }
}

#if defined(__HIP_DEVICE_COMPILE__)
#pragma clang attribute pop
#endif

constexpr int KQ = S_ / 4;
#define ATT_P1 6
#define ATT_P2 10
#define ATT_P3 14
#define ATT_THR 6.0f
#define ATT_NQ 4
#define ATT_LMAX 1.0e12f
#if defined(__HIP_DEVICE_COMPILE__)
#pragma clang attribute push(__attribute__((target("no-packed-fp32-ops"))), apply_to = function)
#endif
__global__ __launch_bounds__(256 * ATT_NQ, 4) void attb_kernel(const u16* __restrict__ Qp, const u16* __restrict__ KV, u16* __restrict__ O) {
  __shared__ float mrg_[ATT_NQ][3][2][13][64];
  const int tid = __builtin_amdgcn_workitem_id_x(), bid = __builtin_amdgcn_workgroup_id_x();
  __shared__ unsigned sink[64];
  own_code<4800>(tid, sink);
  const int lane = tid & 63, wid = __builtin_amdgcn_readfirstlane(tid >> 6), kh = wid / ATT_NQ, r = lane & 31, hh = lane >> 5;
  const int hd = (bid & 7) * 4 + ((bid >> 3) & 3), qb = (bid >> 5) * ATT_NQ + wid % ATT_NQ;
  float (*mrg)[2][13][64] = mrg_[wid % ATT_NQ];
  const int q0 = qb * 64;
  bf16x8 qa[2]; u32x4 qb2[2];
#pragma unroll
  for (int qt = 0; qt < 2; ++qt) {
    { const u32x2* qp = (const u32x2*)(Qp + ((size_t)hd * S_ + q0 + qt * 32 + r) * QST + 8 * hh); const u32x2 q0_ = qp[0], q1_ = qp[1]; qa[qt] = mk8(q0_[0], q0_[1], q1_[0], q1_[1]); }
    const u32x2 t = *(const u32x2*)(Qp + ((size_t)hd * S_ + q0 + qt * 32 + r) * QST + 16);
    qb2[qt] = (u32x4){hh ? 0u : t[0], hh ? 0u : t[1], 0u, 0u};
  }
  const __amdgpu_buffer_rsrc_t rk = __builtin_amdgcn_make_buffer_rsrc((void*)(KV + ((size_t)hd * 64 + kh * 16) * (KVT / 2)), 0, 16 * KVT, 0x00020000);
  unsigned ok1 = lane * 16, ok2 = hh ? 1u << 30 : 1024 + r * 8, ov = r > 20 ? 1u << 30 : 1280 + (hh * 21 + r) * 16;
  f32x16 o[2];
  float m[2];
  const f32x16 Z16 = {0.f, 0.f, 0.f, 0.f, 0.f, 0.f, 0.f, 0.f, 0.f, 0.f, 0.f, 0.f, 0.f, 0.f, 0.f, 0.f};
  struct KvT { u32x4 k1, v0, v1; u32x2 k2; };
  auto fetch = [&](int t) {
    KvT x;
    __builtin_amdgcn_sched_barrier(0);
    x.k1 = __builtin_amdgcn_raw_buffer_load_b128(rk, ok1, t * KVT, 0); x.k2 = __builtin_amdgcn_raw_buffer_load_b64(rk, ok2, t * KVT, 0);
    x.v0 = __builtin_amdgcn_raw_buffer_load_b128(rk, ov, t * KVT, 0); x.v1 = __builtin_amdgcn_raw_buffer_load_b128(rk, ov + 672, t * KVT, 0);
    __builtin_amdgcn_sched_barrier(0);
    return x;
  };
  auto tile = [&](const KvT& x, auto MODE) {
    const bf16x8 ckb = mk8(x.k2[0], x.k2[1], 0x3f803f80u, 0x00003f80u);
    f32x16 sa[2];
#pragma unroll
    for (int qt = 0; qt < 2; ++qt) {
      sa[qt] = __builtin_amdgcn_mfma_f32_32x32x16_bf16(as8(x.k1), qa[qt], Z16, 0, 0, 0);
      sa[qt] = __builtin_amdgcn_mfma_f32_32x32x16_bf16(ckb, as8(qb2[qt]), sa[qt], 0, 0, 0);
    }
#pragma unroll
    for (int qt = 0; qt < 2; ++qt) {
      if (MODE != 0) {
        float mx = fmx(fmx(sa[qt][0], sa[qt][1]), sa[qt][2]);
#pragma unroll
        for (int i = 3; i < 15; i += 2) mx = fmx(fmx(mx, sa[qt][i]), sa[qt][i + 1]);
        mx = fmx(mx, sa[qt][15]);
        if (MODE == 1 || !wave_all(mx <= ATT_THR)) {
          mx = x32mx(mx);
          if (MODE != 1) mx = fmx(mx, 0.f);
          const float mn = m[qt] + mx;
          const unsigned h1 = f2bf(mn); const float r1 = mn - bf2f(h1);
          const unsigned h2 = f2bf(r1); const float r2 = r1 - bf2f(h2);
          const unsigned h3 = f2bf(r2);
          const float me = (bf2f(h1) + bf2f(h2)) + bf2f(h3);
          const float dm = me - m[qt];
          m[qt] = me;
          if (hh == 0) { qb2[qt][2] = (h1 | (h2 << 16)) ^ 0x80008000u; qb2[qt][3] = h3 ^ 0x8000u; }
          const float alpha = __builtin_amdgcn_exp2f(-dm);
#pragma unroll
          for (int i = 0; i < 16; ++i) { if (MODE != 1) o[qt][i] *= alpha; sa[qt][i] -= dm; }
        }
      }
      float p[16];
#pragma unroll
      for (int i = 0; i < 16; ++i) p[i] = __builtin_amdgcn_exp2f(sa[qt][i]);
      bf16x8 p0 = mk8(pack2(p[0], p[1]), pack2(p[2], p[3]), pack2(p[4], p[5]), pack2(p[6], p[7]));
      bf16x8 p1 = mk8(pack2(p[8], p[9]), pack2(p[10], p[11]), pack2(p[12], p[13]), pack2(p[14], p[15]));
      o[qt] = __builtin_amdgcn_mfma_f32_32x32x16_bf16(as8(x.v0), p0, MODE == 1 ? Z16 : o[qt], 0, 0, 0);
      o[qt] = __builtin_amdgcn_mfma_f32_32x32x16_bf16(as8(x.v1), p1, o[qt], 0, 0, 0);
    }
  };
  typedef std::integral_constant<int, 0> I0; typedef std::integral_constant<int, 1> I1; typedef std::integral_constant<int, 2> I2;
  auto pass = [&](auto MODE) {
    asm volatile("" : "+v"(ok1), "+v"(ok2), "+v"(ov) :: "memory");
    m[0] = m[1] = 0.f;
    qb2[0][2] = qb2[0][3] = qb2[1][2] = qb2[1][3] = 0u;
    KvT xa = fetch(0), xb = fetch(1);
    tile(xa, I1{});
    xa = fetch(2);
    tile(xb, MODE);
#pragma unroll 1
    for (int t = 2; t < KQ / 32; t += 2) {
      if (t == ATT_P1) __builtin_amdgcn_s_setprio(2);
      if (t == ATT_P2) __builtin_amdgcn_s_setprio(1);
      if (t == ATT_P3) __builtin_amdgcn_s_setprio(0);
      xb = fetch(t + 1);
      tile(xa, MODE);
      xa = fetch(t + 2);
      tile(xb, MODE);
    }
  };
  if (ATT_P1) __builtin_amdgcn_s_setprio(3);
  pass(I0{});
  {
    float big = 0.f;
#pragma unroll
    for (int qt = 0; qt < 2; ++qt) {
      big = fmx(big, hh ? o[qt][8] * (3.0e38f / ATT_LMAX) : __builtin_fabsf(o[qt][8]));
#pragma unroll
      for (int i = 0; i < 12; ++i) if (i != 8) big = fmx(big, (i > 8 && hh) ? 0.f : __builtin_fabsf(o[qt][i]));
    }
    if (__builtin_expect(!wave_all(big <= 3.0e38f), 0)) pass(I2{});
  }
  if (kh > 0) {
#pragma unroll
    for (int qt = 0; qt < 2; ++qt) {
#pragma unroll
      for (int i = 0; i < 12; ++i) mrg[kh - 1][qt][i][lane] = o[qt][i];
      mrg[kh - 1][qt][12][lane] = m[qt];
    }
  }
  asm volatile("s_waitcnt lgkmcnt(0)\n\ts_barrier" ::: "memory");
  if (kh == 0) {
#pragma unroll
    for (int qt = 0; qt < 2; ++qt) {
      float m1 = mrg[0][qt][12][lane], m2 = mrg[1][qt][12][lane], m3 = mrg[2][qt][12][lane];
      float mn = fmx(fmx(m[qt], m1), fmx(m2, m3));
      float a0 = __builtin_amdgcn_exp2f(m[qt] - mn), a1 = __builtin_amdgcn_exp2f(m1 - mn);
      float a2 = __builtin_amdgcn_exp2f(m2 - mn), a3 = __builtin_amdgcn_exp2f(m3 - mn);
      float v[12];
#pragma unroll
      for (int i = 0; i < 12; ++i) v[i] = (o[qt][i] * a0 + mrg[0][qt][i][lane] * a1) + (mrg[1][qt][i][lane] * a2 + mrg[2][qt][i][lane] * a3);
      auto lr = __builtin_amdgcn_permlane32_swap(f2u(v[8]), f2u(v[8]), false, false);
      float inv = __builtin_amdgcn_rcpf(u2f(lr[1]));
      const int row = q0 + qt * 32 + r;
      auto oaddr = [&](int k) { return O + (((size_t)(row >> 4) * 20 + (k >> 5)) * 64 + ((k & 31) >> 3) * 16 + (row & 15)) * 8 + (k & 7); };
      const int k0 = hd * BD + 4 * hh;
      *(u32x2*)oaddr(k0) = (u32x2){pack2(v[0] * inv, v[1] * inv), pack2(v[2] * inv, v[3] * inv)};
      *(u32x2*)oaddr(k0 + 8) = (u32x2){pack2(v[4] * inv, v[5] * inv), pack2(v[6] * inv, v[7] * inv)};
      if (hh == 0) *(u32x2*)oaddr(k0 + 16) = (u32x2){pack2(v[8] * inv, v[9] * inv), pack2(v[10] * inv, v[11] * inv)};
    }
  }
}
#if defined(__HIP_DEVICE_COMPILE__)
#pragma clang attribute pop
#endif

__global__ __launch_bounds__(640, 3) void proj_kernel(const u16* __restrict__ Of, const u16* __restrict__ Wf, const u16* __restrict__ xold,
                                                      u16* __restrict__ xnew) {
  __shared__ __attribute__((aligned(16))) char ob[40960];
  const int tid = threadIdx.x, lane = tid & 63, w = __builtin_amdgcn_readfirstlane(tid >> 6), g = lane >> 4, c = lane & 15;
  __shared__ unsigned sink[64];
  own_code<1500>(tid, sink);
  const int rb = (blockIdx.x & 7) + 8 * (blockIdx.x >> 5), cb = (blockIdx.x >> 3) & 3;
  const int col = cb * 160 + w * 16 + 4 * g;
  const u16* wp = Wf + (size_t)(cb * 10 + w) * 20 * 512 + lane * 8;
  u32x4 st[4];
#pragma unroll
  for (int i = 0; i < 4; ++i) st[i] = *(const u32x4*)(Of + (size_t)rb * 40 * 512 + (i * 640 + tid) * 8);
  bf16x8 wf[20];
#pragma unroll
  for (int ks = 0; ks < 20; ++ks) wf[ks] = *(const bf16x8*)(wp + (size_t)ks * 512);
  u32x2 xo[2];
#pragma unroll
  for (int t = 0; t < 2; ++t) xo[t] = *(const u32x2*)(xold + (((size_t)(rb * 2 + t) * 20 + (col >> 5)) * 64 + ((col & 31) >> 3) * 16 + c) * 8 + (col & 7));
#pragma unroll
  for (int i = 0; i < 4; ++i) *(u32x4*)(ob + (i * 640 + tid) * 16) = st[i];
  BAR();
  f32x4 acc[2] = {{0.f, 0.f, 0.f, 0.f}, {0.f, 0.f, 0.f, 0.f}};
#pragma unroll
  for (int ks = 0; ks < 20; ++ks)
#pragma unroll
    for (int t = 0; t < 2; ++t) acc[t] = mfma32(wf[ks], *(const bf16x8*)(ob + (t * 20 + ks) * 1024 + lane * 16), acc[t]);
#pragma unroll
  for (int t = 0; t < 2; ++t) {
    const unsigned r01 = pack2(acc[t][0], acc[t][1]), r23 = pack2(acc[t][2], acc[t][3]);
    const unsigned p01 = pack2(u2f(xo[t][0] << 16) + u2f(r01 << 16), u2f(xo[t][0] & 0xffff0000u) + u2f(r01 & 0xffff0000u));
    const unsigned p23 = pack2(u2f(xo[t][1] << 16) + u2f(r23 << 16), u2f(xo[t][1] & 0xffff0000u) + u2f(r23 & 0xffff0000u));
    *(u32x2*)(xnew + (size_t)(rb * 32 + t * 16 + c) * DM + col) = (u32x2){p01, p23};
  }
}

extern "C" void kernel_launch(void* const* d_in, const int* in_sizes, int n_in, void* d_out, int out_size, void* d_ws,
                              size_t ws_size, hipStream_t stream) {
  const u16* A_x = (const u16*)d_in[0];
  const u16* A_mem0 = (const u16*)d_in[1];
  const u16* A_Wq = (const u16*)d_in[2];
  const u16* A_Wk = (const u16*)d_in[3];
  const u16* A_Wv = (const u16*)d_in[4];
  const u16* A_Wo = (const u16*)d_in[5];
  const u16* B_wqkv = (const u16*)d_in[8];
  const u16* B_wo = (const u16*)d_in[9];
  char* ws = (char*)(((uintptr_t)d_ws + ((size_t)WS_OFF << 20) + WS_AL - 1) & ~(uintptr_t)(WS_AL - 1));
  u16* xa = (u16*)(ws + OFF_XA); u16* Ob = (u16*)(ws + OFF_OB);

  prologue_kernel<<<256, 640, 0, stream>>>(ProArgs{A_x, A_Wq, A_Wk, A_Wv, A_Wo, B_wqkv, B_wo, ws});
  for (int layer = 0; layer < 2; ++layer) {
    rec_kernel<<<256, 512, 0, stream>>>(RecArgs{A_mem0, A_Wq, A_Wk, layer ? (const u16*)(ws + OFF_XB) : A_x, xa, ws, (unsigned)layer, 0u});
    attb_kernel<<<BH * 32 / ATT_NQ, 256 * ATT_NQ, 0, stream>>>((const u16*)(ws + OFF_QB), (const u16*)(ws + OFF_KV), Ob);
  }
  proj_kernel<<<256, 640, 0, stream>>>(Ob, (const u16*)(ws + OFF_WOBT), xa, (u16*)d_out);
}
```

```cpp
#include <hip/hip_runtime.h>
#include <hip/hip_bf16.h>
#include <stdint.h>
#include <type_traits>

typedef unsigned short u16;
typedef unsigned long long u64;
typedef __attribute__((ext_vector_type(8))) short bf16x8;
typedef __attribute__((ext_vector_type(16))) short bf16x16;
typedef __attribute__((ext_vector_type(4))) float f32x4;
typedef __attribute__((ext_vector_type(2))) float f32x2;
typedef __attribute__((ext_vector_type(16))) float f32x16;
typedef __attribute__((ext_vector_type(4))) unsigned u32x4;
typedef __attribute__((ext_vector_type(2))) unsigned u32x2;
#define GAS __attribute__((address_space(1)))
#define LAS __attribute__((address_space(3)))
typedef GAS unsigned long long gu64;
#define BAR() do { __builtin_amdgcn_s_waitcnt(0xC07F); asm volatile("s_waitcnt lgkmcnt(0)\n\ts_barrier" ::: "memory"); } while (0)

constexpr int S_ = 2048, DM = 640;
constexpr int NH = 32, DH = 192, HD = NH * DH;
constexpr int MEM = 8, SEG = 32, NSEG = S_ / SEG;
constexpr int BH = 32, BD = 20;

constexpr size_t al256(size_t x) { return (x + 255) & ~(size_t)255; }
constexpr int LAG3 = 2, LAG4 = 3, LAG5 = 6;
#define SR_ 8
constexpr int SR = SR_;
constexpr unsigned SCT_STEP = 131072, OFF_SCM = SR * SCT_STEP, SCM_STEP = 32768;
constexpr int ACC_NB = 192;
constexpr size_t ACC_STEP = 65536;
constexpr unsigned ACC_TS = ACC_NB * 256;
constexpr size_t ACC_LAYER = SR * ACC_STEP;
constexpr size_t OFF_ACC = 3 * 524288;
static_assert(SR == 8, "regions");
__device__ __forceinline__ unsigned ts_piece(int i) { return ACC_TS + (unsigned)(i < 64 ? i * 256 : 128); }
constexpr size_t OFF_LNX = OFF_SCM + SR * SCM_STEP;
constexpr size_t OFF_RDY = OFF_LNX + 65536;
constexpr size_t PG = 2097152;
#define RING_ 4
constexpr int RING = RING_;
constexpr size_t TS_SLOT = (size_t)NH * 8 * 2560, PT_SLOT = (size_t)NH * 2560, OP_SLOT = (size_t)256 * 2560;
__device__ __forceinline__ unsigned ts_tok(int h, int j) { return (unsigned)((h & 15) * 32768 + (j >> 2) * 16384 + (j & 3) * 2048); }
__device__ __forceinline__ unsigned ts_mem(int h, int j) { return (unsigned)(524288 + ((h & 15) >> 1) * 16384 + (h & 1) * 4096 + j * 512); }
__device__ __forceinline__ unsigned pt_tok(int h) { return (unsigned)(((h & 15) >> 2) * 16384 + (h & 3) * 2048); }
__device__ __forceinline__ unsigned pt_mem(int h) { return (unsigned)(65536 + (h & 15) * 512); }
constexpr size_t OFF_X = 2 * PG + TS_SLOT + 3 * PT_SLOT, X_BYTES = 7 * OP_SLOT + 5 * PT_SLOT;
constexpr size_t OFF_OP = OFF_X + TS_SLOT + 5 * PT_SLOT;
constexpr size_t COMM_BYTES = OFF_X + X_BYTES + RING * OP_SLOT;
__device__ __forceinline__ unsigned near_half(unsigned ck, unsigned o) { return o + ((ck >> (o >= 2 * PG ? 8 : 4)) & 8192u); }
__device__ __forceinline__ unsigned ts_slot(unsigned ck, int sl) {
  return near_half(ck, sl == NSEG - 1 ? (unsigned)OFF_X : ((sl & 3) == 3 ? (unsigned)(2 * PG) : (unsigned)PG + (unsigned)(sl & 3) * (unsigned)TS_SLOT));
}
__device__ __forceinline__ unsigned pt_slot(unsigned ck, int sl) {
  return near_half(ck, sl >= NSEG - 5 ? (unsigned)(OFF_X + TS_SLOT) + (unsigned)(sl - (NSEG - 5)) * (unsigned)PT_SLOT
                                      : ((sl & 3) == 0 ? (unsigned)(PG + 3 * TS_SLOT) : (unsigned)(2 * PG + TS_SLOT - PT_SLOT) + (unsigned)(sl & 3) * (unsigned)PT_SLOT));
}
__device__ __forceinline__ unsigned op_slot(int sl) { return (unsigned)OFF_OP + (unsigned)(sl >= NSEG - 6 ? sl - (NSEG - 6) : 6 + sl % RING) * (unsigned)OP_SLOT; }
static_assert(RING == 4 && OFF_OP <= 3 * PG && OFF_RDY + 2048 <= OFF_ACC && OFF_ACC + ACC_LAYER == PG && TS_SLOT % 16384 == 0 && PT_SLOT % 16384 == 0, "pages");
static_assert(TS_SLOT == OP_SLOT && RING >= 4 && NSEG % SR == 0 && SR >= 5 && X_BYTES % 4096 == 0 && X_BYTES <= 256 * 20 * 1024, "slots");
constexpr size_t OFF_WB = al256(COMM_BYTES);
constexpr size_t OFF_BNRM = OFF_WB + (size_t)256 * 100 * 1024;
constexpr size_t OFF_LAT = OFF_BNRM + 1024;
constexpr size_t OFF_XNRM = OFF_LAT + 2048;
constexpr size_t OFF_WQKVBT = OFF_XNRM + al256(S_ * 16);
constexpr size_t OFF_WOBT = OFF_WQKVBT + al256((size_t)3 * DM * DM * 2);
constexpr size_t OFF_XN = OFF_WOBT + al256((size_t)DM * DM * 2);
constexpr int QST = BD;
constexpr size_t OFF_XA = OFF_XN + al256((size_t)S_ * DM * 2);
constexpr size_t OFF_XB = OFF_XA + al256((size_t)S_ * DM * 2);
constexpr size_t OFF_QB = OFF_XB + al256((size_t)S_ * DM * 2);
constexpr int KVT = 2624;
constexpr size_t OFF_KV = OFF_QB + al256((size_t)BH * S_ * QST * 2);
constexpr size_t OFF_OB = OFF_KV + al256((size_t)BH * 64 * KVT);
constexpr size_t OFF_DBG = OFF_OB + al256((size_t)S_ * DM * 2);
constexpr size_t WS_TOTAL = OFF_DBG + (size_t)2 * 64 * 8 * DM * 2;
#define WS_AL 2097152
#define WS_OFF 0
static_assert(WS_TOTAL + WS_AL + ((size_t)WS_OFF << 20) <= ((size_t)256 << 20), "workspace budget");

#if defined(__HIP_DEVICE_COMPILE__)
#pragma clang attribute push(__attribute__((target("no-packed-fp32-ops"))), apply_to = function)
#endif
__device__ __forceinline__ float u2f(unsigned v) { return __builtin_bit_cast(float, v); }
__device__ __forceinline__ unsigned f2u(float v) { return __builtin_bit_cast(unsigned, v); }
__device__ __forceinline__ float bf2f(unsigned v) { return __builtin_bit_cast(float, v << 16); }
__device__ __forceinline__ unsigned f2bf(float f) { __bf16 b = (__bf16)f; return (unsigned)__builtin_bit_cast(u16, b); }
__device__ __forceinline__ float rbf(float f) { return bf2f(f2bf(f)); }
typedef __attribute__((ext_vector_type(2))) __bf16 hbf16x2;
__device__ __forceinline__ unsigned pack2(float lo, float hi) { f32x2 v = {lo, hi}; hbf16x2 b = __builtin_convertvector(v, hbf16x2); return __builtin_bit_cast(unsigned, b); }
__device__ __forceinline__ f32x4 mfma32(bf16x8 a, bf16x8 b, f32x4 c) { return __builtin_amdgcn_mfma_f32_16x16x32_bf16(a, b, c, 0, 0, 0); }
typedef __attribute__((ext_vector_type(8))) __bf16 hbf16x8;
typedef __attribute__((ext_vector_type(16))) __bf16 hbf16x16;
__device__ __forceinline__ f32x4 smfma64(bf16x8 a, bf16x16 b, f32x4 c, int idx) {
  return __builtin_amdgcn_smfmac_f32_16x16x64_bf16(__builtin_bit_cast(hbf16x8, a), __builtin_bit_cast(hbf16x16, b), c, idx, 0, 0);
}
__device__ __forceinline__ bf16x16 cat16(bf16x8 a, bf16x8 b) { return __builtin_shufflevector(a, b, 0, 1, 2, 3, 4, 5, 6, 7, 8, 9, 10, 11, 12, 13, 14, 15); }
__device__ __forceinline__ bf16x8 half8(bf16x16 v, int hi) { return hi ? __builtin_shufflevector(v, v, 8, 9, 10, 11, 12, 13, 14, 15) : __builtin_shufflevector(v, v, 0, 1, 2, 3, 4, 5, 6, 7); }
__device__ __forceinline__ bf16x8 mk8(unsigned a, unsigned b, unsigned c, unsigned d) { u32x4 v = {a, b, c, d}; return __builtin_bit_cast(bf16x8, v); }
__device__ __forceinline__ unsigned dpp_ror8(unsigned v) { return (unsigned)__builtin_amdgcn_mov_dpp((int)v, 0x128, 0xf, 0xf, true); }
__device__ __forceinline__ float fmx(float a, float b) { return __builtin_elementwise_maximum(a, b); }

__device__ __forceinline__ int imax(int a, int b) { return a > b ? a : b; }
__device__ __forceinline__ int imin(int a, int b) { return a < b ? a : b; }
__device__ __forceinline__ unsigned umin(unsigned a, unsigned b) { return a < b ? a : b; }
__device__ __forceinline__ unsigned umax(unsigned a, unsigned b) { return a > b ? a : b; }
__device__ __forceinline__ bool wave_all(bool p) { return __builtin_amdgcn_ballot_w64(!p) == 0ull; }
__device__ __forceinline__ float x16mx(float x) { unsigned u = __builtin_bit_cast(unsigned, x); auto r = __builtin_amdgcn_permlane16_swap(u, u, false, false); return fmx(u2f(r[0]), u2f(r[1])); }
__device__ __forceinline__ float x32mx(float x) { unsigned u = __builtin_bit_cast(unsigned, x); auto r = __builtin_amdgcn_permlane32_swap(u, u, false, false); return fmx(u2f(r[0]), u2f(r[1])); }
__device__ __forceinline__ float x16sum(float x) { unsigned u = __builtin_bit_cast(unsigned, x); auto r = __builtin_amdgcn_permlane16_swap(u, u, false, false); return u2f(r[0]) + u2f(r[1]); }
__device__ __forceinline__ float x32sum(float x) { unsigned u = __builtin_bit_cast(unsigned, x); auto r = __builtin_amdgcn_permlane32_swap(u, u, false, false); return u2f(r[0]) + u2f(r[1]); }
template <int CTRL> __device__ __forceinline__ float dppf(float v) { return u2f((unsigned)__builtin_amdgcn_mov_dpp((int)f2u(v), CTRL, 0xf, 0xf, true)); }
#define OWN_PF 1
template <int BYTES> __device__ __forceinline__ void own_code(int tid, unsigned* sink) {
  if (!OWN_PF || __builtin_amdgcn_readfirstlane(tid >> 6) * 4096 >= BYTES) return;
  const char* pc; unsigned keep;
  asm volatile("s_getpc_b64 %0" : "=s"(pc));
  asm volatile("s_mov_b32 %0, m0\n\ts_mov_b32 m0, %3\n\ts_nop 0\n\tglobal_load_lds_dword %1, %2\n\ts_mov_b32 m0, %0"
               : "=&s"(keep) : "v"(tid * 64 < BYTES ? tid * 64 : 0), "s"(pc), "s"((unsigned)(uintptr_t)(LAS unsigned*)sink) : "memory");
}
constexpr int DPP_XOR1 = 0xB1, DPP_XOR2 = 0x4E, DPP_ROR4 = 0x124, DPP_ROR8 = 0x128;
#if defined(__HIP_DEVICE_COMPILE__)
#pragma clang attribute pop
#endif

typedef short v4i16_t __attribute__((ext_vector_type(4)));
__device__ __forceinline__ bf16x8 tr_frag(const char* lo_, const char* hi_) {
  const v4i16_t lo = __builtin_amdgcn_ds_read_tr16_b64_v4i16((LAS v4i16_t*)lo_), hi = __builtin_amdgcn_ds_read_tr16_b64_v4i16((LAS v4i16_t*)hi_);
  return (bf16x8){lo[0], lo[1], lo[2], lo[3], hi[0], hi[1], hi[2], hi[3]};
}
__device__ __forceinline__ int rec_wg(int gp, int m) { return (((gp & 1) * 16 + m) << 3) | (gp >> 1); }
__device__ __forceinline__ float wsum(float v) {
  v += dppf<DPP_XOR1>(v); v += dppf<DPP_XOR2>(v); v += dppf<DPP_ROR4>(v); v += dppf<DPP_ROR8>(v);
  return x32sum(x16sum(v));
}
__device__ __forceinline__ float ln_row(int lane, u32x4 (&xo)[2]) {
  const bool two = lane < 16;
  float v[16];
#pragma unroll
  for (int hf = 0; hf < 2; ++hf)
#pragma unroll
    for (int i = 0; i < 4; ++i) { const unsigned u = xo[hf][i]; v[hf * 8 + 2 * i] = bf2f(u & 0xffff); v[hf * 8 + 2 * i + 1] = bf2f(u >> 16); }
  float s0 = 0.f, s1 = 0.f;
#pragma unroll
  for (int i = 0; i < 8; ++i) { s0 += v[i]; s1 += v[8 + i]; }
  const float mean = wsum(two ? s0 + s1 : s0) * (1.0f / DM);
  s0 = s1 = 0.f;
#pragma unroll
  for (int i = 0; i < 8; ++i) { const float d0 = v[i] - mean, d1 = v[8 + i] - mean; s0 += d0 * d0; s1 += d1 * d1; }
  const float var = rbf(wsum(two ? s0 + s1 : s0) * (1.0f / DM));
  const float mb = rbf(mean);
  const float rden = 1.0f / rbf(sqrtf(rbf(var + 1e-5f)));
  float n2[2] = {0.f, 0.f};
#pragma unroll
  for (int hf = 0; hf < 2; ++hf) {
    float y[8];
#pragma unroll
    for (int i = 0; i < 8; ++i) { y[i] = rbf(rbf(v[hf * 8 + i] - mb) * rden); n2[hf] += y[i] * y[i]; }
    xo[hf] = (u32x4){pack2(y[0], y[1]), pack2(y[2], y[3]), pack2(y[4], y[5]), pack2(y[6], y[7])};
  }
  return wsum(two ? n2[0] + n2[1] : n2[0]);
}

#define PROT(k)
#define WARM_CODE 1
#define PRB_N0 0
#define PRB_N1 3
#define PRB_LOG(i, v)
#define PLD(p) __builtin_nontemporal_load((const u32x4*)(p))
struct ProArgs { const u16* x; const u16* wq; const u16* wk; const u16* wv; const u16* wo; const u16* wqkvb; const u16* wob; char* ws; };
__global__ __launch_bounds__(640, 3) void prologue_kernel(ProArgs a) {
  __shared__ __attribute__((aligned(16))) char img[192 * 320];
  __shared__ __attribute__((aligned(16))) char slots[10 * 6144];
  __shared__ float red[10][160];
  __shared__ unsigned latw[8];
  const int b = blockIdx.x, tid = threadIdx.x, lane = tid & 63, v = __builtin_amdgcn_readfirstlane(tid >> 6), c = lane & 15, g = lane >> 4;
  const int h = (b & 7) * 4 + ((b >> 3) & 3), dh = (b >> 5) & 1, cq = b >> 6, ks = dh * 10 + v;
  char* const slot = slots + v * 6144;
  PROT(0);
  __shared__ unsigned sink[64];
  own_code<9600>(tid, sink);
  const unsigned zo = v < 8 ? (unsigned)(((b >> 6) * 512 + ((b & 7) * 2 + (v >> 2)) * 32 + ((b >> 3) & 7) * 4 + (v & 3)) * 1024) : (unsigned)(((v - 7) * 2048 + (b & 7) * 32 + ((b >> 3) & 7) * 4 + (b >> 6)) * 1024);
  __builtin_amdgcn_raw_buffer_store_b128((u32x4){0u, 0u, 0u, 0u}, __builtin_amdgcn_make_buffer_rsrc(a.ws, 0, (int)(3 * PG), 0x00020000), zo + lane * 16, 0, 16);
  __builtin_amdgcn_sched_barrier(0);
  u32x4 wa[2][6], wb[6], tr[4], xo[2];
  const int xrow = b * 8 + (v & 7);
  xo[0] = *(const u32x4*)(a.x + (size_t)xrow * DM + lane * 8);
  xo[1] = *(const u32x4*)(a.x + (size_t)xrow * DM + 512 + c * 8);
  const int job = imin(b + 256 * (9 - v), 800);
  const int jn = job < 600 ? job / 20 : (job % 200) / 20, jk = job % 20;
  {
    const u16* src = job < 600 ? a.wqkvb + (size_t)(32 * jk) * 3 * DM : a.wob + (size_t)(32 * jk) * DM;
#pragma unroll
    for (int i = 0; i < 4; ++i) tr[i] = *(const u32x4*)(src + (size_t)(i * 8 + (lane >> 3)) * (job < 600 ? 3 * DM : DM) + jn * 64 + (lane & 7) * 8);
  }
#pragma unroll
  for (int i = 0; i < 6; ++i) {
    const int idx = (v * 6 + i) * 64 + lane, row = idx / 20;
    wb[i] = PLD(a.wo + (size_t)(h * DH + row) * DM + cq * 160 + (idx - row * 20) * 8);
  }
#pragma unroll
  for (int i = 0; i < 6; ++i) {
    const int idx = i * 64 + lane, r = idx / 24;
#pragma unroll
    for (int u = 0; u < 2; ++u) wa[u][i] = PLD(a.wv + (size_t)(32 * ks + 8 * (r >> 2) + (r & 3) + 4 * u) * HD + h * DH + (idx - r * 24) * 8);
  }
  unsigned pf[2] = {0u, 0u};
  auto prefetch = [&]() {
#pragma unroll
    for (int i = 0; i < 2; ++i) pf[i] = *(const unsigned*)((i ? a.wq : a.wk) + ((size_t)(b * 10 + v) * 48 + imin(lane, 47)) * 32);
  };
  prefetch();
  __builtin_amdgcn_sched_barrier(0);
  unsigned lat = ~0u;
  auto probe = [&](int n0, int n) {
    const char* p = a.ws + zo;
#pragma unroll 1
    for (int i = n0; i < n0 + n; ++i) {
      u64 t0, t1; unsigned d;
      asm volatile("s_memtime %0\n\ts_waitcnt lgkmcnt(0)\n\ts_load_dword %2, %3, %4\n\ts_waitcnt lgkmcnt(0)\n\ts_memtime %1\n\ts_waitcnt lgkmcnt(0)"
                   : "=&s"(t0), "=&s"(t1), "=&s"(d) : "s"(p), "s"((i & 3) * 256 + (i >> 2) * 128) : "memory");
      PRB_LOG(i, (unsigned)t1 - (unsigned)t0);
      lat = umin(lat, (unsigned)t1 - (unsigned)t0);
    }
  };
  if (PRB_N0) { asm volatile("s_waitcnt vmcnt(26)" ::: "memory"); probe(0, PRB_N0); }
  __builtin_amdgcn_sched_barrier(0);
  const float xn2 = u2f(__builtin_amdgcn_readfirstlane(f2u(ln_row(lane, xo))));
#pragma unroll
  for (int i = 0; i < 4; ++i) *(u32x4*)(slot + (i * 8 + (lane >> 3)) * 144 + (lane & 7) * 16) = tr[i];
  __builtin_amdgcn_fence(__ATOMIC_RELEASE, "wavefront"); __builtin_amdgcn_wave_barrier();
  bf16x8 tf[4];
#pragma unroll
  for (int t = 0; t < 4; ++t) {
    const char* bp = slot + (8 * g + (c >> 2)) * 144 + t * 32 + (c & 3) * 8;
    tf[t] = tr_frag(bp, bp + 4 * 144);
  }
  __builtin_amdgcn_fence(__ATOMIC_RELEASE, "wavefront"); __builtin_amdgcn_wave_barrier();
  __builtin_amdgcn_sched_barrier(0);
#pragma unroll
  for (int i = 0; i < 6; ++i) {
    const int idx = (v * 6 + i) * 64 + lane, row = idx / 20;
    *(u32x4*)(img + row * 320 + (((idx - row * 20) ^ ((row >> 2) & 2)) * 16)) = wb[i];
  }
  bf16x8 A[2][6];
#pragma unroll
  for (int u = 0; u < 2; ++u) {
#pragma unroll
    for (int i = 0; i < 6; ++i) {
      const int idx = i * 64 + lane, r = idx / 24;
      *(u32x4*)(slot + r * 384 + (((idx - r * 24) ^ (r >> 1)) * 16)) = wa[u][i];
    }
    __builtin_amdgcn_fence(__ATOMIC_RELEASE, "wavefront"); __builtin_amdgcn_wave_barrier();
#pragma unroll
    for (int e = 0; e < 6; ++e) A[u][e] = *(const bf16x8*)(slot + c * 384 + (((4 * e + g) ^ (c >> 1)) * 16));
    __builtin_amdgcn_fence(__ATOMIC_RELEASE, "wavefront"); __builtin_amdgcn_wave_barrier();
  }
  BAR();
  PROT(1);
  asm volatile("" :: "v"(pf[0]), "v"(pf[1]));
  if (v < 8) {
    u16* xn = (u16*)(a.ws + OFF_XN) + (((size_t)(xrow >> 4) * 20 + (lane >> 2)) * 64 + (lane & 3) * 16 + (xrow & 15)) * 8;
    *(u32x4*)xn = xo[0];
    if (lane < 16) *(u32x4*)(xn + 16 * 512) = xo[1];
    if (lane < 4) ((float*)(a.ws + OFF_XNRM))[xrow * 4 + lane] = lane == 0 ? xn2 : 0.f;
  }
  if (job < 800) {
    u16* dst = (u16*)(a.ws + (job < 600 ? OFF_WQKVBT : OFF_WOBT)) + ((size_t)jn * 80 + jk) * 512 + lane * 8;
#pragma unroll
    for (int t = 0; t < 4; ++t) *(bf16x8*)(dst + (size_t)t * 20 * 512) = tf[t];
  }
  const int gp = h >> 1, hh = h & 1;
  const __amdgpu_buffer_rsrc_t rwb = __builtin_amdgcn_make_buffer_rsrc(a.ws + OFF_WB, 0, 256 * 100 * 1024, 0x00020000);
#pragma unroll
  for (int n = 0; n < 10; ++n) {
    const int off = n * 32 + 8 * (c & 3), T = cq * 10 + n;
    const char* bp = img + (8 * g + (c >> 2)) * 320 + (off ^ ((g & 1) << 5));
    f32x4 ac[2] = {{0.f, 0.f, 0.f, 0.f}, {0.f, 0.f, 0.f, 0.f}};
#pragma unroll
    for (int e = 0; e < 6; ++e) {
      const bf16x8 bf = tr_frag(bp + e * 32 * 320, bp + (e * 32 + 4) * 320);
      ac[0] = mfma32(A[0][e], bf, ac[0]); ac[1] = mfma32(A[1][e], bf, ac[1]);
    }
    const u32x4 o = {pack2(ac[0][0], ac[0][1]), pack2(ac[0][2], ac[0][3]), pack2(ac[1][0], ac[1][1]), pack2(ac[1][2], ac[1][3])};
    const unsigned oo = T < 32 ? (unsigned)((rec_wg(gp, T & 15) * 100 + (hh * 3 + (T >> 4)) * 20 + ks) * 1024 + lane * 16)
                               : (unsigned)((rec_wg(gp, 2 * (T - 32) + (c >> 3)) * 100 + 40 + ks) * 1024 + (g * 16 + hh * 8 + (c & 7)) * 16);
    __builtin_amdgcn_raw_buffer_store_b128(o, rwb, oo, 0, 0);
    float n2 = 0.f;
#pragma unroll
    for (int r = 0; r < 4; ++r) n2 += ac[0][r] * ac[0][r] + ac[1][r] * ac[1][r];
    n2 = x32sum(x16sum(n2));
    if (g == 0) red[v][n * 16 + c] = n2;
  }
  PROT(2);
  probe(PRB_N0, PRB_N1);
  if (v < 8) latw[v] = lat;
  else if (b < 64 && lane == 0) ((unsigned*)(a.ws + OFF_LAT))[(v - 4) * 64 + b] = umin(lat, 60000u);
  if (WARM_CODE && v == 7) {
    const char* pc; int d0, d1;
    asm("s_getpc_b64 %0\n\ts_mov_b32 %1, _Z10rec_kernel7RecArgs@rel32@lo+4\n\ts_mov_b32 %2, _Z11proj_kernelPKtS0_S0_Pt@rel32@lo+12" : "=s"(pc), "=s"(d0), "=s"(d1));
    const char *c0 = pc + d0, *c1 = pc + d1;
    const long o = ((long)b * 64 + lane) * 64;
    if (o < c1 - c0) asm volatile("" :: "v"(*(const volatile GAS unsigned*)(c0 + o)));
  }
  if (v == 9 && lane < 32) {
    const int q = b * 32 + lane;
    *(u32x4*)(a.ws + OFF_KV + (size_t)(q >> 2) * KVT + 1280 + (q & 3) * 336 + 320) = (u32x4){0x3f803f80u, 0x3f803f80u, 0x3f803f80u, 0x3f803f80u};
  }
  BAR();
  if (v == 1 && lane == 0) ((unsigned*)(a.ws + OFF_LAT))[b] = umin(umin(umin(latw[0], latw[4]) + umin(latw[1], latw[5]), 30000u) + umin(umin(latw[2], latw[6]) + umin(latw[3], latw[7]), 30000u), 60000u);
  if (v == 0) {
    float mx = 0.f;
#pragma unroll
    for (int k = 0; k < 3; ++k) {
      const int col = k * 64 + lane;
      float sm = 0.f;
#pragma unroll
      for (int i = 0; i < 10; ++i) sm += red[i][col < 160 ? col : 0];
      mx = fmx(mx, sm);
    }
    mx = fmx(mx, dppf<DPP_XOR1>(mx)); mx = fmx(mx, dppf<DPP_XOR2>(mx)); mx = fmx(mx, dppf<DPP_ROR4>(mx)); mx = fmx(mx, dppf<DPP_ROR8>(mx));
    mx = x32mx(x16mx(mx));
    if (lane == 0) ((float*)(a.ws + OFF_BNRM))[(cq * 32 + h) * 2 + dh] = mx;
  }
  PROT(3);
}

struct RecArgs {
  const u16* mem0; const u16* wq; const u16* wk; const u16* xold; u16* xnew; char* ws;
  unsigned layer; unsigned pad;
};
#define RLX_AGENT __ATOMIC_RELAXED, __HIP_MEMORY_SCOPE_AGENT
constexpr unsigned SPIN_CAP = 1u << 20;
#define TSTAMP(k, v)
#define ARRIVE(k)
#define ARRIVE_OUT()
#define ARRIVE_DECL()
#define LOGV(k, v)
#define LOGF(k, v)
#define LOGP(k)
#define LOGQ(k)
#define LOGF2(k)
#define LOGD(k)
#define LOGT(k, v)
#define STAMP_DECL()
#define STAMP(k)
#define STAMPV(k, v)
#define STAMP_OUT()
#define REC_D1 520
#define SCH_V 16
#define SCH_VMIN 0
#define SCH_DMIN 4
#define SCH_PEN 170
#define SCH_VMIN2 (-4)
#define SCH_PEN2 40
#define SCH_GU 1
#define SCH_GD 2
#define SCH_UP 6
#define SCH_D0 212
#define SCH_FIRST 120
#define EXP_NODMA 0
#define BG_HOLD 300
#define QBATCH 0
#define WPF_N 12
#define WPF_W 0
#define WPF0 (63 - WPF_N)
#define BG_HOLD4 330
#define D1_W1 -20
#define F3_D 1200
#define T2W 2
#define AT_ASM 1
#define PM_Z 1
#define P2_X 1
#define ST_KS 14
#define ST_KS0 ST_KS
#define ST_KS1 ST_KS
#define PADR 0x6666
#define PADT 0x000
#define CODE_PAD(n) asm volatile(".p2align 6\n.rept %0\ns_nop 0\n.endr" :: "n"(n))
#define ST_X 290
#define ST_ALL 4
#define CPF_S 61
#define FR_D 700
#define FR_PACE 6
#define END_E60 600
#define END_E1 1300
#define END_E2 5200
#define END_E62 3600
#define END_E3 9800
#define END_E4 14500
#define EARLY_N 9
#define EARLY_D1 120
#define EARLY_V 8
#define EARLY_V1 30
#define F3 8
#define F3A 6
#define F3A_D 400
#define T4_D 300
#define T4_P 1
#define P2_W 1
#define PS_SL 1
#define F2B 6
#define F0C 0
#define F2C 0
#define W2_P 3
#define SCH_W 1
#define SP_SL 0
#define D1_M 400
#define D1_MIN 200
#define D1_C 96
#define D1_G 2
#define BG_HOLD6 0
#define W2_D 150
#define END_E61 2600
#define D1_LATE 200
#define P2_G0 10
#define P2_G2 6
#define P2_G3 6
#define SCH_RL 0
#define SCH_P 3
#define MMA_P 0
#define SP_P 0
#define F6 0
#define F7 8
#define XPF_W 2
#define SCH_LATE 0
#define LATE_V 6
#define QW 3
#define KP0 0
#define KP1 0
#define XPF 1
#define W1_D 0
#define F4 12
#define F5 0
#define F67 0
#define KP2 0
#define REC_THR 32.0f
#define SCH_DRAIN 40
#define PL_X 0
#define FC(k)
#define PIN_V(x) asm volatile("" : "+v"(x))
#define PIN_S(x) asm volatile("" : "+s"(x))
#define VM_DRAIN() do { asm volatile("s_waitcnt vmcnt(0)" ::: "memory"); __builtin_amdgcn_s_waitcnt(0x0F70); } while (0)
#define SC1_LOAD(off) __builtin_amdgcn_raw_buffer_load_b128(rs, (off), 0, 16)
#define SC1_LOADS(off, soff) __builtin_amdgcn_raw_buffer_load_b128(rs, (off), (soff), 16)
#define SC1_STORE8S(v, off, soff) __builtin_amdgcn_raw_buffer_store_b64((v), rs, (off), (soff), 16)
#define SC1_LOAD8(off) __builtin_amdgcn_raw_buffer_load_b64(rs, (off), 0, 16)
#define SC1_STORE(v, off) __builtin_amdgcn_raw_buffer_store_b128((v), rs, (off), 0, 16)
#define SC1_STORE8(v, off) __builtin_amdgcn_raw_buffer_store_b64((v), rs, (off), 0, 16)
#if defined(__HIP_DEVICE_COMPILE__)
#pragma clang attribute push(__attribute__((target("no-packed-fp32-ops"))), apply_to = function)
#endif
__device__ __forceinline__ unsigned now32() { return (unsigned)__builtin_amdgcn_s_memrealtime(); }
__device__ __forceinline__ void wait_until(unsigned t) { for (;;) { const int d = (int)(t - now32()); if (d <= 0) break; if (d > 8) __builtin_amdgcn_s_sleep(1); } }
__device__ __forceinline__ unsigned cyc32() { return (unsigned)__builtin_amdgcn_s_memtime(); }
__device__ __forceinline__ void wait_cyc(unsigned t) { for (;;) { const int d = (int)(t - cyc32()); if (d <= 0) break; if (d > 200) __builtin_amdgcn_s_sleep(1); } }
#define WAIT_ASM(clk, far_) "1:\n\t" clk " vcc\n\ts_waitcnt lgkmcnt(0)\n\ts_sub_i32 %0, %1, vcc_lo\n\ts_cmp_lt_i32 %0, " far_ "\n\ts_cbranch_scc1 3f\n\ts_sleep 1\n\ts_branch 1b\n" \
                            "2:\n\t" clk " vcc\n\ts_waitcnt lgkmcnt(0)\n\ts_sub_i32 %0, %1, vcc_lo\n3:\n\ts_cmp_gt_i32 %0, 0\n\ts_cbranch_scc1 2b"
__device__ __forceinline__ int spin_until(unsigned t) { int d; asm volatile(WAIT_ASM("s_memrealtime", "9") : "=&s"(d) : "s"(t) : "vcc", "scc", "memory"); return d; }
__device__ __forceinline__ int spin_cyc(unsigned t) { int d; asm volatile(WAIT_ASM("s_memtime", "200") : "=&s"(d) : "s"(t) : "vcc", "scc", "memory"); return d; }
template <int OFF, int SL> __device__ __forceinline__ void lds_spin(unsigned lds0, unsigned want) {
  unsigned v;
  asm volatile("1:\n\tds_read_b32 %0, %1 offset:%3\n\t.if %4\n\ts_sleep %4\n\t.endif\n\ts_waitcnt lgkmcnt(0)\n\tv_cmp_gt_u32 vcc, %2, %0\n\ts_cbranch_vccnz 1b"
               : "=&v"(v) : "v"(lds0), "s"(want), "n"(OFF), "n"(SL) : "vcc", "memory");
}
template <int CTRL> __device__ __forceinline__ int dppi(int v) { return __builtin_amdgcn_mov_dpp(v, CTRL, 0xf, 0xf, true); }
__device__ __forceinline__ int wave_imax(int l) {
  l = imax(l, dppi<DPP_XOR1>(l)); l = imax(l, dppi<DPP_XOR2>(l)); l = imax(l, dppi<DPP_ROR4>(l)); l = imax(l, dppi<DPP_ROR8>(l));
  int a = imax(__builtin_amdgcn_readlane(l, 0), __builtin_amdgcn_readlane(l, 16)), b = imax(__builtin_amdgcn_readlane(l, 32), __builtin_amdgcn_readlane(l, 48));
  PIN_S(a); PIN_S(b);
  return imax(a, b);
}
__device__ __forceinline__ void glds16(const void* gsrc, unsigned lds_dst) {
  unsigned keep;
  asm volatile("s_mov_b32 %0, m0\n\ts_mov_b32 m0, %2\n\ts_nop 0\n\tglobal_load_lds_dwordx4 %1, off\n\ts_mov_b32 m0, %0" : "=&s"(keep) : "v"(gsrc), "s"(lds_dst) : "memory");
}
__device__ __forceinline__ void glds16_sc1(const void* gsrc, unsigned lds_dst) {
  unsigned keep;
  asm volatile("s_mov_b32 %0, m0\n\ts_mov_b32 m0, %2\n\ts_nop 0\n\tglobal_load_lds_dwordx4 %1, off sc1\n\ts_mov_b32 m0, %0" : "=&s"(keep) : "v"(gsrc), "s"(lds_dst) : "memory");
}
__device__ __forceinline__ unsigned acc_off(int G, int row) {
  return (unsigned)(G < 128 ? ((G & 31) | ((G & 32) << 1) | ((G & 64) >> 1)) * 256 + row * 16 : (2 * G - 128 + (row >> 2)) * 256 + (row & 3) * 16);
}
__device__ __forceinline__ unsigned nz(unsigned v) { return v > 1u ? v : 1u; }
__device__ __forceinline__ unsigned pack2n(float lo, float hi) { return nz(pack2(lo, hi)); }
__device__ __forceinline__ unsigned min4(u32x4 v) { return umin(umin(v[0], v[1]), umin(v[2], v[3])); }
__device__ __forceinline__ bf16x8 as8(u32x4 v) { return __builtin_bit_cast(bf16x8, v); }

constexpr int NV = 7, NKQ = 2;
constexpr int L_RED1 = 0;
constexpr int L_BM = L_RED1 + 8 * 1536;
constexpr int L_STG = L_BM + 10240;
constexpr int L_ST = L_STG + 2048 + 2560;
constexpr int L_MISC = L_ST + 512;
constexpr int L_KX = L_MISC + 128;
constexpr int L_QX = L_KX + NKQ * 2048;
constexpr int L_XN = L_QX + NKQ * 2048;
constexpr int L_VOX = L_XN + 81920;
constexpr int L_VOM = L_VOX + NV * 5120;
constexpr int L_BYTES = L_VOM + NV * 1280;
constexpr int S_IMG = L_XN + 40960;
static_assert(S_IMG + 61440 <= L_VOM, "start-up image");
static_assert(L_BYTES <= 160 * 1024, "LDS budget");

__device__ __forceinline__ bf16x8 wqk_frag(const char* lds, int t, int ks, int lane) {
  const int c = lane & 15, g = lane >> 4, p = c & 3;
  const char* q = lds + S_IMG + (32 * ks + 8 * g + (c >> 2)) * 96 + 8 * p + (t == 0 ? 0 : (t == 1 ? 48 : (p < 2 ? 32 : 64)));
  const v4i16_t lo = __builtin_amdgcn_ds_read_tr16_b64_v4i16((LAS v4i16_t*)q), hi = __builtin_amdgcn_ds_read_tr16_b64_v4i16((LAS v4i16_t*)(q + 4 * 96));
  return (bf16x8){lo[0], lo[1], lo[2], lo[3], hi[0], hi[1], hi[2], hi[3]};
}
__device__ __forceinline__ bf16x8 sel_frag(int c, int base) {
  bf16x8 r;
#pragma unroll
  for (int i = 0; i < 8; ++i) r[i] = (c == base + i) ? (short)0x3f80 : (short)0;
  return r;
}
__device__ __forceinline__ bf16x8 qk_frag(const char* lds, int g, int c) {
  const char* p4 = lds + L_RED1 + c * 112 + g * 16;
  const char* p2 = lds + L_RED1 + c * 112 + 64 + g * 8;
  f32x4 a0 = *(const f32x4*)(p4), a1 = *(const f32x4*)(p4 + 1792), a2 = *(const f32x4*)(p4 + 3584), a3 = *(const f32x4*)(p4 + 5376);
  f32x2 b0 = *(const f32x2*)(p2), b1 = *(const f32x2*)(p2 + 1792), b2 = *(const f32x2*)(p2 + 3584), b3 = *(const f32x2*)(p2 + 5376);
  f32x4 va = (a0 + a1) + (a2 + a3);
  f32x2 vb = (b0 + b1) + (b2 + b3);
  return mk8(pack2(va[0], va[1]), pack2(va[2], va[3]), pack2(vb[0], vb[1]), 0u);
}
__device__ __forceinline__ void x_fetch(const u16* src, unsigned lds0, int seg, int p, int lane) {
  if (EXP_NODMA) return;
  glds16_sc1(src + (size_t)p * 512 + lane * 8, lds0 + L_XN + (seg & 1) * 40960 + p * 1024);
}
template <int N> __device__ __forceinline__ void x_fetch_n(const u16* src, unsigned lds0, int seg, int p0, unsigned voff) {
  if (EXP_NODMA) return;
#pragma unroll
  for (int i = 0; i < N; i += 4) {
    unsigned keep;
    asm volatile("s_mov_b32 %0, m0\n\ts_mov_b32 m0, %3\n\ts_nop 0\n\tglobal_load_lds_dwordx4 %1, %2 sc1\n\t"
                 ".if %4 > 1\n\tglobal_load_lds_dwordx4 %1, %2 offset:1024 sc1\n\t.endif\n\t.if %4 > 2\n\tglobal_load_lds_dwordx4 %1, %2 offset:2048 sc1\n\t.endif\n\t"
                 ".if %4 > 3\n\tglobal_load_lds_dwordx4 %1, %2 offset:3072 sc1\n\t.endif\n\ts_mov_b32 m0, %0"
                 : "=&s"(keep) : "v"(voff), "s"(src + (size_t)(p0 + i) * 512), "s"(lds0 + L_XN + (seg & 1) * 40960 + (p0 + i) * 1024), "n"(N - i < 4 ? N - i : 4) : "memory");
  }
}
__device__ __forceinline__ int qkv_row(int b) { return 2 * (b & 7) + ((b >> 3) >= 15); }
__device__ __forceinline__ const u16* x_src(const RecArgs& a, int seg, int b) {
  return seg < NSEG ? (const u16*)(a.ws + OFF_XN) + (size_t)seg * 32 * DM : a.xnew + (size_t)(qkv_row(b) + 16 * (seg - NSEG)) * 32 * DM;
}
template <int N>
__device__ __forceinline__ void img_fetch(const RecArgs& a, unsigned lds0, int h, int j, int part, int lane) {
#pragma unroll
  for (int i = 0; i < N; ++i) {
    const int n = part * N + i, idx = n * 64 + lane, row = idx / 6, pc = idx - row * 6;
    if (FR_PACE && N == 20) asm volatile("s_waitcnt vmcnt(%0)" :: "n"(FR_PACE) : "memory");
    glds16((pc < 3 ? a.wq : a.wk - 24) + (size_t)row * HD + h * DH + j * 24 + pc * 8, lds0 + S_IMG + n * 1024);
  }
}
constexpr int P2B = F4 + F5 + 2 * F67 + F6 + F7, P0C = P2B + F2B, F3C = 40 - F3A - F3 - P0C - 2 * F0C - F2C;
static_assert(F3C >= 0, "fetch plan");
template <int OFF> __device__ __forceinline__ void lds_inc(unsigned lds0) {
  asm volatile("s_mov_b64 exec, 1\n\tds_add_u32 %0, %1 offset:%2\n\ts_mov_b64 exec, -1" :: "v"(lds0), "v"(1u), "n"(OFF) : "memory");
}
__device__ __forceinline__ void lds_arrive(unsigned* p, int lane) {
  asm volatile("s_waitcnt lgkmcnt(0)" ::: "memory");
  if (lane == 0) __hip_atomic_fetch_add(p, 1u, __ATOMIC_RELAXED, __HIP_MEMORY_SCOPE_WORKGROUP);
}
__device__ __forceinline__ void lds_wait(unsigned* p, unsigned want) {
  while (__hip_atomic_load(p, __ATOMIC_RELAXED, __HIP_MEMORY_SCOPE_WORKGROUP) < want) __builtin_amdgcn_s_sleep(1);
  asm volatile("" ::: "memory");
}
constexpr float KEXP = 0.07216878364870322f * 1.4426950408889634f;
#define FRESH_LANE() int lane = lane0; asm volatile("" : "+v"(lane)); const int c = lane & 15, g = lane >> 4; (void)c; (void)g

struct OutTask {
  u32x4 y[4]; u32x2 xo, out;
  bf16x8 sel;
  unsigned t5x, t5f, t5o;
  __device__ __forceinline__ void init(int b, int lane) {
    const int um = b / 10, uq = (b / 5) & 1, up = b % 5, tok = lane & 15, k = up < 4 ? (up >> 1) * 256 + um * 16 + (up & 1) * 8 : 512 + um * 8, q4 = ((lane >> 4) & 1) * 4;
    t5x = (unsigned)((uq * 16 + tok) * DM + k + q4);
    t5f = (unsigned)(((uq * 20 + (k >> 5)) * 64 + ((k & 31) >> 3) * 16 + tok) * 8 + q4);
    t5o = (unsigned)(((um * 2 + uq) * 16 + (lane >> 4)) * 1280 + up * 256 + tok * 16);
    if (b >= 160) { t5x = t5f = 0u; t5o = 0x7ffffff0u; }
    sel = sel_frag(tok, 0);
    PIN_V(sel);
  }
  static __device__ __forceinline__ bool ok(int sl, int b) { return sl >= 0 && b < 160; }
  __device__ __forceinline__ void load(const RecArgs& a, const __amdgpu_buffer_rsrc_t rs, int sl, int b, int part = 3) {
    const unsigned o = t5o | (sl < 0 ? 0x7ff00000u : 0u);
    sl = imax(sl, 0);
#pragma unroll
    for (int k = 0; k < 4; ++k) if (part & 1) y[k] = SC1_LOADS(o, op_slot(sl) + k * 5120);
    if (part & 2) xo = __builtin_bit_cast(u32x2, __hip_atomic_load((const gu64*)(a.xold + (size_t)sl * 32 * DM + (a.layer ? t5f : t5x)), RLX_AGENT));
  }
  __device__ __forceinline__ bool ready(int b) { return b >= 160 || wave_all(umin(umin(min4(y[0]), min4(y[1])), umin(min4(y[2]), min4(y[3]))) != 0u); }
  __device__ __forceinline__ void run(int lane) {
    f32x4 d = {0.f, 0.f, 0.f, 0.f};
#pragma unroll
    for (int k = 0; k < 4; ++k) d = mfma32(sel, as8(y[k]), d);
#pragma unroll
    for (int i = 0; i < 2; ++i) {
      const unsigned r = pack2(d[2 * i], d[2 * i + 1]), x = xo[i];
      out[i] = pack2(u2f(x << 16) + u2f(r << 16), u2f(x & 0xffff0000u) + u2f(r & 0xffff0000u));
    }
    PIN_V(out);
  }
  __device__ __forceinline__ void store(const RecArgs& a, int sl, int b, int lane) {
    if (ok(sl, b) && lane < 32) __hip_atomic_store((gu64*)(a.xnew + (size_t)sl * 32 * DM + t5f), (u64)out[0] | ((u64)out[1] << 32), RLX_AGENT);
  }
};

struct PvTask {
  u32x4 y[2]; u32x2 xo[2];
  __device__ __forceinline__ void load(const __amdgpu_buffer_rsrc_t rs, unsigned ck, int sl, int gp, int qt, int lane) {
    const unsigned o = pt_slot(ck, sl);
#pragma unroll
    for (int k = 0; k < 2; ++k) {
      y[k] = SC1_LOAD(o + pt_tok(2 * gp) + k * 2048 + qt * 1024 + lane * 16);
      xo[k] = SC1_LOAD8(o + pt_mem(2 * gp) + k * 512 + qt * 256 + (lane & 31) * 8);
    }
  }
  __device__ __forceinline__ bool ready() { return wave_all(umin(umin(min4(y[0]), min4(y[1])), umin(umin(xo[0][0], xo[0][1]), umin(xo[1][0], xo[1][1]))) != 0u); }
  __device__ __forceinline__ void run(char* lds, const __amdgpu_buffer_rsrc_t rs, int sl, int gp, int m, int qt, int lane, bool direct) {
    const f32x4 Z4 = {0.f, 0.f, 0.f, 0.f};
    PIN_V(y[0]); PIN_V(y[1]); PIN_V(xo[0]); PIN_V(xo[1]);
    const int sv = (sl + 2 * NV) % NV;
    const char* vp = lds + L_VOX + sv * 5120 + lane * 16;
    const char* mp = lds + L_VOM + sv * 1280 + (lane & 31) * 8;
    bf16x8 pm[2], vm[5], vx[5];
#pragma unroll
    for (int t = 0; t < 5; ++t) { vx[t] = *(const bf16x8*)(vp + t * 1024); const u32x2 v = *(const u32x2*)(mp + t * 256); vm[t] = mk8(v[0], v[1], 0u, 0u); }
#pragma unroll
    for (int k = 0; k < 2; ++k) pm[k] = mk8(lane < 32 ? xo[k][0] : 0u, lane < 32 ? xo[k][1] : 0u, 0u, 0u);
    __builtin_amdgcn_sched_barrier(0);
    f32x4 o[4];
#pragma unroll
    for (int k = 0; k < 2; ++k) { o[k] = mfma32(vx[k], as8(y[0]), Z4); o[2 + k] = mfma32(vx[2], as8(y[k]), Z4); }
#pragma unroll
    for (int k = 0; k < 2; ++k) { o[k] = mfma32(vm[k], pm[0], o[k]); o[2 + k] = mfma32(vm[2], pm[k], o[2 + k]); }
#pragma unroll
    for (int k = 0; k < 2; ++k) o[k] = mfma32(vx[3 + k], as8(y[1]), o[k]);
#pragma unroll
    for (int k = 0; k < 2; ++k) o[k] = mfma32(vm[3 + k], pm[1], o[k]);
    float oc[4];
#pragma unroll
    for (int r = 0; r < 4; ++r) {
      auto sw = __builtin_amdgcn_permlane32_swap(f2u(o[3][r]), f2u(o[3][r]), false, false);
      oc[r] = o[2][r] + u2f(sw[1]);
    }
    const u32x2 v0 = {pack2n(o[0][0], o[0][1]), pack2n(o[0][2], o[0][3])}, v1 = {pack2n(o[1][0], o[1][1]), pack2n(o[1][2], o[1][3])};
    const u32x2 v2 = {pack2n(oc[0], oc[1]), pack2n(oc[2], oc[3])};
    const unsigned po = (unsigned)(((lane >> 5) * 16 + (lane & 15)) * 16 + ((lane >> 4) & 1) * 8);
    if (direct) {
      const unsigned oo = op_slot(sl) + (unsigned)((m * 32 + qt * 16 + gp) * 1280) + po;
      SC1_STORE8(v0, oo); SC1_STORE8(v1, oo + 512);
      if (lane < 32) SC1_STORE8(v2, oo + 1024);
    } else {
      char* sp = lds + L_STG + 2048 + qt * 1280 + po;
      *(u32x2*)(sp) = v0; *(u32x2*)(sp + 512) = v1;
      if (lane < 32) *(u32x2*)(sp + 1024) = v2;
    }
  }
};

struct PvTask2 {
  u32x4 y[2][2], pa[2], pb[2];
  unsigned lo, lm, vr, ma, mz, so, sz;
  __device__ __forceinline__ void init(int gp, int lane) {
    const int hi = lane >> 5;
    lo = pt_tok(2 * gp) + (unsigned)(lane * 16); lm = pt_mem(2 * gp + hi) + (unsigned)((lane & 31) * 8);
    vr = (unsigned)(L_VOX + 2048 + (lane ^ 8) * 16);
    ma = (unsigned)(L_VOM + hi * 768 + (lane & 31) * 8); mz = (unsigned)(L_VOM + 512 + ((lane & 31) ^ (hi * 8)) * 8);
    so = (unsigned)(L_STG + 2048 + (hi * 16 + (lane & 15)) * 16 + ((lane >> 4) & 1) * 8);
    sz = hi ? (unsigned)(L_RED1 + 8704 + lane * 8) : so + 1024;
    PIN_V(lo); PIN_V(lm); PIN_V(vr); PIN_V(ma); PIN_V(mz); PIN_V(so); PIN_V(sz);
#pragma unroll
    for (int q = 0; q < 2; ++q) { pa[q] = pb[q] = y[q][0] = y[q][1] = (u32x4){0u, 0u, 0u, 0u}; PIN_V(pa[q]); PIN_V(pb[q]); }
  }
  __device__ __forceinline__ void load(const __amdgpu_buffer_rsrc_t rs, unsigned o) {
#pragma unroll
    for (int q = 0; q < 2; ++q) {
      y[q][0] = SC1_LOADS(lo + q * 1024, o); y[q][1] = SC1_LOADS(lo + 2048 + q * 1024, o);
      const u32x2 p = __builtin_amdgcn_raw_buffer_load_b64(rs, lm + q * 256, o, 16);
      pa[q][0] = p[0]; pa[q][1] = p[1];
    }
  }
  __device__ __forceinline__ void run(char* lds, int sv, int lane) {
    const f32x4 Z4 = {0.f, 0.f, 0.f, 0.f};
    const char* vp = lds + L_VOX + sv * 5120 + lane * 16;
    bf16x8 vx[6];
#pragma unroll
    for (int t = 0; t < 5; ++t) vx[t] = *(const bf16x8*)(vp + t * 1024);
    vx[5] = *(const bf16x8*)(lds + vr + sv * 5120);
    const u32x2 a0 = *(const u32x2*)(lds + ma + sv * 1280), a1 = *(const u32x2*)(lds + ma + sv * 1280 + 256);
    const u32x2 z0 = *(const u32x2*)(lds + mz + sv * 1280), z1 = *(const u32x2*)(lds + mz + sv * 1280 + 256);
    const bf16x8 vm = mk8(a0[0], a0[1], a1[0], a1[1]), vz = mk8(z0[0], z0[1], z1[0], z1[1]);
#pragma unroll
    for (int q = 0; q < 2; ++q) { PIN_V(pa[q]); pb[q][2] = pa[q][0]; pb[q][3] = pa[q][1]; PIN_V(pb[q]); }
    __builtin_amdgcn_sched_barrier(0);
    f32x4 o[2][3];
#pragma unroll
    for (int q = 0; q < 2; ++q) {
#pragma unroll
      for (int t = 0; t < 3; ++t) o[q][t] = mfma32(vx[t], as8(y[q][0]), Z4);
#pragma unroll
      for (int t = 0; t < 3; ++t) o[q][t] = mfma32(vx[3 + t], as8(y[q][1]), o[q][t]);
      o[q][0] = mfma32(vm, as8(pa[q]), o[q][0]); o[q][1] = mfma32(vm, as8(pb[q]), o[q][1]); o[q][2] = mfma32(vz, as8(pa[q]), o[q][2]);
    }
#pragma unroll
    for (int q = 0; q < 2; ++q) {
#pragma unroll
      for (int t = 0; t < 3; ++t)
        *(u32x2*)(lds + (t < 2 ? so + t * 512 : sz) + q * 1280) = (u32x2){pack2n(o[q][t][0], o[q][t][1]), pack2n(o[q][t][2], o[q][t][3])};
    }
  }
};

struct TokScores {
  bf16x8 k[2], q[2]; u32x4 v[2];
  __device__ __forceinline__ void get(const char* lds, int seg, int lane) {
#pragma unroll
    for (int i = 0; i < 2; ++i) { k[i] = *(const bf16x8*)(lds + L_KX + (seg & 1) * 2048 + i * 1024 + lane * 16); q[i] = *(const bf16x8*)(lds + L_QX + (seg & 1) * 2048 + i * 1024 + lane * 16); }
  }
  __device__ __forceinline__ void run(int qt) {
    const f32x4 Z4 = {0.f, 0.f, 0.f, 0.f};
    const f32x4 d1 = mfma32(k[0], q[qt], Z4), d2 = mfma32(k[1], q[qt], Z4);
    v[qt] = (u32x4){pack2n(d1[0], d1[1]), pack2n(d1[2], d1[3]), pack2n(d2[0], d2[1]), pack2n(d2[2], d2[3])};
    PIN_V(v[qt]);
  }
  __device__ __forceinline__ void store(const __amdgpu_buffer_rsrc_t rs, unsigned off, unsigned o) {
    __builtin_amdgcn_raw_buffer_store_b128(v[0], rs, off, o, 16); __builtin_amdgcn_raw_buffer_store_b128(v[1], rs, off + 1024, o, 16);
    asm volatile("s_nop 1" ::: "memory");
  }
};

__device__ __forceinline__ void touch(const char* base, unsigned off, unsigned lds0) {
  unsigned keep;
  asm volatile("s_mov_b32 %0, m0\n\ts_mov_b32 m0, %3\n\ts_nop 0\n\tglobal_load_lds_dword %1, %2 sc1\n\ts_mov_b32 m0, %0" : "=&s"(keep) : "v"(off), "s"(base), "s"(lds0 + L_RED1 + 8192) : "memory");
}
template <int N> __device__ __forceinline__ void stouch(const char* p, unsigned& d) {
  asm volatile(".set pf_o, 0\n\t.rept %2\n\ts_load_dword %0, %1, pf_o\n\t.set pf_o, pf_o + 128\n\t.endr" : "+s"(d) : "s"(p), "n"(N) : "memory");
}
__device__ __forceinline__ void code_touch(int b, int lane, unsigned lds0) {
  const char* pc; int d0, d1;
  asm volatile("s_getpc_b64 %0\n\ts_mov_b32 %1, _Z10rec_kernel7RecArgs@rel32@lo+4\n\ts_mov_b32 %2, _Z11attb_kernelPKtS0_Pt@rel32@lo+12" : "=s"(pc), "=s"(d0), "=s"(d1));
  const int o = (lane * 32 + (b >> 3)) * 128;
  touch(pc + d0, (unsigned)(o < d1 - d0 ? o : 0), lds0);
}
__device__ __forceinline__ unsigned zero_base(int b, int h, int j, int ln) {
  return ln < 32 ? (unsigned)((h & 3) * 32768 + j * 512 + ln * 16) : ln < 40 ? (unsigned)((h & 2) * 1024 + j * 256 + (ln & 4) * 32 + (h & 1) * 64 + (ln & 3) * 16)
                 : (unsigned)OFF_ACC + (b < ACC_NB ? (unsigned)(b * 256 + (ln - 40) * 16) : ts_piece((b - ACC_NB) * 8 + ln - 40));
}
__device__ __forceinline__ unsigned sct_slot(unsigned ck, unsigned sl) { return sl * SCT_STEP + ((ck >> (sl & 4u)) & 7u) * 4096u; }
__device__ __forceinline__ unsigned scm_slot(unsigned ck, unsigned sl) { return OFF_SCM + sl * SCM_STEP + ((ck >> 8) & 7u) * 4096u; }
__device__ __forceinline__ int ck_rot(int x) { return ((x & 1) << 2) | (x >> 1); }
struct Place {
  unsigned t[8];
  __device__ __forceinline__ void load(const char* ws, int lane) {
#pragma unroll
    for (int x = 0; x < 8; ++x) t[x] = *(const unsigned*)(ws + OFF_LAT + ((imin(lane >> 3, 5) * 8 + (ck_rot(x) ^ (lane & 7))) * 8 + x) * 4);
  }
  __device__ __forceinline__ unsigned get(int b, int lane) {
    int c = (int)((((t[0] + t[1]) + (t[2] + t[3])) + ((t[4] + t[5]) + (t[6] + t[7]))) * 8u) + (lane & 7);
    c = imin(c, dppi<DPP_XOR1>(c)); c = imin(c, dppi<DPP_XOR2>(c)); c = imin(c, dppi<0x141>(c));
    unsigned k = 0u;
#pragma unroll
    for (int r = 0; r < 6; ++r) k |= (unsigned)(__builtin_amdgcn_readlane(c, 8 * r) & 7) << (4 * r);
    return (k ^ PL_X) ^ (unsigned)ck_rot(b & 7) * 0x111111u;
  }
};

struct Sched {
  u32x4 tsv, rec;
  __device__ __forceinline__ void load(const __amdgpu_buffer_rsrc_t rs, int s, int lane) {
    const unsigned o = (unsigned)OFF_ACC + (unsigned)((s + SR - 1) % SR) * (unsigned)ACC_STEP + ACC_TS;
    tsv = SC1_LOADS(lane * 256, o); rec = SC1_LOADS(128, o);
  }
  __device__ __forceinline__ unsigned last(unsigned ref) {
    const unsigned o = 0x40000000u - ref;
    int l = (int)umax(umax(tsv[0] ? tsv[0] + o : 0u, tsv[1] ? tsv[1] + o : 0u), umax(tsv[2] ? tsv[2] + o : 0u, tsv[3] ? tsv[3] + o : 0u));
    return (unsigned)wave_imax(l) - o;
  }
  __device__ __forceinline__ unsigned run(const RecArgs& a, const __amdgpu_buffer_rsrc_t rs, int s, int b, int lane) {
    constexpr int w = 3; (void)w;
    unsigned acur = __builtin_amdgcn_readfirstlane(rec[0]), aprev = __builtin_amdgcn_readfirstlane(rec[1]), hist = __builtin_amdgcn_readfirstlane(rec[3]);
    int D8 = (int)__builtin_amdgcn_readfirstlane(rec[2]);
    const int d = wave_imax((int)(umax(umax(tsv[0], tsv[1]), umax(tsv[2], tsv[3])) - aprev)), dd = 8 * d - D8;
    int Dn = D8 + (dd >> (dd > 0 ? SCH_GU : SCH_GD));
    unsigned anext = (acur + (unsigned)((Dn + 4) >> 3) + SCH_V + (s < EARLY_N ? EARLY_V : (s >= WPF0 ? LATE_V : 0))) | 1u;
    if (__builtin_expect(hist < 2u || aprev == 0u || d < 1 || d + SCH_VMIN > (int)(acur - aprev), 0)) {
      if (s == 0) {
        for (int it = 0; it < 40 && !wave_all(min4(tsv) != 0u); ++it) {
          __builtin_amdgcn_s_sleep(ST_ALL);
          asm volatile("" ::: "memory");
          load(rs, s, lane);
        }
      }
      const unsigned now = now32(), L = last(now);
      if (acur == 0u) { D8 = 8 * SCH_D0; hist = 0u; }
      const bool late = acur == 0u || (int)(L + SCH_VMIN - acur) > 0;
      if (late) { if (b == 0) FC(3); if (acur) D8 += 8 * (hist ? SCH_UP : 3 * SCH_UP); }
      else if (aprev) { if (hist < 2u) D8 = 8 * imin(imax((int)(L - aprev), 1), 4000); hist = umin(hist + 1u, 2u); }
      const unsigned base = acur == 0u ? L + SCH_FIRST : (late ? L + SCH_V + ((int)(L + SCH_VMIN2 - acur) <= 0 ? SCH_PEN2 : SCH_PEN) : acur);
      anext = (base + (unsigned)((D8 + 4) >> 3) + SCH_V + (s == 1 ? EARLY_V1 : (s < EARLY_N ? EARLY_V : 0))) | 1u;
      if (s == 0) anext = (L + ST_X) | 1u;
      if ((int)(now - L) > 50000) anext = 0u;
      if (late) acur = 0u;
      Dn = D8;
    }
    if (__builtin_expect(b == 0, 0))
      __builtin_amdgcn_raw_buffer_store_b128((u32x4){anext, acur, (unsigned)Dn, hist}, rs, lane == 0 ? 0 : 0x7ffffff0, (unsigned)OFF_ACC + (unsigned)(s % SR) * (unsigned)ACC_STEP + ACC_TS + 128u, 16);
    LOGV(0, anext); LOGV(1, acur == 0u); LOGV(2, (Dn + 4) >> 3); LOGV(3, d); LOGV(4, acur - aprev - d); LOGV(5, aprev);
    return anext;
  }
};

template <int w>
__device__ __forceinline__ void token_waves(const RecArgs& a, char* lds, const __amdgpu_buffer_rsrc_t rs) {
  const int lane0 = __builtin_amdgcn_workitem_id_x() & 63, b = __builtin_amdgcn_workgroup_id_x(), wid = w + 4; (void)wid;
  const int gp = (b & 7) * 2 + (b >> 7), m = (b >> 3) & 15, h0 = 2 * gp, h = h0 + (m >> 3), j = m & 7;
  float* const misc = (float*)(lds + L_MISC);
  const f32x4 Z4 = {0.f, 0.f, 0.f, 0.f};
  const unsigned lds0 = (unsigned)(uintptr_t)(LAS char*)lds;
  unsigned* const cnt = (unsigned*)(misc + 6);
  const float* const xnrm = (const float*)(a.ws + OFF_XNRM);
  bf16x16 W[2][10];
  auto btile = [&](int i) { return w == 1 ? 0 : (w == 2 ? 1 + i : 3 + i); };
  if (w == 1) code_touch(b, lane0, lds0);
  Place pl;
  if (w == 3) pl.load(a.ws, lane0);
  if (a.layer) {
    if (w != 0) img_fetch<20>(a, lds0, h, j, w - 1, lane0);
  } else {
    if (w == 0) img_fetch<12>(a, lds0, h, j, 4, lane0);
#pragma unroll
    for (int i = 0; i < 10; ++i) x_fetch(x_src(a, 0, b), lds0, 0, 10 * w + i, lane0);
  }
#pragma unroll
  for (int i = 0; i < 2; ++i)
#pragma unroll
    for (int p = 0; p < 10; ++p)
      if (w >= 2 || (w == 1 && i == 1)) {
        const char* wp = a.ws + OFF_WB + ((size_t)(b * 5 + btile(i)) * 20 + 2 * p) * 1024 + lane0 * 16;
        if (FR_PACE && a.layer) asm volatile("s_waitcnt vmcnt(%0)" :: "n"(FR_PACE) : "memory");
        W[i][p] = cat16(*(const bf16x8*)wp, *(const bf16x8*)(wp + 1024));
      }
  f32x4 bn = Z4;
  if (w == 0) {
    bn = *(const f32x4*)(a.ws + OFF_BNRM + lane0 * 16);
    if (!a.layer) asm volatile("s_waitcnt vmcnt(0)" ::: "memory");
  } else asm volatile("s_waitcnt vmcnt(%0)" :: "n"(w == 1 ? 20 : 40) : "memory");
  unsigned ck = 0u;
  if (w == 3) { ck = pl.get(b, lane0); *(unsigned*)(misc + 11) = ck; }
  BAR();
#pragma unroll
  for (int p = 0; p < 10; ++p) {
    if (w < 2) W[0][p] = cat16(wqk_frag(lds, 2 * w, 2 * p, lane0), wqk_frag(lds, 2 * w, 2 * p + 1, lane0));
    if (w == 0) W[1][p] = cat16(wqk_frag(lds, 1, 2 * p, lane0), wqk_frag(lds, 1, 2 * p + 1, lane0));
  }
  float cb2 = 0.f;
  if (w == 0) {
    float n = __builtin_amdgcn_sqrtf(bn[0] + bn[1]) + __builtin_amdgcn_sqrtf(bn[2] + bn[3]);
    n += dppf<DPP_XOR1>(n); n += dppf<DPP_XOR2>(n); n += dppf<DPP_ROR4>(n); n += dppf<DPP_ROR8>(n);
    n = x32mx(x16mx(n));
    cb2 = u2f(__builtin_amdgcn_readfirstlane(f2u(1.15f * n * n)));
  }
  const float cm2 = u2f((f2u(cb2) & 0x7fffffu) | 0x40000000u);
  const int ek = imax((int)((f2u(cb2) >> 23) & 0xffu), 1) - 252;
  if (w >= 2) {
#pragma unroll
    for (int i = 0; i < 4; ++i) *(unsigned*)(lds + L_KX + (w - 2) * 4096 + i * 1024 + lane0 * 16 + 12) = 0u;
  }
  OutTask ot;
  const int zl = lane0 - 16;
  const bool zon = w == 3 && zl >= 0 && (zl < 40 || b < ACC_NB + 9);
  unsigned zo = zon ? zero_base(b, h, j, zl) + (zl < 32 ? sct_slot(ck, 0u) : (zl < 40 ? scm_slot(ck, 0u) : 0u)) : 0x7ffffff0u;
  unsigned zm = zon ? (zl < 32 ? SCT_STEP + 1u : (zl < 40 ? SCM_STEP : (unsigned)ACC_STEP)) : 0u;
  const unsigned zx = ((ck ^ (ck >> 4)) & 7u) * 4096u;
  if (w == 3) { PIN_V(zo); PIN_V(zm); }
  auto st_out = [&](int s, int lane) {
    if (w < 2) return;
    const int sl = s - 2 - LAG4;
    const char* sp = lds + L_STG + 2048 + (w & 1) * 1280;
    const u32x4 v0 = *(const u32x4*)(sp + lane * 16), v1 = *(const u32x4*)(sp + 1024 + (lane & 15) * 16), z = {0u, 0u, 0u, 0u};
    const unsigned oo = (sl < 0 ? 0x7ff00000u : op_slot(sl) + (unsigned)((m * 32 + (w & 1) * 16 + gp) * 1280)) + (unsigned)(lane * 16);
    if (w == 2) {
      ot.store(a, s - 2 - LAG5, b, lane);
      SC1_STORE(v0, oo);
      SC1_STORE(v1, lane < 16 ? oo + 1024 : 0x7ffffff0u);
    } else {
      const unsigned zs = (unsigned)(s + 2) % SR;
      unsigned za = (zo + (s < 0 ? 1024u : zs) * (zm & ~1u)) ^ ((zm & 1u) * (zs & 4u ? zx : 0u));
      za = lane < 16 ? oo + 1024 : za;
      PIN_V(za);
      SC1_STORE(v0, oo);
      u32x4 vz;
#pragma unroll
      for (int i = 0; i < 4; ++i) { vz[i] = lane < 16 ? v1[i] : z[i]; PIN_V(vz[i]); }
      SC1_STORE(vz, za);
      const unsigned xo_ = (unsigned)((s - 8) * 1024 + lane * 16);
      if (__builtin_expect((unsigned)(s - 8) < 20u, 0)) SC1_STORE(z, xo_ < X_BYTES / 256 ? (unsigned)(OFF_X + b * (X_BYTES / 256)) + xo_ : 0x7ffffff0u);
    }
  };
  STAMP_DECL(); ARRIVE_DECL();
  auto mma_seg = [&](int seg, int sv, int lane, auto pause) {
    const int c = lane & 15, g = lane >> 4;
    const char* xp = lds + L_XN + (seg & 1) * 40960 + lane * 16;
    f32x4 acc[2][2] = {{Z4, Z4}, {Z4, Z4}};
    bf16x8 x[4][2];
#pragma unroll
    for (int p = 0; p < 3; ++p) { x[p][0] = *(const bf16x8*)(xp + p * 1024); x[p][1] = *(const bf16x8*)(xp + (20 + p) * 1024); }
    unsigned eo[2];
#pragma unroll
    for (int ks = 0; ks < 20; ++ks) {
      if (ks == (w == 0 ? KP0 : (w == 1 ? KP1 : KP2))) { pause(); __builtin_amdgcn_sched_barrier(0); }
      if (ks == 17) {
#pragma unroll
        for (int i = 0; i < 2; ++i) {
          eo[i] = (unsigned)(w == 0 ? (i ? L_KX : L_QX) + (seg % NKQ) * 2048 + lane * 16
                                    : (w == 1 && i == 0 ? ((g >> 1) ? L_KX : L_QX) + (seg % NKQ) * 2048 + (2 * (g & 1) * 16 + c) * 16 + 8 : L_VOX + sv * 5120 + btile(i) * 1024 + lane * 16));
          if (i == 0 || w == 1) PIN_V(eo[i]);
        }
        if (w == 0) eo[1] = eo[0] + (L_KX - L_QX);
        if (w >= 2) eo[1] = eo[0] + 1024;
      }
      __builtin_amdgcn_sched_barrier(0);
#pragma unroll
      for (int q = 0; q < 4; ++q) {
        const int tt = 1 - (q >> 1), i = q & 1;
        const bool isqk = w == 0 || (w == 1 && i == 0);
        const bf16x8 wf = half8(W[i][ks >> 1], ks & 1);
        acc[i][tt] = isqk ? mfma32(wf, x[ks & 3][tt], acc[i][tt]) : mfma32(x[ks & 3][tt], wf, acc[i][tt]);
        __builtin_amdgcn_sched_barrier(0);
        if (q < 2 && ks + 3 < 20) x[(ks + 3) & 3][q] = *(const bf16x8*)(xp + (q * 20 + ks + 3) * 1024);
        __builtin_amdgcn_sched_barrier(0);
      }
    }
#pragma unroll
    for (int i = 0; i < 2; ++i) {
      if (w + i == 1) { const float ks_ = (w == 0 || (g >> 1)) ? KEXP : 1.0f; acc[i][0] *= ks_; acc[i][1] *= ks_; }
      const u32x4 v = {pack2(acc[i][0][0], acc[i][0][1]), pack2(acc[i][0][2], acc[i][0][3]), pack2(acc[i][1][0], acc[i][1][1]), pack2(acc[i][1][2], acc[i][1][3])};
      char* p = lds + eo[i];
      if (w == 0) { *(u32x2*)(p) = (u32x2){v[0], v[1]}; *(u32x2*)(p + 1024) = (u32x2){v[2], v[3]}; }
      else if (w == 1 && i == 0) { *(unsigned*)(p) = v[0]; *(unsigned*)(p + 256) = v[1]; *(unsigned*)(p + 1024) = v[2]; *(unsigned*)(p + 1280) = v[3]; }
      else *(u32x4*)p = v;
    }
  };
  auto vo_mem = [&](int s, int sv, unsigned t0, int lane, auto last) {
    unsigned vp = (unsigned)(L_VOM + sv * 1280 + (btile(0) * 32 + (w == 1 ? lane & 31 : lane)) * 8);
    float inv = misc[4 + (s & 1)], xmax = misc[s & 3];
    unsigned mp = (unsigned)(L_BM + lane * 16);
    int sidx = (lane & 8) ? 0xEEEE : 0x4444;
    f32x4 acc[2] = {Z4, Z4};
    int ex = 0, ei = 0;
    unsigned ip = (unsigned)(L_MISC + 16 + ((s + 1) & 1) * 4);
    PIN_V(inv); PIN_V(mp);
    if (w != 0) { PIN_V(vp); PIN_V(sidx); PIN_V(acc[1]); }
    if (w >= 2) PIN_V(acc[0]);
    __builtin_amdgcn_s_setprio(2);
    if (w >= 2 && BG_HOLD6) spin_cyc(t0);
    bf16x8 x[10];
#pragma unroll
    for (int p = 0; p < 10; ++p) x[p] = *(const bf16x8*)(lds + mp + p * 1024);
    __builtin_amdgcn_sched_barrier(0);
    if (w == 0) {
      ex = imax((int)((__builtin_amdgcn_readfirstlane(f2u(__builtin_amdgcn_sqrtf(cb2 * xmax))) >> 23) & 0xffu) + 1, 32);
      ei = (int)(__builtin_amdgcn_readfirstlane(f2u(inv)) >> 23);
      PIN_S(ex); PIN_S(ei); PIN_V(ip);
    }
    if (w < 2) spin_cyc(t0);
    STAMP(5);
    __builtin_amdgcn_sched_barrier(0);
#pragma unroll
    for (int p = 0; p < 10; ++p) {
      if (w == 0) acc[0] = mfma32(x[p], x[p], acc[0]);
#pragma unroll
      for (int i = (w == 1); i < 2 && w != 0; ++i) acc[i] = smfma64(x[p], W[i][p], acc[i], sidx);
      __builtin_amdgcn_sched_barrier(0);
    }
    STAMP(7);
    if (w != 0) {
      float v[4];
#pragma unroll
      for (int r = 0; r < 4; ++r) {
        const auto sw = __builtin_amdgcn_permlane32_swap(f2u(acc[w == 1][r]), f2u(acc[1][r]), false, false);
        v[r] = (u2f(sw[0]) + u2f(sw[1])) * inv;
      }
      *(u32x2*)(lds + vp) = (u32x2){pack2(v[0], v[1]), pack2(v[2], v[3])};
    } else {
      const int t = (int)f2u(fmx(fmx(acc[0][0], acc[0][1]), fmx(acc[0][2], acc[0][3])) * cm2);
      const int et = wave_imax(t) >> 23, em = ((et + ek) >> 1) + ei;
      int e = imax(et == 255 ? 256 : em, ex);
      PIN_S(e);
      e = imin(e, 256);
      *(unsigned*)(lds + ip) = (unsigned)(e - 31) << 23;
      *(u32x2*)(misc + 8) = (u32x2){(unsigned)(285 - e) << 23, (unsigned)(e - 87) < 79u ? f2u(REC_THR) : f2u(-1.0f)};
    }
    if (last == 0 && w >= 2) asm volatile("s_waitcnt vmcnt(%0)" :: "n"(w == SCH_W ? 2 : 0) : "memory");
    lds_inc<L_MISC + 28>(lds0);
    STAMP(8);
    if (last) __builtin_amdgcn_s_setprio(0);
    if (last == 0) { if (w >= 2 && F67) lds_inc<L_MISC + 112>(lds0); }
    else if (w == 2) { VM_DRAIN(); lds_inc<L_MISC + 28>(lds0); }
  };
  unsigned* const cntq = (unsigned*)(misc + 12);
  unsigned* const cntt = (unsigned*)(misc + 26 + (w & 1));
  unsigned* const cntp = (unsigned*)(misc + 23);
  unsigned* const cntx2 = (unsigned*)(misc + 28);
  float xn4 = 0.f;
  auto wpf = [&](int s, int lane) {
    touch(a.ws + OFF_WQKVBT + (size_t)(b >> 3) * 76800 + (s - WPF0) * (76800 / WPF_N), imin(lane, 600 / WPF_N - 1) * 128, lds0);
  };
  Sched sch;
  auto late = [&](int s, int lane) {
    if (w == SCH_W && SCH_LATE && __builtin_expect(s >= 0, 1)) *(u32x2*)(misc + 24) = (u32x2){sch.run(a, rs, s, b, lane), (unsigned)(s + 1)};
    if (w == 2 || (w == 0 && F4) || (w == 1 && F5) || (w == 3 && F7)) lds_spin<L_MISC + 48, 1>(lds0, 2u * (unsigned)imax(s + 1, 0));
    if (w == 2) {
      ot.load(a, rs, s - LAG5, b);
      xn4 = u2f(__hip_atomic_load((const GAS unsigned*)xnrm + (imax(s + 1, 0) * 32 + (lane & 31)) * 4, RLX_AGENT));
    }
    if (w == XPF_W && XPF == 2) touch(a.ws + OFF_XN + (size_t)imin(s + 4, NSEG - 1) * (s < 1 ? 0 : 40960) + (b >> 3) * 1280, imin(lane, 9) * 128, lds0);
    if (w >= 2) { x_fetch_n<w == 2 ? F6 : F7>(x_src(a, s + 3, b), lds0, s + 3, F4 + F5 + 2 * F67 + (w == 3 ? F6 : 0), lane * 16); return; }
    unsigned sd = 0u;
    if (w == 0 && XPF == 3) stouch<10>(a.ws + OFF_XN + (size_t)imin(s + 4, NSEG - 1) * (s < 1 ? 0 : 40960) + (b >> 3) * 1280, sd);
    if (w == 0 && WPF_W == 4 && __builtin_expect(s >= WPF0 && s < WPF0 + WPF_N, 0))
      stouch<600 / WPF_N>(a.ws + OFF_WQKVBT + (size_t)(b >> 3) * 76800 + (s - WPF0) * (76800 / WPF_N), sd);
    x_fetch_n<w == 0 ? F4 : F5>(x_src(a, s + 3, b), lds0, s + 3, w == 0 ? 0 : F4, lane * 16);
    if (WPF_W == 0 && w == 0 && __builtin_expect(s >= WPF0 && s < WPF0 + WPF_N, 0)) wpf(s, lane);
    if (w == 1 && __builtin_expect(s == CPF_S, 0)) code_touch(b, lane, lds0);
    lds_spin<L_MISC + 104 + 4 * (w & 1), PS_SL>(lds0, (unsigned)imax(s + 1, 0));
    asm volatile("" :: "s"(sd));
  };
  auto xn_put = [&](int s) {
    float xn2 = xn4;
    xn2 = fmx(xn2, dppf<DPP_XOR1>(xn2)); xn2 = fmx(xn2, dppf<DPP_XOR2>(xn2));
    xn2 = fmx(xn2, dppf<DPP_ROR4>(xn2)); xn2 = fmx(xn2, dppf<DPP_ROR8>(xn2));
    misc[s & 3] = x16mx(xn2);
  };
  if (w == 2) { ot.init(b, lane0); ot.out = (u32x2){0u, 0u}; }
  VM_DRAIN();
  lds_arrive(cnt, lane0);
  lds_wait(cnt, 8u);
  sch.tsv = sch.rec = (u32x4){0u, 0u, 0u, 0u};
  int sv = NV - 1;
  CODE_PAD((PADT >> (4 * w)) & 15);
#pragma unroll 1
  for (int s = -1; s < NSEG - 1; ++s) {
    const int sv1 = sv == NV - 1 ? 0 : sv + 1;
    {
      FRESH_LANE();
      STAMP(0);
      if (F67) lds_spin<L_MISC + 112, 1>(lds0, 2u * (unsigned)imax(s, 0));
      mma_seg(s + 1, sv1, lane, [&]() { STAMP(1); late(s - 1, lane); STAMP(9); });
      STAMP(2);
      __builtin_amdgcn_sched_barrier(0);
      VM_DRAIN();
      u32x2 keep;
      if (w == 2) { keep = ot.out; ot.run(lane); xn_put(s); const u32x2 t = ot.out; ot.out = keep; keep = t; }
      if (w >= 2) { lds_spin<L_MISC + 92, 1>(lds0, 4u * (unsigned)imax(s, 0)); if (W1_D) spin_cyc(cyc32() + W1_D); }
      STAMP(10);
      if (WPF_W == 3 && w == 3 && __builtin_expect(s >= WPF0 && s < WPF0 + WPF_N, 0)) wpf(s, lane);
      st_out(s, lane);
      if (w == 2) ot.out = keep;
      __builtin_amdgcn_sched_barrier(0);
      if (w == 2 && XPF == 1) touch(a.ws + OFF_XN + (size_t)imin(imax(s + 3, s < 2 ? 0 : 5), NSEG - 1) * (s < 2 ? 0 : 40960) + (b >> 3) * 1280, imin(lane, 9) * 128, lds0);
      if (w >= 2 && s > 0) x_fetch_n<F67>(x_src(a, s + 2, b), lds0, s + 2, F4 + F5 + (w - 2) * F67, lane * 16);
      STAMP(3);
    }
    BAR();
    {
      FRESH_LANE();
      STAMP(4);
      const unsigned tb1 = cyc32();
      if (__builtin_expect(s >= 0, 1)) {
        if (w == SCH_W) { sch.load(rs, s, lane); __builtin_amdgcn_sched_barrier(0); }
        vo_mem(s, sv, tb1 + (s > 0 ? (w == 0 ? BG_HOLD4 : (w == 1 ? BG_HOLD : BG_HOLD6)) : 0), lane, std::integral_constant<int, 0>{});
        STAMP(6);
        if (w == SCH_W && !SCH_LATE) {
          __builtin_amdgcn_s_setprio(SCH_P);
          *(u32x2*)(misc + 24) = (u32x2){sch.run(a, rs, s, b, lane), (unsigned)(s + 1)};
        }
        __builtin_amdgcn_s_setprio(MMA_P);
        STAMP(11);
        ARRIVE(0);
      } else {
        BAR();
      }
    }
    ARRIVE(1);
    ARRIVE_OUT();
    sv = sv1;
  }
  STAMP_OUT();
  late(NSEG - 2, lane0);
  if (w >= 2) {
    lds_wait(cntp, 4u * (NSEG - 1));
    if (w == 2) { const u32x2 keep = ot.out; ot.run(lane0); ot.store(a, NSEG - 2 - LAG5, b, lane0); ot.out = keep; }
    __builtin_amdgcn_sched_barrier(0);
    PvTask pv;
    const unsigned ck2 = __builtin_amdgcn_readfirstlane(*(const unsigned*)(misc + 11));
    pv.load(rs, ck2, NSEG - 2 - LAG4, gp, w & 1, lane0);
    st_out(NSEG - 1, lane0);
    for (unsigned spins = 0; !pv.ready() && spins < SPIN_CAP; ++spins) { asm volatile("" ::: "memory"); pv.load(rs, ck2, NSEG - 2 - LAG4, gp, w & 1, lane0); }
    pv.run(lds, rs, NSEG - 2 - LAG4, gp, m, w & 1, lane0, true);
    x_fetch_n<F67>(x_src(a, NSEG + 1, b), lds0, NSEG + 1, F4 + F5 + (w - 2) * F67, lane0 * 16);
  }
  BAR();
  if (w != 0) vo_mem(NSEG - 1, (NSEG - 1) % NV, cyc32(), lane0, std::integral_constant<int, 1>{});
}

__device__ __forceinline__ void qkv_tail(const RecArgs& a, char* lds, const int w) {
  const int lane0 = __builtin_amdgcn_workitem_id_x() & 63, b = __builtin_amdgcn_workgroup_id_x();
  const int r = qkv_row(b), cb = (b >> 3) - 15 * (r & 1);
  if (cb >= 15) return;
  const unsigned lds0 = (unsigned)(uintptr_t)(LAS char*)lds;
  unsigned* const stage = (unsigned*)(lds + L_MISC) + 17;
  unsigned* const cnty = stage + 1;
  const f32x4 Z4 = {0.f, 0.f, 0.f, 0.f};
  auto fetch = [&](int i) {
    x_fetch_n<10>(x_src(a, NSEG + i, b), lds0, i, 10 * w, lane0 * 16);
  };
  unsigned want = 0;
  auto sync = [&]() { VM_DRAIN(); want += 4u; lds_inc<L_MISC + 72>(lds0); while (__hip_atomic_load(cnty, __ATOMIC_RELAXED, __HIP_MEMORY_SCOPE_WORKGROUP) < want) { } asm volatile("" ::: "memory"); };
  LOGQ(0);
  bf16x8 W[2][20];
  const __amdgpu_buffer_rsrc_t rw = __builtin_amdgcn_make_buffer_rsrc(a.ws + OFF_WQKVBT + (size_t)(cb * 8 + 2 * w) * 20480, 0, 40960, 0x00020000);
  auto w_get = [&](int ks) {
#pragma unroll
    for (int i = 0; i < 2; ++i) W[i][ks] = as8(__builtin_amdgcn_raw_buffer_load_b128(rw, lane0 * 16 + (ks & 3) * 1024, (i * 20 + (ks & ~3)) * 1024, 0));
    __builtin_amdgcn_sched_barrier(0);
  };
#pragma unroll
  for (int ks = 0; ks < QW; ++ks) w_get(ks);
  bf16x8 ones = mk8(0x3f803f80u, 0x3f803f80u, 0x3f803f80u, 0x3f803f80u);
  PIN_V(ones);
  const __amdgpu_buffer_rsrc_t ro = __builtin_amdgcn_make_buffer_rsrc(a.ws + OFF_QB, 0, (int)(OFF_OB - OFF_QB), 0x00020000);
  const int t0 = w & 1, col0 = (cb % 5) * 128 + w * 32;
  auto body = [&](auto SW, auto SQ) {
    unsigned vo[2][2];
    {
      FRESH_LANE();
#pragma unroll
      for (int i = 0; i < 2; ++i)
#pragma unroll
        for (int tt = 0; tt < 2; ++tt) {
          const int tq = tt ^ t0, tk = tq * 16 + c, cc = col0 + i * 16 + (SW ? 4 * g : c), hd = cc / BD, e = cc - hd * BD;
          const int kv = (int)(OFF_KV - OFF_QB) + hd * 64 * KVT;
          vo[i][tt] = (unsigned)(!SW ? kv + 1280 + tq * 672 + ((g & 1) * 21 + e) * 16 + (g >> 1) * 8
                                     : (cb < 5 ? (hd * S_ + tk) * 2 * QST + e * 2 : kv + (e < 16 ? (tk + 32 * (e >> 3)) * 16 + (e & 7) * 2 : 1024 + tk * 8)));
          PIN_V(vo[i][tt]);
        }
    }
    const float qs = cb < 5 ? 0.22360679774997896f * 1.4426950408889634f : 1.0f;
    f32x4 cs[2] = {Z4, Z4};
    LOGQ(1);
    f32x4 acc[2][2], st;
    auto mma = [&](int it, auto FIRST) {
      const char* xp0 = lds + L_XN + (it & 1) * 40960 + t0 * 20480 + lane0 * 16;
      const char* xp1 = lds + L_XN + (it & 1) * 40960 + (1 - t0) * 20480 + lane0 * 16;
      acc[0][0] = acc[0][1] = acc[1][0] = acc[1][1] = st = Z4;
      bf16x8 x[4][2];
#pragma unroll
      for (int p = 0; p < 3; ++p) { x[p][0] = *(const bf16x8*)(xp0 + p * 1024); x[p][1] = *(const bf16x8*)(xp1 + p * 1024); }
      __builtin_amdgcn_sched_barrier(0);
#pragma unroll
      for (int ks = 0; ks < 20; ++ks) {
        if (FIRST && ks + QW < 20) w_get(ks + QW);
#pragma unroll
        for (int q = 0; q < 4; ++q) {
          const int tt = 1 - (q >> 1), i = 1 - (q & 1);
          acc[i][tt] = SW ? mfma32(W[i][ks], x[ks & 3][tt], acc[i][tt]) : mfma32(x[ks & 3][tt], W[i][ks], acc[i][tt]);
          __builtin_amdgcn_sched_barrier(0);
          if (q < 2 && ks + 3 < 20) x[(ks + 3) & 3][q] = *(const bf16x8*)((q ? xp1 : xp0) + (ks + 3) * 1024);
          __builtin_amdgcn_sched_barrier(0);
        }
        st = mfma32(SQ ? x[ks & 3][0] : ones, x[ks & 3][0], st);
        if (FIRST) {
#pragma unroll
          for (int i = 0; i < 2; ++i) cs[i] = SW ? mfma32(W[i][ks], ones, cs[i]) : mfma32(ones, W[i][ks], cs[i]);
        }
        __builtin_amdgcn_sched_barrier(0);
      }
    };
    mma(0, std::integral_constant<bool, true>{});
#pragma unroll 1
    for (int it = 0; it < 4; ++it) {
      if (it) mma(it, std::integral_constant<bool, false>{});
      LOGQ(2 + 3 * it);
      FRESH_LANE();
      float* const sp = (float*)(lds + L_ST) + (it & 1) * 64;
      if (SQ) { if ((c >> 2) == g) sp[32 + t0 * 16 + c] = (c & 3) == 0 ? st[0] : ((c & 3) == 1 ? st[1] : ((c & 3) == 2 ? st[2] : st[3])); }
      else if (g == 0) sp[t0 * 16 + c] = st[0];
      sync();
      LOGQ(3 + 3 * it);
      if (it == 0) fetch(2);
      const bool late = r + 48 >= 55;
      if (it == 1 && !late) fetch(3);
      const bool now = it == 2 && late && __builtin_amdgcn_readfirstlane(*(volatile unsigned*)stage) != 0u;
      if (now) fetch(3);
      const unsigned so = (unsigned)((r + 16 * it) * (cb < 5 ? 64 * QST : KVT));
      auto stat = [&](float sm, float sq, float& mean, float& rstd) {
        mean = sm * (1.0f / DM);
        rstd = __builtin_amdgcn_rsqf(__builtin_fmaxf(sq * (1.0f / DM) - mean * mean, 0.f) + 1e-5f);
      };
#pragma unroll
      for (int tt = 0; tt < 2; ++tt) {
        const int tq = tt ^ t0;
        float mean[4], rstd[4];
        if (SW) {
          stat(sp[tq * 16 + c], sp[32 + tq * 16 + c], mean[0], rstd[0]);
          rstd[0] *= qs;
        } else {
          const f32x4 sm = *(const f32x4*)(sp + tq * 16 + 4 * g), sq = *(const f32x4*)(sp + 32 + tq * 16 + 4 * g);
#pragma unroll
          for (int k = 0; k < 4; ++k) stat(sm[k], sq[k], mean[k], rstd[k]);
        }
#pragma unroll
        for (int i = 0; i < 2; ++i) {
          float v[4];
#pragma unroll
          for (int k = 0; k < 4; ++k) v[k] = (acc[i][tt][k] - mean[SW ? 0 : k] * cs[i][SW ? k : 0]) * rstd[SW ? 0 : k];
          __builtin_amdgcn_raw_buffer_store_b64((u32x2){pack2(v[0], v[1]), pack2(v[2], v[3])}, ro, vo[i][tt], so, 0);
        }
      }
      LOGQ(4 + 3 * it);
      if (it == 2 && late) {
        if (!now) { lds_wait(stage, 1u); fetch(3); }
        sync();
      }
    }
  };
  typedef std::integral_constant<bool, false> F_; typedef std::integral_constant<bool, true> T_;
  if (cb < 10) { if (w >> 1) body(T_{}, T_{}); else body(T_{}, F_{}); }
  else { if (w >> 1) body(F_{}, T_{}); else body(F_{}, F_{}); }
}

struct TokTask {
  u32x4 y[2]; u32x2 ym; u32x4 res;
  unsigned l0, l1, lm, s0, s1;
  static __device__ __forceinline__ bool ok(int sl) { return sl >= 0 && sl < NSEG; }
  __device__ __forceinline__ void init(int h, int j, int lane) {
    const int c = lane & 15, g = lane >> 4, qc = 4 * (j & 3) + (c >> 2);
    l0 = ts_tok(h, g) + (unsigned)((j >> 2) * 1024 + ((c & 3) * 16 + qc) * 16); l1 = l0 + 16384u;
    lm = ts_mem(h, g + 4 * (c & 1)) + (unsigned)((j >> 2) * 256 + (((c >> 1) & 1) * 16 + qc) * 8);
    res = (u32x4){0u, 0u, 0u, 0u};
    const int qi = 4 * j + (c >> 2);
    s0 = g < 2 ? pt_tok(h) + (unsigned)((qi >> 4) * 1024 + ((c & 3) * 16 + (qi & 15)) * 16 + g * 8) : 0x7ffffff0u;
    s1 = g == 0 && (c & 1) == 0 ? pt_mem(h) + (unsigned)((qi >> 4) * 256 + (((c >> 1) & 1) * 16 + (qi & 15)) * 8) : 0x7ffffff0u;
    PIN_V(l0); PIN_V(l1); PIN_V(lm); PIN_V(s0); PIN_V(s1);
  }
  __device__ __forceinline__ void load(const __amdgpu_buffer_rsrc_t rs, unsigned o) {
    y[0] = SC1_LOADS(l0, o); y[1] = SC1_LOADS(l1, o);
    ym = __builtin_amdgcn_raw_buffer_load_b64(rs, lm, o, 16);
  }
  __device__ __forceinline__ bool ready() { return wave_all(umin(umin(min4(y[0]), min4(y[1])), umin(ym[0], ym[1])) != 0u); }
  __device__ __forceinline__ void run(int j, int lane, const bf16x8 SelLo) {
    const int c = lane & 15, g = lane >> 4;
    const f32x4 Z4 = {0.f, 0.f, 0.f, 0.f};
    const f32x4 d1 = mfma32(SelLo, as8(y[1]), mfma32(SelLo, as8(y[0]), Z4));
    const f32x4 d0 = mfma32(SelLo, mk8(ym[0], ym[1], 0u, 0u), Z4);
    const int qi = 4 * j + (c >> 2), k0 = 16 * g + 4 * (c & 3);
    float t[4], mv[4];
    float mx = -INFINITY;
#pragma unroll
    for (int r = 0; r < 4; ++r) {
      t[r] = (g < 2 && k0 + r <= qi) ? d1[r] : -INFINITY;
      const float mt = d0[r] + dppf<DPP_XOR1>(d0[r]);
      mv[r] = (g == 0 && (c & 1) == 0) ? mt : -INFINITY;
      mx = fmx(mx, fmx(t[r], mv[r]));
    }
    mx = fmx(mx, dppf<DPP_XOR1>(mx)); mx = fmx(mx, dppf<DPP_XOR2>(mx));
    mx = x16mx(mx);
    float sum = 0.f;
#pragma unroll
    for (int r = 0; r < 4; ++r) {
      t[r] = __builtin_amdgcn_exp2f(t[r] - mx); mv[r] = __builtin_amdgcn_exp2f(mv[r] - mx);
      sum += t[r] + mv[r];
    }
    sum += dppf<DPP_XOR1>(sum); sum += dppf<DPP_XOR2>(sum);
    sum = x16sum(sum);
    const float rs_ = __builtin_amdgcn_rcpf(sum);
    res = (u32x4){pack2n(t[0] * rs_, t[1] * rs_), pack2n(t[2] * rs_, t[3] * rs_), pack2n(mv[0] * rs_, mv[1] * rs_), pack2n(mv[2] * rs_, mv[3] * rs_)};
    PIN_V(res);
  }
  __device__ __forceinline__ void store(const __amdgpu_buffer_rsrc_t rs, unsigned o, unsigned no = 0u) {
    SC1_STORE8S(((u32x2){res[0], res[1]}), s0 | no, o);
    SC1_STORE8S(((u32x2){res[2], res[3]}), s1 | no, o);
  }
};

__device__ __forceinline__ void t2_mem(const bf16x8 fr, const bf16x8 q0, const bf16x8 q1, const __amdgpu_buffer_rsrc_t rs, unsigned o, int h, int j, float inv, int lane) {
  const f32x4 Z4 = {0.f, 0.f, 0.f, 0.f};
  const f32x4 d0 = mfma32(fr, q0, Z4) * inv, d1 = mfma32(fr, q1, Z4) * inv;
  const unsigned a0 = pack2n(d0[0], d0[1]), a1 = pack2n(d0[2], d0[3]), b0 = pack2n(d1[0], d1[1]), b1 = pack2n(d1[2], d1[3]);
  const auto r0 = __builtin_amdgcn_permlane32_swap(a0, b0, false, false), r1 = __builtin_amdgcn_permlane32_swap(a1, b1, false, false);
  SC1_STORE8S(((u32x2){r0[1], r1[1]}), ts_mem(h, j) + lane * 8, o);
}

template <int w>
__device__ __forceinline__ void rec_waves(const RecArgs& a, char* lds, const __amdgpu_buffer_rsrc_t rs) {
  const int lane = __builtin_amdgcn_workitem_id_x() & 63, b = __builtin_amdgcn_workgroup_id_x(), c = lane & 15, g = lane >> 4, wid = w; (void)wid;
  const int gp = (b & 7) * 2 + (b >> 7), m = (b >> 3) & 15, h0 = 2 * gp, h = h0 + (m >> 3), j = m & 7;
  float* const misc = (float*)(lds + L_MISC);
  const f32x4 Z4 = {0.f, 0.f, 0.f, 0.f};
  LOGP(0);
  __builtin_amdgcn_s_setprio(3);
  const unsigned lds0 = (unsigned)(uintptr_t)(LAS char*)lds;
  if (!a.layer) img_fetch<12>(a, lds0, h, j, w, lane);
  constexpr int NC = w < 2 ? 2 : 3;
  const int far = ((b >> 2) & 1) ^ 1;
  auto chunk = [&](int f) { return f == 0 ? 4 * far + w : (w == 2 && f == 1 ? 8 + far : (w == 3 && f == 2 ? 9 - far : 4 - 4 * far + w)); };
  bf16x16 Wqk[3][NC];
  u32x4 bm0[NC][2];
#pragma unroll
  for (int f = 0; f < 2 * NC; ++f) bm0[f >> 1][f & 1] = *(const u32x4*)(a.mem0 + (size_t)(c & 7) * DM + 64 * chunk(f >> 1) + 16 * g + 8 * (f & 1));
  if (!a.layer) asm volatile("s_waitcnt vmcnt(0)" ::: "memory");
  BAR();
  LOGP(9);
  const unsigned ck = __builtin_amdgcn_readfirstlane(*(const unsigned*)(misc + 11));
  const unsigned flip = (ck ^ (unsigned)(b << 11)) & 8192u;
#pragma unroll
  for (int t = 0; t < 3; ++t)
#pragma unroll
    for (int f = 0; f < NC; ++f) Wqk[t][f] = cat16(wqk_frag(lds, t, 2 * chunk(f), lane), wqk_frag(lds, t, 2 * chunk(f) + 1, lane));
  if (!a.layer) lds_arrive((unsigned*)(misc + 6), lane);
  else {
    if (w == 0) {
      for (unsigned spins = 0; spins < SPIN_CAP; ++spins) {
        if (__builtin_amdgcn_raw_buffer_load_b32(rs, (b & 7) * 256, (unsigned)OFF_RDY, 16) >= 16u) break;
        __builtin_amdgcn_s_sleep(4);
        asm volatile("" ::: "memory");
      }
      lds_inc<L_MISC + 84>(lds0);
    }
    lds_wait((unsigned*)(misc + 21), 2u);
#pragma unroll
    for (int f = 0; f < 2 * NC; ++f) PIN_V(bm0[f >> 1][f & 1]);
#pragma unroll
    for (int i = 0; i < 10; ++i) x_fetch(x_src(a, 0, b), lds0, 0, 10 * w + i, lane);
  }
  const bf16x8 SelLo = sel_frag(c, 0), SelHi = sel_frag(c, 8);
  bf16x8 ONES = mk8(0x3f803f80u, 0x3f803f80u, 0x3f803f80u, 0x3f803f80u);
  if (w < 2) PIN_V(ONES);
  bool dead = false;
  float inv = 1.0f;
  unsigned* const cnt2 = (unsigned*)(misc + 7);
  unsigned l2off[2 * NC];
#pragma unroll
  for (int f = 0; f < 2 * NC; ++f) { l2off[f] = acc_off(16 * chunk(f >> 1) + 4 * g + 2 * (f & 1) + (c >> 3), c & 7) ^ flip; PIN_V(l2off[f]); }
  int sidx = (c >> 3) ? 0xEEEE : 0x4444;
  PIN_V(sidx);
  const unsigned p1off = (unsigned)((w == 0 ? (h & 2) * 1024 + j * 256 + (h & 1) * 64 + (g & 1) * 8 : (h & 3) * 32768 + j * 512 + (g >> 1) * 256 + (g & 1) * 64) + (c & 4) * 32 + (c & 3) * 16);
  unsigned p1v = (c < 8) && (w != 0 || g >= 2) ? p1off : 0x7ffffff0u, p1cur = 0x7ffffff0u;
  PIN_V(p1v); PIN_V(p1cur);
  const int hd = (c >> 2) & 1, mr = hd ? 3 - (c & 3) : (c & 3);
  const unsigned l1off = (unsigned)(((h0 + hd) & 3) * 32768 + g * 512 + (w & 1) * 128 + (c >> 3) * 64 + mr * 16);
  const unsigned l1offm = (unsigned)((h0 & 2) * 1024 + (g + 4 * (c >> 3)) * 256 + (w & 1) * 128 + hd * 64 + mr * 16);
  const int plane = (lane & ~6) | ((lane & 2) << 1) | ((lane & 4) >> 1);
  unsigned acco = c < 8 ? (acc_off(64 * far + m * 4 + g, 4 * (w & 1) + mr) + (unsigned)(hd * 8)) ^ flip : 0x7ffffff0u;
  unsigned acco2 = c >= 8 ? (acc_off(64 - 64 * far + m * 4 + g, 4 * (w & 1) + mr) + (unsigned)(hd * 8)) ^ flip
                          : (c < 4 ? (acc_off(128 + m * 2 + (g & 1), 4 * (w & 1) + mr) + (unsigned)((g >> 1) * 8)) ^ flip : 0x7ffffff0u);
  if (w < 2) { PIN_V(acco); PIN_V(acco2); }
  unsigned bmo[NC];
#pragma unroll
  for (int f = 0; f < NC; ++f) { bmo[f] = (unsigned)(chunk(f) * 1024 + lane * 16); PIN_V(bmo[f]); }
  bf16x8 bm[NC];
  auto bm_put = [&](int f) { *(bf16x8*)(lds + L_BM + bmo[f]) = bm[f]; __builtin_amdgcn_sched_barrier(0); };
  f32x4 acc[3] = {Z4, Z4, Z4};
  const int rt2 = ((c >> 3) * 8 + 4 * (g & 1)) * 28 + 16 + (c & 7);
  unsigned redo[3] = {(unsigned)(4 * g * 28 + c), (unsigned)(g < 2 ? (4 * g + 2) * 28 + c : rt2), (unsigned)(g < 2 ? (4 * g + 10) * 28 + c : rt2 + 56)};
#pragma unroll
  for (int k = 0; k < 3; ++k) { redo[k] = L_RED1 + w * 1792 + redo[k] * 4; PIN_V(redo[k]); }
  auto red_put = [&]() {
    auto hs = [&](float x, float y) { const auto sw = __builtin_amdgcn_permlane32_swap(f2u(x), f2u(y), false, false); return u2f(sw[0]) + u2f(sw[1]); };
#pragma unroll
    for (int k = 0; k < 3; ++k) {
#pragma unroll
      for (int i = 0; i < 2; ++i) *(float*)(lds + redo[k] + i * 112) = k == 0 ? hs(acc[0][i], acc[1][i]) : hs(acc[k - 1][2 + i], acc[2][2 * k - 2 + i]);
      __builtin_amdgcn_sched_barrier(0);
    }
  };
#pragma unroll
  for (int f = 0; f < NC; ++f) {
    const u32x4 u = bm0[f][0], v = bm0[f][1];
    bm[f] = c < 8 ? mk8(u[0], u[2], v[0], v[2]) : mk8(u[1], u[3], v[1], v[3]);
    bm_put(f);
#pragma unroll
    for (int t = 0; t < 3; ++t) acc[t] = smfma64(bm[f], Wqk[t][f], acc[t], sidx);
  }
  red_put();
  VM_DRAIN();
  if (a.layer) lds_arrive((unsigned*)(misc + 6), lane);
  lds_wait((unsigned*)(misc + 6), 8u);
#pragma unroll
  for (int i = 0; i < 10; ++i) x_fetch(x_src(a, 1, b), lds0, 1, 10 * w + i, lane);
  LOGP(1);
  BAR();
  STAMP_DECL(); ARRIVE_DECL();
  bf16x8 fr, qx[2];
  auto kq_get = [&](int seg) {
    const char* p = lds + (w == 1 ? L_KX : L_QX) + (seg % NKQ) * 2048 + lane * 16;
    if (w == 1 || w == 2 || (w == 3 && seg == NSEG - 1)) { qx[0] = *(const bf16x8*)p; qx[1] = *(const bf16x8*)(p + 1024); }
  };
  auto pub1 = [&](unsigned off, unsigned o1) {
    fr = qk_frag(lds, g, c);
    const f32x4 sc = mfma32(w == 0 ? fr : qx[0], fr, Z4);
    if (w == 0) SC1_STORE8S(((u32x2){pack2n(sc[0], sc[1]), pack2n(sc[2], sc[3])}), off, o1);
    else {
      const f32x4 s2 = mfma32(qx[1], fr, Z4);
      __builtin_amdgcn_raw_buffer_store_b128((u32x4){pack2n(sc[0], sc[1]), pack2n(sc[2], sc[3]), pack2n(s2[0], s2[1]), pack2n(s2[2], s2[3])}, rs, off, o1, 16);
    }
  };
  VM_DRAIN();
  kq_get(0);
  unsigned o1 = sct_slot(ck, 0u), o1m = scm_slot(ck, 0u);
  unsigned t2o = ts_tok(h, j) + (unsigned)(lane * 16);
  TokScores t2;
  if (w == 3) { PIN_V(t2o); t2.get(lds, 0, lane); t2.run(0); t2.run(1); t2.store(rs, t2o, ts_slot(ck, 0)); }
  if (w < 2) pub1(p1v, w == 0 ? o1m : o1);
  const unsigned tst = now32() | 1u;
  if (w == 3 && ST_X) __builtin_amdgcn_raw_buffer_store_b32(tst, rs, lane == 0 ? (b >> 2) * 256 + (b & 3) * 4 : 0x7ffffff0, (unsigned)(OFF_ACC + (SR - 1) * ACC_STEP) + ACC_TS, 16);
  LOGP(2);
  BAR();
  LOGP(3);
#pragma unroll
  for (int i = 0; i < 10; ++i) x_fetch(x_src(a, 2, b), lds0, 2, 10 * w + i, lane);
  VM_DRAIN();
  unsigned o2 = (unsigned)OFF_ACC, o2p = 0u, tw = 0u;
  asm volatile("" : "+s"(o2p));
  unsigned tc0 = 0u, tstar = 0u;
  int dmin = 0, dmax = 0;
  int e1 = 100000;
  unsigned offs = 100u;
  TokTask tk;
  OutTask ot;
  PvTask pv;
  PvTask2 pv2;
  if (w == 3) pv2.init(gp, lane);
  if (w == 2) tk.init(h, j, lane);
  unsigned o4 = 0u;
  unsigned o3, o3p;
  u32x4 pmz = {0u, 0u, 0u, 0u}, vom[5];
  PIN_V(pmz);
#pragma unroll
  for (int k = 0; k < 5; ++k) { vom[k] = pmz; if (w < 2) PIN_V(vom[k]); }
  unsigned tso = lane == 0 ? ACC_TS + (unsigned)((b >> 2) * 256 + (b & 3) * 4) : 0x7ffffff0u;
  if (w < 2) PIN_V(tso);
  float minf = g >= 2 ? -INFINITY : 0.f;
  if (w < 2) PIN_V(minf);
  int sv = NV - 1;
  bf16x8 vox[5];
  unsigned vma[3], need2;
  u32x4 slo, shi;
  float k1, va0, va1, vb0, vb1;
  const u16* xsrc;
  float invv = 1.0f;
  auto prep0 = [&](int s) {
    xsrc = x_src(a, s + 3, b);
    PIN_S(xsrc);
    if (w == 2) { o3p = ts_slot(ck, (s - LAG3) & 3); o3 = ts_slot(ck, s & 3); o4 = pt_slot(ck, imax(s - 1 - LAG3, 0)); PIN_S(o3p); kq_get(s); }
    if (w == 3) { o3 = pt_slot(ck, (s - 1 - LAG4) & 3); o4 = ts_slot(ck, s); t2.get(lds, s, lane); }
    PIN_S(o3); PIN_S(o4);
  };
  auto prep = [&](int s) {
    invv = misc[4 + (s & 1)];
    o2 = (unsigned)OFF_ACC + (unsigned)(s % SR) * (unsigned)ACC_STEP;
    sv = sv == NV - 1 ? 0 : sv + 1;
    PIN_S(o2); PIN_S(sv);
    if (w >= 2) return;
    kq_get(s);
    o1 = sct_slot(ck, s % SR); o1m = scm_slot(ck, s % SR);
    need2 = 5u * (unsigned)(s + 1) + 2u * ((unsigned)(s + 2) / NSEG);
    PIN_S(o1); PIN_S(o1m); PIN_S(need2);
    inv = u2f(__builtin_amdgcn_readfirstlane(f2u(invv)));
  };
  auto prep2 = [&]() {
#pragma unroll
    for (int k = 0; k < 3; ++k) {
      const int t = k == 0 ? 2 : (k == 1 ? far : 1 - far);
      const char* p = lds + L_VOX + sv * 5120 + t * 1024 + (plane & 47) * 16 + (g & 1) * 8;
      auto rd = [&](const char* q) { const u32x2 a0 = *(const u32x2*)q, a1 = *(const u32x2*)(q + 256); return mk8(a0[0], a0[1], a1[0], a1[1]); };
      vox[k == 0 ? 0 : 2 * k - 1] = rd(p);
      if (k) vox[2 * k] = rd(p + 3072);
      vma[k] = (unsigned)(L_VOM + sv * 1280 + t * 256 + (plane & 31) * 8);
      PIN_V(vma[k]);
    }
    slo = __builtin_bit_cast(u32x4, SelLo); shi = __builtin_bit_cast(u32x4, SelHi);
#pragma unroll
    for (int i = 0; i < 4; ++i) { slo[i] = ((slo[i] >> 7) & 0x10001u) * (f2u(inv) >> 16); shi[i] = ((shi[i] >> 7) & 0x10001u) * (f2u(inv) >> 16); }
    k1 = KEXP * inv;
    va0 = 32.0f; va1 = 0.f; vb0 = 32.0f; vb1 = 0.f;
    PIN_V(k1); PIN_V(slo); PIN_V(shi); PIN_V(va0); PIN_V(va1); PIN_V(vb0); PIN_V(vb1);
    __builtin_amdgcn_sched_barrier(0);
  };
  auto rows = [&](int s) {
    const int dl = spin_until(tw);
    if (w < 2) tc0 = cyc32();
    __builtin_amdgcn_sched_barrier(0);
    STAMP(1);
    unsigned spins = 0;
    auto pass = [&](bool first) {
      u32x4 x[2 * NC];
#pragma unroll
      for (int f = 0; f < NC; ++f) {
        if (f && w == 3 && first) t2.run(f - 1);
        else if (f) asm volatile(".rept %0\n\ts_nop 3\n\t.endr" :: "n"(w < 2 ? P2_G0 : (f == 1 ? P2_G2 : P2_G3)));
        __builtin_amdgcn_sched_barrier(0);
        x[2 * f] = SC1_LOADS(l2off[2 * f], o2p); x[2 * f + 1] = SC1_LOADS(l2off[2 * f + 1], o2p);
        __builtin_amdgcn_sched_barrier(0);
      }
      if (first) lds_inc<L_MISC + 92>(lds0);
      if (first) prep(s);
      if (w >= 2 && first && P2_W) lds_spin<L_MISC + 92, 0>(lds0, 4u * (unsigned)s);
      if (w == 2 && first) tk.store(rs, o4, s > LAG3 ? 0u : 0x7ff00000u);
      if (w == 3 && first) t2.store(rs, t2o, o4);
      if (first && w < 2) {
        p1cur = p1v; offs = (offs - (unsigned)dl) & ~3u;
        PIN_V(p1cur); PIN_S(offs);
        tstar = tc0 + (unsigned)(e1 + D1_M + (w == 1 ? D1_W1 : 0)) - offs * 20u;
        dmin = offs < 40u ? D1_MIN : REC_D1;
        dmin = s < EARLY_N ? REC_D1 + EARLY_D1 : dmin;
        dmax = REC_D1;
        PIN_S(tstar); PIN_S(dmin); PIN_S(dmax);
      }
      __builtin_amdgcn_sched_barrier(0);
      unsigned ok = 32u;
#pragma unroll
      for (int t = 0; t < 3; ++t) { acc[t] = Z4; PIN_V(acc[t]); }
      __builtin_amdgcn_sched_barrier(0);
      bool r = true;
#pragma unroll
      for (int f = 0; f < NC; ++f) {
        unsigned p[4];
#pragma unroll
        for (int k = 0; k < 2; ++k) {
          const u32x4 v = x[2 * f + k];
          p[2 * k] = pack2((float)(int)v[0], (float)(int)v[1]); p[2 * k + 1] = pack2((float)(int)v[2], (float)(int)v[3]);
          asm volatile("" : "+v"(p[2 * k]), "+v"(p[2 * k + 1]));
          __builtin_amdgcn_sched_barrier(0);
        }
        ok = (ok & x[2 * f][0] & x[2 * f][2]) & (x[2 * f + 1][0] & x[2 * f + 1][2]);
        PIN_V(ok);
        __builtin_amdgcn_sched_barrier(0);
        bm[f] = mk8(p[0], p[1], p[2], p[3]);
        acc[0] = smfma64(bm[f], Wqk[0][f], acc[0], sidx);
        __builtin_amdgcn_sched_barrier(0);
        bm_put(f);
        acc[1] = smfma64(bm[f], Wqk[1][f], acc[1], sidx);
        __builtin_amdgcn_sched_barrier(0);
        if (f == NC - 1) { u64 bad = __builtin_amdgcn_ballot_w64(ok == 0u); PIN_S(bad); r = bad == 0ull; }
        __builtin_amdgcn_sched_barrier(0);
        acc[2] = smfma64(bm[f], Wqk[2][f], acc[2], sidx);
        __builtin_amdgcn_sched_barrier(0);
      }
      red_put();
      return r;
    };
    if (__builtin_expect(!pass(true), 0)) {
      FC(0);
      do {
        if (dead || ++spins > SPIN_CAP) { dead = true; break; }
        asm volatile("" ::: "memory");
      } while (!pass(false));
    }
    STAMPV(12, spins); LOGF(1, spins);
    TSTAMP(2, now32()); TSTAMP(3, spins);
  };
  if (w >= 2) prep0(0);
  prep(0);
  CODE_PAD((PADR >> (4 * w)) & 15);
#pragma unroll 1
  for (int s = 0; s < NSEG - 1; ++s) {
    if (__builtin_expect(o2p != 0u, 1)) rows(s);
    STAMP(2);
    BAR();
    STAMP(3);
    if (w < 2) pub1(p1cur, w == 0 ? o1m : o1);
    if (w == 2) tk.load(rs, o3p);
    if (w == 3) pv2.load(rs, o3);
    const unsigned tp1 = cyc32();
    __builtin_amdgcn_sched_barrier(0);
    if (w == 2) {
      __builtin_amdgcn_s_setprio(W2_P);
      spin_cyc(tp1 + W2_D);
      fr = qk_frag(lds, g, c);
      t2_mem(fr, qx[0], qx[1], rs, o3, h, j, invv * KEXP, lane);
      __builtin_amdgcn_sched_barrier(0);
      asm volatile("s_waitcnt vmcnt(1)" ::: "memory");
      lds_inc<L_MISC + 28>(lds0);
    }
    if (w >= 2 && __builtin_expect(s == NSEG - 2, 0)) { VM_DRAIN(); lds_inc<L_MISC + 28>(lds0); }
    TSTAMP(0, now32());
    STAMP(4);
    __builtin_amdgcn_sched_barrier(0);
    unsigned tpub = 0;
    u32x2 sw;
    if (w == 2) {
      STAMP(5);
      __builtin_amdgcn_sched_barrier(0);
      STAMP(6);
      ARRIVE(0);
      tk.run(j, lane, SelLo);
      if (F2B) { lds_spin<L_MISC + 48, 1>(lds0, 2u * (unsigned)(s + 1)); x_fetch_n<F2B>(xsrc, lds0, s + 3, P2B, lane * 16); }
      __builtin_amdgcn_s_setprio(SP_P);
    }
    if (w == 3) {
      if (F3A) { spin_cyc(tp1 + F3A_D); x_fetch_n<F3A>(xsrc, lds0, s + 3, 40 - F3C - F3 - F3A, lane * 16); }
      __builtin_amdgcn_s_setprio(T4_P);
      if (!F3A || T4_D > F3A_D) spin_cyc(tp1 + T4_D);
      pv2.run(lds, sv < 1 + LAG4 ? sv + NV - 1 - LAG4 : sv - 1 - LAG4, lane);
      STAMP(11);
      __builtin_amdgcn_s_setprio(SP_P);
      STAMP(5);
      ARRIVE(0);
      if (F3) { lds_spin<L_MISC + 48, 1>(lds0, 2u * (unsigned)(s + 1)); x_fetch_n<F3>(xsrc, lds0, s + 3, 40 - F3C - F3, lane * 16); }
      STAMP(6);
    }
    if (w < 2) {
      prep2();
      int dq = imin((int)(tstar - tp1), dmax);
      dq = dq < -D1_LATE ? dmax : dq;
      PIN_S(dq);
      spin_cyc(tp1 + (unsigned)imax(dq, dmin));
      asm volatile("" : "+v"(vox[0]), "+v"(vox[1]), "+v"(vox[2]), "+v"(vox[3]), "+v"(vox[4]));
      STAMP(5);
      u32x4 y[5];
      y[4] = SC1_LOADS(l1offm, o1m);
#pragma unroll
      for (int k = 0; k < 4; ++k) { __builtin_amdgcn_sched_barrier(0); y[k] = SC1_LOADS(l1off + (k & 1) * 2048 + (k >> 1) * 256, o1); }
      __builtin_amdgcn_sched_barrier(0);
      lds_inc<L_MISC + 48>(lds0);
      {
        const int de = imin((int)(tp1 - tc0) - e1, D1_C);
        e1 += s == 0 ? 0 : de >> (s < EARLY_N && de < -D1_C ? 0 : D1_G);
        PIN_S(e1);
      }
      STAMP(6);
      ARRIVE(0);
      f32x2 st;
      auto vom_get = [&](int k) {
        const u32x2 v = *(const u32x2*)(lds + vma[(k + 1) >> 1] + (k && !(k & 1) ? 768 : 0));
        vom[k][0] = v[0]; vom[k][1] = v[1];
        __builtin_amdgcn_sched_barrier(0);
      };
      lds_spin<L_MISC + 28, 0>(lds0, need2);
#pragma unroll
      for (int k = 0; k < 5; ++k) vom_get(k);
      st = *(const f32x2*)(misc + 8);
      STAMP(11);
      __builtin_amdgcn_sched_barrier(0);
      {
        unsigned spins = 0;
        const bool lo = c < 8;
        f32x4 d1, o[5], sm;
        float m0, m1, chk;
        unsigned mn;
        auto pv_mem = [&](float a0, float a1) {
          const unsigned mm = pack2(__builtin_amdgcn_exp2f(a0), __builtin_amdgcn_exp2f(a1));
          pmz[0] = mm; pmz[1] = dpp_ror8(mm);
          PIN_V(pmz);
          return __builtin_bit_cast(bf16x8, pmz);
        };
        auto sum8 = [&]() {
          const f32x4 d0 = mfma32(as8(slo), as8(y[4]), Z4);
          mn = umin(umin(y[4][0], y[4][1]), y[4][2]);
          PIN_V(mn);
          __builtin_amdgcn_sched_barrier(0);
          d1 = mfma32(as8(slo), as8(y[0]), Z4);
          mn = umin(umin(umin(umin(mn, y[4][3]), y[0][0]), y[0][1]), y[0][2]);
          PIN_V(mn);
          __builtin_amdgcn_sched_barrier(0);
          d1 = mfma32(as8(slo), as8(y[1]), d1);
          __builtin_amdgcn_sched_barrier(0);
          m0 = (lo ? d0[0] : d0[2]) + dppf<DPP_ROR8>(lo ? d0[2] : d0[0]);
          m1 = (lo ? d0[1] : d0[3]) + dppf<DPP_ROR8>(lo ? d0[3] : d0[1]);
          m0 = __builtin_fmaf(m0, k1, minf); m1 = __builtin_fmaf(m1, k1, minf);
          PIN_V(m0); PIN_V(m1);
          __builtin_amdgcn_sched_barrier(0);
          d1 = mfma32(as8(shi), as8(y[2]), d1);
          __builtin_amdgcn_sched_barrier(0);
          float e0 = __builtin_amdgcn_exp2f(m0), e1 = __builtin_amdgcn_exp2f(m1);
          PIN_V(e0); PIN_V(e1);
          __builtin_amdgcn_sched_barrier(0);
          d1 = mfma32(as8(shi), as8(y[3]), d1);
          __builtin_amdgcn_sched_barrier(0);
          pmz[0] = pack2(e0, e1);
          PIN_V(pmz);
          mn = umin(umin(umin(umin(mn, y[0][3]), y[1][0]), y[1][1]), y[1][2]);
          PIN_V(mn);
          __builtin_amdgcn_sched_barrier(0);
          pmz[1] = dpp_ror8(pmz[0]);
          PIN_V(pmz);
          __builtin_amdgcn_sched_barrier(0);
          mn = umin(umin(umin(umin(mn, y[1][3]), y[2][0]), y[2][1]), y[2][2]);
          PIN_V(mn);
          asm volatile("" : "+v"(vom[0]), "+v"(vom[1]), "+v"(vom[2]), "+v"(vom[3]), "+v"(vom[4]), "+v"(st));
          __builtin_amdgcn_sched_barrier(0);
          const bf16x8 pm = __builtin_bit_cast(bf16x8, pmz);
          sm = mfma32(ONES, pm, Z4);
          __builtin_amdgcn_sched_barrier(0);
          return pm;
        };
        auto p_tok = [&](const f32x4 a, const bf16x8 pm) {
          float e[4];
#pragma unroll
          for (int i = 0; i < 4; ++i) {
            e[i] = __builtin_amdgcn_exp2f(a[i]);
            __builtin_amdgcn_sched_barrier(0);
            if (i == 1 || i == 3) o[1 + (i >> 1)] = mfma32(as8(vom[1 + (i >> 1)]), pm, Z4);
            __builtin_amdgcn_sched_barrier(0);
          }
          const unsigned t01 = pack2(e[0], e[1]), t23 = pack2(e[2], e[3]);
          __builtin_amdgcn_sched_barrier(0);
          o[3] = mfma32(as8(vom[3]), pm, Z4);
          __builtin_amdgcn_sched_barrier(0);
          const bf16x8 r = mk8(t01, t23, dpp_ror8(t01), dpp_ror8(t23));
          __builtin_amdgcn_sched_barrier(0);
          o[4] = mfma32(as8(vom[4]), pm, Z4);
          o[0] = mfma32(as8(vom[0]), pm, Z4);
          __builtin_amdgcn_sched_barrier(0);
          return r;
        };
        auto chk_a = [&]() { chk = fmx(fmx(__builtin_fabsf(d1[0]), __builtin_fabsf(d1[1])), __builtin_fabsf(d1[2])); };
        auto chk_b = [&]() { chk = fmx(fmx(chk, __builtin_fabsf(d1[3])), g < 2 ? fmx(__builtin_fabsf(m0), __builtin_fabsf(m1)) : 0.f); };
        bf16x8 pm = sum8();
        bf16x8 pt = p_tok(d1, pm);
        sm = mfma32(ONES, pt, sm);
        __builtin_amdgcn_sched_barrier(0);
        mn = umin(umin(umin(umin(mn, y[2][3]), y[3][0]), y[3][1]), y[3][2]);
        __builtin_amdgcn_sched_barrier(0);
        o[1] = mfma32(vox[1], pt, o[1]);
        __builtin_amdgcn_sched_barrier(0);
        mn = umin(mn, y[3][3]);
        __builtin_amdgcn_sched_barrier(0);
        o[2] = mfma32(vox[2], pt, o[2]);
        __builtin_amdgcn_sched_barrier(0);
        u64 bad = __builtin_amdgcn_ballot_w64(mn == 0u);
        chk_a();
        __builtin_amdgcn_sched_barrier(0);
        float rs_ = __builtin_amdgcn_rcpf(sm[0]);
        __builtin_amdgcn_sched_barrier(0);
        o[3] = mfma32(vox[3], pt, o[3]);
        __builtin_amdgcn_sched_barrier(0);
        rs_ *= st[0];
        chk_b();
        __builtin_amdgcn_sched_barrier(0);
        o[4] = mfma32(vox[4], pt, o[4]);
        __builtin_amdgcn_sched_barrier(0);
        float rm = dppf<0x141>(rs_);
        bad |= __builtin_amdgcn_ballot_w64(!(chk <= st[1]));
        __builtin_amdgcn_sched_barrier(0);
        o[0] = mfma32(vox[0], pt, o[0]);
        __builtin_amdgcn_sched_barrier(0);
        const bool good = bad == 0u;
        if (__builtin_expect(!good, 0)) {
          mn = umin(umin(umin(min4(y[0]), min4(y[1])), umin(min4(y[2]), min4(y[3]))), min4(y[4]));
          if (!wave_all(mn != 0u) && !dead) {
            FC(1);
            do {
              if (++spins > SPIN_CAP) { dead = true; break; }
              asm volatile("" ::: "memory");
#pragma unroll
              for (int k = 0; k < 4; ++k) y[k] = SC1_LOADS(l1off + (k & 1) * 2048 + (k >> 1) * 256, o1);
              y[4] = SC1_LOADS(l1offm, o1m);
              mn = umin(umin(umin(min4(y[0]), min4(y[1])), umin(min4(y[2]), min4(y[3]))), min4(y[4]));
            } while (!wave_all(mn != 0u));
          } else FC(2);
          pm = sum8();
          chk_a(); chk_b();
          float mx = 0.f;
          if (!wave_all(chk <= st[1])) {
            mx = fmx(fmx(fmx(d1[0], d1[1]), d1[2]), fmx(fmx(d1[3], m0), m1));
            mx = fmx(mx, dppf<DPP_ROR8>(mx));
            mx = x32mx(x16mx(mx));
            pm = pv_mem(m0 - mx, m1 - mx);
            sm = mfma32(ONES, pm, Z4);
          }
          pt = p_tok(d1 - mx, pm);
          sm = mfma32(ONES, pt, sm);
#pragma unroll
          for (int k = 0; k < 5; ++k) o[k] = mfma32(vox[k], pt, o[k]);
          __builtin_amdgcn_sched_barrier(0);
#pragma unroll
          for (int k = 0; k < 12; ++k) __builtin_amdgcn_s_setprio(3);
          __builtin_amdgcn_sched_barrier(0);
          rs_ = __builtin_amdgcn_rcpf(sm[0]) * st[0];
          rm = dppf<0x141>(rs_);
          PIN_V(rs_); PIN_V(rm);
          __builtin_amdgcn_sched_barrier(0);
        }
        STAMP(7); STAMPV(13, spins); LOGF(0, spins);
        {
          STAMP(0);
          __builtin_amdgcn_sched_barrier(0);
#define FM(d, x, r, ctrl) "v_fmac_f32_dpp " d ", " x ", " r " " ctrl "\n\t"
#define ID_ "quad_perm:[0,1,2,3] row_mask:0xf bank_mask:"
#define HM_ "row_half_mirror row_mask:0xf bank_mask:"
#define R8_ "row_ror:8 row_mask:0xf bank_mask:"
#define MI_ "row_mirror row_mask:0xf bank_mask:"
          auto add = [&](float v0, float v1, unsigned off) {
            const u32x2 v = {((unsigned)(int)v0 & ~63u) | 2u, (unsigned)(int)v1};
            asm volatile("buffer_atomic_add_x2 %0, %1, %2, %3 offen" :: "v"(v), "v"(off), "s"(rs), "s"(o2) : "memory");
            __builtin_amdgcn_sched_barrier(0);
          };
          asm volatile(FM("%0", "%4", "%2", ID_ "0x1") FM("%1", "%5", "%2", ID_ "0x1") FM("%0", "%6", "%3", HM_ "0x2") FM("%1", "%7", "%3", HM_ "0x2")
                       FM("%0", "%8", "%3", HM_ "0x1") FM("%1", "%9", "%3", HM_ "0x1") FM("%0", "%10", "%2", ID_ "0x2") FM("%1", "%11", "%2", ID_ "0x2")
                       : "+v"(va0), "+v"(va1) : "v"(rs_), "v"(rm), "v"(o[1][0]), "v"(o[1][1]), "v"(o[1][2]), "v"(o[1][3]), "v"(o[2][0]), "v"(o[2][1]), "v"(o[2][2]), "v"(o[2][3]));
          __builtin_amdgcn_sched_barrier(0);
          add(va0, va1, acco);
          sw = __builtin_bit_cast(u32x2, __hip_atomic_load((u64*)(misc + 24), __ATOMIC_RELAXED, __HIP_MEMORY_SCOPE_WORKGROUP));
          __builtin_amdgcn_sched_barrier(0);
          const auto z0 = __builtin_amdgcn_permlane32_swap(f2u(o[0][0]), f2u(o[0][2]), false, false), z1 = __builtin_amdgcn_permlane32_swap(f2u(o[0][1]), f2u(o[0][3]), false, false);
          asm volatile(FM("%0", "%4", "%2", R8_ "0x4") FM("%1", "%5", "%2", R8_ "0x4") FM("%0", "%6", "%3", MI_ "0x8") FM("%1", "%7", "%3", MI_ "0x8")
                       FM("%0", "%8", "%3", MI_ "0x4") FM("%1", "%9", "%3", MI_ "0x4") FM("%0", "%10", "%2", R8_ "0x8") FM("%1", "%11", "%2", R8_ "0x8")
                       FM("%0", "%12", "%2", ID_ "0x1") FM("%1", "%13", "%2", ID_ "0x1") FM("%0", "%14", "%3", HM_ "0x1") FM("%1", "%15", "%3", HM_ "0x1")
                       : "+v"(vb0), "+v"(vb1) : "v"(rs_), "v"(rm), "v"(o[3][0]), "v"(o[3][1]), "v"(o[3][2]), "v"(o[3][3]), "v"(o[4][0]), "v"(o[4][1]), "v"(o[4][2]), "v"(o[4][3]),
                         "v"(z0[0]), "v"(z1[0]), "v"(z0[1]), "v"(z1[1]));
          __builtin_amdgcn_sched_barrier(0);
          add(vb0, vb1, acco2);
          tpub = now32();
          lds_inc<L_MISC + 104 + 4 * (w & 1)>(lds0);
          STAMP(10);
          asm volatile("buffer_atomic_umax %0, %1, %2, %3 offen" :: "v"(tpub | 1u), "v"(tso), "s"(rs), "s"(o2) : "memory");
        }
      }
    }
    STAMP(8);
    ARRIVE(1);
    o2p = o2;
    PIN_S(o2p);
    if (w < 2) {
      if (__builtin_expect(__builtin_amdgcn_readfirstlane(sw[1]) <= (unsigned)s, 0)) {
        lds_spin<L_MISC + 100, 0>(lds0, (unsigned)s + 1u);
        sw[0] = __hip_atomic_load((unsigned*)(misc + 24), __ATOMIC_RELAXED, __HIP_MEMORY_SCOPE_WORKGROUP);
      }
    } else {
      u32x4 q;
      prep0(s + 1);
      asm volatile("1:\n\tds_read_b128 %0, %1 offset:%3\n\t.if %4\n\ts_sleep 1\n\t.endif\n\ts_waitcnt lgkmcnt(0)\n\tv_min3_u32 v201, v201, v202, v203\n\tv_cmp_ge_u32 vcc, %2, v201\n\ts_cbranch_vccnz 1b"
                   : "=&{v[200:203]}"(q) : "v"(lds0), "s"(s), "n"(L_MISC + 96), "n"(SP_SL) : "vcc", "memory");
      __builtin_amdgcn_s_setprio(3);
      tpub = now32();
      sw[0] = q[0];
    }
    STAMP(9);
    static_assert(F3C == 0 && F2C == 0 && F0C == 0, "nothing behind the atomics");
    LOGT(0, tpub); LOGT(1, tb2_);
    TSTAMP(1, tpub);
    ARRIVE_OUT();
    const unsigned an = __builtin_amdgcn_readfirstlane(sw[0]);
    const int ahead = (int)(an - tpub);
    tw = tpub + (unsigned)(an == 0u || ahead > 1000 ? SCH_FIRST : imax(ahead, w < 2 ? SCH_DMIN : 0));
    offs = tw - an;
  }
  rows(NSEG - 1);
  STAMP_OUT();
  LOGP(4);
  static_assert(LAG3 == 2 && LAG4 == 3 && LAG5 == 6, "drain schedule");
  unsigned* const cntx = (unsigned*)(misc + 16);
  auto poll = [&](auto load, auto ready) {
    load();
    for (unsigned spins = 0; !ready() && !dead;) {
      if (++spins > SPIN_CAP) dead = true;
      asm volatile("" ::: "memory");
      load();
    }
  };
  auto mark = [&](int k) { if (w == 0) *(volatile unsigned*)(misc + k) = cyc32() | 1u; };
  auto after = [&](int k, unsigned d) { lds_wait((unsigned*)(misc + k), 1u); wait_cyc(__builtin_amdgcn_readfirstlane(*(volatile unsigned*)(misc + k)) + d); };
  const int qt = w >> 1;
  auto t4 = [&](int sl) { poll([&]() { pv.load(rs, ck, sl, gp, qt, lane); }, [&]() { return pv.ready(); }); pv.run(lds, rs, sl, gp, m, qt, lane, true); };
  auto t5 = [&](int sl, int part) { poll([&]() { ot.load(a, rs, sl, b, part); }, [&]() { return ot.ready(b); }); ot.run(lane); ot.store(a, sl, b, lane); };
  BAR();
  const unsigned tp1 = cyc32();
  if (w == 2) {
    TokTask tb = tk;
    tb.load(rs, ts_slot(ck, NSEG - 3)); tk.load(rs, ts_slot(ck, NSEG - 2));
    tb.run(j, lane, SelLo); tb.store(rs, pt_slot(ck, NSEG - 3));
  }
  if (w == 3) { kq_get(NSEG - 1); t2_mem(qk_frag(lds, g, c), qx[0], qx[1], rs, ts_slot(ck, NSEG - 1), h, j, invv * KEXP, lane); }
  if (w == 0) tk.init(h, j, lane);
  LOGD(0);
  if (w == 0) {
    ot.init(b, lane);
    wait_cyc(tp1 + END_E1);
    poll([&]() { tk.load(rs, ts_slot(ck, NSEG - 1)); }, [&]() { return tk.ready(); });
    LOGD(1);
  }
  if (!(w & 1)) {
    tk.run(j, lane, SelLo); tk.store(rs, pt_slot(ck, NSEG - 1 - qt));
    mark(14);
    LOGD(2);
    if (w == 2) ot.init(b, lane);
    ot.load(a, rs, NSEG - 4 + w, b, 2);
    lds_wait(cnt2, 5u * NSEG + 1u);
    after(14, 600); wait_cyc(tp1 + END_E2);
    t4(NSEG - 1);
    mark(15);
    LOGD(3);
  } else {
    ot.init(b, lane);
#pragma unroll 1
    for (int i = 0; i < 3; ++i) {
      wait_cyc(tp1 + (i == 0 ? END_E60 : (i == 1 ? END_E61 : END_E62)));
      t4(NSEG - 4 + i);
      if (i == 1 || (i == 2 && w == 1)) t5(i == 1 ? NSEG - 7 + qt : NSEG - 5, 3);
    }
    ot.load(a, rs, NSEG - 4 + w, b, 2);
    LOGP(5);
  }
  after(15, 600); wait_cyc(tp1 + END_E3);
  t5(NSEG - 4 + w, 1);
  LOGD(4);
  VM_DRAIN();
  if (w != 0) lds_inc<L_MISC + 64>(lds0);
  else {
    lds_wait(cntx, 3u);
    LOGD(5);
    if (lane == 0) __hip_atomic_fetch_add((gu64*)((GAS char*)(a.ws + o2) + acc_off(m * 4, 0)), (u64)1, RLX_AGENT);
    wait_until(now32() + SCH_DRAIN);
    wait_cyc(tp1 + END_E4);
    unsigned x;
    poll([&]() { x = __builtin_amdgcn_raw_buffer_load_b32(rs, acc_off(c * 4, 0), o2, 16); }, [&]() { return wave_all((x & 16u) != 0u); });
    lds_inc<L_MISC + 68>(lds0);
    LOGD(6);
  }
  LOGP(8);
}

__device__ __forceinline__ void proj_front(const RecArgs& a, char* lds, const __amdgpu_buffer_rsrc_t rs, const int v) {
  const int lane = __builtin_amdgcn_workitem_id_x() & 63, b = __builtin_amdgcn_workgroup_id_x(), c = lane & 15, g = lane >> 4;
  const int rb = (b & 7) + 8 * (b >> 5), cq = (b >> 3) & 3;
  const unsigned lds0 = (unsigned)(uintptr_t)(LAS char*)lds;
  unsigned* const cnt = (unsigned*)(lds + L_MISC) + 20;
  LOGF2(0);
  float* const red = (float*)(lds + L_RED1);
  float* const fin = red + 640;
  const f32x4 Z4 = {0.f, 0.f, 0.f, 0.f};
#pragma unroll
  for (int i = 0; i < 8; ++i) x_fetch((const u16*)(a.ws + OFF_OB) + (size_t)rb * 32 * DM, lds0, 0, 8 * v + i, lane);
  bf16x8 W[2][20];
#pragma unroll
  for (int k = 0; k < 40; ++k) W[k & 1][k >> 1] = *(const bf16x8*)(a.ws + OFF_WOBT + ((size_t)(cq * 10 + 2 * v + (k & 1)) * 20 + (k >> 1)) * 1024 + lane * 16);
  const unsigned fo = (unsigned)(rb * 40960 + (cq * 5 + v) * 1024 + ((g >> 1) * 16 + c) * 16), f8 = fo + (g & 1) * 8, f16 = fo + (g & 1) * 20480;
  u32x2 xo[2][2];
#pragma unroll
  for (int k = 0; k < 4; ++k)
    xo[k >> 1][k & 1] = __builtin_bit_cast(u32x2, __hip_atomic_load((const gu64*)((const char*)a.xnew + f8 + (k >> 1) * 512 + (k & 1) * 20480), RLX_AGENT));
  asm volatile("s_waitcnt vmcnt(44)" ::: "memory");
  lds_inc<L_MISC + 80>(lds0);
  lds_wait(cnt, 5u);
  LOGF2(1);
  f32x4 acc[2][2] = {{Z4, Z4}, {Z4, Z4}};
#pragma unroll
  for (int ks = 0; ks < 20; ++ks)
#pragma unroll
    for (int t = 0; t < 2; ++t) {
      const bf16x8 x = *(const bf16x8*)(lds + L_XN + (t * 20 + ks) * 1024 + lane * 16);
#pragma unroll
      for (int i = 0; i < 2; ++i) acc[i][t] = mfma32(W[i][ks], x, acc[i][t]);
    }
  u32x2 xp[2][2];
  PIN_V(acc[1][1]);
  LOGF2(2);
#pragma unroll
  for (int k = 0; k < 4; ++k) {
    const int i = k >> 1, t = k & 1;
#pragma unroll
    for (int q = 0; q < 2; ++q) {
      const unsigned r = pack2(acc[i][t][2 * q], acc[i][t][2 * q + 1]), x = xo[i][t][q];
      xp[i][t][q] = pack2(u2f(x << 16) + u2f(r << 16), u2f(x & 0xffff0000u) + u2f(r & 0xffff0000u));
      acc[i][t][2 * q] = u2f(xp[i][t][q] << 16); acc[i][t][2 * q + 1] = u2f(xp[i][t][q] & 0xffff0000u);
    }
    const float ml = x32sum(x16sum((acc[i][t][0] + acc[i][t][1]) + (acc[i][t][2] + acc[i][t][3]))) * (1.0f / 16.0f);
    float q = 0.f;
#pragma unroll
    for (int r = 0; r < 4; ++r) { const float d = acc[i][t][r] - ml; q += d * d; }
    q = x32sum(x16sum(q));
    if (g == 0) { red[(2 * v + i) * 32 + t * 16 + c] = ml; red[320 + (2 * v + i) * 32 + t * 16 + c] = q; }
  }
  lds_inc<L_MISC + 80>(lds0);
  if (v == 0) {
    lds_wait(cnt, 10u);
    LOGF2(3);
    float mw[10], ml = 0.f, m2 = 0.f;
#pragma unroll
    for (int i = 0; i < 10; ++i) { mw[i] = red[i * 32 + (lane & 31)]; ml += mw[i]; m2 += red[320 + i * 32 + (lane & 31)]; }
    ml *= 0.1f;
#pragma unroll
    for (int i = 0; i < 10; ++i) m2 += 16.0f * (mw[i] - ml) * (mw[i] - ml);
    const unsigned xs = (unsigned)OFF_LNX + rb * 1024;
    if (lane < 32) SC1_STORE8S(((u32x2){f2u(ml), nz(f2u(m2))}), (cq * 32 + lane) * 8, xs);
    wait_cyc(cyc32() + FR_D);
    u32x2 x0, x1;
    for (unsigned spins = 0; spins < SPIN_CAP; ++spins) {
      x0 = __builtin_amdgcn_raw_buffer_load_b64(rs, lane * 8, xs, 16); x1 = __builtin_amdgcn_raw_buffer_load_b64(rs, 512 + lane * 8, xs, 16);
      if (wave_all(x0[1] != 0u && x1[1] != 0u)) break;
      asm volatile("" ::: "memory");
    }
    const float m0 = u2f(x0[0]), m1 = u2f(x1[0]);
    const float mean = x32sum(m0 + m1) * 0.25f;
    const float M2 = x32sum((u2f(x0[1]) + u2f(x1[1])) + 160.0f * ((m0 - mean) * (m0 - mean) + (m1 - mean) * (m1 - mean)));
    const float mb = rbf(mean), den = rbf(__builtin_sqrtf(rbf(rbf(M2 * (1.0f / DM)) + 1e-5f)));
    if (lane < 32) {
      fin[lane] = mb; fin[32 + lane] = 1.0f / den;
      if (cq == 0) __hip_atomic_store((GAS unsigned*)(a.ws + OFF_XNRM) + (rb * 32 + lane) * 4, f2u(1.02f * (M2 + DM * (mean - mb) * (mean - mb)) / (den * den)), RLX_AGENT);
    }
    lds_inc<L_MISC + 84>(lds0);
  }
  lds_wait(cnt + 1, 1u);
  LOGF2(4);
  const __amdgpu_buffer_rsrc_t rx = __builtin_amdgcn_make_buffer_rsrc(a.ws + OFF_XN, 0, (int)(OFF_QB - OFF_XN), 0x00020000);
  auto put = [&](const u32x2 p0, const u32x2 p1, unsigned so) {
    const auto r0 = __builtin_amdgcn_permlane16_swap(p0[0], p1[0], false, false), r1 = __builtin_amdgcn_permlane16_swap(p0[1], p1[1], false, false);
    __builtin_amdgcn_raw_buffer_store_b128((u32x4){r0[0], r1[0], r0[1], r1[1]}, rx, f16, so, 16);
  };
#pragma unroll
  for (int i = 0; i < 2; ++i) {
    u32x2 y[2];
#pragma unroll
    for (int t = 0; t < 2; ++t) {
      const float mb = fin[t * 16 + c], rden = fin[32 + t * 16 + c];
      y[t] = (u32x2){pack2(rbf(acc[i][t][0] - mb) * rden, rbf(acc[i][t][1] - mb) * rden), pack2(rbf(acc[i][t][2] - mb) * rden, rbf(acc[i][t][3] - mb) * rden)};
    }
    put(y[0], y[1], i * 512);
  }
  VM_DRAIN();
  lds_inc<L_MISC + 80>(lds0);
#pragma unroll
  for (int i = 0; i < 2; ++i) put(xp[i][0], xp[i][1], (unsigned)(OFF_XB - OFF_XN) + i * 512);
  if (v == 0) {
    lds_wait(cnt, 15u);
    if (rb < 4 && lane < 8) __hip_atomic_fetch_add((GAS unsigned*)(a.ws + OFF_RDY) + lane * 64, 1u, RLX_AGENT);
  }
  LOGF2(5);
}

__global__ __launch_bounds__(512, 2) void rec_kernel(RecArgs a) {
  __shared__ __attribute__((aligned(16))) char lds[L_BYTES];
  const int tid = __builtin_amdgcn_workitem_id_x(), wid = __builtin_amdgcn_readfirstlane(tid >> 6), w = wid & 3;
  const __amdgpu_buffer_rsrc_t rs = __builtin_amdgcn_make_buffer_rsrc(a.ws, 0, (int)COMM_BYTES, 0x00020000);
  if (tid < 32) ((float*)(lds + L_MISC))[tid] = (tid == 4 || tid == 5) ? 1.0f : 0.0f;
  BAR();
  if (a.layer && wid < 5) proj_front(a, lds, rs, wid);
  if (wid < 4) {
    if (w == 0) rec_waves<0>(a, lds, rs);
    else if (w == 1) rec_waves<1>(a, lds, rs);
    else if (w == 2) rec_waves<2>(a, lds, rs);
    else rec_waves<3>(a, lds, rs);
  } else {
    if (w == 0) token_waves<0>(a, lds, rs);
    else if (w == 1) token_waves<1>(a, lds, rs);
    else if (w == 2) token_waves<2>(a, lds, rs);
    else token_waves<3>(a, lds, rs);
    qkv_tail(a, lds, w);
  }
}

#if defined(__HIP_DEVICE_COMPILE__)
#pragma clang attribute pop
#endif

constexpr int KQ = S_ / 4;
#define ATT_P1 6
#define ATT_P2 10
#define ATT_P3 14
#define ATT_THR 6.0f
#define ATT_NQ 4
#define ATT_LMAX 1.0e12f
#if defined(__HIP_DEVICE_COMPILE__)
#pragma clang attribute push(__attribute__((target("no-packed-fp32-ops"))), apply_to = function)
#endif
__global__ __launch_bounds__(256 * ATT_NQ, 4) void attb_kernel(const u16* __restrict__ Qp, const u16* __restrict__ KV, u16* __restrict__ O) {
  __shared__ float mrg_[ATT_NQ][3][2][13][64];
  const int tid = __builtin_amdgcn_workitem_id_x(), bid = __builtin_amdgcn_workgroup_id_x();
  __shared__ unsigned sink[64];
  own_code<4800>(tid, sink);
  const int lane = tid & 63, wid = __builtin_amdgcn_readfirstlane(tid >> 6), kh = wid / ATT_NQ, r = lane & 31, hh = lane >> 5;
  const int hd = (bid & 7) * 4 + ((bid >> 3) & 3), qb = (bid >> 5) * ATT_NQ + wid % ATT_NQ;
  float (*mrg)[2][13][64] = mrg_[wid % ATT_NQ];
  const int q0 = qb * 64;
  bf16x8 qa[2]; u32x4 qb2[2];
#pragma unroll
  for (int qt = 0; qt < 2; ++qt) {
    { const u32x2* qp = (const u32x2*)(Qp + ((size_t)hd * S_ + q0 + qt * 32 + r) * QST + 8 * hh); const u32x2 q0_ = qp[0], q1_ = qp[1]; qa[qt] = mk8(q0_[0], q0_[1], q1_[0], q1_[1]); }
    const u32x2 t = *(const u32x2*)(Qp + ((size_t)hd * S_ + q0 + qt * 32 + r) * QST + 16);
    qb2[qt] = (u32x4){hh ? 0u : t[0], hh ? 0u : t[1], 0u, 0u};
  }
  const __amdgpu_buffer_rsrc_t rk = __builtin_amdgcn_make_buffer_rsrc((void*)(KV + ((size_t)hd * 64 + kh * 16) * (KVT / 2)), 0, 16 * KVT, 0x00020000);
  unsigned ok1 = lane * 16, ok2 = hh ? 1u << 30 : 1024 + r * 8, ov = r > 20 ? 1u << 30 : 1280 + (hh * 21 + r) * 16;
  f32x16 o[2];
  float m[2];
  const f32x16 Z16 = {0.f, 0.f, 0.f, 0.f, 0.f, 0.f, 0.f, 0.f, 0.f, 0.f, 0.f, 0.f, 0.f, 0.f, 0.f, 0.f};
  struct KvT { u32x4 k1, v0, v1; u32x2 k2; };
  auto fetch = [&](int t) {
    KvT x;
    __builtin_amdgcn_sched_barrier(0);
    x.k1 = __builtin_amdgcn_raw_buffer_load_b128(rk, ok1, t * KVT, 0); x.k2 = __builtin_amdgcn_raw_buffer_load_b64(rk, ok2, t * KVT, 0);
    x.v0 = __builtin_amdgcn_raw_buffer_load_b128(rk, ov, t * KVT, 0); x.v1 = __builtin_amdgcn_raw_buffer_load_b128(rk, ov + 672, t * KVT, 0);
    __builtin_amdgcn_sched_barrier(0);
    return x;
  };
  auto tile = [&](const KvT& x, auto MODE) {
    const bf16x8 ckb = mk8(x.k2[0], x.k2[1], 0x3f803f80u, 0x00003f80u);
    f32x16 sa[2];
#pragma unroll
    for (int qt = 0; qt < 2; ++qt) {
      sa[qt] = __builtin_amdgcn_mfma_f32_32x32x16_bf16(as8(x.k1), qa[qt], Z16, 0, 0, 0);
      sa[qt] = __builtin_amdgcn_mfma_f32_32x32x16_bf16(ckb, as8(qb2[qt]), sa[qt], 0, 0, 0);
    }
#pragma unroll
    for (int qt = 0; qt < 2; ++qt) {
      if (MODE != 0) {
        float mx = fmx(fmx(sa[qt][0], sa[qt][1]), sa[qt][2]);
#pragma unroll
        for (int i = 3; i < 15; i += 2) mx = fmx(fmx(mx, sa[qt][i]), sa[qt][i + 1]);
        mx = fmx(mx, sa[qt][15]);
        if (MODE == 1 || !wave_all(mx <= ATT_THR)) {
          mx = x32mx(mx);
          if (MODE != 1) mx = fmx(mx, 0.f);
          const float mn = m[qt] + mx;
          const unsigned h1 = f2bf(mn); const float r1 = mn - bf2f(h1);
          const unsigned h2 = f2bf(r1); const float r2 = r1 - bf2f(h2);
          const unsigned h3 = f2bf(r2);
          const float me = (bf2f(h1) + bf2f(h2)) + bf2f(h3);
          const float dm = me - m[qt];
          m[qt] = me;
          if (hh == 0) { qb2[qt][2] = (h1 | (h2 << 16)) ^ 0x80008000u; qb2[qt][3] = h3 ^ 0x8000u; }
          const float alpha = __builtin_amdgcn_exp2f(-dm);
#pragma unroll
          for (int i = 0; i < 16; ++i) { if (MODE != 1) o[qt][i] *= alpha; sa[qt][i] -= dm; }
        }
      }
      float p[16];
#pragma unroll
      for (int i = 0; i < 16; ++i) p[i] = __builtin_amdgcn_exp2f(sa[qt][i]);
      bf16x8 p0 = mk8(pack2(p[0], p[1]), pack2(p[2], p[3]), pack2(p[4], p[5]), pack2(p[6], p[7]));
      bf16x8 p1 = mk8(pack2(p[8], p[9]), pack2(p[10], p[11]), pack2(p[12], p[13]), pack2(p[14], p[15]));
      o[qt] = __builtin_amdgcn_mfma_f32_32x32x16_bf16(as8(x.v0), p0, MODE == 1 ? Z16 : o[qt], 0, 0, 0);
      o[qt] = __builtin_amdgcn_mfma_f32_32x32x16_bf16(as8(x.v1), p1, o[qt], 0, 0, 0);
    }
  };
  typedef std::integral_constant<int, 0> I0; typedef std::integral_constant<int, 1> I1; typedef std::integral_constant<int, 2> I2;
  auto pass = [&](auto MODE) {
    asm volatile("" : "+v"(ok1), "+v"(ok2), "+v"(ov) :: "memory");
    m[0] = m[1] = 0.f;
    qb2[0][2] = qb2[0][3] = qb2[1][2] = qb2[1][3] = 0u;
    KvT xa = fetch(0), xb = fetch(1);
    tile(xa, I1{});
    xa = fetch(2);
    tile(xb, MODE);
#pragma unroll 1
    for (int t = 2; t < KQ / 32; t += 2) {
      if (t == ATT_P1) __builtin_amdgcn_s_setprio(2);
      if (t == ATT_P2) __builtin_amdgcn_s_setprio(1);
      if (t == ATT_P3) __builtin_amdgcn_s_setprio(0);
      xb = fetch(t + 1);
      tile(xa, MODE);
      xa = fetch(t + 2);
      tile(xb, MODE);
    }
  };
  if (ATT_P1) __builtin_amdgcn_s_setprio(3);
  pass(I0{});
  {
    float big = 0.f;
#pragma unroll
    for (int qt = 0; qt < 2; ++qt) {
      big = fmx(big, hh ? o[qt][8] * (3.0e38f / ATT_LMAX) : __builtin_fabsf(o[qt][8]));
#pragma unroll
      for (int i = 0; i < 12; ++i) if (i != 8) big = fmx(big, (i > 8 && hh) ? 0.f : __builtin_fabsf(o[qt][i]));
    }
    if (__builtin_expect(!wave_all(big <= 3.0e38f), 0)) pass(I2{});
  }
  if (kh > 0) {
#pragma unroll
    for (int qt = 0; qt < 2; ++qt) {
#pragma unroll
      for (int i = 0; i < 12; ++i) mrg[kh - 1][qt][i][lane] = o[qt][i];
      mrg[kh - 1][qt][12][lane] = m[qt];
    }
  }
  asm volatile("s_waitcnt lgkmcnt(0)\n\ts_barrier" ::: "memory");
  if (kh == 0) {
#pragma unroll
    for (int qt = 0; qt < 2; ++qt) {
      float m1 = mrg[0][qt][12][lane], m2 = mrg[1][qt][12][lane], m3 = mrg[2][qt][12][lane];
      float mn = fmx(fmx(m[qt], m1), fmx(m2, m3));
      float a0 = __builtin_amdgcn_exp2f(m[qt] - mn), a1 = __builtin_amdgcn_exp2f(m1 - mn);
      float a2 = __builtin_amdgcn_exp2f(m2 - mn), a3 = __builtin_amdgcn_exp2f(m3 - mn);
      float v[12];
#pragma unroll
      for (int i = 0; i < 12; ++i) v[i] = (o[qt][i] * a0 + mrg[0][qt][i][lane] * a1) + (mrg[1][qt][i][lane] * a2 + mrg[2][qt][i][lane] * a3);
      auto lr = __builtin_amdgcn_permlane32_swap(f2u(v[8]), f2u(v[8]), false, false);
      float inv = __builtin_amdgcn_rcpf(u2f(lr[1]));
      const int row = q0 + qt * 32 + r;
      auto oaddr = [&](int k) { return O + (((size_t)(row >> 4) * 20 + (k >> 5)) * 64 + ((k & 31) >> 3) * 16 + (row & 15)) * 8 + (k & 7); };
      const int k0 = hd * BD + 4 * hh;
      *(u32x2*)oaddr(k0) = (u32x2){pack2(v[0] * inv, v[1] * inv), pack2(v[2] * inv, v[3] * inv)};
      *(u32x2*)oaddr(k0 + 8) = (u32x2){pack2(v[4] * inv, v[5] * inv), pack2(v[6] * inv, v[7] * inv)};
      if (hh == 0) *(u32x2*)oaddr(k0 + 16) = (u32x2){pack2(v[8] * inv, v[9] * inv), pack2(v[10] * inv, v[11] * inv)};
    }
  }
}
#if defined(__HIP_DEVICE_COMPILE__)
#pragma clang attribute pop
#endif

__global__ __launch_bounds__(640, 3) void proj_kernel(const u16* __restrict__ Of, const u16* __restrict__ Wf, const u16* __restrict__ xold,
                                                      u16* __restrict__ xnew) {
  __shared__ __attribute__((aligned(16))) char ob[40960];
  const int tid = threadIdx.x, lane = tid & 63, w = __builtin_amdgcn_readfirstlane(tid >> 6), g = lane >> 4, c = lane & 15;
  __shared__ unsigned sink[64];
  own_code<1500>(tid, sink);
  const int rb = (blockIdx.x & 7) + 8 * (blockIdx.x >> 5), cb = (blockIdx.x >> 3) & 3;
  const int col = cb * 160 + w * 16 + 4 * g;
  const u16* wp = Wf + (size_t)(cb * 10 + w) * 20 * 512 + lane * 8;
  u32x4 st[4];
#pragma unroll
  for (int i = 0; i < 4; ++i) st[i] = *(const u32x4*)(Of + (size_t)rb * 40 * 512 + (i * 640 + tid) * 8);
  bf16x8 wf[20];
#pragma unroll
  for (int ks = 0; ks < 20; ++ks) wf[ks] = *(const bf16x8*)(wp + (size_t)ks * 512);
  u32x2 xo[2];
#pragma unroll
  for (int t = 0; t < 2; ++t) xo[t] = *(const u32x2*)(xold + (((size_t)(rb * 2 + t) * 20 + (col >> 5)) * 64 + ((col & 31) >> 3) * 16 + c) * 8 + (col & 7));
#pragma unroll
  for (int i = 0; i < 4; ++i) *(u32x4*)(ob + (i * 640 + tid) * 16) = st[i];
  BAR();
  f32x4 acc[2] = {{0.f, 0.f, 0.f, 0.f}, {0.f, 0.f, 0.f, 0.f}};
#pragma unroll
  for (int ks = 0; ks < 20; ++ks)
#pragma unroll
    for (int t = 0; t < 2; ++t) acc[t] = mfma32(wf[ks], *(const bf16x8*)(ob + (t * 20 + ks) * 1024 + lane * 16), acc[t]);
#pragma unroll
  for (int t = 0; t < 2; ++t) {
    const unsigned r01 = pack2(acc[t][0], acc[t][1]), r23 = pack2(acc[t][2], acc[t][3]);
    const unsigned p01 = pack2(u2f(xo[t][0] << 16) + u2f(r01 << 16), u2f(xo[t][0] & 0xffff0000u) + u2f(r01 & 0xffff0000u));
    const unsigned p23 = pack2(u2f(xo[t][1] << 16) + u2f(r23 << 16), u2f(xo[t][1] & 0xffff0000u) + u2f(r23 & 0xffff0000u));
    *(u32x2*)(xnew + (size_t)(rb * 32 + t * 16 + c) * DM + col) = (u32x2){p01, p23};
  }
}

extern "C" void kernel_launch(void* const* d_in, const int* in_sizes, int n_in, void* d_out, int out_size, void* d_ws,
                              size_t ws_size, hipStream_t stream) {
  const u16* A_x = (const u16*)d_in[0];
  const u16* A_mem0 = (const u16*)d_in[1];
  const u16* A_Wq = (const u16*)d_in[2];
  const u16* A_Wk = (const u16*)d_in[3];
  const u16* A_Wv = (const u16*)d_in[4];
  const u16* A_Wo = (const u16*)d_in[5];
  const u16* B_wqkv = (const u16*)d_in[8];
  const u16* B_wo = (const u16*)d_in[9];
  char* ws = (char*)(((uintptr_t)d_ws + ((size_t)WS_OFF << 20) + WS_AL - 1) & ~(uintptr_t)(WS_AL - 1));
  u16* xa = (u16*)(ws + OFF_XA); u16* Ob = (u16*)(ws + OFF_OB);

  prologue_kernel<<<256, 640, 0, stream>>>(ProArgs{A_x, A_Wq, A_Wk, A_Wv, A_Wo, B_wqkv, B_wo, ws});
  for (int layer = 0; layer < 2; ++layer) {
    rec_kernel<<<256, 512, 0, stream>>>(RecArgs{A_mem0, A_Wq, A_Wk, layer ? (const u16*)(ws + OFF_XB) : A_x, xa, ws, (unsigned)layer, 0u});
    attb_kernel<<<BH * 32 / ATT_NQ, 256 * ATT_NQ, 0, stream>>>((const u16*)(ws + OFF_QB), (const u16*)(ws + OFF_KV), Ob);
  }
  proj_kernel<<<256, 640, 0, stream>>>(Ob, (const u16*)(ws + OFF_WOBT), xa, (u16*)d_out);
}
```

```cpp
#include <hip/hip_runtime.h>
#include <hip/hip_bf16.h>
#include <stdint.h>
#include <type_traits>

typedef unsigned short u16;
typedef unsigned long long u64;
typedef __attribute__((ext_vector_type(8))) short bf16x8;
typedef __attribute__((ext_vector_type(16))) short bf16x16;
typedef __attribute__((ext_vector_type(4))) float f32x4;
typedef __attribute__((ext_vector_type(2))) float f32x2;
typedef __attribute__((ext_vector_type(16))) float f32x16;
typedef __attribute__((ext_vector_type(4))) unsigned u32x4;
typedef __attribute__((ext_vector_type(2))) unsigned u32x2;
typedef __attribute__((ext_vector_type(3))) unsigned u32x3;
typedef __attribute__((ext_vector_type(8))) unsigned u32x8;
typedef __attribute__((ext_vector_type(16))) unsigned u32x16;
typedef __attribute__((ext_vector_type(32))) unsigned u32x32;
#define GAS __attribute__((address_space(1)))
#define LAS __attribute__((address_space(3)))
typedef GAS unsigned long long gu64;
#define BAR() do { __builtin_amdgcn_s_waitcnt(0xC07F); asm volatile("s_waitcnt lgkmcnt(0)\n\ts_barrier" ::: "memory"); } while (0)

constexpr int S_ = 2048, DM = 640;
constexpr int NH = 32, DH = 192, HD = NH * DH;
constexpr int MEM = 8, SEG = 32, NSEG = S_ / SEG;
constexpr int BH = 32, BD = 20;

constexpr size_t al256(size_t x) { return (x + 255) & ~(size_t)255; }
constexpr int LAG3 = 2, LAG4 = 3, LAG5 = 6;
#define SR_ 8
constexpr int SR = SR_;
constexpr unsigned SCT_STEP = 131072, OFF_SCM = SR * SCT_STEP, SCM_STEP = 32768;
constexpr int ACC_NB = 192;
constexpr size_t ACC_STEP = 65536;
constexpr unsigned ACC_TS = ACC_NB * 256;
constexpr size_t ACC_LAYER = SR * ACC_STEP;
constexpr size_t OFF_ACC = 3 * 524288;
static_assert(SR == 8, "regions");
__device__ __forceinline__ unsigned ts_piece(int i) { return ACC_TS + (unsigned)(i < 128 ? (i >> 1) * 256 + (i & 1) * 16 : 128 + (i & 1) * 8192); }
constexpr size_t OFF_LNX = OFF_SCM + SR * SCM_STEP;
constexpr size_t OFF_RDY = OFF_LNX + 65536;
constexpr size_t PG = 2097152;
#define RING_ 4
constexpr int RING = RING_;
constexpr size_t TS_SLOT = (size_t)NH * 8 * 2560, PT_SLOT = (size_t)NH * 2560, OP_SLOT = (size_t)256 * 2560;
__device__ __forceinline__ unsigned ts_tok(int h, int j) { return (unsigned)((h & 15) * 32768 + (j >> 2) * 16384 + (j & 3) * 2048); }
__device__ __forceinline__ unsigned ts_mem(int h, int j) { return (unsigned)(524288 + ((h & 15) >> 1) * 16384 + (h & 1) * 4096 + j * 512); }
__device__ __forceinline__ unsigned pt_tok(int h) { return (unsigned)(((h & 15) >> 2) * 16384 + (h & 3) * 2048); }
__device__ __forceinline__ unsigned pt_mem(int h) { return (unsigned)(65536 + (h & 15) * 512); }
constexpr size_t OFF_X = 2 * PG + TS_SLOT + 3 * PT_SLOT, X_BYTES = 7 * OP_SLOT + 5 * PT_SLOT;
constexpr size_t OFF_OP = OFF_X + TS_SLOT + 5 * PT_SLOT;
constexpr size_t COMM_BYTES = OFF_X + X_BYTES + RING * OP_SLOT;
__device__ __forceinline__ unsigned near_half(unsigned ck, unsigned o) { return o + ((ck >> (o >= 2 * PG ? 8 : 4)) & 8192u); }
__device__ __forceinline__ unsigned ts_slot(unsigned ck, int sl) {
  return near_half(ck, sl == NSEG - 1 ? (unsigned)OFF_X : ((sl & 3) == 3 ? (unsigned)(2 * PG) : (unsigned)PG + (unsigned)(sl & 3) * (unsigned)TS_SLOT));
}
__device__ __forceinline__ unsigned pt_slot(unsigned ck, int sl) {
  return near_half(ck, sl >= NSEG - 5 ? (unsigned)(OFF_X + TS_SLOT) + (unsigned)(sl - (NSEG - 5)) * (unsigned)PT_SLOT
                                      : ((sl & 3) == 0 ? (unsigned)(PG + 3 * TS_SLOT) : (unsigned)(2 * PG + TS_SLOT - PT_SLOT) + (unsigned)(sl & 3) * (unsigned)PT_SLOT));
}
__device__ __forceinline__ unsigned op_slot(int sl) { return (unsigned)OFF_OP + (unsigned)(sl >= NSEG - 6 ? sl - (NSEG - 6) : 6 + sl % RING) * (unsigned)OP_SLOT; }
static_assert(RING == 4 && OFF_OP <= 3 * PG && OFF_RDY + 2048 <= OFF_ACC && OFF_ACC + ACC_LAYER == PG && TS_SLOT % 16384 == 0 && PT_SLOT % 16384 == 0, "pages");
static_assert(TS_SLOT == OP_SLOT && RING >= 4 && NSEG % SR == 0 && SR >= 5 && X_BYTES % 4096 == 0 && X_BYTES <= 256 * 20 * 1024, "slots");
constexpr size_t OFF_WB = al256(COMM_BYTES);
constexpr size_t OFF_BNRM = OFF_WB + (size_t)256 * 100 * 1024;
constexpr size_t OFF_LAT = OFF_BNRM + 1024;
constexpr size_t OFF_XNRM = OFF_LAT + 2048;
constexpr size_t OFF_WQKVBT = OFF_XNRM + al256(S_ * 16);
constexpr size_t OFF_WOBT = OFF_WQKVBT + al256((size_t)3 * DM * DM * 2);
constexpr size_t OFF_XN = OFF_WOBT + al256((size_t)DM * DM * 2);
constexpr int QST = BD;
constexpr size_t OFF_XA = OFF_XN + al256((size_t)S_ * DM * 2);
constexpr size_t OFF_XB = OFF_XA + al256((size_t)S_ * DM * 2);
constexpr size_t OFF_QB = OFF_XB + al256((size_t)S_ * DM * 2);
constexpr int KVT = 2624;
constexpr size_t OFF_KV = OFF_QB + al256((size_t)BH * S_ * QST * 2);
constexpr size_t OFF_OB = OFF_KV + al256((size_t)BH * 64 * KVT);
constexpr size_t OFF_DBG = OFF_OB + al256((size_t)S_ * DM * 2);
constexpr size_t WS_TOTAL = OFF_DBG + (size_t)2 * 64 * 8 * DM * 2;
#define WS_AL 2097152
#define WS_OFF 0
static_assert(WS_TOTAL + WS_AL + ((size_t)WS_OFF << 20) <= ((size_t)256 << 20), "workspace budget");

#if defined(__HIP_DEVICE_COMPILE__)
#pragma clang attribute push(__attribute__((target("no-packed-fp32-ops"))), apply_to = function)
#endif
__device__ __forceinline__ float u2f(unsigned v) { return __builtin_bit_cast(float, v); }
__device__ __forceinline__ unsigned f2u(float v) { return __builtin_bit_cast(unsigned, v); }
__device__ __forceinline__ float bf2f(unsigned v) { return __builtin_bit_cast(float, v << 16); }
__device__ __forceinline__ unsigned f2bf(float f) { __bf16 b = (__bf16)f; return (unsigned)__builtin_bit_cast(u16, b); }
__device__ __forceinline__ float rbf(float f) { return bf2f(f2bf(f)); }
typedef __attribute__((ext_vector_type(2))) __bf16 hbf16x2;
__device__ __forceinline__ unsigned pack2(float lo, float hi) { f32x2 v = {lo, hi}; hbf16x2 b = __builtin_convertvector(v, hbf16x2); return __builtin_bit_cast(unsigned, b); }
__device__ __forceinline__ f32x4 mfma32(bf16x8 a, bf16x8 b, f32x4 c) { return __builtin_amdgcn_mfma_f32_16x16x32_bf16(a, b, c, 0, 0, 0); }
typedef __attribute__((ext_vector_type(8))) __bf16 hbf16x8;
typedef __attribute__((ext_vector_type(16))) __bf16 hbf16x16;
__device__ __forceinline__ f32x4 smfma64(bf16x8 a, bf16x16 b, f32x4 c, int idx) {
  return __builtin_amdgcn_smfmac_f32_16x16x64_bf16(__builtin_bit_cast(hbf16x8, a), __builtin_bit_cast(hbf16x16, b), c, idx, 0, 0);
}
__device__ __forceinline__ bf16x16 cat16(bf16x8 a, bf16x8 b) { return __builtin_shufflevector(a, b, 0, 1, 2, 3, 4, 5, 6, 7, 8, 9, 10, 11, 12, 13, 14, 15); }
__device__ __forceinline__ bf16x8 half8(bf16x16 v, int hi) { return hi ? __builtin_shufflevector(v, v, 8, 9, 10, 11, 12, 13, 14, 15) : __builtin_shufflevector(v, v, 0, 1, 2, 3, 4, 5, 6, 7); }
__device__ __forceinline__ bf16x8 mk8(unsigned a, unsigned b, unsigned c, unsigned d) { u32x4 v = {a, b, c, d}; return __builtin_bit_cast(bf16x8, v); }
__device__ __forceinline__ unsigned dpp_ror8(unsigned v) { return (unsigned)__builtin_amdgcn_mov_dpp((int)v, 0x128, 0xf, 0xf, true); }
__device__ __forceinline__ float fmx(float a, float b) { return __builtin_elementwise_maximum(a, b); }

__device__ __forceinline__ int imax(int a, int b) { return a > b ? a : b; }
__device__ __forceinline__ int imin(int a, int b) { return a < b ? a : b; }
__device__ __forceinline__ unsigned umin(unsigned a, unsigned b) { return a < b ? a : b; }
__device__ __forceinline__ unsigned umax(unsigned a, unsigned b) { return a > b ? a : b; }
__device__ __forceinline__ bool wave_all(bool p) { return __builtin_amdgcn_ballot_w64(!p) == 0ull; }
__device__ __forceinline__ float x16mx(float x) { unsigned u = __builtin_bit_cast(unsigned, x); auto r = __builtin_amdgcn_permlane16_swap(u, u, false, false); return fmx(u2f(r[0]), u2f(r[1])); }
__device__ __forceinline__ float x32mx(float x) { unsigned u = __builtin_bit_cast(unsigned, x); auto r = __builtin_amdgcn_permlane32_swap(u, u, false, false); return fmx(u2f(r[0]), u2f(r[1])); }
__device__ __forceinline__ float x16sum(float x) { unsigned u = __builtin_bit_cast(unsigned, x); auto r = __builtin_amdgcn_permlane16_swap(u, u, false, false); return u2f(r[0]) + u2f(r[1]); }
__device__ __forceinline__ float x32sum(float x) { unsigned u = __builtin_bit_cast(unsigned, x); auto r = __builtin_amdgcn_permlane32_swap(u, u, false, false); return u2f(r[0]) + u2f(r[1]); }
template <int CTRL> __device__ __forceinline__ float dppf(float v) { return u2f((unsigned)__builtin_amdgcn_mov_dpp((int)f2u(v), CTRL, 0xf, 0xf, true)); }
#define OWN_PF 1
template <int BYTES> __device__ __forceinline__ void own_code(int tid, unsigned* sink) {
  if (!OWN_PF || __builtin_amdgcn_readfirstlane(tid >> 6) * 4096 >= BYTES) return;
  const char* pc; unsigned keep;
  asm volatile("s_getpc_b64 %0" : "=s"(pc));
  asm volatile("s_mov_b32 %0, m0\n\ts_mov_b32 m0, %3\n\ts_nop 0\n\tglobal_load_lds_dword %1, %2\n\ts_mov_b32 m0, %0"
               : "=&s"(keep) : "v"(tid * 64 < BYTES ? tid * 64 : 0), "s"(pc), "s"((unsigned)(uintptr_t)(LAS unsigned*)sink) : "memory");
}
constexpr int DPP_XOR1 = 0xB1, DPP_XOR2 = 0x4E, DPP_ROR4 = 0x124, DPP_ROR8 = 0x128;
#if defined(__HIP_DEVICE_COMPILE__)
#pragma clang attribute pop
#endif

typedef short v4i16_t __attribute__((ext_vector_type(4)));
__device__ __forceinline__ bf16x8 tr_frag(const char* lo_, const char* hi_) {
  const v4i16_t lo = __builtin_amdgcn_ds_read_tr16_b64_v4i16((LAS v4i16_t*)lo_), hi = __builtin_amdgcn_ds_read_tr16_b64_v4i16((LAS v4i16_t*)hi_);
  return (bf16x8){lo[0], lo[1], lo[2], lo[3], hi[0], hi[1], hi[2], hi[3]};
}
__device__ __forceinline__ int rec_wg(int gp, int m) { return (((gp & 1) * 16 + m) << 3) | (gp >> 1); }
__device__ __forceinline__ float wsum(float v) {
  v += dppf<DPP_XOR1>(v); v += dppf<DPP_XOR2>(v); v += dppf<DPP_ROR4>(v); v += dppf<DPP_ROR8>(v);
  return x32sum(x16sum(v));
}
__device__ __forceinline__ float ln_row(int lane, u32x4 (&xo)[2]) {
  const bool two = lane < 16;
  float v[16];
#pragma unroll
  for (int hf = 0; hf < 2; ++hf)
#pragma unroll
    for (int i = 0; i < 4; ++i) { const unsigned u = xo[hf][i]; v[hf * 8 + 2 * i] = bf2f(u & 0xffff); v[hf * 8 + 2 * i + 1] = bf2f(u >> 16); }
  float s0 = 0.f, s1 = 0.f;
#pragma unroll
  for (int i = 0; i < 8; ++i) { s0 += v[i]; s1 += v[8 + i]; }
  const float mean = wsum(two ? s0 + s1 : s0) * (1.0f / DM);
  s0 = s1 = 0.f;
#pragma unroll
  for (int i = 0; i < 8; ++i) { const float d0 = v[i] - mean, d1 = v[8 + i] - mean; s0 += d0 * d0; s1 += d1 * d1; }
  const float var = rbf(wsum(two ? s0 + s1 : s0) * (1.0f / DM));
  const float mb = rbf(mean);
  const float rden = 1.0f / rbf(sqrtf(rbf(var + 1e-5f)));
  float n2[2] = {0.f, 0.f};
#pragma unroll
  for (int hf = 0; hf < 2; ++hf) {
    float y[8];
#pragma unroll
    for (int i = 0; i < 8; ++i) { y[i] = rbf(rbf(v[hf * 8 + i] - mb) * rden); n2[hf] += y[i] * y[i]; }
    xo[hf] = (u32x4){pack2(y[0], y[1]), pack2(y[2], y[3]), pack2(y[4], y[5]), pack2(y[6], y[7])};
  }
  return wsum(two ? n2[0] + n2[1] : n2[0]);
}

#define PROT(k)
#define WARM_CODE 1
#define PRB_N0 0
#define PRB_N1 3
#define PRB_LOG(i, v)
#define PLD(p) __builtin_nontemporal_load((const u32x4*)(p))
struct ProArgs { const u16* x; const u16* wq; const u16* wk; const u16* wv; const u16* wo; const u16* wqkvb; const u16* wob; char* ws; };
__global__ __launch_bounds__(640, 3) void prologue_kernel(ProArgs a) {
  __shared__ __attribute__((aligned(16))) char img[192 * 320];
  __shared__ __attribute__((aligned(16))) char slots[10 * 6144];
  __shared__ float red[10][160];
  __shared__ unsigned latw[8];
  const int b = blockIdx.x, tid = threadIdx.x, lane = tid & 63, v = __builtin_amdgcn_readfirstlane(tid >> 6), c = lane & 15, g = lane >> 4;
  const int h = (b & 7) * 4 + ((b >> 3) & 3), dh = (b >> 5) & 1, cq = b >> 6, ks = dh * 10 + v;
  char* const slot = slots + v * 6144;
  PROT(0);
  __shared__ unsigned sink[64];
  own_code<9600>(tid, sink);
  const unsigned zo = v < 8 ? (unsigned)(((b >> 6) * 512 + ((b & 7) * 2 + (v >> 2)) * 32 + ((b >> 3) & 7) * 4 + (v & 3)) * 1024) : (unsigned)(((v - 7) * 2048 + (b & 7) * 32 + ((b >> 3) & 7) * 4 + (b >> 6)) * 1024);
  __builtin_amdgcn_raw_buffer_store_b128((u32x4){0u, 0u, 0u, 0u}, __builtin_amdgcn_make_buffer_rsrc(a.ws, 0, (int)(3 * PG), 0x00020000), zo + lane * 16, 0, 16);
  __builtin_amdgcn_sched_barrier(0);
  u32x4 wa[2][6], wb[6], tr[4], xo[2];
  const int xrow = b * 8 + (v & 7);
  xo[0] = *(const u32x4*)(a.x + (size_t)xrow * DM + lane * 8);
  xo[1] = *(const u32x4*)(a.x + (size_t)xrow * DM + 512 + c * 8);
  const int job = imin(b + 256 * (9 - v), 800);
  const int jn = job < 600 ? job / 20 : (job % 200) / 20, jk = job % 20;
  {
    const u16* src = job < 600 ? a.wqkvb + (size_t)(32 * jk) * 3 * DM : a.wob + (size_t)(32 * jk) * DM;
#pragma unroll
    for (int i = 0; i < 4; ++i) tr[i] = *(const u32x4*)(src + (size_t)(i * 8 + (lane >> 3)) * (job < 600 ? 3 * DM : DM) + jn * 64 + (lane & 7) * 8);
  }
#pragma unroll
  for (int i = 0; i < 6; ++i) {
    const int idx = (v * 6 + i) * 64 + lane, row = idx / 20;
    wb[i] = PLD(a.wo + (size_t)(h * DH + row) * DM + cq * 160 + (idx - row * 20) * 8);
  }
#pragma unroll
  for (int i = 0; i < 6; ++i) {
    const int idx = i * 64 + lane, r = idx / 24;
#pragma unroll
    for (int u = 0; u < 2; ++u) wa[u][i] = PLD(a.wv + (size_t)(32 * ks + 8 * (r >> 2) + (r & 3) + 4 * u) * HD + h * DH + (idx - r * 24) * 8);
  }
  unsigned pf[2] = {0u, 0u};
  auto prefetch = [&]() {
#pragma unroll
    for (int i = 0; i < 2; ++i) pf[i] = *(const unsigned*)((i ? a.wq : a.wk) + ((size_t)(b * 10 + v) * 48 + imin(lane, 47)) * 32);
  };
  prefetch();
  __builtin_amdgcn_sched_barrier(0);
  unsigned lat = ~0u;
  auto probe = [&](int n0, int n) {
    const char* p = a.ws + zo;
#pragma unroll 1
    for (int i = n0; i < n0 + n; ++i) {
      u64 t0, t1; unsigned d;
      asm volatile("s_memtime %0\n\ts_waitcnt lgkmcnt(0)\n\ts_load_dword %2, %3, %4\n\ts_waitcnt lgkmcnt(0)\n\ts_memtime %1\n\ts_waitcnt lgkmcnt(0)"
                   : "=&s"(t0), "=&s"(t1), "=&s"(d) : "s"(p), "s"((i & 3) * 256 + (i >> 2) * 128) : "memory");
      PRB_LOG(i, (unsigned)t1 - (unsigned)t0);
      lat = umin(lat, (unsigned)t1 - (unsigned)t0);
    }
  };
  if (PRB_N0) { asm volatile("s_waitcnt vmcnt(26)" ::: "memory"); probe(0, PRB_N0); }
  __builtin_amdgcn_sched_barrier(0);
  const size_t xfo = (((size_t)(xrow >> 4) * 20 + (lane >> 2)) * 64 + (lane & 3) * 16 + (xrow & 15)) * 8;
  if (v < 8) {
    *(u32x4*)((u16*)(a.ws + OFF_XB) + xfo) = xo[0];
    if (lane < 16) *(u32x4*)((u16*)(a.ws + OFF_XB) + xfo + 16 * 512) = xo[1];
  }
  const float xn2 = u2f(__builtin_amdgcn_readfirstlane(f2u(ln_row(lane, xo))));
#pragma unroll
  for (int i = 0; i < 4; ++i) *(u32x4*)(slot + (i * 8 + (lane >> 3)) * 144 + (lane & 7) * 16) = tr[i];
  __builtin_amdgcn_fence(__ATOMIC_RELEASE, "wavefront"); __builtin_amdgcn_wave_barrier();
  bf16x8 tf[4];
#pragma unroll
  for (int t = 0; t < 4; ++t) {
    const char* bp = slot + (8 * g + (c >> 2)) * 144 + t * 32 + (c & 3) * 8;
    tf[t] = tr_frag(bp, bp + 4 * 144);
  }
  __builtin_amdgcn_fence(__ATOMIC_RELEASE, "wavefront"); __builtin_amdgcn_wave_barrier();
  __builtin_amdgcn_sched_barrier(0);
#pragma unroll
  for (int i = 0; i < 6; ++i) {
    const int idx = (v * 6 + i) * 64 + lane, row = idx / 20;
    *(u32x4*)(img + row * 320 + (((idx - row * 20) ^ ((row >> 2) & 2)) * 16)) = wb[i];
  }
  bf16x8 A[2][6];
#pragma unroll
  for (int u = 0; u < 2; ++u) {
#pragma unroll
    for (int i = 0; i < 6; ++i) {
      const int idx = i * 64 + lane, r = idx / 24;
      *(u32x4*)(slot + r * 384 + (((idx - r * 24) ^ (r >> 1)) * 16)) = wa[u][i];
    }
    __builtin_amdgcn_fence(__ATOMIC_RELEASE, "wavefront"); __builtin_amdgcn_wave_barrier();
#pragma unroll
    for (int e = 0; e < 6; ++e) A[u][e] = *(const bf16x8*)(slot + c * 384 + (((4 * e + g) ^ (c >> 1)) * 16));
    __builtin_amdgcn_fence(__ATOMIC_RELEASE, "wavefront"); __builtin_amdgcn_wave_barrier();
  }
  BAR();
  PROT(1);
  asm volatile("" :: "v"(pf[0]), "v"(pf[1]));
  if (v < 8) {
    u16* xn = (u16*)(a.ws + OFF_XN) + xfo;
    *(u32x4*)xn = xo[0];
    if (lane < 16) *(u32x4*)(xn + 16 * 512) = xo[1];
    if (lane < 4) ((float*)(a.ws + OFF_XNRM))[xrow * 4 + lane] = lane == 0 ? xn2 : 0.f;
  }
  if (job < 800) {
    u16* dst = (u16*)(a.ws + (job < 600 ? OFF_WQKVBT : OFF_WOBT)) + ((size_t)jn * 80 + jk) * 512 + lane * 8;
#pragma unroll
    for (int t = 0; t < 4; ++t) *(bf16x8*)(dst + (size_t)t * 20 * 512) = tf[t];
  }
  const int gp = h >> 1, hh = h & 1;
  const __amdgpu_buffer_rsrc_t rwb = __builtin_amdgcn_make_buffer_rsrc(a.ws + OFF_WB, 0, 256 * 100 * 1024, 0x00020000);
#pragma unroll
  for (int n = 0; n < 10; ++n) {
    const int off = n * 32 + 8 * (c & 3), T = cq * 10 + n;
    const char* bp = img + (8 * g + (c >> 2)) * 320 + (off ^ ((g & 1) << 5));
    f32x4 ac[2] = {{0.f, 0.f, 0.f, 0.f}, {0.f, 0.f, 0.f, 0.f}};
#pragma unroll
    for (int e = 0; e < 6; ++e) {
      const bf16x8 bf = tr_frag(bp + e * 32 * 320, bp + (e * 32 + 4) * 320);
      ac[0] = mfma32(A[0][e], bf, ac[0]); ac[1] = mfma32(A[1][e], bf, ac[1]);
    }
    const u32x4 o = {pack2(ac[0][0], ac[0][1]), pack2(ac[0][2], ac[0][3]), pack2(ac[1][0], ac[1][1]), pack2(ac[1][2], ac[1][3])};
    const unsigned oo = T < 32 ? (unsigned)((rec_wg(gp, T & 15) * 100 + (hh * 3 + (T >> 4)) * 20 + ks) * 1024 + lane * 16)
                               : (unsigned)((rec_wg(gp, 2 * (T - 32) + (c >> 3)) * 100 + 40 + ks) * 1024 + (g * 16 + hh * 8 + (c & 7)) * 16);
    __builtin_amdgcn_raw_buffer_store_b128(o, rwb, oo, 0, 0);
    float n2 = 0.f;
#pragma unroll
    for (int r = 0; r < 4; ++r) n2 += ac[0][r] * ac[0][r] + ac[1][r] * ac[1][r];
    n2 = x32sum(x16sum(n2));
    if (g == 0) red[v][n * 16 + c] = n2;
  }
  PROT(2);
  probe(PRB_N0, PRB_N1);
  if (v < 8) latw[v] = lat;
  else if (b < 64 && lane == 0) ((unsigned*)(a.ws + OFF_LAT))[(v - 4) * 64 + b] = umin(lat, 60000u);
  if (WARM_CODE && v == 7) {
    const char* pc; int d0, d1;
    asm("s_getpc_b64 %0\n\ts_mov_b32 %1, _Z10rec_kernel7RecArgs@rel32@lo+4\n\ts_mov_b32 %2, _Z11proj_kernelPKtS0_S0_Pt@rel32@lo+12" : "=s"(pc), "=s"(d0), "=s"(d1));
    const char *c0 = pc + d0, *c1 = pc + d1;
    const long o = ((long)b * 64 + lane) * 64;
    if (o < c1 - c0) asm volatile("" :: "v"(*(const volatile GAS unsigned*)(c0 + o)));
  }
  if (v == 9 && lane < 32) {
    const int q = b * 32 + lane;
    *(u32x4*)(a.ws + OFF_KV + (size_t)(q >> 2) * KVT + 1280 + (q & 3) * 336 + 320) = (u32x4){0x3f803f80u, 0x3f803f80u, 0x3f803f80u, 0x3f803f80u};
  }
  BAR();
  if (v == 1 && lane == 0) ((unsigned*)(a.ws + OFF_LAT))[b] = umin(umin(umin(latw[0], latw[4]) + umin(latw[1], latw[5]), 30000u) + umin(umin(latw[2], latw[6]) + umin(latw[3], latw[7]), 30000u), 60000u);
  if (v == 0) {
    float mx = 0.f;
#pragma unroll
    for (int k = 0; k < 3; ++k) {
      const int col = k * 64 + lane;
      float sm = 0.f;
#pragma unroll
      for (int i = 0; i < 10; ++i) sm += red[i][col < 160 ? col : 0];
      mx = fmx(mx, sm);
    }
    mx = fmx(mx, dppf<DPP_XOR1>(mx)); mx = fmx(mx, dppf<DPP_XOR2>(mx)); mx = fmx(mx, dppf<DPP_ROR4>(mx)); mx = fmx(mx, dppf<DPP_ROR8>(mx));
    mx = x32mx(x16mx(mx));
    if (lane == 0) ((float*)(a.ws + OFF_BNRM))[(cq * 32 + h) * 2 + dh] = mx;
  }
  PROT(3);
}

struct RecArgs {
  const u16* mem0; const u16* wq; const u16* wk; const u16* xold; u16* xnew; char* ws;
  unsigned layer; unsigned pad;
};
#define RLX_AGENT __ATOMIC_RELAXED, __HIP_MEMORY_SCOPE_AGENT
constexpr unsigned SPIN_CAP = 1u << 20;
#define TSTAMP(k, v)
#define ARRIVE(k)
#define ARRIVE_OUT()
#define ARRIVE_DECL()
#define LOGV(k, v)
#define LOGF(k, v)
#define LOGP(k)
#define LOGQ(k)
#define LOGF2(k)
#define LOGD(k)
#define LOGT(k, v)
#define STAMP_DECL()
#define STAMP(k)
#define STAMPV(k, v)
#define STAMP_OUT()
#define REC_D1 520
#define SCH_V 18
#define SCH_VMIN 0
#define SCH_DMIN 4
#define SCH_PEN 170
#define SCH_VMIN2 (-4)
#define SCH_PEN2 40
#define SCH_GU 1
#define SCH_GD 2
#define SCH_UP 6
#define SCH_D0 196
#define SCH_FIRST 120
#define EXP_NODMA 0
#define BG_HOLD 220
#define QBATCH 0
#define WPF_N 12
#define WPF_W 0
#define WPF0 (63 - WPF_N)
#define BG_HOLD4 250
#define D1_W1 20
#define F3_D 1200
#define T2W 2
#define AT_ASM 1
#define PM_Z 1
#define P2_X 1
#define ST_KS 14
#define ST_KS0 ST_KS
#define ST_KS1 ST_KS
#define PADR 0x6666
#define PADT 0x000
#define CODE_PAD(n) asm volatile(".p2align 6\n.rept %0\ns_nop 0\n.endr" :: "n"(n))
#define ST_X 320
#define ST_ALL 4
#define CPF_S 61
#define FR_D 700
#define FR_PACE 6
#define END_E60 600
#define END_E1 1300
#define END_E2 5200
#define END_E62 3600
#define END_E3 9800
#define END_E4 14500
#define EARLY_N 9
#define EARLY_D1 120
#define EARLY_V 8
#define EARLY_V1 30
#define F3 0
#define F3A 0
#define F3A_D 400
#define T4_D 300
#define T4_P 1
#define P2_W 1
#define PS_SL 1
#define F2B 4
#define F0C 0
#define F2C 0
#define W2_P 3
#define SCH_W 1
#define SP_SL 0
#define D1_M 320
#define D1_MIN 200
#define D1_C 96
#define D1_G 2
#define BG_HOLD6 0
#define W2_D 0
#define END_E61 2600
#define D1_LATE 200
#define P2_G0 10
#define P2_G2 6
#define P2_G3 6
#define SCH_RL 0
#define SCH_P 3
#define MMA_P 0
#define SP_P 0
#define F6 8
#define F7 10
#define XPF_W 2
#define SCH_LATE 0
#define LATE_V 6
#define QW 3
#define FB_SL 0
#define P2_G1 1
#define P1_G 0
#define F4Z 0
#define F5Z 0
#define T5_Z 2
#define F0A 3
#define FB_E 0
#define EXACT 1
#define D1_P 3
#define KP0 0
#define KP1 0
#define XPF 3
#define W1_D 0
#define F4 12
#define F5 0
#define F67 0
#define KP2 0
#define REC_RNG 70u
#define SCH_DRAIN 40
#define PL_X 0
#define FC(k)
#define PIN_V(x) asm volatile("" : "+v"(x))
#define PIN_S(x) asm volatile("" : "+s"(x))
#define VM_DRAIN() do { asm volatile("s_waitcnt vmcnt(0)" ::: "memory"); __builtin_amdgcn_s_waitcnt(0x0F70); } while (0)
#define SC1_LOAD(off) __builtin_amdgcn_raw_buffer_load_b128(rs, (off), 0, 16)
#define SC1_LOADS(off, soff) __builtin_amdgcn_raw_buffer_load_b128(rs, (off), (soff), 16)
#define SC1_STORE8S(v, off, soff) __builtin_amdgcn_raw_buffer_store_b64((v), rs, (off), (soff), 16)
#define SC1_LOAD8(off) __builtin_amdgcn_raw_buffer_load_b64(rs, (off), 0, 16)
#define SC1_STORE(v, off) __builtin_amdgcn_raw_buffer_store_b128((v), rs, (off), 0, 16)
#define SC1_STORE8(v, off) __builtin_amdgcn_raw_buffer_store_b64((v), rs, (off), 0, 16)
#if defined(__HIP_DEVICE_COMPILE__)
#pragma clang attribute push(__attribute__((target("no-packed-fp32-ops"))), apply_to = function)
#endif
__device__ __forceinline__ unsigned now32() { return (unsigned)__builtin_amdgcn_s_memrealtime(); }
__device__ __forceinline__ void wait_until(unsigned t) { for (;;) { const int d = (int)(t - now32()); if (d <= 0) break; if (d > 8) __builtin_amdgcn_s_sleep(1); } }
__device__ __forceinline__ unsigned cyc32() { return (unsigned)__builtin_amdgcn_s_memtime(); }
__device__ __forceinline__ void wait_cyc(unsigned t) { for (;;) { const int d = (int)(t - cyc32()); if (d <= 0) break; if (d > 200) __builtin_amdgcn_s_sleep(1); } }
#define WAIT_ASM(clk, far_) "1:\n\t" clk " vcc\n\ts_waitcnt lgkmcnt(0)\n\ts_sub_i32 %0, %1, vcc_lo\n\ts_cmp_lt_i32 %0, " far_ "\n\ts_cbranch_scc1 3f\n\ts_sleep 1\n\ts_branch 1b\n" \
                            "2:\n\t" clk " vcc\n\ts_waitcnt lgkmcnt(0)\n\ts_sub_i32 %0, %1, vcc_lo\n3:\n\ts_cmp_gt_i32 %0, 0\n\ts_cbranch_scc1 2b"
__device__ __forceinline__ int spin_until(unsigned t) { int d; asm volatile(WAIT_ASM("s_memrealtime", "9") : "=&s"(d) : "s"(t) : "vcc", "scc", "memory"); return d; }
__device__ __forceinline__ int spin_cyc(unsigned t) { int d; asm volatile(WAIT_ASM("s_memtime", "200") : "=&s"(d) : "s"(t) : "vcc", "scc", "memory"); return d; }
__device__ __forceinline__ void spin_exact(unsigned t) {
  if (!EXACT) { spin_cyc(t); return; }
  int d;
  asm volatile("1:\n\ts_memtime vcc\n\ts_waitcnt lgkmcnt(0)\n\ts_sub_i32 %0, %1, vcc_lo\n\ts_cmp_gt_i32 %0, 95\n\ts_cbranch_scc1 1b\n\t"
               "s_max_i32 %0, %0, 0\n\ts_andn2_b32 %0, %0, 3\n\ts_sub_i32 %0, 3f-2f, %0\n\ts_getpc_b64 vcc\n"
               "2:\n\ts_add_u32 vcc_lo, vcc_lo, %0\n\ts_addc_u32 vcc_hi, vcc_hi, 0\n\ts_setpc_b64 vcc\n\t.rept 24\n\ts_nop 0\n\t.endr\n3:"
               : "=&s"(d) : "s"(t) : "scc", "vcc", "memory");
}
template <int OFF, int SL> __device__ __forceinline__ void lds_spin(unsigned lds0, unsigned want) {
  unsigned v;
  asm volatile("1:\n\tds_read_b32 %0, %1 offset:%3\n\t.if %4\n\ts_sleep %4\n\t.endif\n\ts_waitcnt lgkmcnt(0)\n\tv_cmp_gt_u32 vcc, %2, %0\n\ts_cbranch_vccnz 1b"
               : "=&v"(v) : "v"(lds0), "s"(want), "n"(OFF), "n"(SL) : "vcc", "memory");
}
template <int CTRL> __device__ __forceinline__ int dppi(int v) { return __builtin_amdgcn_mov_dpp(v, CTRL, 0xf, 0xf, true); }
__device__ __forceinline__ int wave_imax(int l) {
  l = imax(l, dppi<DPP_XOR1>(l)); l = imax(l, dppi<DPP_XOR2>(l)); l = imax(l, dppi<DPP_ROR4>(l)); l = imax(l, dppi<DPP_ROR8>(l));
  int a = imax(__builtin_amdgcn_readlane(l, 0), __builtin_amdgcn_readlane(l, 16)), b = imax(__builtin_amdgcn_readlane(l, 32), __builtin_amdgcn_readlane(l, 48));
  PIN_S(a); PIN_S(b);
  return imax(a, b);
}
__device__ __forceinline__ void glds16(const void* gsrc, unsigned lds_dst) {
  unsigned keep;
  asm volatile("s_mov_b32 %0, m0\n\ts_mov_b32 m0, %2\n\ts_nop 0\n\tglobal_load_lds_dwordx4 %1, off\n\ts_mov_b32 m0, %0" : "=&s"(keep) : "v"(gsrc), "s"(lds_dst) : "memory");
}
__device__ __forceinline__ void glds16_sc1(const void* gsrc, unsigned lds_dst) {
  unsigned keep;
  asm volatile("s_mov_b32 %0, m0\n\ts_mov_b32 m0, %2\n\ts_nop 0\n\tglobal_load_lds_dwordx4 %1, off sc1\n\ts_mov_b32 m0, %0" : "=&s"(keep) : "v"(gsrc), "s"(lds_dst) : "memory");
}
__device__ __forceinline__ unsigned acc_off(int G, int row) {
  return (unsigned)(G < 128 ? ((G & 31) | ((G & 32) << 1) | ((G & 64) >> 1)) * 256 + row * 16 : (2 * G - 128 + (row >> 2)) * 256 + (row & 3) * 16);
}
__device__ __forceinline__ unsigned nz(unsigned v) { return v > 1u ? v : 1u; }
__device__ __forceinline__ unsigned pack2n(float lo, float hi) { return nz(pack2(lo, hi)); }
__device__ __forceinline__ unsigned min4(u32x4 v) { return umin(umin(v[0], v[1]), umin(v[2], v[3])); }
__device__ __forceinline__ bf16x8 as8(u32x4 v) { return __builtin_bit_cast(bf16x8, v); }

constexpr int NV = 7, NKQ = 2;
constexpr int L_RED1 = 0;
constexpr int L_BM = L_RED1 + 8 * 1536;
constexpr int L_STG = L_BM + 10240;
constexpr int L_ST = L_STG + 2048 + 2560;
constexpr int L_MISC = L_ST + 512;
constexpr int L_KX = L_MISC + 128;
constexpr int L_QX = L_KX + NKQ * 2048;
constexpr int L_XN = L_QX + NKQ * 2048;
constexpr int L_VOX = L_XN + 81920;
constexpr int L_VOM = L_VOX + NV * 5120;
constexpr int L_BYTES = L_VOM + NV * 1280;
constexpr int S_IMG = L_XN + 40960;
static_assert(S_IMG + 61440 <= L_VOM, "start-up image");
static_assert(L_BYTES <= 160 * 1024, "LDS budget");

__device__ __forceinline__ bf16x8 wqk_frag(const char* lds, int t, int ks, int lane) {
  const int c = lane & 15, g = lane >> 4, p = c & 3;
  const char* q = lds + S_IMG + (32 * ks + 8 * g + (c >> 2)) * 96 + 8 * p + (t == 0 ? 0 : (t == 1 ? 48 : (p < 2 ? 32 : 64)));
  const v4i16_t lo = __builtin_amdgcn_ds_read_tr16_b64_v4i16((LAS v4i16_t*)q), hi = __builtin_amdgcn_ds_read_tr16_b64_v4i16((LAS v4i16_t*)(q + 4 * 96));
  return (bf16x8){lo[0], lo[1], lo[2], lo[3], hi[0], hi[1], hi[2], hi[3]};
}
__device__ __forceinline__ bf16x8 sel_frag(int c, int base) {
  bf16x8 r;
#pragma unroll
  for (int i = 0; i < 8; ++i) r[i] = (c == base + i) ? (short)0x3f80 : (short)0;
  return r;
}
__device__ __forceinline__ bf16x8 qk_frag(const char* p4, const char* p2, const char* p2b) {
  f32x4 a0 = *(const f32x4*)(p4), a1 = *(const f32x4*)(p4 + 1792), a2 = *(const f32x4*)(p4 + 3584), a3 = *(const f32x4*)(p4 + 5376);
  f32x2 b0 = *(const f32x2*)(p2), b1 = *(const f32x2*)(p2 + 1792), b2 = *(const f32x2*)(p2b), b3 = *(const f32x2*)(p2b + 1792);
  f32x4 va = (a0 + a1) + (a2 + a3);
  f32x2 vb = (b0 + b1) + (b2 + b3);
  return mk8(pack2(va[0], va[1]), pack2(va[2], va[3]), pack2(vb[0], vb[1]), 0u);
}
__device__ __forceinline__ void x_fetch(const u16* src, unsigned lds0, int seg, int p, int lane) {
  if (EXP_NODMA) return;
  glds16_sc1(src + (size_t)p * 512 + lane * 8, lds0 + L_XN + (seg & 1) * 40960 + p * 1024);
}
template <int N> __device__ __forceinline__ void x_fetch_n(const u16* src, unsigned lds0, int seg, int p0, unsigned voff) {
  if (EXP_NODMA) return;
#pragma unroll
  for (int i = 0; i < N; i += 4) {
    unsigned keep;
    asm volatile("s_mov_b32 %0, m0\n\ts_mov_b32 m0, %3\n\ts_nop 0\n\tglobal_load_lds_dwordx4 %1, %2 sc1\n\t"
                 ".if %4 > 1\n\tglobal_load_lds_dwordx4 %1, %2 offset:1024 sc1\n\t.endif\n\t.if %4 > 2\n\tglobal_load_lds_dwordx4 %1, %2 offset:2048 sc1\n\t.endif\n\t"
                 ".if %4 > 3\n\tglobal_load_lds_dwordx4 %1, %2 offset:3072 sc1\n\t.endif\n\ts_mov_b32 m0, %0"
                 : "=&s"(keep) : "v"(voff), "s"(src + (size_t)(p0 + i) * 512), "s"(lds0 + L_XN + (seg & 1) * 40960 + (p0 + i) * 1024), "n"(N - i < 4 ? N - i : 4) : "memory");
  }
}
__device__ __forceinline__ int qkv_row(int b) { return 2 * (b & 7) + ((b >> 3) >= 15); }
__device__ __forceinline__ const u16* x_src(const RecArgs& a, int seg, int b) {
  return seg < NSEG ? (const u16*)(a.ws + OFF_XN) + (size_t)seg * 32 * DM : a.xnew + (size_t)(qkv_row(b) + 16 * (seg - NSEG)) * 32 * DM;
}
template <int N>
__device__ __forceinline__ void img_fetch(const RecArgs& a, unsigned lds0, int h, int j, int part, int lane) {
#pragma unroll
  for (int i = 0; i < N; ++i) {
    const int n = part * N + i, idx = n * 64 + lane, row = idx / 6, pc = idx - row * 6;
    if (FR_PACE && N == 20) asm volatile("s_waitcnt vmcnt(%0)" :: "n"(FR_PACE) : "memory");
    glds16((pc < 3 ? a.wq : a.wk - 24) + (size_t)row * HD + h * DH + j * 24 + pc * 8, lds0 + S_IMG + n * 1024);
  }
}
constexpr int P2B = F4 + F5 + 2 * F67 + F6 + F7, PZ = P2B + F2B, PA = PZ + F4Z + F5Z, P0C = PA + 2 * F0A, F3C = 40 - F3A - F3 - P0C - 2 * F0C - F2C;
static_assert(F3C >= 0, "fetch plan");
template <int OFF> __device__ __forceinline__ void lds_inc(unsigned lds0) {
  asm volatile("s_mov_b64 exec, 1\n\tds_add_u32 %0, %1 offset:%2\n\ts_mov_b64 exec, -1" :: "v"(lds0), "v"(1u), "n"(OFF) : "memory");
}
__device__ __forceinline__ void lds_arrive(unsigned* p, int lane) {
  asm volatile("s_waitcnt lgkmcnt(0)" ::: "memory");
  if (lane == 0) __hip_atomic_fetch_add(p, 1u, __ATOMIC_RELAXED, __HIP_MEMORY_SCOPE_WORKGROUP);
}
__device__ __forceinline__ void lds_wait(unsigned* p, unsigned want) {
  while (__hip_atomic_load(p, __ATOMIC_RELAXED, __HIP_MEMORY_SCOPE_WORKGROUP) < want) __builtin_amdgcn_s_sleep(1);
  asm volatile("" ::: "memory");
}
constexpr float KEXP = 0.07216878364870322f * 1.4426950408889634f;
#define FRESH_LANE() int lane = lane0; asm volatile("" : "+v"(lane)); const int c = lane & 15, g = lane >> 4; (void)c; (void)g

struct OutTask {
  u32x4 y[4]; u32x2 xo, out;
  bf16x8 sel;
  unsigned t5f, t5o;
  __device__ __forceinline__ void init(int b, int lane) {
    const int um = b / 10, uq = (b / 5) & 1, up = b % 5, tok = lane & 15, k = up < 4 ? (up >> 1) * 256 + um * 16 + (up & 1) * 8 : 512 + um * 8, q4 = ((lane >> 4) & 1) * 4;
    t5f = (unsigned)(((uq * 20 + (k >> 5)) * 64 + ((k & 31) >> 3) * 16 + tok) * 8 + q4);
    t5o = (unsigned)(((um * 2 + uq) * 16 + (lane >> 4)) * 1280 + up * 256 + tok * 16);
    if (b >= 160) { t5f = 0u; t5o = 0x7ffffff0u; }
    sel = sel_frag(tok, 0);
    PIN_V(sel);
  }
  static __device__ __forceinline__ bool ok(int sl, int b) { return sl >= 0 && b < 160; }
  __device__ __forceinline__ void load(const RecArgs& a, const __amdgpu_buffer_rsrc_t rs, int sl, int b, int part = 3) {
    const unsigned o = t5o | (sl < 0 ? 0x7ff00000u : 0u);
    sl = imax(sl, 0);
#pragma unroll
    for (int k = 0; k < 4; ++k) if (part & 1) y[k] = SC1_LOADS(o, op_slot(sl) + k * 5120);
    if (part & 2) xo = __builtin_bit_cast(u32x2, __hip_atomic_load((const gu64*)(a.xold + (size_t)sl * 32 * DM + t5f), RLX_AGENT));
  }
  __device__ __forceinline__ bool ready(int b) { return b >= 160 || wave_all(umin(umin(min4(y[0]), min4(y[1])), umin(min4(y[2]), min4(y[3]))) != 0u); }
  __device__ __forceinline__ void run(int lane) {
    f32x4 d = {0.f, 0.f, 0.f, 0.f};
#pragma unroll
    for (int k = 0; k < 4; ++k) d = mfma32(sel, as8(y[k]), d);
#pragma unroll
    for (int i = 0; i < 2; ++i) {
      const unsigned r = pack2(d[2 * i], d[2 * i + 1]), x = xo[i];
      out[i] = pack2(u2f(x << 16) + u2f(r << 16), u2f(x & 0xffff0000u) + u2f(r & 0xffff0000u));
    }
    PIN_V(out);
  }
  __device__ __forceinline__ void store(const RecArgs& a, int sl, int b, int lane) {
    if (ok(sl, b) && lane < 32) __hip_atomic_store((gu64*)(a.xnew + (size_t)sl * 32 * DM + t5f), (u64)out[0] | ((u64)out[1] << 32), RLX_AGENT);
  }
};

struct PvTask {
  u32x4 y[2]; u32x2 xo[2];
  __device__ __forceinline__ void load(const __amdgpu_buffer_rsrc_t rs, unsigned ck, int sl, int gp, int qt, int lane) {
    const unsigned o = pt_slot(ck, sl);
#pragma unroll
    for (int k = 0; k < 2; ++k) {
      y[k] = SC1_LOAD(o + pt_tok(2 * gp) + k * 2048 + qt * 1024 + lane * 16);
      xo[k] = SC1_LOAD8(o + pt_mem(2 * gp) + k * 512 + qt * 256 + (lane & 31) * 8);
    }
  }
  __device__ __forceinline__ bool ready() { return wave_all(umin(umin(min4(y[0]), min4(y[1])), umin(umin(xo[0][0], xo[0][1]), umin(xo[1][0], xo[1][1]))) != 0u); }
  __device__ __forceinline__ void run(char* lds, const __amdgpu_buffer_rsrc_t rs, int sl, int gp, int m, int qt, int lane, bool direct) {
    const f32x4 Z4 = {0.f, 0.f, 0.f, 0.f};
    PIN_V(y[0]); PIN_V(y[1]); PIN_V(xo[0]); PIN_V(xo[1]);
    const int sv = (sl + 2 * NV) % NV;
    const char* vp = lds + L_VOX + sv * 5120 + lane * 16;
    const char* mp = lds + L_VOM + sv * 1280 + (lane & 31) * 8;
    bf16x8 pm[2], vm[5], vx[5];
#pragma unroll
    for (int t = 0; t < 5; ++t) { vx[t] = *(const bf16x8*)(vp + t * 1024); const u32x2 v = *(const u32x2*)(mp + t * 256); vm[t] = mk8(v[0], v[1], 0u, 0u); }
#pragma unroll
    for (int k = 0; k < 2; ++k) pm[k] = mk8(lane < 32 ? xo[k][0] : 0u, lane < 32 ? xo[k][1] : 0u, 0u, 0u);
    __builtin_amdgcn_sched_barrier(0);
    f32x4 o[4];
#pragma unroll
    for (int k = 0; k < 2; ++k) { o[k] = mfma32(vx[k], as8(y[0]), Z4); o[2 + k] = mfma32(vx[2], as8(y[k]), Z4); }
#pragma unroll
    for (int k = 0; k < 2; ++k) { o[k] = mfma32(vm[k], pm[0], o[k]); o[2 + k] = mfma32(vm[2], pm[k], o[2 + k]); }
#pragma unroll
    for (int k = 0; k < 2; ++k) o[k] = mfma32(vx[3 + k], as8(y[1]), o[k]);
#pragma unroll
    for (int k = 0; k < 2; ++k) o[k] = mfma32(vm[3 + k], pm[1], o[k]);
    float oc[4];
#pragma unroll
    for (int r = 0; r < 4; ++r) {
      auto sw = __builtin_amdgcn_permlane32_swap(f2u(o[3][r]), f2u(o[3][r]), false, false);
      oc[r] = o[2][r] + u2f(sw[1]);
    }
    const u32x2 v0 = {pack2n(o[0][0], o[0][1]), pack2n(o[0][2], o[0][3])}, v1 = {pack2n(o[1][0], o[1][1]), pack2n(o[1][2], o[1][3])};
    const u32x2 v2 = {pack2n(oc[0], oc[1]), pack2n(oc[2], oc[3])};
    const unsigned po = (unsigned)(((lane >> 5) * 16 + (lane & 15)) * 16 + ((lane >> 4) & 1) * 8);
    if (direct) {
      const unsigned oo = op_slot(sl) + (unsigned)((m * 32 + qt * 16 + gp) * 1280) + po;
      SC1_STORE8(v0, oo); SC1_STORE8(v1, oo + 512);
      if (lane < 32) SC1_STORE8(v2, oo + 1024);
    } else {
      char* sp = lds + L_STG + 2048 + qt * 1280 + po;
      *(u32x2*)(sp) = v0; *(u32x2*)(sp + 512) = v1;
      if (lane < 32) *(u32x2*)(sp + 1024) = v2;
    }
  }
};

struct PvTask2 {
  u32x4 y[2][2], pa[2], pb[2];
  unsigned lo, lm, vr, ma, mz, so, sz;
  __device__ __forceinline__ void init(int gp, int lane) {
    const int hi = lane >> 5;
    lo = pt_tok(2 * gp) + (unsigned)(lane * 16); lm = pt_mem(2 * gp + hi) + (unsigned)((lane & 31) * 8);
    vr = (unsigned)(L_VOX + 2048 + (lane ^ 8) * 16);
    ma = (unsigned)(L_VOM + hi * 768 + (lane & 31) * 8); mz = (unsigned)(L_VOM + 512 + ((lane & 31) ^ (hi * 8)) * 8);
    so = (unsigned)(L_STG + 2048 + (hi * 16 + (lane & 15)) * 16 + ((lane >> 4) & 1) * 8);
    sz = hi ? (unsigned)(L_RED1 + 8704 + lane * 8) : so + 1024;
    PIN_V(lo); PIN_V(lm); PIN_V(vr); PIN_V(ma); PIN_V(mz); PIN_V(so); PIN_V(sz);
#pragma unroll
    for (int q = 0; q < 2; ++q) { pa[q] = pb[q] = y[q][0] = y[q][1] = (u32x4){0u, 0u, 0u, 0u}; PIN_V(pa[q]); PIN_V(pb[q]); }
  }
  __device__ __forceinline__ void load(const __amdgpu_buffer_rsrc_t rs, unsigned o) {
#pragma unroll
    for (int q = 0; q < 2; ++q) {
      y[q][0] = SC1_LOADS(lo + q * 1024, o); y[q][1] = SC1_LOADS(lo + 2048 + q * 1024, o);
      const u32x2 p = __builtin_amdgcn_raw_buffer_load_b64(rs, lm + q * 256, o, 16);
      pa[q][0] = p[0]; pa[q][1] = p[1];
    }
  }
  __device__ __forceinline__ void run(char* lds, int sv, int lane) {
    const f32x4 Z4 = {0.f, 0.f, 0.f, 0.f};
    const char* vp = lds + L_VOX + sv * 5120 + lane * 16;
    bf16x8 vx[6];
#pragma unroll
    for (int t = 0; t < 5; ++t) vx[t] = *(const bf16x8*)(vp + t * 1024);
    vx[5] = *(const bf16x8*)(lds + vr + sv * 5120);
    const u32x2 a0 = *(const u32x2*)(lds + ma + sv * 1280), a1 = *(const u32x2*)(lds + ma + sv * 1280 + 256);
    const u32x2 z0 = *(const u32x2*)(lds + mz + sv * 1280), z1 = *(const u32x2*)(lds + mz + sv * 1280 + 256);
    const bf16x8 vm = mk8(a0[0], a0[1], a1[0], a1[1]), vz = mk8(z0[0], z0[1], z1[0], z1[1]);
#pragma unroll
    for (int q = 0; q < 2; ++q) { PIN_V(pa[q]); pb[q][2] = pa[q][0]; pb[q][3] = pa[q][1]; PIN_V(pb[q]); }
    __builtin_amdgcn_sched_barrier(0);
    f32x4 o[2][3];
#pragma unroll
    for (int q = 0; q < 2; ++q) {
#pragma unroll
      for (int t = 0; t < 3; ++t) o[q][t] = mfma32(vx[t], as8(y[q][0]), Z4);
#pragma unroll
      for (int t = 0; t < 3; ++t) o[q][t] = mfma32(vx[3 + t], as8(y[q][1]), o[q][t]);
      o[q][0] = mfma32(vm, as8(pa[q]), o[q][0]); o[q][1] = mfma32(vm, as8(pb[q]), o[q][1]); o[q][2] = mfma32(vz, as8(pa[q]), o[q][2]);
    }
#pragma unroll
    for (int q = 0; q < 2; ++q) {
#pragma unroll
      for (int t = 0; t < 3; ++t)
        *(u32x2*)(lds + (t < 2 ? so + t * 512 : sz) + q * 1280) = (u32x2){pack2n(o[q][t][0], o[q][t][1]), pack2n(o[q][t][2], o[q][t][3])};
    }
  }
};

struct TokScores {
  bf16x8 k[2], q[2]; u32x4 v[2];
  __device__ __forceinline__ void get(const char* lds, int seg, int lane) {
#pragma unroll
    for (int i = 0; i < 2; ++i) { k[i] = *(const bf16x8*)(lds + L_KX + (seg & 1) * 2048 + i * 1024 + lane * 16); q[i] = *(const bf16x8*)(lds + L_QX + (seg & 1) * 2048 + i * 1024 + lane * 16); }
  }
  __device__ __forceinline__ void run(int qt) {
    const f32x4 Z4 = {0.f, 0.f, 0.f, 0.f};
    const f32x4 d1 = mfma32(k[0], q[qt], Z4), d2 = mfma32(k[1], q[qt], Z4);
    v[qt] = (u32x4){pack2n(d1[0], d1[1]), pack2n(d1[2], d1[3]), pack2n(d2[0], d2[1]), pack2n(d2[2], d2[3])};
    PIN_V(v[qt]);
  }
  __device__ __forceinline__ void store(const __amdgpu_buffer_rsrc_t rs, unsigned off, unsigned o) {
    __builtin_amdgcn_raw_buffer_store_b128(v[0], rs, off, o, 16); __builtin_amdgcn_raw_buffer_store_b128(v[1], rs, off + 1024, o, 16);
    asm volatile("s_nop 1" ::: "memory");
  }
};

__device__ __forceinline__ void touch(const char* base, unsigned off, unsigned lds0) {
  unsigned keep;
  asm volatile("s_mov_b32 %0, m0\n\ts_mov_b32 m0, %3\n\ts_nop 0\n\tglobal_load_lds_dword %1, %2 sc1\n\ts_mov_b32 m0, %0" : "=&s"(keep) : "v"(off), "s"(base), "s"(lds0 + L_RED1 + 8192) : "memory");
}
template <int N> __device__ __forceinline__ void stouch(const char* p, unsigned& d) {
  asm volatile(".set pf_o, 0\n\t.rept %2\n\ts_load_dword %0, %1, pf_o\n\t.set pf_o, pf_o + 128\n\t.endr" : "+s"(d) : "s"(p), "n"(N) : "memory");
}
__device__ __forceinline__ void code_touch(int b, int lane, unsigned lds0) {
  const char* pc; int d0, d1;
  asm volatile("s_getpc_b64 %0\n\ts_mov_b32 %1, _Z10rec_kernel7RecArgs@rel32@lo+4\n\ts_mov_b32 %2, _Z11attb_kernelPKtS0_Pt@rel32@lo+12" : "=s"(pc), "=s"(d0), "=s"(d1));
  const int o = (lane * 32 + (b >> 3)) * 128;
  touch(pc + d0, (unsigned)(o < d1 - d0 ? o : 0), lds0);
}
__device__ __forceinline__ unsigned zero_base(int b, int h, int j, int ln) {
  return ln < 32 ? (unsigned)((h & 3) * 32768 + j * 512 + ln * 16) : ln < 40 ? (unsigned)((h & 2) * 1024 + j * 256 + (ln & 4) * 32 + (h & 1) * 64 + (ln & 3) * 16)
                 : (unsigned)OFF_ACC + (b < ACC_NB ? (unsigned)(b * 256 + (ln - 40) * 16) : ts_piece((b - ACC_NB) * 8 + ln - 40));
}
__device__ __forceinline__ unsigned sct_slot(unsigned ck, unsigned sl) { return sl * SCT_STEP + ((ck >> (sl & 4u)) & 7u) * 4096u; }
__device__ __forceinline__ unsigned scm_slot(unsigned ck, unsigned sl) { return OFF_SCM + sl * SCM_STEP + ((ck >> 8) & 7u) * 4096u; }
__device__ __forceinline__ int ck_rot(int x) { return ((x & 1) << 2) | (x >> 1); }
struct Place {
  unsigned t[8];
  __device__ __forceinline__ void load(const char* ws, int lane) {
#pragma unroll
    for (int x = 0; x < 8; ++x) t[x] = *(const unsigned*)(ws + OFF_LAT + ((imin(lane >> 3, 5) * 8 + (ck_rot(x) ^ (lane & 7))) * 8 + x) * 4);
  }
  __device__ __forceinline__ unsigned get(int b, int lane) {
    int c = (int)((((t[0] + t[1]) + (t[2] + t[3])) + ((t[4] + t[5]) + (t[6] + t[7]))) * 8u) + (lane & 7);
    c = imin(c, dppi<DPP_XOR1>(c)); c = imin(c, dppi<DPP_XOR2>(c)); c = imin(c, dppi<0x141>(c));
    unsigned k = 0u;
#pragma unroll
    for (int r = 0; r < 6; ++r) k |= (unsigned)(__builtin_amdgcn_readlane(c, 8 * r) & 7) << (4 * r);
    return (k ^ PL_X) ^ (unsigned)ck_rot(b & 7) * 0x111111u;
  }
};

struct Sched {
  u32x4 tsv, rec;
  unsigned side;
  __device__ __forceinline__ void load(const __amdgpu_buffer_rsrc_t rs, int s, int lane) {
    const unsigned o = (unsigned)OFF_ACC + (unsigned)((s + SR - 1) % SR) * (unsigned)ACC_STEP + ACC_TS + side;
    tsv = SC1_LOADS(ts_piece(lane & 63) - ACC_TS, o); rec = SC1_LOADS(128, o);
  }
  __device__ __forceinline__ unsigned last(unsigned ref) {
    const unsigned o = 0x40000000u - ref;
    int l = (int)umax(umax(tsv[0] ? tsv[0] + o : 0u, tsv[1] ? tsv[1] + o : 0u), umax(tsv[2] ? tsv[2] + o : 0u, tsv[3] ? tsv[3] + o : 0u));
    return (unsigned)wave_imax(l) - o;
  }
  __device__ __forceinline__ unsigned run(const RecArgs& a, const __amdgpu_buffer_rsrc_t rs, int s, int b, int lane) {
    constexpr int w = 3; (void)w;
    unsigned acur = __builtin_amdgcn_readfirstlane(rec[0]), aprev = __builtin_amdgcn_readfirstlane(rec[1]), hist = __builtin_amdgcn_readfirstlane(rec[3]);
    int D8 = (int)__builtin_amdgcn_readfirstlane(rec[2]);
    const int d = wave_imax((int)(umax(umax(tsv[0], tsv[1]), umax(tsv[2], tsv[3])) - aprev)), dd = 8 * d - D8;
    int Dn = D8 + (dd >> (dd > 0 ? SCH_GU : SCH_GD));
    unsigned anext = (acur + (unsigned)((Dn + 4) >> 3) + SCH_V + (s < EARLY_N ? EARLY_V : (s >= WPF0 ? LATE_V : 0))) | 1u;
    if (__builtin_expect(hist < 2u || aprev == 0u || d < 1 || d + SCH_VMIN > (int)(acur - aprev), 0)) {
      if (s == 0) {
        for (int it = 0; it < 40 && !wave_all(min4(tsv) != 0u); ++it) {
          __builtin_amdgcn_s_sleep(ST_ALL);
          asm volatile("" ::: "memory");
          load(rs, s, lane);
        }
      }
      const unsigned now = now32(), L = last(now);
      if (acur == 0u) { D8 = 8 * SCH_D0; hist = 0u; }
      const bool late = acur == 0u || (int)(L + SCH_VMIN - acur) > 0;
      if (late) { if (b == 0) FC(3); if (acur) D8 += 8 * (hist ? SCH_UP : 3 * SCH_UP); }
      else if (aprev) { if (hist < 2u) D8 = 8 * imin(imax((int)(L - aprev), 1), 4000); hist = umin(hist + 1u, 2u); }
      const unsigned base = acur == 0u ? L + SCH_FIRST : (late ? L + SCH_V + ((int)(L + SCH_VMIN2 - acur) <= 0 ? SCH_PEN2 : SCH_PEN) : acur);
      anext = (base + (unsigned)((D8 + 4) >> 3) + SCH_V + (s == 1 ? EARLY_V1 : (s < EARLY_N ? EARLY_V : 0))) | 1u;
      if (s == 0) anext = (L + ST_X) | 1u;
      if ((int)(now - L) > 50000) anext = 0u;
      if (late) acur = 0u;
      Dn = D8;
    }
    if (__builtin_expect(b == 0, 0))
      __builtin_amdgcn_raw_buffer_store_b128((u32x4){anext, acur, (unsigned)Dn, hist}, rs, lane < 2 ? lane * 8192 : 0x7ffffff0, (unsigned)OFF_ACC + (unsigned)(s % SR) * (unsigned)ACC_STEP + ACC_TS + 128u, 16);
    LOGV(0, anext); LOGV(1, acur == 0u); LOGV(2, (Dn + 4) >> 3); LOGV(3, d); LOGV(4, acur - aprev - d); LOGV(5, aprev);
    return anext;
  }
};

template <int w>
__device__ __forceinline__ void token_waves(const RecArgs& a, char* lds, const __amdgpu_buffer_rsrc_t rs) {
  const int lane0 = __builtin_amdgcn_workitem_id_x() & 63, b = __builtin_amdgcn_workgroup_id_x(), wid = w + 4; (void)wid;
  const int gp = (b & 7) * 2 + (b >> 7), m = (b >> 3) & 15, h0 = 2 * gp, h = h0 + (m >> 3), j = m & 7;
  float* const misc = (float*)(lds + L_MISC);
  const f32x4 Z4 = {0.f, 0.f, 0.f, 0.f};
  const unsigned lds0 = (unsigned)(uintptr_t)(LAS char*)lds;
  unsigned* const cnt = (unsigned*)(misc + 6);
  const float* const xnrm = (const float*)(a.ws + OFF_XNRM);
  bf16x16 W[2][10];
  auto btile = [&](int i) { return w == 1 ? 0 : (w == 2 ? 1 + i : 3 + i); };
  if (w == 1) code_touch(b, lane0, lds0);
  Place pl;
  if (w == 3) pl.load(a.ws, lane0);
  if (a.layer) {
    if (w != 0) img_fetch<20>(a, lds0, h, j, w - 1, lane0);
  } else {
    if (w == 0) img_fetch<12>(a, lds0, h, j, 4, lane0);
#pragma unroll
    for (int i = 0; i < 10; ++i) x_fetch(x_src(a, 0, b), lds0, 0, 10 * w + i, lane0);
  }
#pragma unroll
  for (int i = 0; i < 2; ++i)
#pragma unroll
    for (int p = 0; p < 10; ++p)
      if (w >= 2 || (w == 1 && i == 1)) {
        const char* wp = a.ws + OFF_WB + ((size_t)(b * 5 + btile(i)) * 20 + 2 * p) * 1024 + lane0 * 16;
        if (FR_PACE && a.layer) asm volatile("s_waitcnt vmcnt(%0)" :: "n"(FR_PACE) : "memory");
        W[i][p] = cat16(*(const bf16x8*)wp, *(const bf16x8*)(wp + 1024));
      }
  f32x4 bn = Z4;
  if (w == 0) {
    bn = *(const f32x4*)(a.ws + OFF_BNRM + lane0 * 16);
    if (!a.layer) asm volatile("s_waitcnt vmcnt(0)" ::: "memory");
  } else asm volatile("s_waitcnt vmcnt(%0)" :: "n"(w == 1 ? 20 : 40) : "memory");
  unsigned ck = 0u;
  if (w == 3) { ck = pl.get(b, lane0); *(unsigned*)(misc + 11) = ck; }
  BAR();
#pragma unroll
  for (int p = 0; p < 10; ++p) {
    if (w < 2) W[0][p] = cat16(wqk_frag(lds, 2 * w, 2 * p, lane0), wqk_frag(lds, 2 * w, 2 * p + 1, lane0));
    if (w == 0) W[1][p] = cat16(wqk_frag(lds, 1, 2 * p, lane0), wqk_frag(lds, 1, 2 * p + 1, lane0));
  }
  float cb2 = 0.f;
  if (w == 0) {
    float n = __builtin_amdgcn_sqrtf(bn[0] + bn[1]) + __builtin_amdgcn_sqrtf(bn[2] + bn[3]);
    n += dppf<DPP_XOR1>(n); n += dppf<DPP_XOR2>(n); n += dppf<DPP_ROR4>(n); n += dppf<DPP_ROR8>(n);
    n = x32mx(x16mx(n));
    cb2 = u2f(__builtin_amdgcn_readfirstlane(f2u(1.15f * n * n)));
  }
  const float cm2 = u2f((f2u(cb2) & 0x7fffffu) | 0x40000000u);
  const int ek = imax((int)((f2u(cb2) >> 23) & 0xffu), 1) - 252;
  if (w >= 2) {
#pragma unroll
    for (int i = 0; i < 4; ++i) *(unsigned*)(lds + L_KX + (w - 2) * 4096 + i * 1024 + lane0 * 16 + 12) = 0u;
  }
  OutTask ot;
  const int zl = lane0 - 16;
  const bool zon = w == 3 && zl >= 0 && (zl < 40 || b < ACC_NB + 17);
  unsigned zo = zon ? zero_base(b, h, j, zl) + (zl < 32 ? sct_slot(ck, 0u) : (zl < 40 ? scm_slot(ck, 0u) : 0u)) : 0x7ffffff0u;
  unsigned zm = zon ? (zl < 32 ? SCT_STEP + 1u : (zl < 40 ? SCM_STEP : (unsigned)ACC_STEP)) : 0u;
  const unsigned zx = ((ck ^ (ck >> 4)) & 7u) * 4096u;
  if (w == 3) { PIN_V(zo); PIN_V(zm); }
  auto st_out = [&](int s, int lane) {
    if (w < 2) return;
    const int sl = s - 2 - LAG4;
    const char* sp = lds + L_STG + 2048 + (w & 1) * 1280;
    const u32x4 v0 = *(const u32x4*)(sp + lane * 16), v1 = *(const u32x4*)(sp + 1024 + (lane & 15) * 16), z = {0u, 0u, 0u, 0u};
    const unsigned oo = (sl < 0 ? 0x7ff00000u : op_slot(sl) + (unsigned)((m * 32 + (w & 1) * 16 + gp) * 1280)) + (unsigned)(lane * 16);
    if (w == 2) {
      ot.store(a, s - 2 - LAG5, b, lane);
      SC1_STORE(v0, oo);
      SC1_STORE(v1, lane < 16 ? oo + 1024 : 0x7ffffff0u);
    } else {
      const unsigned zs = (unsigned)(s + 2) % SR;
      unsigned za = (zo + (s < 0 ? 1024u : zs) * (zm & ~1u)) ^ ((zm & 1u) * (zs & 4u ? zx : 0u));
      za = lane < 16 ? oo + 1024 : za;
      PIN_V(za);
      SC1_STORE(v0, oo);
      u32x4 vz;
#pragma unroll
      for (int i = 0; i < 4; ++i) { vz[i] = lane < 16 ? v1[i] : z[i]; PIN_V(vz[i]); }
      SC1_STORE(vz, za);
      const unsigned xo_ = (unsigned)((s - 8) * 1024 + lane * 16);
      if (__builtin_expect((unsigned)(s - 8) < 20u, 0)) SC1_STORE(z, xo_ < X_BYTES / 256 ? (unsigned)(OFF_X + b * (X_BYTES / 256)) + xo_ : 0x7ffffff0u);
    }
  };
  STAMP_DECL(); ARRIVE_DECL();
  auto mma_seg = [&](int seg, int sv, int lane, auto pause) {
    const int c = lane & 15, g = lane >> 4;
    const char* xp = lds + L_XN + (seg & 1) * 40960 + lane * 16;
    f32x4 acc[2][2] = {{Z4, Z4}, {Z4, Z4}};
    bf16x8 x[4][2];
#pragma unroll
    for (int p = 0; p < 3; ++p) { x[p][0] = *(const bf16x8*)(xp + p * 1024); x[p][1] = *(const bf16x8*)(xp + (20 + p) * 1024); }
    unsigned eo[2];
#pragma unroll
    for (int ks = 0; ks < 20; ++ks) {
      if (ks == (w == 0 ? KP0 : (w == 1 ? KP1 : KP2))) { pause(); __builtin_amdgcn_sched_barrier(0); }
      if (ks == 17) {
#pragma unroll
        for (int i = 0; i < 2; ++i) {
          eo[i] = (unsigned)(w == 0 ? (i ? L_KX : L_QX) + (seg % NKQ) * 2048 + lane * 16
                                    : (w == 1 && i == 0 ? ((g >> 1) ? L_KX : L_QX) + (seg % NKQ) * 2048 + (2 * (g & 1) * 16 + c) * 16 + 8 : L_VOX + sv * 5120 + btile(i) * 1024 + lane * 16));
          if (i == 0 || w == 1) PIN_V(eo[i]);
        }
        if (w == 0) eo[1] = eo[0] + (L_KX - L_QX);
        if (w >= 2) eo[1] = eo[0] + 1024;
      }
      __builtin_amdgcn_sched_barrier(0);
#pragma unroll
      for (int q = 0; q < 4; ++q) {
        const int tt = 1 - (q >> 1), i = q & 1;
        const bool isqk = w == 0 || (w == 1 && i == 0);
        const bf16x8 wf = half8(W[i][ks >> 1], ks & 1);
        acc[i][tt] = isqk ? mfma32(wf, x[ks & 3][tt], acc[i][tt]) : mfma32(x[ks & 3][tt], wf, acc[i][tt]);
        __builtin_amdgcn_sched_barrier(0);
        if (q < 2 && ks + 3 < 20) x[(ks + 3) & 3][q] = *(const bf16x8*)(xp + (q * 20 + ks + 3) * 1024);
        __builtin_amdgcn_sched_barrier(0);
      }
    }
#pragma unroll
    for (int i = 0; i < 2; ++i) {
      if (w + i == 1) { const float ks_ = (w == 0 || (g >> 1)) ? KEXP : 1.0f; acc[i][0] *= ks_; acc[i][1] *= ks_; }
      const u32x4 v = {pack2(acc[i][0][0], acc[i][0][1]), pack2(acc[i][0][2], acc[i][0][3]), pack2(acc[i][1][0], acc[i][1][1]), pack2(acc[i][1][2], acc[i][1][3])};
      char* p = lds + eo[i];
      if (w == 0) { *(u32x2*)(p) = (u32x2){v[0], v[1]}; *(u32x2*)(p + 1024) = (u32x2){v[2], v[3]}; }
      else if (w == 1 && i == 0) { *(unsigned*)(p) = v[0]; *(unsigned*)(p + 256) = v[1]; *(unsigned*)(p + 1024) = v[2]; *(unsigned*)(p + 1280) = v[3]; }
      else *(u32x4*)p = v;
    }
  };
  auto vo_mem = [&](int s, int sv, unsigned t0, int lane, auto last) {
    unsigned vp = (unsigned)(L_VOM + sv * 1280 + (btile(0) * 32 + (w == 1 ? lane & 31 : lane)) * 8);
    float inv = misc[4 + (s & 1)], xmax = misc[s & 3];
    unsigned mp = (unsigned)(L_BM + lane * 16);
    int sidx = (lane & 8) ? 0xEEEE : 0x4444;
    f32x4 acc[2] = {Z4, Z4};
    int ex = 0, ei = 0;
    unsigned ip = (unsigned)(L_MISC + 16 + ((s + 1) & 1) * 4);
    PIN_V(inv); PIN_V(mp);
    if (w != 0) { PIN_V(vp); PIN_V(sidx); PIN_V(acc[1]); }
    if (w >= 2) PIN_V(acc[0]);
    __builtin_amdgcn_s_setprio(2);
    if (w >= 2 && BG_HOLD6) spin_cyc(t0);
    bf16x8 x[10];
#pragma unroll
    for (int p = 0; p < 10; ++p) x[p] = *(const bf16x8*)(lds + mp + p * 1024);
    __builtin_amdgcn_sched_barrier(0);
    if (w == 0) {
      ex = imax((int)((__builtin_amdgcn_readfirstlane(f2u(__builtin_amdgcn_sqrtf(cb2 * xmax))) >> 23) & 0xffu) + 1, 32);
      ei = (int)(__builtin_amdgcn_readfirstlane(f2u(inv)) >> 23);
      PIN_S(ex); PIN_S(ei); PIN_V(ip);
    }
    if (w < 2 && last == 0) x_fetch_n<w == 0 ? F4Z : F5Z>(x_src(a, s + 3, b), lds0, s + 3, PZ + (w ? F4Z : 0), lane * 16);
    if (w < 2) spin_exact(t0);
    STAMP(5);
    __builtin_amdgcn_sched_barrier(0);
#pragma unroll
    for (int p = 0; p < 10; ++p) {
      if (w == 0) acc[0] = mfma32(x[p], x[p], acc[0]);
#pragma unroll
      for (int i = (w == 1); i < 2 && w != 0; ++i) acc[i] = smfma64(x[p], W[i][p], acc[i], sidx);
      __builtin_amdgcn_sched_barrier(0);
    }
    STAMP(7);
    if (w != 0) {
      float v[4];
#pragma unroll
      for (int r = 0; r < 4; ++r) {
        const auto sw = __builtin_amdgcn_permlane32_swap(f2u(acc[w == 1][r]), f2u(acc[1][r]), false, false);
        v[r] = (u2f(sw[0]) + u2f(sw[1])) * inv;
      }
      *(u32x2*)(lds + vp) = (u32x2){pack2(v[0], v[1]), pack2(v[2], v[3])};
    } else {
      const int t = (int)f2u(fmx(fmx(acc[0][0], acc[0][1]), fmx(acc[0][2], acc[0][3])) * cm2);
      const int et = wave_imax(t) >> 23, em = ((et + ek) >> 1) + ei;
      int e = imax(et == 255 ? 256 : em, ex);
      PIN_S(e);
      e = imin(e, 256);
      *(unsigned*)(lds + ip) = (unsigned)(e - 31) << 23;
      const u32x3 sr = {(unsigned)(285 - e) << 23, (unsigned)(e - 87) < 79u ? REC_RNG << 23 : 0u, (unsigned)(s + 1)};
      asm volatile("ds_write_b96 %0, %1 offset:%2" :: "v"(lds0), "v"(sr), "n"(L_MISC + 32) : "memory");
    }
    if (last == 0 && w >= 2) asm volatile("s_waitcnt vmcnt(%0)" :: "n"(w == SCH_W ? 2 : (w == 2 && T5_Z == 1 ? 6 : 0)) : "memory");
    if (w != 0) lds_inc<L_MISC + 28>(lds0);
    STAMP(8);
    if (last) __builtin_amdgcn_s_setprio(0);
    if (last == 0) { if (w >= 2 && F67) lds_inc<L_MISC + 112>(lds0); }
    else if (w == 2) { VM_DRAIN(); lds_inc<L_MISC + 28>(lds0); }
  };
  unsigned* const cntq = (unsigned*)(misc + 12);
  unsigned* const cntt = (unsigned*)(misc + 26 + (w & 1));
  unsigned* const cntp = (unsigned*)(misc + 23);
  unsigned* const cntx2 = (unsigned*)(misc + 28);
  float xn4 = 0.f;
  auto wpf = [&](int s, int lane) {
    touch(a.ws + OFF_WQKVBT + (size_t)(b >> 3) * 76800 + (s - WPF0) * (76800 / WPF_N), imin(lane, 600 / WPF_N - 1) * 128, lds0);
  };
  auto t5_get = [&](int s, int lane) {
    ot.load(a, rs, s - LAG5 - (T5_Z == 2), b);
    xn4 = u2f(__hip_atomic_load((const GAS unsigned*)xnrm + (imax(s + 1, 0) * 32 + (lane & 31)) * 4, RLX_AGENT));
    __builtin_amdgcn_sched_barrier(0);
  };
  Sched sch;
  auto late = [&](int s, int lane) {
    if (w == SCH_W && SCH_LATE && __builtin_expect(s >= 0, 1)) *(u32x2*)(misc + 24) = (u32x2){sch.run(a, rs, s, b, lane), (unsigned)(s + 1)};
    if ((w == 2 && (!T5_Z || F6)) || (w == 0 && F4) || (w == 1 && F5) || (w == 3 && F7)) { lds_spin<L_MISC + 48, 1>(lds0, 2u * (unsigned)imax(s + 1, 0)); if (FB_SL && w != 2) __builtin_amdgcn_s_sleep(FB_SL); }
    if (w == 2 && !T5_Z) t5_get(s, lane);
    if (w == XPF_W && XPF == 2) touch(a.ws + OFF_XN + (size_t)imin(s + 4, NSEG - 1) * (s < 1 ? 0 : 40960) + (b >> 3) * 1280, imin(lane, 9) * 128, lds0);
    if (w >= 2) { x_fetch_n<w == 2 ? F6 : F7>(x_src(a, s + 3, b), lds0, s + 3, F4 + F5 + 2 * F67 + (w == 3 ? F6 : 0), lane * 16); return; }
    unsigned sd = 0u;
    if ((w == 0 && XPF == 3) || XPF == 4 || (w == 1 && XPF == 5)) stouch<10>(a.ws + OFF_XN + (size_t)imin(s + 4, NSEG - 1) * (s < 1 ? 0 : 40960) + (b >> 3) * 1280 + w * 64, sd);
    if (w == 0 && WPF_W == 4 && __builtin_expect(s >= WPF0 && s < WPF0 + WPF_N, 0))
      stouch<600 / WPF_N>(a.ws + OFF_WQKVBT + (size_t)(b >> 3) * 76800 + (s - WPF0) * (76800 / WPF_N), sd);
    x_fetch_n<w == 0 ? F4 : F5>(x_src(a, s + 3, b), lds0, s + 3, w == 0 ? 0 : F4, lane * 16);
    if (WPF_W == 0 && w == 0 && __builtin_expect(s >= WPF0 && s < WPF0 + WPF_N, 0)) wpf(s, lane);
    if (w == 1 && __builtin_expect(s == CPF_S, 0)) code_touch(b, lane, lds0);
    lds_spin<L_MISC + 104 + 4 * (w & 1), PS_SL>(lds0, (unsigned)imax(s + 1, 0));
    asm volatile("" :: "s"(sd));
  };
  auto xn_put = [&](int s) {
    float xn2 = xn4;
    xn2 = fmx(xn2, dppf<DPP_XOR1>(xn2)); xn2 = fmx(xn2, dppf<DPP_XOR2>(xn2));
    xn2 = fmx(xn2, dppf<DPP_ROR4>(xn2)); xn2 = fmx(xn2, dppf<DPP_ROR8>(xn2));
    misc[s & 3] = x16mx(xn2);
  };
  if (w == 2) { ot.init(b, lane0); ot.out = (u32x2){0u, 0u}; if (T5_Z) { ot.xo = ot.out; ot.y[0] = ot.y[1] = ot.y[2] = ot.y[3] = (u32x4){0u, 0u, 0u, 0u}; } }
  VM_DRAIN();
  lds_arrive(cnt, lane0);
  lds_wait(cnt, 8u);
  sch.tsv = sch.rec = (u32x4){0u, 0u, 0u, 0u};
  sch.side = __builtin_amdgcn_readfirstlane(*(const unsigned*)(misc + 11)) & 8192u;
  int sv = NV - 1;
  CODE_PAD((PADT >> (4 * w)) & 15);
#pragma unroll 1
  for (int s = -1; s < NSEG - 1; ++s) {
    const int sv1 = sv == NV - 1 ? 0 : sv + 1;
    {
      FRESH_LANE();
      STAMP(0);
      if (F67) lds_spin<L_MISC + 112, 1>(lds0, 2u * (unsigned)imax(s, 0));
      mma_seg(s + 1, sv1, lane, [&]() { STAMP(1); late(s - 1, lane); STAMP(9); });
      STAMP(2);
      __builtin_amdgcn_sched_barrier(0);
      VM_DRAIN();
      u32x2 keep;
      if (w == 2) { keep = ot.out; ot.run(lane); xn_put(s); const u32x2 t = ot.out; if (T5_Z != 2) ot.out = keep; keep = t; }
      if (w >= 2) { lds_spin<L_MISC + 92, 1>(lds0, 4u * (unsigned)imax(s, 0)); if (W1_D) spin_cyc(cyc32() + W1_D); }
      STAMP(10);
      if (WPF_W == 3 && w == 3 && __builtin_expect(s >= WPF0 && s < WPF0 + WPF_N, 0)) wpf(s, lane);
      if (w == 2 && T5_Z == 2) t5_get(s, lane);
      st_out(s, lane);
      if (w == 2) ot.out = keep;
      __builtin_amdgcn_sched_barrier(0);
      if (w == 2 && XPF == 1) touch(a.ws + OFF_XN + (size_t)imin(imax(s + 3, s < 2 ? 0 : 5), NSEG - 1) * (s < 2 ? 0 : 40960) + (b >> 3) * 1280, imin(lane, 9) * 128, lds0);
      if (w >= 2 && s > 0) x_fetch_n<F67>(x_src(a, s + 2, b), lds0, s + 2, F4 + F5 + (w - 2) * F67, lane * 16);
      STAMP(3);
    }
    BAR();
    {
      FRESH_LANE();
      STAMP(4);
      const unsigned tb1 = cyc32();
      if (w == 2 && T5_Z == 1) t5_get(s, lane);
      if (WPF_W == 5 && w == 0 && __builtin_expect(s >= WPF0 && s < WPF0 + WPF_N, 0)) wpf(s, lane);
      if (__builtin_expect(s >= 0, 1)) {
        if (w == SCH_W) { sch.load(rs, s, lane); __builtin_amdgcn_sched_barrier(0); }
        vo_mem(s, sv, tb1 + (s > 0 ? (w == 0 ? BG_HOLD4 : (w == 1 ? BG_HOLD : BG_HOLD6)) : 0), lane, std::integral_constant<int, 0>{});
        STAMP(6);
        if (w == SCH_W && !SCH_LATE) {
          __builtin_amdgcn_s_setprio(SCH_P);
          *(u32x2*)(misc + 24) = (u32x2){sch.run(a, rs, s, b, lane), (unsigned)(s + 1)};
        }
        __builtin_amdgcn_s_setprio(MMA_P);
        STAMP(11);
        ARRIVE(0);
      } else {
        BAR();
      }
    }
    ARRIVE(1);
    ARRIVE_OUT();
    sv = sv1;
  }
  STAMP_OUT();
  late(NSEG - 2, lane0);
  if (w >= 2) {
    lds_wait(cntp, 4u * (NSEG - 1));
    if (w == 2 && T5_Z == 2) { ot.run(lane0); ot.load(a, rs, NSEG - 2 - LAG5, b); }
    if (w == 2) { const u32x2 keep = ot.out; ot.run(lane0); ot.store(a, NSEG - 2 - LAG5, b, lane0); ot.out = keep; }
    __builtin_amdgcn_sched_barrier(0);
    PvTask pv;
    const unsigned ck2 = __builtin_amdgcn_readfirstlane(*(const unsigned*)(misc + 11));
    pv.load(rs, ck2, NSEG - 2 - LAG4, gp, w & 1, lane0);
    st_out(NSEG - 1, lane0);
    for (unsigned spins = 0; !pv.ready() && spins < SPIN_CAP; ++spins) { asm volatile("" ::: "memory"); pv.load(rs, ck2, NSEG - 2 - LAG4, gp, w & 1, lane0); }
    pv.run(lds, rs, NSEG - 2 - LAG4, gp, m, w & 1, lane0, true);
    x_fetch_n<F67>(x_src(a, NSEG + 1, b), lds0, NSEG + 1, F4 + F5 + (w - 2) * F67, lane0 * 16);
  }
  BAR();
  if (w != 0) vo_mem(NSEG - 1, (NSEG - 1) % NV, cyc32(), lane0, std::integral_constant<int, 1>{});
}

__device__ __forceinline__ void qkv_tail(const RecArgs& a, char* lds, const int w) {
  const int lane0 = __builtin_amdgcn_workitem_id_x() & 63, b = __builtin_amdgcn_workgroup_id_x();
  const int r = qkv_row(b), cb = (b >> 3) - 15 * (r & 1);
  if (cb >= 15) return;
  const unsigned lds0 = (unsigned)(uintptr_t)(LAS char*)lds;
  unsigned* const stage = (unsigned*)(lds + L_MISC) + 17;
  unsigned* const cnty = stage + 1;
  const f32x4 Z4 = {0.f, 0.f, 0.f, 0.f};
  auto fetch = [&](int i) {
    x_fetch_n<10>(x_src(a, NSEG + i, b), lds0, i, 10 * w, lane0 * 16);
  };
  unsigned want = 0;
  auto sync = [&]() { VM_DRAIN(); want += 4u; lds_inc<L_MISC + 72>(lds0); while (__hip_atomic_load(cnty, __ATOMIC_RELAXED, __HIP_MEMORY_SCOPE_WORKGROUP) < want) { } asm volatile("" ::: "memory"); };
  LOGQ(0);
  bf16x8 W[2][20];
  const __amdgpu_buffer_rsrc_t rw = __builtin_amdgcn_make_buffer_rsrc(a.ws + OFF_WQKVBT + (size_t)(cb * 8 + 2 * w) * 20480, 0, 40960, 0x00020000);
  auto w_get = [&](int ks) {
#pragma unroll
    for (int i = 0; i < 2; ++i) W[i][ks] = as8(__builtin_amdgcn_raw_buffer_load_b128(rw, lane0 * 16 + (ks & 3) * 1024, (i * 20 + (ks & ~3)) * 1024, 0));
    __builtin_amdgcn_sched_barrier(0);
  };
#pragma unroll
  for (int ks = 0; ks < QW; ++ks) w_get(ks);
  bf16x8 ones = mk8(0x3f803f80u, 0x3f803f80u, 0x3f803f80u, 0x3f803f80u);
  PIN_V(ones);
  const __amdgpu_buffer_rsrc_t ro = __builtin_amdgcn_make_buffer_rsrc(a.ws + OFF_QB, 0, (int)(OFF_OB - OFF_QB), 0x00020000);
  const int t0 = w & 1, col0 = (cb % 5) * 128 + w * 32;
  auto body = [&](auto SW, auto SQ) {
    unsigned vo[2][2];
    {
      FRESH_LANE();
#pragma unroll
      for (int i = 0; i < 2; ++i)
#pragma unroll
        for (int tt = 0; tt < 2; ++tt) {
          const int tq = tt ^ t0, tk = tq * 16 + c, cc = col0 + i * 16 + (SW ? 4 * g : c), hd = cc / BD, e = cc - hd * BD;
          const int kv = (int)(OFF_KV - OFF_QB) + hd * 64 * KVT;
          vo[i][tt] = (unsigned)(!SW ? kv + 1280 + tq * 672 + ((g & 1) * 21 + e) * 16 + (g >> 1) * 8
                                     : (cb < 5 ? (hd * S_ + tk) * 2 * QST + e * 2 : kv + (e < 16 ? (tk + 32 * (e >> 3)) * 16 + (e & 7) * 2 : 1024 + tk * 8)));
          PIN_V(vo[i][tt]);
        }
    }
    const float qs = cb < 5 ? 0.22360679774997896f * 1.4426950408889634f : 1.0f;
    f32x4 cs[2] = {Z4, Z4};
    LOGQ(1);
    f32x4 acc[2][2], st;
    auto mma = [&](int it, auto FIRST) {
      const char* xp0 = lds + L_XN + (it & 1) * 40960 + t0 * 20480 + lane0 * 16;
      const char* xp1 = lds + L_XN + (it & 1) * 40960 + (1 - t0) * 20480 + lane0 * 16;
      acc[0][0] = acc[0][1] = acc[1][0] = acc[1][1] = st = Z4;
      bf16x8 x[4][2];
#pragma unroll
      for (int p = 0; p < 3; ++p) { x[p][0] = *(const bf16x8*)(xp0 + p * 1024); x[p][1] = *(const bf16x8*)(xp1 + p * 1024); }
      __builtin_amdgcn_sched_barrier(0);
#pragma unroll
      for (int ks = 0; ks < 20; ++ks) {
        if (FIRST && ks + QW < 20) w_get(ks + QW);
#pragma unroll
        for (int q = 0; q < 4; ++q) {
          const int tt = 1 - (q >> 1), i = 1 - (q & 1);
          acc[i][tt] = SW ? mfma32(W[i][ks], x[ks & 3][tt], acc[i][tt]) : mfma32(x[ks & 3][tt], W[i][ks], acc[i][tt]);
          __builtin_amdgcn_sched_barrier(0);
          if (q < 2 && ks + 3 < 20) x[(ks + 3) & 3][q] = *(const bf16x8*)((q ? xp1 : xp0) + (ks + 3) * 1024);
          __builtin_amdgcn_sched_barrier(0);
        }
        st = mfma32(SQ ? x[ks & 3][0] : ones, x[ks & 3][0], st);
        if (FIRST) {
#pragma unroll
          for (int i = 0; i < 2; ++i) cs[i] = SW ? mfma32(W[i][ks], ones, cs[i]) : mfma32(ones, W[i][ks], cs[i]);
        }
        __builtin_amdgcn_sched_barrier(0);
      }
    };
    mma(0, std::integral_constant<bool, true>{});
#pragma unroll 1
    for (int it = 0; it < 4; ++it) {
      if (it) mma(it, std::integral_constant<bool, false>{});
      LOGQ(2 + 3 * it);
      FRESH_LANE();
      float* const sp = (float*)(lds + L_ST) + (it & 1) * 64;
      if (SQ) { if ((c >> 2) == g) sp[32 + t0 * 16 + c] = (c & 3) == 0 ? st[0] : ((c & 3) == 1 ? st[1] : ((c & 3) == 2 ? st[2] : st[3])); }
      else if (g == 0) sp[t0 * 16 + c] = st[0];
      sync();
      LOGQ(3 + 3 * it);
      if (it == 0) fetch(2);
      const bool late = r + 48 >= 55;
      if (it == 1 && !late) fetch(3);
      const bool now = it == 2 && late && __builtin_amdgcn_readfirstlane(*(volatile unsigned*)stage) != 0u;
      if (now) fetch(3);
      const unsigned so = (unsigned)((r + 16 * it) * (cb < 5 ? 64 * QST : KVT));
      auto stat = [&](float sm, float sq, float& mean, float& rstd) {
        mean = sm * (1.0f / DM);
        rstd = __builtin_amdgcn_rsqf(__builtin_fmaxf(sq * (1.0f / DM) - mean * mean, 0.f) + 1e-5f);
      };
#pragma unroll
      for (int tt = 0; tt < 2; ++tt) {
        const int tq = tt ^ t0;
        float mean[4], rstd[4];
        if (SW) {
          stat(sp[tq * 16 + c], sp[32 + tq * 16 + c], mean[0], rstd[0]);
          rstd[0] *= qs;
        } else {
          const f32x4 sm = *(const f32x4*)(sp + tq * 16 + 4 * g), sq = *(const f32x4*)(sp + 32 + tq * 16 + 4 * g);
#pragma unroll
          for (int k = 0; k < 4; ++k) stat(sm[k], sq[k], mean[k], rstd[k]);
        }
#pragma unroll
        for (int i = 0; i < 2; ++i) {
          float v[4];
#pragma unroll
          for (int k = 0; k < 4; ++k) v[k] = (acc[i][tt][k] - mean[SW ? 0 : k] * cs[i][SW ? k : 0]) * rstd[SW ? 0 : k];
          __builtin_amdgcn_raw_buffer_store_b64((u32x2){pack2(v[0], v[1]), pack2(v[2], v[3])}, ro, vo[i][tt], so, 0);
        }
      }
      LOGQ(4 + 3 * it);
      if (it == 2 && late) {
        if (!now) { lds_wait(stage, 1u); fetch(3); }
        sync();
      }
    }
  };
  typedef std::integral_constant<bool, false> F_; typedef std::integral_constant<bool, true> T_;
  if (cb < 10) { if (w >> 1) body(T_{}, T_{}); else body(T_{}, F_{}); }
  else { if (w >> 1) body(F_{}, T_{}); else body(F_{}, F_{}); }
}

struct TokTask {
  u32x4 y[2]; u32x2 ym; u32x4 res;
  unsigned l0, l1, lm, s0, s1;
  static __device__ __forceinline__ bool ok(int sl) { return sl >= 0 && sl < NSEG; }
  __device__ __forceinline__ void init(int h, int j, int lane) {
    const int c = lane & 15, g = lane >> 4;
    l0 = ts_tok(h, g) + (unsigned)(j * 256 + c * 16); l1 = l0 + 16384u;
    lm = ts_mem(h, g + 4 * (c >> 3)) + (unsigned)(j * 64 + (c & 7) * 8);
    res = (u32x4){0u, 0u, 0u, 0u};
    const int qi = 4 * j + (c & 3);
    s0 = g < 2 ? pt_tok(h) + (unsigned)((qi >> 4) * 1024 + ((c >> 2) * 16 + (qi & 15)) * 16 + g * 8) : 0x7ffffff0u;
    s1 = lane < 8 ? pt_mem(h) + (unsigned)((qi >> 4) * 256 + ((c >> 2) * 16 + (qi & 15)) * 8) : 0x7ffffff0u;
    PIN_V(l0); PIN_V(l1); PIN_V(lm); PIN_V(s0); PIN_V(s1);
  }
  __device__ __forceinline__ void load(const __amdgpu_buffer_rsrc_t rs, unsigned o) {
    y[0] = SC1_LOADS(l0, o); y[1] = SC1_LOADS(l1, o);
    ym = __builtin_amdgcn_raw_buffer_load_b64(rs, lm, o, 16);
  }
  __device__ __forceinline__ bool ready() { return wave_all(umin(umin(min4(y[0]), min4(y[1])), umin(ym[0], ym[1])) != 0u); }
  __device__ __forceinline__ void run(int j, int lane, const bf16x8 SelLo) {
    const int c = lane & 15, g = lane >> 4;
    const f32x4 Z4 = {0.f, 0.f, 0.f, 0.f};
    const f32x4 d1 = mfma32(SelLo, as8(y[1]), mfma32(SelLo, as8(y[0]), Z4));
    const f32x4 d0 = mfma32(SelLo, mk8(ym[0], ym[1], 0u, 0u), Z4);
    const int qi = 4 * j + (c & 3), k0 = 16 * g + 4 * (c >> 2);
    float t[4], mv[4];
    float mx = -INFINITY;
#pragma unroll
    for (int r = 0; r < 4; ++r) {
      t[r] = (g < 2 && k0 + r <= qi) ? d1[r] : -INFINITY;
      const float mt = d0[r] + dppf<DPP_ROR8>(d0[r]);
      mv[r] = lane < 8 ? mt : -INFINITY;
      mx = fmx(mx, fmx(t[r], mv[r]));
    }
    mx = fmx(mx, dppf<DPP_ROR8>(mx)); mx = fmx(mx, dppf<DPP_ROR4>(mx));
    mx = x16mx(mx);
    float sum = 0.f;
#pragma unroll
    for (int r = 0; r < 4; ++r) {
      t[r] = __builtin_amdgcn_exp2f(t[r] - mx); mv[r] = __builtin_amdgcn_exp2f(mv[r] - mx);
      sum += t[r] + mv[r];
    }
    sum += dppf<DPP_ROR8>(sum); sum += dppf<DPP_ROR4>(sum);
    sum = x16sum(sum);
    const float rs_ = __builtin_amdgcn_rcpf(sum);
    res = (u32x4){pack2n(t[0] * rs_, t[1] * rs_), pack2n(t[2] * rs_, t[3] * rs_), pack2n(mv[0] * rs_, mv[1] * rs_), pack2n(mv[2] * rs_, mv[3] * rs_)};
    PIN_V(res);
  }
  __device__ __forceinline__ void store(const __amdgpu_buffer_rsrc_t rs, unsigned o, unsigned no = 0u) {
    SC1_STORE8S(((u32x2){res[0], res[1]}), s0 | no, o);
    SC1_STORE8S(((u32x2){res[2], res[3]}), s1 | no, o);
  }
};

__device__ __forceinline__ void t2_mem(const bf16x8 fr, const bf16x8 q0, const bf16x8 q1, const __amdgpu_buffer_rsrc_t rs, unsigned o, int h, int j, float inv, int lane) {
  const f32x4 Z4 = {0.f, 0.f, 0.f, 0.f};
  const f32x4 d0 = mfma32(fr, q0, Z4) * inv, d1 = mfma32(fr, q1, Z4) * inv;
  const unsigned a0 = pack2n(d0[0], d0[1]), a1 = pack2n(d0[2], d0[3]), b0 = pack2n(d1[0], d1[1]), b1 = pack2n(d1[2], d1[3]);
  const auto r0 = __builtin_amdgcn_permlane32_swap(a0, b0, false, false), r1 = __builtin_amdgcn_permlane32_swap(a1, b1, false, false);
  SC1_STORE8S(((u32x2){r0[1], r1[1]}), ts_mem(h, j) + (lane >> 5) * 256 + (lane & 12) * 16 + (lane & 16) * 2 + (lane & 3) * 8, o);
}

template <int w>
__device__ __forceinline__ void rec_waves(const RecArgs& a, char* lds, __amdgpu_buffer_rsrc_t rs) {
  PIN_S(rs);
  const int lane = __builtin_amdgcn_workitem_id_x() & 63, b = __builtin_amdgcn_workgroup_id_x(), c = lane & 15, g = lane >> 4, wid = w; (void)wid;
  const int gp = (b & 7) * 2 + (b >> 7), m = (b >> 3) & 15, h0 = 2 * gp, h = h0 + (m >> 3), j = m & 7;
  float* const misc = (float*)(lds + L_MISC);
  const f32x4 Z4 = {0.f, 0.f, 0.f, 0.f};
  LOGP(0);
  __builtin_amdgcn_s_setprio(3);
  const unsigned lds0 = (unsigned)(uintptr_t)(LAS char*)lds;
  if (!a.layer) img_fetch<12>(a, lds0, h, j, w, lane);
  constexpr int NC = w < 2 ? 2 : 3;
  const int far = ((b >> 2) & 1) ^ 1;
  auto chunk = [&](int f) { return f == 0 ? 4 * far + w : (w == 2 && f == 1 ? 8 + far : (w == 3 && f == 2 ? 9 - far : 4 - 4 * far + w)); };
  bf16x16 Wqk[3][NC];
  u32x4 bm0[NC][2];
#pragma unroll
  for (int f = 0; f < 2 * NC; ++f) bm0[f >> 1][f & 1] = *(const u32x4*)(a.mem0 + (size_t)(c & 7) * DM + 64 * chunk(f >> 1) + 16 * g + 8 * (f & 1));
  if (!a.layer) asm volatile("s_waitcnt vmcnt(0)" ::: "memory");
  BAR();
  LOGP(9);
  const unsigned ck = __builtin_amdgcn_readfirstlane(*(const unsigned*)(misc + 11));
  const unsigned flip = (ck ^ (unsigned)(b << 11)) & 8192u;
#pragma unroll
  for (int t = 0; t < 3; ++t)
#pragma unroll
    for (int f = 0; f < NC; ++f) Wqk[t][f] = cat16(wqk_frag(lds, t, 2 * chunk(f), lane), wqk_frag(lds, t, 2 * chunk(f) + 1, lane));
  if (!a.layer) lds_arrive((unsigned*)(misc + 6), lane);
  else {
    if (w == 0) {
      for (unsigned spins = 0; spins < SPIN_CAP; ++spins) {
        if (__builtin_amdgcn_raw_buffer_load_b32(rs, (b & 7) * 256, (unsigned)OFF_RDY, 16) >= 16u) break;
        __builtin_amdgcn_s_sleep(4);
        asm volatile("" ::: "memory");
      }
      lds_inc<L_MISC + 84>(lds0);
    }
    lds_wait((unsigned*)(misc + 21), 2u);
#pragma unroll
    for (int f = 0; f < 2 * NC; ++f) PIN_V(bm0[f >> 1][f & 1]);
#pragma unroll
    for (int i = 0; i < 10; ++i) x_fetch(x_src(a, 0, b), lds0, 0, 10 * w + i, lane);
  }
  const bf16x8 SelLo = sel_frag(c, 0), SelHi = sel_frag(c, 8);
  bf16x8 ONES = mk8(0x3f803f80u, 0x3f803f80u, 0x3f803f80u, 0x3f803f80u);
  if (w < 2) PIN_V(ONES);
  bool dead = false;
  float inv = 1.0f;
  unsigned* const cnt2 = (unsigned*)(misc + 7);
  unsigned l2off[2 * NC];
#pragma unroll
  for (int f = 0; f < 2 * NC; ++f) { l2off[f] = acc_off(16 * chunk(f >> 1) + 4 * g + 2 * (f & 1) + (c >> 3), c & 7) ^ flip; PIN_V(l2off[f]); }
  int sidx = (c >> 3) ? 0xEEEE : 0x4444;
  PIN_V(sidx);
  const unsigned p1off = (unsigned)((w == 0 ? (h & 2) * 1024 + j * 256 + (h & 1) * 64 + (g & 1) * 8 : (h & 3) * 32768 + j * 512 + (g >> 1) * 256 + (g & 1) * 64) + (c & 4) * 32 + (c & 3) * 16);
  unsigned p1v = (c < 8) && (w != 0 || g >= 2) ? p1off : 0x7ffffff0u, p1cur = 0x7ffffff0u;
  PIN_V(p1v); PIN_V(p1cur);
  const int hd = (c >> 2) & 1, mr = hd ? 3 - (c & 3) : (c & 3);
  const unsigned l1off = (unsigned)(((h0 + hd) & 3) * 32768 + g * 512 + (w & 1) * 128 + (c >> 3) * 64 + mr * 16);
  const unsigned l1offm = (unsigned)((h0 & 2) * 1024 + (g + 4 * (c >> 3)) * 256 + (w & 1) * 128 + hd * 64 + mr * 16);
  const int plane = (lane & ~6) | ((lane & 2) << 1) | ((lane & 4) >> 1);
  unsigned acco = c < 8 ? (acc_off(64 * far + m * 4 + g, 4 * (w & 1) + mr) + (unsigned)(hd * 8)) ^ flip : 0x7ffffff0u;
  unsigned acco2 = c >= 8 ? (acc_off(64 - 64 * far + m * 4 + g, 4 * (w & 1) + mr) + (unsigned)(hd * 8)) ^ flip
                          : (c < 4 ? (acc_off(128 + m * 2 + (g & 1), 4 * (w & 1) + mr) + (unsigned)((g >> 1) * 8)) ^ flip : 0x7ffffff0u);
  if (w < 2) { PIN_V(acco); PIN_V(acco2); }
  unsigned bmo[NC];
#pragma unroll
  for (int f = 0; f < NC; ++f) { bmo[f] = (unsigned)(chunk(f) * 1024 + lane * 16); PIN_V(bmo[f]); }
  bf16x8 bm[NC];
  auto bm_put = [&](int f) { *(bf16x8*)(lds + L_BM + bmo[f]) = bm[f]; __builtin_amdgcn_sched_barrier(0); };
  f32x4 acc[3] = {Z4, Z4, Z4};
  const int rt2 = ((c >> 3) * 8 + 4 * (g & 1)) * 28 + 16 + (c & 7);
  unsigned redo[3] = {(unsigned)(4 * g * 28 + c), (unsigned)(g < 2 ? (4 * g + 2) * 28 + c : rt2), (unsigned)(g < 2 ? (4 * g + 10) * 28 + c : rt2 + 56)};
#pragma unroll
  for (int k = 0; k < 3; ++k) { redo[k] = L_RED1 + w * 1792 + redo[k] * 4; PIN_V(redo[k]); }
  auto red_put = [&]() {
    auto hs = [&](float x, float y) { const auto sw = __builtin_amdgcn_permlane32_swap(f2u(x), f2u(y), false, false); return u2f(sw[0]) + u2f(sw[1]); };
#pragma unroll
    for (int k = 0; k < 3; ++k) {
#pragma unroll
      for (int i = 0; i < 2; ++i) *(float*)(lds + redo[k] + i * 112) = k == 0 ? hs(acc[0][i], acc[1][i]) : hs(acc[k - 1][2 + i], acc[2][2 * k - 2 + i]);
      __builtin_amdgcn_sched_barrier(0);
    }
  };
#pragma unroll
  for (int f = 0; f < NC; ++f) {
    const u32x4 u = bm0[f][0], v = bm0[f][1];
    bm[f] = c < 8 ? mk8(u[0], u[2], v[0], v[2]) : mk8(u[1], u[3], v[1], v[3]);
    bm_put(f);
#pragma unroll
    for (int t = 0; t < 3; ++t) acc[t] = smfma64(bm[f], Wqk[t][f], acc[t], sidx);
  }
  red_put();
  VM_DRAIN();
  if (a.layer) lds_arrive((unsigned*)(misc + 6), lane);
  lds_wait((unsigned*)(misc + 6), 8u);
#pragma unroll
  for (int i = 0; i < 10; ++i) x_fetch(x_src(a, 1, b), lds0, 1, 10 * w + i, lane);
  LOGP(1);
  BAR();
  STAMP_DECL(); ARRIVE_DECL();
  bf16x8 fr, qx[2];
  auto kq_get = [&](int seg) {
    const char* p = lds + (w == 1 ? L_KX : L_QX) + (seg % NKQ) * 2048 + lane * 16;
    if (w == 1 || w == 2 || (w == 3 && seg == NSEG - 1)) { qx[0] = *(const bf16x8*)p; qx[1] = *(const bf16x8*)(p + 1024); }
  };
  unsigned qo4 = (unsigned)(L_RED1 + (w == 1 ? (c & 7) * 112 + (c >> 3) * 3584 : c * 112) + g * 16), qo2 = qo4 + 64u - (unsigned)(g * 8), qo2b = qo2 + 3584u;
  if (w < 3) { PIN_V(qo4); PIN_V(qo2); if (w != 1) PIN_V(qo2b); }
  auto pub1 = [&](unsigned off, unsigned o1) {
    if (w == 0) fr = qk_frag(lds + qo4, lds + qo2, lds + qo2b);
    else {
      const f32x4 a0 = *(const f32x4*)(lds + qo4), a1 = *(const f32x4*)(lds + qo4 + 1792);
      const f32x2 b0 = *(const f32x2*)(lds + qo2), b1 = *(const f32x2*)(lds + qo2 + 1792);
      float v[6];
#pragma unroll
      for (int i = 0; i < 6; ++i) v[i] = i < 4 ? a0[i] + a1[i] : b0[i - 4] + b1[i - 4];
#pragma unroll
      for (int i = 0; i < 6; ++i) v[i] += dppf<DPP_ROR8>(v[i]);
      fr = mk8(pack2(v[0], v[1]), pack2(v[2], v[3]), pack2(v[4], v[5]), 0u);
    }
    const f32x4 sc = mfma32(w == 0 ? fr : qx[0], fr, Z4);
    if (w == 0) SC1_STORE8S(((u32x2){pack2n(sc[0], sc[1]), pack2n(sc[2], sc[3])}), off, o1);
    else {
      const f32x4 s2 = mfma32(qx[1], fr, Z4);
      __builtin_amdgcn_raw_buffer_store_b128((u32x4){pack2n(sc[0], sc[1]), pack2n(sc[2], sc[3]), pack2n(s2[0], s2[1]), pack2n(s2[2], s2[3])}, rs, off, o1, 16);
    }
  };
  VM_DRAIN();
  kq_get(0);
  unsigned o1 = sct_slot(ck, 0u), o1m = scm_slot(ck, 0u);
  unsigned t2o = ts_tok(h, j) + (unsigned)((c >> 2) * 256 + g * 64 + (c & 3) * 16);
  TokScores t2;
  if (w == 3) { PIN_V(t2o); t2.get(lds, 0, lane); t2.run(0); t2.run(1); t2.store(rs, t2o, ts_slot(ck, 0)); }
  if (w < 2) pub1(p1v, w == 0 ? o1m : o1);
  const unsigned tst = now32() | 1u;
  if (w == 3 && ST_X) __builtin_amdgcn_raw_buffer_store_b32(tst, rs, lane < 2 ? ts_piece(b >> 2) - ACC_TS + (b & 3) * 4 + lane * 8192 : 0x7ffffff0, (unsigned)(OFF_ACC + (SR - 1) * ACC_STEP) + ACC_TS, 16);
  LOGP(2);
  BAR();
  LOGP(3);
#pragma unroll
  for (int i = 0; i < 10; ++i) x_fetch(x_src(a, 2, b), lds0, 2, 10 * w + i, lane);
  VM_DRAIN();
  unsigned o2 = (unsigned)OFF_ACC, o2p = 0u, tw = 0u;
  asm volatile("" : "+s"(o2p));
  unsigned tc0 = 0u, tstar = 0u;
  int dmin = 0, dmax = 0;
  int e1 = 100000;
  unsigned offs = 100u;
  TokTask tk;
  OutTask ot;
  PvTask pv;
  PvTask2 pv2;
  if (w == 3) pv2.init(gp, lane);
  if (w == 2) tk.init(h, j, lane);
  unsigned o4 = 0u;
  unsigned o3, o3p;
  u32x16 P = {0u, 0u, 0u, 0u, 0u, 0u, 0u, 0u, 0u, 0u, 0u, 0u, 0u, 0u, 0u, 0u}, V = P;
  u32x8 V8 = {0u, 0u, 0u, 0u, 0u, 0u, 0u, 0u};
  if (w < 2) asm volatile("" : "+{v[208:223]}"(P), "+{v[184:191]}"(V8), "+{v[192:207]}"(V));
  unsigned tso = lane < 2 ? ts_piece(b >> 2) + (unsigned)((b & 3) * 4 + lane * 8192) : 0x7ffffff0u;
  if (w < 2) PIN_V(tso);
  float minf = g >= 2 ? -INFINITY : 0.f;
  if (w < 2) PIN_V(minf);
  int sv = NV - 1;
  bf16x8 vox[5];
  unsigned vma[3], need2;
  u32x4 slo, shi;
  float k1;
  const u16* xsrc;
  float invv = 1.0f;
  auto prep0 = [&](int s) {
    xsrc = x_src(a, s + 3, b);
    if (w == 2) { o3p = ts_slot(ck, (s + 1 - LAG3) & 3); o3 = ts_slot(ck, s & 3); o4 = pt_slot(ck, imax(s - LAG3, 0)); PIN_S(o3); }
    if (w == 3) { o3p = pt_slot(ck, (s - LAG4) & 3); o4 = ts_slot(ck, s + 1); }
    PIN_S(xsrc); PIN_S(o3p); PIN_S(o4);
    __builtin_amdgcn_sched_barrier(0);
  };
  auto prep = [&](int s) {
    o2 = (unsigned)OFF_ACC + (unsigned)(s % SR) * (unsigned)ACC_STEP;
    sv = sv == NV - 1 ? 0 : sv + 1;
    PIN_S(o2); PIN_S(sv);
    if (w < 2) {
      PIN_S(tc0);
      o1 = sct_slot(ck, s % SR); o1m = scm_slot(ck, s % SR);
      need2 = 4u * (unsigned)(s + 1) + 2u * ((unsigned)(s + 2) / NSEG);
      PIN_S(o1); PIN_S(o1m); PIN_S(need2);
      __builtin_amdgcn_sched_barrier(0);
      kq_get(s);
    }
    invv = misc[4 + (s & 1)];
  };
  auto prep2 = [&](int s) {
#pragma unroll
    for (int k = 0; k < 3; ++k) {
      const int t = k == 0 ? 2 : (k == 1 ? far : 1 - far);
      const char* p = lds + L_VOX + sv * 5120 + t * 1024 + (plane & 47) * 16 + (g & 1) * 8;
      auto rd = [&](const char* q) { const u32x2 a0 = *(const u32x2*)q, a1 = *(const u32x2*)(q + 256); return mk8(a0[0], a0[1], a1[0], a1[1]); };
      vox[k == 0 ? 0 : 2 * k - 1] = rd(p);
      if (k) vox[2 * k] = rd(p + 3072);
      vma[k] = lds0 + (unsigned)(L_VOM + sv * 1280 + t * 256 + (plane & 31) * 8);
      PIN_V(vma[k]);
    }
    __builtin_amdgcn_sched_barrier(0);
    inv = u2f(__builtin_amdgcn_readfirstlane(f2u(invv)));
    slo = __builtin_bit_cast(u32x4, SelLo); shi = __builtin_bit_cast(u32x4, SelHi);
#pragma unroll
    for (int i = 0; i < 4; ++i) { slo[i] = ((slo[i] >> 7) & 0x10001u) * (f2u(inv) >> 16); shi[i] = ((shi[i] >> 7) & 0x10001u) * (f2u(inv) >> 16); }
    if (F0A) { xsrc = x_src(a, s + 3, b); PIN_S(xsrc); }
    k1 = KEXP * inv;
    PIN_V(k1); PIN_V(slo); PIN_V(shi);
    asm volatile("" : "+v"(vox[0]), "+v"(vox[1]), "+v"(vox[2]), "+v"(vox[3]), "+v"(vox[4]));
    __builtin_amdgcn_sched_barrier(0);
  };
  auto rows = [&](int s) {
    const int dl = spin_until(tw);
    if (w < 2) tc0 = cyc32();
    __builtin_amdgcn_sched_barrier(0);
    STAMP(1);
    unsigned spins = 0;
    auto pass = [&](bool first) {
      u32x4 x[2 * NC];
#pragma unroll
      for (int f = 0; f < NC; ++f) {
        if (f && w < 2 && first) { prep(s); asm volatile(".rept %0\n\ts_nop 3\n\t.endr" :: "n"(P2_G1)); }
        else if (f) asm volatile(".rept %0\n\ts_nop 3\n\t.endr" :: "n"(w < 2 ? P2_G0 : (f == 1 ? P2_G2 : P2_G3)));
        __builtin_amdgcn_sched_barrier(0);
        x[2 * f] = SC1_LOADS(l2off[2 * f], o2p); x[2 * f + 1] = SC1_LOADS(l2off[2 * f + 1], o2p);
        __builtin_amdgcn_sched_barrier(0);
      }
      if (first) lds_inc<L_MISC + 92>(lds0);
      if (first) { if (w >= 2) prep(s); else prep2(s); }
      if (w >= 2 && first && P2_W) lds_spin<L_MISC + 92, 0>(lds0, 4u * (unsigned)s);
      if (w == 2 && first) tk.store(rs, o4, s > LAG3 ? 0u : 0x7ff00000u);
      if (w == 3 && first) t2.store(rs, t2o, o4);
      if (first && w < 2) {
        p1cur = p1v; offs = (offs - (unsigned)dl) & ~3u;
        PIN_V(p1cur); PIN_S(offs);
        tstar = tc0 + (unsigned)(e1 + D1_M + (w == 1 ? D1_W1 : 0)) - offs * 20u;
        dmin = offs < 40u ? D1_MIN : REC_D1;
        dmin = s < EARLY_N ? REC_D1 + EARLY_D1 : dmin;
        dmax = REC_D1;
        PIN_S(tstar); PIN_S(dmin); PIN_S(dmax);
      }
      __builtin_amdgcn_sched_barrier(0);
      unsigned ok = 32u;
#pragma unroll
      for (int t = 0; t < 3; ++t) { acc[t] = Z4; PIN_V(acc[t]); }
      __builtin_amdgcn_sched_barrier(0);
      bool r = true;
#pragma unroll
      for (int f = 0; f < NC; ++f) {
        unsigned p[4];
#pragma unroll
        for (int k = 0; k < 2; ++k) {
          const u32x4 v = x[2 * f + k];
          p[2 * k] = pack2((float)(int)v[0], (float)(int)v[1]); p[2 * k + 1] = pack2((float)(int)v[2], (float)(int)v[3]);
          asm volatile("" : "+v"(p[2 * k]), "+v"(p[2 * k + 1]));
          __builtin_amdgcn_sched_barrier(0);
        }
        ok = (ok & x[2 * f][0] & x[2 * f][2]) & (x[2 * f + 1][0] & x[2 * f + 1][2]);
        PIN_V(ok);
        __builtin_amdgcn_sched_barrier(0);
        bm[f] = mk8(p[0], p[1], p[2], p[3]);
        acc[0] = smfma64(bm[f], Wqk[0][f], acc[0], sidx);
        __builtin_amdgcn_sched_barrier(0);
        bm_put(f);
        acc[1] = smfma64(bm[f], Wqk[1][f], acc[1], sidx);
        __builtin_amdgcn_sched_barrier(0);
        if (f == NC - 1) { u64 bad = __builtin_amdgcn_ballot_w64(ok == 0u); PIN_S(bad); r = bad == 0ull; }
        __builtin_amdgcn_sched_barrier(0);
        acc[2] = smfma64(bm[f], Wqk[2][f], acc[2], sidx);
        __builtin_amdgcn_sched_barrier(0);
      }
      red_put();
      return r;
    };
    if (__builtin_expect(!pass(true), 0)) {
      FC(0);
      do {
        if (dead || ++spins > SPIN_CAP) { dead = true; break; }
        asm volatile("" ::: "memory");
      } while (!pass(false));
    }
    STAMPV(12, spins); LOGF(1, spins);
    TSTAMP(2, now32()); TSTAMP(3, spins);
  };
  if (w >= 2) prep0(-1);
  prep(0);
  if (w < 2) prep2(0);
  CODE_PAD((PADR >> (4 * w)) & 15);
#pragma unroll 1
  for (int s = 0; s < NSEG - 1; ++s) {
    if (__builtin_expect(o2p != 0u, 1)) rows(s);
    STAMP(2);
    BAR();
    STAMP(3);
    if (w < 2) pub1(p1cur, w == 0 ? o1m : o1);
    if (w == 2) tk.load(rs, o3p);
    if (w == 3) pv2.load(rs, o3p);
    const unsigned tp1 = cyc32();
    __builtin_amdgcn_sched_barrier(0);
    if (w == 2) {
      __builtin_amdgcn_s_setprio(W2_P);
      prep0(s);
      if (W2_D) spin_cyc(tp1 + W2_D);
      fr = qk_frag(lds + qo4, lds + qo2, lds + qo2b);
      t2_mem(fr, qx[0], qx[1], rs, o3, h, j, invv * KEXP, lane);
      __builtin_amdgcn_sched_barrier(0);
      asm volatile("s_waitcnt vmcnt(1)" ::: "memory");
      lds_inc<L_MISC + 28>(lds0);
      kq_get(s + 1);
    }
    if (w == 3) {
      __builtin_amdgcn_s_setprio(T4_P);
      prep0(s);
      t2.get(lds, s + 1, lane); t2.run(0); t2.run(1);
      __builtin_amdgcn_sched_barrier(0);
    }
    if (w >= 2 && __builtin_expect(s == NSEG - 2, 0)) { VM_DRAIN(); lds_inc<L_MISC + 28>(lds0); }
    TSTAMP(0, now32());
    STAMP(4);
    __builtin_amdgcn_sched_barrier(0);
    unsigned tpub = 0;
    u32x2 sw;
    if (w == 2) {
      STAMP(5);
      __builtin_amdgcn_sched_barrier(0);
      STAMP(6);
      ARRIVE(0);
      if (FB_E) { PIN_V(tk.y[0]); PIN_V(tk.y[1]); PIN_V(tk.ym); }
      else tk.run(j, lane, SelLo);
      if (F2B) { lds_spin<L_MISC + 48, 1>(lds0, 2u * (unsigned)(s + 1)); x_fetch_n<F2B>(xsrc, lds0, s + 3, P2B, lane * 16); }
      if (FB_E) tk.run(j, lane, SelLo);
      __builtin_amdgcn_s_setprio(SP_P);
    }
    if (w == 3) {
      if (F3A) { spin_cyc(tp1 + F3A_D); x_fetch_n<F3A>(xsrc, lds0, s + 3, 40 - F3C - F3 - F3A, lane * 16); }
      if (T4_D > F3A_D) spin_cyc(tp1 + T4_D);
      if (F3 && FB_E) { VM_DRAIN(); lds_spin<L_MISC + 48, 1>(lds0, 2u * (unsigned)(s + 1)); x_fetch_n<F3>(xsrc, lds0, s + 3, 40 - F3C - F3, lane * 16); }
      pv2.run(lds, sv < 1 + LAG4 ? sv + NV - 1 - LAG4 : sv - 1 - LAG4, lane);
      STAMP(11);
      __builtin_amdgcn_s_setprio(SP_P);
      STAMP(5);
      ARRIVE(0);
      if (F3 && !FB_E) { lds_spin<L_MISC + 48, 1>(lds0, 2u * (unsigned)(s + 1)); x_fetch_n<F3>(xsrc, lds0, s + 3, 40 - F3C - F3, lane * 16); }
      STAMP(6);
    }
    if (w < 2) {
      __builtin_amdgcn_s_setprio(D1_P);
      x_fetch_n<F0A>(xsrc, lds0, s + 3, PA + w * F0A, lane * 16);
      int dq = imin((int)(tstar - tp1), dmax);
      dq = dq < -D1_LATE ? dmax : dq;
      PIN_S(dq);
      spin_exact(tp1 + (unsigned)imax(dq, dmin));
      STAMP(5);
      u32x32 S;
      auto sc_load = [&]() {
        asm volatile("buffer_load_dwordx4 v[224:227], %2, %4, %5 offen sc1\n\t.rept %7\n\ts_nop 3\n\t.endr\n\t"
                     "buffer_load_dwordx4 v[228:231], %3, %4, %6 offen sc1\n\tbuffer_load_dwordx4 v[232:235], %3, %4, %6 offen offset:2048 sc1\n\t"
                     "buffer_load_dwordx4 v[236:239], %3, %4, %6 offen offset:256 sc1\n\tbuffer_load_dwordx4 v[240:243], %3, %4, %6 offen offset:2304 sc1\n\t"
                     "v_mov_b32 v220, 0x42000000\n\tv_mov_b32 v221, 0\n\tv_mov_b32 v222, 0x42000000\n\tv_mov_b32 v223, 0"
                     : "=&{v[224:255]}"(S), "+{v[208:223]}"(P) : "v"(l1offm), "v"(l1off), "s"(rs), "s"(o1m), "s"(o1), "n"(P1_G) : "memory");
      };
      sc_load();
      __builtin_amdgcn_sched_barrier(0);
      lds_inc<L_MISC + 48>(lds0);
      STAMP(6);
      ARRIVE(0);
      lds_spin<L_MISC + 28, 0>(lds0, need2);
      __builtin_amdgcn_s_setprio(3);
      asm volatile("ds_read_b64 v[192:193], %3\n\tds_read_b64 v[196:197], %3 offset:768\n\tds_read_b64 v[200:201], %4\n\tds_read_b64 v[204:205], %4 offset:768\n\tds_read_b64 v[184:185], %2"
                   : "+{v[184:191]}"(V8), "+{v[192:207]}"(V) : "v"(vma[0]), "v"(vma[1]), "v"(vma[2]) : "memory");
      __builtin_amdgcn_sched_barrier(0);
      {
        const int de = imin((int)(tp1 - tc0) - e1, D1_C);
        e1 += s == 0 ? 0 : de >> (s < EARLY_N && de < -D1_C ? 0 : D1_G);
        PIN_S(e1);
      }
      STAMP(11);
      __builtin_amdgcn_sched_barrier(0);
      {
        unsigned spins = 0;
        u64 miss, bad;
#define MM(d, a, b, c) "v_mfma_f32_16x16x32_bf16 " d ", " a ", " b ", " c "\n\t"
#define FM(d, x, r, ctrl) "v_fmac_f32_dpp " d ", " x ", " r " " ctrl "\n\t"
#define ID_ "quad_perm:[0,1,2,3] row_mask:0xf bank_mask:"
#define HM_ "row_half_mirror row_mask:0xf bank_mask:"
#define R8_ "row_ror:8 row_mask:0xf bank_mask:"
#define MI_ "row_mirror row_mask:0xf bank_mask:"
        auto soft = [&]() {
          asm volatile(
            "s_waitcnt vmcnt(4)\n\t" MM("v[244:247]", "%6", "v[224:227]", "0") "v_min3_u32 v214, v224, v225, v226\n\t"
            "s_waitcnt vmcnt(3)\n\t" MM("v[248:251]", "%6", "v[228:231]", "0") "v_min3_u32 v214, v214, v227, v228\n\tv_min3_u32 v214, v214, v229, v230\n\t"
            "s_waitcnt vmcnt(2)\n\t" MM("v[248:251]", "%6", "v[232:235]", "v[248:251]") "v_min3_u32 v214, v214, v231, v232\n\t"
            "v_add_f32_dpp v212, v244, v244 " R8_ "0x3\n\tv_add_f32_dpp v213, v245, v245 " R8_ "0x3\n\t"
            "s_waitcnt vmcnt(1)\n\t" MM("v[248:251]", "%7", "v[236:239]", "v[248:251]")
            "v_add_f32_dpp v212, v246, v246 " R8_ "0xc\n\tv_add_f32_dpp v213, v247, v247 " R8_ "0xc\n\t"
            "s_waitcnt vmcnt(0) lgkmcnt(0)\n\t" MM("v[248:251]", "%7", "v[240:243]", "v[248:251]")
            "v_fma_f32 v212, v212, %14, %15\n\tv_fma_f32 v213, v213, %14, %15\n\tv_exp_f32 v216, v212\n\tv_exp_f32 v217, v213\n\t"
            "v_min3_u32 v214, v214, v233, v234\n\tv_cvt_pk_bf16_f32 v208, v216, v217\n\tv_min3_u32 v214, v214, v235, v236\n\tds_read_b96 v[188:190], %16 offset:%18\n\t"
            "v_exp_f32 v216, v248\n\tv_mov_b32_dpp v209, v208 " R8_ "0xf\n\tv_exp_f32 v217, v249\n\tv_exp_f32 v218, v250\n\t"
            MM("v[252:255]", "%8", "v[208:211]", "0") "v_exp_f32 v219, v251\n\tv_cvt_pk_bf16_f32 v232, v216, v217\n\t"
            MM("v[224:227]", "v[192:195]", "v[208:211]", "0") "v_cvt_pk_bf16_f32 v233, v218, v219\n\tv_min3_u32 v214, v214, v237, v238\n\tv_mov_b32_dpp v234, v232 " R8_ "0xf\n\t"
            MM("v[228:231]", "v[196:199]", "v[208:211]", "0") "v_mov_b32_dpp v235, v233 " R8_ "0xf\n\t"
            MM("v[244:247]", "v[204:207]", "v[208:211]", "0") "v_min3_u32 v214, v214, v239, v240\n\t"
            MM("v[252:255]", "%8", "v[232:235]", "v[252:255]") MM("v[224:227]", "%10", "v[232:235]", "v[224:227]") "v_min3_u32 v214, v214, v241, v242\n\t"
            MM("v[228:231]", "%11", "v[232:235]", "v[228:231]") "s_waitcnt lgkmcnt(0)\n\tv_sub_u32 v215, v190, %17 clamp\n\t"
            MM("v[236:239]", "v[200:203]", "v[208:211]", "0") "v_min3_u32 v214, v214, v243, v215\n\tv_cmp_eq_u32 %4, 0, v214\n\tv_rcp_f32 v216, v252\n\t"
            MM("v[240:243]", "v[184:187]", "v[208:211]", "0") "v_mul_f32 v216, v216, v188\n\tv_subrev_u32 v215, 0x2f800000, v252\n\t"
            MM("v[236:239]", "%12", "v[232:235]", "v[236:239]") FM("v220", "v224", "v216", ID_ "0x1") FM("v221", "v225", "v216", ID_ "0x1")
            "v_mov_b32_dpp v217, v216 " HM_ "0xf\n\t"
            MM("v[244:247]", "%13", "v[232:235]", "v[244:247]") FM("v220", "v230", "v216", ID_ "0x2") FM("v221", "v231", "v216", ID_ "0x2")
            MM("v[240:243]", "%9", "v[232:235]", "v[240:243]") FM("v220", "v228", "v217", HM_ "0x1") FM("v221", "v229", "v217", HM_ "0x1")
            "v_cmp_ge_u32 vcc, v215, v189\n\t" FM("v220", "v226", "v217", HM_ "0x2") FM("v221", "v227", "v217", HM_ "0x2")
            "s_or_b64 %5, %4, vcc"
            : "+{v[224:255]}"(S), "+{v[208:223]}"(P), "+{v[184:191]}"(V8), "+{v[192:207]}"(V), "=&s"(miss), "=&s"(bad)
            : "v"(slo), "v"(shi), "v"(ONES), "v"(vox[0]), "v"(vox[1]), "v"(vox[2]), "v"(vox[3]), "v"(vox[4]), "v"(k1), "v"(minf), "v"(lds0), "s"(s), "n"(L_MISC + 32)
            : "vcc", "scc", "memory");
        };
        soft();
        if (__builtin_expect(bad != 0ull, 0)) {
          if (miss) FC(1); else FC(2);
          while (miss != 0ull && !dead) {
            if (++spins > SPIN_CAP) dead = true;
            asm volatile("s_nop 7\n\ts_nop 7" ::: "memory");
            sc_load(); soft();
          }
          if (bad != 0ull) {
            u32x4 y[5];
#pragma unroll
            for (int k = 0; k < 4; ++k) y[k] = SC1_LOADS(l1off + (k & 1) * 2048 + (k >> 1) * 256, o1);
            y[4] = SC1_LOADS(l1offm, o1m);
            const f32x4 d0 = mfma32(as8(slo), as8(y[4]), Z4);
            f32x4 d1 = mfma32(as8(shi), as8(y[3]), mfma32(as8(shi), as8(y[2]), mfma32(as8(slo), as8(y[1]), mfma32(as8(slo), as8(y[0]), Z4))));
            const bool lo = c < 8;
            const float m0 = __builtin_fmaf((lo ? d0[0] : d0[2]) + dppf<DPP_ROR8>(lo ? d0[2] : d0[0]), k1, minf);
            const float m1 = __builtin_fmaf((lo ? d0[1] : d0[3]) + dppf<DPP_ROR8>(lo ? d0[3] : d0[1]), k1, minf);
            float mx = fmx(fmx(fmx(d1[0], d1[1]), d1[2]), fmx(fmx(d1[3], m0), m1));
            mx = fmx(mx, dppf<DPP_ROR8>(mx));
            mx = x32mx(x16mx(mx));
            d1 -= mx;
            const unsigned mm = pack2(__builtin_amdgcn_exp2f(m0 - mx), __builtin_amdgcn_exp2f(m1 - mx));
            const unsigned t01 = pack2(__builtin_amdgcn_exp2f(d1[0]), __builtin_amdgcn_exp2f(d1[1])), t23 = pack2(__builtin_amdgcn_exp2f(d1[2]), __builtin_amdgcn_exp2f(d1[3]));
            const bf16x8 pm = mk8(mm, dpp_ror8(mm), 0u, 0u), pt = mk8(t01, t23, dpp_ror8(t01), dpp_ror8(t23));
            const float r = __builtin_amdgcn_rcpf(mfma32(ONES, pt, mfma32(ONES, pm, Z4))[0]) * u2f(V8[4]), rm = dppf<0x141>(r);
            f32x4 o[5];
#pragma unroll
            for (int k = 0; k < 5; ++k) o[k] = mfma32(vox[k], pt, mfma32(k ? mk8(V[4 * k - 4], V[4 * k - 3], 0u, 0u) : mk8(V8[0], V8[1], 0u, 0u), pm, Z4));
#pragma unroll
            for (int i = 0; i < 2; ++i) {
              const float oa = dppf<0x141>(o[2][i]), ob = dppf<0x141>(o[1][2 + i]);
              P[12 + i] = f2u(__builtin_fmaf(hd ? ob : oa, rm, __builtin_fmaf(hd ? o[2][2 + i] : o[1][i], r, i ? 0.f : 32.0f)));
            }
            P[8] = f2u(r); P[9] = f2u(rm);
#pragma unroll
            for (int i = 0; i < 4; ++i) { S[12 + i] = f2u(o[3][i]); S[16 + i] = f2u(o[0][i]); S[20 + i] = f2u(o[4][i]); }
            asm volatile("s_nop 7\n\ts_nop 7" : "+{v[224:255]}"(S), "+{v[208:223]}"(P));
          }
        }
        STAMP(7); STAMPV(13, spins); LOGF(0, spins);
        {
          asm volatile("v_cvt_i32_f32 v218, v220\n\tv_cvt_i32_f32 v219, v221\n\tv_and_or_b32 v218, v218, %7, 2\n\tbuffer_atomic_add_x2 v[218:219], %3, %5, %6 offen\n\t"
                       FM("v222", "v236", "v216", R8_ "0x4") FM("v223", "v237", "v216", R8_ "0x4") "ds_read_b64 %2, %8 offset:%9\n\t"
                       FM("v222", "v238", "v217", MI_ "0x8") FM("v223", "v239", "v217", MI_ "0x8") "v_permlane32_swap_b32 v240, v242\n\t"
                       FM("v222", "v244", "v217", MI_ "0x4") FM("v223", "v245", "v217", MI_ "0x4") "v_permlane32_swap_b32 v241, v243\n\t"
                       FM("v222", "v246", "v216", R8_ "0x8") FM("v223", "v247", "v216", R8_ "0x8")
                       FM("v222", "v240", "v216", ID_ "0x1") FM("v223", "v241", "v216", ID_ "0x1") FM("v222", "v242", "v217", HM_ "0x1") FM("v223", "v243", "v217", HM_ "0x1")
                       "v_cvt_i32_f32 v212, v222\n\tv_cvt_i32_f32 v213, v223\n\tv_and_or_b32 v212, v212, %7, 2\n\tbuffer_atomic_add_x2 v[212:213], %4, %5, %6 offen\n\t"
                       "s_waitcnt lgkmcnt(0)"
                       : "+{v[224:255]}"(S), "+{v[208:223]}"(P), "=&v"(sw) : "v"(acco), "v"(acco2), "s"(rs), "s"(o2), "s"(~63u), "v"(lds0), "n"(L_MISC + 96) : "memory");
          __builtin_amdgcn_sched_barrier(0);
          tpub = now32();
          lds_inc<L_MISC + 104 + 4 * (w & 1)>(lds0);
          asm volatile("buffer_atomic_umax %0, %1, %2, %3 offen" :: "v"(tpub | 1u), "v"(tso), "s"(rs), "s"(o2) : "memory");
          STAMP(10);
        }
      }
    }
    STAMP(8);
    ARRIVE(1);
    o2p = o2;
    PIN_S(o2p);
    if (w < 2) {
      if (__builtin_expect(__builtin_amdgcn_readfirstlane(sw[1]) <= (unsigned)s, 0)) {
        lds_spin<L_MISC + 100, 0>(lds0, (unsigned)s + 1u);
        sw[0] = __hip_atomic_load((unsigned*)(misc + 24), __ATOMIC_RELAXED, __HIP_MEMORY_SCOPE_WORKGROUP);
      }
    } else {
      u32x4 q;
      asm volatile("1:\n\tds_read_b128 %0, %1 offset:%3\n\t.if %4\n\ts_sleep 1\n\t.endif\n\ts_waitcnt lgkmcnt(0)\n\tv_min3_u32 v201, v201, v202, v203\n\tv_cmp_ge_u32 vcc, %2, v201\n\ts_cbranch_vccnz 1b"
                   : "=&{v[200:203]}"(q) : "v"(lds0), "s"(s), "n"(L_MISC + 96), "n"(SP_SL) : "vcc", "memory");
      __builtin_amdgcn_s_setprio(3);
      tpub = now32();
      sw[0] = q[0];
    }
    STAMP(9);
    static_assert(F3C == 0 && F2C == 0 && F0C == 0, "nothing behind the atomics");
    LOGT(0, tpub); LOGT(1, tb2_);
    TSTAMP(1, tpub);
    ARRIVE_OUT();
    const unsigned an = __builtin_amdgcn_readfirstlane(sw[0]);
    const int ahead = (int)(an - tpub);
    tw = tpub + (unsigned)(an == 0u || ahead > 1000 ? SCH_FIRST : imax(ahead, w < 2 ? SCH_DMIN : 0));
    offs = tw - an;
  }
  rows(NSEG - 1);
  STAMP_OUT();
  LOGP(4);
  static_assert(LAG3 == 2 && LAG4 == 3 && LAG5 == 6, "drain schedule");
  unsigned* const cntx = (unsigned*)(misc + 16);
  auto poll = [&](auto load, auto ready) {
    load();
    for (unsigned spins = 0; !ready() && !dead;) {
      if (++spins > SPIN_CAP) dead = true;
      asm volatile("" ::: "memory");
      load();
    }
  };
  auto mark = [&](int k) { if (w == 0) *(volatile unsigned*)(misc + k) = cyc32() | 1u; };
  auto after = [&](int k, unsigned d) { lds_wait((unsigned*)(misc + k), 1u); wait_cyc(__builtin_amdgcn_readfirstlane(*(volatile unsigned*)(misc + k)) + d); };
  const int qt = w >> 1;
  auto t4 = [&](int sl) { poll([&]() { pv.load(rs, ck, sl, gp, qt, lane); }, [&]() { return pv.ready(); }); pv.run(lds, rs, sl, gp, m, qt, lane, true); };
  auto t5 = [&](int sl, int part) { poll([&]() { ot.load(a, rs, sl, b, part); }, [&]() { return ot.ready(b); }); ot.run(lane); ot.store(a, sl, b, lane); };
  BAR();
  const unsigned tp1 = cyc32();
  if (w == 2) {
    TokTask tb = tk;
    tb.load(rs, ts_slot(ck, NSEG - 3)); tk.load(rs, ts_slot(ck, NSEG - 2));
    tb.run(j, lane, SelLo); tb.store(rs, pt_slot(ck, NSEG - 3));
  }
  if (w == 3) { kq_get(NSEG - 1); t2_mem(qk_frag(lds + qo4, lds + qo2, lds + qo2b), qx[0], qx[1], rs, ts_slot(ck, NSEG - 1), h, j, invv * KEXP, lane); }
  if (w == 0) tk.init(h, j, lane);
  LOGD(0);
  if (w == 0) {
    ot.init(b, lane);
    wait_cyc(tp1 + END_E1);
    poll([&]() { tk.load(rs, ts_slot(ck, NSEG - 1)); }, [&]() { return tk.ready(); });
    LOGD(1);
  }
  if (!(w & 1)) {
    tk.run(j, lane, SelLo); tk.store(rs, pt_slot(ck, NSEG - 1 - qt));
    mark(14);
    LOGD(2);
    if (w == 2) ot.init(b, lane);
    ot.load(a, rs, NSEG - 4 + w, b, 2);
    lds_wait(cnt2, 4u * NSEG + 2u);
    after(14, 600); wait_cyc(tp1 + END_E2);
    t4(NSEG - 1);
    mark(15);
    LOGD(3);
  } else {
    ot.init(b, lane);
#pragma unroll 1
    for (int i = 0; i < 3; ++i) {
      wait_cyc(tp1 + (i == 0 ? END_E60 : (i == 1 ? END_E61 : END_E62)));
      t4(NSEG - 4 + i);
      if (i == 1 || (i == 2 && w == 1)) t5(i == 1 ? NSEG - 7 + qt : NSEG - 5, 3);
    }
    ot.load(a, rs, NSEG - 4 + w, b, 2);
    LOGP(5);
  }
  after(15, 600); wait_cyc(tp1 + END_E3);
  t5(NSEG - 4 + w, 1);
  LOGD(4);
  VM_DRAIN();
  if (w != 0) lds_inc<L_MISC + 64>(lds0);
  else {
    lds_wait(cntx, 3u);
    LOGD(5);
    if (lane == 0) __hip_atomic_fetch_add((gu64*)((GAS char*)(a.ws + o2) + acc_off(m * 4, 0)), (u64)1, RLX_AGENT);
    wait_until(now32() + SCH_DRAIN);
    wait_cyc(tp1 + END_E4);
    unsigned x;
    poll([&]() { x = __builtin_amdgcn_raw_buffer_load_b32(rs, acc_off(c * 4, 0), o2, 16); }, [&]() { return wave_all((x & 16u) != 0u); });
    lds_inc<L_MISC + 68>(lds0);
    LOGD(6);
  }
  LOGP(8);
}

__device__ __forceinline__ void proj_front(const RecArgs& a, char* lds, const __amdgpu_buffer_rsrc_t rs, const int v) {
  const int lane = __builtin_amdgcn_workitem_id_x() & 63, b = __builtin_amdgcn_workgroup_id_x(), c = lane & 15, g = lane >> 4;
  const int rb = (b & 7) + 8 * (b >> 5), cq = (b >> 3) & 3;
  const unsigned lds0 = (unsigned)(uintptr_t)(LAS char*)lds;
  unsigned* const cnt = (unsigned*)(lds + L_MISC) + 20;
  LOGF2(0);
  float* const red = (float*)(lds + L_RED1);
  float* const fin = red + 640;
  const f32x4 Z4 = {0.f, 0.f, 0.f, 0.f};
#pragma unroll
  for (int i = 0; i < 8; ++i) x_fetch((const u16*)(a.ws + OFF_OB) + (size_t)rb * 32 * DM, lds0, 0, 8 * v + i, lane);
  bf16x8 W[2][20];
#pragma unroll
  for (int k = 0; k < 40; ++k) W[k & 1][k >> 1] = *(const bf16x8*)(a.ws + OFF_WOBT + ((size_t)(cq * 10 + 2 * v + (k & 1)) * 20 + (k >> 1)) * 1024 + lane * 16);
  const unsigned fo = (unsigned)(rb * 40960 + (cq * 5 + v) * 1024 + ((g >> 1) * 16 + c) * 16), f8 = fo + (g & 1) * 8, f16 = fo + (g & 1) * 20480;
  u32x2 xo[2][2];
#pragma unroll
  for (int k = 0; k < 4; ++k)
    xo[k >> 1][k & 1] = __builtin_bit_cast(u32x2, __hip_atomic_load((const gu64*)((const char*)a.xnew + f8 + (k >> 1) * 512 + (k & 1) * 20480), RLX_AGENT));
  asm volatile("s_waitcnt vmcnt(44)" ::: "memory");
  lds_inc<L_MISC + 80>(lds0);
  lds_wait(cnt, 5u);
  LOGF2(1);
  f32x4 acc[2][2] = {{Z4, Z4}, {Z4, Z4}};
#pragma unroll
  for (int ks = 0; ks < 20; ++ks)
#pragma unroll
    for (int t = 0; t < 2; ++t) {
      const bf16x8 x = *(const bf16x8*)(lds + L_XN + (t * 20 + ks) * 1024 + lane * 16);
#pragma unroll
      for (int i = 0; i < 2; ++i) acc[i][t] = mfma32(W[i][ks], x, acc[i][t]);
    }
  u32x2 xp[2][2];
  PIN_V(acc[1][1]);
  LOGF2(2);
#pragma unroll
  for (int k = 0; k < 4; ++k) {
    const int i = k >> 1, t = k & 1;
#pragma unroll
    for (int q = 0; q < 2; ++q) {
      const unsigned r = pack2(acc[i][t][2 * q], acc[i][t][2 * q + 1]), x = xo[i][t][q];
      xp[i][t][q] = pack2(u2f(x << 16) + u2f(r << 16), u2f(x & 0xffff0000u) + u2f(r & 0xffff0000u));
      acc[i][t][2 * q] = u2f(xp[i][t][q] << 16); acc[i][t][2 * q + 1] = u2f(xp[i][t][q] & 0xffff0000u);
    }
    const float ml = x32sum(x16sum((acc[i][t][0] + acc[i][t][1]) + (acc[i][t][2] + acc[i][t][3]))) * (1.0f / 16.0f);
    float q = 0.f;
#pragma unroll
    for (int r = 0; r < 4; ++r) { const float d = acc[i][t][r] - ml; q += d * d; }
    q = x32sum(x16sum(q));
    if (g == 0) { red[(2 * v + i) * 32 + t * 16 + c] = ml; red[320 + (2 * v + i) * 32 + t * 16 + c] = q; }
  }
  lds_inc<L_MISC + 80>(lds0);
  if (v == 0) {
    lds_wait(cnt, 10u);
    LOGF2(3);
    float mw[10], ml = 0.f, m2 = 0.f;
#pragma unroll
    for (int i = 0; i < 10; ++i) { mw[i] = red[i * 32 + (lane & 31)]; ml += mw[i]; m2 += red[320 + i * 32 + (lane & 31)]; }
    ml *= 0.1f;
#pragma unroll
    for (int i = 0; i < 10; ++i) m2 += 16.0f * (mw[i] - ml) * (mw[i] - ml);
    const unsigned xs = (unsigned)OFF_LNX + rb * 1024;
    if (lane < 32) SC1_STORE8S(((u32x2){f2u(ml), nz(f2u(m2))}), (cq * 32 + lane) * 8, xs);
    wait_cyc(cyc32() + FR_D);
    u32x2 x0, x1;
    for (unsigned spins = 0; spins < SPIN_CAP; ++spins) {
      x0 = __builtin_amdgcn_raw_buffer_load_b64(rs, lane * 8, xs, 16); x1 = __builtin_amdgcn_raw_buffer_load_b64(rs, 512 + lane * 8, xs, 16);
      if (wave_all(x0[1] != 0u && x1[1] != 0u)) break;
      asm volatile("" ::: "memory");
    }
    const float m0 = u2f(x0[0]), m1 = u2f(x1[0]);
    const float mean = x32sum(m0 + m1) * 0.25f;
    const float M2 = x32sum((u2f(x0[1]) + u2f(x1[1])) + 160.0f * ((m0 - mean) * (m0 - mean) + (m1 - mean) * (m1 - mean)));
    const float mb = rbf(mean), den = rbf(__builtin_sqrtf(rbf(rbf(M2 * (1.0f / DM)) + 1e-5f)));
    if (lane < 32) {
      fin[lane] = mb; fin[32 + lane] = 1.0f / den;
      if (cq == 0) __hip_atomic_store((GAS unsigned*)(a.ws + OFF_XNRM) + (rb * 32 + lane) * 4, f2u(1.02f * (M2 + DM * (mean - mb) * (mean - mb)) / (den * den)), RLX_AGENT);
    }
    lds_inc<L_MISC + 84>(lds0);
  }
  lds_wait(cnt + 1, 1u);
  LOGF2(4);
  const __amdgpu_buffer_rsrc_t rx = __builtin_amdgcn_make_buffer_rsrc(a.ws + OFF_XN, 0, (int)(OFF_QB - OFF_XN), 0x00020000);
  auto put = [&](const u32x2 p0, const u32x2 p1, unsigned so) {
    const auto r0 = __builtin_amdgcn_permlane16_swap(p0[0], p1[0], false, false), r1 = __builtin_amdgcn_permlane16_swap(p0[1], p1[1], false, false);
    __builtin_amdgcn_raw_buffer_store_b128((u32x4){r0[0], r1[0], r0[1], r1[1]}, rx, f16, so, 16);
  };
#pragma unroll
  for (int i = 0; i < 2; ++i) {
    u32x2 y[2];
#pragma unroll
    for (int t = 0; t < 2; ++t) {
      const float mb = fin[t * 16 + c], rden = fin[32 + t * 16 + c];
      y[t] = (u32x2){pack2(rbf(acc[i][t][0] - mb) * rden, rbf(acc[i][t][1] - mb) * rden), pack2(rbf(acc[i][t][2] - mb) * rden, rbf(acc[i][t][3] - mb) * rden)};
    }
    put(y[0], y[1], i * 512);
  }
  VM_DRAIN();
  lds_inc<L_MISC + 80>(lds0);
#pragma unroll
  for (int i = 0; i < 2; ++i) put(xp[i][0], xp[i][1], (unsigned)(OFF_XB - OFF_XN) + i * 512);
  if (v == 0) {
    lds_wait(cnt, 15u);
    if (rb < 4 && lane < 8) __hip_atomic_fetch_add((GAS unsigned*)(a.ws + OFF_RDY) + lane * 64, 1u, RLX_AGENT);
  }
  LOGF2(5);
}

__global__ __launch_bounds__(512, 2) void rec_kernel(RecArgs a) {
  __shared__ __attribute__((aligned(16))) char lds[L_BYTES];
  const int tid = __builtin_amdgcn_workitem_id_x(), wid = __builtin_amdgcn_readfirstlane(tid >> 6), w = wid & 3;
  const __amdgpu_buffer_rsrc_t rs = __builtin_amdgcn_make_buffer_rsrc(a.ws, 0, (int)COMM_BYTES, 0x00020000);
  if (tid < 32) ((float*)(lds + L_MISC))[tid] = (tid == 4 || tid == 5) ? 1.0f : 0.0f;
  BAR();
  if (a.layer && wid < 5) proj_front(a, lds, rs, wid);
  if (wid < 4) {
    if (w == 0) rec_waves<0>(a, lds, rs);
    else if (w == 1) rec_waves<1>(a, lds, rs);
    else if (w == 2) rec_waves<2>(a, lds, rs);
    else rec_waves<3>(a, lds, rs);
  } else {
    if (w == 0) token_waves<0>(a, lds, rs);
    else if (w == 1) token_waves<1>(a, lds, rs);
    else if (w == 2) token_waves<2>(a, lds, rs);
    else token_waves<3>(a, lds, rs);
    qkv_tail(a, lds, w);
  }
}

#if defined(__HIP_DEVICE_COMPILE__)
#pragma clang attribute pop
#endif

constexpr int KQ = S_ / 4;
#define ATT_P1 6
#define ATT_P2 10
#define ATT_P3 14
#define ATT_THR 6.0f
#define ATT_NQ 4
#define ATT_LMAX 1.0e12f
#if defined(__HIP_DEVICE_COMPILE__)
#pragma clang attribute push(__attribute__((target("no-packed-fp32-ops"))), apply_to = function)
#endif
__global__ __launch_bounds__(256 * ATT_NQ, 4) void attb_kernel(const u16* __restrict__ Qp, const u16* __restrict__ KV, u16* __restrict__ O) {
  __shared__ float mrg_[ATT_NQ][3][2][13][64];
  const int tid = __builtin_amdgcn_workitem_id_x(), bid = __builtin_amdgcn_workgroup_id_x();
  __shared__ unsigned sink[64];
  own_code<4800>(tid, sink);
  const int lane = tid & 63, wid = __builtin_amdgcn_readfirstlane(tid >> 6), kh = wid / ATT_NQ, r = lane & 31, hh = lane >> 5;
  const int hd = (bid & 7) * 4 + ((bid >> 3) & 3), qb = (bid >> 5) * ATT_NQ + wid % ATT_NQ;
  float (*mrg)[2][13][64] = mrg_[wid % ATT_NQ];
  const int q0 = qb * 64;
  bf16x8 qa[2]; u32x4 qb2[2];
#pragma unroll
  for (int qt = 0; qt < 2; ++qt) {
    { const u32x2* qp = (const u32x2*)(Qp + ((size_t)hd * S_ + q0 + qt * 32 + r) * QST + 8 * hh); const u32x2 q0_ = qp[0], q1_ = qp[1]; qa[qt] = mk8(q0_[0], q0_[1], q1_[0], q1_[1]); }
    const u32x2 t = *(const u32x2*)(Qp + ((size_t)hd * S_ + q0 + qt * 32 + r) * QST + 16);
    qb2[qt] = (u32x4){hh ? 0u : t[0], hh ? 0u : t[1], 0u, 0u};
  }
  const __amdgpu_buffer_rsrc_t rk = __builtin_amdgcn_make_buffer_rsrc((void*)(KV + ((size_t)hd * 64 + kh * 16) * (KVT / 2)), 0, 16 * KVT, 0x00020000);
  unsigned ok1 = lane * 16, ok2 = hh ? 1u << 30 : 1024 + r * 8, ov = r > 20 ? 1u << 30 : 1280 + (hh * 21 + r) * 16;
  f32x16 o[2];
  float m[2];
  const f32x16 Z16 = {0.f, 0.f, 0.f, 0.f, 0.f, 0.f, 0.f, 0.f, 0.f, 0.f, 0.f, 0.f, 0.f, 0.f, 0.f, 0.f};
  struct KvT { u32x4 k1, v0, v1; u32x2 k2; };
  auto fetch = [&](int t) {
    KvT x;
    __builtin_amdgcn_sched_barrier(0);
    x.k1 = __builtin_amdgcn_raw_buffer_load_b128(rk, ok1, t * KVT, 0); x.k2 = __builtin_amdgcn_raw_buffer_load_b64(rk, ok2, t * KVT, 0);
    x.v0 = __builtin_amdgcn_raw_buffer_load_b128(rk, ov, t * KVT, 0); x.v1 = __builtin_amdgcn_raw_buffer_load_b128(rk, ov + 672, t * KVT, 0);
    __builtin_amdgcn_sched_barrier(0);
    return x;
  };
  auto tile = [&](const KvT& x, auto MODE) {
    const bf16x8 ckb = mk8(x.k2[0], x.k2[1], 0x3f803f80u, 0x00003f80u);
    f32x16 sa[2];
#pragma unroll
    for (int qt = 0; qt < 2; ++qt) {
      sa[qt] = __builtin_amdgcn_mfma_f32_32x32x16_bf16(as8(x.k1), qa[qt], Z16, 0, 0, 0);
      sa[qt] = __builtin_amdgcn_mfma_f32_32x32x16_bf16(ckb, as8(qb2[qt]), sa[qt], 0, 0, 0);
    }
#pragma unroll
    for (int qt = 0; qt < 2; ++qt) {
      if (MODE != 0) {
        float mx = fmx(fmx(sa[qt][0], sa[qt][1]), sa[qt][2]);
#pragma unroll
        for (int i = 3; i < 15; i += 2) mx = fmx(fmx(mx, sa[qt][i]), sa[qt][i + 1]);
        mx = fmx(mx, sa[qt][15]);
        if (MODE == 1 || !wave_all(mx <= ATT_THR)) {
          mx = x32mx(mx);
          if (MODE != 1) mx = fmx(mx, 0.f);
          const float mn = m[qt] + mx;
          const unsigned h1 = f2bf(mn); const float r1 = mn - bf2f(h1);
          const unsigned h2 = f2bf(r1); const float r2 = r1 - bf2f(h2);
          const unsigned h3 = f2bf(r2);
          const float me = (bf2f(h1) + bf2f(h2)) + bf2f(h3);
          const float dm = me - m[qt];
          m[qt] = me;
          if (hh == 0) { qb2[qt][2] = (h1 | (h2 << 16)) ^ 0x80008000u; qb2[qt][3] = h3 ^ 0x8000u; }
          const float alpha = __builtin_amdgcn_exp2f(-dm);
#pragma unroll
          for (int i = 0; i < 16; ++i) { if (MODE != 1) o[qt][i] *= alpha; sa[qt][i] -= dm; }
        }
      }
      float p[16];
#pragma unroll
      for (int i = 0; i < 16; ++i) p[i] = __builtin_amdgcn_exp2f(sa[qt][i]);
      bf16x8 p0 = mk8(pack2(p[0], p[1]), pack2(p[2], p[3]), pack2(p[4], p[5]), pack2(p[6], p[7]));
      bf16x8 p1 = mk8(pack2(p[8], p[9]), pack2(p[10], p[11]), pack2(p[12], p[13]), pack2(p[14], p[15]));
      o[qt] = __builtin_amdgcn_mfma_f32_32x32x16_bf16(as8(x.v0), p0, MODE == 1 ? Z16 : o[qt], 0, 0, 0);
      o[qt] = __builtin_amdgcn_mfma_f32_32x32x16_bf16(as8(x.v1), p1, o[qt], 0, 0, 0);
    }
  };
  typedef std::integral_constant<int, 0> I0; typedef std::integral_constant<int, 1> I1; typedef std::integral_constant<int, 2> I2;
  auto pass = [&](auto MODE) {
    asm volatile("" : "+v"(ok1), "+v"(ok2), "+v"(ov) :: "memory");
    m[0] = m[1] = 0.f;
    qb2[0][2] = qb2[0][3] = qb2[1][2] = qb2[1][3] = 0u;
    KvT xa = fetch(0), xb = fetch(1);
    tile(xa, I1{});
    xa = fetch(2);
    tile(xb, MODE);
#pragma unroll 1
    for (int t = 2; t < KQ / 32; t += 2) {
      if (t == ATT_P1) __builtin_amdgcn_s_setprio(2);
      if (t == ATT_P2) __builtin_amdgcn_s_setprio(1);
      if (t == ATT_P3) __builtin_amdgcn_s_setprio(0);
      xb = fetch(t + 1);
      tile(xa, MODE);
      xa = fetch(t + 2);
      tile(xb, MODE);
    }
  };
  if (ATT_P1) __builtin_amdgcn_s_setprio(3);
  pass(I0{});
  {
    float big = 0.f;
#pragma unroll
    for (int qt = 0; qt < 2; ++qt) {
      big = fmx(big, hh ? o[qt][8] * (3.0e38f / ATT_LMAX) : __builtin_fabsf(o[qt][8]));
#pragma unroll
      for (int i = 0; i < 12; ++i) if (i != 8) big = fmx(big, (i > 8 && hh) ? 0.f : __builtin_fabsf(o[qt][i]));
    }
    if (__builtin_expect(!wave_all(big <= 3.0e38f), 0)) pass(I2{});
  }
  if (kh > 0) {
#pragma unroll
    for (int qt = 0; qt < 2; ++qt) {
#pragma unroll
      for (int i = 0; i < 12; ++i) mrg[kh - 1][qt][i][lane] = o[qt][i];
      mrg[kh - 1][qt][12][lane] = m[qt];
    }
  }
  asm volatile("s_waitcnt lgkmcnt(0)\n\ts_barrier" ::: "memory");
  if (kh == 0) {
#pragma unroll
    for (int qt = 0; qt < 2; ++qt) {
      float m1 = mrg[0][qt][12][lane], m2 = mrg[1][qt][12][lane], m3 = mrg[2][qt][12][lane];
      float mn = fmx(fmx(m[qt], m1), fmx(m2, m3));
      float a0 = __builtin_amdgcn_exp2f(m[qt] - mn), a1 = __builtin_amdgcn_exp2f(m1 - mn);
      float a2 = __builtin_amdgcn_exp2f(m2 - mn), a3 = __builtin_amdgcn_exp2f(m3 - mn);
      float v[12];
#pragma unroll
      for (int i = 0; i < 12; ++i) v[i] = (o[qt][i] * a0 + mrg[0][qt][i][lane] * a1) + (mrg[1][qt][i][lane] * a2 + mrg[2][qt][i][lane] * a3);
      auto lr = __builtin_amdgcn_permlane32_swap(f2u(v[8]), f2u(v[8]), false, false);
      float inv = __builtin_amdgcn_rcpf(u2f(lr[1]));
      const int row = q0 + qt * 32 + r;
      auto oaddr = [&](int k) { return O + (((size_t)(row >> 4) * 20 + (k >> 5)) * 64 + ((k & 31) >> 3) * 16 + (row & 15)) * 8 + (k & 7); };
      const int k0 = hd * BD + 4 * hh;
      *(u32x2*)oaddr(k0) = (u32x2){pack2(v[0] * inv, v[1] * inv), pack2(v[2] * inv, v[3] * inv)};
      *(u32x2*)oaddr(k0 + 8) = (u32x2){pack2(v[4] * inv, v[5] * inv), pack2(v[6] * inv, v[7] * inv)};
      if (hh == 0) *(u32x2*)oaddr(k0 + 16) = (u32x2){pack2(v[8] * inv, v[9] * inv), pack2(v[10] * inv, v[11] * inv)};
    }
  }
}
#if defined(__HIP_DEVICE_COMPILE__)
#pragma clang attribute pop
#endif

__global__ __launch_bounds__(640, 3) void proj_kernel(const u16* __restrict__ Of, const u16* __restrict__ Wf, const u16* __restrict__ xold,
                                                      u16* __restrict__ xnew) {
  __shared__ __attribute__((aligned(16))) char ob[40960];
  const int tid = threadIdx.x, lane = tid & 63, w = __builtin_amdgcn_readfirstlane(tid >> 6), g = lane >> 4, c = lane & 15;
  __shared__ unsigned sink[64];
  own_code<1500>(tid, sink);
  const int rb = (blockIdx.x & 7) + 8 * (blockIdx.x >> 5), cb = (blockIdx.x >> 3) & 3;
  const int col = cb * 160 + w * 16 + 4 * g;
  const u16* wp = Wf + (size_t)(cb * 10 + w) * 20 * 512 + lane * 8;
  u32x4 st[4];
#pragma unroll
  for (int i = 0; i < 4; ++i) st[i] = *(const u32x4*)(Of + (size_t)rb * 40 * 512 + (i * 640 + tid) * 8);
  bf16x8 wf[20];
#pragma unroll
  for (int ks = 0; ks < 20; ++ks) wf[ks] = *(const bf16x8*)(wp + (size_t)ks * 512);
  u32x2 xo[2];
#pragma unroll
  for (int t = 0; t < 2; ++t) xo[t] = *(const u32x2*)(xold + (((size_t)(rb * 2 + t) * 20 + (col >> 5)) * 64 + ((col & 31) >> 3) * 16 + c) * 8 + (col & 7));
#pragma unroll
  for (int i = 0; i < 4; ++i) *(u32x4*)(ob + (i * 640 + tid) * 16) = st[i];
  BAR();
  f32x4 acc[2] = {{0.f, 0.f, 0.f, 0.f}, {0.f, 0.f, 0.f, 0.f}};
#pragma unroll
  for (int ks = 0; ks < 20; ++ks)
#pragma unroll
    for (int t = 0; t < 2; ++t) acc[t] = mfma32(wf[ks], *(const bf16x8*)(ob + (t * 20 + ks) * 1024 + lane * 16), acc[t]);
#pragma unroll
  for (int t = 0; t < 2; ++t) {
    const unsigned r01 = pack2(acc[t][0], acc[t][1]), r23 = pack2(acc[t][2], acc[t][3]);
    const unsigned p01 = pack2(u2f(xo[t][0] << 16) + u2f(r01 << 16), u2f(xo[t][0] & 0xffff0000u) + u2f(r01 & 0xffff0000u));
    const unsigned p23 = pack2(u2f(xo[t][1] << 16) + u2f(r23 << 16), u2f(xo[t][1] & 0xffff0000u) + u2f(r23 & 0xffff0000u));
    *(u32x2*)(xnew + (size_t)(rb * 32 + t * 16 + c) * DM + col) = (u32x2){p01, p23};
  }
}

extern "C" void kernel_launch(void* const* d_in, const int* in_sizes, int n_in, void* d_out, int out_size, void* d_ws,
                              size_t ws_size, hipStream_t stream) {
  const u16* A_x = (const u16*)d_in[0];
  const u16* A_mem0 = (const u16*)d_in[1];
  const u16* A_Wq = (const u16*)d_in[2];
  const u16* A_Wk = (const u16*)d_in[3];
  const u16* A_Wv = (const u16*)d_in[4];
  const u16* A_Wo = (const u16*)d_in[5];
  const u16* B_wqkv = (const u16*)d_in[8];
  const u16* B_wo = (const u16*)d_in[9];
  char* ws = (char*)(((uintptr_t)d_ws + ((size_t)WS_OFF << 20) + WS_AL - 1) & ~(uintptr_t)(WS_AL - 1));
  u16* xa = (u16*)(ws + OFF_XA); u16* Ob = (u16*)(ws + OFF_OB);

  prologue_kernel<<<256, 640, 0, stream>>>(ProArgs{A_x, A_Wq, A_Wk, A_Wv, A_Wo, B_wqkv, B_wo, ws});
  for (int layer = 0; layer < 2; ++layer) {
    rec_kernel<<<256, 512, 0, stream>>>(RecArgs{A_mem0, A_Wq, A_Wk, (const u16*)(ws + OFF_XB), xa, ws, (unsigned)layer, 0u});
    attb_kernel<<<BH * 32 / ATT_NQ, 256 * ATT_NQ, 0, stream>>>((const u16*)(ws + OFF_QB), (const u16*)(ws + OFF_KV), Ob);
  }
  proj_kernel<<<256, 640, 0, stream>>>(Ob, (const u16*)(ws + OFF_WOBT), xa, (u16*)d_out);
}
```

```cpp
#include <hip/hip_runtime.h>
#include <hip/hip_bf16.h>
#include <stdint.h>
#include <type_traits>

typedef unsigned short u16;
typedef unsigned long long u64;
typedef __attribute__((ext_vector_type(8))) short bf16x8;
typedef __attribute__((ext_vector_type(16))) short bf16x16;
typedef __attribute__((ext_vector_type(4))) float f32x4;
typedef __attribute__((ext_vector_type(2))) float f32x2;
typedef __attribute__((ext_vector_type(16))) float f32x16;
typedef __attribute__((ext_vector_type(4))) unsigned u32x4;
typedef __attribute__((ext_vector_type(2))) unsigned u32x2;
typedef __attribute__((ext_vector_type(3))) unsigned u32x3;
typedef __attribute__((ext_vector_type(8))) unsigned u32x8;
typedef __attribute__((ext_vector_type(16))) unsigned u32x16;
typedef __attribute__((ext_vector_type(32))) unsigned u32x32;
#define GAS __attribute__((address_space(1)))
#define LAS __attribute__((address_space(3)))
typedef GAS unsigned long long gu64;
#define BAR() do { __builtin_amdgcn_s_waitcnt(0xC07F); asm volatile("s_waitcnt lgkmcnt(0)\n\ts_barrier" ::: "memory"); } while (0)

constexpr int S_ = 2048, DM = 640;
constexpr int NH = 32, DH = 192, HD = NH * DH;
constexpr int MEM = 8, SEG = 32, NSEG = S_ / SEG;
constexpr int BH = 32, BD = 20;

constexpr size_t al256(size_t x) { return (x + 255) & ~(size_t)255; }
constexpr int LAG3 = 2, LAG4 = 3, LAG5 = 6;
#define SR_ 8
constexpr int SR = SR_;
constexpr unsigned SCT_STEP = 131072, OFF_SCM = SR * SCT_STEP, SCM_STEP = 32768;
constexpr int ACC_NB = 192;
constexpr size_t ACC_STEP = 65536;
constexpr unsigned ACC_TS = ACC_NB * 256;
constexpr size_t ACC_LAYER = SR * ACC_STEP;
constexpr size_t OFF_ACC = 3 * 524288;
static_assert(SR == 8, "regions");
__device__ __forceinline__ unsigned ts_piece(int i) { return ACC_TS + (unsigned)(i < 128 ? (i >> 1) * 256 + (i & 1) * 16 : 128 + (i & 1) * 8192); }
constexpr size_t OFF_LNX = OFF_SCM + SR * SCM_STEP;
constexpr size_t OFF_RDY = OFF_LNX + 65536;
constexpr size_t PG = 2097152;
#define RING_ 4
constexpr int RING = RING_;
constexpr size_t TS_SLOT = (size_t)NH * 8 * 2560, PT_SLOT = (size_t)NH * 2560, OP_SLOT = (size_t)256 * 2560;
__device__ __forceinline__ unsigned ts_tok(int h, int j) { return (unsigned)((h & 15) * 32768 + (j >> 2) * 16384 + (j & 3) * 2048); }
__device__ __forceinline__ unsigned ts_mem(int h, int j) { return (unsigned)(524288 + ((h & 15) >> 1) * 16384 + (h & 1) * 4096 + j * 512); }
__device__ __forceinline__ unsigned pt_tok(int h) { return (unsigned)(((h & 15) >> 2) * 16384 + (h & 3) * 2048); }
__device__ __forceinline__ unsigned pt_mem(int h) { return (unsigned)(65536 + (h & 15) * 512); }
constexpr size_t OFF_X = 2 * PG + TS_SLOT + 3 * PT_SLOT, X_BYTES = 7 * OP_SLOT + 5 * PT_SLOT;
constexpr size_t OFF_OP = OFF_X + TS_SLOT + 5 * PT_SLOT;
constexpr size_t COMM_BYTES = OFF_X + X_BYTES + RING * OP_SLOT;
__device__ __forceinline__ unsigned near_half(unsigned ck, unsigned o) { return o + ((ck >> (o >= 2 * PG ? 8 : 4)) & 8192u); }
__device__ __forceinline__ unsigned ts_slot(unsigned ck, int sl) {
  return near_half(ck, sl == NSEG - 1 ? (unsigned)OFF_X : ((sl & 3) == 3 ? (unsigned)(2 * PG) : (unsigned)PG + (unsigned)(sl & 3) * (unsigned)TS_SLOT));
}
__device__ __forceinline__ unsigned pt_slot(unsigned ck, int sl) {
  return near_half(ck, sl >= NSEG - 5 ? (unsigned)(OFF_X + TS_SLOT) + (unsigned)(sl - (NSEG - 5)) * (unsigned)PT_SLOT
                                      : ((sl & 3) == 0 ? (unsigned)(PG + 3 * TS_SLOT) : (unsigned)(2 * PG + TS_SLOT - PT_SLOT) + (unsigned)(sl & 3) * (unsigned)PT_SLOT));
}
__device__ __forceinline__ unsigned op_slot(int sl) { return (unsigned)OFF_OP + (unsigned)(sl >= NSEG - 6 ? sl - (NSEG - 6) : 6 + sl % RING) * (unsigned)OP_SLOT; }
static_assert(RING == 4 && OFF_OP <= 3 * PG && OFF_RDY + 2048 <= OFF_ACC && OFF_ACC + ACC_LAYER == PG && TS_SLOT % 16384 == 0 && PT_SLOT % 16384 == 0, "pages");
static_assert(TS_SLOT == OP_SLOT && RING >= 4 && NSEG % SR == 0 && SR >= 5 && X_BYTES % 4096 == 0 && X_BYTES <= 256 * 20 * 1024, "slots");
constexpr size_t OFF_WB = al256(COMM_BYTES);
constexpr size_t OFF_BNRM = OFF_WB + (size_t)256 * 100 * 1024;
constexpr size_t OFF_LAT = OFF_BNRM + 1024;
constexpr size_t OFF_XNRM = OFF_LAT + 2048;
constexpr size_t OFF_WQKVBT = OFF_XNRM + al256(S_ * 16);
constexpr size_t OFF_WOBT = OFF_WQKVBT + al256((size_t)3 * DM * DM * 2);
constexpr size_t OFF_XN = OFF_WOBT + al256((size_t)DM * DM * 2);
constexpr int QST = BD;
constexpr size_t OFF_XA = OFF_XN + al256((size_t)S_ * DM * 2);
constexpr size_t OFF_XB = OFF_XA + al256((size_t)S_ * DM * 2);
constexpr size_t OFF_QB = OFF_XB + al256((size_t)S_ * DM * 2);
constexpr int KVT = 2624;
constexpr size_t OFF_KV = OFF_QB + al256((size_t)BH * S_ * QST * 2);
constexpr size_t OFF_OB = OFF_KV + al256((size_t)BH * 64 * KVT);
constexpr size_t OFF_DBG = OFF_OB + al256((size_t)S_ * DM * 2);
constexpr size_t WS_TOTAL = OFF_DBG + (size_t)2 * 64 * 8 * DM * 2;
#define WS_AL 2097152
#define WS_OFF 0
static_assert(WS_TOTAL + WS_AL + ((size_t)WS_OFF << 20) <= ((size_t)256 << 20), "workspace budget");

#if defined(__HIP_DEVICE_COMPILE__)
#pragma clang attribute push(__attribute__((target("no-packed-fp32-ops"))), apply_to = function)
#endif
__device__ __forceinline__ float u2f(unsigned v) { return __builtin_bit_cast(float, v); }
__device__ __forceinline__ unsigned f2u(float v) { return __builtin_bit_cast(unsigned, v); }
__device__ __forceinline__ float bf2f(unsigned v) { return __builtin_bit_cast(float, v << 16); }
__device__ __forceinline__ unsigned f2bf(float f) { __bf16 b = (__bf16)f; return (unsigned)__builtin_bit_cast(u16, b); }
__device__ __forceinline__ float rbf(float f) { return bf2f(f2bf(f)); }
typedef __attribute__((ext_vector_type(2))) __bf16 hbf16x2;
__device__ __forceinline__ unsigned pack2(float lo, float hi) { f32x2 v = {lo, hi}; hbf16x2 b = __builtin_convertvector(v, hbf16x2); return __builtin_bit_cast(unsigned, b); }
__device__ __forceinline__ f32x4 mfma32(bf16x8 a, bf16x8 b, f32x4 c) { return __builtin_amdgcn_mfma_f32_16x16x32_bf16(a, b, c, 0, 0, 0); }
typedef __attribute__((ext_vector_type(8))) __bf16 hbf16x8;
typedef __attribute__((ext_vector_type(16))) __bf16 hbf16x16;
__device__ __forceinline__ f32x4 smfma64(bf16x8 a, bf16x16 b, f32x4 c, int idx) {
  return __builtin_amdgcn_smfmac_f32_16x16x64_bf16(__builtin_bit_cast(hbf16x8, a), __builtin_bit_cast(hbf16x16, b), c, idx, 0, 0);
}
__device__ __forceinline__ bf16x16 cat16(bf16x8 a, bf16x8 b) { return __builtin_shufflevector(a, b, 0, 1, 2, 3, 4, 5, 6, 7, 8, 9, 10, 11, 12, 13, 14, 15); }
__device__ __forceinline__ bf16x8 half8(bf16x16 v, int hi) { return hi ? __builtin_shufflevector(v, v, 8, 9, 10, 11, 12, 13, 14, 15) : __builtin_shufflevector(v, v, 0, 1, 2, 3, 4, 5, 6, 7); }
__device__ __forceinline__ bf16x8 mk8(unsigned a, unsigned b, unsigned c, unsigned d) { u32x4 v = {a, b, c, d}; return __builtin_bit_cast(bf16x8, v); }
__device__ __forceinline__ unsigned dpp_ror8(unsigned v) { return (unsigned)__builtin_amdgcn_mov_dpp((int)v, 0x128, 0xf, 0xf, true); }
__device__ __forceinline__ float fmx(float a, float b) { return __builtin_elementwise_maximum(a, b); }

__device__ __forceinline__ int imax(int a, int b) { return a > b ? a : b; }
__device__ __forceinline__ int imin(int a, int b) { return a < b ? a : b; }
__device__ __forceinline__ unsigned umin(unsigned a, unsigned b) { return a < b ? a : b; }
__device__ __forceinline__ unsigned umax(unsigned a, unsigned b) { return a > b ? a : b; }
__device__ __forceinline__ bool wave_all(bool p) { return __builtin_amdgcn_ballot_w64(!p) == 0ull; }
__device__ __forceinline__ float x16mx(float x) { unsigned u = __builtin_bit_cast(unsigned, x); auto r = __builtin_amdgcn_permlane16_swap(u, u, false, false); return fmx(u2f(r[0]), u2f(r[1])); }
__device__ __forceinline__ float x32mx(float x) { unsigned u = __builtin_bit_cast(unsigned, x); auto r = __builtin_amdgcn_permlane32_swap(u, u, false, false); return fmx(u2f(r[0]), u2f(r[1])); }
__device__ __forceinline__ float x16sum(float x) { unsigned u = __builtin_bit_cast(unsigned, x); auto r = __builtin_amdgcn_permlane16_swap(u, u, false, false); return u2f(r[0]) + u2f(r[1]); }
__device__ __forceinline__ float x32sum(float x) { unsigned u = __builtin_bit_cast(unsigned, x); auto r = __builtin_amdgcn_permlane32_swap(u, u, false, false); return u2f(r[0]) + u2f(r[1]); }
template <int CTRL> __device__ __forceinline__ float dppf(float v) { return u2f((unsigned)__builtin_amdgcn_mov_dpp((int)f2u(v), CTRL, 0xf, 0xf, true)); }
#define OWN_PF 1
template <int BYTES> __device__ __forceinline__ void own_code(int tid, unsigned* sink) {
  if (!OWN_PF || __builtin_amdgcn_readfirstlane(tid >> 6) * 4096 >= BYTES) return;
  const char* pc; unsigned keep;
  asm volatile("s_getpc_b64 %0" : "=s"(pc));
  asm volatile("s_mov_b32 %0, m0\n\ts_mov_b32 m0, %3\n\ts_nop 0\n\tglobal_load_lds_dword %1, %2\n\ts_mov_b32 m0, %0"
               : "=&s"(keep) : "v"(tid * 64 < BYTES ? tid * 64 : 0), "s"(pc), "s"((unsigned)(uintptr_t)(LAS unsigned*)sink) : "memory");
}
constexpr int DPP_XOR1 = 0xB1, DPP_XOR2 = 0x4E, DPP_ROR4 = 0x124, DPP_ROR8 = 0x128;
#if defined(__HIP_DEVICE_COMPILE__)
#pragma clang attribute pop
#endif

typedef short v4i16_t __attribute__((ext_vector_type(4)));
__device__ __forceinline__ bf16x8 tr_frag(const char* lo_, const char* hi_) {
  const v4i16_t lo = __builtin_amdgcn_ds_read_tr16_b64_v4i16((LAS v4i16_t*)lo_), hi = __builtin_amdgcn_ds_read_tr16_b64_v4i16((LAS v4i16_t*)hi_);
  return (bf16x8){lo[0], lo[1], lo[2], lo[3], hi[0], hi[1], hi[2], hi[3]};
}
__device__ __forceinline__ int rec_wg(int gp, int m) { return (((gp & 1) * 16 + m) << 3) | (gp >> 1); }
__device__ __forceinline__ float wsum(float v) {
  v += dppf<DPP_XOR1>(v); v += dppf<DPP_XOR2>(v); v += dppf<DPP_ROR4>(v); v += dppf<DPP_ROR8>(v);
  return x32sum(x16sum(v));
}
__device__ __forceinline__ float ln_row(int lane, u32x4 (&xo)[2]) {
  const bool two = lane < 16;
  float v[16];
#pragma unroll
  for (int hf = 0; hf < 2; ++hf)
#pragma unroll
    for (int i = 0; i < 4; ++i) { const unsigned u = xo[hf][i]; v[hf * 8 + 2 * i] = bf2f(u & 0xffff); v[hf * 8 + 2 * i + 1] = bf2f(u >> 16); }
  float s0 = 0.f, s1 = 0.f;
#pragma unroll
  for (int i = 0; i < 8; ++i) { s0 += v[i]; s1 += v[8 + i]; }
  const float mean = wsum(two ? s0 + s1 : s0) * (1.0f / DM);
  s0 = s1 = 0.f;
#pragma unroll
  for (int i = 0; i < 8; ++i) { const float d0 = v[i] - mean, d1 = v[8 + i] - mean; s0 += d0 * d0; s1 += d1 * d1; }
  const float var = rbf(wsum(two ? s0 + s1 : s0) * (1.0f / DM));
  const float mb = rbf(mean);
  const float rden = 1.0f / rbf(sqrtf(rbf(var + 1e-5f)));
  float n2[2] = {0.f, 0.f};
#pragma unroll
  for (int hf = 0; hf < 2; ++hf) {
    float y[8];
#pragma unroll
    for (int i = 0; i < 8; ++i) { y[i] = rbf(rbf(v[hf * 8 + i] - mb) * rden); n2[hf] += y[i] * y[i]; }
    xo[hf] = (u32x4){pack2(y[0], y[1]), pack2(y[2], y[3]), pack2(y[4], y[5]), pack2(y[6], y[7])};
  }
  return wsum(two ? n2[0] + n2[1] : n2[0]);
}

#define PROT(k)
#define WARM_CODE 1
#define PRB_N0 0
#define PRB_N1 3
#define PRB_LOG(i, v)
#define PLD(p) __builtin_nontemporal_load((const u32x4*)(p))
struct ProArgs { const u16* x; const u16* wq; const u16* wk; const u16* wv; const u16* wo; const u16* wqkvb; const u16* wob; char* ws; };
__global__ __launch_bounds__(640, 3) void prologue_kernel(ProArgs a) {
  __shared__ __attribute__((aligned(16))) char img[192 * 320];
  __shared__ __attribute__((aligned(16))) char slots[10 * 6144];
  __shared__ float red[10][160];
  __shared__ unsigned latw[8];
  const int b = blockIdx.x, tid = threadIdx.x, lane = tid & 63, v = __builtin_amdgcn_readfirstlane(tid >> 6), c = lane & 15, g = lane >> 4;
  const int h = (b & 7) * 4 + ((b >> 3) & 3), dh = (b >> 5) & 1, cq = b >> 6, ks = dh * 10 + v;
  char* const slot = slots + v * 6144;
  PROT(0);
  __shared__ unsigned sink[64];
  own_code<9600>(tid, sink);
  const unsigned zo = v < 8 ? (unsigned)(((b >> 6) * 512 + ((b & 7) * 2 + (v >> 2)) * 32 + ((b >> 3) & 7) * 4 + (v & 3)) * 1024) : (unsigned)(((v - 7) * 2048 + (b & 7) * 32 + ((b >> 3) & 7) * 4 + (b >> 6)) * 1024);
  __builtin_amdgcn_raw_buffer_store_b128((u32x4){0u, 0u, 0u, 0u}, __builtin_amdgcn_make_buffer_rsrc(a.ws, 0, (int)(3 * PG), 0x00020000), zo + lane * 16, 0, 16);
  __builtin_amdgcn_sched_barrier(0);
  u32x4 wa[2][6], wb[6], tr[4], xo[2];
  const int xrow = b * 8 + (v & 7);
  xo[0] = *(const u32x4*)(a.x + (size_t)xrow * DM + lane * 8);
  xo[1] = *(const u32x4*)(a.x + (size_t)xrow * DM + 512 + c * 8);
  const int job = imin(b + 256 * (9 - v), 800);
  const int jn = job < 600 ? job / 20 : (job % 200) / 20, jk = job % 20;
  {
    const u16* src = job < 600 ? a.wqkvb + (size_t)(32 * jk) * 3 * DM : a.wob + (size_t)(32 * jk) * DM;
#pragma unroll
    for (int i = 0; i < 4; ++i) tr[i] = *(const u32x4*)(src + (size_t)(i * 8 + (lane >> 3)) * (job < 600 ? 3 * DM : DM) + jn * 64 + (lane & 7) * 8);
  }
#pragma unroll
  for (int i = 0; i < 6; ++i) {
    const int idx = (v * 6 + i) * 64 + lane, row = idx / 20;
    wb[i] = PLD(a.wo + (size_t)(h * DH + row) * DM + cq * 160 + (idx - row * 20) * 8);
  }
#pragma unroll
  for (int i = 0; i < 6; ++i) {
    const int idx = i * 64 + lane, r = idx / 24;
#pragma unroll
    for (int u = 0; u < 2; ++u) wa[u][i] = PLD(a.wv + (size_t)(32 * ks + 8 * (r >> 2) + (r & 3) + 4 * u) * HD + h * DH + (idx - r * 24) * 8);
  }
  unsigned pf[2] = {0u, 0u};
  auto prefetch = [&]() {
#pragma unroll
    for (int i = 0; i < 2; ++i) pf[i] = *(const unsigned*)((i ? a.wq : a.wk) + ((size_t)(b * 10 + v) * 48 + imin(lane, 47)) * 32);
  };
  prefetch();
  __builtin_amdgcn_sched_barrier(0);
  unsigned lat = ~0u;
  auto probe = [&](int n0, int n) {
    const char* p = a.ws + zo;
#pragma unroll 1
    for (int i = n0; i < n0 + n; ++i) {
      u64 t0, t1; unsigned d;
      asm volatile("s_memtime %0\n\ts_waitcnt lgkmcnt(0)\n\ts_load_dword %2, %3, %4\n\ts_waitcnt lgkmcnt(0)\n\ts_memtime %1\n\ts_waitcnt lgkmcnt(0)"
                   : "=&s"(t0), "=&s"(t1), "=&s"(d) : "s"(p), "s"((i & 3) * 256 + (i >> 2) * 128) : "memory");
      PRB_LOG(i, (unsigned)t1 - (unsigned)t0);
      lat = umin(lat, (unsigned)t1 - (unsigned)t0);
    }
  };
  if (PRB_N0) { asm volatile("s_waitcnt vmcnt(26)" ::: "memory"); probe(0, PRB_N0); }
  __builtin_amdgcn_sched_barrier(0);
  const size_t xfo = (((size_t)(xrow >> 4) * 20 + (lane >> 2)) * 64 + (lane & 3) * 16 + (xrow & 15)) * 8;
  if (v < 8) {
    *(u32x4*)((u16*)(a.ws + OFF_XB) + xfo) = xo[0];
    if (lane < 16) *(u32x4*)((u16*)(a.ws + OFF_XB) + xfo + 16 * 512) = xo[1];
  }
  const float xn2 = u2f(__builtin_amdgcn_readfirstlane(f2u(ln_row(lane, xo))));
#pragma unroll
  for (int i = 0; i < 4; ++i) *(u32x4*)(slot + (i * 8 + (lane >> 3)) * 144 + (lane & 7) * 16) = tr[i];
  __builtin_amdgcn_fence(__ATOMIC_RELEASE, "wavefront"); __builtin_amdgcn_wave_barrier();
  bf16x8 tf[4];
#pragma unroll
  for (int t = 0; t < 4; ++t) {
    const char* bp = slot + (8 * g + (c >> 2)) * 144 + t * 32 + (c & 3) * 8;
    tf[t] = tr_frag(bp, bp + 4 * 144);
  }
  __builtin_amdgcn_fence(__ATOMIC_RELEASE, "wavefront"); __builtin_amdgcn_wave_barrier();
  __builtin_amdgcn_sched_barrier(0);
#pragma unroll
  for (int i = 0; i < 6; ++i) {
    const int idx = (v * 6 + i) * 64 + lane, row = idx / 20;
    *(u32x4*)(img + row * 320 + (((idx - row * 20) ^ ((row >> 2) & 2)) * 16)) = wb[i];
  }
  bf16x8 A[2][6];
#pragma unroll
  for (int u = 0; u < 2; ++u) {
#pragma unroll
    for (int i = 0; i < 6; ++i) {
      const int idx = i * 64 + lane, r = idx / 24;
      *(u32x4*)(slot + r * 384 + (((idx - r * 24) ^ (r >> 1)) * 16)) = wa[u][i];
    }
    __builtin_amdgcn_fence(__ATOMIC_RELEASE, "wavefront"); __builtin_amdgcn_wave_barrier();
#pragma unroll
    for (int e = 0; e < 6; ++e) A[u][e] = *(const bf16x8*)(slot + c * 384 + (((4 * e + g) ^ (c >> 1)) * 16));
    __builtin_amdgcn_fence(__ATOMIC_RELEASE, "wavefront"); __builtin_amdgcn_wave_barrier();
  }
  BAR();
  PROT(1);
  asm volatile("" :: "v"(pf[0]), "v"(pf[1]));
  if (v < 8) {
    u16* xn = (u16*)(a.ws + OFF_XN) + xfo;
    *(u32x4*)xn = xo[0];
    if (lane < 16) *(u32x4*)(xn + 16 * 512) = xo[1];
    if (lane < 4) ((float*)(a.ws + OFF_XNRM))[xrow * 4 + lane] = lane == 0 ? xn2 : 0.f;
  }
  if (job < 800) {
    u16* dst = (u16*)(a.ws + (job < 600 ? OFF_WQKVBT : OFF_WOBT)) + ((size_t)jn * 80 + jk) * 512 + lane * 8;
#pragma unroll
    for (int t = 0; t < 4; ++t) *(bf16x8*)(dst + (size_t)t * 20 * 512) = tf[t];
  }
  const int gp = h >> 1, hh = h & 1;
  const __amdgpu_buffer_rsrc_t rwb = __builtin_amdgcn_make_buffer_rsrc(a.ws + OFF_WB, 0, 256 * 100 * 1024, 0x00020000);
#pragma unroll
  for (int n = 0; n < 10; ++n) {
    const int off = n * 32 + 8 * (c & 3), T = cq * 10 + n;
    const char* bp = img + (8 * g + (c >> 2)) * 320 + (off ^ ((g & 1) << 5));
    f32x4 ac[2] = {{0.f, 0.f, 0.f, 0.f}, {0.f, 0.f, 0.f, 0.f}};
#pragma unroll
    for (int e = 0; e < 6; ++e) {
      const bf16x8 bf = tr_frag(bp + e * 32 * 320, bp + (e * 32 + 4) * 320);
      ac[0] = mfma32(A[0][e], bf, ac[0]); ac[1] = mfma32(A[1][e], bf, ac[1]);
    }
    const u32x4 o = {pack2(ac[0][0], ac[0][1]), pack2(ac[0][2], ac[0][3]), pack2(ac[1][0], ac[1][1]), pack2(ac[1][2], ac[1][3])};
    const unsigned oo = T < 32 ? (unsigned)((rec_wg(gp, T & 15) * 100 + (hh * 3 + (T >> 4)) * 20 + ks) * 1024 + lane * 16)
                               : (unsigned)((rec_wg(gp, 2 * (T - 32) + (c >> 3)) * 100 + 40 + ks) * 1024 + (g * 16 + hh * 8 + (c & 7)) * 16);
    __builtin_amdgcn_raw_buffer_store_b128(o, rwb, oo, 0, 0);
    float n2 = 0.f;
#pragma unroll
    for (int r = 0; r < 4; ++r) n2 += ac[0][r] * ac[0][r] + ac[1][r] * ac[1][r];
    n2 = x32sum(x16sum(n2));
    if (g == 0) red[v][n * 16 + c] = n2;
  }
  PROT(2);
  probe(PRB_N0, PRB_N1);
  if (v < 8) latw[v] = lat;
  else if (b < 64 && lane == 0) ((unsigned*)(a.ws + OFF_LAT))[(v - 4) * 64 + b] = umin(lat, 60000u);
  if (WARM_CODE && v == 7) {
    const char* pc; int d0, d1;
    asm("s_getpc_b64 %0\n\ts_mov_b32 %1, _Z10rec_kernel7RecArgs@rel32@lo+4\n\ts_mov_b32 %2, _Z11proj_kernelPKtS0_S0_Pt@rel32@lo+12" : "=s"(pc), "=s"(d0), "=s"(d1));
    const char *c0 = pc + d0, *c1 = pc + d1;
    const long o = ((long)b * 64 + lane) * 64;
    if (o < c1 - c0) asm volatile("" :: "v"(*(const volatile GAS unsigned*)(c0 + o)));
  }
  if (v == 9 && lane < 32) {
    const int q = b * 32 + lane;
    *(u32x4*)(a.ws + OFF_KV + (size_t)(q >> 2) * KVT + 1280 + (q & 3) * 336 + 320) = (u32x4){0x3f803f80u, 0x3f803f80u, 0x3f803f80u, 0x3f803f80u};
  }
  BAR();
  if (v == 1 && lane == 0) ((unsigned*)(a.ws + OFF_LAT))[b] = umin(umin(umin(latw[0], latw[4]) + umin(latw[1], latw[5]), 30000u) + umin(umin(latw[2], latw[6]) + umin(latw[3], latw[7]), 30000u), 60000u);
  if (v == 0) {
    float mx = 0.f;
#pragma unroll
    for (int k = 0; k < 3; ++k) {
      const int col = k * 64 + lane;
      float sm = 0.f;
#pragma unroll
      for (int i = 0; i < 10; ++i) sm += red[i][col < 160 ? col : 0];
      mx = fmx(mx, sm);
    }
    mx = fmx(mx, dppf<DPP_XOR1>(mx)); mx = fmx(mx, dppf<DPP_XOR2>(mx)); mx = fmx(mx, dppf<DPP_ROR4>(mx)); mx = fmx(mx, dppf<DPP_ROR8>(mx));
    mx = x32mx(x16mx(mx));
    if (lane == 0) ((float*)(a.ws + OFF_BNRM))[(cq * 32 + h) * 2 + dh] = mx;
  }
  PROT(3);
}

struct RecArgs {
  const u16* mem0; const u16* wq; const u16* wk; const u16* xold; u16* xnew; char* ws;
  unsigned layer; unsigned pad;
};
#define RLX_AGENT __ATOMIC_RELAXED, __HIP_MEMORY_SCOPE_AGENT
constexpr unsigned SPIN_CAP = 1u << 20;
#define TSTAMP(k, v)
#define ARRIVE(k)
#define ARRIVE_OUT()
#define ARRIVE_DECL()
#define LOGV(k, v)
#define LOGF(k, v)
#define LOGP(k)
#define LOGQ(k)
#define LOGF2(k)
#define LOGD(k)
#define LOGT(k, v)
#define STAMP_DECL()
#define STAMP(k)
#define STAMPV(k, v)
#define STAMP_OUT()
#define REC_D1 520
#define SCH_V 18
#define SCH_VMIN 0
#define SCH_DMIN 4
#define SCH_PEN 170
#define SCH_VMIN2 (-4)
#define SCH_PEN2 40
#define SCH_GU 1
#define SCH_GD 2
#define SCH_UP 6
#define SCH_D0 196
#define SCH_FIRST 120
#define EXP_NODMA 0
#define BG_HOLD 220
#define QBATCH 0
#define WPF_N 12
#define WPF_W 0
#define WPF0 (63 - WPF_N)
#define BG_HOLD4 250
#define D1_W1 20
#define F3_D 1200
#define T2W 2
#define AT_ASM 1
#define PM_Z 1
#define P2_X 1
#define ST_KS 14
#define ST_KS0 ST_KS
#define ST_KS1 ST_KS
#define PADR 0x6666
#define PADT 0x000
#define CODE_PAD(n) asm volatile(".p2align 6\n.rept %0\ns_nop 0\n.endr" :: "n"(n))
#define ST_X 320
#define ST_ALL 4
#define CPF_S 61
#define FR_D 700
#define FR_PACE 6
#define END_E60 600
#define END_E1 1300
#define END_E2 5200
#define END_E62 3600
#define END_E3 9800
#define END_E4 14500
#define EARLY_N 9
#define EARLY_D1 120
#define EARLY_V 8
#define EARLY_V1 30
#define F3 0
#define F3A 0
#define F3A_D 400
#define T4_D 300
#define T4_P 1
#define P2_W 1
#define PS_SL 1
#define F2B 4
#define F0C 0
#define F2C 0
#define W2_P 1
#define SCH_W 1
#define SP_SL 0
#define D1_M 300
#define D1_MIN 260
#define D1_C 96
#define D1_G 2
#define BG_HOLD6 0
#define W2_D 0
#define END_E61 2600
#define D1_LATE 200
#define P2_G0 10
#define P2_G2 6
#define P2_G3 6
#define SCH_RL 0
#define SCH_P 3
#define MMA_P 0
#define SP_P 0
#define F6 8
#define F7 10
#define XPF_W 2
#define SCH_LATE 0
#define LATE_V 6
#define QW 3
#define FB_SL 0
#define P2_G1 1
#define P1_G 0
#define F4Z 0
#define F5Z 0
#define T5_Z 2
#define F0A 3
#define FB_E 0
#define EXACT 1
#define D1_P 3
#define VO_A0 9
#define VO_A2 10
#define VO_B0 3
#define VO_B1 0
#define VO_N2 7
#define VO_N3 10
#define VO_W -1
#define UNIT 0
#define UN_P 2
#define VP_E 1
#define TS_B 0
#define ST_E 1
#define W1_SL 0
#define VO_A1 4
#define KP0 0
#define KP1 0
#define XPF 3
#define W1_D 0
#define F4 12
#define F5 0
#define F67 0
#define KP2 0
#define REC_RNG 70u
#define SCH_DRAIN 40
#define PL_X 0
#define FC(k)
#define PIN_V(x) asm volatile("" : "+v"(x))
#define PIN_S(x) asm volatile("" : "+s"(x))
#define VM_DRAIN() do { asm volatile("s_waitcnt vmcnt(0)" ::: "memory"); __builtin_amdgcn_s_waitcnt(0x0F70); } while (0)
#define SC1_LOAD(off) __builtin_amdgcn_raw_buffer_load_b128(rs, (off), 0, 16)
#define SC1_LOADS(off, soff) __builtin_amdgcn_raw_buffer_load_b128(rs, (off), (soff), 16)
#define SC1_STORE8S(v, off, soff) __builtin_amdgcn_raw_buffer_store_b64((v), rs, (off), (soff), 16)
#define SC1_LOAD8(off) __builtin_amdgcn_raw_buffer_load_b64(rs, (off), 0, 16)
#define SC1_STORE(v, off) __builtin_amdgcn_raw_buffer_store_b128((v), rs, (off), 0, 16)
#define SC1_STORE8(v, off) __builtin_amdgcn_raw_buffer_store_b64((v), rs, (off), 0, 16)
#if defined(__HIP_DEVICE_COMPILE__)
#pragma clang attribute push(__attribute__((target("no-packed-fp32-ops"))), apply_to = function)
#endif
__device__ __forceinline__ unsigned now32() { return (unsigned)__builtin_amdgcn_s_memrealtime(); }
__device__ __forceinline__ void wait_until(unsigned t) { for (;;) { const int d = (int)(t - now32()); if (d <= 0) break; if (d > 8) __builtin_amdgcn_s_sleep(1); } }
__device__ __forceinline__ unsigned cyc32() { return (unsigned)__builtin_amdgcn_s_memtime(); }
__device__ __forceinline__ void wait_cyc(unsigned t) { for (;;) { const int d = (int)(t - cyc32()); if (d <= 0) break; if (d > 200) __builtin_amdgcn_s_sleep(1); } }
#define WAIT_ASM(clk, far_) "1:\n\t" clk " vcc\n\ts_waitcnt lgkmcnt(0)\n\ts_sub_i32 %0, %1, vcc_lo\n\ts_cmp_lt_i32 %0, " far_ "\n\ts_cbranch_scc1 3f\n\ts_sleep 1\n\ts_branch 1b\n" \
                            "2:\n\t" clk " vcc\n\ts_waitcnt lgkmcnt(0)\n\ts_sub_i32 %0, %1, vcc_lo\n3:\n\ts_cmp_gt_i32 %0, 0\n\ts_cbranch_scc1 2b"
__device__ __forceinline__ int spin_until(unsigned t) { int d; asm volatile(WAIT_ASM("s_memrealtime", "9") : "=&s"(d) : "s"(t) : "vcc", "scc", "memory"); return d; }
__device__ __forceinline__ int spin_cyc(unsigned t) { int d; asm volatile(WAIT_ASM("s_memtime", "200") : "=&s"(d) : "s"(t) : "vcc", "scc", "memory"); return d; }
__device__ __forceinline__ void spin_exact(unsigned t) {
  if (!EXACT) { spin_cyc(t); return; }
  int d;
  asm volatile("1:\n\ts_memtime vcc\n\ts_waitcnt lgkmcnt(0)\n\ts_sub_i32 %0, %1, vcc_lo\n\ts_cmp_gt_i32 %0, 95\n\ts_cbranch_scc1 1b\n\t"
               "s_max_i32 %0, %0, 0\n\ts_andn2_b32 %0, %0, 3\n\ts_sub_i32 %0, 3f-2f, %0\n\ts_getpc_b64 vcc\n"
               "2:\n\ts_add_u32 vcc_lo, vcc_lo, %0\n\ts_addc_u32 vcc_hi, vcc_hi, 0\n\ts_setpc_b64 vcc\n\t.rept 24\n\ts_nop 0\n\t.endr\n3:"
               : "=&s"(d) : "s"(t) : "scc", "vcc", "memory");
}
template <int OFF, int SL> __device__ __forceinline__ void lds_spin(unsigned lds0, unsigned want) {
  unsigned v;
  asm volatile("1:\n\tds_read_b32 %0, %1 offset:%3\n\t.if %4\n\ts_sleep %4\n\t.endif\n\ts_waitcnt lgkmcnt(0)\n\tv_cmp_gt_u32 vcc, %2, %0\n\ts_cbranch_vccnz 1b"
               : "=&v"(v) : "v"(lds0), "s"(want), "n"(OFF), "n"(SL) : "vcc", "memory");
}
template <int CTRL> __device__ __forceinline__ int dppi(int v) { return __builtin_amdgcn_mov_dpp(v, CTRL, 0xf, 0xf, true); }
__device__ __forceinline__ int wave_imax(int l) {
  l = imax(l, dppi<DPP_XOR1>(l)); l = imax(l, dppi<DPP_XOR2>(l)); l = imax(l, dppi<DPP_ROR4>(l)); l = imax(l, dppi<DPP_ROR8>(l));
  int a = imax(__builtin_amdgcn_readlane(l, 0), __builtin_amdgcn_readlane(l, 16)), b = imax(__builtin_amdgcn_readlane(l, 32), __builtin_amdgcn_readlane(l, 48));
  PIN_S(a); PIN_S(b);
  return imax(a, b);
}
__device__ __forceinline__ void glds16(const void* gsrc, unsigned lds_dst) {
  unsigned keep;
  asm volatile("s_mov_b32 %0, m0\n\ts_mov_b32 m0, %2\n\ts_nop 0\n\tglobal_load_lds_dwordx4 %1, off\n\ts_mov_b32 m0, %0" : "=&s"(keep) : "v"(gsrc), "s"(lds_dst) : "memory");
}
__device__ __forceinline__ void glds16_sc1(const void* gsrc, unsigned lds_dst) {
  unsigned keep;
  asm volatile("s_mov_b32 %0, m0\n\ts_mov_b32 m0, %2\n\ts_nop 0\n\tglobal_load_lds_dwordx4 %1, off sc1\n\ts_mov_b32 m0, %0" : "=&s"(keep) : "v"(gsrc), "s"(lds_dst) : "memory");
}
__device__ __forceinline__ unsigned acc_off(int G, int row) {
  return (unsigned)(G < 128 ? ((G & 31) | ((G & 32) << 1) | ((G & 64) >> 1)) * 256 + row * 16 : (2 * G - 128 + (row >> 2)) * 256 + (row & 3) * 16);
}
__device__ __forceinline__ unsigned nz(unsigned v) { return v > 1u ? v : 1u; }
__device__ __forceinline__ unsigned pack2n(float lo, float hi) { return nz(pack2(lo, hi)); }
__device__ __forceinline__ unsigned min4(u32x4 v) { return umin(umin(v[0], v[1]), umin(v[2], v[3])); }
__device__ __forceinline__ bf16x8 as8(u32x4 v) { return __builtin_bit_cast(bf16x8, v); }

constexpr int NV = 7, NKQ = 2;
constexpr int L_RED1 = 0;
constexpr int L_BM = L_RED1 + 8 * 1536;
constexpr int L_STG = L_BM + 10240;
constexpr int L_ST = L_STG + 2048 + 2560;
constexpr int L_MISC = L_ST + 512;
constexpr int L_KX = L_MISC + 128;
constexpr int L_QX = L_KX + NKQ * 2048;
constexpr int L_XN = L_QX + NKQ * 2048;
constexpr int L_VOX = L_XN + 81920;
constexpr int L_VOM = L_VOX + NV * 5120;
constexpr int L_BYTES = L_VOM + NV * 1280;
constexpr int S_IMG = L_XN + 40960;
static_assert(S_IMG + 61440 <= L_VOM, "start-up image");
static_assert(L_BYTES <= 160 * 1024, "LDS budget");

__device__ __forceinline__ bf16x8 wqk_frag(const char* lds, int t, int ks, int lane) {
  const int c = lane & 15, g = lane >> 4, p = c & 3;
  const char* q = lds + S_IMG + (32 * ks + 8 * g + (c >> 2)) * 96 + 8 * p + (t == 0 ? 0 : (t == 1 ? 48 : (p < 2 ? 32 : 64)));
  const v4i16_t lo = __builtin_amdgcn_ds_read_tr16_b64_v4i16((LAS v4i16_t*)q), hi = __builtin_amdgcn_ds_read_tr16_b64_v4i16((LAS v4i16_t*)(q + 4 * 96));
  return (bf16x8){lo[0], lo[1], lo[2], lo[3], hi[0], hi[1], hi[2], hi[3]};
}
__device__ __forceinline__ bf16x8 sel_frag(int c, int base) {
  bf16x8 r;
#pragma unroll
  for (int i = 0; i < 8; ++i) r[i] = (c == base + i) ? (short)0x3f80 : (short)0;
  return r;
}
__device__ __forceinline__ bf16x8 qk_frag(const char* p4, const char* p2, const char* p2b) {
  f32x4 a0 = *(const f32x4*)(p4), a1 = *(const f32x4*)(p4 + 1792), a2 = *(const f32x4*)(p4 + 3584), a3 = *(const f32x4*)(p4 + 5376);
  f32x2 b0 = *(const f32x2*)(p2), b1 = *(const f32x2*)(p2 + 1792), b2 = *(const f32x2*)(p2b), b3 = *(const f32x2*)(p2b + 1792);
  f32x4 va = (a0 + a1) + (a2 + a3);
  f32x2 vb = (b0 + b1) + (b2 + b3);
  return mk8(pack2(va[0], va[1]), pack2(va[2], va[3]), pack2(vb[0], vb[1]), 0u);
}
__device__ __forceinline__ void x_fetch(const u16* src, unsigned lds0, int seg, int p, int lane) {
  if (EXP_NODMA) return;
  glds16_sc1(src + (size_t)p * 512 + lane * 8, lds0 + L_XN + (seg & 1) * 40960 + p * 1024);
}
template <int N> __device__ __forceinline__ void x_fetch_n(const u16* src, unsigned lds0, int seg, int p0, unsigned voff) {
  if (EXP_NODMA) return;
#pragma unroll
  for (int i = 0; i < N; i += 4) {
    unsigned keep;
    asm volatile("s_mov_b32 %0, m0\n\ts_mov_b32 m0, %3\n\ts_nop 0\n\tglobal_load_lds_dwordx4 %1, %2 sc1\n\t"
                 ".if %4 > 1\n\tglobal_load_lds_dwordx4 %1, %2 offset:1024 sc1\n\t.endif\n\t.if %4 > 2\n\tglobal_load_lds_dwordx4 %1, %2 offset:2048 sc1\n\t.endif\n\t"
                 ".if %4 > 3\n\tglobal_load_lds_dwordx4 %1, %2 offset:3072 sc1\n\t.endif\n\ts_mov_b32 m0, %0"
                 : "=&s"(keep) : "v"(voff), "s"(src + (size_t)(p0 + i) * 512), "s"(lds0 + L_XN + (seg & 1) * 40960 + (p0 + i) * 1024), "n"(N - i < 4 ? N - i : 4) : "memory");
  }
}
__device__ __forceinline__ int qkv_row(int b) { return 2 * (b & 7) + ((b >> 3) >= 15); }
__device__ __forceinline__ const u16* x_src(const RecArgs& a, int seg, int b) {
  return seg < NSEG ? (const u16*)(a.ws + OFF_XN) + (size_t)seg * 32 * DM : a.xnew + (size_t)(qkv_row(b) + 16 * (seg - NSEG)) * 32 * DM;
}
template <int N>
__device__ __forceinline__ void img_fetch(const RecArgs& a, unsigned lds0, int h, int j, int part, int lane) {
#pragma unroll
  for (int i = 0; i < N; ++i) {
    const int n = part * N + i, idx = n * 64 + lane, row = idx / 6, pc = idx - row * 6;
    if (FR_PACE && N == 20) asm volatile("s_waitcnt vmcnt(%0)" :: "n"(FR_PACE) : "memory");
    glds16((pc < 3 ? a.wq : a.wk - 24) + (size_t)row * HD + h * DH + j * 24 + pc * 8, lds0 + S_IMG + n * 1024);
  }
}
constexpr int P2B = F4 + F5 + 2 * F67 + F6 + F7, PZ = P2B + F2B, PA = PZ, P0C = PA + 2 * F0A, F3C = 40 - F3A - F3 - P0C - 2 * F0C - F2C;
static_assert(F3C >= 0, "fetch plan");
template <int OFF> __device__ __forceinline__ void lds_inc(unsigned lds0) {
  asm volatile("s_mov_b64 exec, 1\n\tds_add_u32 %0, %1 offset:%2\n\ts_mov_b64 exec, -1" :: "v"(lds0), "v"(1u), "n"(OFF) : "memory");
}
__device__ __forceinline__ void lds_arrive(unsigned* p, int lane) {
  asm volatile("s_waitcnt lgkmcnt(0)" ::: "memory");
  if (lane == 0) __hip_atomic_fetch_add(p, 1u, __ATOMIC_RELAXED, __HIP_MEMORY_SCOPE_WORKGROUP);
}
__device__ __forceinline__ void lds_wait(unsigned* p, unsigned want) {
  while (__hip_atomic_load(p, __ATOMIC_RELAXED, __HIP_MEMORY_SCOPE_WORKGROUP) < want) __builtin_amdgcn_s_sleep(1);
  asm volatile("" ::: "memory");
}
constexpr float KEXP = 0.07216878364870322f * 1.4426950408889634f;
#define FRESH_LANE() int lane = lane0; asm volatile("" : "+v"(lane)); const int c = lane & 15, g = lane >> 4; (void)c; (void)g

struct OutTask {
  u32x4 y[4]; u32x2 xo, out;
  bf16x8 sel;
  unsigned t5f, t5o;
  __device__ __forceinline__ void init(int b, int lane) {
    const int um = b / 10, uq = (b / 5) & 1, up = b % 5, tok = lane & 15, k = up < 4 ? (up >> 1) * 256 + um * 16 + (up & 1) * 8 : 512 + um * 8, q4 = ((lane >> 4) & 1) * 4;
    t5f = (unsigned)(((uq * 20 + (k >> 5)) * 64 + ((k & 31) >> 3) * 16 + tok) * 8 + q4);
    t5o = (unsigned)(((um * 2 + uq) * 16 + (lane >> 4)) * 1280 + up * 256 + tok * 16);
    if (b >= 160) { t5f = 0u; t5o = 0x7ffffff0u; }
    sel = sel_frag(tok, 0);
    PIN_V(sel);
  }
  static __device__ __forceinline__ bool ok(int sl, int b) { return sl >= 0 && b < 160; }
  __device__ __forceinline__ void load(const RecArgs& a, const __amdgpu_buffer_rsrc_t rs, int sl, int b, int part = 3) {
    const unsigned o = t5o | (sl < 0 ? 0x7ff00000u : 0u);
    sl = imax(sl, 0);
#pragma unroll
    for (int k = 0; k < 4; ++k) if (part & 1) y[k] = SC1_LOADS(o, op_slot(sl) + k * 5120);
    if (part & 2) xo = __builtin_bit_cast(u32x2, __hip_atomic_load((const gu64*)(a.xold + (size_t)sl * 32 * DM + t5f), RLX_AGENT));
  }
  __device__ __forceinline__ bool ready(int b) { return b >= 160 || wave_all(umin(umin(min4(y[0]), min4(y[1])), umin(min4(y[2]), min4(y[3]))) != 0u); }
  __device__ __forceinline__ void run(int lane) {
    f32x4 d = {0.f, 0.f, 0.f, 0.f};
#pragma unroll
    for (int k = 0; k < 4; ++k) d = mfma32(sel, as8(y[k]), d);
#pragma unroll
    for (int i = 0; i < 2; ++i) {
      const unsigned r = pack2(d[2 * i], d[2 * i + 1]), x = xo[i];
      out[i] = pack2(u2f(x << 16) + u2f(r << 16), u2f(x & 0xffff0000u) + u2f(r & 0xffff0000u));
    }
    PIN_V(out);
  }
  __device__ __forceinline__ void store(const RecArgs& a, int sl, int b, int lane) {
    __builtin_amdgcn_raw_buffer_store_b64(out, __builtin_amdgcn_make_buffer_rsrc(a.xnew, 0, ok(sl, b) ? S_ * DM * 2 : 0, 0x00020000), (t5f + (unsigned)sl * 32 * DM) * 2u | (unsigned)(lane & 32) << 25, 0, 16);
  }
};

struct PvTask {
  u32x4 y[2]; u32x2 xo[2];
  __device__ __forceinline__ void load(const __amdgpu_buffer_rsrc_t rs, unsigned ck, int sl, int gp, int qt, int lane) {
    const unsigned o = pt_slot(ck, sl);
#pragma unroll
    for (int k = 0; k < 2; ++k) {
      y[k] = SC1_LOAD(o + pt_tok(2 * gp) + k * 2048 + qt * 1024 + lane * 16);
      xo[k] = SC1_LOAD8(o + pt_mem(2 * gp) + k * 512 + qt * 256 + (lane & 31) * 8);
    }
  }
  __device__ __forceinline__ bool ready() { return wave_all(umin(umin(min4(y[0]), min4(y[1])), umin(umin(xo[0][0], xo[0][1]), umin(xo[1][0], xo[1][1]))) != 0u); }
  __device__ __forceinline__ void run(char* lds, const __amdgpu_buffer_rsrc_t rs, int sl, int gp, int m, int qt, int lane, bool direct) {
    const f32x4 Z4 = {0.f, 0.f, 0.f, 0.f};
    PIN_V(y[0]); PIN_V(y[1]); PIN_V(xo[0]); PIN_V(xo[1]);
    const int sv = (sl + 2 * NV) % NV;
    const char* vp = lds + L_VOX + sv * 5120 + lane * 16;
    const char* mp = lds + L_VOM + sv * 1280 + (lane & 31) * 8;
    bf16x8 pm[2], vm[5], vx[5];
#pragma unroll
    for (int t = 0; t < 5; ++t) { vx[t] = *(const bf16x8*)(vp + t * 1024); const u32x2 v = *(const u32x2*)(mp + t * 256); vm[t] = mk8(v[0], v[1], 0u, 0u); }
#pragma unroll
    for (int k = 0; k < 2; ++k) pm[k] = mk8(lane < 32 ? xo[k][0] : 0u, lane < 32 ? xo[k][1] : 0u, 0u, 0u);
    __builtin_amdgcn_sched_barrier(0);
    f32x4 o[4];
#pragma unroll
    for (int k = 0; k < 2; ++k) { o[k] = mfma32(vx[k], as8(y[0]), Z4); o[2 + k] = mfma32(vx[2], as8(y[k]), Z4); }
#pragma unroll
    for (int k = 0; k < 2; ++k) { o[k] = mfma32(vm[k], pm[0], o[k]); o[2 + k] = mfma32(vm[2], pm[k], o[2 + k]); }
#pragma unroll
    for (int k = 0; k < 2; ++k) o[k] = mfma32(vx[3 + k], as8(y[1]), o[k]);
#pragma unroll
    for (int k = 0; k < 2; ++k) o[k] = mfma32(vm[3 + k], pm[1], o[k]);
    float oc[4];
#pragma unroll
    for (int r = 0; r < 4; ++r) {
      auto sw = __builtin_amdgcn_permlane32_swap(f2u(o[3][r]), f2u(o[3][r]), false, false);
      oc[r] = o[2][r] + u2f(sw[1]);
    }
    const u32x2 v0 = {pack2n(o[0][0], o[0][1]), pack2n(o[0][2], o[0][3])}, v1 = {pack2n(o[1][0], o[1][1]), pack2n(o[1][2], o[1][3])};
    const u32x2 v2 = {pack2n(oc[0], oc[1]), pack2n(oc[2], oc[3])};
    const unsigned po = (unsigned)(((lane >> 5) * 16 + (lane & 15)) * 16 + ((lane >> 4) & 1) * 8);
    if (direct) {
      const unsigned oo = op_slot(sl) + (unsigned)((m * 32 + qt * 16 + gp) * 1280) + po;
      SC1_STORE8(v0, oo); SC1_STORE8(v1, oo + 512);
      if (lane < 32) SC1_STORE8(v2, oo + 1024);
    } else {
      char* sp = lds + L_STG + 2048 + qt * 1280 + po;
      *(u32x2*)(sp) = v0; *(u32x2*)(sp + 512) = v1;
      if (lane < 32) *(u32x2*)(sp + 1024) = v2;
    }
  }
};

struct PvTask2 {
  u32x4 y[2][2], pa[2], pb[2];
  unsigned lo, lm, vr, ma, mz, so, sz;
  __device__ __forceinline__ void init(int gp, int lane) {
    const int hi = lane >> 5;
    lo = pt_tok(2 * gp) + (unsigned)(lane * 16); lm = pt_mem(2 * gp + hi) + (unsigned)((lane & 31) * 8);
    vr = (unsigned)(L_VOX + 2048 + (lane ^ 8) * 16);
    ma = (unsigned)(L_VOM + hi * 768 + (lane & 31) * 8); mz = (unsigned)(L_VOM + 512 + ((lane & 31) ^ (hi * 8)) * 8);
    so = (unsigned)(L_STG + 2048 + (hi * 16 + (lane & 15)) * 16 + ((lane >> 4) & 1) * 8);
    sz = hi ? (unsigned)(L_RED1 + 8704 + lane * 8) : so + 1024;
    PIN_V(lo); PIN_V(lm); PIN_V(vr); PIN_V(ma); PIN_V(mz); PIN_V(so); PIN_V(sz);
#pragma unroll
    for (int q = 0; q < 2; ++q) { pa[q] = pb[q] = y[q][0] = y[q][1] = (u32x4){0u, 0u, 0u, 0u}; PIN_V(pa[q]); PIN_V(pb[q]); }
  }
  __device__ __forceinline__ void load(const __amdgpu_buffer_rsrc_t rs, unsigned o) {
#pragma unroll
    for (int q = 0; q < 2; ++q) {
      y[q][0] = SC1_LOADS(lo + q * 1024, o); y[q][1] = SC1_LOADS(lo + 2048 + q * 1024, o);
      const u32x2 p = __builtin_amdgcn_raw_buffer_load_b64(rs, lm + q * 256, o, 16);
      pa[q][0] = p[0]; pa[q][1] = p[1];
    }
  }
  __device__ __forceinline__ void run(char* lds, int sv, int lane) {
    const f32x4 Z4 = {0.f, 0.f, 0.f, 0.f};
    const char* vp = lds + L_VOX + sv * 5120 + lane * 16;
    bf16x8 vx[6];
#pragma unroll
    for (int t = 0; t < 5; ++t) vx[t] = *(const bf16x8*)(vp + t * 1024);
    vx[5] = *(const bf16x8*)(lds + vr + sv * 5120);
    const u32x2 a0 = *(const u32x2*)(lds + ma + sv * 1280), a1 = *(const u32x2*)(lds + ma + sv * 1280 + 256);
    const u32x2 z0 = *(const u32x2*)(lds + mz + sv * 1280), z1 = *(const u32x2*)(lds + mz + sv * 1280 + 256);
    const bf16x8 vm = mk8(a0[0], a0[1], a1[0], a1[1]), vz = mk8(z0[0], z0[1], z1[0], z1[1]);
#pragma unroll
    for (int q = 0; q < 2; ++q) { PIN_V(pa[q]); pb[q][2] = pa[q][0]; pb[q][3] = pa[q][1]; PIN_V(pb[q]); }
    __builtin_amdgcn_sched_barrier(0);
    f32x4 o[2][3];
#pragma unroll
    for (int q = 0; q < 2; ++q) {
#pragma unroll
      for (int t = 0; t < 3; ++t) o[q][t] = mfma32(vx[t], as8(y[q][0]), Z4);
#pragma unroll
      for (int t = 0; t < 3; ++t) o[q][t] = mfma32(vx[3 + t], as8(y[q][1]), o[q][t]);
      o[q][0] = mfma32(vm, as8(pa[q]), o[q][0]); o[q][1] = mfma32(vm, as8(pb[q]), o[q][1]); o[q][2] = mfma32(vz, as8(pa[q]), o[q][2]);
    }
#pragma unroll
    for (int q = 0; q < 2; ++q) {
#pragma unroll
      for (int t = 0; t < 3; ++t)
        *(u32x2*)(lds + (t < 2 ? so + t * 512 : sz) + q * 1280) = (u32x2){pack2n(o[q][t][0], o[q][t][1]), pack2n(o[q][t][2], o[q][t][3])};
    }
  }
};

struct TokScores {
  bf16x8 k[2], q[2]; u32x4 v[2];
  __device__ __forceinline__ void get(const char* lds, int seg, int lane) {
#pragma unroll
    for (int i = 0; i < 2; ++i) { k[i] = *(const bf16x8*)(lds + L_KX + (seg & 1) * 2048 + i * 1024 + lane * 16); q[i] = *(const bf16x8*)(lds + L_QX + (seg & 1) * 2048 + i * 1024 + lane * 16); }
  }
  __device__ __forceinline__ void run(int qt) {
    const f32x4 Z4 = {0.f, 0.f, 0.f, 0.f};
    const f32x4 d1 = mfma32(k[0], q[qt], Z4), d2 = mfma32(k[1], q[qt], Z4);
    v[qt] = (u32x4){pack2n(d1[0], d1[1]), pack2n(d1[2], d1[3]), pack2n(d2[0], d2[1]), pack2n(d2[2], d2[3])};
    PIN_V(v[qt]);
  }
  __device__ __forceinline__ void store(const __amdgpu_buffer_rsrc_t rs, unsigned off, unsigned o) {
    __builtin_amdgcn_raw_buffer_store_b128(v[0], rs, off, o, 16); __builtin_amdgcn_raw_buffer_store_b128(v[1], rs, off + 1024, o, 16);
    asm volatile("s_nop 1" ::: "memory");
  }
};

__device__ __forceinline__ void touch(const char* base, unsigned off, unsigned lds0) {
  unsigned keep;
  asm volatile("s_mov_b32 %0, m0\n\ts_mov_b32 m0, %3\n\ts_nop 0\n\tglobal_load_lds_dword %1, %2 sc1\n\ts_mov_b32 m0, %0" : "=&s"(keep) : "v"(off), "s"(base), "s"(lds0 + L_RED1 + 8192) : "memory");
}
template <int N> __device__ __forceinline__ void stouch(const char* p, unsigned& d) {
  asm volatile(".set pf_o, 0\n\t.rept %2\n\ts_load_dword %0, %1, pf_o\n\t.set pf_o, pf_o + 128\n\t.endr" : "+s"(d) : "s"(p), "n"(N) : "memory");
}
__device__ __forceinline__ void code_touch(int b, int lane, unsigned lds0) {
  const char* pc; int d0, d1;
  asm volatile("s_getpc_b64 %0\n\ts_mov_b32 %1, _Z10rec_kernel7RecArgs@rel32@lo+4\n\ts_mov_b32 %2, _Z11attb_kernelPKtS0_Pt@rel32@lo+12" : "=s"(pc), "=s"(d0), "=s"(d1));
  const int o = (lane * 32 + (b >> 3)) * 128;
  touch(pc + d0, (unsigned)(o < d1 - d0 ? o : 0), lds0);
}
__device__ __forceinline__ unsigned zero_base(int b, int h, int j, int ln) {
  return ln < 32 ? (unsigned)((h & 3) * 32768 + j * 512 + ln * 16) : ln < 40 ? (unsigned)((h & 2) * 1024 + j * 256 + (ln & 4) * 32 + (h & 1) * 64 + (ln & 3) * 16)
                 : (unsigned)OFF_ACC + (b < ACC_NB ? (unsigned)(b * 256 + (ln - 40) * 16) : ts_piece((b - ACC_NB) * 8 + ln - 40));
}
__device__ __forceinline__ unsigned sct_slot(unsigned ck, unsigned sl) { return sl * SCT_STEP + ((ck >> (sl & 4u)) & 7u) * 4096u; }
__device__ __forceinline__ unsigned scm_slot(unsigned ck, unsigned sl) { return OFF_SCM + sl * SCM_STEP + ((ck >> 8) & 7u) * 4096u; }
__device__ __forceinline__ int ck_rot(int x) { return ((x & 1) << 2) | (x >> 1); }
struct Place {
  unsigned t[8];
  __device__ __forceinline__ void load(const char* ws, int lane) {
#pragma unroll
    for (int x = 0; x < 8; ++x) t[x] = *(const unsigned*)(ws + OFF_LAT + ((imin(lane >> 3, 5) * 8 + (ck_rot(x) ^ (lane & 7))) * 8 + x) * 4);
  }
  __device__ __forceinline__ unsigned get(int b, int lane) {
    int c = (int)((((t[0] + t[1]) + (t[2] + t[3])) + ((t[4] + t[5]) + (t[6] + t[7]))) * 8u) + (lane & 7);
    c = imin(c, dppi<DPP_XOR1>(c)); c = imin(c, dppi<DPP_XOR2>(c)); c = imin(c, dppi<0x141>(c));
    unsigned k = 0u;
#pragma unroll
    for (int r = 0; r < 6; ++r) k |= (unsigned)(__builtin_amdgcn_readlane(c, 8 * r) & 7) << (4 * r);
    return (k ^ PL_X) ^ (unsigned)ck_rot(b & 7) * 0x111111u;
  }
};

struct Sched {
  u32x4 tsv, rec;
  unsigned side, o;
  __device__ __forceinline__ void pre(int s) { o = (unsigned)OFF_ACC + (unsigned)((s + SR - 1) % SR) * (unsigned)ACC_STEP + ACC_TS + side; PIN_S(o); }
  __device__ __forceinline__ void load(const __amdgpu_buffer_rsrc_t rs, int lane) {
    tsv = SC1_LOADS(ts_piece(lane & 63) - ACC_TS, o); rec = SC1_LOADS(128, o);
  }
  __device__ __forceinline__ unsigned last(unsigned ref) {
    const unsigned o = 0x40000000u - ref;
    int l = (int)umax(umax(tsv[0] ? tsv[0] + o : 0u, tsv[1] ? tsv[1] + o : 0u), umax(tsv[2] ? tsv[2] + o : 0u, tsv[3] ? tsv[3] + o : 0u));
    return (unsigned)wave_imax(l) - o;
  }
  __device__ __forceinline__ unsigned run(const RecArgs& a, const __amdgpu_buffer_rsrc_t rs, int s, int b, int lane) {
    constexpr int w = 3; (void)w;
    unsigned acur = __builtin_amdgcn_readfirstlane(rec[0]), aprev = __builtin_amdgcn_readfirstlane(rec[1]), hist = __builtin_amdgcn_readfirstlane(rec[3]);
    int D8 = (int)__builtin_amdgcn_readfirstlane(rec[2]);
    const int d = wave_imax((int)(umax(umax(tsv[0], tsv[1]), umax(tsv[2], tsv[3])) - aprev)), dd = 8 * d - D8;
    int Dn = D8 + (dd >> (dd > 0 ? SCH_GU : SCH_GD));
    unsigned anext = (acur + (unsigned)((Dn + 4) >> 3) + SCH_V + (s < EARLY_N ? EARLY_V : (s >= WPF0 ? LATE_V : 0))) | 1u;
    if (__builtin_expect(hist < 2u || aprev == 0u || d < 1 || d + SCH_VMIN > (int)(acur - aprev), 0)) {
      if (s == 0) {
        for (int it = 0; it < 40 && !wave_all(min4(tsv) != 0u); ++it) {
          __builtin_amdgcn_s_sleep(ST_ALL);
          asm volatile("" ::: "memory");
          load(rs, lane);
        }
      }
      const unsigned now = now32(), L = last(now);
      if (acur == 0u) { D8 = 8 * SCH_D0; hist = 0u; }
      const bool late = acur == 0u || (int)(L + SCH_VMIN - acur) > 0;
      if (late) { if (b == 0) FC(3); if (acur) D8 += 8 * (hist ? SCH_UP : 3 * SCH_UP); }
      else if (aprev) { if (hist < 2u) D8 = 8 * imin(imax((int)(L - aprev), 1), 4000); hist = umin(hist + 1u, 2u); }
      const unsigned base = acur == 0u ? L + SCH_FIRST : (late ? L + SCH_V + ((int)(L + SCH_VMIN2 - acur) <= 0 ? SCH_PEN2 : SCH_PEN) : acur);
      anext = (base + (unsigned)((D8 + 4) >> 3) + SCH_V + (s == 1 ? EARLY_V1 : (s < EARLY_N ? EARLY_V : 0))) | 1u;
      if (s == 0) anext = (L + ST_X) | 1u;
      if ((int)(now - L) > 50000) anext = 0u;
      if (late) acur = 0u;
      Dn = D8;
    }
    if (__builtin_expect(b == 0, 0))
      __builtin_amdgcn_raw_buffer_store_b128((u32x4){anext, acur, (unsigned)Dn, hist}, rs, lane < 2 ? lane * 8192 : 0x7ffffff0, (unsigned)OFF_ACC + (unsigned)(s % SR) * (unsigned)ACC_STEP + ACC_TS + 128u, 16);
    LOGV(0, anext); LOGV(1, acur == 0u); LOGV(2, (Dn + 4) >> 3); LOGV(3, d); LOGV(4, acur - aprev - d); LOGV(5, aprev);
    return anext;
  }
};

template <int w>
__device__ __forceinline__ void token_waves(const RecArgs& a, char* lds, const __amdgpu_buffer_rsrc_t rs) {
  const int lane0 = __builtin_amdgcn_workitem_id_x() & 63, b = __builtin_amdgcn_workgroup_id_x(), wid = w + 4; (void)wid;
  const int gp = (b & 7) * 2 + (b >> 7), m = (b >> 3) & 15, h0 = 2 * gp, h = h0 + (m >> 3), j = m & 7;
  float* const misc = (float*)(lds + L_MISC);
  const f32x4 Z4 = {0.f, 0.f, 0.f, 0.f};
  const unsigned lds0 = (unsigned)(uintptr_t)(LAS char*)lds;
  unsigned* const cnt = (unsigned*)(misc + 6);
  const float* const xnrm = (const float*)(a.ws + OFF_XNRM);
  bf16x16 W[2][10];
  auto btile = [&](int i) { return w == 1 ? 0 : (w == 2 ? 1 + i : 3 + i); };
  if (w == 1) code_touch(b, lane0, lds0);
  Place pl;
  if (w == 3) pl.load(a.ws, lane0);
  if (a.layer) {
    if (w != 0) img_fetch<20>(a, lds0, h, j, w - 1, lane0);
  } else {
    if (w == 0) img_fetch<12>(a, lds0, h, j, 4, lane0);
#pragma unroll
    for (int i = 0; i < 10; ++i) x_fetch(x_src(a, 0, b), lds0, 0, 10 * w + i, lane0);
  }
#pragma unroll
  for (int i = 0; i < 2; ++i)
#pragma unroll
    for (int p = 0; p < 10; ++p)
      if (w >= 2 || (w == 1 && i == 1)) {
        const char* wp = a.ws + OFF_WB + ((size_t)(b * 5 + btile(i)) * 20 + 2 * p) * 1024 + lane0 * 16;
        if (FR_PACE && a.layer) asm volatile("s_waitcnt vmcnt(%0)" :: "n"(FR_PACE) : "memory");
        W[i][p] = cat16(*(const bf16x8*)wp, *(const bf16x8*)(wp + 1024));
      }
  f32x4 bn = Z4;
  if (w == 0) {
    bn = *(const f32x4*)(a.ws + OFF_BNRM + lane0 * 16);
    if (!a.layer) asm volatile("s_waitcnt vmcnt(0)" ::: "memory");
  } else asm volatile("s_waitcnt vmcnt(%0)" :: "n"(w == 1 ? 20 : 40) : "memory");
  unsigned ck = 0u;
  if (w == 3) { ck = pl.get(b, lane0); *(unsigned*)(misc + 11) = ck; }
  BAR();
#pragma unroll
  for (int p = 0; p < 10; ++p) {
    if (w < 2) W[0][p] = cat16(wqk_frag(lds, 2 * w, 2 * p, lane0), wqk_frag(lds, 2 * w, 2 * p + 1, lane0));
    if (w == 0) W[1][p] = cat16(wqk_frag(lds, 1, 2 * p, lane0), wqk_frag(lds, 1, 2 * p + 1, lane0));
  }
  float cb2 = 0.f;
  if (w == 0) {
    float n = __builtin_amdgcn_sqrtf(bn[0] + bn[1]) + __builtin_amdgcn_sqrtf(bn[2] + bn[3]);
    n += dppf<DPP_XOR1>(n); n += dppf<DPP_XOR2>(n); n += dppf<DPP_ROR4>(n); n += dppf<DPP_ROR8>(n);
    n = x32mx(x16mx(n));
    cb2 = u2f(__builtin_amdgcn_readfirstlane(f2u(1.15f * n * n)));
  }
  const float cm2 = u2f((f2u(cb2) & 0x7fffffu) | 0x40000000u);
  const int ek = imax((int)((f2u(cb2) >> 23) & 0xffu), 1) - 252;
  if (w >= 2) {
#pragma unroll
    for (int i = 0; i < 4; ++i) *(unsigned*)(lds + L_KX + (w - 2) * 4096 + i * 1024 + lane0 * 16 + 12) = 0u;
  }
  OutTask ot;
  const int zl = lane0 - 16;
  const bool zon = w == 3 && zl >= 0 && (zl < 40 || b < ACC_NB + 17);
  unsigned zo = zon ? zero_base(b, h, j, zl) + (zl < 32 ? sct_slot(ck, 0u) : (zl < 40 ? scm_slot(ck, 0u) : 0u)) : 0x7ffffff0u;
  unsigned zm = zon ? (zl < 32 ? SCT_STEP + 1u : (zl < 40 ? SCM_STEP : (unsigned)ACC_STEP)) : 0u;
  const unsigned zx = ((ck ^ (ck >> 4)) & 7u) * 4096u;
  if (w == 3) { PIN_V(zo); PIN_V(zm); }
  auto st_out = [&](int s, int lane, auto go) {
    if (w < 2) return;
    if (ST_E) lds_spin<L_MISC + 112, 1>(lds0, (unsigned)imax(s, 0)); else go();
    const int sl = s - 2 - LAG4;
    const char* sp = lds + L_STG + 2048 + (w & 1) * 1280;
    u32x4 v0 = *(const u32x4*)(sp + lane * 16), v1 = *(const u32x4*)(sp + 1024 + (lane & 15) * 16);
    const u32x4 z = {0u, 0u, 0u, 0u};
    unsigned oo = (sl < 0 ? 0x7ff00000u : op_slot(sl) + (unsigned)((m * 32 + (w & 1) * 16 + gp) * 1280)) + (unsigned)(lane * 16), za = 0x7ffffff0u;
    if (w == 3) {
      const unsigned zs = (unsigned)(s + 2) % SR;
      za = (zo + (s < 0 ? 1024u : zs) * (zm & ~1u)) ^ ((zm & 1u) * (zs & 4u ? zx : 0u));
#pragma unroll
      for (int i = 0; i < 4; ++i) { v1[i] = lane < 16 ? v1[i] : z[i]; PIN_V(v1[i]); }
    }
    za = lane < 16 ? oo + 1024 : za;
    PIN_V(za); PIN_V(oo); PIN_V(v0); PIN_V(v1);
    __builtin_amdgcn_sched_barrier(0);
    if (ST_E) go();
    SC1_STORE(v0, oo);
    SC1_STORE(v1, za);
    __builtin_amdgcn_sched_barrier(0);
    if (w == 2) ot.store(a, s - 2 - LAG5, b, lane);
    else {
      const unsigned xo_ = (unsigned)((s - 8) * 1024 + lane * 16);
      if (__builtin_expect((unsigned)(s - 8) < 20u, 0)) SC1_STORE(z, xo_ < X_BYTES / 256 ? (unsigned)(OFF_X + b * (X_BYTES / 256)) + xo_ : 0x7ffffff0u);
    }
  };
  STAMP_DECL(); ARRIVE_DECL();
  auto mma_seg = [&](int seg, int sv, int lane, auto pause) {
    const int c = lane & 15, g = lane >> 4;
    const char* xp = lds + L_XN + (seg & 1) * 40960 + lane * 16;
    f32x4 acc[2][2] = {{Z4, Z4}, {Z4, Z4}};
    bf16x8 x[4][2];
#pragma unroll
    for (int p = 0; p < 3; ++p) { x[p][0] = *(const bf16x8*)(xp + p * 1024); x[p][1] = *(const bf16x8*)(xp + (20 + p) * 1024); }
    unsigned eo[2];
#pragma unroll
    for (int ks = 0; ks < 20; ++ks) {
      if (ks == (w == 0 ? KP0 : (w == 1 ? KP1 : KP2))) { pause(); __builtin_amdgcn_sched_barrier(0); }
      if (ks == 17) {
#pragma unroll
        for (int i = 0; i < 2; ++i) {
          eo[i] = (unsigned)(w == 0 ? (i ? L_KX : L_QX) + (seg % NKQ) * 2048 + lane * 16
                                    : (w == 1 && i == 0 ? ((g >> 1) ? L_KX : L_QX) + (seg % NKQ) * 2048 + (2 * (g & 1) * 16 + c) * 16 + 8 : L_VOX + sv * 5120 + btile(i) * 1024 + lane * 16));
          if (i == 0 || w == 1) PIN_V(eo[i]);
        }
        if (w == 0) eo[1] = eo[0] + (L_KX - L_QX);
        if (w >= 2) eo[1] = eo[0] + 1024;
      }
      __builtin_amdgcn_sched_barrier(0);
#pragma unroll
      for (int q = 0; q < 4; ++q) {
        const int tt = 1 - (q >> 1), i = q & 1;
        const bool isqk = w == 0 || (w == 1 && i == 0);
        const bf16x8 wf = half8(W[i][ks >> 1], ks & 1);
        constexpr int q0 = UNIT && w < 2;
        if (q < q0) continue;
        acc[i][tt] = isqk ? mfma32(wf, x[ks & 3][tt], acc[i][tt]) : mfma32(x[ks & 3][tt], wf, acc[i][tt]);
        __builtin_amdgcn_sched_barrier(0);
        if (q - q0 < 2 && ks + 3 < 20) x[(ks + 3) & 3][q - q0] = *(const bf16x8*)(xp + ((q - q0) * 20 + ks + 3) * 1024);
        __builtin_amdgcn_sched_barrier(0);
      }
    }
#pragma unroll
    for (int i = 0; i < 2; ++i) {
      if (w + i == 1) { const float ks_ = (w == 0 || (g >> 1)) ? KEXP : 1.0f; acc[i][0] *= ks_; acc[i][1] *= ks_; }
      const u32x4 v = {pack2(acc[i][0][0], acc[i][0][1]), pack2(acc[i][0][2], acc[i][0][3]), pack2(acc[i][1][0], acc[i][1][1]), pack2(acc[i][1][2], acc[i][1][3])};
      char* p = lds + eo[i];
      if (w == 0) { *(u32x2*)(p) = (u32x2){v[0], v[1]}; if (i || !UNIT) *(u32x2*)(p + 1024) = (u32x2){v[2], v[3]}; }
      else if (w == 1 && i == 0) { *(unsigned*)(p) = v[0]; *(unsigned*)(p + 256) = v[1]; if (!UNIT) { *(unsigned*)(p + 1024) = v[2]; *(unsigned*)(p + 1280) = v[3]; } }
      else *(u32x4*)p = v;
    }
  };
  unsigned vp, mp, ip;
  int sidx, ei = 127;
  float inv;
  f32x4 acc[2];
  auto vo_pre = [&](int s, int sv, int lane) {
    vp = (unsigned)(w == 0 ? L_MISC + (s & 3) * 4 : L_VOM + sv * 1280 + (btile(0) * 32 + (w == 1 ? lane & 31 : lane)) * 8);
    mp = (unsigned)(L_BM + lane * 16);
    PIN_V(vp); PIN_V(mp);
    if (w == 0) { ip = (unsigned)(L_MISC + 16 + ((s + 1) & 1) * 4); PIN_V(ip); }
    else {
      inv = misc[4 + (s & 1)];
      sidx = (lane & 8) ? 0xEEEE : 0x4444;
      acc[0] = acc[1] = Z4;
      PIN_V(sidx); PIN_V(acc[1]);
      if (w >= 2) { PIN_V(acc[0]); PIN_V(inv); }
    }
    __builtin_amdgcn_sched_barrier(0);
  };
  auto vo_mem = [&](int s, auto last) {
    __builtin_amdgcn_s_setprio(2);
    asm volatile(".rept %0\n\ts_nop 3\n\t.endr" :: "n"(w == 0 ? VO_A0 : (w == 1 ? VO_A1 : VO_A2)) : "memory");
    float xmax = 0.f;
    if (w == 0) xmax = *(const float*)(lds + vp);
    constexpr int NX = w == 2 ? VO_N2 : (w == 3 ? VO_N3 : 10);
    bf16x8 x[10];
#pragma unroll
    for (int p = 0; p < NX; ++p) x[p] = *(const bf16x8*)(lds + mp + p * 1024);
    __builtin_amdgcn_sched_barrier(0);
    if (w < 2) asm volatile(".rept %0\n\ts_nop 3\n\t.endr" :: "n"(w == 0 ? VO_B0 : VO_B1) : "memory");
    if (VO_W >= 0) asm volatile("s_waitcnt lgkmcnt(%0)" :: "n"(VO_W < 0 ? 0 : VO_W) : "memory");
    STAMP(5);
    __builtin_amdgcn_sched_barrier(0);
    int ex = 0;
#pragma unroll
    for (int p = 0; p < 10; ++p) {
      if (w == 0) acc[0] = mfma32(x[p], x[p], p ? acc[0] : Z4);
      if (w == 0 && p == 9) { ex = imax((int)((__builtin_amdgcn_readfirstlane(f2u(__builtin_amdgcn_sqrtf(cb2 * xmax))) >> 23) & 0xffu) + 1, 32); PIN_S(ex); }
#pragma unroll
      for (int i = (w == 1); i < 2 && w != 0; ++i) acc[i] = smfma64(x[p], W[i][p], acc[i], sidx);
      __builtin_amdgcn_sched_barrier(0);
      if (p + NX < 10) x[p + NX] = *(const bf16x8*)(lds + mp + (p + NX) * 1024);
      __builtin_amdgcn_sched_barrier(0);
    }
    STAMP(7);
    if (w != 0) {
      float v[4];
#pragma unroll
      for (int r = 0; r < 4; ++r) {
        const auto sw = __builtin_amdgcn_permlane32_swap(f2u(acc[w == 1][r]), f2u(acc[1][r]), false, false);
        v[r] = (u2f(sw[0]) + u2f(sw[1])) * inv;
      }
      *(u32x2*)(lds + vp) = (u32x2){pack2(v[0], v[1]), pack2(v[2], v[3])};
    } else {
      const int t = (int)f2u(fmx(fmx(acc[0][0], acc[0][1]), fmx(acc[0][2], acc[0][3])) * cm2);
      const int et = wave_imax(t) >> 23, em = ((et + ek) >> 1) + ei;
      int e = imax(et == 255 ? 256 : em, ex);
      PIN_S(e);
      e = imin(e, 256);
      ei = e - 31;
      *(unsigned*)(lds + ip) = (unsigned)ei << 23;
      const u32x3 sr = {(unsigned)(285 - e) << 23, (unsigned)(e - 87) < 79u ? REC_RNG << 23 : 0u, (unsigned)(s + 1)};
      asm volatile("ds_write_b96 %0, %1 offset:%2" :: "v"(lds0), "v"(sr), "n"(L_MISC + 32) : "memory");
    }
    if (last == 0 && w >= 2) asm volatile("s_waitcnt vmcnt(%0)" :: "n"(w == SCH_W ? 2 : 0) : "memory");
    if (w != 0) lds_inc<L_MISC + 28>(lds0);
    STAMP(8);
    if (last) __builtin_amdgcn_s_setprio(0);
    if (last && w == 2) { VM_DRAIN(); lds_inc<L_MISC + 28>(lds0); }
  };
  unsigned* const cntq = (unsigned*)(misc + 12);
  unsigned* const cntt = (unsigned*)(misc + 26 + (w & 1));
  unsigned* const cntp = (unsigned*)(misc + 23);
  unsigned* const cntx2 = (unsigned*)(misc + 28);
  float xn4 = 0.f;
  auto wpf = [&](int s, int lane) {
    touch(a.ws + OFF_WQKVBT + (size_t)(b >> 3) * 76800, (s - WPF0) * (76800 / WPF_N) + imin(lane, 600 / WPF_N - 1) * 128, lds0);
  };
  auto t5_get = [&](int s, int lane) {
    ot.load(a, rs, s - 1 - LAG5, b);
    xn4 = u2f(__hip_atomic_load((const GAS unsigned*)xnrm + (imax(s + 1, 0) * 32 + (lane & 31)) * 4, RLX_AGENT));
    __builtin_amdgcn_sched_barrier(0);
  };
  Sched sch;
  auto late = [&](int s, int lane) {
    if (w == SCH_W && SCH_LATE && __builtin_expect(s >= 0, 1)) *(u32x2*)(misc + 24) = (u32x2){sch.run(a, rs, s, b, lane), (unsigned)(s + 1)};
    if ((w == 2 && F6) || (w == 0 && F4) || (w == 1 && F5) || (w == 3 && F7)) { lds_spin<L_MISC + 48, 1>(lds0, 2u * (unsigned)imax(s + 1, 0)); if (FB_SL && w != 2) __builtin_amdgcn_s_sleep(FB_SL); }
    if (w >= 2) { x_fetch_n<w == 2 ? F6 : F7>(x_src(a, s + 3, b), lds0, s + 3, F4 + F5 + 2 * F67 + (w == 3 ? F6 : 0), lane * 16); return; }
    unsigned sd = 0u;
    if ((w == 0 && XPF == 3) || XPF == 4 || (w == 1 && XPF == 5)) stouch<10>(a.ws + OFF_XN + (size_t)imin(s + 4, NSEG - 1) * (s < 1 ? 0 : 40960) + (b >> 3) * 1280 + w * 64, sd);
    if (w == 0 && WPF_W == 4 && __builtin_expect(s >= WPF0 && s < WPF0 + WPF_N, 0))
      stouch<600 / WPF_N>(a.ws + OFF_WQKVBT + (size_t)(b >> 3) * 76800 + (s - WPF0) * (76800 / WPF_N), sd);
    x_fetch_n<w == 0 ? F4 : F5>(x_src(a, s + 3, b), lds0, s + 3, w == 0 ? 0 : F4, lane * 16);
    if (WPF_W == 0 && w == 0 && __builtin_expect(s >= WPF0 && s < WPF0 + WPF_N, 0)) wpf(s, lane);
    if (w == 1 && __builtin_expect(s == CPF_S, 0)) code_touch(b, lane, lds0);
    lds_spin<L_MISC + 104 + 4 * (w & 1), PS_SL>(lds0, (unsigned)imax(s + 1, 0));
    asm volatile("" :: "s"(sd));
  };
  auto xn_put = [&](int s) {
    float xn2 = xn4;
    xn2 = fmx(xn2, dppf<DPP_XOR1>(xn2)); xn2 = fmx(xn2, dppf<DPP_XOR2>(xn2));
    xn2 = fmx(xn2, dppf<DPP_ROR4>(xn2)); xn2 = fmx(xn2, dppf<DPP_ROR8>(xn2));
    misc[s & 3] = x16mx(xn2);
  };
  if (w == 2) { ot.init(b, lane0); ot.out = (u32x2){0u, 0u}; ot.xo = ot.out; ot.y[0] = ot.y[1] = ot.y[2] = ot.y[3] = (u32x4){0u, 0u, 0u, 0u}; }
  VM_DRAIN();
  lds_arrive(cnt, lane0);
  lds_wait(cnt, 8u);
  sch.tsv = sch.rec = (u32x4){0u, 0u, 0u, 0u};
  sch.side = __builtin_amdgcn_readfirstlane(*(const unsigned*)(misc + 11)) & 8192u;
  sch.o = 0u;
  int sv = NV - 1;
  CODE_PAD((PADT >> (4 * w)) & 15);
#pragma unroll 1
  for (int s = -1; s < NSEG - 1; ++s) {
    const int sv1 = sv == NV - 1 ? 0 : sv + 1;
    FRESH_LANE();
    STAMP(0);
    mma_seg(s + 1, sv1, lane, [&]() { STAMP(1); late(s - 1, lane); if (w == SCH_W) sch.pre(s); STAMP(9); });
    STAMP(2);
    __builtin_amdgcn_sched_barrier(0);
    VM_DRAIN();
    if (w == 2) { ot.run(lane); xn_put(s); }
    STAMP(10);
    st_out(s, lane, [&]() { lds_spin<L_MISC + 92, W1_SL>(lds0, 4u * (unsigned)imax(s, 0)); });
    if (w == 2) t5_get(s, lane);
    __builtin_amdgcn_sched_barrier(0);
    if (w >= 2) vo_pre(s, sv, lane);
    STAMP(3);
    BAR();
    STAMP(4);
    if (__builtin_expect(s >= 0, 1)) {
      if (w == SCH_W) { sch.load(rs, lane0); __builtin_amdgcn_sched_barrier(0); }
      if (w < 2) vo_pre(s, sv, lane);
      vo_mem(s, std::integral_constant<int, 0>{});
      STAMP(6);
      if (w == SCH_W && !SCH_LATE) {
        __builtin_amdgcn_s_setprio(SCH_P);
        *(u32x2*)(misc + 24) = (u32x2){sch.run(a, rs, s, b, lane0), (unsigned)(s + 1)};
      }
      STAMP(11);
    } else BAR();
    __builtin_amdgcn_s_setprio(MMA_P);
    __builtin_amdgcn_sched_barrier(0);
    sv = sv1;
  }
  __builtin_amdgcn_s_setprio(0);
  STAMP_OUT();
  late(NSEG - 2, lane0);
  if (w >= 2) {
    lds_wait(cntp, 4u * (NSEG - 1));
    if (w == 2) { ot.run(lane0); ot.load(a, rs, NSEG - 2 - LAG5, b); }
    if (w == 2) { const u32x2 keep = ot.out; ot.run(lane0); ot.store(a, NSEG - 2 - LAG5, b, lane0); ot.out = keep; }
    __builtin_amdgcn_sched_barrier(0);
    PvTask pv;
    const unsigned ck2 = __builtin_amdgcn_readfirstlane(*(const unsigned*)(misc + 11));
    pv.load(rs, ck2, NSEG - 2 - LAG4, gp, w & 1, lane0);
    st_out(NSEG - 1, lane0, []() {});
    for (unsigned spins = 0; !pv.ready() && spins < SPIN_CAP; ++spins) { asm volatile("" ::: "memory"); pv.load(rs, ck2, NSEG - 2 - LAG4, gp, w & 1, lane0); }
    pv.run(lds, rs, NSEG - 2 - LAG4, gp, m, w & 1, lane0, true);
  }
  if (w != 0) vo_pre(NSEG - 1, (NSEG - 1) % NV, lane0);
  BAR();
  if (w != 0) vo_mem(NSEG - 1, std::integral_constant<int, 1>{});
}

__device__ __forceinline__ void qkv_tail(const RecArgs& a, char* lds, const int w) {
  const int lane0 = __builtin_amdgcn_workitem_id_x() & 63, b = __builtin_amdgcn_workgroup_id_x();
  const int r = qkv_row(b), cb = (b >> 3) - 15 * (r & 1);
  if (cb >= 15) return;
  const unsigned lds0 = (unsigned)(uintptr_t)(LAS char*)lds;
  unsigned* const stage = (unsigned*)(lds + L_MISC) + 17;
  unsigned* const cnty = stage + 1;
  const f32x4 Z4 = {0.f, 0.f, 0.f, 0.f};
  auto fetch = [&](int i) {
    x_fetch_n<10>(x_src(a, NSEG + i, b), lds0, i, 10 * w, lane0 * 16);
  };
  unsigned want = 0;
  auto sync = [&]() { VM_DRAIN(); want += 4u; lds_inc<L_MISC + 72>(lds0); while (__hip_atomic_load(cnty, __ATOMIC_RELAXED, __HIP_MEMORY_SCOPE_WORKGROUP) < want) { } asm volatile("" ::: "memory"); };
  LOGQ(0);
  bf16x8 W[2][20];
  const __amdgpu_buffer_rsrc_t rw = __builtin_amdgcn_make_buffer_rsrc(a.ws + OFF_WQKVBT + (size_t)(cb * 8 + 2 * w) * 20480, 0, 40960, 0x00020000);
  auto w_get = [&](int ks) {
#pragma unroll
    for (int i = 0; i < 2; ++i) W[i][ks] = as8(__builtin_amdgcn_raw_buffer_load_b128(rw, lane0 * 16 + (ks & 3) * 1024, (i * 20 + (ks & ~3)) * 1024, 0));
    __builtin_amdgcn_sched_barrier(0);
  };
#pragma unroll
  for (int ks = 0; ks < QW; ++ks) w_get(ks);
  bf16x8 ones = mk8(0x3f803f80u, 0x3f803f80u, 0x3f803f80u, 0x3f803f80u);
  PIN_V(ones);
  const __amdgpu_buffer_rsrc_t ro = __builtin_amdgcn_make_buffer_rsrc(a.ws + OFF_QB, 0, (int)(OFF_OB - OFF_QB), 0x00020000);
  const int t0 = w & 1, col0 = (cb % 5) * 128 + w * 32;
  auto body = [&](auto SW, auto SQ) {
    unsigned vo[2][2];
    {
      FRESH_LANE();
#pragma unroll
      for (int i = 0; i < 2; ++i)
#pragma unroll
        for (int tt = 0; tt < 2; ++tt) {
          const int tq = tt ^ t0, tk = tq * 16 + c, cc = col0 + i * 16 + (SW ? 4 * g : c), hd = cc / BD, e = cc - hd * BD;
          const int kv = (int)(OFF_KV - OFF_QB) + hd * 64 * KVT;
          vo[i][tt] = (unsigned)(!SW ? kv + 1280 + tq * 672 + ((g & 1) * 21 + e) * 16 + (g >> 1) * 8
                                     : (cb < 5 ? (hd * S_ + tk) * 2 * QST + e * 2 : kv + (e < 16 ? (tk + 32 * (e >> 3)) * 16 + (e & 7) * 2 : 1024 + tk * 8)));
          PIN_V(vo[i][tt]);
        }
    }
    const float qs = cb < 5 ? 0.22360679774997896f * 1.4426950408889634f : 1.0f;
    f32x4 cs[2] = {Z4, Z4};
    LOGQ(1);
    f32x4 acc[2][2], st;
    auto mma = [&](int it, auto FIRST) {
      const char* xp0 = lds + L_XN + (it & 1) * 40960 + t0 * 20480 + lane0 * 16;
      const char* xp1 = lds + L_XN + (it & 1) * 40960 + (1 - t0) * 20480 + lane0 * 16;
      acc[0][0] = acc[0][1] = acc[1][0] = acc[1][1] = st = Z4;
      bf16x8 x[4][2];
#pragma unroll
      for (int p = 0; p < 3; ++p) { x[p][0] = *(const bf16x8*)(xp0 + p * 1024); x[p][1] = *(const bf16x8*)(xp1 + p * 1024); }
      __builtin_amdgcn_sched_barrier(0);
#pragma unroll
      for (int ks = 0; ks < 20; ++ks) {
        if (FIRST && ks + QW < 20) w_get(ks + QW);
#pragma unroll
        for (int q = 0; q < 4; ++q) {
          const int tt = 1 - (q >> 1), i = 1 - (q & 1);
          acc[i][tt] = SW ? mfma32(W[i][ks], x[ks & 3][tt], acc[i][tt]) : mfma32(x[ks & 3][tt], W[i][ks], acc[i][tt]);
          __builtin_amdgcn_sched_barrier(0);
          if (q < 2 && ks + 3 < 20) x[(ks + 3) & 3][q] = *(const bf16x8*)((q ? xp1 : xp0) + (ks + 3) * 1024);
          __builtin_amdgcn_sched_barrier(0);
        }
        st = mfma32(SQ ? x[ks & 3][0] : ones, x[ks & 3][0], st);
        if (FIRST) {
#pragma unroll
          for (int i = 0; i < 2; ++i) cs[i] = SW ? mfma32(W[i][ks], ones, cs[i]) : mfma32(ones, W[i][ks], cs[i]);
        }
        __builtin_amdgcn_sched_barrier(0);
      }
    };
    mma(0, std::integral_constant<bool, true>{});
#pragma unroll 1
    for (int it = 0; it < 4; ++it) {
      if (it) mma(it, std::integral_constant<bool, false>{});
      LOGQ(2 + 3 * it);
      FRESH_LANE();
      float* const sp = (float*)(lds + L_ST) + (it & 1) * 64;
      if (SQ) { if ((c >> 2) == g) sp[32 + t0 * 16 + c] = (c & 3) == 0 ? st[0] : ((c & 3) == 1 ? st[1] : ((c & 3) == 2 ? st[2] : st[3])); }
      else if (g == 0) sp[t0 * 16 + c] = st[0];
      sync();
      LOGQ(3 + 3 * it);
      if (it == 0) fetch(2);
      const bool late = r + 48 >= 55;
      if (it == 1 && !late) fetch(3);
      const bool now = it == 2 && late && __builtin_amdgcn_readfirstlane(*(volatile unsigned*)stage) != 0u;
      if (now) fetch(3);
      const unsigned so = (unsigned)((r + 16 * it) * (cb < 5 ? 64 * QST : KVT));
      auto stat = [&](float sm, float sq, float& mean, float& rstd) {
        mean = sm * (1.0f / DM);
        rstd = __builtin_amdgcn_rsqf(__builtin_fmaxf(sq * (1.0f / DM) - mean * mean, 0.f) + 1e-5f);
      };
#pragma unroll
      for (int tt = 0; tt < 2; ++tt) {
        const int tq = tt ^ t0;
        float mean[4], rstd[4];
        if (SW) {
          stat(sp[tq * 16 + c], sp[32 + tq * 16 + c], mean[0], rstd[0]);
          rstd[0] *= qs;
        } else {
          const f32x4 sm = *(const f32x4*)(sp + tq * 16 + 4 * g), sq = *(const f32x4*)(sp + 32 + tq * 16 + 4 * g);
#pragma unroll
          for (int k = 0; k < 4; ++k) stat(sm[k], sq[k], mean[k], rstd[k]);
        }
#pragma unroll
        for (int i = 0; i < 2; ++i) {
          float v[4];
#pragma unroll
          for (int k = 0; k < 4; ++k) v[k] = (acc[i][tt][k] - mean[SW ? 0 : k] * cs[i][SW ? k : 0]) * rstd[SW ? 0 : k];
          __builtin_amdgcn_raw_buffer_store_b64((u32x2){pack2(v[0], v[1]), pack2(v[2], v[3])}, ro, vo[i][tt], so, 0);
        }
      }
      LOGQ(4 + 3 * it);
      if (it == 2 && late) {
        if (!now) { lds_wait(stage, 1u); fetch(3); }
        sync();
      }
    }
  };
  typedef std::integral_constant<bool, false> F_; typedef std::integral_constant<bool, true> T_;
  if (cb < 10) { if (w >> 1) body(T_{}, T_{}); else body(T_{}, F_{}); }
  else { if (w >> 1) body(F_{}, T_{}); else body(F_{}, F_{}); }
}

struct TokTask {
  u32x4 y[2]; u32x2 ym; u32x4 res;
  unsigned l0, l1, lm, s0, s1;
  static __device__ __forceinline__ bool ok(int sl) { return sl >= 0 && sl < NSEG; }
  __device__ __forceinline__ void init(int h, int j, int lane) {
    const int c = lane & 15, g = lane >> 4;
    l0 = ts_tok(h, g) + (unsigned)(j * 256 + c * 16); l1 = l0 + 16384u;
    lm = ts_mem(h, g + 4 * (c >> 3)) + (unsigned)(j * 64 + (c & 7) * 8);
    res = (u32x4){0u, 0u, 0u, 0u};
    const int qi = 4 * j + (c & 3);
    s0 = g < 2 ? pt_tok(h) + (unsigned)((qi >> 4) * 1024 + ((c >> 2) * 16 + (qi & 15)) * 16 + g * 8) : 0x7ffffff0u;
    s1 = lane < 8 ? pt_mem(h) + (unsigned)((qi >> 4) * 256 + ((c >> 2) * 16 + (qi & 15)) * 8) : 0x7ffffff0u;
    PIN_V(l0); PIN_V(l1); PIN_V(lm); PIN_V(s0); PIN_V(s1);
  }
  __device__ __forceinline__ void load(const __amdgpu_buffer_rsrc_t rs, unsigned o) {
    y[0] = SC1_LOADS(l0, o); y[1] = SC1_LOADS(l1, o);
    ym = __builtin_amdgcn_raw_buffer_load_b64(rs, lm, o, 16);
  }
  __device__ __forceinline__ bool ready() { return wave_all(umin(umin(min4(y[0]), min4(y[1])), umin(ym[0], ym[1])) != 0u); }
  __device__ __forceinline__ void run(int j, int lane, const bf16x8 SelLo) {
    const int c = lane & 15, g = lane >> 4;
    const f32x4 Z4 = {0.f, 0.f, 0.f, 0.f};
    const f32x4 d1 = mfma32(SelLo, as8(y[1]), mfma32(SelLo, as8(y[0]), Z4));
    const f32x4 d0 = mfma32(SelLo, mk8(ym[0], ym[1], 0u, 0u), Z4);
    const int qi = 4 * j + (c & 3), k0 = 16 * g + 4 * (c >> 2);
    float t[4], mv[4];
    float mx = -INFINITY;
#pragma unroll
    for (int r = 0; r < 4; ++r) {
      t[r] = (g < 2 && k0 + r <= qi) ? d1[r] : -INFINITY;
      const float mt = d0[r] + dppf<DPP_ROR8>(d0[r]);
      mv[r] = lane < 8 ? mt : -INFINITY;
      mx = fmx(mx, fmx(t[r], mv[r]));
    }
    mx = fmx(mx, dppf<DPP_ROR8>(mx)); mx = fmx(mx, dppf<DPP_ROR4>(mx));
    mx = x16mx(mx);
    float sum = 0.f;
#pragma unroll
    for (int r = 0; r < 4; ++r) {
      t[r] = __builtin_amdgcn_exp2f(t[r] - mx); mv[r] = __builtin_amdgcn_exp2f(mv[r] - mx);
      sum += t[r] + mv[r];
    }
    sum += dppf<DPP_ROR8>(sum); sum += dppf<DPP_ROR4>(sum);
    sum = x16sum(sum);
    const float rs_ = __builtin_amdgcn_rcpf(sum);
    res = (u32x4){pack2n(t[0] * rs_, t[1] * rs_), pack2n(t[2] * rs_, t[3] * rs_), pack2n(mv[0] * rs_, mv[1] * rs_), pack2n(mv[2] * rs_, mv[3] * rs_)};
    PIN_V(res);
  }
  __device__ __forceinline__ void store(const __amdgpu_buffer_rsrc_t rs, unsigned o, unsigned no = 0u) {
    SC1_STORE8S(((u32x2){res[0], res[1]}), s0 | no, o);
    SC1_STORE8S(((u32x2){res[2], res[3]}), s1 | no, o);
  }
};

__device__ __forceinline__ void t2_mem(const bf16x8 fr, const bf16x8 q0, const bf16x8 q1, const __amdgpu_buffer_rsrc_t rs, unsigned o, int h, int j, float inv, int lane) {
  const f32x4 Z4 = {0.f, 0.f, 0.f, 0.f};
  const f32x4 d0 = mfma32(fr, q0, Z4) * inv, d1 = mfma32(fr, q1, Z4) * inv;
  const unsigned a0 = pack2n(d0[0], d0[1]), a1 = pack2n(d0[2], d0[3]), b0 = pack2n(d1[0], d1[1]), b1 = pack2n(d1[2], d1[3]);
  const auto r0 = __builtin_amdgcn_permlane32_swap(a0, b0, false, false), r1 = __builtin_amdgcn_permlane32_swap(a1, b1, false, false);
  SC1_STORE8S(((u32x2){r0[1], r1[1]}), ts_mem(h, j) + (lane >> 5) * 256 + (lane & 12) * 16 + (lane & 16) * 2 + (lane & 3) * 8, o);
}

template <int w>
__device__ __forceinline__ void rec_waves(const RecArgs& a, char* lds, __amdgpu_buffer_rsrc_t rs) {
  PIN_S(rs);
  const int lane = __builtin_amdgcn_workitem_id_x() & 63, b = __builtin_amdgcn_workgroup_id_x(), c = lane & 15, g = lane >> 4, wid = w; (void)wid;
  const int gp = (b & 7) * 2 + (b >> 7), m = (b >> 3) & 15, h0 = 2 * gp, h = h0 + (m >> 3), j = m & 7;
  float* const misc = (float*)(lds + L_MISC);
  const f32x4 Z4 = {0.f, 0.f, 0.f, 0.f};
  LOGP(0);
  __builtin_amdgcn_s_setprio(3);
  const unsigned lds0 = (unsigned)(uintptr_t)(LAS char*)lds;
  if (!a.layer) img_fetch<12>(a, lds0, h, j, w, lane);
  constexpr int NC = w < 2 ? 2 : 3;
  const int far = ((b >> 2) & 1) ^ 1;
  auto chunk = [&](int f) { return f == 0 ? 4 * far + w : (w == 2 && f == 1 ? 8 + far : (w == 3 && f == 2 ? 9 - far : 4 - 4 * far + w)); };
  bf16x16 Wqk[3][NC];
  u32x4 bm0[NC][2];
#pragma unroll
  for (int f = 0; f < 2 * NC; ++f) bm0[f >> 1][f & 1] = *(const u32x4*)(a.mem0 + (size_t)(c & 7) * DM + 64 * chunk(f >> 1) + 16 * g + 8 * (f & 1));
  if (!a.layer) asm volatile("s_waitcnt vmcnt(0)" ::: "memory");
  BAR();
  LOGP(9);
  const unsigned ck = __builtin_amdgcn_readfirstlane(*(const unsigned*)(misc + 11));
  const unsigned flip = (ck ^ (unsigned)(b << 11)) & 8192u;
#pragma unroll
  for (int t = 0; t < 3; ++t)
#pragma unroll
    for (int f = 0; f < NC; ++f) Wqk[t][f] = cat16(wqk_frag(lds, t, 2 * chunk(f), lane), wqk_frag(lds, t, 2 * chunk(f) + 1, lane));
  bf16x16 We[7];
  auto echunk = [&](int f) { return f < 6 ? (f / 3) * 4 + f % 3 + (f % 3 >= w) : (f == 6 ? 17 - chunk(w - 1) : chunk(f - 7)); };
  if (UNIT && w >= 2) {
#pragma unroll
    for (int f = 0; f < 7; ++f) We[f] = cat16(wqk_frag(lds, 2 * w - 4, 2 * echunk(f), lane), wqk_frag(lds, 2 * w - 4, 2 * echunk(f) + 1, lane));
  }
  auto unit = [&](int seg) {
    if (!UNIT || w < 2) return;
    const char* xp = lds + L_XN + (seg & 1) * 40960 + 20480 + lane * 16;
    bf16x8 x[20];
    f32x4 d = Z4;
#pragma unroll
    for (int k = 0; k < 26; ++k) {
      if (k >= 6) d = mfma32(half8(k - 6 < 14 ? We[(k - 6) >> 1] : Wqk[w < 2 ? 0 : 2 * w - 4][k - 6 < 14 ? 0 : ((k - 6) >> 1) - 7], k & 1), x[k - 6], d);
      __builtin_amdgcn_sched_barrier(0);
      if (k < 20) x[k] = *(const bf16x8*)(xp + echunk(k >> 1) * 2048 + (k & 1) * 1024);
      __builtin_amdgcn_sched_barrier(0);
    }
    if (w == 2) *(u32x2*)(lds + L_QX + (seg % NKQ) * 2048 + 1024 + lane * 16) = (u32x2){pack2(d[0], d[1]), pack2(d[2], d[3])};
    else {
      d *= (g >> 1) ? KEXP : 1.0f;
      char* p = lds + ((g >> 1) ? L_KX : L_QX) + (seg % NKQ) * 2048 + 1024 + (2 * (g & 1) * 16 + c) * 16 + 8;
      *(unsigned*)p = pack2(d[0], d[1]); *(unsigned*)(p + 256) = pack2(d[2], d[3]);
    }
    __builtin_amdgcn_sched_barrier(0);
  };
  if (!a.layer) lds_arrive((unsigned*)(misc + 6), lane);
  else {
    if (w == 0) {
      for (unsigned spins = 0; spins < SPIN_CAP; ++spins) {
        if (__builtin_amdgcn_raw_buffer_load_b32(rs, (b & 7) * 256, (unsigned)OFF_RDY, 16) >= 16u) break;
        __builtin_amdgcn_s_sleep(4);
        asm volatile("" ::: "memory");
      }
      lds_inc<L_MISC + 84>(lds0);
    }
    lds_wait((unsigned*)(misc + 21), 2u);
#pragma unroll
    for (int f = 0; f < 2 * NC; ++f) PIN_V(bm0[f >> 1][f & 1]);
#pragma unroll
    for (int i = 0; i < 10; ++i) x_fetch(x_src(a, 0, b), lds0, 0, 10 * w + i, lane);
  }
  const bf16x8 SelLo = sel_frag(c, 0), SelHi = sel_frag(c, 8);
  bf16x8 ONES = mk8(0x3f803f80u, 0x3f803f80u, 0x3f803f80u, 0x3f803f80u);
  if (w < 2) PIN_V(ONES);
  bool dead = false;
  float inv = 1.0f;
  unsigned* const cnt2 = (unsigned*)(misc + 7);
  unsigned l2off[2 * NC];
#pragma unroll
  for (int f = 0; f < 2 * NC; ++f) { l2off[f] = acc_off(16 * chunk(f >> 1) + 4 * g + 2 * (f & 1) + (c >> 3), c & 7) ^ flip; PIN_V(l2off[f]); }
  int sidx = (c >> 3) ? 0xEEEE : 0x4444;
  PIN_V(sidx);
  const unsigned p1off = (unsigned)((w == 0 ? (h & 2) * 1024 + j * 256 + (h & 1) * 64 + (g & 1) * 8 : (h & 3) * 32768 + j * 512 + (g >> 1) * 256 + (g & 1) * 64) + (c & 4) * 32 + (c & 3) * 16);
  unsigned p1v = (c < 8) && (w != 0 || g >= 2) ? p1off : 0x7ffffff0u, p1cur = 0x7ffffff0u;
  PIN_V(p1v); PIN_V(p1cur);
  const int hd = (c >> 2) & 1, mr = hd ? 3 - (c & 3) : (c & 3);
  const unsigned l1off = (unsigned)(((h0 + hd) & 3) * 32768 + g * 512 + (w & 1) * 128 + (c >> 3) * 64 + mr * 16);
  const unsigned l1offm = (unsigned)((h0 & 2) * 1024 + (g + 4 * (c >> 3)) * 256 + (w & 1) * 128 + hd * 64 + mr * 16);
  const int plane = (lane & ~6) | ((lane & 2) << 1) | ((lane & 4) >> 1);
  unsigned acco = c < 8 ? (acc_off(64 * far + m * 4 + g, 4 * (w & 1) + mr) + (unsigned)(hd * 8)) ^ flip : 0x7ffffff0u;
  unsigned acco2 = c >= 8 ? (acc_off(64 - 64 * far + m * 4 + g, 4 * (w & 1) + mr) + (unsigned)(hd * 8)) ^ flip
                          : (c < 4 ? (acc_off(128 + m * 2 + (g & 1), 4 * (w & 1) + mr) + (unsigned)((g >> 1) * 8)) ^ flip : 0x7ffffff0u);
  if (w < 2) { PIN_V(acco); PIN_V(acco2); }
  unsigned bmo[NC];
#pragma unroll
  for (int f = 0; f < NC; ++f) { bmo[f] = (unsigned)(chunk(f) * 1024 + lane * 16); PIN_V(bmo[f]); }
  bf16x8 bm[NC];
  auto bm_put = [&](int f) { *(bf16x8*)(lds + L_BM + bmo[f]) = bm[f]; __builtin_amdgcn_sched_barrier(0); };
  f32x4 acc[3] = {Z4, Z4, Z4};
  const int rt2 = ((c >> 3) * 8 + 4 * (g & 1)) * 28 + 16 + (c & 7);
  unsigned redo[3] = {(unsigned)(4 * g * 28 + c), (unsigned)(g < 2 ? (4 * g + 2) * 28 + c : rt2), (unsigned)(g < 2 ? (4 * g + 10) * 28 + c : rt2 + 56)};
#pragma unroll
  for (int k = 0; k < 3; ++k) { redo[k] = L_RED1 + w * 1792 + redo[k] * 4; PIN_V(redo[k]); }
  auto red_put = [&]() {
    auto hs = [&](float x, float y) { const auto sw = __builtin_amdgcn_permlane32_swap(f2u(x), f2u(y), false, false); return u2f(sw[0]) + u2f(sw[1]); };
#pragma unroll
    for (int k = 0; k < 3; ++k) {
#pragma unroll
      for (int i = 0; i < 2; ++i) *(float*)(lds + redo[k] + i * 112) = k == 0 ? hs(acc[0][i], acc[1][i]) : hs(acc[k - 1][2 + i], acc[2][2 * k - 2 + i]);
      __builtin_amdgcn_sched_barrier(0);
    }
  };
#pragma unroll
  for (int f = 0; f < NC; ++f) {
    const u32x4 u = bm0[f][0], v = bm0[f][1];
    bm[f] = c < 8 ? mk8(u[0], u[2], v[0], v[2]) : mk8(u[1], u[3], v[1], v[3]);
    bm_put(f);
#pragma unroll
    for (int t = 0; t < 3; ++t) acc[t] = smfma64(bm[f], Wqk[t][f], acc[t], sidx);
  }
  red_put();
  VM_DRAIN();
  if (a.layer) lds_arrive((unsigned*)(misc + 6), lane);
  lds_wait((unsigned*)(misc + 6), 8u);
#pragma unroll
  for (int i = 0; i < 10; ++i) x_fetch(x_src(a, 1, b), lds0, 1, 10 * w + i, lane);
  LOGP(1);
  unit(0);
  BAR();
  STAMP_DECL(); ARRIVE_DECL();
  bf16x8 fr, qx[2];
  auto kq_get = [&](int seg) {
    const char* p = lds + (w == 1 ? L_KX : L_QX) + (seg % NKQ) * 2048 + lane * 16;
    if (w == 1 || w == 2 || (w == 3 && seg == NSEG - 1)) { qx[0] = *(const bf16x8*)p; qx[1] = *(const bf16x8*)(p + 1024); }
  };
  unsigned qo4 = (unsigned)(L_RED1 + (w == 1 ? (c & 7) * 112 + (c >> 3) * 3584 : c * 112) + g * 16), qo2 = qo4 + 64u - (unsigned)(g * 8), qo2b = qo2 + 3584u;
  if (w < 3) { PIN_V(qo4); PIN_V(qo2); if (w != 1) PIN_V(qo2b); }
  auto pub1 = [&](unsigned off, unsigned o1) {
    if (w == 0) fr = qk_frag(lds + qo4, lds + qo2, lds + qo2b);
    else {
      const f32x4 a0 = *(const f32x4*)(lds + qo4), a1 = *(const f32x4*)(lds + qo4 + 1792);
      const f32x2 b0 = *(const f32x2*)(lds + qo2), b1 = *(const f32x2*)(lds + qo2 + 1792);
      float v[6];
#pragma unroll
      for (int i = 0; i < 6; ++i) v[i] = i < 4 ? a0[i] + a1[i] : b0[i - 4] + b1[i - 4];
#pragma unroll
      for (int i = 0; i < 6; ++i) v[i] += dppf<DPP_ROR8>(v[i]);
      fr = mk8(pack2(v[0], v[1]), pack2(v[2], v[3]), pack2(v[4], v[5]), 0u);
    }
    const f32x4 sc = mfma32(w == 0 ? fr : qx[0], fr, Z4);
    if (w == 0) SC1_STORE8S(((u32x2){pack2n(sc[0], sc[1]), pack2n(sc[2], sc[3])}), off, o1);
    else {
      const f32x4 s2 = mfma32(qx[1], fr, Z4);
      __builtin_amdgcn_raw_buffer_store_b128((u32x4){pack2n(sc[0], sc[1]), pack2n(sc[2], sc[3]), pack2n(s2[0], s2[1]), pack2n(s2[2], s2[3])}, rs, off, o1, 16);
    }
  };
  VM_DRAIN();
  kq_get(0);
  unsigned o1 = sct_slot(ck, 0u), o1m = scm_slot(ck, 0u);
  unsigned t2o = ts_tok(h, j) + (unsigned)((c >> 2) * 256 + g * 64 + (c & 3) * 16);
  TokScores t2;
  if (w == 3) { PIN_V(t2o); t2.get(lds, 0, lane); t2.run(0); t2.run(1); t2.store(rs, t2o, ts_slot(ck, 0)); }
  if (w < 2) pub1(p1v, w == 0 ? o1m : o1);
  const unsigned tst = now32() | 1u;
  if (w == 3 && ST_X) __builtin_amdgcn_raw_buffer_store_b32(tst, rs, lane < 2 ? ts_piece(b >> 2) - ACC_TS + (b & 3) * 4 + lane * 8192 : 0x7ffffff0, (unsigned)(OFF_ACC + (SR - 1) * ACC_STEP) + ACC_TS, 16);
  LOGP(2);
  BAR();
  LOGP(3);
#pragma unroll
  for (int i = 0; i < 10; ++i) x_fetch(x_src(a, 2, b), lds0, 2, 10 * w + i, lane);
  unit(1);
  VM_DRAIN();
  unsigned o2 = (unsigned)OFF_ACC, o2p = 0u, tw = 0u;
  asm volatile("" : "+s"(o2p));
  unsigned tc0 = 0u, tstar = 0u;
  int dmin = 0, dmax = 0;
  int e1 = 100000;
  unsigned offs = 100u;
  TokTask tk;
  OutTask ot;
  PvTask pv;
  PvTask2 pv2;
  if (w == 3) pv2.init(gp, lane);
  if (w == 2) tk.init(h, j, lane);
  unsigned o4 = 0u;
  unsigned o3, o3p;
  u32x16 P = {0u, 0u, 0u, 0u, 0u, 0u, 0u, 0u, 0u, 0u, 0u, 0u, 0u, 0u, 0u, 0u}, V = P;
  u32x8 V8 = {0u, 0u, 0u, 0u, 0u, 0u, 0u, 0u};
  if (w < 2) asm volatile("" : "+{v[208:223]}"(P), "+{v[184:191]}"(V8), "+{v[192:207]}"(V));
  unsigned tso = lane < 2 ? ts_piece(b >> 2) + (unsigned)((b & 3) * 4 + lane * 8192) : 0x7ffffff0u;
  if (w < 2) PIN_V(tso);
  float minf = g >= 2 ? -INFINITY : 0.f;
  if (w < 2) PIN_V(minf);
  int sv = NV - 1;
  bf16x8 vox[5];
  unsigned vma[3], need2;
  u32x4 slo, shi;
  float k1;
  const u16* xsrc;
  float invv = 1.0f;
  auto prep0 = [&](int s) {
    xsrc = x_src(a, s + 3, b);
    if (w == 2) { o3p = ts_slot(ck, (s + 1 - LAG3) & 3); o3 = ts_slot(ck, s & 3); o4 = pt_slot(ck, imax(s - LAG3, 0)); PIN_S(o3); }
    if (w == 3) { o3p = pt_slot(ck, (s - LAG4) & 3); o4 = ts_slot(ck, s + 1); }
    PIN_S(xsrc); PIN_S(o3p); PIN_S(o4);
    __builtin_amdgcn_sched_barrier(0);
  };
  auto prep = [&](int s) {
    o2 = (unsigned)OFF_ACC + (unsigned)(s % SR) * (unsigned)ACC_STEP;
    sv = sv == NV - 1 ? 0 : sv + 1;
    PIN_S(o2); PIN_S(sv);
    if (w < 2) {
      PIN_S(tc0);
      o1 = sct_slot(ck, s % SR); o1m = scm_slot(ck, s % SR);
      need2 = 4u * (unsigned)(s + 1) + 2u * ((unsigned)(s + 2) / NSEG);
      PIN_S(o1); PIN_S(o1m); PIN_S(need2);
      __builtin_amdgcn_sched_barrier(0);
      kq_get(s);
    }
    invv = misc[4 + (s & 1)];
  };
  auto prep2 = [&](int s) {
#pragma unroll
    for (int k = 0; k < 3; ++k) {
      const int t = k == 0 ? 2 : (k == 1 ? far : 1 - far);
      const char* p = lds + L_VOX + sv * 5120 + t * 1024 + (plane & 47) * 16 + (g & 1) * 8;
      auto rd = [&](const char* q) { const u32x2 a0 = *(const u32x2*)q, a1 = *(const u32x2*)(q + 256); return mk8(a0[0], a0[1], a1[0], a1[1]); };
      vox[k == 0 ? 0 : 2 * k - 1] = rd(p);
      if (k) vox[2 * k] = rd(p + 3072);
      vma[k] = lds0 + (unsigned)(L_VOM + sv * 1280 + t * 256 + (plane & 31) * 8);
      PIN_V(vma[k]);
    }
    __builtin_amdgcn_sched_barrier(0);
    inv = u2f(__builtin_amdgcn_readfirstlane(f2u(invv)));
    slo = __builtin_bit_cast(u32x4, SelLo); shi = __builtin_bit_cast(u32x4, SelHi);
#pragma unroll
    for (int i = 0; i < 4; ++i) { slo[i] = ((slo[i] >> 7) & 0x10001u) * (f2u(inv) >> 16); shi[i] = ((shi[i] >> 7) & 0x10001u) * (f2u(inv) >> 16); }
    if (F0A) { xsrc = x_src(a, s + 3, b); PIN_S(xsrc); }
    k1 = KEXP * inv;
    PIN_V(k1); PIN_V(slo); PIN_V(shi);
    asm volatile("" : "+v"(vox[0]), "+v"(vox[1]), "+v"(vox[2]), "+v"(vox[3]), "+v"(vox[4]));
    __builtin_amdgcn_sched_barrier(0);
  };
  auto rows = [&](int s) {
    const int dl = spin_until(tw);
    if (w < 2) tc0 = cyc32();
    __builtin_amdgcn_sched_barrier(0);
    STAMP(1);
    unsigned spins = 0;
    auto pass = [&](bool first) {
      u32x4 x[2 * NC];
#pragma unroll
      for (int f = 0; f < NC; ++f) {
        if (f && w < 2 && first) { prep(s); asm volatile(".rept %0\n\ts_nop 3\n\t.endr" :: "n"(P2_G1)); }
        else if (f) asm volatile(".rept %0\n\ts_nop 3\n\t.endr" :: "n"(w < 2 ? P2_G0 : (f == 1 ? P2_G2 : P2_G3)));
        __builtin_amdgcn_sched_barrier(0);
        x[2 * f] = SC1_LOADS(l2off[2 * f], o2p); x[2 * f + 1] = SC1_LOADS(l2off[2 * f + 1], o2p);
        __builtin_amdgcn_sched_barrier(0);
      }
      if (first) lds_inc<L_MISC + 92>(lds0);
      if (first) { if (w >= 2) prep(s); else prep2(s); }
      if ((w == 2 || (w == 3 && !TS_B)) && first && P2_W) lds_spin<L_MISC + 92, 0>(lds0, 4u * (unsigned)s);
      if (w == 2 && first) tk.store(rs, o4, s > LAG3 ? 0u : 0x7ff00000u);
      if (w == 3 && first && !TS_B) t2.store(rs, t2o, o4);
      if (first && w < 2) {
        p1cur = p1v; offs = (offs - (unsigned)dl) & ~3u;
        PIN_V(p1cur); PIN_S(offs);
        tstar = tc0 + (unsigned)(e1 + D1_M + (w == 1 ? D1_W1 : 0)) - offs * 20u;
        dmin = offs < 40u ? D1_MIN : REC_D1;
        dmin = s < EARLY_N ? REC_D1 + EARLY_D1 : dmin;
        dmax = REC_D1;
        PIN_S(tstar); PIN_S(dmin); PIN_S(dmax);
      }
      __builtin_amdgcn_sched_barrier(0);
      unsigned ok = 32u;
#pragma unroll
      for (int t = 0; t < 3; ++t) { acc[t] = Z4; PIN_V(acc[t]); }
      __builtin_amdgcn_sched_barrier(0);
      bool r = true;
#pragma unroll
      for (int f = 0; f < NC; ++f) {
        unsigned p[4];
#pragma unroll
        for (int k = 0; k < 2; ++k) {
          const u32x4 v = x[2 * f + k];
          p[2 * k] = pack2((float)(int)v[0], (float)(int)v[1]); p[2 * k + 1] = pack2((float)(int)v[2], (float)(int)v[3]);
          asm volatile("" : "+v"(p[2 * k]), "+v"(p[2 * k + 1]));
          __builtin_amdgcn_sched_barrier(0);
        }
        ok = (ok & x[2 * f][0] & x[2 * f][2]) & (x[2 * f + 1][0] & x[2 * f + 1][2]);
        PIN_V(ok);
        __builtin_amdgcn_sched_barrier(0);
        bm[f] = mk8(p[0], p[1], p[2], p[3]);
        acc[0] = smfma64(bm[f], Wqk[0][f], acc[0], sidx);
        __builtin_amdgcn_sched_barrier(0);
        bm_put(f);
        acc[1] = smfma64(bm[f], Wqk[1][f], acc[1], sidx);
        __builtin_amdgcn_sched_barrier(0);
        if (f == NC - 1) { u64 bad = __builtin_amdgcn_ballot_w64(ok == 0u); PIN_S(bad); r = bad == 0ull; }
        __builtin_amdgcn_sched_barrier(0);
        acc[2] = smfma64(bm[f], Wqk[2][f], acc[2], sidx);
        __builtin_amdgcn_sched_barrier(0);
      }
      red_put();
      return r;
    };
    if (__builtin_expect(!pass(true), 0)) {
      FC(0);
      do {
        if (dead || ++spins > SPIN_CAP) { dead = true; break; }
        asm volatile("" ::: "memory");
      } while (!pass(false));
    }
    STAMPV(12, spins); LOGF(1, spins);
    TSTAMP(2, now32()); TSTAMP(3, spins);
  };
  if (w >= 2) prep0(-1);
  prep(0);
  if (w < 2) prep2(0);
  CODE_PAD((PADR >> (4 * w)) & 15);
#pragma unroll 1
  for (int s = 0; s < NSEG - 1; ++s) {
    if (__builtin_expect(o2p != 0u, 1)) rows(s);
    STAMP(2);
    BAR();
    STAMP(3);
    if (w < 2) pub1(p1cur, w == 0 ? o1m : o1);
    if (w == 2) tk.load(rs, o3p);
    if (w == 3) pv2.load(rs, o3p);
    const unsigned tp1 = cyc32();
    __builtin_amdgcn_sched_barrier(0);
    if (w == 2) {
      __builtin_amdgcn_s_setprio(W2_P);
      prep0(s);
      if (W2_D) spin_cyc(tp1 + W2_D);
      fr = qk_frag(lds + qo4, lds + qo2, lds + qo2b);
      t2_mem(fr, qx[0], qx[1], rs, o3, h, j, invv * KEXP, lane);
      __builtin_amdgcn_sched_barrier(0);
      asm volatile("s_waitcnt vmcnt(1)" ::: "memory");
      lds_inc<L_MISC + 28>(lds0);
      kq_get(s + 1);
    }
    if (w == 3) {
      __builtin_amdgcn_s_setprio(T4_P);
      prep0(s);
      t2.get(lds, s + 1, lane); t2.run(0); t2.run(1);
      __builtin_amdgcn_sched_barrier(0);
    }
    if (w >= 2 && __builtin_expect(s == NSEG - 2, 0)) { VM_DRAIN(); lds_inc<L_MISC + 28>(lds0); }
    if (w >= 2) { __builtin_amdgcn_s_setprio(UN_P); unit(s + 2); }
    TSTAMP(0, now32());
    STAMP(4);
    __builtin_amdgcn_sched_barrier(0);
    unsigned tpub = 0;
    u32x2 sw;
    if (w == 2) {
      STAMP(5);
      __builtin_amdgcn_sched_barrier(0);
      STAMP(6);
      ARRIVE(0);
      if (FB_E) { PIN_V(tk.y[0]); PIN_V(tk.y[1]); PIN_V(tk.ym); }
      else tk.run(j, lane, SelLo);
      if (F2B) { lds_spin<L_MISC + 48, 1>(lds0, 2u * (unsigned)(s + 1)); x_fetch_n<F2B>(xsrc, lds0, s + 3, P2B, lane * 16); }
      if (FB_E) tk.run(j, lane, SelLo);
      __builtin_amdgcn_s_setprio(SP_P);
    }
    if (w == 3) {
      if (F3A) { spin_cyc(tp1 + F3A_D); x_fetch_n<F3A>(xsrc, lds0, s + 3, 40 - F3C - F3 - F3A, lane * 16); }
      if (TS_B == 1) { PIN_V(pv2.pa[1]); lds_spin<L_MISC + 48, 1>(lds0, 2u * (unsigned)(s + 1)); t2.store(rs, t2o, o4); }
      if (F3 && FB_E) { VM_DRAIN(); lds_spin<L_MISC + 48, 1>(lds0, 2u * (unsigned)(s + 1)); x_fetch_n<F3>(xsrc, lds0, s + 3, 40 - F3C - F3, lane * 16); }
      pv2.run(lds, sv < 1 + LAG4 ? sv + NV - 1 - LAG4 : sv - 1 - LAG4, lane);
      if (TS_B == 2) t2.store(rs, t2o, o4);
      if (ST_E) lds_inc<L_MISC + 112>(lds0);
      STAMP(11);
      __builtin_amdgcn_s_setprio(SP_P);
      STAMP(5);
      ARRIVE(0);
      if (F3 && !FB_E) { lds_spin<L_MISC + 48, 1>(lds0, 2u * (unsigned)(s + 1)); x_fetch_n<F3>(xsrc, lds0, s + 3, 40 - F3C - F3, lane * 16); }
      STAMP(6);
    }
    if (w < 2) {
      __builtin_amdgcn_s_setprio(D1_P);
      x_fetch_n<F0A>(xsrc, lds0, s + 3, PA + w * F0A, lane * 16);
      int dq = imin((int)(tstar - tp1), dmax);
      dq = dq < -D1_LATE ? dmax : dq;
      PIN_S(dq);
      spin_exact(tp1 + (unsigned)imax(dq, dmin));
      STAMP(5);
      u32x32 S;
      auto sc_load = [&]() {
        asm volatile("buffer_load_dwordx4 v[224:227], %2, %4, %5 offen sc1\n\t.rept %7\n\ts_nop 3\n\t.endr\n\t"
                     "buffer_load_dwordx4 v[228:231], %3, %4, %6 offen sc1\n\tbuffer_load_dwordx4 v[232:235], %3, %4, %6 offen offset:2048 sc1\n\t"
                     "buffer_load_dwordx4 v[236:239], %3, %4, %6 offen offset:256 sc1\n\tbuffer_load_dwordx4 v[240:243], %3, %4, %6 offen offset:2304 sc1\n\t"
                     "v_mov_b32 v220, 0x42000000\n\tv_mov_b32 v221, 0\n\tv_mov_b32 v222, 0x42000000\n\tv_mov_b32 v223, 0"
                     : "=&{v[224:255]}"(S), "+{v[208:223]}"(P) : "v"(l1offm), "v"(l1off), "s"(rs), "s"(o1m), "s"(o1), "n"(P1_G) : "memory");
      };
      sc_load();
      __builtin_amdgcn_sched_barrier(0);
      lds_inc<L_MISC + 48>(lds0);
      STAMP(6);
      ARRIVE(0);
      lds_spin<L_MISC + 28, 0>(lds0, need2);
      __builtin_amdgcn_s_setprio(3);
      asm volatile("ds_read_b64 v[192:193], %3\n\tds_read_b64 v[196:197], %3 offset:768\n\tds_read_b64 v[200:201], %4\n\tds_read_b64 v[204:205], %4 offset:768\n\tds_read_b64 v[184:185], %2"
                   : "+{v[184:191]}"(V8), "+{v[192:207]}"(V) : "v"(vma[0]), "v"(vma[1]), "v"(vma[2]) : "memory");
      __builtin_amdgcn_sched_barrier(0);
      {
        const int de = imin((int)(tp1 - tc0) - e1, D1_C);
        e1 += s == 0 ? 0 : de >> (s < EARLY_N && de < -D1_C ? 0 : D1_G);
        PIN_S(e1);
      }
      STAMP(11);
      __builtin_amdgcn_sched_barrier(0);
      {
        unsigned spins = 0;
        u64 miss, bad;
#define MM(d, a, b, c) "v_mfma_f32_16x16x32_bf16 " d ", " a ", " b ", " c "\n\t"
#define FM(d, x, r, ctrl) "v_fmac_f32_dpp " d ", " x ", " r " " ctrl "\n\t"
#define ID_ "quad_perm:[0,1,2,3] row_mask:0xf bank_mask:"
#define HM_ "row_half_mirror row_mask:0xf bank_mask:"
#define R8_ "row_ror:8 row_mask:0xf bank_mask:"
#define MI_ "row_mirror row_mask:0xf bank_mask:"
        auto soft = [&]() {
          asm volatile(
            "s_waitcnt vmcnt(4)\n\t" MM("v[244:247]", "%6", "v[224:227]", "0") "v_min3_u32 v214, v224, v225, v226\n\t"
            "s_waitcnt vmcnt(3)\n\t" MM("v[248:251]", "%6", "v[228:231]", "0") "v_min3_u32 v214, v214, v227, v228\n\tv_min3_u32 v214, v214, v229, v230\n\t"
            "s_waitcnt vmcnt(2)\n\t" MM("v[248:251]", "%6", "v[232:235]", "v[248:251]") "v_min3_u32 v214, v214, v231, v232\n\t"
            "v_add_f32_dpp v212, v244, v244 " R8_ "0x3\n\tv_add_f32_dpp v213, v245, v245 " R8_ "0x3\n\t"
            "s_waitcnt vmcnt(1)\n\t" MM("v[248:251]", "%7", "v[236:239]", "v[248:251]")
            "v_add_f32_dpp v212, v246, v246 " R8_ "0xc\n\tv_add_f32_dpp v213, v247, v247 " R8_ "0xc\n\t"
            "s_waitcnt vmcnt(0) lgkmcnt(0)\n\t" MM("v[248:251]", "%7", "v[240:243]", "v[248:251]")
            "v_fma_f32 v212, v212, %14, %15\n\tv_fma_f32 v213, v213, %14, %15\n\tv_exp_f32 v216, v212\n\tv_exp_f32 v217, v213\n\t"
            "v_min3_u32 v214, v214, v233, v234\n\tv_cvt_pk_bf16_f32 v208, v216, v217\n\tv_min3_u32 v214, v214, v235, v236\n\tds_read_b96 v[188:190], %16 offset:%18\n\t"
            "v_exp_f32 v216, v248\n\tv_mov_b32_dpp v209, v208 " R8_ "0xf\n\tv_exp_f32 v217, v249\n\tv_exp_f32 v218, v250\n\t"
            MM("v[252:255]", "%8", "v[208:211]", "0") "v_exp_f32 v219, v251\n\tv_cvt_pk_bf16_f32 v232, v216, v217\n\t"
            MM("v[224:227]", "v[192:195]", "v[208:211]", "0") "v_cvt_pk_bf16_f32 v233, v218, v219\n\tv_min3_u32 v214, v214, v237, v238\n\tv_mov_b32_dpp v234, v232 " R8_ "0xf\n\t"
            MM("v[228:231]", "v[196:199]", "v[208:211]", "0") "v_mov_b32_dpp v235, v233 " R8_ "0xf\n\t"
            MM("v[244:247]", "v[204:207]", "v[208:211]", "0") "v_min3_u32 v214, v214, v239, v240\n\t"
            MM("v[252:255]", "%8", "v[232:235]", "v[252:255]") MM("v[224:227]", "%10", "v[232:235]", "v[224:227]") "v_min3_u32 v214, v214, v241, v242\n\t"
            MM("v[228:231]", "%11", "v[232:235]", "v[228:231]") "s_waitcnt lgkmcnt(0)\n\tv_sub_u32 v215, v190, %17 clamp\n\t"
            MM("v[236:239]", "v[200:203]", "v[208:211]", "0") "v_min3_u32 v214, v214, v243, v215\n\tv_cmp_eq_u32 %4, 0, v214\n\tv_rcp_f32 v216, v252\n\t"
            MM("v[240:243]", "v[184:187]", "v[208:211]", "0") "v_mul_f32 v216, v216, v188\n\tv_subrev_u32 v215, 0x2f800000, v252\n\t"
            MM("v[236:239]", "%12", "v[232:235]", "v[236:239]") FM("v220", "v224", "v216", ID_ "0x1") FM("v221", "v225", "v216", ID_ "0x1")
            "v_mov_b32_dpp v217, v216 " HM_ "0xf\n\t"
            MM("v[244:247]", "%13", "v[232:235]", "v[244:247]") FM("v220", "v230", "v216", ID_ "0x2") FM("v221", "v231", "v216", ID_ "0x2")
            MM("v[240:243]", "%9", "v[232:235]", "v[240:243]") FM("v220", "v228", "v217", HM_ "0x1") FM("v221", "v229", "v217", HM_ "0x1")
            "v_cmp_ge_u32 vcc, v215, v189\n\t" FM("v220", "v226", "v217", HM_ "0x2") FM("v221", "v227", "v217", HM_ "0x2")
            "s_or_b64 %5, %4, vcc"
            : "+{v[224:255]}"(S), "+{v[208:223]}"(P), "+{v[184:191]}"(V8), "+{v[192:207]}"(V), "=&s"(miss), "=&s"(bad)
            : "v"(slo), "v"(shi), "v"(ONES), "v"(vox[0]), "v"(vox[1]), "v"(vox[2]), "v"(vox[3]), "v"(vox[4]), "v"(k1), "v"(minf), "v"(lds0), "s"(s), "n"(L_MISC + 32)
            : "vcc", "scc", "memory");
        };
        soft();
        if (__builtin_expect(bad != 0ull, 0)) {
          if (miss) FC(1); else FC(2);
          while (miss != 0ull && !dead) {
            if (++spins > SPIN_CAP) dead = true;
            asm volatile("s_nop 7\n\ts_nop 7" ::: "memory");
            sc_load(); soft();
          }
          if (bad != 0ull) {
            u32x4 y[5];
#pragma unroll
            for (int k = 0; k < 4; ++k) y[k] = SC1_LOADS(l1off + (k & 1) * 2048 + (k >> 1) * 256, o1);
            y[4] = SC1_LOADS(l1offm, o1m);
            const f32x4 d0 = mfma32(as8(slo), as8(y[4]), Z4);
            f32x4 d1 = mfma32(as8(shi), as8(y[3]), mfma32(as8(shi), as8(y[2]), mfma32(as8(slo), as8(y[1]), mfma32(as8(slo), as8(y[0]), Z4))));
            const bool lo = c < 8;
            const float m0 = __builtin_fmaf((lo ? d0[0] : d0[2]) + dppf<DPP_ROR8>(lo ? d0[2] : d0[0]), k1, minf);
            const float m1 = __builtin_fmaf((lo ? d0[1] : d0[3]) + dppf<DPP_ROR8>(lo ? d0[3] : d0[1]), k1, minf);
            float mx = fmx(fmx(fmx(d1[0], d1[1]), d1[2]), fmx(fmx(d1[3], m0), m1));
            mx = fmx(mx, dppf<DPP_ROR8>(mx));
            mx = x32mx(x16mx(mx));
            d1 -= mx;
            const unsigned mm = pack2(__builtin_amdgcn_exp2f(m0 - mx), __builtin_amdgcn_exp2f(m1 - mx));
            const unsigned t01 = pack2(__builtin_amdgcn_exp2f(d1[0]), __builtin_amdgcn_exp2f(d1[1])), t23 = pack2(__builtin_amdgcn_exp2f(d1[2]), __builtin_amdgcn_exp2f(d1[3]));
            const bf16x8 pm = mk8(mm, dpp_ror8(mm), 0u, 0u), pt = mk8(t01, t23, dpp_ror8(t01), dpp_ror8(t23));
            const float r = __builtin_amdgcn_rcpf(mfma32(ONES, pt, mfma32(ONES, pm, Z4))[0]) * u2f(V8[4]), rm = dppf<0x141>(r);
            f32x4 o[5];
#pragma unroll
            for (int k = 0; k < 5; ++k) o[k] = mfma32(vox[k], pt, mfma32(k ? mk8(V[4 * k - 4], V[4 * k - 3], 0u, 0u) : mk8(V8[0], V8[1], 0u, 0u), pm, Z4));
#pragma unroll
            for (int i = 0; i < 2; ++i) {
              const float oa = dppf<0x141>(o[2][i]), ob = dppf<0x141>(o[1][2 + i]);
              P[12 + i] = f2u(__builtin_fmaf(hd ? ob : oa, rm, __builtin_fmaf(hd ? o[2][2 + i] : o[1][i], r, i ? 0.f : 32.0f)));
            }
            P[8] = f2u(r); P[9] = f2u(rm);
#pragma unroll
            for (int i = 0; i < 4; ++i) { S[12 + i] = f2u(o[3][i]); S[16 + i] = f2u(o[0][i]); S[20 + i] = f2u(o[4][i]); }
            asm volatile("s_nop 7\n\ts_nop 7" : "+{v[224:255]}"(S), "+{v[208:223]}"(P));
          }
        }
        STAMP(7); STAMPV(13, spins); LOGF(0, spins);
        {
          asm volatile("v_cvt_i32_f32 v218, v220\n\tv_cvt_i32_f32 v219, v221\n\tv_and_or_b32 v218, v218, %7, 2\n\tbuffer_atomic_add_x2 v[218:219], %3, %5, %6 offen\n\t"
                       FM("v222", "v236", "v216", R8_ "0x4") FM("v223", "v237", "v216", R8_ "0x4") "ds_read_b64 %2, %8 offset:%9\n\t"
                       FM("v222", "v238", "v217", MI_ "0x8") FM("v223", "v239", "v217", MI_ "0x8") "v_permlane32_swap_b32 v240, v242\n\t"
                       FM("v222", "v244", "v217", MI_ "0x4") FM("v223", "v245", "v217", MI_ "0x4") "v_permlane32_swap_b32 v241, v243\n\t"
                       FM("v222", "v246", "v216", R8_ "0x8") FM("v223", "v247", "v216", R8_ "0x8")
                       FM("v222", "v240", "v216", ID_ "0x1") FM("v223", "v241", "v216", ID_ "0x1") FM("v222", "v242", "v217", HM_ "0x1") FM("v223", "v243", "v217", HM_ "0x1")
                       "v_cvt_i32_f32 v212, v222\n\tv_cvt_i32_f32 v213, v223\n\tv_and_or_b32 v212, v212, %7, 2\n\tbuffer_atomic_add_x2 v[212:213], %4, %5, %6 offen\n\t"
                       "s_waitcnt lgkmcnt(0)"
                       : "+{v[224:255]}"(S), "+{v[208:223]}"(P), "=&v"(sw) : "v"(acco), "v"(acco2), "s"(rs), "s"(o2), "s"(~63u), "v"(lds0), "n"(L_MISC + 96) : "memory");
          __builtin_amdgcn_sched_barrier(0);
          tpub = now32();
          lds_inc<L_MISC + 104 + 4 * (w & 1)>(lds0);
          asm volatile("buffer_atomic_umax %0, %1, %2, %3 offen" :: "v"(tpub | 1u), "v"(tso), "s"(rs), "s"(o2) : "memory");
          STAMP(10);
        }
      }
    }
    STAMP(8);
    ARRIVE(1);
    o2p = o2;
    PIN_S(o2p);
    if (w < 2) {
      if (__builtin_expect(__builtin_amdgcn_readfirstlane(sw[1]) <= (unsigned)s, 0)) {
        lds_spin<L_MISC + 100, 0>(lds0, (unsigned)s + 1u);
        sw[0] = __hip_atomic_load((unsigned*)(misc + 24), __ATOMIC_RELAXED, __HIP_MEMORY_SCOPE_WORKGROUP);
      }
    } else {
      u32x4 q;
      asm volatile("1:\n\tds_read_b128 %0, %1 offset:%3\n\t.if %4\n\ts_sleep 1\n\t.endif\n\ts_waitcnt lgkmcnt(0)\n\tv_min3_u32 v201, v201, v202, v203\n\tv_cmp_ge_u32 vcc, %2, v201\n\ts_cbranch_vccnz 1b"
                   : "=&{v[200:203]}"(q) : "v"(lds0), "s"(s), "n"(L_MISC + 96), "n"(SP_SL) : "vcc", "memory");
      __builtin_amdgcn_s_setprio(3);
      tpub = now32();
      sw[0] = q[0];
    }
    STAMP(9);
    static_assert(F3C == 0 && F2C == 0 && F0C == 0, "nothing behind the atomics");
    LOGT(0, tpub); LOGT(1, tb2_);
    TSTAMP(1, tpub);
    ARRIVE_OUT();
    const unsigned an = __builtin_amdgcn_readfirstlane(sw[0]);
    const int ahead = (int)(an - tpub);
    tw = tpub + (unsigned)(an == 0u || ahead > 1000 ? SCH_FIRST : imax(ahead, w < 2 ? SCH_DMIN : 0));
    offs = tw - an;
  }
  rows(NSEG - 1);
  STAMP_OUT();
  LOGP(4);
  static_assert(LAG3 == 2 && LAG4 == 3 && LAG5 == 6, "drain schedule");
  unsigned* const cntx = (unsigned*)(misc + 16);
  auto poll = [&](auto load, auto ready) {
    load();
    for (unsigned spins = 0; !ready() && !dead;) {
      if (++spins > SPIN_CAP) dead = true;
      asm volatile("" ::: "memory");
      load();
    }
  };
  auto mark = [&](int k) { if (w == 0) *(volatile unsigned*)(misc + k) = cyc32() | 1u; };
  auto after = [&](int k, unsigned d) { lds_wait((unsigned*)(misc + k), 1u); wait_cyc(__builtin_amdgcn_readfirstlane(*(volatile unsigned*)(misc + k)) + d); };
  const int qt = w >> 1;
  auto t4 = [&](int sl) { poll([&]() { pv.load(rs, ck, sl, gp, qt, lane); }, [&]() { return pv.ready(); }); pv.run(lds, rs, sl, gp, m, qt, lane, true); };
  auto t5 = [&](int sl, int part) { poll([&]() { ot.load(a, rs, sl, b, part); }, [&]() { return ot.ready(b); }); ot.run(lane); ot.store(a, sl, b, lane); };
  BAR();
  const unsigned tp1 = cyc32();
  if (w == 2) {
    TokTask tb = tk;
    tb.load(rs, ts_slot(ck, NSEG - 3)); tk.load(rs, ts_slot(ck, NSEG - 2));
    tb.run(j, lane, SelLo); tb.store(rs, pt_slot(ck, NSEG - 3));
  }
  if (w == 3) { kq_get(NSEG - 1); t2_mem(qk_frag(lds + qo4, lds + qo2, lds + qo2b), qx[0], qx[1], rs, ts_slot(ck, NSEG - 1), h, j, invv * KEXP, lane); }
  if (w == 0) tk.init(h, j, lane);
  LOGD(0);
  if (w == 0) {
    ot.init(b, lane);
    wait_cyc(tp1 + END_E1);
    poll([&]() { tk.load(rs, ts_slot(ck, NSEG - 1)); }, [&]() { return tk.ready(); });
    LOGD(1);
  }
  if (!(w & 1)) {
    tk.run(j, lane, SelLo); tk.store(rs, pt_slot(ck, NSEG - 1 - qt));
    mark(14);
    LOGD(2);
    if (w == 2) ot.init(b, lane);
    ot.load(a, rs, NSEG - 4 + w, b, 2);
    lds_wait(cnt2, 4u * NSEG + 2u);
    after(14, 600); wait_cyc(tp1 + END_E2);
    t4(NSEG - 1);
    mark(15);
    LOGD(3);
  } else {
    ot.init(b, lane);
#pragma unroll 1
    for (int i = 0; i < 3; ++i) {
      wait_cyc(tp1 + (i == 0 ? END_E60 : (i == 1 ? END_E61 : END_E62)));
      t4(NSEG - 4 + i);
      if (i == 1 || (i == 2 && w == 1)) t5(i == 1 ? NSEG - 7 + qt : NSEG - 5, 3);
    }
    ot.load(a, rs, NSEG - 4 + w, b, 2);
    LOGP(5);
  }
  after(15, 600); wait_cyc(tp1 + END_E3);
  t5(NSEG - 4 + w, 1);
  LOGD(4);
  VM_DRAIN();
  if (w != 0) lds_inc<L_MISC + 64>(lds0);
  else {
    lds_wait(cntx, 3u);
    LOGD(5);
    if (lane == 0) __hip_atomic_fetch_add((gu64*)((GAS char*)(a.ws + o2) + acc_off(m * 4, 0)), (u64)1, RLX_AGENT);
    wait_until(now32() + SCH_DRAIN);
    wait_cyc(tp1 + END_E4);
    unsigned x;
    poll([&]() { x = __builtin_amdgcn_raw_buffer_load_b32(rs, acc_off(c * 4, 0), o2, 16); }, [&]() { return wave_all((x & 16u) != 0u); });
    lds_inc<L_MISC + 68>(lds0);
    LOGD(6);
  }
  LOGP(8);
}

__device__ __forceinline__ void proj_front(const RecArgs& a, char* lds, const __amdgpu_buffer_rsrc_t rs, const int v) {
  const int lane = __builtin_amdgcn_workitem_id_x() & 63, b = __builtin_amdgcn_workgroup_id_x(), c = lane & 15, g = lane >> 4;
  const int rb = (b & 7) + 8 * (b >> 5), cq = (b >> 3) & 3;
  const unsigned lds0 = (unsigned)(uintptr_t)(LAS char*)lds;
  unsigned* const cnt = (unsigned*)(lds + L_MISC) + 20;
  LOGF2(0);
  float* const red = (float*)(lds + L_RED1);
  float* const fin = red + 640;
  const f32x4 Z4 = {0.f, 0.f, 0.f, 0.f};
#pragma unroll
  for (int i = 0; i < 8; ++i) x_fetch((const u16*)(a.ws + OFF_OB) + (size_t)rb * 32 * DM, lds0, 0, 8 * v + i, lane);
  bf16x8 W[2][20];
#pragma unroll
  for (int k = 0; k < 40; ++k) W[k & 1][k >> 1] = *(const bf16x8*)(a.ws + OFF_WOBT + ((size_t)(cq * 10 + 2 * v + (k & 1)) * 20 + (k >> 1)) * 1024 + lane * 16);
  const unsigned fo = (unsigned)(rb * 40960 + (cq * 5 + v) * 1024 + ((g >> 1) * 16 + c) * 16), f8 = fo + (g & 1) * 8, f16 = fo + (g & 1) * 20480;
  u32x2 xo[2][2];
#pragma unroll
  for (int k = 0; k < 4; ++k)
    xo[k >> 1][k & 1] = __builtin_bit_cast(u32x2, __hip_atomic_load((const gu64*)((const char*)a.xnew + f8 + (k >> 1) * 512 + (k & 1) * 20480), RLX_AGENT));
  asm volatile("s_waitcnt vmcnt(44)" ::: "memory");
  lds_inc<L_MISC + 80>(lds0);
  lds_wait(cnt, 5u);
  LOGF2(1);
  f32x4 acc[2][2] = {{Z4, Z4}, {Z4, Z4}};
#pragma unroll
  for (int ks = 0; ks < 20; ++ks)
#pragma unroll
    for (int t = 0; t < 2; ++t) {
      const bf16x8 x = *(const bf16x8*)(lds + L_XN + (t * 20 + ks) * 1024 + lane * 16);
#pragma unroll
      for (int i = 0; i < 2; ++i) acc[i][t] = mfma32(W[i][ks], x, acc[i][t]);
    }
  u32x2 xp[2][2];
  PIN_V(acc[1][1]);
  LOGF2(2);
#pragma unroll
  for (int k = 0; k < 4; ++k) {
    const int i = k >> 1, t = k & 1;
#pragma unroll
    for (int q = 0; q < 2; ++q) {
      const unsigned r = pack2(acc[i][t][2 * q], acc[i][t][2 * q + 1]), x = xo[i][t][q];
      xp[i][t][q] = pack2(u2f(x << 16) + u2f(r << 16), u2f(x & 0xffff0000u) + u2f(r & 0xffff0000u));
      acc[i][t][2 * q] = u2f(xp[i][t][q] << 16); acc[i][t][2 * q + 1] = u2f(xp[i][t][q] & 0xffff0000u);
    }
    const float ml = x32sum(x16sum((acc[i][t][0] + acc[i][t][1]) + (acc[i][t][2] + acc[i][t][3]))) * (1.0f / 16.0f);
    float q = 0.f;
#pragma unroll
    for (int r = 0; r < 4; ++r) { const float d = acc[i][t][r] - ml; q += d * d; }
    q = x32sum(x16sum(q));
    if (g == 0) { red[(2 * v + i) * 32 + t * 16 + c] = ml; red[320 + (2 * v + i) * 32 + t * 16 + c] = q; }
  }
  lds_inc<L_MISC + 80>(lds0);
  if (v == 0) {
    lds_wait(cnt, 10u);
    LOGF2(3);
    float mw[10], ml = 0.f, m2 = 0.f;
#pragma unroll
    for (int i = 0; i < 10; ++i) { mw[i] = red[i * 32 + (lane & 31)]; ml += mw[i]; m2 += red[320 + i * 32 + (lane & 31)]; }
    ml *= 0.1f;
#pragma unroll
    for (int i = 0; i < 10; ++i) m2 += 16.0f * (mw[i] - ml) * (mw[i] - ml);
    const unsigned xs = (unsigned)OFF_LNX + rb * 1024;
    if (lane < 32) SC1_STORE8S(((u32x2){f2u(ml), nz(f2u(m2))}), (cq * 32 + lane) * 8, xs);
    wait_cyc(cyc32() + FR_D);
    u32x2 x0, x1;
    for (unsigned spins = 0; spins < SPIN_CAP; ++spins) {
      x0 = __builtin_amdgcn_raw_buffer_load_b64(rs, lane * 8, xs, 16); x1 = __builtin_amdgcn_raw_buffer_load_b64(rs, 512 + lane * 8, xs, 16);
      if (wave_all(x0[1] != 0u && x1[1] != 0u)) break;
      asm volatile("" ::: "memory");
    }
    const float m0 = u2f(x0[0]), m1 = u2f(x1[0]);
    const float mean = x32sum(m0 + m1) * 0.25f;
    const float M2 = x32sum((u2f(x0[1]) + u2f(x1[1])) + 160.0f * ((m0 - mean) * (m0 - mean) + (m1 - mean) * (m1 - mean)));
    const float mb = rbf(mean), den = rbf(__builtin_sqrtf(rbf(rbf(M2 * (1.0f / DM)) + 1e-5f)));
    if (lane < 32) {
      fin[lane] = mb; fin[32 + lane] = 1.0f / den;
      if (cq == 0) __hip_atomic_store((GAS unsigned*)(a.ws + OFF_XNRM) + (rb * 32 + lane) * 4, f2u(1.02f * (M2 + DM * (mean - mb) * (mean - mb)) / (den * den)), RLX_AGENT);
    }
    lds_inc<L_MISC + 84>(lds0);
  }
  lds_wait(cnt + 1, 1u);
  LOGF2(4);
  const __amdgpu_buffer_rsrc_t rx = __builtin_amdgcn_make_buffer_rsrc(a.ws + OFF_XN, 0, (int)(OFF_QB - OFF_XN), 0x00020000);
  auto put = [&](const u32x2 p0, const u32x2 p1, unsigned so) {
    const auto r0 = __builtin_amdgcn_permlane16_swap(p0[0], p1[0], false, false), r1 = __builtin_amdgcn_permlane16_swap(p0[1], p1[1], false, false);
    __builtin_amdgcn_raw_buffer_store_b128((u32x4){r0[0], r1[0], r0[1], r1[1]}, rx, f16, so, 16);
  };
#pragma unroll
  for (int i = 0; i < 2; ++i) {
    u32x2 y[2];
#pragma unroll
    for (int t = 0; t < 2; ++t) {
      const float mb = fin[t * 16 + c], rden = fin[32 + t * 16 + c];
      y[t] = (u32x2){pack2(rbf(acc[i][t][0] - mb) * rden, rbf(acc[i][t][1] - mb) * rden), pack2(rbf(acc[i][t][2] - mb) * rden, rbf(acc[i][t][3] - mb) * rden)};
    }
    put(y[0], y[1], i * 512);
  }
  VM_DRAIN();
  lds_inc<L_MISC + 80>(lds0);
#pragma unroll
  for (int i = 0; i < 2; ++i) put(xp[i][0], xp[i][1], (unsigned)(OFF_XB - OFF_XN) + i * 512);
  if (v == 0) {
    lds_wait(cnt, 15u);
    if (rb < 4 && lane < 8) __hip_atomic_fetch_add((GAS unsigned*)(a.ws + OFF_RDY) + lane * 64, 1u, RLX_AGENT);
  }
  LOGF2(5);
}

__global__ __launch_bounds__(512, 2) void rec_kernel(RecArgs a) {
  __shared__ __attribute__((aligned(16))) char lds[L_BYTES];
  const int tid = __builtin_amdgcn_workitem_id_x(), wid = __builtin_amdgcn_readfirstlane(tid >> 6), w = wid & 3;
  const __amdgpu_buffer_rsrc_t rs = __builtin_amdgcn_make_buffer_rsrc(a.ws, 0, (int)COMM_BYTES, 0x00020000);
  if (tid < 32) ((float*)(lds + L_MISC))[tid] = (tid == 4 || tid == 5) ? 1.0f : 0.0f;
  BAR();
  if (a.layer && wid < 5) proj_front(a, lds, rs, wid);
  if (wid < 4) {
    if (w == 0) rec_waves<0>(a, lds, rs);
    else if (w == 1) rec_waves<1>(a, lds, rs);
    else if (w == 2) rec_waves<2>(a, lds, rs);
    else rec_waves<3>(a, lds, rs);
  } else {
    if (w == 0) token_waves<0>(a, lds, rs);
    else if (w == 1) token_waves<1>(a, lds, rs);
    else if (w == 2) token_waves<2>(a, lds, rs);
    else token_waves<3>(a, lds, rs);
    qkv_tail(a, lds, w);
  }
}

#if defined(__HIP_DEVICE_COMPILE__)
#pragma clang attribute pop
#endif

constexpr int KQ = S_ / 4;
#define ATT_P1 6
#define ATT_P2 10
#define ATT_P3 14
#define ATT_THR 6.0f
#define ATT_NQ 4
#define ATT_LMAX 1.0e12f
#if defined(__HIP_DEVICE_COMPILE__)
#pragma clang attribute push(__attribute__((target("no-packed-fp32-ops"))), apply_to = function)
#endif
__global__ __launch_bounds__(256 * ATT_NQ, 4) void attb_kernel(const u16* __restrict__ Qp, const u16* __restrict__ KV, u16* __restrict__ O) {
  __shared__ float mrg_[ATT_NQ][3][2][13][64];
  const int tid = __builtin_amdgcn_workitem_id_x(), bid = __builtin_amdgcn_workgroup_id_x();
  __shared__ unsigned sink[64];
  own_code<4800>(tid, sink);
  const int lane = tid & 63, wid = __builtin_amdgcn_readfirstlane(tid >> 6), kh = wid / ATT_NQ, r = lane & 31, hh = lane >> 5;
  const int hd = (bid & 7) * 4 + ((bid >> 3) & 3), qb = (bid >> 5) * ATT_NQ + wid % ATT_NQ;
  float (*mrg)[2][13][64] = mrg_[wid % ATT_NQ];
  const int q0 = qb * 64;
  bf16x8 qa[2]; u32x4 qb2[2];
#pragma unroll
  for (int qt = 0; qt < 2; ++qt) {
    { const u32x2* qp = (const u32x2*)(Qp + ((size_t)hd * S_ + q0 + qt * 32 + r) * QST + 8 * hh); const u32x2 q0_ = qp[0], q1_ = qp[1]; qa[qt] = mk8(q0_[0], q0_[1], q1_[0], q1_[1]); }
    const u32x2 t = *(const u32x2*)(Qp + ((size_t)hd * S_ + q0 + qt * 32 + r) * QST + 16);
    qb2[qt] = (u32x4){hh ? 0u : t[0], hh ? 0u : t[1], 0u, 0u};
  }
  const __amdgpu_buffer_rsrc_t rk = __builtin_amdgcn_make_buffer_rsrc((void*)(KV + ((size_t)hd * 64 + kh * 16) * (KVT / 2)), 0, 16 * KVT, 0x00020000);
  unsigned ok1 = lane * 16, ok2 = hh ? 1u << 30 : 1024 + r * 8, ov = r > 20 ? 1u << 30 : 1280 + (hh * 21 + r) * 16;
  f32x16 o[2];
  float m[2];
  const f32x16 Z16 = {0.f, 0.f, 0.f, 0.f, 0.f, 0.f, 0.f, 0.f, 0.f, 0.f, 0.f, 0.f, 0.f, 0.f, 0.f, 0.f};
  struct KvT { u32x4 k1, v0, v1; u32x2 k2; };
  auto fetch = [&](int t) {
    KvT x;
    __builtin_amdgcn_sched_barrier(0);
    x.k1 = __builtin_amdgcn_raw_buffer_load_b128(rk, ok1, t * KVT, 0); x.k2 = __builtin_amdgcn_raw_buffer_load_b64(rk, ok2, t * KVT, 0);
    x.v0 = __builtin_amdgcn_raw_buffer_load_b128(rk, ov, t * KVT, 0); x.v1 = __builtin_amdgcn_raw_buffer_load_b128(rk, ov + 672, t * KVT, 0);
    __builtin_amdgcn_sched_barrier(0);
    return x;
  };
  auto tile = [&](const KvT& x, auto MODE) {
    const bf16x8 ckb = mk8(x.k2[0], x.k2[1], 0x3f803f80u, 0x00003f80u);
    f32x16 sa[2];
#pragma unroll
    for (int qt = 0; qt < 2; ++qt) {
      sa[qt] = __builtin_amdgcn_mfma_f32_32x32x16_bf16(as8(x.k1), qa[qt], Z16, 0, 0, 0);
      sa[qt] = __builtin_amdgcn_mfma_f32_32x32x16_bf16(ckb, as8(qb2[qt]), sa[qt], 0, 0, 0);
    }
#pragma unroll
    for (int qt = 0; qt < 2; ++qt) {
      if (MODE != 0) {
        float mx = fmx(fmx(sa[qt][0], sa[qt][1]), sa[qt][2]);
#pragma unroll
        for (int i = 3; i < 15; i += 2) mx = fmx(fmx(mx, sa[qt][i]), sa[qt][i + 1]);
        mx = fmx(mx, sa[qt][15]);
        if (MODE == 1 || !wave_all(mx <= ATT_THR)) {
          mx = x32mx(mx);
          if (MODE != 1) mx = fmx(mx, 0.f);
          const float mn = m[qt] + mx;
          const unsigned h1 = f2bf(mn); const float r1 = mn - bf2f(h1);
          const unsigned h2 = f2bf(r1); const float r2 = r1 - bf2f(h2);
          const unsigned h3 = f2bf(r2);
          const float me = (bf2f(h1) + bf2f(h2)) + bf2f(h3);
          const float dm = me - m[qt];
          m[qt] = me;
          if (hh == 0) { qb2[qt][2] = (h1 | (h2 << 16)) ^ 0x80008000u; qb2[qt][3] = h3 ^ 0x8000u; }
          const float alpha = __builtin_amdgcn_exp2f(-dm);
#pragma unroll
          for (int i = 0; i < 16; ++i) { if (MODE != 1) o[qt][i] *= alpha; sa[qt][i] -= dm; }
        }
      }
      float p[16];
#pragma unroll
      for (int i = 0; i < 16; ++i) p[i] = __builtin_amdgcn_exp2f(sa[qt][i]);
      bf16x8 p0 = mk8(pack2(p[0], p[1]), pack2(p[2], p[3]), pack2(p[4], p[5]), pack2(p[6], p[7]));
      bf16x8 p1 = mk8(pack2(p[8], p[9]), pack2(p[10], p[11]), pack2(p[12], p[13]), pack2(p[14], p[15]));
      o[qt] = __builtin_amdgcn_mfma_f32_32x32x16_bf16(as8(x.v0), p0, MODE == 1 ? Z16 : o[qt], 0, 0, 0);
      o[qt] = __builtin_amdgcn_mfma_f32_32x32x16_bf16(as8(x.v1), p1, o[qt], 0, 0, 0);
    }
  };
  typedef std::integral_constant<int, 0> I0; typedef std::integral_constant<int, 1> I1; typedef std::integral_constant<int, 2> I2;
  auto pass = [&](auto MODE) {
    asm volatile("" : "+v"(ok1), "+v"(ok2), "+v"(ov) :: "memory");
    m[0] = m[1] = 0.f;
    qb2[0][2] = qb2[0][3] = qb2[1][2] = qb2[1][3] = 0u;
    KvT xa = fetch(0), xb = fetch(1);
    tile(xa, I1{});
    xa = fetch(2);
    tile(xb, MODE);
#pragma unroll 1
    for (int t = 2; t < KQ / 32; t += 2) {
      if (t == ATT_P1) __builtin_amdgcn_s_setprio(2);
      if (t == ATT_P2) __builtin_amdgcn_s_setprio(1);
      if (t == ATT_P3) __builtin_amdgcn_s_setprio(0);
      xb = fetch(t + 1);
      tile(xa, MODE);
      xa = fetch(t + 2);
      tile(xb, MODE);
    }
  };
  if (ATT_P1) __builtin_amdgcn_s_setprio(3);
  pass(I0{});
  {
    float big = 0.f;
#pragma unroll
    for (int qt = 0; qt < 2; ++qt) {
      big = fmx(big, hh ? o[qt][8] * (3.0e38f / ATT_LMAX) : __builtin_fabsf(o[qt][8]));
#pragma unroll
      for (int i = 0; i < 12; ++i) if (i != 8) big = fmx(big, (i > 8 && hh) ? 0.f : __builtin_fabsf(o[qt][i]));
    }
    if (__builtin_expect(!wave_all(big <= 3.0e38f), 0)) pass(I2{});
  }
  if (kh > 0) {
#pragma unroll
    for (int qt = 0; qt < 2; ++qt) {
#pragma unroll
      for (int i = 0; i < 12; ++i) mrg[kh - 1][qt][i][lane] = o[qt][i];
      mrg[kh - 1][qt][12][lane] = m[qt];
    }
  }
  asm volatile("s_waitcnt lgkmcnt(0)\n\ts_barrier" ::: "memory");
  if (kh == 0) {
#pragma unroll
    for (int qt = 0; qt < 2; ++qt) {
      float m1 = mrg[0][qt][12][lane], m2 = mrg[1][qt][12][lane], m3 = mrg[2][qt][12][lane];
      float mn = fmx(fmx(m[qt], m1), fmx(m2, m3));
      float a0 = __builtin_amdgcn_exp2f(m[qt] - mn), a1 = __builtin_amdgcn_exp2f(m1 - mn);
      float a2 = __builtin_amdgcn_exp2f(m2 - mn), a3 = __builtin_amdgcn_exp2f(m3 - mn);
      float v[12];
#pragma unroll
      for (int i = 0; i < 12; ++i) v[i] = (o[qt][i] * a0 + mrg[0][qt][i][lane] * a1) + (mrg[1][qt][i][lane] * a2 + mrg[2][qt][i][lane] * a3);
      auto lr = __builtin_amdgcn_permlane32_swap(f2u(v[8]), f2u(v[8]), false, false);
      float inv = __builtin_amdgcn_rcpf(u2f(lr[1]));
      const int row = q0 + qt * 32 + r;
      auto oaddr = [&](int k) { return O + (((size_t)(row >> 4) * 20 + (k >> 5)) * 64 + ((k & 31) >> 3) * 16 + (row & 15)) * 8 + (k & 7); };
      const int k0 = hd * BD + 4 * hh;
      *(u32x2*)oaddr(k0) = (u32x2){pack2(v[0] * inv, v[1] * inv), pack2(v[2] * inv, v[3] * inv)};
      *(u32x2*)oaddr(k0 + 8) = (u32x2){pack2(v[4] * inv, v[5] * inv), pack2(v[6] * inv, v[7] * inv)};
      if (hh == 0) *(u32x2*)oaddr(k0 + 16) = (u32x2){pack2(v[8] * inv, v[9] * inv), pack2(v[10] * inv, v[11] * inv)};
    }
  }
}
#if defined(__HIP_DEVICE_COMPILE__)
#pragma clang attribute pop
#endif

__global__ __launch_bounds__(640, 3) void proj_kernel(const u16* __restrict__ Of, const u16* __restrict__ Wf, const u16* __restrict__ xold,
                                                      u16* __restrict__ xnew) {
  __shared__ __attribute__((aligned(16))) char ob[40960];
  const int tid = threadIdx.x, lane = tid & 63, w = __builtin_amdgcn_readfirstlane(tid >> 6), g = lane >> 4, c = lane & 15;
  __shared__ unsigned sink[64];
  own_code<1500>(tid, sink);
  const int rb = (blockIdx.x & 7) + 8 * (blockIdx.x >> 5), cb = (blockIdx.x >> 3) & 3;
  const int col = cb * 160 + w * 16 + 4 * g;
  const u16* wp = Wf + (size_t)(cb * 10 + w) * 20 * 512 + lane * 8;
  u32x4 st[4];
#pragma unroll
  for (int i = 0; i < 4; ++i) st[i] = *(const u32x4*)(Of + (size_t)rb * 40 * 512 + (i * 640 + tid) * 8);
  bf16x8 wf[20];
#pragma unroll
  for (int ks = 0; ks < 20; ++ks) wf[ks] = *(const bf16x8*)(wp + (size_t)ks * 512);
  u32x2 xo[2];
#pragma unroll
  for (int t = 0; t < 2; ++t) xo[t] = *(const u32x2*)(xold + (((size_t)(rb * 2 + t) * 20 + (col >> 5)) * 64 + ((col & 31) >> 3) * 16 + c) * 8 + (col & 7));
#pragma unroll
  for (int i = 0; i < 4; ++i) *(u32x4*)(ob + (i * 640 + tid) * 16) = st[i];
  BAR();
  f32x4 acc[2] = {{0.f, 0.f, 0.f, 0.f}, {0.f, 0.f, 0.f, 0.f}};
#pragma unroll
  for (int ks = 0; ks < 20; ++ks)
#pragma unroll
    for (int t = 0; t < 2; ++t) acc[t] = mfma32(wf[ks], *(const bf16x8*)(ob + (t * 20 + ks) * 1024 + lane * 16), acc[t]);
#pragma unroll
  for (int t = 0; t < 2; ++t) {
    const unsigned r01 = pack2(acc[t][0], acc[t][1]), r23 = pack2(acc[t][2], acc[t][3]);
    const unsigned p01 = pack2(u2f(xo[t][0] << 16) + u2f(r01 << 16), u2f(xo[t][0] & 0xffff0000u) + u2f(r01 & 0xffff0000u));
    const unsigned p23 = pack2(u2f(xo[t][1] << 16) + u2f(r23 << 16), u2f(xo[t][1] & 0xffff0000u) + u2f(r23 & 0xffff0000u));
    *(u32x2*)(xnew + (size_t)(rb * 32 + t * 16 + c) * DM + col) = (u32x2){p01, p23};
  }
}

extern "C" void kernel_launch(void* const* d_in, const int* in_sizes, int n_in, void* d_out, int out_size, void* d_ws,
                              size_t ws_size, hipStream_t stream) {
  const u16* A_x = (const u16*)d_in[0];
  const u16* A_mem0 = (const u16*)d_in[1];
  const u16* A_Wq = (const u16*)d_in[2];
  const u16* A_Wk = (const u16*)d_in[3];
  const u16* A_Wv = (const u16*)d_in[4];
  const u16* A_Wo = (const u16*)d_in[5];
  const u16* B_wqkv = (const u16*)d_in[8];
  const u16* B_wo = (const u16*)d_in[9];
  char* ws = (char*)(((uintptr_t)d_ws + ((size_t)WS_OFF << 20) + WS_AL - 1) & ~(uintptr_t)(WS_AL - 1));
  u16* xa = (u16*)(ws + OFF_XA); u16* Ob = (u16*)(ws + OFF_OB);

  prologue_kernel<<<256, 640, 0, stream>>>(ProArgs{A_x, A_Wq, A_Wk, A_Wv, A_Wo, B_wqkv, B_wo, ws});
  for (int layer = 0; layer < 2; ++layer) {
    rec_kernel<<<256, 512, 0, stream>>>(RecArgs{A_mem0, A_Wq, A_Wk, (const u16*)(ws + OFF_XB), xa, ws, (unsigned)layer, 0u});
    attb_kernel<<<BH * 32 / ATT_NQ, 256 * ATT_NQ, 0, stream>>>((const u16*)(ws + OFF_QB), (const u16*)(ws + OFF_KV), Ob);
  }
  proj_kernel<<<256, 640, 0, stream>>>(Ob, (const u16*)(ws + OFF_WOBT), xa, (u16*)d_out);
}
```

```cpp
#include <hip/hip_runtime.h>
#include <hip/hip_bf16.h>
#include <stdint.h>
#include <type_traits>

typedef unsigned short u16;
typedef unsigned long long u64;
typedef __attribute__((ext_vector_type(8))) short bf16x8;
typedef __attribute__((ext_vector_type(16))) short bf16x16;
typedef __attribute__((ext_vector_type(4))) float f32x4;
typedef __attribute__((ext_vector_type(2))) float f32x2;
typedef __attribute__((ext_vector_type(16))) float f32x16;
typedef __attribute__((ext_vector_type(8))) float f32x8;
typedef __attribute__((ext_vector_type(4))) unsigned u32x4;
typedef __attribute__((ext_vector_type(2))) unsigned u32x2;
typedef __attribute__((ext_vector_type(3))) unsigned u32x3;
typedef __attribute__((ext_vector_type(8))) unsigned u32x8;
typedef __attribute__((ext_vector_type(16))) unsigned u32x16;
typedef __attribute__((ext_vector_type(32))) unsigned u32x32;
#define GAS __attribute__((address_space(1)))
#define LAS __attribute__((address_space(3)))
typedef GAS unsigned long long gu64;
#define BAR() do { __builtin_amdgcn_s_waitcnt(0xC07F); asm volatile("s_waitcnt lgkmcnt(0)\n\ts_barrier" ::: "memory"); } while (0)

constexpr int S_ = 2048, DM = 640;
constexpr int NH = 32, DH = 192, HD = NH * DH;
constexpr int MEM = 8, SEG = 32, NSEG = S_ / SEG;
constexpr int BH = 32, BD = 20;

constexpr size_t al256(size_t x) { return (x + 255) & ~(size_t)255; }
constexpr int LAG3 = 2, LAG4 = 3, LAG5 = 6;
#define SR_ 8
constexpr int SR = SR_;
constexpr unsigned SCT_STEP = 131072, OFF_SCM = SR * SCT_STEP, SCM_STEP = 32768;
constexpr int ACC_NB = 192;
constexpr size_t ACC_STEP = 65536;
constexpr unsigned ACC_TS = ACC_NB * 256;
constexpr size_t ACC_LAYER = SR * ACC_STEP;
constexpr size_t OFF_ACC = 3 * 524288;
static_assert(SR == 8, "regions");
__device__ __forceinline__ unsigned ts_piece(int i) { return ACC_TS + (unsigned)(i < 128 ? (i >> 1) * 256 + (i & 1) * 16 : 128 + (i & 1) * 8192); }
constexpr size_t OFF_LNX = OFF_SCM + SR * SCM_STEP;
constexpr size_t OFF_RDY = OFF_LNX + 65536;
constexpr size_t PG = 2097152;
#define RING_ 4
constexpr int RING = RING_;
constexpr size_t TS_SLOT = (size_t)NH * 8 * 2560, PT_SLOT = (size_t)NH * 2560, OP_SLOT = (size_t)256 * 2560;
__device__ __forceinline__ unsigned ts_tok(int h, int j) { return (unsigned)((h & 15) * 32768 + (j >> 2) * 16384 + (j & 3) * 2048); }
__device__ __forceinline__ unsigned ts_mem(int h, int j) { return (unsigned)(524288 + ((h & 15) >> 1) * 16384 + (h & 1) * 4096 + j * 512); }
__device__ __forceinline__ unsigned pt_tok(int h) { return (unsigned)(((h & 15) >> 2) * 16384 + (h & 3) * 2048); }
__device__ __forceinline__ unsigned pt_mem(int h) { return (unsigned)(65536 + (h & 15) * 512); }
constexpr size_t OFF_X = 2 * PG + TS_SLOT + 3 * PT_SLOT, X_BYTES = 7 * OP_SLOT + 5 * PT_SLOT;
constexpr size_t OFF_OP = OFF_X + TS_SLOT + 5 * PT_SLOT;
constexpr size_t COMM_BYTES = OFF_X + X_BYTES + RING * OP_SLOT;
__device__ __forceinline__ unsigned near_half(unsigned ck, unsigned o) { return o + ((ck >> (o >= 2 * PG ? 8 : 4)) & 8192u); }
__device__ __forceinline__ unsigned ts_slot(unsigned ck, int sl) {
  return near_half(ck, sl == NSEG - 1 ? (unsigned)OFF_X : ((sl & 3) == 3 ? (unsigned)(2 * PG) : (unsigned)PG + (unsigned)(sl & 3) * (unsigned)TS_SLOT));
}
__device__ __forceinline__ unsigned pt_slot(unsigned ck, int sl) {
  return near_half(ck, sl >= NSEG - 5 ? (unsigned)(OFF_X + TS_SLOT) + (unsigned)(sl - (NSEG - 5)) * (unsigned)PT_SLOT
                                      : ((sl & 3) == 0 ? (unsigned)(PG + 3 * TS_SLOT) : (unsigned)(2 * PG + TS_SLOT - PT_SLOT) + (unsigned)(sl & 3) * (unsigned)PT_SLOT));
}
__device__ __forceinline__ unsigned op_slot(int sl) { return (unsigned)OFF_OP + (unsigned)(sl >= NSEG - 6 ? sl - (NSEG - 6) : 6 + sl % RING) * (unsigned)OP_SLOT; }
static_assert(RING == 4 && OFF_OP <= 3 * PG && OFF_RDY + 2048 <= OFF_ACC && OFF_ACC + ACC_LAYER == PG && TS_SLOT % 16384 == 0 && PT_SLOT % 16384 == 0, "pages");
static_assert(TS_SLOT == OP_SLOT && RING >= 4 && NSEG % SR == 0 && SR >= 5 && X_BYTES % 4096 == 0 && X_BYTES <= 256 * 20 * 1024, "slots");
constexpr size_t OFF_WB = al256(COMM_BYTES);
constexpr size_t OFF_BNRM = OFF_WB + (size_t)256 * 100 * 1024;
constexpr size_t OFF_LAT = OFF_BNRM + 1024;
constexpr size_t OFF_XNRM = OFF_LAT + 2048;
constexpr size_t OFF_WQKVBT = OFF_XNRM + al256(S_ * 16);
constexpr size_t OFF_WOBT = OFF_WQKVBT + al256((size_t)3 * DM * DM * 2);
constexpr size_t OFF_XN = OFF_WOBT + al256((size_t)DM * DM * 2);
constexpr int QST = BD;
constexpr size_t OFF_XA = OFF_XN + al256((size_t)S_ * DM * 2);
constexpr size_t OFF_XB = OFF_XA + al256((size_t)S_ * DM * 2);
constexpr size_t OFF_QB = OFF_XB + al256((size_t)S_ * DM * 2);
constexpr int KVT = 2624;
constexpr size_t OFF_KV = OFF_QB + al256((size_t)BH * S_ * QST * 2);
constexpr size_t OFF_OB = OFF_KV + al256((size_t)BH * 64 * KVT);
constexpr size_t OFF_DBG = OFF_OB + al256((size_t)S_ * DM * 2);
constexpr size_t WS_TOTAL = OFF_DBG + (size_t)2 * 64 * 8 * DM * 2;
#define WS_AL 2097152
#define WS_OFF 0
static_assert(WS_TOTAL + WS_AL + ((size_t)WS_OFF << 20) <= ((size_t)256 << 20), "workspace budget");

#if defined(__HIP_DEVICE_COMPILE__)
#pragma clang attribute push(__attribute__((target("no-packed-fp32-ops"))), apply_to = function)
#endif
__device__ __forceinline__ float u2f(unsigned v) { return __builtin_bit_cast(float, v); }
__device__ __forceinline__ unsigned f2u(float v) { return __builtin_bit_cast(unsigned, v); }
__device__ __forceinline__ float bf2f(unsigned v) { return __builtin_bit_cast(float, v << 16); }
__device__ __forceinline__ unsigned f2bf(float f) { __bf16 b = (__bf16)f; return (unsigned)__builtin_bit_cast(u16, b); }
__device__ __forceinline__ float rbf(float f) { return bf2f(f2bf(f)); }
typedef __attribute__((ext_vector_type(2))) __bf16 hbf16x2;
__device__ __forceinline__ unsigned pack2(float lo, float hi) { f32x2 v = {lo, hi}; hbf16x2 b = __builtin_convertvector(v, hbf16x2); return __builtin_bit_cast(unsigned, b); }
__device__ __forceinline__ f32x4 mfma32(bf16x8 a, bf16x8 b, f32x4 c) { return __builtin_amdgcn_mfma_f32_16x16x32_bf16(a, b, c, 0, 0, 0); }
typedef __attribute__((ext_vector_type(8))) __bf16 hbf16x8;
typedef __attribute__((ext_vector_type(16))) __bf16 hbf16x16;
__device__ __forceinline__ f32x4 smfma64(bf16x8 a, bf16x16 b, f32x4 c, int idx) {
  return __builtin_amdgcn_smfmac_f32_16x16x64_bf16(__builtin_bit_cast(hbf16x8, a), __builtin_bit_cast(hbf16x16, b), c, idx, 0, 0);
}
__device__ __forceinline__ bf16x16 cat16(bf16x8 a, bf16x8 b) { return __builtin_shufflevector(a, b, 0, 1, 2, 3, 4, 5, 6, 7, 8, 9, 10, 11, 12, 13, 14, 15); }
__device__ __forceinline__ bf16x8 half8(bf16x16 v, int hi) { return hi ? __builtin_shufflevector(v, v, 8, 9, 10, 11, 12, 13, 14, 15) : __builtin_shufflevector(v, v, 0, 1, 2, 3, 4, 5, 6, 7); }
__device__ __forceinline__ bf16x8 mk8(unsigned a, unsigned b, unsigned c, unsigned d) { u32x4 v = {a, b, c, d}; return __builtin_bit_cast(bf16x8, v); }
__device__ __forceinline__ unsigned dpp_ror8(unsigned v) { return (unsigned)__builtin_amdgcn_mov_dpp((int)v, 0x128, 0xf, 0xf, true); }
__device__ __forceinline__ float fmx(float a, float b) { return __builtin_elementwise_maximum(a, b); }

__device__ __forceinline__ int imax(int a, int b) { return a > b ? a : b; }
__device__ __forceinline__ int imin(int a, int b) { return a < b ? a : b; }
__device__ __forceinline__ unsigned umin(unsigned a, unsigned b) { return a < b ? a : b; }
__device__ __forceinline__ unsigned umax(unsigned a, unsigned b) { return a > b ? a : b; }
__device__ __forceinline__ bool wave_all(bool p) { return __builtin_amdgcn_ballot_w64(!p) == 0ull; }
__device__ __forceinline__ float x16mx(float x) { unsigned u = __builtin_bit_cast(unsigned, x); auto r = __builtin_amdgcn_permlane16_swap(u, u, false, false); return fmx(u2f(r[0]), u2f(r[1])); }
__device__ __forceinline__ float x32mx(float x) { unsigned u = __builtin_bit_cast(unsigned, x); auto r = __builtin_amdgcn_permlane32_swap(u, u, false, false); return fmx(u2f(r[0]), u2f(r[1])); }
__device__ __forceinline__ float x16sum(float x) { unsigned u = __builtin_bit_cast(unsigned, x); auto r = __builtin_amdgcn_permlane16_swap(u, u, false, false); return u2f(r[0]) + u2f(r[1]); }
__device__ __forceinline__ float x32sum(float x) { unsigned u = __builtin_bit_cast(unsigned, x); auto r = __builtin_amdgcn_permlane32_swap(u, u, false, false); return u2f(r[0]) + u2f(r[1]); }
template <int CTRL> __device__ __forceinline__ float dppf(float v) { return u2f((unsigned)__builtin_amdgcn_mov_dpp((int)f2u(v), CTRL, 0xf, 0xf, true)); }
#define OWN_PF 1
template <int BYTES> __device__ __forceinline__ void own_code(int tid, unsigned* sink) {
  if (!OWN_PF || __builtin_amdgcn_readfirstlane(tid >> 6) * 4096 >= BYTES) return;
  const char* pc; unsigned keep;
  asm volatile("s_getpc_b64 %0" : "=s"(pc));
  asm volatile("s_mov_b32 %0, m0\n\ts_mov_b32 m0, %3\n\ts_nop 0\n\tglobal_load_lds_dword %1, %2\n\ts_mov_b32 m0, %0"
               : "=&s"(keep) : "v"(tid * 64 < BYTES ? tid * 64 : 0), "s"(pc), "s"((unsigned)(uintptr_t)(LAS unsigned*)sink) : "memory");
}
constexpr int DPP_XOR1 = 0xB1, DPP_XOR2 = 0x4E, DPP_ROR4 = 0x124, DPP_ROR8 = 0x128;
#if defined(__HIP_DEVICE_COMPILE__)
#pragma clang attribute pop
#endif

typedef short v4i16_t __attribute__((ext_vector_type(4)));
__device__ __forceinline__ bf16x8 tr_frag(const char* lo_, const char* hi_) {
  const v4i16_t lo = __builtin_amdgcn_ds_read_tr16_b64_v4i16((LAS v4i16_t*)lo_), hi = __builtin_amdgcn_ds_read_tr16_b64_v4i16((LAS v4i16_t*)hi_);
  return (bf16x8){lo[0], lo[1], lo[2], lo[3], hi[0], hi[1], hi[2], hi[3]};
}
__device__ __forceinline__ int rec_wg(int gp, int m) { return (((gp & 1) * 16 + m) << 3) | (gp >> 1); }
__device__ __forceinline__ float wsum(float v) {
  v += dppf<DPP_XOR1>(v); v += dppf<DPP_XOR2>(v); v += dppf<DPP_ROR4>(v); v += dppf<DPP_ROR8>(v);
  return x32sum(x16sum(v));
}
__device__ __forceinline__ float ln_row(int lane, u32x4 (&xo)[2]) {
  const bool two = lane < 16;
  float v[16];
#pragma unroll
  for (int hf = 0; hf < 2; ++hf)
#pragma unroll
    for (int i = 0; i < 4; ++i) { const unsigned u = xo[hf][i]; v[hf * 8 + 2 * i] = bf2f(u & 0xffff); v[hf * 8 + 2 * i + 1] = bf2f(u >> 16); }
  float s0 = 0.f, s1 = 0.f;
#pragma unroll
  for (int i = 0; i < 8; ++i) { s0 += v[i]; s1 += v[8 + i]; }
  const float mean = wsum(two ? s0 + s1 : s0) * (1.0f / DM);
  s0 = s1 = 0.f;
#pragma unroll
  for (int i = 0; i < 8; ++i) { const float d0 = v[i] - mean, d1 = v[8 + i] - mean; s0 += d0 * d0; s1 += d1 * d1; }
  const float var = rbf(wsum(two ? s0 + s1 : s0) * (1.0f / DM));
  const float mb = rbf(mean);
  const float rden = 1.0f / rbf(sqrtf(rbf(var + 1e-5f)));
  float n2[2] = {0.f, 0.f};
#pragma unroll
  for (int hf = 0; hf < 2; ++hf) {
    float y[8];
#pragma unroll
    for (int i = 0; i < 8; ++i) { y[i] = rbf(rbf(v[hf * 8 + i] - mb) * rden); n2[hf] += y[i] * y[i]; }
    xo[hf] = (u32x4){pack2(y[0], y[1]), pack2(y[2], y[3]), pack2(y[4], y[5]), pack2(y[6], y[7])};
  }
  return wsum(two ? n2[0] + n2[1] : n2[0]);
}

#define PROT(k)
#define WARM_CODE 1
#define B_AUX 16
#define X_AUX 16
#define T_AUX 16
#define PF_POS 0
#define PRB_N0 3
#define PRB_N1 0
#define PRB_LOG(i, v)
#define PLD(p) __builtin_nontemporal_load((const u32x4*)(p))
struct ProArgs { const u16* x; const u16* wq; const u16* wk; const u16* wv; const u16* wo; const u16* wqkvb; const u16* wob; char* ws; };
__global__ __launch_bounds__(640, 3) void prologue_kernel(ProArgs a) {
  __shared__ __attribute__((aligned(16))) char img[192 * 320];
  __shared__ __attribute__((aligned(16))) char slots[10 * 6144];
  __shared__ float red[10][160];
  __shared__ unsigned latw[8];
  __shared__ u32x4 xst[2][640];
  const int b = blockIdx.x, tid = threadIdx.x, lane = tid & 63, v = __builtin_amdgcn_readfirstlane(tid >> 6), c = lane & 15, g = lane >> 4;
  const int h = (b & 7) * 4 + ((b >> 3) & 3), dh = (b >> 5) & 1, cq = b >> 6, ks = dh * 10 + v;
  char* const slot = slots + v * 6144;
  PROT(0);
  __shared__ unsigned sink[64];
  own_code<9600>(tid, sink);
  const unsigned zo = v < 8 ? (unsigned)(((b >> 6) * 512 + ((b & 7) * 2 + (v >> 2)) * 32 + ((b >> 3) & 7) * 4 + (v & 3)) * 1024) : (unsigned)(((v - 7) * 2048 + (b & 7) * 32 + ((b >> 3) & 7) * 4 + (b >> 6)) * 1024);
  __builtin_amdgcn_raw_buffer_store_b128((u32x4){0u, 0u, 0u, 0u}, __builtin_amdgcn_make_buffer_rsrc(a.ws, 0, (int)(3 * PG), 0x00020000), zo + lane * 16, 0, 16);
  __builtin_amdgcn_sched_barrier(0);
  u32x4 wa[2][6], wb[6], tr[4], xo[2];
  const int xrow = b * 8 + (v & 7);
  xo[0] = *(const u32x4*)(a.x + (size_t)xrow * DM + lane * 8);
  xo[1] = *(const u32x4*)(a.x + (size_t)xrow * DM + 512 + c * 8);
  const int job = imin(b + 256 * (9 - v), 800);
  const int jn = job < 600 ? job / 20 : (job % 200) / 20, jk = job % 20;
  {
    const u16* src = job < 600 ? a.wqkvb + (size_t)(32 * jk) * 3 * DM : a.wob + (size_t)(32 * jk) * DM;
#pragma unroll
    for (int i = 0; i < 4; ++i) tr[i] = *(const u32x4*)(src + (size_t)(i * 8 + (lane >> 3)) * (job < 600 ? 3 * DM : DM) + jn * 64 + (lane & 7) * 8);
  }
#pragma unroll
  for (int i = 0; i < 6; ++i) {
    const int idx = (v * 6 + i) * 64 + lane, row = idx / 20;
    wb[i] = PLD(a.wo + (size_t)(h * DH + row) * DM + cq * 160 + (idx - row * 20) * 8);
  }
#pragma unroll
  for (int i = 0; i < 6; ++i) {
    const int idx = i * 64 + lane, r = idx / 24;
#pragma unroll
    for (int u = 0; u < 2; ++u) wa[u][i] = PLD(a.wv + (size_t)(32 * ks + 8 * (r >> 2) + (r & 3) + 4 * u) * HD + h * DH + (idx - r * 24) * 8);
  }
  unsigned pf[2] = {0u, 0u};
  auto prefetch = [&]() {
#pragma unroll
    for (int i = 0; i < 2; ++i) pf[i] = *(const unsigned*)((i ? a.wq : a.wk) + ((size_t)(b * 10 + v) * 48 + imin(lane, 47)) * 32);
  };
  if (PF_POS == 0) prefetch();
  __builtin_amdgcn_sched_barrier(0);
  unsigned lat = ~0u;
  auto probe = [&](int n0, int n) {
    const char* p = a.ws + zo;
#pragma unroll 1
    for (int i = n0; i < n0 + n; ++i) {
      u64 t0, t1; unsigned d;
      asm volatile("s_memtime %0\n\ts_waitcnt lgkmcnt(0)\n\ts_load_dword %2, %3, %4\n\ts_waitcnt lgkmcnt(0)\n\ts_memtime %1\n\ts_waitcnt lgkmcnt(0)"
                   : "=&s"(t0), "=&s"(t1), "=&s"(d) : "s"(p), "s"((i & 3) * 256 + (i >> 2) * 128) : "memory");
      PRB_LOG(i, (unsigned)t1 - (unsigned)t0);
      lat = umin(lat, (unsigned)t1 - (unsigned)t0);
    }
  };
  if (PRB_N0) { asm volatile("s_waitcnt vmcnt(26)" ::: "memory"); probe(0, PRB_N0); }
  __builtin_amdgcn_sched_barrier(0);
  auto xput = [&](int k) {
    if (v < 8) { xst[k][lane * 8 + (v ^ (lane & 7))] = xo[0]; if (lane < 16) xst[k][(64 + lane) * 8 + (v ^ (lane & 7))] = xo[1]; }
  };
  xput(0);
  const float xn2 = u2f(__builtin_amdgcn_readfirstlane(f2u(ln_row(lane, xo))));
  xput(1);
#pragma unroll
  for (int i = 0; i < 4; ++i) *(u32x4*)(slot + (i * 8 + (lane >> 3)) * 144 + (lane & 7) * 16) = tr[i];
  __builtin_amdgcn_fence(__ATOMIC_RELEASE, "wavefront"); __builtin_amdgcn_wave_barrier();
  bf16x8 tf[4];
#pragma unroll
  for (int t = 0; t < 4; ++t) {
    const char* bp = slot + (8 * g + (c >> 2)) * 144 + t * 32 + (c & 3) * 8;
    tf[t] = tr_frag(bp, bp + 4 * 144);
  }
  __builtin_amdgcn_fence(__ATOMIC_RELEASE, "wavefront"); __builtin_amdgcn_wave_barrier();
  __builtin_amdgcn_sched_barrier(0);
#pragma unroll
  for (int i = 0; i < 6; ++i) {
    const int idx = (v * 6 + i) * 64 + lane, row = idx / 20;
    *(u32x4*)(img + row * 320 + (((idx - row * 20) ^ ((row >> 2) & 2)) * 16)) = wb[i];
  }
  bf16x8 A[2][6];
#pragma unroll
  for (int u = 0; u < 2; ++u) {
#pragma unroll
    for (int i = 0; i < 6; ++i) {
      const int idx = i * 64 + lane, r = idx / 24;
      *(u32x4*)(slot + r * 384 + (((idx - r * 24) ^ (r >> 1)) * 16)) = wa[u][i];
    }
    __builtin_amdgcn_fence(__ATOMIC_RELEASE, "wavefront"); __builtin_amdgcn_wave_barrier();
#pragma unroll
    for (int e = 0; e < 6; ++e) A[u][e] = *(const bf16x8*)(slot + c * 384 + (((4 * e + g) ^ (c >> 1)) * 16));
    __builtin_amdgcn_fence(__ATOMIC_RELEASE, "wavefront"); __builtin_amdgcn_wave_barrier();
  }
  BAR();
  PROT(1);
  if (PF_POS == 0) asm volatile("" :: "v"(pf[0]), "v"(pf[1]));
  if (PF_POS == 1) prefetch();
  {
    const unsigned xf = (unsigned)((b >> 1) * 20480 + (tid >> 3) * 256 + (b & 1) * 128 + (tid & 7) * 16);
    const __amdgpu_buffer_rsrc_t rx = __builtin_amdgcn_make_buffer_rsrc(a.ws + OFF_XN, 0, (int)(OFF_QB - OFF_XN), 0x00020000);
    __builtin_amdgcn_raw_buffer_store_b128(xst[1][tid ^ ((tid >> 3) & 7)], rx, xf, 0, X_AUX);
    __builtin_amdgcn_raw_buffer_store_b128(xst[0][tid ^ ((tid >> 3) & 7)], rx, xf + (unsigned)(OFF_XB - OFF_XN), 0, X_AUX);
    if (v < 8 && lane < 4) ((float*)(a.ws + OFF_XNRM))[xrow * 4 + lane] = lane == 0 ? xn2 : 0.f;
  }
  if (job < 800) {
    const __amdgpu_buffer_rsrc_t rt = __builtin_amdgcn_make_buffer_rsrc(a.ws + (job < 600 ? OFF_WQKVBT : OFF_WOBT) + ((size_t)jn * 80 + jk) * 1024, 0, 81920, 0x00020000);
#pragma unroll
    for (int t = 0; t < 4; ++t) __builtin_amdgcn_raw_buffer_store_b128(__builtin_bit_cast(u32x4, tf[t]), rt, lane * 16 + t * 20480, 0, T_AUX);
  }
  const int gp = h >> 1, hh = h & 1;
  const __amdgpu_buffer_rsrc_t rwb = __builtin_amdgcn_make_buffer_rsrc(a.ws + OFF_WB, 0, 256 * 100 * 1024, 0x00020000);
#pragma unroll
  for (int n = 0; n < 10; ++n) {
    const int off = n * 32 + 8 * (c & 3), T = cq * 10 + n;
    const char* bp = img + (8 * g + (c >> 2)) * 320 + (off ^ ((g & 1) << 5));
    f32x4 ac[2] = {{0.f, 0.f, 0.f, 0.f}, {0.f, 0.f, 0.f, 0.f}};
#pragma unroll
    for (int e = 0; e < 6; ++e) {
      const bf16x8 bf = tr_frag(bp + e * 32 * 320, bp + (e * 32 + 4) * 320);
      ac[0] = mfma32(A[0][e], bf, ac[0]); ac[1] = mfma32(A[1][e], bf, ac[1]);
    }
    const u32x4 o = {pack2(ac[0][0], ac[0][1]), pack2(ac[0][2], ac[0][3]), pack2(ac[1][0], ac[1][1]), pack2(ac[1][2], ac[1][3])};
    const unsigned oo = T < 32 ? (unsigned)((rec_wg(gp, T & 15) * 100 + (hh * 3 + (T >> 4)) * 20 + ks) * 1024 + lane * 16)
                               : (unsigned)((rec_wg(gp, 2 * (T - 32) + (c >> 3)) * 100 + 40 + ks) * 1024 + (g * 16 + hh * 8 + (c & 7)) * 16);
    __builtin_amdgcn_raw_buffer_store_b128(o, rwb, oo, 0, B_AUX);
    float n2 = 0.f;
#pragma unroll
    for (int r = 0; r < 4; ++r) n2 += ac[0][r] * ac[0][r] + ac[1][r] * ac[1][r];
    n2 = x32sum(x16sum(n2));
    if (g == 0) red[v][n * 16 + c] = n2;
  }
  PROT(2);
  if (PF_POS == 1) asm volatile("" :: "v"(pf[0]), "v"(pf[1]));
  probe(PRB_N0, PRB_N1);
  if (v < 8) latw[v] = lat;
  else if (b < 64 && lane == 0) ((unsigned*)(a.ws + OFF_LAT))[(v - 4) * 64 + b] = umin(lat, 60000u);
  if (WARM_CODE && v == 7) {
    const char* pc; int d0, d1;
    asm("s_getpc_b64 %0\n\ts_mov_b32 %1, _Z10rec_kernel7RecArgs@rel32@lo+4\n\ts_mov_b32 %2, _Z11proj_kernelPKtS0_S0_Pt@rel32@lo+12" : "=s"(pc), "=s"(d0), "=s"(d1));
    const char *c0 = pc + d0, *c1 = pc + d1;
    const long o = ((long)b * 64 + lane) * 64;
    if (o < c1 - c0) asm volatile("" :: "v"(*(const volatile GAS unsigned*)(c0 + o)));
  }
  if (v == 9 && lane < 32) {
    const int q = b * 32 + lane;
    *(u32x4*)(a.ws + OFF_KV + (size_t)(q >> 2) * KVT + 1280 + (q & 3) * 336 + 320) = (u32x4){0x3f803f80u, 0x3f803f80u, 0x3f803f80u, 0x3f803f80u};
  }
  BAR();
  if (v == 1 && lane == 0) ((unsigned*)(a.ws + OFF_LAT))[b] = umin(umin(umin(latw[0], latw[4]) + umin(latw[1], latw[5]), 30000u) + umin(umin(latw[2], latw[6]) + umin(latw[3], latw[7]), 30000u), 60000u);
  if (v == 0) {
    float mx = 0.f;
#pragma unroll
    for (int k = 0; k < 3; ++k) {
      const int col = k * 64 + lane;
      float sm = 0.f;
#pragma unroll
      for (int i = 0; i < 10; ++i) sm += red[i][col < 160 ? col : 0];
      mx = fmx(mx, sm);
    }
    mx = fmx(mx, dppf<DPP_XOR1>(mx)); mx = fmx(mx, dppf<DPP_XOR2>(mx)); mx = fmx(mx, dppf<DPP_ROR4>(mx)); mx = fmx(mx, dppf<DPP_ROR8>(mx));
    mx = x32mx(x16mx(mx));
    if (lane == 0) ((float*)(a.ws + OFF_BNRM))[(cq * 32 + h) * 2 + dh] = mx;
  }
  PROT(3);
}

struct RecArgs {
  const u16* mem0; const u16* wq; const u16* wk; const u16* xold; u16* xnew; char* ws;
  unsigned layer; unsigned pad;
};
#define RLX_AGENT __ATOMIC_RELAXED, __HIP_MEMORY_SCOPE_AGENT
constexpr unsigned SPIN_CAP = 1u << 20;
#define TSTAMP(k, v)
#define ARRIVE(k)
#define ARRIVE_OUT()
#define ARRIVE_DECL()
#define LOGV(k, v)
#define LOGF(k, v)
#define LOGP(k)
#define LOGQ(k)
#define LOGF2(k)
#define LOGD(k)
#define LOGT(k, v)
#define STAMP_DECL()
#define STAMP(k)
#define STAMPV(k, v)
#define STAMP_OUT()
#define REC_D1 520
#define SCH_V 18
#define SCH_VMIN 0
#define SCH_DMIN 4
#define SCH_PEN 170
#define SCH_VMIN2 (-4)
#define SCH_PEN2 40
#define SCH_GU 1
#define SCH_GD 2
#define SCH_UP 6
#define SCH_D0 196
#define SCH_FIRST 120
#define EXP_NODMA 0
#define BG_HOLD 220
#define QBATCH 0
#define WPF_N 12
#define WPF_W 0
#define WPF0 (63 - WPF_N)
#define BG_HOLD4 250
#define D1_W1 20
#define F3_D 1200
#define T2W 2
#define AT_ASM 1
#define PM_Z 1
#define P2_X 1
#define ST_KS 14
#define ST_KS0 ST_KS
#define ST_KS1 ST_KS
#define PADR 0x6666
#define PADT 0x000
#define CODE_PAD(n) asm volatile(".p2align 6\n.rept %0\ns_nop 0\n.endr" :: "n"(n))
#define ST_X 300
#define ST_ALL 4
#define CPF_S 61
#define FR_D 700
#define FR_PACE 6
#define END_E60 600
#define END_E1 1300
#define END_E2 5200
#define END_E62 3600
#define END_E3 9800
#define END_E4 14500
#define EARLY_N 4
#define EARLY_D1 0
#define EARLY_V 4
#define EARLY_V1 10
#define F3 0
#define F3A 0
#define F3A_D 400
#define T4_D 300
#define T4_P 1
#define P2_W 1
#define PS_SL 1
#define F2B 4
#define F0C 0
#define F2C 0
#define W2_P 1
#define SCH_W 1
#define SP_SL 0
#define D1_M 300
#define D1_MIN 260
#define D1_C 96
#define D1_G 2
#define BG_HOLD6 0
#define W2_D 0
#define END_E61 2600
#define D1_LATE 200
#define P2_G0 10
#define P2_G2 6
#define P2_G3 6
#define SCH_RL 0
#define SCH_P 3
#define MMA_P 0
#define SP_P 0
#define F6 8
#define F7 10
#define XPF_W 2
#define SCH_LATE 0
#define LATE_V 6
#define QW 3
#define FB_SL 0
#define P2_G1 1
#define P1_G 0
#define F4Z 0
#define F5Z 0
#define T5_Z 2
#define F0A 3
#define FB_E 0
#define EXACT 1
#define D1_P 3
#define VO_A0 9
#define VO_A2 10
#define VO_B0 3
#define VO_B1 0
#define VO_N2 7
#define VO_N3 10
#define VO_W -1
#define UNIT 0
#define UN_P 2
#define VP_E 1
#define TS_B 0
#define ST_E 1
#define W1_SL 0
#define VO_A1 4
#define LAST_V 18
#define KP0 0
#define KP1 0
#define XPF 3
#define W1_D 0
#define F4 12
#define F5 0
#define F67 0
#define KP2 0
#define REC_RNG 70u
#define SCH_DRAIN 40
#define PL_X 0
#define FC(k)
#define PIN_V(x) asm volatile("" : "+v"(x))
#define PIN_S(x) asm volatile("" : "+s"(x))
#define VM_DRAIN() do { asm volatile("s_waitcnt vmcnt(0)" ::: "memory"); __builtin_amdgcn_s_waitcnt(0x0F70); } while (0)
#define SC1_LOAD(off) __builtin_amdgcn_raw_buffer_load_b128(rs, (off), 0, 16)
#define SC1_LOADS(off, soff) __builtin_amdgcn_raw_buffer_load_b128(rs, (off), (soff), 16)
#define SC1_STORE8S(v, off, soff) __builtin_amdgcn_raw_buffer_store_b64((v), rs, (off), (soff), 16)
#define SC1_LOAD8(off) __builtin_amdgcn_raw_buffer_load_b64(rs, (off), 0, 16)
#define SC1_STORE(v, off) __builtin_amdgcn_raw_buffer_store_b128((v), rs, (off), 0, 16)
#define SC1_STORE8(v, off) __builtin_amdgcn_raw_buffer_store_b64((v), rs, (off), 0, 16)
#if defined(__HIP_DEVICE_COMPILE__)
#pragma clang attribute push(__attribute__((target("no-packed-fp32-ops"))), apply_to = function)
#endif
__device__ __forceinline__ unsigned now32() { return (unsigned)__builtin_amdgcn_s_memrealtime(); }
__device__ __forceinline__ void wait_until(unsigned t) { for (;;) { const int d = (int)(t - now32()); if (d <= 0) break; if (d > 8) __builtin_amdgcn_s_sleep(1); } }
__device__ __forceinline__ unsigned cyc32() { return (unsigned)__builtin_amdgcn_s_memtime(); }
__device__ __forceinline__ void wait_cyc(unsigned t) { for (;;) { const int d = (int)(t - cyc32()); if (d <= 0) break; if (d > 200) __builtin_amdgcn_s_sleep(1); } }
#define WAIT_ASM(clk, far_) "1:\n\t" clk " vcc\n\ts_waitcnt lgkmcnt(0)\n\ts_sub_i32 %0, %1, vcc_lo\n\ts_cmp_lt_i32 %0, " far_ "\n\ts_cbranch_scc1 3f\n\ts_sleep 1\n\ts_branch 1b\n" \
                            "2:\n\t" clk " vcc\n\ts_waitcnt lgkmcnt(0)\n\ts_sub_i32 %0, %1, vcc_lo\n3:\n\ts_cmp_gt_i32 %0, 0\n\ts_cbranch_scc1 2b"
__device__ __forceinline__ int spin_until(unsigned t) { int d; asm volatile(WAIT_ASM("s_memrealtime", "9") : "=&s"(d) : "s"(t) : "vcc", "scc", "memory"); return d; }
__device__ __forceinline__ int spin_cyc(unsigned t) { int d; asm volatile(WAIT_ASM("s_memtime", "200") : "=&s"(d) : "s"(t) : "vcc", "scc", "memory"); return d; }
__device__ __forceinline__ void spin_exact(unsigned t) {
  if (!EXACT) { spin_cyc(t); return; }
  int d;
  asm volatile("1:\n\ts_memtime vcc\n\ts_waitcnt lgkmcnt(0)\n\ts_sub_i32 %0, %1, vcc_lo\n\ts_cmp_gt_i32 %0, 95\n\ts_cbranch_scc1 1b\n\t"
               "s_max_i32 %0, %0, 0\n\ts_andn2_b32 %0, %0, 3\n\ts_sub_i32 %0, 3f-2f, %0\n\ts_getpc_b64 vcc\n"
               "2:\n\ts_add_u32 vcc_lo, vcc_lo, %0\n\ts_addc_u32 vcc_hi, vcc_hi, 0\n\ts_setpc_b64 vcc\n\t.rept 24\n\ts_nop 0\n\t.endr\n3:"
               : "=&s"(d) : "s"(t) : "scc", "vcc", "memory");
}
template <int OFF, int SL> __device__ __forceinline__ void lds_spin(unsigned lds0, unsigned want) {
  unsigned v;
  asm volatile("1:\n\tds_read_b32 %0, %1 offset:%3\n\t.if %4\n\ts_sleep %4\n\t.endif\n\ts_waitcnt lgkmcnt(0)\n\tv_cmp_gt_u32 vcc, %2, %0\n\ts_cbranch_vccnz 1b"
               : "=&v"(v) : "v"(lds0), "s"(want), "n"(OFF), "n"(SL) : "vcc", "memory");
}
template <int CTRL> __device__ __forceinline__ int dppi(int v) { return __builtin_amdgcn_mov_dpp(v, CTRL, 0xf, 0xf, true); }
__device__ __forceinline__ int wave_imax(int l) {
  l = imax(l, dppi<DPP_XOR1>(l)); l = imax(l, dppi<DPP_XOR2>(l)); l = imax(l, dppi<DPP_ROR4>(l)); l = imax(l, dppi<DPP_ROR8>(l));
  int a = imax(__builtin_amdgcn_readlane(l, 0), __builtin_amdgcn_readlane(l, 16)), b = imax(__builtin_amdgcn_readlane(l, 32), __builtin_amdgcn_readlane(l, 48));
  PIN_S(a); PIN_S(b);
  return imax(a, b);
}
__device__ __forceinline__ void glds16(const void* gsrc, unsigned lds_dst) {
  unsigned keep;
  asm volatile("s_mov_b32 %0, m0\n\ts_mov_b32 m0, %2\n\ts_nop 0\n\tglobal_load_lds_dwordx4 %1, off\n\ts_mov_b32 m0, %0" : "=&s"(keep) : "v"(gsrc), "s"(lds_dst) : "memory");
}
__device__ __forceinline__ void glds16_sc1(const void* gsrc, unsigned lds_dst) {
  unsigned keep;
  asm volatile("s_mov_b32 %0, m0\n\ts_mov_b32 m0, %2\n\ts_nop 0\n\tglobal_load_lds_dwordx4 %1, off sc1\n\ts_mov_b32 m0, %0" : "=&s"(keep) : "v"(gsrc), "s"(lds_dst) : "memory");
}
__device__ __forceinline__ unsigned acc_off(int G, int row) {
  return (unsigned)(G < 128 ? ((G & 31) | ((G & 32) << 1) | ((G & 64) >> 1)) * 256 + row * 16 : (2 * G - 128 + (row >> 2)) * 256 + (row & 3) * 16);
}
__device__ __forceinline__ unsigned nz(unsigned v) { return v > 1u ? v : 1u; }
__device__ __forceinline__ unsigned pack2n(float lo, float hi) { return nz(pack2(lo, hi)); }
__device__ __forceinline__ unsigned min4(u32x4 v) { return umin(umin(v[0], v[1]), umin(v[2], v[3])); }
__device__ __forceinline__ bf16x8 as8(u32x4 v) { return __builtin_bit_cast(bf16x8, v); }

constexpr int NV = 7, NKQ = 2;
constexpr int L_RED1 = 0;
constexpr int L_BM = L_RED1 + 8 * 1536;
constexpr int L_STG = L_BM + 10240;
constexpr int L_ST = L_STG + 2048 + 2560;
constexpr int L_MISC = L_ST + 512;
constexpr int L_KX = L_MISC + 128;
constexpr int L_QX = L_KX + NKQ * 2048;
constexpr int L_XN = L_QX + NKQ * 2048;
constexpr int L_VOX = L_XN + 81920;
constexpr int L_VOM = L_VOX + NV * 5120;
constexpr int L_BYTES = L_VOM + NV * 1280;
constexpr int S_IMG = L_XN + 40960;
static_assert(S_IMG + 61440 <= L_VOM, "start-up image");
static_assert(L_BYTES <= 160 * 1024, "LDS budget");

__device__ __forceinline__ bf16x8 wqk_frag(const char* lds, int t, int ks, int lane) {
  const int c = lane & 15, g = lane >> 4, p = c & 3;
  const char* q = lds + S_IMG + (32 * ks + 8 * g + (c >> 2)) * 96 + 8 * p + (t == 0 ? 0 : (t == 1 ? 48 : (p < 2 ? 32 : 64)));
  const v4i16_t lo = __builtin_amdgcn_ds_read_tr16_b64_v4i16((LAS v4i16_t*)q), hi = __builtin_amdgcn_ds_read_tr16_b64_v4i16((LAS v4i16_t*)(q + 4 * 96));
  return (bf16x8){lo[0], lo[1], lo[2], lo[3], hi[0], hi[1], hi[2], hi[3]};
}
__device__ __forceinline__ bf16x8 sel_frag(int c, int base) {
  bf16x8 r;
#pragma unroll
  for (int i = 0; i < 8; ++i) r[i] = (c == base + i) ? (short)0x3f80 : (short)0;
  return r;
}
__device__ __forceinline__ bf16x8 qk_frag(const char* p4, const char* p2, const char* p2b) {
  f32x4 a0 = *(const f32x4*)(p4), a1 = *(const f32x4*)(p4 + 1792), a2 = *(const f32x4*)(p4 + 3584), a3 = *(const f32x4*)(p4 + 5376);
  f32x2 b0 = *(const f32x2*)(p2), b1 = *(const f32x2*)(p2 + 1792), b2 = *(const f32x2*)(p2b), b3 = *(const f32x2*)(p2b + 1792);
  f32x4 va = (a0 + a1) + (a2 + a3);
  f32x2 vb = (b0 + b1) + (b2 + b3);
  return mk8(pack2(va[0], va[1]), pack2(va[2], va[3]), pack2(vb[0], vb[1]), 0u);
}
__device__ __forceinline__ void x_fetch(const u16* src, unsigned lds0, int seg, int p, int lane) {
  if (EXP_NODMA) return;
  glds16_sc1(src + (size_t)p * 512 + lane * 8, lds0 + L_XN + (seg & 1) * 40960 + p * 1024);
}
template <int N> __device__ __forceinline__ void x_fetch_n(const u16* src, unsigned lds0, int seg, int p0, unsigned voff) {
  if (EXP_NODMA) return;
#pragma unroll
  for (int i = 0; i < N; i += 4) {
    unsigned keep;
    asm volatile("s_mov_b32 %0, m0\n\ts_mov_b32 m0, %3\n\ts_nop 0\n\tglobal_load_lds_dwordx4 %1, %2 sc1\n\t"
                 ".if %4 > 1\n\tglobal_load_lds_dwordx4 %1, %2 offset:1024 sc1\n\t.endif\n\t.if %4 > 2\n\tglobal_load_lds_dwordx4 %1, %2 offset:2048 sc1\n\t.endif\n\t"
                 ".if %4 > 3\n\tglobal_load_lds_dwordx4 %1, %2 offset:3072 sc1\n\t.endif\n\ts_mov_b32 m0, %0"
                 : "=&s"(keep) : "v"(voff), "s"(src + (size_t)(p0 + i) * 512), "s"(lds0 + L_XN + (seg & 1) * 40960 + (p0 + i) * 1024), "n"(N - i < 4 ? N - i : 4) : "memory");
  }
}
__device__ __forceinline__ int qkv_row(int b) { return 2 * (b & 7) + ((b >> 3) >= 15); }
__device__ __forceinline__ const u16* x_src(const RecArgs& a, int seg, int b) {
  return seg < NSEG ? (const u16*)(a.ws + OFF_XN) + (size_t)seg * 32 * DM : a.xnew + (size_t)(qkv_row(b) + 16 * (seg - NSEG)) * 32 * DM;
}
template <int N>
__device__ __forceinline__ void img_fetch(const RecArgs& a, unsigned lds0, int h, int j, int part, int lane) {
#pragma unroll
  for (int i = 0; i < N; ++i) {
    const int n = part * N + i, idx = n * 64 + lane, row = idx / 6, pc = idx - row * 6;
    if (FR_PACE && N == 20) asm volatile("s_waitcnt vmcnt(%0)" :: "n"(FR_PACE) : "memory");
    glds16((pc < 3 ? a.wq : a.wk - 24) + (size_t)row * HD + h * DH + j * 24 + pc * 8, lds0 + S_IMG + n * 1024);
  }
}
constexpr int P2B = F4 + F5 + 2 * F67 + F6 + F7, PZ = P2B + F2B, PA = PZ, P0C = PA + 2 * F0A, F3C = 40 - F3A - F3 - P0C - 2 * F0C - F2C;
static_assert(F3C >= 0, "fetch plan");
template <int OFF> __device__ __forceinline__ void lds_inc(unsigned lds0) {
  asm volatile("s_mov_b64 exec, 1\n\tds_add_u32 %0, %1 offset:%2\n\ts_mov_b64 exec, -1" :: "v"(lds0), "v"(1u), "n"(OFF) : "memory");
}
__device__ __forceinline__ void lds_arrive(unsigned* p, int lane) {
  asm volatile("s_waitcnt lgkmcnt(0)" ::: "memory");
  if (lane == 0) __hip_atomic_fetch_add(p, 1u, __ATOMIC_RELAXED, __HIP_MEMORY_SCOPE_WORKGROUP);
}
__device__ __forceinline__ void lds_wait(unsigned* p, unsigned want) {
  while (__hip_atomic_load(p, __ATOMIC_RELAXED, __HIP_MEMORY_SCOPE_WORKGROUP) < want) __builtin_amdgcn_s_sleep(1);
  asm volatile("" ::: "memory");
}
constexpr float KEXP = 0.07216878364870322f * 1.4426950408889634f;
#define FRESH_LANE() int lane = lane0; asm volatile("" : "+v"(lane)); const int c = lane & 15, g = lane >> 4; (void)c; (void)g

struct OutTask {
  u32x4 y[4]; u32x2 xo, out;
  bf16x8 sel;
  unsigned t5f, t5o;
  __device__ __forceinline__ void init(int b, int lane) {
    const int um = b / 10, uq = (b / 5) & 1, up = b % 5, tok = lane & 15, k = up < 4 ? (up >> 1) * 256 + um * 16 + (up & 1) * 8 : 512 + um * 8, q4 = ((lane >> 4) & 1) * 4;
    t5f = (unsigned)(((uq * 20 + (k >> 5)) * 64 + ((k & 31) >> 3) * 16 + tok) * 8 + q4);
    t5o = (unsigned)(((um * 2 + uq) * 16 + (lane >> 4)) * 1280 + up * 256 + tok * 16);
    if (b >= 160) { t5f = 0u; t5o = 0x7ffffff0u; }
    sel = sel_frag(tok, 0);
    PIN_V(sel);
  }
  static __device__ __forceinline__ bool ok(int sl, int b) { return sl >= 0 && b < 160; }
  __device__ __forceinline__ void load(const RecArgs& a, const __amdgpu_buffer_rsrc_t rs, int sl, int b, int part = 3) {
    const unsigned o = t5o | (sl < 0 ? 0x7ff00000u : 0u);
    sl = imax(sl, 0);
#pragma unroll
    for (int k = 0; k < 4; ++k) if (part & 1) y[k] = SC1_LOADS(o, op_slot(sl) + k * 5120);
    if (part & 2) xo = __builtin_bit_cast(u32x2, __hip_atomic_load((const gu64*)(a.xold + (size_t)sl * 32 * DM + t5f), RLX_AGENT));
  }
  __device__ __forceinline__ bool ready(int b) { return b >= 160 || wave_all(umin(umin(min4(y[0]), min4(y[1])), umin(min4(y[2]), min4(y[3]))) != 0u); }
  __device__ __forceinline__ void run(int lane) {
    f32x4 d = {0.f, 0.f, 0.f, 0.f};
#pragma unroll
    for (int k = 0; k < 4; ++k) d = mfma32(sel, as8(y[k]), d);
#pragma unroll
    for (int i = 0; i < 2; ++i) {
      const unsigned r = pack2(d[2 * i], d[2 * i + 1]), x = xo[i];
      out[i] = pack2(u2f(x << 16) + u2f(r << 16), u2f(x & 0xffff0000u) + u2f(r & 0xffff0000u));
    }
    PIN_V(out);
  }
  __device__ __forceinline__ void store(const RecArgs& a, int sl, int b, int lane) {
    __builtin_amdgcn_raw_buffer_store_b64(out, __builtin_amdgcn_make_buffer_rsrc(a.xnew, 0, ok(sl, b) ? S_ * DM * 2 : 0, 0x00020000), (t5f + (unsigned)sl * 32 * DM) * 2u | (unsigned)(lane & 32) << 25, 0, 16);
  }
};

struct PvTask {
  u32x4 y[2]; u32x2 xo[2];
  __device__ __forceinline__ void load(const __amdgpu_buffer_rsrc_t rs, unsigned ck, int sl, int gp, int qt, int lane) {
    const unsigned o = pt_slot(ck, sl);
#pragma unroll
    for (int k = 0; k < 2; ++k) {
      y[k] = SC1_LOAD(o + pt_tok(2 * gp) + k * 2048 + qt * 1024 + lane * 16);
      xo[k] = SC1_LOAD8(o + pt_mem(2 * gp) + k * 512 + qt * 256 + (lane & 31) * 8);
    }
  }
  __device__ __forceinline__ bool ready() { return wave_all(umin(umin(min4(y[0]), min4(y[1])), umin(umin(xo[0][0], xo[0][1]), umin(xo[1][0], xo[1][1]))) != 0u); }
  __device__ __forceinline__ void run(char* lds, const __amdgpu_buffer_rsrc_t rs, int sl, int gp, int m, int qt, int lane, bool direct) {
    const f32x4 Z4 = {0.f, 0.f, 0.f, 0.f};
    PIN_V(y[0]); PIN_V(y[1]); PIN_V(xo[0]); PIN_V(xo[1]);
    const int sv = (sl + 2 * NV) % NV;
    const char* vp = lds + L_VOX + sv * 5120 + lane * 16;
    const char* mp = lds + L_VOM + sv * 1280 + (lane & 31) * 8;
    bf16x8 pm[2], vm[5], vx[5];
#pragma unroll
    for (int t = 0; t < 5; ++t) { vx[t] = *(const bf16x8*)(vp + t * 1024); const u32x2 v = *(const u32x2*)(mp + t * 256); vm[t] = mk8(v[0], v[1], 0u, 0u); }
#pragma unroll
    for (int k = 0; k < 2; ++k) pm[k] = mk8(lane < 32 ? xo[k][0] : 0u, lane < 32 ? xo[k][1] : 0u, 0u, 0u);
    __builtin_amdgcn_sched_barrier(0);
    f32x4 o[4];
#pragma unroll
    for (int k = 0; k < 2; ++k) { o[k] = mfma32(vx[k], as8(y[0]), Z4); o[2 + k] = mfma32(vx[2], as8(y[k]), Z4); }
#pragma unroll
    for (int k = 0; k < 2; ++k) { o[k] = mfma32(vm[k], pm[0], o[k]); o[2 + k] = mfma32(vm[2], pm[k], o[2 + k]); }
#pragma unroll
    for (int k = 0; k < 2; ++k) o[k] = mfma32(vx[3 + k], as8(y[1]), o[k]);
#pragma unroll
    for (int k = 0; k < 2; ++k) o[k] = mfma32(vm[3 + k], pm[1], o[k]);
    float oc[4];
#pragma unroll
    for (int r = 0; r < 4; ++r) {
      auto sw = __builtin_amdgcn_permlane32_swap(f2u(o[3][r]), f2u(o[3][r]), false, false);
      oc[r] = o[2][r] + u2f(sw[1]);
    }
    const u32x2 v0 = {pack2n(o[0][0], o[0][1]), pack2n(o[0][2], o[0][3])}, v1 = {pack2n(o[1][0], o[1][1]), pack2n(o[1][2], o[1][3])};
    const u32x2 v2 = {pack2n(oc[0], oc[1]), pack2n(oc[2], oc[3])};
    const unsigned po = (unsigned)(((lane >> 5) * 16 + (lane & 15)) * 16 + ((lane >> 4) & 1) * 8);
    if (direct) {
      const unsigned oo = op_slot(sl) + (unsigned)((m * 32 + qt * 16 + gp) * 1280) + po;
      SC1_STORE8(v0, oo); SC1_STORE8(v1, oo + 512);
      if (lane < 32) SC1_STORE8(v2, oo + 1024);
    } else {
      char* sp = lds + L_STG + 2048 + qt * 1280 + po;
      *(u32x2*)(sp) = v0; *(u32x2*)(sp + 512) = v1;
      if (lane < 32) *(u32x2*)(sp + 1024) = v2;
    }
  }
};

struct PvTask2 {
  u32x4 y[2][2], pa[2], pb[2];
  unsigned lo, lm, vr, ma, mz, so, sz;
  __device__ __forceinline__ void init(int gp, int lane) {
    const int hi = lane >> 5;
    lo = pt_tok(2 * gp) + (unsigned)(lane * 16); lm = pt_mem(2 * gp + hi) + (unsigned)((lane & 31) * 8);
    vr = (unsigned)(L_VOX + 2048 + (lane ^ 8) * 16);
    ma = (unsigned)(L_VOM + hi * 768 + (lane & 31) * 8); mz = (unsigned)(L_VOM + 512 + ((lane & 31) ^ (hi * 8)) * 8);
    so = (unsigned)(L_STG + 2048 + (hi * 16 + (lane & 15)) * 16 + ((lane >> 4) & 1) * 8);
    sz = hi ? (unsigned)(L_RED1 + 8704 + lane * 8) : so + 1024;
    PIN_V(lo); PIN_V(lm); PIN_V(vr); PIN_V(ma); PIN_V(mz); PIN_V(so); PIN_V(sz);
#pragma unroll
    for (int q = 0; q < 2; ++q) { pa[q] = pb[q] = y[q][0] = y[q][1] = (u32x4){0u, 0u, 0u, 0u}; PIN_V(pa[q]); PIN_V(pb[q]); }
  }
  __device__ __forceinline__ void load(const __amdgpu_buffer_rsrc_t rs, unsigned o) {
#pragma unroll
    for (int q = 0; q < 2; ++q) {
      y[q][0] = SC1_LOADS(lo + q * 1024, o); y[q][1] = SC1_LOADS(lo + 2048 + q * 1024, o);
      const u32x2 p = __builtin_amdgcn_raw_buffer_load_b64(rs, lm + q * 256, o, 16);
      pa[q][0] = p[0]; pa[q][1] = p[1];
    }
  }
  __device__ __forceinline__ void run(char* lds, int sv, int lane) {
    const f32x4 Z4 = {0.f, 0.f, 0.f, 0.f};
    const char* vp = lds + L_VOX + sv * 5120 + lane * 16;
    bf16x8 vx[6];
#pragma unroll
    for (int t = 0; t < 5; ++t) vx[t] = *(const bf16x8*)(vp + t * 1024);
    vx[5] = *(const bf16x8*)(lds + vr + sv * 5120);
    const u32x2 a0 = *(const u32x2*)(lds + ma + sv * 1280), a1 = *(const u32x2*)(lds + ma + sv * 1280 + 256);
    const u32x2 z0 = *(const u32x2*)(lds + mz + sv * 1280), z1 = *(const u32x2*)(lds + mz + sv * 1280 + 256);
    const bf16x8 vm = mk8(a0[0], a0[1], a1[0], a1[1]), vz = mk8(z0[0], z0[1], z1[0], z1[1]);
#pragma unroll
    for (int q = 0; q < 2; ++q) { PIN_V(pa[q]); pb[q][2] = pa[q][0]; pb[q][3] = pa[q][1]; PIN_V(pb[q]); }
    __builtin_amdgcn_sched_barrier(0);
    f32x4 o[2][3];
#pragma unroll
    for (int q = 0; q < 2; ++q) {
#pragma unroll
      for (int t = 0; t < 3; ++t) o[q][t] = mfma32(vx[t], as8(y[q][0]), Z4);
#pragma unroll
      for (int t = 0; t < 3; ++t) o[q][t] = mfma32(vx[3 + t], as8(y[q][1]), o[q][t]);
      o[q][0] = mfma32(vm, as8(pa[q]), o[q][0]); o[q][1] = mfma32(vm, as8(pb[q]), o[q][1]); o[q][2] = mfma32(vz, as8(pa[q]), o[q][2]);
    }
#pragma unroll
    for (int q = 0; q < 2; ++q) {
#pragma unroll
      for (int t = 0; t < 3; ++t)
        *(u32x2*)(lds + (t < 2 ? so + t * 512 : sz) + q * 1280) = (u32x2){pack2n(o[q][t][0], o[q][t][1]), pack2n(o[q][t][2], o[q][t][3])};
    }
  }
};

struct TokScores {
  bf16x8 k[2], q[2]; u32x4 v[2];
  __device__ __forceinline__ void get(const char* lds, int seg, int lane) {
#pragma unroll
    for (int i = 0; i < 2; ++i) { k[i] = *(const bf16x8*)(lds + L_KX + (seg & 1) * 2048 + i * 1024 + lane * 16); q[i] = *(const bf16x8*)(lds + L_QX + (seg & 1) * 2048 + i * 1024 + lane * 16); }
  }
  __device__ __forceinline__ void run(int qt) {
    const f32x4 Z4 = {0.f, 0.f, 0.f, 0.f};
    const f32x4 d1 = mfma32(k[0], q[qt], Z4), d2 = mfma32(k[1], q[qt], Z4);
    v[qt] = (u32x4){pack2n(d1[0], d1[1]), pack2n(d1[2], d1[3]), pack2n(d2[0], d2[1]), pack2n(d2[2], d2[3])};
    PIN_V(v[qt]);
  }
  __device__ __forceinline__ void store(const __amdgpu_buffer_rsrc_t rs, unsigned off, unsigned o) {
    __builtin_amdgcn_raw_buffer_store_b128(v[0], rs, off, o, 16); __builtin_amdgcn_raw_buffer_store_b128(v[1], rs, off + 1024, o, 16);
    asm volatile("s_nop 1" ::: "memory");
  }
};

__device__ __forceinline__ void touch(const char* base, unsigned off, unsigned lds0) {
  unsigned keep;
  asm volatile("s_mov_b32 %0, m0\n\ts_mov_b32 m0, %3\n\ts_nop 0\n\tglobal_load_lds_dword %1, %2 sc1\n\ts_mov_b32 m0, %0" : "=&s"(keep) : "v"(off), "s"(base), "s"(lds0 + L_RED1 + 8192) : "memory");
}
template <int N> __device__ __forceinline__ void stouch(const char* p, unsigned& d) {
  asm volatile(".set pf_o, 0\n\t.rept %2\n\ts_load_dword %0, %1, pf_o\n\t.set pf_o, pf_o + 128\n\t.endr" : "+s"(d) : "s"(p), "n"(N) : "memory");
}
__device__ __forceinline__ void code_touch(int b, int lane, unsigned lds0) {
  const char* pc; int d0, d1;
  asm volatile("s_getpc_b64 %0\n\ts_mov_b32 %1, _Z10rec_kernel7RecArgs@rel32@lo+4\n\ts_mov_b32 %2, _Z11attb_kernelPKtS0_Pt@rel32@lo+12" : "=s"(pc), "=s"(d0), "=s"(d1));
  const int o = (lane * 32 + (b >> 3)) * 128;
  touch(pc + d0, (unsigned)(o < d1 - d0 ? o : 0), lds0);
}
__device__ __forceinline__ unsigned zero_base(int b, int h, int j, int ln) {
  return ln < 32 ? (unsigned)((h & 3) * 32768 + j * 512 + ln * 16) : ln < 40 ? (unsigned)((h & 2) * 1024 + j * 256 + (ln & 4) * 32 + (h & 1) * 64 + (ln & 3) * 16)
                 : (unsigned)OFF_ACC + (b < ACC_NB ? (unsigned)(b * 256 + (ln - 40) * 16) : ts_piece((b - ACC_NB) * 8 + ln - 40));
}
__device__ __forceinline__ unsigned sct_slot(unsigned ck, unsigned sl) { return sl * SCT_STEP + ((ck >> (sl & 4u)) & 7u) * 4096u; }
__device__ __forceinline__ unsigned scm_slot(unsigned ck, unsigned sl) { return OFF_SCM + sl * SCM_STEP + ((ck >> 8) & 7u) * 4096u; }
__device__ __forceinline__ int ck_rot(int x) { return ((x & 1) << 2) | (x >> 1); }
struct Place {
  unsigned t[8];
  __device__ __forceinline__ void load(const char* ws, int lane) {
#pragma unroll
    for (int x = 0; x < 8; ++x) t[x] = *(const unsigned*)(ws + OFF_LAT + ((imin(lane >> 3, 5) * 8 + (ck_rot(x) ^ (lane & 7))) * 8 + x) * 4);
  }
  __device__ __forceinline__ unsigned get(int b, int lane) {
    int c = (int)((((t[0] + t[1]) + (t[2] + t[3])) + ((t[4] + t[5]) + (t[6] + t[7]))) * 8u) + (lane & 7);
    c = imin(c, dppi<DPP_XOR1>(c)); c = imin(c, dppi<DPP_XOR2>(c)); c = imin(c, dppi<0x141>(c));
    unsigned k = 0u;
#pragma unroll
    for (int r = 0; r < 6; ++r) k |= (unsigned)(__builtin_amdgcn_readlane(c, 8 * r) & 7) << (4 * r);
    return (k ^ PL_X) ^ (unsigned)ck_rot(b & 7) * 0x111111u;
  }
};

struct Sched {
  u32x4 tsv, rec;
  unsigned side, o;
  __device__ __forceinline__ void pre(int s) { o = (unsigned)OFF_ACC + (unsigned)((s + SR - 1) % SR) * (unsigned)ACC_STEP + ACC_TS + side; PIN_S(o); }
  __device__ __forceinline__ void load(const __amdgpu_buffer_rsrc_t rs, int lane) {
    tsv = SC1_LOADS(ts_piece(lane & 63) - ACC_TS, o); rec = SC1_LOADS(128, o);
  }
  __device__ __forceinline__ unsigned last(unsigned ref) {
    const unsigned o = 0x40000000u - ref;
    int l = (int)umax(umax(tsv[0] ? tsv[0] + o : 0u, tsv[1] ? tsv[1] + o : 0u), umax(tsv[2] ? tsv[2] + o : 0u, tsv[3] ? tsv[3] + o : 0u));
    return (unsigned)wave_imax(l) - o;
  }
  __device__ __forceinline__ unsigned run(const RecArgs& a, const __amdgpu_buffer_rsrc_t rs, int s, int b, int lane) {
    constexpr int w = 3; (void)w;
    unsigned acur = __builtin_amdgcn_readfirstlane(rec[0]), aprev = __builtin_amdgcn_readfirstlane(rec[1]), hist = __builtin_amdgcn_readfirstlane(rec[3]);
    int D8 = (int)__builtin_amdgcn_readfirstlane(rec[2]);
    const int d = wave_imax((int)(umax(umax(tsv[0], tsv[1]), umax(tsv[2], tsv[3])) - aprev)), dd = 8 * d - D8;
    int Dn = D8 + (dd >> (dd > 0 ? SCH_GU : SCH_GD));
    unsigned anext = (acur + (unsigned)((Dn + 4) >> 3) + SCH_V + (s < EARLY_N ? EARLY_V : (s >= WPF0 ? (s == NSEG - 2 ? LAST_V : LATE_V) : 0))) | 1u;
    if (__builtin_expect(hist < 2u || aprev == 0u || d < 1 || d + SCH_VMIN > (int)(acur - aprev), 0)) {
      if (s == 0) {
        for (int it = 0; it < 40 && !wave_all(min4(tsv) != 0u); ++it) {
          __builtin_amdgcn_s_sleep(ST_ALL);
          asm volatile("" ::: "memory");
          load(rs, lane);
        }
      }
      const unsigned now = now32(), L = last(now);
      if (acur == 0u) { D8 = 8 * SCH_D0; hist = 0u; }
      const bool late = acur == 0u || (int)(L + SCH_VMIN - acur) > 0;
      if (late) { if (b == 0) FC(3); if (acur) D8 += 8 * (hist ? SCH_UP : 3 * SCH_UP); }
      else if (aprev) { if (hist < 2u) D8 = 8 * imin(imax((int)(L - aprev), 1), 4000); hist = umin(hist + 1u, 2u); }
      const unsigned base = acur == 0u ? L + SCH_FIRST : (late ? L + SCH_V + ((int)(L + SCH_VMIN2 - acur) <= 0 ? SCH_PEN2 : SCH_PEN) : acur);
      anext = (base + (unsigned)((D8 + 4) >> 3) + SCH_V + (s == 1 ? EARLY_V1 : (s < EARLY_N ? EARLY_V : 0))) | 1u;
      if (s == 0) anext = (L + ST_X) | 1u;
      if ((int)(now - L) > 50000) anext = 0u;
      if (late) acur = 0u;
      Dn = D8;
    }
    if (__builtin_expect(b == 0, 0))
      __builtin_amdgcn_raw_buffer_store_b128((u32x4){anext, acur, (unsigned)Dn, hist}, rs, lane < 2 ? lane * 8192 : 0x7ffffff0, (unsigned)OFF_ACC + (unsigned)(s % SR) * (unsigned)ACC_STEP + ACC_TS + 128u, 16);
    LOGV(0, anext); LOGV(1, acur == 0u); LOGV(2, (Dn + 4) >> 3); LOGV(3, d); LOGV(4, acur - aprev - d); LOGV(5, aprev);
    return anext;
  }
};

template <int w>
__device__ __forceinline__ void token_waves(const RecArgs& a, char* lds, const __amdgpu_buffer_rsrc_t rs) {
  const int lane0 = __builtin_amdgcn_workitem_id_x() & 63, b = __builtin_amdgcn_workgroup_id_x(), wid = w + 4; (void)wid;
  const int gp = (b & 7) * 2 + (b >> 7), m = (b >> 3) & 15, h0 = 2 * gp, h = h0 + (m >> 3), j = m & 7;
  float* const misc = (float*)(lds + L_MISC);
  const f32x4 Z4 = {0.f, 0.f, 0.f, 0.f};
  const unsigned lds0 = (unsigned)(uintptr_t)(LAS char*)lds;
  unsigned* const cnt = (unsigned*)(misc + 6);
  const float* const xnrm = (const float*)(a.ws + OFF_XNRM);
  bf16x16 W[2][10];
  auto btile = [&](int i) { return w == 1 ? 0 : (w == 2 ? 1 + i : 3 + i); };
  if (w == 1) code_touch(b, lane0, lds0);
  Place pl;
  if (w == 3) pl.load(a.ws, lane0);
  if (a.layer) {
    if (w != 0) img_fetch<20>(a, lds0, h, j, w - 1, lane0);
  } else {
    if (w == 0) img_fetch<12>(a, lds0, h, j, 4, lane0);
#pragma unroll
    for (int i = 0; i < 10; ++i) x_fetch(x_src(a, 0, b), lds0, 0, 10 * w + i, lane0);
  }
#pragma unroll
  for (int i = 0; i < 2; ++i)
#pragma unroll
    for (int p = 0; p < 10; ++p)
      if (w >= 2 || (w == 1 && i == 1)) {
        const char* wp = a.ws + OFF_WB + ((size_t)(b * 5 + btile(i)) * 20 + 2 * p) * 1024 + lane0 * 16;
        if (FR_PACE && a.layer) asm volatile("s_waitcnt vmcnt(%0)" :: "n"(FR_PACE) : "memory");
        W[i][p] = cat16(*(const bf16x8*)wp, *(const bf16x8*)(wp + 1024));
      }
  f32x4 bn = Z4;
  if (w == 0) {
    bn = *(const f32x4*)(a.ws + OFF_BNRM + lane0 * 16);
    if (!a.layer) asm volatile("s_waitcnt vmcnt(0)" ::: "memory");
  } else asm volatile("s_waitcnt vmcnt(%0)" :: "n"(w == 1 ? 20 : 40) : "memory");
  unsigned ck = 0u;
  if (w == 3) { ck = pl.get(b, lane0); *(unsigned*)(misc + 11) = ck; }
  BAR();
#pragma unroll
  for (int p = 0; p < 10; ++p) {
    if (w < 2) W[0][p] = cat16(wqk_frag(lds, 2 * w, 2 * p, lane0), wqk_frag(lds, 2 * w, 2 * p + 1, lane0));
    if (w == 0) W[1][p] = cat16(wqk_frag(lds, 1, 2 * p, lane0), wqk_frag(lds, 1, 2 * p + 1, lane0));
  }
  float cb2 = 0.f;
  if (w == 0) {
    float n = __builtin_amdgcn_sqrtf(bn[0] + bn[1]) + __builtin_amdgcn_sqrtf(bn[2] + bn[3]);
    n += dppf<DPP_XOR1>(n); n += dppf<DPP_XOR2>(n); n += dppf<DPP_ROR4>(n); n += dppf<DPP_ROR8>(n);
    n = x32mx(x16mx(n));
    cb2 = u2f(__builtin_amdgcn_readfirstlane(f2u(1.15f * n * n)));
  }
  const float cm2 = u2f((f2u(cb2) & 0x7fffffu) | 0x40000000u);
  const int ek = imax((int)((f2u(cb2) >> 23) & 0xffu), 1) - 252;
  if (w >= 2) {
#pragma unroll
    for (int i = 0; i < 4; ++i) *(unsigned*)(lds + L_KX + (w - 2) * 4096 + i * 1024 + lane0 * 16 + 12) = 0u;
  }
  OutTask ot;
  const int zl = lane0 - 16;
  const bool zon = w == 3 && zl >= 0 && (zl < 40 || b < ACC_NB + 17);
  unsigned zo = zon ? zero_base(b, h, j, zl) + (zl < 32 ? sct_slot(ck, 0u) : (zl < 40 ? scm_slot(ck, 0u) : 0u)) : 0x7ffffff0u;
  unsigned zm = zon ? (zl < 32 ? SCT_STEP + 1u : (zl < 40 ? SCM_STEP : (unsigned)ACC_STEP)) : 0u;
  const unsigned zx = ((ck ^ (ck >> 4)) & 7u) * 4096u;
  if (w == 3) { PIN_V(zo); PIN_V(zm); }
  auto st_out = [&](int s, int lane, auto go) {
    if (w < 2) return;
    if (ST_E) lds_spin<L_MISC + 112, 1>(lds0, (unsigned)imax(s, 0)); else go();
    const int sl = s - 2 - LAG4;
    const char* sp = lds + L_STG + 2048 + (w & 1) * 1280;
    u32x4 v0 = *(const u32x4*)(sp + lane * 16), v1 = *(const u32x4*)(sp + 1024 + (lane & 15) * 16);
    const u32x4 z = {0u, 0u, 0u, 0u};
    unsigned oo = (sl < 0 ? 0x7ff00000u : op_slot(sl) + (unsigned)((m * 32 + (w & 1) * 16 + gp) * 1280)) + (unsigned)(lane * 16), za = 0x7ffffff0u;
    if (w == 3) {
      const unsigned zs = (unsigned)(s + 2) % SR;
      za = (zo + (s < 0 ? 1024u : zs) * (zm & ~1u)) ^ ((zm & 1u) * (zs & 4u ? zx : 0u));
#pragma unroll
      for (int i = 0; i < 4; ++i) { v1[i] = lane < 16 ? v1[i] : z[i]; PIN_V(v1[i]); }
    }
    za = lane < 16 ? oo + 1024 : za;
    PIN_V(za); PIN_V(oo); PIN_V(v0); PIN_V(v1);
    __builtin_amdgcn_sched_barrier(0);
    if (ST_E) go();
    SC1_STORE(v0, oo);
    SC1_STORE(v1, za);
    __builtin_amdgcn_sched_barrier(0);
    if (w == 2) ot.store(a, s - 2 - LAG5, b, lane);
    else {
      const unsigned xo_ = (unsigned)((s - 8) * 1024 + lane * 16);
      if (__builtin_expect((unsigned)(s - 8) < 20u, 0)) SC1_STORE(z, xo_ < X_BYTES / 256 ? (unsigned)(OFF_X + b * (X_BYTES / 256)) + xo_ : 0x7ffffff0u);
    }
  };
  STAMP_DECL(); ARRIVE_DECL();
  auto mma_seg = [&](int seg, int sv, int lane, auto pause) {
    const int c = lane & 15, g = lane >> 4;
    const char* xp = lds + L_XN + (seg & 1) * 40960 + lane * 16;
    f32x4 acc[2][2] = {{Z4, Z4}, {Z4, Z4}};
    bf16x8 x[4][2];
#pragma unroll
    for (int p = 0; p < 3; ++p) { x[p][0] = *(const bf16x8*)(xp + p * 1024); x[p][1] = *(const bf16x8*)(xp + (20 + p) * 1024); }
    unsigned eo[2];
#pragma unroll
    for (int ks = 0; ks < 20; ++ks) {
      if (ks == (w == 0 ? KP0 : (w == 1 ? KP1 : KP2))) { pause(); __builtin_amdgcn_sched_barrier(0); }
      if (ks == 17) {
#pragma unroll
        for (int i = 0; i < 2; ++i) {
          eo[i] = (unsigned)(w == 0 ? (i ? L_KX : L_QX) + (seg % NKQ) * 2048 + lane * 16
                                    : (w == 1 && i == 0 ? ((g >> 1) ? L_KX : L_QX) + (seg % NKQ) * 2048 + (2 * (g & 1) * 16 + c) * 16 + 8 : L_VOX + sv * 5120 + btile(i) * 1024 + lane * 16));
          if (i == 0 || w == 1) PIN_V(eo[i]);
        }
        if (w == 0) eo[1] = eo[0] + (L_KX - L_QX);
        if (w >= 2) eo[1] = eo[0] + 1024;
      }
      __builtin_amdgcn_sched_barrier(0);
#pragma unroll
      for (int q = 0; q < 4; ++q) {
        const int tt = 1 - (q >> 1), i = q & 1;
        const bool isqk = w == 0 || (w == 1 && i == 0);
        const bf16x8 wf = half8(W[i][ks >> 1], ks & 1);
        constexpr int q0 = UNIT && w < 2;
        if (q < q0) continue;
        acc[i][tt] = isqk ? mfma32(wf, x[ks & 3][tt], acc[i][tt]) : mfma32(x[ks & 3][tt], wf, acc[i][tt]);
        __builtin_amdgcn_sched_barrier(0);
        if (q - q0 < 2 && ks + 3 < 20) x[(ks + 3) & 3][q - q0] = *(const bf16x8*)(xp + ((q - q0) * 20 + ks + 3) * 1024);
        __builtin_amdgcn_sched_barrier(0);
      }
    }
#pragma unroll
    for (int i = 0; i < 2; ++i) {
      if (w + i == 1) { const float ks_ = (w == 0 || (g >> 1)) ? KEXP : 1.0f; acc[i][0] *= ks_; acc[i][1] *= ks_; }
      const u32x4 v = {pack2(acc[i][0][0], acc[i][0][1]), pack2(acc[i][0][2], acc[i][0][3]), pack2(acc[i][1][0], acc[i][1][1]), pack2(acc[i][1][2], acc[i][1][3])};
      char* p = lds + eo[i];
      if (w == 0) { *(u32x2*)(p) = (u32x2){v[0], v[1]}; if (i || !UNIT) *(u32x2*)(p + 1024) = (u32x2){v[2], v[3]}; }
      else if (w == 1 && i == 0) { *(unsigned*)(p) = v[0]; *(unsigned*)(p + 256) = v[1]; if (!UNIT) { *(unsigned*)(p + 1024) = v[2]; *(unsigned*)(p + 1280) = v[3]; } }
      else *(u32x4*)p = v;
    }
  };
  unsigned vp, mp, ip;
  int sidx, ei = 127;
  float inv;
  f32x4 acc[2];
  auto vo_pre = [&](int s, int sv, int lane) {
    vp = (unsigned)(w == 0 ? L_MISC + (s & 3) * 4 : L_VOM + sv * 1280 + (btile(0) * 32 + (w == 1 ? lane & 31 : lane)) * 8);
    mp = (unsigned)(L_BM + lane * 16);
    PIN_V(vp); PIN_V(mp);
    if (w == 0) { ip = (unsigned)(L_MISC + 16 + ((s + 1) & 1) * 4); PIN_V(ip); }
    else {
      inv = misc[4 + (s & 1)];
      sidx = (lane & 8) ? 0xEEEE : 0x4444;
      acc[0] = acc[1] = Z4;
      PIN_V(sidx); PIN_V(acc[1]);
      if (w >= 2) { PIN_V(acc[0]); PIN_V(inv); }
    }
    __builtin_amdgcn_sched_barrier(0);
  };
  auto vo_mem = [&](int s, auto last) {
    __builtin_amdgcn_s_setprio(2);
    asm volatile(".rept %0\n\ts_nop 3\n\t.endr" :: "n"(w == 0 ? VO_A0 : (w == 1 ? VO_A1 : VO_A2)) : "memory");
    float xmax = 0.f;
    if (w == 0) xmax = *(const float*)(lds + vp);
    constexpr int NX = w == 2 ? VO_N2 : (w == 3 ? VO_N3 : 10);
    bf16x8 x[10];
#pragma unroll
    for (int p = 0; p < NX; ++p) x[p] = *(const bf16x8*)(lds + mp + p * 1024);
    __builtin_amdgcn_sched_barrier(0);
    if (w < 2) asm volatile(".rept %0\n\ts_nop 3\n\t.endr" :: "n"(w == 0 ? VO_B0 : VO_B1) : "memory");
    if (VO_W >= 0) asm volatile("s_waitcnt lgkmcnt(%0)" :: "n"(VO_W < 0 ? 0 : VO_W) : "memory");
    STAMP(5);
    __builtin_amdgcn_sched_barrier(0);
    int ex = 0;
#pragma unroll
    for (int p = 0; p < 10; ++p) {
      if (w == 0) acc[0] = mfma32(x[p], x[p], p ? acc[0] : Z4);
      if (w == 0 && p == 9) { ex = imax((int)((__builtin_amdgcn_readfirstlane(f2u(__builtin_amdgcn_sqrtf(cb2 * xmax))) >> 23) & 0xffu) + 1, 32); PIN_S(ex); }
#pragma unroll
      for (int i = (w == 1); i < 2 && w != 0; ++i) acc[i] = smfma64(x[p], W[i][p], acc[i], sidx);
      __builtin_amdgcn_sched_barrier(0);
      if (p + NX < 10) x[p + NX] = *(const bf16x8*)(lds + mp + (p + NX) * 1024);
      __builtin_amdgcn_sched_barrier(0);
    }
    STAMP(7);
    if (w != 0) {
      float v[4];
#pragma unroll
      for (int r = 0; r < 4; ++r) {
        const auto sw = __builtin_amdgcn_permlane32_swap(f2u(acc[w == 1][r]), f2u(acc[1][r]), false, false);
        v[r] = (u2f(sw[0]) + u2f(sw[1])) * inv;
      }
      *(u32x2*)(lds + vp) = (u32x2){pack2(v[0], v[1]), pack2(v[2], v[3])};
    } else {
      const int t = (int)f2u(fmx(fmx(acc[0][0], acc[0][1]), fmx(acc[0][2], acc[0][3])) * cm2);
      const int et = wave_imax(t) >> 23, em = ((et + ek) >> 1) + ei;
      int e = imax(et == 255 ? 256 : em, ex);
      PIN_S(e);
      e = imin(e, 256);
      ei = e - 31;
      *(unsigned*)(lds + ip) = (unsigned)ei << 23;
      const u32x3 sr = {(unsigned)(285 - e) << 23, (unsigned)(e - 87) < 79u ? REC_RNG << 23 : 0u, (unsigned)(s + 1)};
      asm volatile("ds_write_b96 %0, %1 offset:%2" :: "v"(lds0), "v"(sr), "n"(L_MISC + 32) : "memory");
    }
    if (last == 0 && w >= 2) asm volatile("s_waitcnt vmcnt(%0)" :: "n"(w == SCH_W ? 2 : 0) : "memory");
    if (w != 0) lds_inc<L_MISC + 28>(lds0);
    STAMP(8);
    if (last) __builtin_amdgcn_s_setprio(0);
    if (last && w == 2) { VM_DRAIN(); lds_inc<L_MISC + 28>(lds0); }
  };
  unsigned* const cntq = (unsigned*)(misc + 12);
  unsigned* const cntt = (unsigned*)(misc + 26 + (w & 1));
  unsigned* const cntp = (unsigned*)(misc + 23);
  unsigned* const cntx2 = (unsigned*)(misc + 28);
  float xn4 = 0.f;
  auto wpf = [&](int s, int lane) {
    touch(a.ws + OFF_WQKVBT + (size_t)(b >> 3) * 76800, (s - WPF0) * (76800 / WPF_N) + imin(lane, 600 / WPF_N - 1) * 128, lds0);
  };
  auto t5_get = [&](int s, int lane) {
    ot.load(a, rs, s - 1 - LAG5, b);
    xn4 = u2f(__hip_atomic_load((const GAS unsigned*)xnrm + (imax(s + 1, 0) * 32 + (lane & 31)) * 4, RLX_AGENT));
    __builtin_amdgcn_sched_barrier(0);
  };
  Sched sch;
  auto late = [&](int s, int lane) {
    if (w == SCH_W && SCH_LATE && __builtin_expect(s >= 0, 1)) *(u32x2*)(misc + 24) = (u32x2){sch.run(a, rs, s, b, lane), (unsigned)(s + 1)};
    if ((w == 2 && F6) || (w == 0 && F4) || (w == 1 && F5) || (w == 3 && F7)) { lds_spin<L_MISC + 48, 1>(lds0, 2u * (unsigned)imax(s + 1, 0)); if (FB_SL && w != 2) __builtin_amdgcn_s_sleep(FB_SL); }
    if (w >= 2) { x_fetch_n<w == 2 ? F6 : F7>(x_src(a, s + 3, b), lds0, s + 3, F4 + F5 + 2 * F67 + (w == 3 ? F6 : 0), lane * 16); return; }
    unsigned sd = 0u;
    if ((w == 0 && XPF == 3) || XPF == 4 || (w == 1 && XPF == 5)) stouch<10>(a.ws + OFF_XN + (size_t)imin(s + 4, NSEG - 1) * (s < 1 ? 0 : 40960) + (b >> 3) * 1280 + w * 64, sd);
    if (w == 0 && WPF_W == 4 && __builtin_expect(s >= WPF0 && s < WPF0 + WPF_N, 0))
      stouch<600 / WPF_N>(a.ws + OFF_WQKVBT + (size_t)(b >> 3) * 76800 + (s - WPF0) * (76800 / WPF_N), sd);
    x_fetch_n<w == 0 ? F4 : F5>(x_src(a, s + 3, b), lds0, s + 3, w == 0 ? 0 : F4, lane * 16);
    if (WPF_W == 0 && w == 0 && __builtin_expect(s >= WPF0 && s < WPF0 + WPF_N, 0)) wpf(s, lane);
    if (w == 1 && __builtin_expect(s == CPF_S, 0)) code_touch(b, lane, lds0);
    lds_spin<L_MISC + 104 + 4 * (w & 1), PS_SL>(lds0, (unsigned)imax(s + 1, 0));
    asm volatile("" :: "s"(sd));
  };
  auto xn_put = [&](int s) {
    float xn2 = xn4;
    xn2 = fmx(xn2, dppf<DPP_XOR1>(xn2)); xn2 = fmx(xn2, dppf<DPP_XOR2>(xn2));
    xn2 = fmx(xn2, dppf<DPP_ROR4>(xn2)); xn2 = fmx(xn2, dppf<DPP_ROR8>(xn2));
    misc[s & 3] = x16mx(xn2);
  };
  if (w == 2) { ot.init(b, lane0); ot.out = (u32x2){0u, 0u}; ot.xo = ot.out; ot.y[0] = ot.y[1] = ot.y[2] = ot.y[3] = (u32x4){0u, 0u, 0u, 0u}; }
  VM_DRAIN();
  lds_arrive(cnt, lane0);
  lds_wait(cnt, 8u);
  sch.tsv = sch.rec = (u32x4){0u, 0u, 0u, 0u};
  sch.side = __builtin_amdgcn_readfirstlane(*(const unsigned*)(misc + 11)) & 8192u;
  sch.o = 0u;
  int sv = NV - 1;
  CODE_PAD((PADT >> (4 * w)) & 15);
#pragma unroll 1
  for (int s = -1; s < NSEG - 1; ++s) {
    const int sv1 = sv == NV - 1 ? 0 : sv + 1;
    FRESH_LANE();
    STAMP(0);
    mma_seg(s + 1, sv1, lane, [&]() { STAMP(1); late(s - 1, lane); if (w == SCH_W) sch.pre(s); STAMP(9); });
    STAMP(2);
    __builtin_amdgcn_sched_barrier(0);
    VM_DRAIN();
    if (w == 2) { ot.run(lane); xn_put(s); }
    STAMP(10);
    st_out(s, lane, [&]() { lds_spin<L_MISC + 92, W1_SL>(lds0, 4u * (unsigned)imax(s, 0)); });
    if (w == 2) t5_get(s, lane);
    __builtin_amdgcn_sched_barrier(0);
    if (w >= 2) vo_pre(s, sv, lane);
    STAMP(3);
    BAR();
    STAMP(4);
    if (__builtin_expect(s >= 0, 1)) {
      if (w == SCH_W) { sch.load(rs, lane0); __builtin_amdgcn_sched_barrier(0); }
      if (w < 2) vo_pre(s, sv, lane);
      vo_mem(s, std::integral_constant<int, 0>{});
      STAMP(6);
      if (w == SCH_W && !SCH_LATE) {
        __builtin_amdgcn_s_setprio(SCH_P);
        *(u32x2*)(misc + 24) = (u32x2){sch.run(a, rs, s, b, lane0), (unsigned)(s + 1)};
      }
      STAMP(11);
    } else BAR();
    __builtin_amdgcn_s_setprio(MMA_P);
    __builtin_amdgcn_sched_barrier(0);
    sv = sv1;
  }
  __builtin_amdgcn_s_setprio(0);
  STAMP_OUT();
  late(NSEG - 2, lane0);
  if (w >= 2) {
    lds_wait(cntp, 4u * (NSEG - 1));
    if (w == 2) { ot.run(lane0); ot.load(a, rs, NSEG - 2 - LAG5, b); }
    if (w == 2) { const u32x2 keep = ot.out; ot.run(lane0); ot.store(a, NSEG - 2 - LAG5, b, lane0); ot.out = keep; }
    __builtin_amdgcn_sched_barrier(0);
    PvTask pv;
    const unsigned ck2 = __builtin_amdgcn_readfirstlane(*(const unsigned*)(misc + 11));
    pv.load(rs, ck2, NSEG - 2 - LAG4, gp, w & 1, lane0);
    st_out(NSEG - 1, lane0, []() {});
    for (unsigned spins = 0; !pv.ready() && spins < SPIN_CAP; ++spins) { asm volatile("" ::: "memory"); pv.load(rs, ck2, NSEG - 2 - LAG4, gp, w & 1, lane0); }
    pv.run(lds, rs, NSEG - 2 - LAG4, gp, m, w & 1, lane0, true);
  }
  if (w != 0) vo_pre(NSEG - 1, (NSEG - 1) % NV, lane0);
  BAR();
  if (w != 0) vo_mem(NSEG - 1, std::integral_constant<int, 1>{});
}

__device__ __forceinline__ void qkv_tail(const RecArgs& a, char* lds, const int w) {
  const int lane0 = __builtin_amdgcn_workitem_id_x() & 63, b = __builtin_amdgcn_workgroup_id_x();
  const int r = qkv_row(b), cb = (b >> 3) - 15 * (r & 1);
  if (cb >= 15) return;
  const unsigned lds0 = (unsigned)(uintptr_t)(LAS char*)lds;
  unsigned* const stage = (unsigned*)(lds + L_MISC) + 17;
  unsigned* const cnty = stage + 1;
  const f32x4 Z4 = {0.f, 0.f, 0.f, 0.f};
  auto fetch = [&](int i) {
    x_fetch_n<10>(x_src(a, NSEG + i, b), lds0, i, 10 * w, lane0 * 16);
  };
  unsigned want = 0;
  auto sync = [&]() { VM_DRAIN(); want += 4u; lds_inc<L_MISC + 72>(lds0); while (__hip_atomic_load(cnty, __ATOMIC_RELAXED, __HIP_MEMORY_SCOPE_WORKGROUP) < want) { } asm volatile("" ::: "memory"); };
  LOGQ(0);
  bf16x8 W[2][20];
  const __amdgpu_buffer_rsrc_t rw = __builtin_amdgcn_make_buffer_rsrc(a.ws + OFF_WQKVBT + (size_t)(cb * 8 + 2 * w) * 20480, 0, 40960, 0x00020000);
  auto w_get = [&](int ks) {
#pragma unroll
    for (int i = 0; i < 2; ++i) W[i][ks] = as8(__builtin_amdgcn_raw_buffer_load_b128(rw, lane0 * 16 + (ks & 3) * 1024, (i * 20 + (ks & ~3)) * 1024, 0));
    __builtin_amdgcn_sched_barrier(0);
  };
#pragma unroll
  for (int ks = 0; ks < QW; ++ks) w_get(ks);
  bf16x8 ones = mk8(0x3f803f80u, 0x3f803f80u, 0x3f803f80u, 0x3f803f80u);
  PIN_V(ones);
  const __amdgpu_buffer_rsrc_t ro = __builtin_amdgcn_make_buffer_rsrc(a.ws + OFF_QB, 0, (int)(OFF_OB - OFF_QB), 0x00020000);
  const int t0 = w & 1, col0 = (cb % 5) * 128 + w * 32;
  auto body = [&](auto SW, auto SQ) {
    unsigned vo[2][2];
    {
      FRESH_LANE();
#pragma unroll
      for (int i = 0; i < 2; ++i)
#pragma unroll
        for (int tt = 0; tt < 2; ++tt) {
          const int tq = tt ^ t0, tk = tq * 16 + c, cc = col0 + i * 16 + (SW ? 4 * g : c), hd = cc / BD, e = cc - hd * BD;
          const int kv = (int)(OFF_KV - OFF_QB) + hd * 64 * KVT;
          vo[i][tt] = (unsigned)(!SW ? kv + 1280 + tq * 672 + ((g >> 1) * 21 + e) * 16 + (g & 1) * 8
                                     : (cb < 5 ? (hd * S_ + tk) * 2 * QST + e * 2 : kv + (e < 16 ? (tk + 32 * (e >> 3)) * 16 + (e & 7) * 2 : 1024 + tk * 8)));
          PIN_V(vo[i][tt]);
        }
    }
    const float qs = cb < 5 ? 0.22360679774997896f * 1.4426950408889634f : 1.0f;
    f32x4 cs[2] = {Z4, Z4};
    LOGQ(1);
    f32x4 acc[2][2], st;
    auto mma = [&](int it, auto FIRST) {
      const char* xp0 = lds + L_XN + (it & 1) * 40960 + t0 * 20480 + lane0 * 16;
      const char* xp1 = lds + L_XN + (it & 1) * 40960 + (1 - t0) * 20480 + lane0 * 16;
      acc[0][0] = acc[0][1] = acc[1][0] = acc[1][1] = st = Z4;
      bf16x8 x[4][2];
#pragma unroll
      for (int p = 0; p < 3; ++p) { x[p][0] = *(const bf16x8*)(xp0 + p * 1024); x[p][1] = *(const bf16x8*)(xp1 + p * 1024); }
      __builtin_amdgcn_sched_barrier(0);
#pragma unroll
      for (int ks = 0; ks < 20; ++ks) {
        if (FIRST && ks + QW < 20) w_get(ks + QW);
#pragma unroll
        for (int q = 0; q < 4; ++q) {
          const int tt = 1 - (q >> 1), i = 1 - (q & 1);
          acc[i][tt] = SW ? mfma32(W[i][ks], x[ks & 3][tt], acc[i][tt]) : mfma32(x[ks & 3][tt], W[i][ks], acc[i][tt]);
          __builtin_amdgcn_sched_barrier(0);
          if (q < 2 && ks + 3 < 20) x[(ks + 3) & 3][q] = *(const bf16x8*)((q ? xp1 : xp0) + (ks + 3) * 1024);
          __builtin_amdgcn_sched_barrier(0);
        }
        st = mfma32(SQ ? x[ks & 3][0] : ones, x[ks & 3][0], st);
        if (FIRST) {
#pragma unroll
          for (int i = 0; i < 2; ++i) cs[i] = SW ? mfma32(W[i][ks], ones, cs[i]) : mfma32(ones, W[i][ks], cs[i]);
        }
        __builtin_amdgcn_sched_barrier(0);
      }
    };
    mma(0, std::integral_constant<bool, true>{});
#pragma unroll 1
    for (int it = 0; it < 4; ++it) {
      if (it) mma(it, std::integral_constant<bool, false>{});
      LOGQ(2 + 3 * it);
      FRESH_LANE();
      float* const sp = (float*)(lds + L_ST) + (it & 1) * 64;
      if (SQ) { if ((c >> 2) == g) sp[32 + t0 * 16 + c] = (c & 3) == 0 ? st[0] : ((c & 3) == 1 ? st[1] : ((c & 3) == 2 ? st[2] : st[3])); }
      else if (g == 0) sp[t0 * 16 + c] = st[0];
      sync();
      LOGQ(3 + 3 * it);
      if (it == 0) fetch(2);
      const bool late = r + 48 >= 55;
      if (it == 1 && !late) fetch(3);
      const bool now = it == 2 && late && __builtin_amdgcn_readfirstlane(*(volatile unsigned*)stage) != 0u;
      if (now) fetch(3);
      const unsigned so = (unsigned)((r + 16 * it) * (cb < 5 ? 64 * QST : KVT));
      auto stat = [&](float sm, float sq, float& mean, float& rstd) {
        mean = sm * (1.0f / DM);
        rstd = __builtin_amdgcn_rsqf(__builtin_fmaxf(sq * (1.0f / DM) - mean * mean, 0.f) + 1e-5f);
      };
#pragma unroll
      for (int tt = 0; tt < 2; ++tt) {
        const int tq = tt ^ t0;
        float mean[4], rstd[4];
        if (SW) {
          stat(sp[tq * 16 + c], sp[32 + tq * 16 + c], mean[0], rstd[0]);
          rstd[0] *= qs;
        } else {
          const f32x4 sm = *(const f32x4*)(sp + tq * 16 + 4 * g), sq = *(const f32x4*)(sp + 32 + tq * 16 + 4 * g);
#pragma unroll
          for (int k = 0; k < 4; ++k) stat(sm[k], sq[k], mean[k], rstd[k]);
        }
#pragma unroll
        for (int i = 0; i < 2; ++i) {
          float v[4];
#pragma unroll
          for (int k = 0; k < 4; ++k) v[k] = (acc[i][tt][k] - mean[SW ? 0 : k] * cs[i][SW ? k : 0]) * rstd[SW ? 0 : k];
          __builtin_amdgcn_raw_buffer_store_b64((u32x2){pack2(v[0], v[1]), pack2(v[2], v[3])}, ro, vo[i][tt], so, 0);
        }
      }
      LOGQ(4 + 3 * it);
      if (it == 2 && late) {
        if (!now) { lds_wait(stage, 1u); fetch(3); }
        sync();
      }
    }
  };
  typedef std::integral_constant<bool, false> F_; typedef std::integral_constant<bool, true> T_;
  if (cb < 10) { if (w >> 1) body(T_{}, T_{}); else body(T_{}, F_{}); }
  else { if (w >> 1) body(F_{}, T_{}); else body(F_{}, F_{}); }
}

struct TokTask {
  u32x4 y[2]; u32x2 ym; u32x4 res;
  unsigned l0, l1, lm, s0, s1;
  static __device__ __forceinline__ bool ok(int sl) { return sl >= 0 && sl < NSEG; }
  __device__ __forceinline__ void init(int h, int j, int lane) {
    const int c = lane & 15, g = lane >> 4;
    l0 = ts_tok(h, g) + (unsigned)(j * 256 + c * 16); l1 = l0 + 16384u;
    lm = ts_mem(h, g + 4 * (c >> 3)) + (unsigned)(j * 64 + (c & 7) * 8);
    res = (u32x4){0u, 0u, 0u, 0u};
    const int qi = 4 * j + (c & 3);
    s0 = g < 2 ? pt_tok(h) + (unsigned)((qi >> 4) * 1024 + ((c >> 2) * 16 + (qi & 15)) * 16 + g * 8) : 0x7ffffff0u;
    s1 = lane < 8 ? pt_mem(h) + (unsigned)((qi >> 4) * 256 + ((c >> 2) * 16 + (qi & 15)) * 8) : 0x7ffffff0u;
    PIN_V(l0); PIN_V(l1); PIN_V(lm); PIN_V(s0); PIN_V(s1);
  }
  __device__ __forceinline__ void load(const __amdgpu_buffer_rsrc_t rs, unsigned o) {
    y[0] = SC1_LOADS(l0, o); y[1] = SC1_LOADS(l1, o);
    ym = __builtin_amdgcn_raw_buffer_load_b64(rs, lm, o, 16);
  }
  __device__ __forceinline__ bool ready() { return wave_all(umin(umin(min4(y[0]), min4(y[1])), umin(ym[0], ym[1])) != 0u); }
  __device__ __forceinline__ void run(int j, int lane, const bf16x8 SelLo) {
    const int c = lane & 15, g = lane >> 4;
    const f32x4 Z4 = {0.f, 0.f, 0.f, 0.f};
    const f32x4 d1 = mfma32(SelLo, as8(y[1]), mfma32(SelLo, as8(y[0]), Z4));
    const f32x4 d0 = mfma32(SelLo, mk8(ym[0], ym[1], 0u, 0u), Z4);
    const int qi = 4 * j + (c & 3), k0 = 16 * g + 4 * (c >> 2);
    float t[4], mv[4];
    float mx = -INFINITY;
#pragma unroll
    for (int r = 0; r < 4; ++r) {
      t[r] = (g < 2 && k0 + r <= qi) ? d1[r] : -INFINITY;
      const float mt = d0[r] + dppf<DPP_ROR8>(d0[r]);
      mv[r] = lane < 8 ? mt : -INFINITY;
      mx = fmx(mx, fmx(t[r], mv[r]));
    }
    mx = fmx(mx, dppf<DPP_ROR8>(mx)); mx = fmx(mx, dppf<DPP_ROR4>(mx));
    mx = x16mx(mx);
    float sum = 0.f;
#pragma unroll
    for (int r = 0; r < 4; ++r) {
      t[r] = __builtin_amdgcn_exp2f(t[r] - mx); mv[r] = __builtin_amdgcn_exp2f(mv[r] - mx);
      sum += t[r] + mv[r];
    }
    sum += dppf<DPP_ROR8>(sum); sum += dppf<DPP_ROR4>(sum);
    sum = x16sum(sum);
    const float rs_ = __builtin_amdgcn_rcpf(sum);
    res = (u32x4){pack2n(t[0] * rs_, t[1] * rs_), pack2n(t[2] * rs_, t[3] * rs_), pack2n(mv[0] * rs_, mv[1] * rs_), pack2n(mv[2] * rs_, mv[3] * rs_)};
    PIN_V(res);
  }
  __device__ __forceinline__ void store(const __amdgpu_buffer_rsrc_t rs, unsigned o, unsigned no = 0u) {
    SC1_STORE8S(((u32x2){res[0], res[1]}), s0 | no, o);
    SC1_STORE8S(((u32x2){res[2], res[3]}), s1 | no, o);
  }
};

__device__ __forceinline__ void t2_mem(const bf16x8 fr, const bf16x8 q0, const bf16x8 q1, const __amdgpu_buffer_rsrc_t rs, unsigned o, int h, int j, float inv, int lane) {
  const f32x4 Z4 = {0.f, 0.f, 0.f, 0.f};
  const f32x4 d0 = mfma32(fr, q0, Z4) * inv, d1 = mfma32(fr, q1, Z4) * inv;
  const unsigned a0 = pack2n(d0[0], d0[1]), a1 = pack2n(d0[2], d0[3]), b0 = pack2n(d1[0], d1[1]), b1 = pack2n(d1[2], d1[3]);
  const auto r0 = __builtin_amdgcn_permlane32_swap(a0, b0, false, false), r1 = __builtin_amdgcn_permlane32_swap(a1, b1, false, false);
  SC1_STORE8S(((u32x2){r0[1], r1[1]}), ts_mem(h, j) + (lane >> 5) * 256 + (lane & 12) * 16 + (lane & 16) * 2 + (lane & 3) * 8, o);
}

template <int w>
__device__ __forceinline__ void rec_waves(const RecArgs& a, char* lds, __amdgpu_buffer_rsrc_t rs) {
  PIN_S(rs);
  const int lane = __builtin_amdgcn_workitem_id_x() & 63, b = __builtin_amdgcn_workgroup_id_x(), c = lane & 15, g = lane >> 4, wid = w; (void)wid;
  const int gp = (b & 7) * 2 + (b >> 7), m = (b >> 3) & 15, h0 = 2 * gp, h = h0 + (m >> 3), j = m & 7;
  float* const misc = (float*)(lds + L_MISC);
  const f32x4 Z4 = {0.f, 0.f, 0.f, 0.f};
  LOGP(0);
  __builtin_amdgcn_s_setprio(3);
  const unsigned lds0 = (unsigned)(uintptr_t)(LAS char*)lds;
  if (!a.layer) img_fetch<12>(a, lds0, h, j, w, lane);
  constexpr int NC = w < 2 ? 2 : 3;
  const int far = ((b >> 2) & 1) ^ 1;
  auto chunk = [&](int f) { return f == 0 ? 4 * far + w : (w == 2 && f == 1 ? 8 + far : (w == 3 && f == 2 ? 9 - far : 4 - 4 * far + w)); };
  bf16x16 Wqk[3][NC];
  u32x4 bm0[NC][2];
#pragma unroll
  for (int f = 0; f < 2 * NC; ++f) bm0[f >> 1][f & 1] = *(const u32x4*)(a.mem0 + (size_t)(c & 7) * DM + 64 * chunk(f >> 1) + 16 * g + 8 * (f & 1));
  if (!a.layer) asm volatile("s_waitcnt vmcnt(0)" ::: "memory");
  BAR();
  LOGP(9);
  const unsigned ck = __builtin_amdgcn_readfirstlane(*(const unsigned*)(misc + 11));
  const unsigned flip = (ck ^ (unsigned)(b << 11)) & 8192u;
#pragma unroll
  for (int t = 0; t < 3; ++t)
#pragma unroll
    for (int f = 0; f < NC; ++f) Wqk[t][f] = cat16(wqk_frag(lds, t, 2 * chunk(f), lane), wqk_frag(lds, t, 2 * chunk(f) + 1, lane));
  bf16x16 We[7];
  auto echunk = [&](int f) { return f < 6 ? (f / 3) * 4 + f % 3 + (f % 3 >= w) : (f == 6 ? 17 - chunk(w - 1) : chunk(f - 7)); };
  if (UNIT && w >= 2) {
#pragma unroll
    for (int f = 0; f < 7; ++f) We[f] = cat16(wqk_frag(lds, 2 * w - 4, 2 * echunk(f), lane), wqk_frag(lds, 2 * w - 4, 2 * echunk(f) + 1, lane));
  }
  auto unit = [&](int seg) {
    if (!UNIT || w < 2) return;
    const char* xp = lds + L_XN + (seg & 1) * 40960 + 20480 + lane * 16;
    bf16x8 x[20];
    f32x4 d = Z4;
#pragma unroll
    for (int k = 0; k < 26; ++k) {
      if (k >= 6) d = mfma32(half8(k - 6 < 14 ? We[(k - 6) >> 1] : Wqk[w < 2 ? 0 : 2 * w - 4][k - 6 < 14 ? 0 : ((k - 6) >> 1) - 7], k & 1), x[k - 6], d);
      __builtin_amdgcn_sched_barrier(0);
      if (k < 20) x[k] = *(const bf16x8*)(xp + echunk(k >> 1) * 2048 + (k & 1) * 1024);
      __builtin_amdgcn_sched_barrier(0);
    }
    if (w == 2) *(u32x2*)(lds + L_QX + (seg % NKQ) * 2048 + 1024 + lane * 16) = (u32x2){pack2(d[0], d[1]), pack2(d[2], d[3])};
    else {
      d *= (g >> 1) ? KEXP : 1.0f;
      char* p = lds + ((g >> 1) ? L_KX : L_QX) + (seg % NKQ) * 2048 + 1024 + (2 * (g & 1) * 16 + c) * 16 + 8;
      *(unsigned*)p = pack2(d[0], d[1]); *(unsigned*)(p + 256) = pack2(d[2], d[3]);
    }
    __builtin_amdgcn_sched_barrier(0);
  };
  if (!a.layer) lds_arrive((unsigned*)(misc + 6), lane);
  else {
    if (w == 0) {
      for (unsigned spins = 0; spins < SPIN_CAP; ++spins) {
        if (__builtin_amdgcn_raw_buffer_load_b32(rs, (b & 7) * 256, (unsigned)OFF_RDY, 16) >= 16u) break;
        __builtin_amdgcn_s_sleep(4);
        asm volatile("" ::: "memory");
      }
      lds_inc<L_MISC + 84>(lds0);
    }
    lds_wait((unsigned*)(misc + 21), 2u);
#pragma unroll
    for (int f = 0; f < 2 * NC; ++f) PIN_V(bm0[f >> 1][f & 1]);
#pragma unroll
    for (int i = 0; i < 10; ++i) x_fetch(x_src(a, 0, b), lds0, 0, 10 * w + i, lane);
  }
  const bf16x8 SelLo = sel_frag(c, 0), SelHi = sel_frag(c, 8);
  bf16x8 ONES = mk8(0x3f803f80u, 0x3f803f80u, 0x3f803f80u, 0x3f803f80u);
  if (w < 2) PIN_V(ONES);
  bool dead = false;
  float inv = 1.0f;
  unsigned* const cnt2 = (unsigned*)(misc + 7);
  unsigned l2off[2 * NC];
#pragma unroll
  for (int f = 0; f < 2 * NC; ++f) { l2off[f] = acc_off(16 * chunk(f >> 1) + 4 * g + 2 * (f & 1) + (c >> 3), c & 7) ^ flip; PIN_V(l2off[f]); }
  int sidx = (c >> 3) ? 0xEEEE : 0x4444;
  PIN_V(sidx);
  const unsigned p1off = (unsigned)((w == 0 ? (h & 2) * 1024 + j * 256 + (h & 1) * 64 + (g & 1) * 8 : (h & 3) * 32768 + j * 512 + (g >> 1) * 256 + (g & 1) * 64) + (c & 4) * 32 + (c & 3) * 16);
  unsigned p1v = (c < 8) && (w != 0 || g >= 2) ? p1off : 0x7ffffff0u, p1cur = 0x7ffffff0u;
  PIN_V(p1v); PIN_V(p1cur);
  const int hd = (c >> 2) & 1, mr = hd ? 3 - (c & 3) : (c & 3);
  const unsigned l1off = (unsigned)(((h0 + hd) & 3) * 32768 + g * 512 + (w & 1) * 128 + (c >> 3) * 64 + mr * 16);
  const unsigned l1offm = (unsigned)((h0 & 2) * 1024 + (g + 4 * (c >> 3)) * 256 + (w & 1) * 128 + hd * 64 + mr * 16);
  const int plane = (lane & ~6) | ((lane & 2) << 1) | ((lane & 4) >> 1);
  unsigned acco = c < 8 ? (acc_off(64 * far + m * 4 + g, 4 * (w & 1) + mr) + (unsigned)(hd * 8)) ^ flip : 0x7ffffff0u;
  unsigned acco2 = c >= 8 ? (acc_off(64 - 64 * far + m * 4 + g, 4 * (w & 1) + mr) + (unsigned)(hd * 8)) ^ flip
                          : (c < 4 ? (acc_off(128 + m * 2 + (g & 1), 4 * (w & 1) + mr) + (unsigned)((g >> 1) * 8)) ^ flip : 0x7ffffff0u);
  if (w < 2) { PIN_V(acco); PIN_V(acco2); }
  unsigned bmo[NC];
#pragma unroll
  for (int f = 0; f < NC; ++f) { bmo[f] = (unsigned)(chunk(f) * 1024 + lane * 16); PIN_V(bmo[f]); }
  bf16x8 bm[NC];
  auto bm_put = [&](int f) { *(bf16x8*)(lds + L_BM + bmo[f]) = bm[f]; __builtin_amdgcn_sched_barrier(0); };
  f32x4 acc[3] = {Z4, Z4, Z4};
  const int rt2 = ((c >> 3) * 8 + 4 * (g & 1)) * 28 + 16 + (c & 7);
  unsigned redo[3] = {(unsigned)(4 * g * 28 + c), (unsigned)(g < 2 ? (4 * g + 2) * 28 + c : rt2), (unsigned)(g < 2 ? (4 * g + 10) * 28 + c : rt2 + 56)};
#pragma unroll
  for (int k = 0; k < 3; ++k) { redo[k] = L_RED1 + w * 1792 + redo[k] * 4; PIN_V(redo[k]); }
  auto red_put = [&]() {
    auto hs = [&](float x, float y) { const auto sw = __builtin_amdgcn_permlane32_swap(f2u(x), f2u(y), false, false); return u2f(sw[0]) + u2f(sw[1]); };
#pragma unroll
    for (int k = 0; k < 3; ++k) {
#pragma unroll
      for (int i = 0; i < 2; ++i) *(float*)(lds + redo[k] + i * 112) = k == 0 ? hs(acc[0][i], acc[1][i]) : hs(acc[k - 1][2 + i], acc[2][2 * k - 2 + i]);
      __builtin_amdgcn_sched_barrier(0);
    }
  };
#pragma unroll
  for (int f = 0; f < NC; ++f) {
    const u32x4 u = bm0[f][0], v = bm0[f][1];
    bm[f] = c < 8 ? mk8(u[0], u[2], v[0], v[2]) : mk8(u[1], u[3], v[1], v[3]);
    bm_put(f);
#pragma unroll
    for (int t = 0; t < 3; ++t) acc[t] = smfma64(bm[f], Wqk[t][f], acc[t], sidx);
  }
  red_put();
  VM_DRAIN();
  if (a.layer) lds_arrive((unsigned*)(misc + 6), lane);
  lds_wait((unsigned*)(misc + 6), 8u);
#pragma unroll
  for (int i = 0; i < 10; ++i) x_fetch(x_src(a, 1, b), lds0, 1, 10 * w + i, lane);
  LOGP(1);
  unit(0);
  BAR();
  STAMP_DECL(); ARRIVE_DECL();
  bf16x8 fr, qx[2];
  auto kq_get = [&](int seg) {
    const char* p = lds + (w == 1 ? L_KX : L_QX) + (seg % NKQ) * 2048 + lane * 16;
    if (w == 1 || w == 2 || (w == 3 && seg == NSEG - 1)) { qx[0] = *(const bf16x8*)p; qx[1] = *(const bf16x8*)(p + 1024); }
  };
  unsigned qo4 = (unsigned)(L_RED1 + (w == 1 ? (c & 7) * 112 + (c >> 3) * 3584 : c * 112) + g * 16), qo2 = qo4 + 64u - (unsigned)(g * 8), qo2b = qo2 + 3584u;
  if (w < 3) { PIN_V(qo4); PIN_V(qo2); if (w != 1) PIN_V(qo2b); }
  auto pub1 = [&](unsigned off, unsigned o1) {
    if (w == 0) fr = qk_frag(lds + qo4, lds + qo2, lds + qo2b);
    else {
      const f32x4 a0 = *(const f32x4*)(lds + qo4), a1 = *(const f32x4*)(lds + qo4 + 1792);
      const f32x2 b0 = *(const f32x2*)(lds + qo2), b1 = *(const f32x2*)(lds + qo2 + 1792);
      float v[6];
#pragma unroll
      for (int i = 0; i < 6; ++i) v[i] = i < 4 ? a0[i] + a1[i] : b0[i - 4] + b1[i - 4];
#pragma unroll
      for (int i = 0; i < 6; ++i) v[i] += dppf<DPP_ROR8>(v[i]);
      fr = mk8(pack2(v[0], v[1]), pack2(v[2], v[3]), pack2(v[4], v[5]), 0u);
    }
    const f32x4 sc = mfma32(w == 0 ? fr : qx[0], fr, Z4);
    if (w == 0) SC1_STORE8S(((u32x2){pack2n(sc[0], sc[1]), pack2n(sc[2], sc[3])}), off, o1);
    else {
      const f32x4 s2 = mfma32(qx[1], fr, Z4);
      __builtin_amdgcn_raw_buffer_store_b128((u32x4){pack2n(sc[0], sc[1]), pack2n(sc[2], sc[3]), pack2n(s2[0], s2[1]), pack2n(s2[2], s2[3])}, rs, off, o1, 16);
    }
  };
  VM_DRAIN();
  kq_get(0);
  unsigned o1 = sct_slot(ck, 0u), o1m = scm_slot(ck, 0u);
  unsigned t2o = ts_tok(h, j) + (unsigned)((c >> 2) * 256 + g * 64 + (c & 3) * 16);
  TokScores t2;
  if (w == 3) { PIN_V(t2o); t2.get(lds, 0, lane); t2.run(0); t2.run(1); t2.store(rs, t2o, ts_slot(ck, 0)); }
  if (w < 2) pub1(p1v, w == 0 ? o1m : o1);
  const unsigned tst = now32() | 1u;
  if (w == 3 && ST_X) __builtin_amdgcn_raw_buffer_store_b32(tst, rs, lane < 2 ? ts_piece(b >> 2) - ACC_TS + (b & 3) * 4 + lane * 8192 : 0x7ffffff0, (unsigned)(OFF_ACC + (SR - 1) * ACC_STEP) + ACC_TS, 16);
  LOGP(2);
  BAR();
  LOGP(3);
#pragma unroll
  for (int i = 0; i < 10; ++i) x_fetch(x_src(a, 2, b), lds0, 2, 10 * w + i, lane);
  unit(1);
  VM_DRAIN();
  unsigned o2 = (unsigned)OFF_ACC, o2p = 0u, tw = 0u;
  asm volatile("" : "+s"(o2p));
  unsigned tc0 = 0u, tstar = 0u;
  int dmin = 0, dmax = 0;
  int e1 = 100000;
  unsigned offs = 100u;
  TokTask tk;
  OutTask ot;
  PvTask pv;
  PvTask2 pv2;
  if (w == 3) pv2.init(gp, lane);
  if (w == 2) tk.init(h, j, lane);
  unsigned o4 = 0u;
  unsigned o3, o3p;
  u32x16 P = {0u, 0u, 0u, 0u, 0u, 0u, 0u, 0u, 0u, 0u, 0u, 0u, 0u, 0u, 0u, 0u}, V = P;
  u32x8 V8 = {0u, 0u, 0u, 0u, 0u, 0u, 0u, 0u};
  if (w < 2) asm volatile("" : "+{v[208:223]}"(P), "+{v[184:191]}"(V8), "+{v[192:207]}"(V));
  unsigned tso = lane < 2 ? ts_piece(b >> 2) + (unsigned)((b & 3) * 4 + lane * 8192) : 0x7ffffff0u;
  if (w < 2) PIN_V(tso);
  float minf = g >= 2 ? -INFINITY : 0.f;
  if (w < 2) PIN_V(minf);
  int sv = NV - 1;
  bf16x8 vox[5];
  unsigned vma[3], need2;
  u32x4 slo, shi;
  float k1;
  const u16* xsrc;
  float invv = 1.0f;
  auto prep0 = [&](int s) {
    xsrc = x_src(a, s + 3, b);
    if (w == 2) { o3p = ts_slot(ck, (s + 1 - LAG3) & 3); o3 = ts_slot(ck, s & 3); o4 = pt_slot(ck, imax(s - LAG3, 0)); PIN_S(o3); }
    if (w == 3) { o3p = pt_slot(ck, (s - LAG4) & 3); o4 = ts_slot(ck, s + 1); }
    PIN_S(xsrc); PIN_S(o3p); PIN_S(o4);
    __builtin_amdgcn_sched_barrier(0);
  };
  auto prep = [&](int s) {
    o2 = (unsigned)OFF_ACC + (unsigned)(s % SR) * (unsigned)ACC_STEP;
    sv = sv == NV - 1 ? 0 : sv + 1;
    PIN_S(o2); PIN_S(sv);
    if (w < 2) {
      PIN_S(tc0);
      o1 = sct_slot(ck, s % SR); o1m = scm_slot(ck, s % SR);
      need2 = 4u * (unsigned)(s + 1) + 2u * ((unsigned)(s + 2) / NSEG);
      PIN_S(o1); PIN_S(o1m); PIN_S(need2);
      __builtin_amdgcn_sched_barrier(0);
      kq_get(s);
    }
    invv = misc[4 + (s & 1)];
  };
  auto prep2 = [&](int s) {
#pragma unroll
    for (int k = 0; k < 3; ++k) {
      const int t = k == 0 ? 2 : (k == 1 ? far : 1 - far);
      const char* p = lds + L_VOX + sv * 5120 + t * 1024 + (plane & 47) * 16 + (g & 1) * 8;
      auto rd = [&](const char* q) { const u32x2 a0 = *(const u32x2*)q, a1 = *(const u32x2*)(q + 256); return mk8(a0[0], a0[1], a1[0], a1[1]); };
      vox[k == 0 ? 0 : 2 * k - 1] = rd(p);
      if (k) vox[2 * k] = rd(p + 3072);
      vma[k] = lds0 + (unsigned)(L_VOM + sv * 1280 + t * 256 + (plane & 31) * 8);
      PIN_V(vma[k]);
    }
    __builtin_amdgcn_sched_barrier(0);
    inv = u2f(__builtin_amdgcn_readfirstlane(f2u(invv)));
    slo = __builtin_bit_cast(u32x4, SelLo); shi = __builtin_bit_cast(u32x4, SelHi);
#pragma unroll
    for (int i = 0; i < 4; ++i) { slo[i] = ((slo[i] >> 7) & 0x10001u) * (f2u(inv) >> 16); shi[i] = ((shi[i] >> 7) & 0x10001u) * (f2u(inv) >> 16); }
    if (F0A) { xsrc = x_src(a, s + 3, b); PIN_S(xsrc); }
    k1 = KEXP * inv;
    PIN_V(k1); PIN_V(slo); PIN_V(shi);
    asm volatile("" : "+v"(vox[0]), "+v"(vox[1]), "+v"(vox[2]), "+v"(vox[3]), "+v"(vox[4]));
    __builtin_amdgcn_sched_barrier(0);
  };
  auto rows = [&](int s) {
    const int dl = spin_until(tw);
    if (w < 2) tc0 = cyc32();
    __builtin_amdgcn_sched_barrier(0);
    STAMP(1);
    unsigned spins = 0;
    auto pass = [&](bool first) {
      u32x4 x[2 * NC];
#pragma unroll
      for (int f = 0; f < NC; ++f) {
        if (f && w < 2 && first) { prep(s); asm volatile(".rept %0\n\ts_nop 3\n\t.endr" :: "n"(P2_G1)); }
        else if (f) asm volatile(".rept %0\n\ts_nop 3\n\t.endr" :: "n"(w < 2 ? P2_G0 : (f == 1 ? P2_G2 : P2_G3)));
        __builtin_amdgcn_sched_barrier(0);
        x[2 * f] = SC1_LOADS(l2off[2 * f], o2p); x[2 * f + 1] = SC1_LOADS(l2off[2 * f + 1], o2p);
        __builtin_amdgcn_sched_barrier(0);
      }
      if (first) lds_inc<L_MISC + 92>(lds0);
      if (first) { if (w >= 2) prep(s); else prep2(s); }
      if ((w == 2 || (w == 3 && !TS_B)) && first && P2_W) lds_spin<L_MISC + 92, 0>(lds0, 4u * (unsigned)s);
      if (w == 2 && first) tk.store(rs, o4, s > LAG3 ? 0u : 0x7ff00000u);
      if (w == 3 && first && !TS_B) t2.store(rs, t2o, o4);
      if (first && w < 2) {
        p1cur = p1v; offs = (offs - (unsigned)dl) & ~3u;
        PIN_V(p1cur); PIN_S(offs);
        tstar = tc0 + (unsigned)(e1 + D1_M + (w == 1 ? D1_W1 : 0)) - offs * 20u;
        dmin = offs < 40u ? D1_MIN : REC_D1;
        dmin = s < EARLY_N ? REC_D1 + EARLY_D1 : dmin;
        dmax = REC_D1;
        PIN_S(tstar); PIN_S(dmin); PIN_S(dmax);
      }
      __builtin_amdgcn_sched_barrier(0);
      unsigned ok = 32u;
#pragma unroll
      for (int t = 0; t < 3; ++t) { acc[t] = Z4; PIN_V(acc[t]); }
      __builtin_amdgcn_sched_barrier(0);
      bool r = true;
#pragma unroll
      for (int f = 0; f < NC; ++f) {
        unsigned p[4];
#pragma unroll
        for (int k = 0; k < 2; ++k) {
          const u32x4 v = x[2 * f + k];
          p[2 * k] = pack2((float)(int)v[0], (float)(int)v[1]); p[2 * k + 1] = pack2((float)(int)v[2], (float)(int)v[3]);
          asm volatile("" : "+v"(p[2 * k]), "+v"(p[2 * k + 1]));
          __builtin_amdgcn_sched_barrier(0);
        }
        ok = (ok & x[2 * f][0] & x[2 * f][2]) & (x[2 * f + 1][0] & x[2 * f + 1][2]);
        PIN_V(ok);
        __builtin_amdgcn_sched_barrier(0);
        bm[f] = mk8(p[0], p[1], p[2], p[3]);
        acc[0] = smfma64(bm[f], Wqk[0][f], acc[0], sidx);
        __builtin_amdgcn_sched_barrier(0);
        bm_put(f);
        acc[1] = smfma64(bm[f], Wqk[1][f], acc[1], sidx);
        __builtin_amdgcn_sched_barrier(0);
        if (f == NC - 1) { u64 bad = __builtin_amdgcn_ballot_w64(ok == 0u); PIN_S(bad); r = bad == 0ull; }
        __builtin_amdgcn_sched_barrier(0);
        acc[2] = smfma64(bm[f], Wqk[2][f], acc[2], sidx);
        __builtin_amdgcn_sched_barrier(0);
      }
      red_put();
      return r;
    };
    if (__builtin_expect(!pass(true), 0)) {
      FC(0);
      do {
        if (dead || ++spins > SPIN_CAP) { dead = true; break; }
        asm volatile("" ::: "memory");
      } while (!pass(false));
    }
    STAMPV(12, spins); LOGF(1, spins);
    TSTAMP(2, now32()); TSTAMP(3, spins);
  };
  if (w >= 2) prep0(-1);
  prep(0);
  if (w < 2) prep2(0);
  CODE_PAD((PADR >> (4 * w)) & 15);
#pragma unroll 1
  for (int s = 0; s < NSEG - 1; ++s) {
    if (__builtin_expect(o2p != 0u, 1)) rows(s);
    STAMP(2);
    BAR();
    STAMP(3);
    if (w < 2) pub1(p1cur, w == 0 ? o1m : o1);
    if (w == 2) tk.load(rs, o3p);
    if (w == 3) pv2.load(rs, o3p);
    const unsigned tp1 = cyc32();
    __builtin_amdgcn_sched_barrier(0);
    if (w == 2) {
      __builtin_amdgcn_s_setprio(W2_P);
      prep0(s);
      if (W2_D) spin_cyc(tp1 + W2_D);
      fr = qk_frag(lds + qo4, lds + qo2, lds + qo2b);
      t2_mem(fr, qx[0], qx[1], rs, o3, h, j, invv * KEXP, lane);
      __builtin_amdgcn_sched_barrier(0);
      asm volatile("s_waitcnt vmcnt(1)" ::: "memory");
      lds_inc<L_MISC + 28>(lds0);
      kq_get(s + 1);
    }
    if (w == 3) {
      __builtin_amdgcn_s_setprio(T4_P);
      prep0(s);
      t2.get(lds, s + 1, lane); t2.run(0); t2.run(1);
      __builtin_amdgcn_sched_barrier(0);
    }
    if (w >= 2 && __builtin_expect(s == NSEG - 2, 0)) { VM_DRAIN(); lds_inc<L_MISC + 28>(lds0); }
    if (w >= 2) { __builtin_amdgcn_s_setprio(UN_P); unit(s + 2); }
    TSTAMP(0, now32());
    STAMP(4);
    __builtin_amdgcn_sched_barrier(0);
    unsigned tpub = 0;
    u32x2 sw;
    if (w == 2) {
      STAMP(5);
      __builtin_amdgcn_sched_barrier(0);
      STAMP(6);
      ARRIVE(0);
      if (FB_E) { PIN_V(tk.y[0]); PIN_V(tk.y[1]); PIN_V(tk.ym); }
      else tk.run(j, lane, SelLo);
      if (F2B) { lds_spin<L_MISC + 48, 1>(lds0, 2u * (unsigned)(s + 1)); x_fetch_n<F2B>(xsrc, lds0, s + 3, P2B, lane * 16); }
      if (FB_E) tk.run(j, lane, SelLo);
      __builtin_amdgcn_s_setprio(SP_P);
    }
    if (w == 3) {
      if (F3A) { spin_cyc(tp1 + F3A_D); x_fetch_n<F3A>(xsrc, lds0, s + 3, 40 - F3C - F3 - F3A, lane * 16); }
      if (TS_B == 1) { PIN_V(pv2.pa[1]); lds_spin<L_MISC + 48, 1>(lds0, 2u * (unsigned)(s + 1)); t2.store(rs, t2o, o4); }
      if (F3 && FB_E) { VM_DRAIN(); lds_spin<L_MISC + 48, 1>(lds0, 2u * (unsigned)(s + 1)); x_fetch_n<F3>(xsrc, lds0, s + 3, 40 - F3C - F3, lane * 16); }
      pv2.run(lds, sv < 1 + LAG4 ? sv + NV - 1 - LAG4 : sv - 1 - LAG4, lane);
      if (TS_B == 2) t2.store(rs, t2o, o4);
      if (ST_E) lds_inc<L_MISC + 112>(lds0);
      STAMP(11);
      __builtin_amdgcn_s_setprio(SP_P);
      STAMP(5);
      ARRIVE(0);
      if (F3 && !FB_E) { lds_spin<L_MISC + 48, 1>(lds0, 2u * (unsigned)(s + 1)); x_fetch_n<F3>(xsrc, lds0, s + 3, 40 - F3C - F3, lane * 16); }
      STAMP(6);
    }
    if (w < 2) {
      __builtin_amdgcn_s_setprio(D1_P);
      x_fetch_n<F0A>(xsrc, lds0, s + 3, PA + w * F0A, lane * 16);
      int dq = imin((int)(tstar - tp1), dmax);
      dq = dq < -D1_LATE ? dmax : dq;
      PIN_S(dq);
      spin_exact(tp1 + (unsigned)imax(dq, dmin));
      STAMP(5);
      u32x32 S;
      auto sc_load = [&]() {
        asm volatile("buffer_load_dwordx4 v[224:227], %2, %4, %5 offen sc1\n\t.rept %7\n\ts_nop 3\n\t.endr\n\t"
                     "buffer_load_dwordx4 v[228:231], %3, %4, %6 offen sc1\n\tbuffer_load_dwordx4 v[232:235], %3, %4, %6 offen offset:2048 sc1\n\t"
                     "buffer_load_dwordx4 v[236:239], %3, %4, %6 offen offset:256 sc1\n\tbuffer_load_dwordx4 v[240:243], %3, %4, %6 offen offset:2304 sc1\n\t"
                     "v_mov_b32 v220, 0x42000000\n\tv_mov_b32 v221, 0\n\tv_mov_b32 v222, 0x42000000\n\tv_mov_b32 v223, 0"
                     : "=&{v[224:255]}"(S), "+{v[208:223]}"(P) : "v"(l1offm), "v"(l1off), "s"(rs), "s"(o1m), "s"(o1), "n"(P1_G) : "memory");
      };
      sc_load();
      __builtin_amdgcn_sched_barrier(0);
      lds_inc<L_MISC + 48>(lds0);
      STAMP(6);
      ARRIVE(0);
      lds_spin<L_MISC + 28, 0>(lds0, need2);
      __builtin_amdgcn_s_setprio(3);
      asm volatile("ds_read_b64 v[192:193], %3\n\tds_read_b64 v[196:197], %3 offset:768\n\tds_read_b64 v[200:201], %4\n\tds_read_b64 v[204:205], %4 offset:768\n\tds_read_b64 v[184:185], %2"
                   : "+{v[184:191]}"(V8), "+{v[192:207]}"(V) : "v"(vma[0]), "v"(vma[1]), "v"(vma[2]) : "memory");
      __builtin_amdgcn_sched_barrier(0);
      {
        const int de = imin((int)(tp1 - tc0) - e1, D1_C);
        e1 += s == 0 ? 0 : de >> (s < EARLY_N && de < -D1_C ? 0 : D1_G);
        PIN_S(e1);
      }
      STAMP(11);
      __builtin_amdgcn_sched_barrier(0);
      {
        unsigned spins = 0;
        u64 miss, bad;
#define MM(d, a, b, c) "v_mfma_f32_16x16x32_bf16 " d ", " a ", " b ", " c "\n\t"
#define FM(d, x, r, ctrl) "v_fmac_f32_dpp " d ", " x ", " r " " ctrl "\n\t"
#define ID_ "quad_perm:[0,1,2,3] row_mask:0xf bank_mask:"
#define HM_ "row_half_mirror row_mask:0xf bank_mask:"
#define R8_ "row_ror:8 row_mask:0xf bank_mask:"
#define MI_ "row_mirror row_mask:0xf bank_mask:"
        auto soft = [&]() {
          asm volatile(
            "s_waitcnt vmcnt(4)\n\t" MM("v[244:247]", "%6", "v[224:227]", "0") "v_min3_u32 v214, v224, v225, v226\n\t"
            "s_waitcnt vmcnt(3)\n\t" MM("v[248:251]", "%6", "v[228:231]", "0") "v_min3_u32 v214, v214, v227, v228\n\tv_min3_u32 v214, v214, v229, v230\n\t"
            "s_waitcnt vmcnt(2)\n\t" MM("v[248:251]", "%6", "v[232:235]", "v[248:251]") "v_min3_u32 v214, v214, v231, v232\n\t"
            "v_add_f32_dpp v212, v244, v244 " R8_ "0x3\n\tv_add_f32_dpp v213, v245, v245 " R8_ "0x3\n\t"
            "s_waitcnt vmcnt(1)\n\t" MM("v[248:251]", "%7", "v[236:239]", "v[248:251]")
            "v_add_f32_dpp v212, v246, v246 " R8_ "0xc\n\tv_add_f32_dpp v213, v247, v247 " R8_ "0xc\n\t"
            "s_waitcnt vmcnt(0) lgkmcnt(0)\n\t" MM("v[248:251]", "%7", "v[240:243]", "v[248:251]")
            "v_fma_f32 v212, v212, %14, %15\n\tv_fma_f32 v213, v213, %14, %15\n\tv_exp_f32 v216, v212\n\tv_exp_f32 v217, v213\n\t"
            "v_min3_u32 v214, v214, v233, v234\n\tv_cvt_pk_bf16_f32 v208, v216, v217\n\tv_min3_u32 v214, v214, v235, v236\n\tds_read_b96 v[188:190], %16 offset:%18\n\t"
            "v_exp_f32 v216, v248\n\tv_mov_b32_dpp v209, v208 " R8_ "0xf\n\tv_exp_f32 v217, v249\n\tv_exp_f32 v218, v250\n\t"
            MM("v[252:255]", "%8", "v[208:211]", "0") "v_exp_f32 v219, v251\n\tv_cvt_pk_bf16_f32 v232, v216, v217\n\t"
            MM("v[224:227]", "v[192:195]", "v[208:211]", "0") "v_cvt_pk_bf16_f32 v233, v218, v219\n\tv_min3_u32 v214, v214, v237, v238\n\tv_mov_b32_dpp v234, v232 " R8_ "0xf\n\t"
            MM("v[228:231]", "v[196:199]", "v[208:211]", "0") "v_mov_b32_dpp v235, v233 " R8_ "0xf\n\t"
            MM("v[244:247]", "v[204:207]", "v[208:211]", "0") "v_min3_u32 v214, v214, v239, v240\n\t"
            MM("v[252:255]", "%8", "v[232:235]", "v[252:255]") MM("v[224:227]", "%10", "v[232:235]", "v[224:227]") "v_min3_u32 v214, v214, v241, v242\n\t"
            MM("v[228:231]", "%11", "v[232:235]", "v[228:231]") "s_waitcnt lgkmcnt(0)\n\tv_sub_u32 v215, v190, %17 clamp\n\t"
            MM("v[236:239]", "v[200:203]", "v[208:211]", "0") "v_min3_u32 v214, v214, v243, v215\n\tv_cmp_eq_u32 %4, 0, v214\n\tv_rcp_f32 v216, v252\n\t"
            MM("v[240:243]", "v[184:187]", "v[208:211]", "0") "v_mul_f32 v216, v216, v188\n\tv_subrev_u32 v215, 0x2f800000, v252\n\t"
            MM("v[236:239]", "%12", "v[232:235]", "v[236:239]") FM("v220", "v224", "v216", ID_ "0x1") FM("v221", "v225", "v216", ID_ "0x1")
            "v_mov_b32_dpp v217, v216 " HM_ "0xf\n\t"
            MM("v[244:247]", "%13", "v[232:235]", "v[244:247]") FM("v220", "v230", "v216", ID_ "0x2") FM("v221", "v231", "v216", ID_ "0x2")
            MM("v[240:243]", "%9", "v[232:235]", "v[240:243]") FM("v220", "v228", "v217", HM_ "0x1") FM("v221", "v229", "v217", HM_ "0x1")
            "v_cmp_ge_u32 vcc, v215, v189\n\t" FM("v220", "v226", "v217", HM_ "0x2") FM("v221", "v227", "v217", HM_ "0x2")
            "s_or_b64 %5, %4, vcc"
            : "+{v[224:255]}"(S), "+{v[208:223]}"(P), "+{v[184:191]}"(V8), "+{v[192:207]}"(V), "=&s"(miss), "=&s"(bad)
            : "v"(slo), "v"(shi), "v"(ONES), "v"(vox[0]), "v"(vox[1]), "v"(vox[2]), "v"(vox[3]), "v"(vox[4]), "v"(k1), "v"(minf), "v"(lds0), "s"(s), "n"(L_MISC + 32)
            : "vcc", "scc", "memory");
        };
        soft();
        if (__builtin_expect(bad != 0ull, 0)) {
          if (miss) FC(1); else FC(2);
          while (miss != 0ull && !dead) {
            if (++spins > SPIN_CAP) dead = true;
            asm volatile("s_nop 7\n\ts_nop 7" ::: "memory");
            sc_load(); soft();
          }
          if (bad != 0ull) {
            u32x4 y[5];
#pragma unroll
            for (int k = 0; k < 4; ++k) y[k] = SC1_LOADS(l1off + (k & 1) * 2048 + (k >> 1) * 256, o1);
            y[4] = SC1_LOADS(l1offm, o1m);
            const f32x4 d0 = mfma32(as8(slo), as8(y[4]), Z4);
            f32x4 d1 = mfma32(as8(shi), as8(y[3]), mfma32(as8(shi), as8(y[2]), mfma32(as8(slo), as8(y[1]), mfma32(as8(slo), as8(y[0]), Z4))));
            const bool lo = c < 8;
            const float m0 = __builtin_fmaf((lo ? d0[0] : d0[2]) + dppf<DPP_ROR8>(lo ? d0[2] : d0[0]), k1, minf);
            const float m1 = __builtin_fmaf((lo ? d0[1] : d0[3]) + dppf<DPP_ROR8>(lo ? d0[3] : d0[1]), k1, minf);
            float mx = fmx(fmx(fmx(d1[0], d1[1]), d1[2]), fmx(fmx(d1[3], m0), m1));
            mx = fmx(mx, dppf<DPP_ROR8>(mx));
            mx = x32mx(x16mx(mx));
            d1 -= mx;
            const unsigned mm = pack2(__builtin_amdgcn_exp2f(m0 - mx), __builtin_amdgcn_exp2f(m1 - mx));
            const unsigned t01 = pack2(__builtin_amdgcn_exp2f(d1[0]), __builtin_amdgcn_exp2f(d1[1])), t23 = pack2(__builtin_amdgcn_exp2f(d1[2]), __builtin_amdgcn_exp2f(d1[3]));
            const bf16x8 pm = mk8(mm, dpp_ror8(mm), 0u, 0u), pt = mk8(t01, t23, dpp_ror8(t01), dpp_ror8(t23));
            const float r = __builtin_amdgcn_rcpf(mfma32(ONES, pt, mfma32(ONES, pm, Z4))[0]) * u2f(V8[4]), rm = dppf<0x141>(r);
            f32x4 o[5];
#pragma unroll
            for (int k = 0; k < 5; ++k) o[k] = mfma32(vox[k], pt, mfma32(k ? mk8(V[4 * k - 4], V[4 * k - 3], 0u, 0u) : mk8(V8[0], V8[1], 0u, 0u), pm, Z4));
#pragma unroll
            for (int i = 0; i < 2; ++i) {
              const float oa = dppf<0x141>(o[2][i]), ob = dppf<0x141>(o[1][2 + i]);
              P[12 + i] = f2u(__builtin_fmaf(hd ? ob : oa, rm, __builtin_fmaf(hd ? o[2][2 + i] : o[1][i], r, i ? 0.f : 32.0f)));
            }
            P[8] = f2u(r); P[9] = f2u(rm);
#pragma unroll
            for (int i = 0; i < 4; ++i) { S[12 + i] = f2u(o[3][i]); S[16 + i] = f2u(o[0][i]); S[20 + i] = f2u(o[4][i]); }
            asm volatile("s_nop 7\n\ts_nop 7" : "+{v[224:255]}"(S), "+{v[208:223]}"(P));
          }
        }
        STAMP(7); STAMPV(13, spins); LOGF(0, spins);
        {
          asm volatile("v_cvt_i32_f32 v218, v220\n\tv_cvt_i32_f32 v219, v221\n\tv_and_or_b32 v218, v218, %7, 2\n\tbuffer_atomic_add_x2 v[218:219], %3, %5, %6 offen\n\t"
                       FM("v222", "v236", "v216", R8_ "0x4") FM("v223", "v237", "v216", R8_ "0x4") "ds_read_b64 %2, %8 offset:%9\n\t"
                       FM("v222", "v238", "v217", MI_ "0x8") FM("v223", "v239", "v217", MI_ "0x8") "v_permlane32_swap_b32 v240, v242\n\t"
                       FM("v222", "v244", "v217", MI_ "0x4") FM("v223", "v245", "v217", MI_ "0x4") "v_permlane32_swap_b32 v241, v243\n\t"
                       FM("v222", "v246", "v216", R8_ "0x8") FM("v223", "v247", "v216", R8_ "0x8")
                       FM("v222", "v240", "v216", ID_ "0x1") FM("v223", "v241", "v216", ID_ "0x1") FM("v222", "v242", "v217", HM_ "0x1") FM("v223", "v243", "v217", HM_ "0x1")
                       "v_cvt_i32_f32 v212, v222\n\tv_cvt_i32_f32 v213, v223\n\tv_and_or_b32 v212, v212, %7, 2\n\tbuffer_atomic_add_x2 v[212:213], %4, %5, %6 offen\n\t"
                       "s_waitcnt lgkmcnt(0)"
                       : "+{v[224:255]}"(S), "+{v[208:223]}"(P), "=&v"(sw) : "v"(acco), "v"(acco2), "s"(rs), "s"(o2), "s"(~63u), "v"(lds0), "n"(L_MISC + 96) : "memory");
          __builtin_amdgcn_sched_barrier(0);
          tpub = now32();
          lds_inc<L_MISC + 104 + 4 * (w & 1)>(lds0);
          asm volatile("buffer_atomic_umax %0, %1, %2, %3 offen" :: "v"(tpub | 1u), "v"(tso), "s"(rs), "s"(o2) : "memory");
          STAMP(10);
        }
      }
    }
    STAMP(8);
    ARRIVE(1);
    o2p = o2;
    PIN_S(o2p);
    if (w < 2) {
      if (__builtin_expect(__builtin_amdgcn_readfirstlane(sw[1]) <= (unsigned)s, 0)) {
        lds_spin<L_MISC + 100, 0>(lds0, (unsigned)s + 1u);
        sw[0] = __hip_atomic_load((unsigned*)(misc + 24), __ATOMIC_RELAXED, __HIP_MEMORY_SCOPE_WORKGROUP);
      }
    } else {
      u32x4 q;
      asm volatile("1:\n\tds_read_b128 %0, %1 offset:%3\n\t.if %4\n\ts_sleep 1\n\t.endif\n\ts_waitcnt lgkmcnt(0)\n\tv_min3_u32 v201, v201, v202, v203\n\tv_cmp_ge_u32 vcc, %2, v201\n\ts_cbranch_vccnz 1b"
                   : "=&{v[200:203]}"(q) : "v"(lds0), "s"(s), "n"(L_MISC + 96), "n"(SP_SL) : "vcc", "memory");
      __builtin_amdgcn_s_setprio(3);
      tpub = now32();
      sw[0] = q[0];
    }
    STAMP(9);
    static_assert(F3C == 0 && F2C == 0 && F0C == 0, "nothing behind the atomics");
    LOGT(0, tpub); LOGT(1, tb2_);
    TSTAMP(1, tpub);
    ARRIVE_OUT();
    const unsigned an = __builtin_amdgcn_readfirstlane(sw[0]);
    const int ahead = (int)(an - tpub);
    tw = tpub + (unsigned)(an == 0u || ahead > 1000 ? SCH_FIRST : imax(ahead, w < 2 ? SCH_DMIN : 0));
    offs = tw - an;
  }
  rows(NSEG - 1);
  STAMP_OUT();
  LOGP(4);
  static_assert(LAG3 == 2 && LAG4 == 3 && LAG5 == 6, "drain schedule");
  unsigned* const cntx = (unsigned*)(misc + 16);
  auto poll = [&](auto load, auto ready) {
    load();
    for (unsigned spins = 0; !ready() && !dead;) {
      if (++spins > SPIN_CAP) dead = true;
      asm volatile("" ::: "memory");
      load();
    }
  };
  auto mark = [&](int k) { if (w == 0) *(volatile unsigned*)(misc + k) = cyc32() | 1u; };
  auto after = [&](int k, unsigned d) { lds_wait((unsigned*)(misc + k), 1u); wait_cyc(__builtin_amdgcn_readfirstlane(*(volatile unsigned*)(misc + k)) + d); };
  const int qt = w >> 1;
  auto t4 = [&](int sl) { poll([&]() { pv.load(rs, ck, sl, gp, qt, lane); }, [&]() { return pv.ready(); }); pv.run(lds, rs, sl, gp, m, qt, lane, true); };
  auto t5 = [&](int sl, int part) { poll([&]() { ot.load(a, rs, sl, b, part); }, [&]() { return ot.ready(b); }); ot.run(lane); ot.store(a, sl, b, lane); };
  BAR();
  const unsigned tp1 = cyc32();
  if (w == 2) {
    TokTask tb = tk;
    tb.load(rs, ts_slot(ck, NSEG - 3)); tk.load(rs, ts_slot(ck, NSEG - 2));
    tb.run(j, lane, SelLo); tb.store(rs, pt_slot(ck, NSEG - 3));
  }
  if (w == 3) { kq_get(NSEG - 1); t2_mem(qk_frag(lds + qo4, lds + qo2, lds + qo2b), qx[0], qx[1], rs, ts_slot(ck, NSEG - 1), h, j, invv * KEXP, lane); }
  if (w == 0) tk.init(h, j, lane);
  LOGD(0);
  if (w == 0) {
    ot.init(b, lane);
    wait_cyc(tp1 + END_E1);
    poll([&]() { tk.load(rs, ts_slot(ck, NSEG - 1)); }, [&]() { return tk.ready(); });
    LOGD(1);
  }
  if (!(w & 1)) {
    tk.run(j, lane, SelLo); tk.store(rs, pt_slot(ck, NSEG - 1 - qt));
    mark(14);
    LOGD(2);
    if (w == 2) ot.init(b, lane);
    ot.load(a, rs, NSEG - 4 + w, b, 2);
    lds_wait(cnt2, 4u * NSEG + 2u);
    after(14, 600); wait_cyc(tp1 + END_E2);
    t4(NSEG - 1);
    mark(15);
    LOGD(3);
  } else {
    ot.init(b, lane);
#pragma unroll 1
    for (int i = 0; i < 3; ++i) {
      wait_cyc(tp1 + (i == 0 ? END_E60 : (i == 1 ? END_E61 : END_E62)));
      t4(NSEG - 4 + i);
      if (i == 1 || (i == 2 && w == 1)) t5(i == 1 ? NSEG - 7 + qt : NSEG - 5, 3);
    }
    ot.load(a, rs, NSEG - 4 + w, b, 2);
    LOGP(5);
  }
  after(15, 600); wait_cyc(tp1 + END_E3);
  t5(NSEG - 4 + w, 1);
  LOGD(4);
  VM_DRAIN();
  if (w != 0) lds_inc<L_MISC + 64>(lds0);
  else {
    lds_wait(cntx, 3u);
    LOGD(5);
    if (lane == 0) __hip_atomic_fetch_add((gu64*)((GAS char*)(a.ws + o2) + acc_off(m * 4, 0)), (u64)1, RLX_AGENT);
    wait_until(now32() + SCH_DRAIN);
    wait_cyc(tp1 + END_E4);
    unsigned x;
    poll([&]() { x = __builtin_amdgcn_raw_buffer_load_b32(rs, acc_off(c * 4, 0), o2, 16); }, [&]() { return wave_all((x & 16u) != 0u); });
    lds_inc<L_MISC + 68>(lds0);
    LOGD(6);
  }
  LOGP(8);
}

__device__ __forceinline__ void proj_front(const RecArgs& a, char* lds, const __amdgpu_buffer_rsrc_t rs, const int v) {
  const int lane = __builtin_amdgcn_workitem_id_x() & 63, b = __builtin_amdgcn_workgroup_id_x(), c = lane & 15, g = lane >> 4;
  const int rb = (b & 7) + 8 * (b >> 5), cq = (b >> 3) & 3;
  const unsigned lds0 = (unsigned)(uintptr_t)(LAS char*)lds;
  unsigned* const cnt = (unsigned*)(lds + L_MISC) + 20;
  LOGF2(0);
  float* const red = (float*)(lds + L_RED1);
  float* const fin = red + 640;
  const f32x4 Z4 = {0.f, 0.f, 0.f, 0.f};
#pragma unroll
  for (int i = 0; i < 8; ++i) x_fetch((const u16*)(a.ws + OFF_OB) + (size_t)rb * 32 * DM, lds0, 0, 8 * v + i, lane);
  bf16x8 W[2][20];
#pragma unroll
  for (int k = 0; k < 40; ++k) W[k & 1][k >> 1] = *(const bf16x8*)(a.ws + OFF_WOBT + ((size_t)(cq * 10 + 2 * v + (k & 1)) * 20 + (k >> 1)) * 1024 + lane * 16);
  const unsigned fo = (unsigned)(rb * 40960 + (cq * 5 + v) * 1024 + ((g >> 1) * 16 + c) * 16), f8 = fo + (g & 1) * 8, f16 = fo + (g & 1) * 20480;
  u32x2 xo[2][2];
#pragma unroll
  for (int k = 0; k < 4; ++k)
    xo[k >> 1][k & 1] = __builtin_bit_cast(u32x2, __hip_atomic_load((const gu64*)((const char*)a.xnew + f8 + (k >> 1) * 512 + (k & 1) * 20480), RLX_AGENT));
  asm volatile("s_waitcnt vmcnt(44)" ::: "memory");
  lds_inc<L_MISC + 80>(lds0);
  lds_wait(cnt, 5u);
  LOGF2(1);
  f32x4 acc[2][2] = {{Z4, Z4}, {Z4, Z4}};
#pragma unroll
  for (int ks = 0; ks < 20; ++ks)
#pragma unroll
    for (int t = 0; t < 2; ++t) {
      const bf16x8 x = *(const bf16x8*)(lds + L_XN + (t * 20 + ks) * 1024 + lane * 16);
#pragma unroll
      for (int i = 0; i < 2; ++i) acc[i][t] = mfma32(W[i][ks], x, acc[i][t]);
    }
  u32x2 xp[2][2];
  PIN_V(acc[1][1]);
  LOGF2(2);
#pragma unroll
  for (int k = 0; k < 4; ++k) {
    const int i = k >> 1, t = k & 1;
#pragma unroll
    for (int q = 0; q < 2; ++q) {
      const unsigned r = pack2(acc[i][t][2 * q], acc[i][t][2 * q + 1]), x = xo[i][t][q];
      xp[i][t][q] = pack2(u2f(x << 16) + u2f(r << 16), u2f(x & 0xffff0000u) + u2f(r & 0xffff0000u));
      acc[i][t][2 * q] = u2f(xp[i][t][q] << 16); acc[i][t][2 * q + 1] = u2f(xp[i][t][q] & 0xffff0000u);
    }
    const float ml = x32sum(x16sum((acc[i][t][0] + acc[i][t][1]) + (acc[i][t][2] + acc[i][t][3]))) * (1.0f / 16.0f);
    float q = 0.f;
#pragma unroll
    for (int r = 0; r < 4; ++r) { const float d = acc[i][t][r] - ml; q += d * d; }
    q = x32sum(x16sum(q));
    if (g == 0) { red[(2 * v + i) * 32 + t * 16 + c] = ml; red[320 + (2 * v + i) * 32 + t * 16 + c] = q; }
  }
  lds_inc<L_MISC + 80>(lds0);
  if (v == 0) {
    lds_wait(cnt, 10u);
    LOGF2(3);
    float mw[10], ml = 0.f, m2 = 0.f;
#pragma unroll
    for (int i = 0; i < 10; ++i) { mw[i] = red[i * 32 + (lane & 31)]; ml += mw[i]; m2 += red[320 + i * 32 + (lane & 31)]; }
    ml *= 0.1f;
#pragma unroll
    for (int i = 0; i < 10; ++i) m2 += 16.0f * (mw[i] - ml) * (mw[i] - ml);
    const unsigned xs = (unsigned)OFF_LNX + rb * 1024;
    if (lane < 32) SC1_STORE8S(((u32x2){f2u(ml), nz(f2u(m2))}), (cq * 32 + lane) * 8, xs);
    wait_cyc(cyc32() + FR_D);
    u32x2 x0, x1;
    for (unsigned spins = 0; spins < SPIN_CAP; ++spins) {
      x0 = __builtin_amdgcn_raw_buffer_load_b64(rs, lane * 8, xs, 16); x1 = __builtin_amdgcn_raw_buffer_load_b64(rs, 512 + lane * 8, xs, 16);
      if (wave_all(x0[1] != 0u && x1[1] != 0u)) break;
      asm volatile("" ::: "memory");
    }
    const float m0 = u2f(x0[0]), m1 = u2f(x1[0]);
    const float mean = x32sum(m0 + m1) * 0.25f;
    const float M2 = x32sum((u2f(x0[1]) + u2f(x1[1])) + 160.0f * ((m0 - mean) * (m0 - mean) + (m1 - mean) * (m1 - mean)));
    const float mb = rbf(mean), den = rbf(__builtin_sqrtf(rbf(rbf(M2 * (1.0f / DM)) + 1e-5f)));
    if (lane < 32) {
      fin[lane] = mb; fin[32 + lane] = 1.0f / den;
      if (cq == 0) __hip_atomic_store((GAS unsigned*)(a.ws + OFF_XNRM) + (rb * 32 + lane) * 4, f2u(1.02f * (M2 + DM * (mean - mb) * (mean - mb)) / (den * den)), RLX_AGENT);
    }
    lds_inc<L_MISC + 84>(lds0);
  }
  lds_wait(cnt + 1, 1u);
  LOGF2(4);
  const __amdgpu_buffer_rsrc_t rx = __builtin_amdgcn_make_buffer_rsrc(a.ws + OFF_XN, 0, (int)(OFF_QB - OFF_XN), 0x00020000);
  auto put = [&](const u32x2 p0, const u32x2 p1, unsigned so) {
    const auto r0 = __builtin_amdgcn_permlane16_swap(p0[0], p1[0], false, false), r1 = __builtin_amdgcn_permlane16_swap(p0[1], p1[1], false, false);
    __builtin_amdgcn_raw_buffer_store_b128((u32x4){r0[0], r1[0], r0[1], r1[1]}, rx, f16, so, 16);
  };
#pragma unroll
  for (int i = 0; i < 2; ++i) {
    u32x2 y[2];
#pragma unroll
    for (int t = 0; t < 2; ++t) {
      const float mb = fin[t * 16 + c], rden = fin[32 + t * 16 + c];
      y[t] = (u32x2){pack2(rbf(acc[i][t][0] - mb) * rden, rbf(acc[i][t][1] - mb) * rden), pack2(rbf(acc[i][t][2] - mb) * rden, rbf(acc[i][t][3] - mb) * rden)};
    }
    put(y[0], y[1], i * 512);
  }
  VM_DRAIN();
  lds_inc<L_MISC + 80>(lds0);
#pragma unroll
  for (int i = 0; i < 2; ++i) put(xp[i][0], xp[i][1], (unsigned)(OFF_XB - OFF_XN) + i * 512);
  if (v == 0) {
    lds_wait(cnt, 15u);
    if (rb < 4 && lane < 8) __hip_atomic_fetch_add((GAS unsigned*)(a.ws + OFF_RDY) + lane * 64, 1u, RLX_AGENT);
  }
  LOGF2(5);
}

__global__ __launch_bounds__(512, 2) void rec_kernel(RecArgs a) {
  __shared__ __attribute__((aligned(16))) char lds[L_BYTES];
  const int tid = __builtin_amdgcn_workitem_id_x(), wid = __builtin_amdgcn_readfirstlane(tid >> 6), w = wid & 3;
  const __amdgpu_buffer_rsrc_t rs = __builtin_amdgcn_make_buffer_rsrc(a.ws, 0, (int)COMM_BYTES, 0x00020000);
  if (tid < 32) ((float*)(lds + L_MISC))[tid] = (tid == 4 || tid == 5) ? 1.0f : 0.0f;
  BAR();
  if (a.layer && wid < 5) proj_front(a, lds, rs, wid);
  if (wid < 4) {
    if (w == 0) rec_waves<0>(a, lds, rs);
    else if (w == 1) rec_waves<1>(a, lds, rs);
    else if (w == 2) rec_waves<2>(a, lds, rs);
    else rec_waves<3>(a, lds, rs);
  } else {
    if (w == 0) token_waves<0>(a, lds, rs);
    else if (w == 1) token_waves<1>(a, lds, rs);
    else if (w == 2) token_waves<2>(a, lds, rs);
    else token_waves<3>(a, lds, rs);
    qkv_tail(a, lds, w);
  }
}

#if defined(__HIP_DEVICE_COMPILE__)
#pragma clang attribute pop
#endif

constexpr int KQ = S_ / 4;
#define ATT_P1 6
#define ATT_P2 10
#define ATT_P3 14
#define ATT_THR 6.0f
#define ATT_NQ 4
#define ATT_LMAX 1.0e12f
#if defined(__HIP_DEVICE_COMPILE__)
#pragma clang attribute push(__attribute__((target("no-packed-fp32-ops"))), apply_to = function)
#endif
__global__ __launch_bounds__(256 * ATT_NQ, 4) void attb_kernel(const u16* __restrict__ Qp, const u16* __restrict__ KV, u16* __restrict__ O) {
  __shared__ f32x4 mo_[ATT_NQ][4][2][3][64];
  __shared__ float mm_[ATT_NQ][4][2][64];
  const int tid = __builtin_amdgcn_workitem_id_x(), bid = __builtin_amdgcn_workgroup_id_x();
  __shared__ unsigned sink[64];
  own_code<5000>(tid, sink);
  const int lane = tid & 63, wid = __builtin_amdgcn_readfirstlane(tid >> 6), kh = wid / ATT_NQ, r = lane & 31, hh = lane >> 5;
  const int hd = (bid & 7) * 4 + ((bid >> 3) & 3), qb = (bid >> 5) * ATT_NQ + wid % ATT_NQ;
  const int q0 = qb * 64;
  bf16x8 qa[2]; u32x2 qb2[2];
#pragma unroll
  for (int qt = 0; qt < 2; ++qt) {
    { const u32x2* qp = (const u32x2*)(Qp + ((size_t)hd * S_ + q0 + qt * 32 + r) * QST + 8 * hh); const u32x2 q0_ = qp[0], q1_ = qp[1]; qa[qt] = mk8(q0_[0], q0_[1], q1_[0], q1_[1]); }
    const u32x2 t = *(const u32x2*)(Qp + ((size_t)hd * S_ + q0 + qt * 32 + r) * QST + 16);
    qb2[qt] = t;
  }
  const __amdgpu_buffer_rsrc_t rk = __builtin_amdgcn_make_buffer_rsrc((void*)(KV + ((size_t)hd * 64 + kh * 16) * (KVT / 2)), 0, 16 * KVT, 0x00020000);
  unsigned ok1 = lane * 16, ok2 = hh ? 1600 : 1024 + r * 8, ov = r > 20 ? 1u << 30 : 1280 + (hh * 21 + r) * 16;
  f32x16 o[2];
  float m[2];
  const f32x16 Z16 = {0.f, 0.f, 0.f, 0.f, 0.f, 0.f, 0.f, 0.f, 0.f, 0.f, 0.f, 0.f, 0.f, 0.f, 0.f, 0.f};
  int odd = 0xDDDD;
  asm volatile("" : "+v"(odd));
  struct KvT { u32x4 k1, v0, v1; u32x2 k2; };
  auto fetch_k = [&](KvT& x, int t) {
    x.k1 = __builtin_amdgcn_raw_buffer_load_b128(rk, ok1, imin(t, KQ / 32 - 1) * KVT, 0); x.k2 = __builtin_amdgcn_raw_buffer_load_b64(rk, ok2, imin(t, KQ / 32 - 1) * KVT, 0);
    __builtin_amdgcn_sched_barrier(0);
  };
  auto fetch_v = [&](KvT& x, int t) {
    x.v0 = __builtin_amdgcn_raw_buffer_load_b128(rk, ov, imin(t, KQ / 32 - 1) * KVT, 0); x.v1 = __builtin_amdgcn_raw_buffer_load_b128(rk, ov + 672, imin(t, KQ / 32 - 1) * KVT, 0);
    __builtin_amdgcn_sched_barrier(0);
  };
  auto fetch = [&](int t) { KvT x; __builtin_amdgcn_sched_barrier(0); fetch_k(x, t); fetch_v(x, t); return x; };
  auto qk1 = [&](const KvT& x, int qt) { return __builtin_amdgcn_mfma_f32_32x32x16_bf16(as8(x.k1), qa[qt], Z16, 0, 0, 0); };
  auto qk2 = [&](const KvT& x, int qt, f32x16 c) { return __builtin_amdgcn_mfma_f32_32x32x8bf16_1k(__builtin_bit_cast(v4i16_t, x.k2), __builtin_bit_cast(v4i16_t, qb2[qt]), c, 0, 0, 0); };
  auto pv = [&](const u32x4 v, const f32x16 p, int hi, f32x16 c) {
    const f32x8 ph = hi ? __builtin_shufflevector(p, p, 8, 9, 10, 11, 12, 13, 14, 15) : __builtin_shufflevector(p, p, 0, 1, 2, 3, 4, 5, 6, 7);
    return __builtin_amdgcn_smfmac_f32_32x32x32_bf16(__builtin_bit_cast(hbf16x8, v), __builtin_bit_cast(hbf16x16, ph), c, odd, 0, 0);
  };
  auto tile = [&](const KvT& x, auto MODE) {
    f32x16 sa[2];
#pragma unroll
    for (int qt = 0; qt < 2; ++qt) sa[qt] = qk1(x, qt);
#pragma unroll
    for (int qt = 0; qt < 2; ++qt) sa[qt] = qk2(x, qt, sa[qt]);
#pragma unroll
    for (int qt = 0; qt < 2; ++qt) {
      if (MODE != 0) {
        float mx = fmx(fmx(sa[qt][0], sa[qt][1]), sa[qt][2]);
#pragma unroll
        for (int i = 3; i < 15; i += 2) mx = fmx(fmx(mx, sa[qt][i]), sa[qt][i + 1]);
        mx = fmx(mx, sa[qt][15]);
        if (MODE == 1 || !wave_all(mx <= ATT_THR)) {
          mx = x32mx(mx);
          if (MODE != 1) mx = fmx(mx, 0.f);
          const float mn = m[qt] + mx;
          const unsigned h1 = f2bf(mn); const float r1 = mn - bf2f(h1);
          const unsigned h2 = f2bf(r1); const float r2 = r1 - bf2f(h2);
          const unsigned h3 = f2bf(r2);
          const float me = (bf2f(h1) + bf2f(h2)) + bf2f(h3);
          const float dm = me - m[qt];
          m[qt] = me;
          if (hh) qb2[qt] = (u32x2){(h1 | (h2 << 16)) ^ 0x80008000u, h3 ^ 0x8000u};
          const float alpha = __builtin_amdgcn_exp2f(-dm);
#pragma unroll
          for (int i = 0; i < 16; ++i) { if (MODE != 1) o[qt][i] *= alpha; sa[qt][i] -= dm; }
        }
      }
#pragma unroll
      for (int i = 0; i < 16; ++i) sa[qt][i] = __builtin_amdgcn_exp2f(sa[qt][i]);
      asm volatile("" : "+v"(sa[qt]));
      if (MODE == 1) { o[qt] = Z16; asm volatile("" : "+v"(o[qt])); }
      o[qt] = pv(x.v1, sa[qt], 1, pv(x.v0, sa[qt], 0, o[qt]));
      __builtin_amdgcn_sched_barrier(0);
    }
  };
  typedef std::integral_constant<int, 1> I1; typedef std::integral_constant<int, 2> I2;
  auto reset = [&]() {
    asm volatile("" : "+v"(ok1), "+v"(ok2), "+v"(ov) :: "memory");
    m[0] = m[1] = 0.f;
    if (hh) qb2[0] = qb2[1] = (u32x2){0u, 0u};
  };
  auto slow = [&]() {
    reset();
    tile(fetch(0), I1{});
#pragma unroll 1
    for (int t = 1; t < KQ / 32; ++t) tile(fetch(t), I2{});
  };
  f32x16 sc[2];
  auto phase = [&](int qt, const u32x4 vp, const KvT& xk, const u32x4 v0, auto mid) {
    auto ex = [&](int i0, int i1) {
#pragma unroll
      for (int i = i0; i < i1; ++i) sc[qt][i] = __builtin_amdgcn_exp2f(sc[qt][i]);
      asm volatile("" : "+v"(sc[qt]));
      __builtin_amdgcn_sched_barrier(0);
    };
    o[1 - qt] = pv(vp, sc[1 - qt], 1, o[1 - qt]);
    __builtin_amdgcn_sched_barrier(0);
    ex(0, 3);
    sc[1 - qt] = qk1(xk, 1 - qt);
    __builtin_amdgcn_sched_barrier(0);
    ex(3, 6);
    sc[1 - qt] = qk2(xk, 1 - qt, sc[1 - qt]);
    __builtin_amdgcn_sched_barrier(0);
    ex(6, 10);
    o[qt] = pv(v0, sc[qt], 0, o[qt]);
    __builtin_amdgcn_sched_barrier(0);
    mid();
    ex(10, 16);
  };
  auto fast = [&]() {
    reset();
    KvT xa = fetch(0), xb = fetch(1);
    tile(xa, I1{});
    fetch_k(xa, 2);
    sc[0] = qk2(xb, 0, qk1(xb, 0)); sc[1] = Z16;
    asm volatile("" : "+v"(sc[1]));
    auto step = [&](KvT& x, KvT& y, int t) {
      phase(0, y.v1, x, x.v0, [&]() { fetch_v(y, t + 1); fetch_k(x, t + 2); });
      phase(1, x.v1, y, x.v0, []() {});
    };
    step(xb, xa, 1);
#pragma unroll 1
    for (int t = 2; t < KQ / 32; t += 2) {
      if (t == ATT_P1) __builtin_amdgcn_s_setprio(2);
      if (t == ATT_P2) __builtin_amdgcn_s_setprio(1);
      if (t == ATT_P3) __builtin_amdgcn_s_setprio(0);
      step(xa, xb, t);
      step(xb, xa, t + 1);
    }
    o[1] = pv(xb.v1, sc[1], 1, o[1]);
  };
  if (ATT_P1) __builtin_amdgcn_s_setprio(3);
  fast();
  {
    float big = 0.f;
#pragma unroll
    for (int qt = 0; qt < 2; ++qt) {
      big = fmx(big, hh ? o[qt][8] * (3.0e38f / ATT_LMAX) : __builtin_fabsf(o[qt][8]));
#pragma unroll
      for (int i = 0; i < 12; ++i) if (i != 8) big = fmx(big, (i > 8 && hh) ? 0.f : __builtin_fabsf(o[qt][i]));
    }
    if (__builtin_expect(!wave_all(big <= 3.0e38f), 0)) slow();
  }
  f32x4 (*mo)[2][3][64] = mo_[wid % ATT_NQ];
  float (*mm)[2][64] = mm_[wid % ATT_NQ];
#pragma unroll
  for (int qt = 0; qt < 2; ++qt) {
#pragma unroll
    for (int k = 0; k < 3; ++k) mo[kh][qt][k][lane] = (f32x4){o[qt][4 * k], o[qt][4 * k + 1], o[qt][4 * k + 2], o[qt][4 * k + 3]};
    mm[kh][qt][lane] = m[qt];
  }
  const int qt = kh & 1, grp = kh >> 1, row = q0 + qt * 32 + r, k0 = hd * BD + 4 * hh + 8 * grp;
  auto oaddr = [&](int k) { return O + (((size_t)(row >> 4) * 20 + (k >> 5)) * 64 + ((k & 31) >> 3) * 16 + (row & 15)) * 8 + (k & 7); };
  u16 *op0 = oaddr(k0), *op1 = oaddr(k0 + 16);
  asm volatile("" : "+v"(op0), "+v"(op1));
  asm volatile("s_waitcnt lgkmcnt(0)\n\ts_barrier" ::: "memory");
  {
    float a[4], l[4];
    f32x4 u[4], x[4];
#pragma unroll
    for (int i = 0; i < 4; ++i) { a[i] = mm[i][qt][lane]; l[i] = mo[i][qt][2][lane | 32][0]; u[i] = mo[i][qt][grp][lane]; x[i] = mo[i][qt][2][lane]; }
    const float mn = fmx(fmx(a[0], a[1]), fmx(a[2], a[3]));
#pragma unroll
    for (int i = 0; i < 4; ++i) a[i] = __builtin_amdgcn_exp2f(a[i] - mn);
    const float inv = __builtin_amdgcn_rcpf((l[0] * a[0] + l[1] * a[1]) + (l[2] * a[2] + l[3] * a[3]));
    const f32x4 v = ((u[0] * a[0] + u[1] * a[1]) + (u[2] * a[2] + u[3] * a[3])) * inv;
    *(GAS u32x2*)op0 = (u32x2){pack2(v[0], v[1]), pack2(v[2], v[3])};
    if (grp == 0) {
      const f32x4 y = ((x[0] * a[0] + x[1] * a[1]) + (x[2] * a[2] + x[3] * a[3])) * inv;
      if (hh == 0) *(GAS u32x2*)op1 = (u32x2){pack2(y[0], y[1]), pack2(y[2], y[3])};
    }
  }
}
#if defined(__HIP_DEVICE_COMPILE__)
#pragma clang attribute pop
#endif

__global__ __launch_bounds__(640, 3) void proj_kernel(const u16* __restrict__ Of, const u16* __restrict__ Wf, const u16* __restrict__ xold,
                                                      u16* __restrict__ xnew) {
  __shared__ __attribute__((aligned(16))) char ob[40960];
  const int tid = threadIdx.x, lane = tid & 63, w = __builtin_amdgcn_readfirstlane(tid >> 6), g = lane >> 4, c = lane & 15;
  __shared__ unsigned sink[64];
  own_code<1500>(tid, sink);
  const int rb = (blockIdx.x & 7) + 8 * (blockIdx.x >> 5), cb = (blockIdx.x >> 3) & 3;
  const int col = cb * 160 + w * 16 + 4 * g;
  const u16* wp = Wf + (size_t)(cb * 10 + w) * 20 * 512 + lane * 8;
  u32x4 st[4];
#pragma unroll
  for (int i = 0; i < 4; ++i) st[i] = *(const u32x4*)(Of + (size_t)rb * 40 * 512 + (i * 640 + tid) * 8);
  bf16x8 wf[20];
#pragma unroll
  for (int ks = 0; ks < 20; ++ks) wf[ks] = *(const bf16x8*)(wp + (size_t)ks * 512);
  u32x2 xo[2];
#pragma unroll
  for (int t = 0; t < 2; ++t) xo[t] = *(const u32x2*)(xold + (((size_t)(rb * 2 + t) * 20 + (col >> 5)) * 64 + ((col & 31) >> 3) * 16 + c) * 8 + (col & 7));
#pragma unroll
  for (int i = 0; i < 4; ++i) *(u32x4*)(ob + (i * 640 + tid) * 16) = st[i];
  BAR();
  f32x4 acc[2] = {{0.f, 0.f, 0.f, 0.f}, {0.f, 0.f, 0.f, 0.f}};
#pragma unroll
  for (int ks = 0; ks < 20; ++ks)
#pragma unroll
    for (int t = 0; t < 2; ++t) acc[t] = mfma32(wf[ks], *(const bf16x8*)(ob + (t * 20 + ks) * 1024 + lane * 16), acc[t]);
#pragma unroll
  for (int t = 0; t < 2; ++t) {
    const unsigned r01 = pack2(acc[t][0], acc[t][1]), r23 = pack2(acc[t][2], acc[t][3]);
    const unsigned p01 = pack2(u2f(xo[t][0] << 16) + u2f(r01 << 16), u2f(xo[t][0] & 0xffff0000u) + u2f(r01 & 0xffff0000u));
    const unsigned p23 = pack2(u2f(xo[t][1] << 16) + u2f(r23 << 16), u2f(xo[t][1] & 0xffff0000u) + u2f(r23 & 0xffff0000u));
    *(u32x2*)(xnew + (size_t)(rb * 32 + t * 16 + c) * DM + col) = (u32x2){p01, p23};
  }
}

extern "C" void kernel_launch(void* const* d_in, const int* in_sizes, int n_in, void* d_out, int out_size, void* d_ws,
                              size_t ws_size, hipStream_t stream) {
  const u16* A_x = (const u16*)d_in[0];
  const u16* A_mem0 = (const u16*)d_in[1];
  const u16* A_Wq = (const u16*)d_in[2];
  const u16* A_Wk = (const u16*)d_in[3];
  const u16* A_Wv = (const u16*)d_in[4];
  const u16* A_Wo = (const u16*)d_in[5];
  const u16* B_wqkv = (const u16*)d_in[8];
  const u16* B_wo = (const u16*)d_in[9];
  char* ws = (char*)(((uintptr_t)d_ws + ((size_t)WS_OFF << 20) + WS_AL - 1) & ~(uintptr_t)(WS_AL - 1));
  u16* xa = (u16*)(ws + OFF_XA); u16* Ob = (u16*)(ws + OFF_OB);

  prologue_kernel<<<256, 640, 0, stream>>>(ProArgs{A_x, A_Wq, A_Wk, A_Wv, A_Wo, B_wqkv, B_wo, ws});
  for (int layer = 0; layer < 2; ++layer) {
    rec_kernel<<<256, 512, 0, stream>>>(RecArgs{A_mem0, A_Wq, A_Wk, (const u16*)(ws + OFF_XB), xa, ws, (unsigned)layer, 0u});
    attb_kernel<<<BH * 32 / ATT_NQ, 256 * ATT_NQ, 0, stream>>>((const u16*)(ws + OFF_QB), (const u16*)(ws + OFF_KV), Ob);
  }
  proj_kernel<<<256, 640, 0, stream>>>(Ob, (const u16*)(ws + OFF_WOBT), xa, (u16*)d_out);
}
```

```cpp
#include <hip/hip_runtime.h>
#include <hip/hip_bf16.h>
#include <stdint.h>
#include <type_traits>

typedef unsigned short u16;
typedef unsigned long long u64;
typedef __attribute__((ext_vector_type(8))) short bf16x8;
typedef __attribute__((ext_vector_type(16))) short bf16x16;
typedef __attribute__((ext_vector_type(4))) float f32x4;
typedef __attribute__((ext_vector_type(2))) float f32x2;
typedef __attribute__((ext_vector_type(16))) float f32x16;
typedef __attribute__((ext_vector_type(8))) float f32x8;
typedef __attribute__((ext_vector_type(4))) unsigned u32x4;
typedef __attribute__((ext_vector_type(2))) unsigned u32x2;
typedef __attribute__((ext_vector_type(3))) unsigned u32x3;
typedef __attribute__((ext_vector_type(8))) unsigned u32x8;
typedef __attribute__((ext_vector_type(16))) unsigned u32x16;
typedef __attribute__((ext_vector_type(32))) unsigned u32x32;
#define GAS __attribute__((address_space(1)))
#define LAS __attribute__((address_space(3)))
typedef GAS unsigned long long gu64;
#define BAR() do { __builtin_amdgcn_s_waitcnt(0xC07F); asm volatile("s_waitcnt lgkmcnt(0)\n\ts_barrier" ::: "memory"); } while (0)

constexpr int S_ = 2048, DM = 640;
constexpr int NH = 32, DH = 192, HD = NH * DH;
constexpr int MEM = 8, SEG = 32, NSEG = S_ / SEG;
constexpr int BH = 32, BD = 20;

constexpr size_t al256(size_t x) { return (x + 255) & ~(size_t)255; }
constexpr int LAG3 = 2, LAG4 = 3, LAG5 = 6;
#define SR_ 8
constexpr int SR = SR_;
constexpr unsigned SCT_STEP = 131072, OFF_SCM = SR * SCT_STEP, SCM_STEP = 32768;
constexpr int ACC_NB = 192;
constexpr size_t ACC_STEP = 65536;
constexpr unsigned ACC_TS = ACC_NB * 256;
constexpr size_t ACC_LAYER = SR * ACC_STEP;
constexpr size_t OFF_ACC = 3 * 524288;
static_assert(SR == 8, "regions");
__device__ __forceinline__ unsigned ts_piece(int i) { return ACC_TS + (unsigned)(i < 128 ? (i >> 1) * 256 + (i & 1) * 16 : 128 + (i & 1) * 8192); }
constexpr size_t OFF_LNX = OFF_SCM + SR * SCM_STEP;
constexpr size_t OFF_RDY = OFF_LNX + 65536;
constexpr size_t PG = 2097152;
#define RING_ 4
constexpr int RING = RING_;
constexpr size_t TS_SLOT = (size_t)NH * 8 * 2560, PT_SLOT = (size_t)NH * 2560, OP_SLOT = (size_t)256 * 2560;
__device__ __forceinline__ unsigned ts_tok(int h, int j) { return (unsigned)((h & 15) * 32768 + (j >> 2) * 16384 + (j & 3) * 2048); }
__device__ __forceinline__ unsigned ts_mem(int h, int j) { return (unsigned)(524288 + ((h & 15) >> 1) * 16384 + (h & 1) * 4096 + j * 512); }
__device__ __forceinline__ unsigned pt_tok(int h) { return (unsigned)(((h & 15) >> 2) * 16384 + (h & 3) * 2048); }
__device__ __forceinline__ unsigned pt_mem(int h) { return (unsigned)(65536 + (h & 15) * 512); }
constexpr size_t OFF_X = 2 * PG + TS_SLOT + 3 * PT_SLOT, X_BYTES = 7 * OP_SLOT + 4 * PT_SLOT;
constexpr size_t OFF_OP = OFF_X + TS_SLOT + 4 * PT_SLOT;
constexpr size_t COMM_BYTES = OFF_X + X_BYTES + RING * OP_SLOT;
__device__ __forceinline__ unsigned near_half(unsigned ck, unsigned o) { return o + ((ck >> (o >= 2 * PG ? 8 : 4)) & 8192u); }
__device__ __forceinline__ unsigned ts_slot(unsigned ck, int sl) {
  return near_half(ck, sl == NSEG - 1 ? (unsigned)OFF_X : ((sl & 3) == 3 ? (unsigned)(2 * PG) : (unsigned)PG + (unsigned)(sl & 3) * (unsigned)TS_SLOT));
}
__device__ __forceinline__ unsigned pt_slot(unsigned ck, int sl) {
  return near_half(ck, sl >= NSEG - 4 ? (unsigned)(OFF_X + TS_SLOT) + (unsigned)(sl - (NSEG - 4)) * (unsigned)PT_SLOT
                                      : ((sl & 3) == 0 ? (unsigned)(PG + 3 * TS_SLOT) : (unsigned)(2 * PG + TS_SLOT - PT_SLOT) + (unsigned)(sl & 3) * (unsigned)PT_SLOT));
}
__device__ __forceinline__ unsigned op_slot(int sl) { return (unsigned)OFF_OP + (unsigned)(sl >= NSEG - 6 ? sl - (NSEG - 6) : 6 + sl % RING) * (unsigned)OP_SLOT; }
static_assert(RING == 4 && OFF_OP <= 3 * PG && OFF_RDY + 2048 <= OFF_ACC && OFF_ACC + ACC_LAYER == PG && TS_SLOT % 16384 == 0 && PT_SLOT % 16384 == 0, "pages");
static_assert(TS_SLOT == OP_SLOT && RING >= 4 && NSEG % SR == 0 && SR >= 5 && X_BYTES % 4096 == 0 && X_BYTES <= 256 * 20 * 1024, "slots");
constexpr size_t OFF_WB = al256(COMM_BYTES);
constexpr size_t OFF_BNRM = OFF_WB + (size_t)256 * 100 * 1024;
constexpr size_t OFF_LAT = OFF_BNRM + 1024;
constexpr size_t OFF_XNRM = OFF_LAT + 2048;
constexpr size_t OFF_WQKVBT = OFF_XNRM + al256(S_ * 16);
constexpr size_t OFF_WOBT = OFF_WQKVBT + al256((size_t)3 * DM * DM * 2);
constexpr size_t OFF_XN = OFF_WOBT + al256((size_t)DM * DM * 2);
constexpr int QST = BD;
constexpr size_t OFF_XA = OFF_XN + al256((size_t)S_ * DM * 2);
constexpr size_t OFF_XB = OFF_XA + al256((size_t)S_ * DM * 2);
constexpr size_t OFF_QB = OFF_XB + al256((size_t)S_ * DM * 2);
constexpr int KVT = 2624;
constexpr size_t OFF_KV = OFF_QB + al256((size_t)BH * S_ * QST * 2);
constexpr size_t OFF_OB = OFF_KV + al256((size_t)BH * 64 * KVT);
constexpr size_t OFF_DBG = OFF_OB + al256((size_t)S_ * DM * 2);
constexpr size_t WS_TOTAL = OFF_DBG + (size_t)2 * 64 * 8 * DM * 2;
#define WS_AL 2097152
#define WS_OFF 0
static_assert(WS_TOTAL + WS_AL + ((size_t)WS_OFF << 20) <= ((size_t)256 << 20), "workspace budget");

#if defined(__HIP_DEVICE_COMPILE__)
#pragma clang attribute push(__attribute__((target("no-packed-fp32-ops"))), apply_to = function)
#endif
__device__ __forceinline__ float u2f(unsigned v) { return __builtin_bit_cast(float, v); }
__device__ __forceinline__ unsigned f2u(float v) { return __builtin_bit_cast(unsigned, v); }
__device__ __forceinline__ float bf2f(unsigned v) { return __builtin_bit_cast(float, v << 16); }
__device__ __forceinline__ unsigned f2bf(float f) { __bf16 b = (__bf16)f; return (unsigned)__builtin_bit_cast(u16, b); }
__device__ __forceinline__ float rbf(float f) { return bf2f(f2bf(f)); }
typedef __attribute__((ext_vector_type(2))) __bf16 hbf16x2;
__device__ __forceinline__ unsigned pack2(float lo, float hi) { f32x2 v = {lo, hi}; hbf16x2 b = __builtin_convertvector(v, hbf16x2); return __builtin_bit_cast(unsigned, b); }
__device__ __forceinline__ f32x4 mfma32(bf16x8 a, bf16x8 b, f32x4 c) { return __builtin_amdgcn_mfma_f32_16x16x32_bf16(a, b, c, 0, 0, 0); }
typedef __attribute__((ext_vector_type(8))) __bf16 hbf16x8;
typedef __attribute__((ext_vector_type(16))) __bf16 hbf16x16;
__device__ __forceinline__ f32x4 smfma64(bf16x8 a, bf16x16 b, f32x4 c, int idx) {
  return __builtin_amdgcn_smfmac_f32_16x16x64_bf16(__builtin_bit_cast(hbf16x8, a), __builtin_bit_cast(hbf16x16, b), c, idx, 0, 0);
}
__device__ __forceinline__ bf16x16 cat16(bf16x8 a, bf16x8 b) { return __builtin_shufflevector(a, b, 0, 1, 2, 3, 4, 5, 6, 7, 8, 9, 10, 11, 12, 13, 14, 15); }
__device__ __forceinline__ bf16x8 half8(bf16x16 v, int hi) { return hi ? __builtin_shufflevector(v, v, 8, 9, 10, 11, 12, 13, 14, 15) : __builtin_shufflevector(v, v, 0, 1, 2, 3, 4, 5, 6, 7); }
__device__ __forceinline__ bf16x8 mk8(unsigned a, unsigned b, unsigned c, unsigned d) { u32x4 v = {a, b, c, d}; return __builtin_bit_cast(bf16x8, v); }
__device__ __forceinline__ unsigned dpp_ror8(unsigned v) { return (unsigned)__builtin_amdgcn_mov_dpp((int)v, 0x128, 0xf, 0xf, true); }
__device__ __forceinline__ float fmx(float a, float b) { return __builtin_elementwise_maximum(a, b); }

__device__ __forceinline__ int imax(int a, int b) { return a > b ? a : b; }
__device__ __forceinline__ int imin(int a, int b) { return a < b ? a : b; }
__device__ __forceinline__ unsigned umin(unsigned a, unsigned b) { return a < b ? a : b; }
__device__ __forceinline__ unsigned umax(unsigned a, unsigned b) { return a > b ? a : b; }
__device__ __forceinline__ bool wave_all(bool p) { return __builtin_amdgcn_ballot_w64(!p) == 0ull; }
__device__ __forceinline__ float x16mx(float x) { unsigned u = __builtin_bit_cast(unsigned, x); auto r = __builtin_amdgcn_permlane16_swap(u, u, false, false); return fmx(u2f(r[0]), u2f(r[1])); }
__device__ __forceinline__ float x32mx(float x) { unsigned u = __builtin_bit_cast(unsigned, x); auto r = __builtin_amdgcn_permlane32_swap(u, u, false, false); return fmx(u2f(r[0]), u2f(r[1])); }
__device__ __forceinline__ float x16sum(float x) { unsigned u = __builtin_bit_cast(unsigned, x); auto r = __builtin_amdgcn_permlane16_swap(u, u, false, false); return u2f(r[0]) + u2f(r[1]); }
__device__ __forceinline__ float x32sum(float x) { unsigned u = __builtin_bit_cast(unsigned, x); auto r = __builtin_amdgcn_permlane32_swap(u, u, false, false); return u2f(r[0]) + u2f(r[1]); }
template <int CTRL> __device__ __forceinline__ float dppf(float v) { return u2f((unsigned)__builtin_amdgcn_mov_dpp((int)f2u(v), CTRL, 0xf, 0xf, true)); }
#define OWN_PF 1
template <int BYTES> __device__ __forceinline__ void own_code(int tid, unsigned* sink) {
  if (!OWN_PF || __builtin_amdgcn_readfirstlane(tid >> 6) * 4096 >= BYTES) return;
  const char* pc; unsigned keep;
  asm volatile("s_getpc_b64 %0" : "=s"(pc));
  asm volatile("s_mov_b32 %0, m0\n\ts_mov_b32 m0, %3\n\ts_nop 0\n\tglobal_load_lds_dword %1, %2\n\ts_mov_b32 m0, %0"
               : "=&s"(keep) : "v"(tid * 64 < BYTES ? tid * 64 : 0), "s"(pc), "s"((unsigned)(uintptr_t)(LAS unsigned*)sink) : "memory");
}
constexpr int DPP_XOR1 = 0xB1, DPP_XOR2 = 0x4E, DPP_ROR4 = 0x124, DPP_ROR8 = 0x128;
#if defined(__HIP_DEVICE_COMPILE__)
#pragma clang attribute pop
#endif

typedef short v4i16_t __attribute__((ext_vector_type(4)));
__device__ __forceinline__ bf16x8 tr_frag(const char* lo_, const char* hi_) {
  const v4i16_t lo = __builtin_amdgcn_ds_read_tr16_b64_v4i16((LAS v4i16_t*)lo_), hi = __builtin_amdgcn_ds_read_tr16_b64_v4i16((LAS v4i16_t*)hi_);
  return (bf16x8){lo[0], lo[1], lo[2], lo[3], hi[0], hi[1], hi[2], hi[3]};
}
__device__ __forceinline__ int rec_wg(int gp, int m) { return (((gp & 1) * 16 + m) << 3) | (gp >> 1); }
__device__ __forceinline__ float wsum(float v) {
  v += dppf<DPP_XOR1>(v); v += dppf<DPP_XOR2>(v); v += dppf<DPP_ROR4>(v); v += dppf<DPP_ROR8>(v);
  return x32sum(x16sum(v));
}
__device__ __forceinline__ float ln_row(int lane, u32x4 (&xo)[2]) {
  const bool two = lane < 16;
  float v[16];
#pragma unroll
  for (int hf = 0; hf < 2; ++hf)
#pragma unroll
    for (int i = 0; i < 4; ++i) { const unsigned u = xo[hf][i]; v[hf * 8 + 2 * i] = bf2f(u & 0xffff); v[hf * 8 + 2 * i + 1] = bf2f(u >> 16); }
  float s0 = 0.f, s1 = 0.f;
#pragma unroll
  for (int i = 0; i < 8; ++i) { s0 += v[i]; s1 += v[8 + i]; }
  const float mean = wsum(two ? s0 + s1 : s0) * (1.0f / DM);
  s0 = s1 = 0.f;
#pragma unroll
  for (int i = 0; i < 8; ++i) { const float d0 = v[i] - mean, d1 = v[8 + i] - mean; s0 += d0 * d0; s1 += d1 * d1; }
  const float var = rbf(wsum(two ? s0 + s1 : s0) * (1.0f / DM));
  const float mb = rbf(mean);
  const float rden = 1.0f / rbf(sqrtf(rbf(var + 1e-5f)));
  float n2[2] = {0.f, 0.f};
#pragma unroll
  for (int hf = 0; hf < 2; ++hf) {
    float y[8];
#pragma unroll
    for (int i = 0; i < 8; ++i) { y[i] = rbf(rbf(v[hf * 8 + i] - mb) * rden); n2[hf] += y[i] * y[i]; }
    xo[hf] = (u32x4){pack2(y[0], y[1]), pack2(y[2], y[3]), pack2(y[4], y[5]), pack2(y[6], y[7])};
  }
  return wsum(two ? n2[0] + n2[1] : n2[0]);
}

#define PROT(k)
#define WARM_CODE 1
#define B_AUX 16
#define X_AUX 16
#define T_AUX 16
#define PF_POS 0
#define PRB_N0 3
#define PRB_N1 0
#define PRB_LOG(i, v)
#define PLD(p) __builtin_nontemporal_load((const u32x4*)(p))
struct ProArgs { const u16* x; const u16* wq; const u16* wk; const u16* wv; const u16* wo; const u16* wqkvb; const u16* wob; char* ws; };
__global__ __launch_bounds__(640, 3) void prologue_kernel(ProArgs a) {
  __shared__ __attribute__((aligned(16))) char img[192 * 320];
  __shared__ __attribute__((aligned(16))) char slots[10 * 6144];
  __shared__ float red[10][160];
  __shared__ unsigned latw[8];
  __shared__ u32x4 xst[2][640];
  const int b = blockIdx.x, tid = threadIdx.x, lane = tid & 63, v = __builtin_amdgcn_readfirstlane(tid >> 6), c = lane & 15, g = lane >> 4;
  const int h = (b & 7) * 4 + ((b >> 3) & 3), dh = (b >> 5) & 1, cq = b >> 6, ks = dh * 10 + v;
  char* const slot = slots + v * 6144;
  PROT(0);
  __shared__ unsigned sink[64];
  own_code<9600>(tid, sink);
  const unsigned zo = v < 8 ? (unsigned)(((b >> 6) * 512 + ((b & 7) * 2 + (v >> 2)) * 32 + ((b >> 3) & 7) * 4 + (v & 3)) * 1024) : (unsigned)(((v - 7) * 2048 + (b & 7) * 32 + ((b >> 3) & 7) * 4 + (b >> 6)) * 1024);
  __builtin_amdgcn_raw_buffer_store_b128((u32x4){0u, 0u, 0u, 0u}, __builtin_amdgcn_make_buffer_rsrc(a.ws, 0, (int)(3 * PG), 0x00020000), zo + lane * 16, 0, 16);
  __builtin_amdgcn_sched_barrier(0);
  u32x4 wa[2][6], wb[6], tr[4], xo[2];
  const int xrow = b * 8 + (v & 7);
  xo[0] = *(const u32x4*)(a.x + (size_t)xrow * DM + lane * 8);
  xo[1] = *(const u32x4*)(a.x + (size_t)xrow * DM + 512 + c * 8);
  const int job = imin(b + 256 * (9 - v), 800);
  const int jn = job < 600 ? job / 20 : (job % 200) / 20, jk = job % 20;
  {
    const u16* src = job < 600 ? a.wqkvb + (size_t)(32 * jk) * 3 * DM : a.wob + (size_t)(32 * jk) * DM;
#pragma unroll
    for (int i = 0; i < 4; ++i) tr[i] = *(const u32x4*)(src + (size_t)(i * 8 + (lane >> 3)) * (job < 600 ? 3 * DM : DM) + jn * 64 + (lane & 7) * 8);
  }
#pragma unroll
  for (int i = 0; i < 6; ++i) {
    const int idx = (v * 6 + i) * 64 + lane, row = idx / 20;
    wb[i] = PLD(a.wo + (size_t)(h * DH + row) * DM + cq * 160 + (idx - row * 20) * 8);
  }
#pragma unroll
  for (int i = 0; i < 6; ++i) {
    const int idx = i * 64 + lane, r = idx / 24;
#pragma unroll
    for (int u = 0; u < 2; ++u) wa[u][i] = PLD(a.wv + (size_t)(32 * ks + 8 * (r >> 2) + (r & 3) + 4 * u) * HD + h * DH + (idx - r * 24) * 8);
  }
  unsigned pf[2] = {0u, 0u};
  auto prefetch = [&]() {
#pragma unroll
    for (int i = 0; i < 2; ++i) pf[i] = *(const unsigned*)((i ? a.wq : a.wk) + ((size_t)(b * 10 + v) * 48 + imin(lane, 47)) * 32);
  };
  if (PF_POS == 0) prefetch();
  __builtin_amdgcn_sched_barrier(0);
  unsigned lat = ~0u;
  auto probe = [&](int n0, int n) {
    const char* p = a.ws + zo;
#pragma unroll 1
    for (int i = n0; i < n0 + n; ++i) {
      u64 t0, t1; unsigned d;
      asm volatile("s_memtime %0\n\ts_waitcnt lgkmcnt(0)\n\ts_load_dword %2, %3, %4\n\ts_waitcnt lgkmcnt(0)\n\ts_memtime %1\n\ts_waitcnt lgkmcnt(0)"
                   : "=&s"(t0), "=&s"(t1), "=&s"(d) : "s"(p), "s"((i & 3) * 256 + (i >> 2) * 128) : "memory");
      PRB_LOG(i, (unsigned)t1 - (unsigned)t0);
      lat = umin(lat, (unsigned)t1 - (unsigned)t0);
    }
  };
  if (PRB_N0) { asm volatile("s_waitcnt vmcnt(26)" ::: "memory"); probe(0, PRB_N0); }
  __builtin_amdgcn_sched_barrier(0);
  auto xput = [&](int k) {
    if (v < 8) { xst[k][lane * 8 + (v ^ (lane & 7))] = xo[0]; if (lane < 16) xst[k][(64 + lane) * 8 + (v ^ (lane & 7))] = xo[1]; }
  };
  xput(0);
  const float xn2 = u2f(__builtin_amdgcn_readfirstlane(f2u(ln_row(lane, xo))));
  xput(1);
#pragma unroll
  for (int i = 0; i < 4; ++i) *(u32x4*)(slot + (i * 8 + (lane >> 3)) * 144 + (lane & 7) * 16) = tr[i];
  __builtin_amdgcn_fence(__ATOMIC_RELEASE, "wavefront"); __builtin_amdgcn_wave_barrier();
  bf16x8 tf[4];
#pragma unroll
  for (int t = 0; t < 4; ++t) {
    const char* bp = slot + (8 * g + (c >> 2)) * 144 + t * 32 + (c & 3) * 8;
    tf[t] = tr_frag(bp, bp + 4 * 144);
  }
  __builtin_amdgcn_fence(__ATOMIC_RELEASE, "wavefront"); __builtin_amdgcn_wave_barrier();
  __builtin_amdgcn_sched_barrier(0);
#pragma unroll
  for (int i = 0; i < 6; ++i) {
    const int idx = (v * 6 + i) * 64 + lane, row = idx / 20;
    *(u32x4*)(img + row * 320 + (((idx - row * 20) ^ ((row >> 2) & 2)) * 16)) = wb[i];
  }
  bf16x8 A[2][6];
#pragma unroll
  for (int u = 0; u < 2; ++u) {
#pragma unroll
    for (int i = 0; i < 6; ++i) {
      const int idx = i * 64 + lane, r = idx / 24;
      *(u32x4*)(slot + r * 384 + (((idx - r * 24) ^ (r >> 1)) * 16)) = wa[u][i];
    }
    __builtin_amdgcn_fence(__ATOMIC_RELEASE, "wavefront"); __builtin_amdgcn_wave_barrier();
#pragma unroll
    for (int e = 0; e < 6; ++e) A[u][e] = *(const bf16x8*)(slot + c * 384 + (((4 * e + g) ^ (c >> 1)) * 16));
    __builtin_amdgcn_fence(__ATOMIC_RELEASE, "wavefront"); __builtin_amdgcn_wave_barrier();
  }
  BAR();
  PROT(1);
  if (PF_POS == 0) asm volatile("" :: "v"(pf[0]), "v"(pf[1]));
  if (PF_POS == 1) prefetch();
  {
    const unsigned xf = (unsigned)((b >> 1) * 20480 + (tid >> 3) * 256 + (b & 1) * 128 + (tid & 7) * 16);
    const __amdgpu_buffer_rsrc_t rx = __builtin_amdgcn_make_buffer_rsrc(a.ws + OFF_XN, 0, (int)(OFF_QB - OFF_XN), 0x00020000);
    __builtin_amdgcn_raw_buffer_store_b128(xst[1][tid ^ ((tid >> 3) & 7)], rx, xf, 0, X_AUX);
    __builtin_amdgcn_raw_buffer_store_b128(xst[0][tid ^ ((tid >> 3) & 7)], rx, xf + (unsigned)(OFF_XB - OFF_XN), 0, X_AUX);
    if (v < 8 && lane < 4) ((float*)(a.ws + OFF_XNRM))[xrow * 4 + lane] = lane == 0 ? xn2 : 0.f;
  }
  if (job < 800) {
    const __amdgpu_buffer_rsrc_t rt = __builtin_amdgcn_make_buffer_rsrc(a.ws + (job < 600 ? OFF_WQKVBT : OFF_WOBT) + ((size_t)jn * 80 + jk) * 1024, 0, 81920, 0x00020000);
#pragma unroll
    for (int t = 0; t < 4; ++t) __builtin_amdgcn_raw_buffer_store_b128(__builtin_bit_cast(u32x4, tf[t]), rt, lane * 16 + t * 20480, 0, T_AUX);
  }
  const int gp = h >> 1, hh = h & 1;
  const __amdgpu_buffer_rsrc_t rwb = __builtin_amdgcn_make_buffer_rsrc(a.ws + OFF_WB, 0, 256 * 100 * 1024, 0x00020000);
#pragma unroll
  for (int n = 0; n < 10; ++n) {
    const int off = n * 32 + 8 * (c & 3), T = cq * 10 + n;
    const char* bp = img + (8 * g + (c >> 2)) * 320 + (off ^ ((g & 1) << 5));
    f32x4 ac[2] = {{0.f, 0.f, 0.f, 0.f}, {0.f, 0.f, 0.f, 0.f}};
#pragma unroll
    for (int e = 0; e < 6; ++e) {
      const bf16x8 bf = tr_frag(bp + e * 32 * 320, bp + (e * 32 + 4) * 320);
      ac[0] = mfma32(A[0][e], bf, ac[0]); ac[1] = mfma32(A[1][e], bf, ac[1]);
    }
    const u32x4 o = {pack2(ac[0][0], ac[0][1]), pack2(ac[0][2], ac[0][3]), pack2(ac[1][0], ac[1][1]), pack2(ac[1][2], ac[1][3])};
    const unsigned oo = T < 32 ? (unsigned)((rec_wg(gp, T & 15) * 100 + (hh * 3 + (T >> 4)) * 20 + ks) * 1024 + lane * 16)
                               : (unsigned)((rec_wg(gp, 2 * (T - 32) + (c >> 3)) * 100 + 40 + ks) * 1024 + (g * 16 + hh * 8 + (c & 7)) * 16);
    __builtin_amdgcn_raw_buffer_store_b128(o, rwb, oo, 0, B_AUX);
    float n2 = 0.f;
#pragma unroll
    for (int r = 0; r < 4; ++r) n2 += ac[0][r] * ac[0][r] + ac[1][r] * ac[1][r];
    n2 = x32sum(x16sum(n2));
    if (g == 0) red[v][n * 16 + c] = n2;
  }
  PROT(2);
  if (PF_POS == 1) asm volatile("" :: "v"(pf[0]), "v"(pf[1]));
  probe(PRB_N0, PRB_N1);
  if (v < 8) latw[v] = lat;
  else if (b < 64 && lane == 0) ((unsigned*)(a.ws + OFF_LAT))[(v - 4) * 64 + b] = umin(lat, 60000u);
  if (WARM_CODE && v == 7) {
    const char* pc; int d0, d1;
    asm("s_getpc_b64 %0\n\ts_mov_b32 %1, _Z10rec_kernel7RecArgs@rel32@lo+4\n\ts_mov_b32 %2, _Z11proj_kernelPKtS0_S0_Pt@rel32@lo+12" : "=s"(pc), "=s"(d0), "=s"(d1));
    const char *c0 = pc + d0, *c1 = pc + d1;
    const long o = ((long)b * 64 + lane) * 64;
    if (o < c1 - c0) asm volatile("" :: "v"(*(const volatile GAS unsigned*)(c0 + o)));
  }
  if (v == 9 && lane < 32) {
    const int q = b * 32 + lane;
    *(u32x4*)(a.ws + OFF_KV + (size_t)(q >> 2) * KVT + 1280 + (q & 3) * 336 + 320) = (u32x4){0x3f803f80u, 0x3f803f80u, 0x3f803f80u, 0x3f803f80u};
  }
  BAR();
  if (v == 1 && lane == 0) ((unsigned*)(a.ws + OFF_LAT))[b] = umin(umin(umin(latw[0], latw[4]) + umin(latw[1], latw[5]), 30000u) + umin(umin(latw[2], latw[6]) + umin(latw[3], latw[7]), 30000u), 60000u);
  if (v == 0) {
    float mx = 0.f;
#pragma unroll
    for (int k = 0; k < 3; ++k) {
      const int col = k * 64 + lane;
      float sm = 0.f;
#pragma unroll
      for (int i = 0; i < 10; ++i) sm += red[i][col < 160 ? col : 0];
      mx = fmx(mx, sm);
    }
    mx = fmx(mx, dppf<DPP_XOR1>(mx)); mx = fmx(mx, dppf<DPP_XOR2>(mx)); mx = fmx(mx, dppf<DPP_ROR4>(mx)); mx = fmx(mx, dppf<DPP_ROR8>(mx));
    mx = x32mx(x16mx(mx));
    if (lane == 0) ((float*)(a.ws + OFF_BNRM))[(cq * 32 + h) * 2 + dh] = mx;
  }
  PROT(3);
}

struct RecArgs {
  const u16* mem0; const u16* wq; const u16* wk; const u16* xold; u16* xnew; char* ws;
  unsigned layer; unsigned pad;
};
#define RLX_AGENT __ATOMIC_RELAXED, __HIP_MEMORY_SCOPE_AGENT
constexpr unsigned SPIN_CAP = 1u << 20;
#define TSTAMP(k, v)
#define ARRIVE(k)
#define ARRIVE_OUT()
#define ARRIVE_DECL()
#define LOGV(k, v)
#define LOGF(k, v)
#define LOGP(k)
#define LOGQ(k)
#define LOGF2(k)
#define LOGD(k)
#define LOGT(k, v)
#define STAMP_DECL()
#define STAMP(k)
#define STAMPV(k, v)
#define STAMP_OUT()
#define REC_D1 520
#define SCH_V 18
#define SCH_VMIN 0
#define SCH_DMIN 4
#define SCH_PEN 170
#define SCH_VMIN2 (-4)
#define SCH_PEN2 40
#define SCH_GU 1
#define SCH_GD 2
#define SCH_UP 6
#define SCH_D0 196
#define SCH_FIRST 120
#define EXP_NODMA 0
#define BG_HOLD 220
#define QBATCH 0
#define WPF_N 5
#define WPF_W 0
#define WPF0 (63 - WPF_N)
#define BG_HOLD4 250
#define D1_W1 20
#define F3_D 1200
#define T2W 2
#define AT_ASM 1
#define PM_Z 1
#define P2_X 1
#define ST_KS 14
#define ST_KS0 ST_KS
#define ST_KS1 ST_KS
#define PADR 0x6666
#define PADT 0x000
#define CODE_PAD(n) asm volatile(".p2align 6\n.rept %0\ns_nop 0\n.endr" :: "n"(n))
#define ST_X 300
#define ST_ALL 4
#define CPF_S 61
#define FR_D 700
#define FR_PACE 6
#define END_E60 600
#define END_E1 1300
#define END_E2 3400
#define END_E62 2800
#define END_E3 6800
#define END_E4 11400
#define EARLY_N 4
#define EARLY_D1 0
#define EARLY_V 4
#define EARLY_V1 10
#define F3 0
#define F3A 0
#define F3A_D 400
#define T4_D 300
#define T4_P 1
#define P2_W 1
#define PS_SL 1
#define F2B 4
#define F0C 0
#define F2C 0
#define W2_P 1
#define SCH_W 1
#define SP_SL 0
#define D1_M 300
#define D1_MIN 260
#define D1_C 96
#define D1_G 2
#define BG_HOLD6 0
#define W2_D 0
#define END_E61 2600
#define D1_LATE 200
#define P2_G0 10
#define P2_G2 6
#define P2_G3 6
#define SCH_RL 0
#define SCH_P 3
#define MMA_P 0
#define SP_P 0
#define F6 8
#define F7 10
#define XPF_W 2
#define SCH_LATE 0
#define LATE_V 6
#define QW 3
#define FB_SL 0
#define P2_G1 1
#define P1_G 0
#define F4Z 0
#define F5Z 0
#define T5_Z 2
#define F0A 3
#define FB_E 0
#define EXACT 1
#define D1_P 3
#define VO_A0 9
#define VO_A2 10
#define VO_B0 3
#define VO_B1 0
#define VO_N2 7
#define VO_N3 10
#define VO_W -1
#define UNIT 0
#define UN_P 2
#define VP_E 1
#define TS_B 0
#define ST_E 1
#define W1_SL 0
#define VO_A1 4
#define LAST_V 18
#define END_E59 3200
#define END_G2 600
#define END_G3 600
#define QR 4
#define WPF_S 128
#define END_SL 4
#define KP0 0
#define KP1 0
#define XPF 3
#define W1_D 0
#define F4 12
#define F5 0
#define F67 0
#define KP2 0
#define REC_RNG 70u
#define SCH_DRAIN 40
#define PL_X 0
#define FC(k)
#define PIN_V(x) asm volatile("" : "+v"(x))
#define PIN_S(x) asm volatile("" : "+s"(x))
#define VM_DRAIN() do { asm volatile("s_waitcnt vmcnt(0)" ::: "memory"); __builtin_amdgcn_s_waitcnt(0x0F70); } while (0)
#define SC1_LOAD(off) __builtin_amdgcn_raw_buffer_load_b128(rs, (off), 0, 16)
#define SC1_LOADS(off, soff) __builtin_amdgcn_raw_buffer_load_b128(rs, (off), (soff), 16)
#define SC1_STORE8S(v, off, soff) __builtin_amdgcn_raw_buffer_store_b64((v), rs, (off), (soff), 16)
#define SC1_LOAD8(off) __builtin_amdgcn_raw_buffer_load_b64(rs, (off), 0, 16)
#define SC1_STORE(v, off) __builtin_amdgcn_raw_buffer_store_b128((v), rs, (off), 0, 16)
#define SC1_STORE8(v, off) __builtin_amdgcn_raw_buffer_store_b64((v), rs, (off), 0, 16)
#if defined(__HIP_DEVICE_COMPILE__)
#pragma clang attribute push(__attribute__((target("no-packed-fp32-ops"))), apply_to = function)
#endif
__device__ __forceinline__ unsigned now32() { return (unsigned)__builtin_amdgcn_s_memrealtime(); }
__device__ __forceinline__ void wait_until(unsigned t) { for (;;) { const int d = (int)(t - now32()); if (d <= 0) break; if (d > 8) __builtin_amdgcn_s_sleep(1); } }
__device__ __forceinline__ unsigned cyc32() { return (unsigned)__builtin_amdgcn_s_memtime(); }
__device__ __forceinline__ void wait_cyc(unsigned t) { for (;;) { const int d = (int)(t - cyc32()); if (d <= 0) break; if (d > 200) __builtin_amdgcn_s_sleep(1); } }
#define WAIT_ASM(clk, far_) "1:\n\t" clk " vcc\n\ts_waitcnt lgkmcnt(0)\n\ts_sub_i32 %0, %1, vcc_lo\n\ts_cmp_lt_i32 %0, " far_ "\n\ts_cbranch_scc1 3f\n\ts_sleep 1\n\ts_branch 1b\n" \
                            "2:\n\t" clk " vcc\n\ts_waitcnt lgkmcnt(0)\n\ts_sub_i32 %0, %1, vcc_lo\n3:\n\ts_cmp_gt_i32 %0, 0\n\ts_cbranch_scc1 2b"
__device__ __forceinline__ int spin_until(unsigned t) { int d; asm volatile(WAIT_ASM("s_memrealtime", "9") : "=&s"(d) : "s"(t) : "vcc", "scc", "memory"); return d; }
__device__ __forceinline__ int spin_cyc(unsigned t) { int d; asm volatile(WAIT_ASM("s_memtime", "200") : "=&s"(d) : "s"(t) : "vcc", "scc", "memory"); return d; }
__device__ __forceinline__ void spin_exact(unsigned t) {
  if (!EXACT) { spin_cyc(t); return; }
  int d;
  asm volatile("1:\n\ts_memtime vcc\n\ts_waitcnt lgkmcnt(0)\n\ts_sub_i32 %0, %1, vcc_lo\n\ts_cmp_gt_i32 %0, 95\n\ts_cbranch_scc1 1b\n\t"
               "s_max_i32 %0, %0, 0\n\ts_andn2_b32 %0, %0, 3\n\ts_sub_i32 %0, 3f-2f, %0\n\ts_getpc_b64 vcc\n"
               "2:\n\ts_add_u32 vcc_lo, vcc_lo, %0\n\ts_addc_u32 vcc_hi, vcc_hi, 0\n\ts_setpc_b64 vcc\n\t.rept 24\n\ts_nop 0\n\t.endr\n3:"
               : "=&s"(d) : "s"(t) : "scc", "vcc", "memory");
}
template <int OFF, int SL> __device__ __forceinline__ void lds_spin(unsigned lds0, unsigned want) {
  unsigned v;
  asm volatile("1:\n\tds_read_b32 %0, %1 offset:%3\n\t.if %4\n\ts_sleep %4\n\t.endif\n\ts_waitcnt lgkmcnt(0)\n\tv_cmp_gt_u32 vcc, %2, %0\n\ts_cbranch_vccnz 1b"
               : "=&v"(v) : "v"(lds0), "s"(want), "n"(OFF), "n"(SL) : "vcc", "memory");
}
template <int CTRL> __device__ __forceinline__ int dppi(int v) { return __builtin_amdgcn_mov_dpp(v, CTRL, 0xf, 0xf, true); }
__device__ __forceinline__ int wave_imax(int l) {
  l = imax(l, dppi<DPP_XOR1>(l)); l = imax(l, dppi<DPP_XOR2>(l)); l = imax(l, dppi<DPP_ROR4>(l)); l = imax(l, dppi<DPP_ROR8>(l));
  int a = imax(__builtin_amdgcn_readlane(l, 0), __builtin_amdgcn_readlane(l, 16)), b = imax(__builtin_amdgcn_readlane(l, 32), __builtin_amdgcn_readlane(l, 48));
  PIN_S(a); PIN_S(b);
  return imax(a, b);
}
__device__ __forceinline__ void glds16(const void* gsrc, unsigned lds_dst) {
  unsigned keep;
  asm volatile("s_mov_b32 %0, m0\n\ts_mov_b32 m0, %2\n\ts_nop 0\n\tglobal_load_lds_dwordx4 %1, off\n\ts_mov_b32 m0, %0" : "=&s"(keep) : "v"(gsrc), "s"(lds_dst) : "memory");
}
__device__ __forceinline__ void glds16_sc1(const void* gsrc, unsigned lds_dst) {
  unsigned keep;
  asm volatile("s_mov_b32 %0, m0\n\ts_mov_b32 m0, %2\n\ts_nop 0\n\tglobal_load_lds_dwordx4 %1, off sc1\n\ts_mov_b32 m0, %0" : "=&s"(keep) : "v"(gsrc), "s"(lds_dst) : "memory");
}
__device__ __forceinline__ unsigned acc_off(int G, int row) {
  return (unsigned)(G < 128 ? ((G & 31) | ((G & 32) << 1) | ((G & 64) >> 1)) * 256 + row * 16 : (2 * G - 128 + (row >> 2)) * 256 + (row & 3) * 16);
}
__device__ __forceinline__ unsigned nz(unsigned v) { return v > 1u ? v : 1u; }
__device__ __forceinline__ unsigned pack2n(float lo, float hi) { return nz(pack2(lo, hi)); }
__device__ __forceinline__ unsigned min4(u32x4 v) { return umin(umin(v[0], v[1]), umin(v[2], v[3])); }
__device__ __forceinline__ bf16x8 as8(u32x4 v) { return __builtin_bit_cast(bf16x8, v); }

constexpr int NV = 7, NKQ = 2;
constexpr int L_RED1 = 0;
constexpr int L_BM = L_RED1 + 8 * 1536;
constexpr int L_STG = L_BM + 10240;
constexpr int L_ST = L_STG + 2048 + 2560;
constexpr int L_MISC = L_ST + 512;
constexpr int L_KX = L_MISC + 128;
constexpr int L_QX = L_KX + NKQ * 2048;
constexpr int L_XN = L_QX + NKQ * 2048;
constexpr int L_VOX = L_XN + 81920;
constexpr int L_VOM = L_VOX + NV * 5120;
constexpr int L_BYTES = L_VOM + NV * 1280;
constexpr int S_IMG = L_XN + 40960;
static_assert(S_IMG + 61440 <= L_VOM, "start-up image");
static_assert(L_BYTES <= 160 * 1024, "LDS budget");

__device__ __forceinline__ bf16x8 wqk_frag(const char* lds, int t, int ks, int lane) {
  const int c = lane & 15, g = lane >> 4, p = c & 3;
  const char* q = lds + S_IMG + (32 * ks + 8 * g + (c >> 2)) * 96 + 8 * p + (t == 0 ? 0 : (t == 1 ? 48 : (p < 2 ? 32 : 64)));
  const v4i16_t lo = __builtin_amdgcn_ds_read_tr16_b64_v4i16((LAS v4i16_t*)q), hi = __builtin_amdgcn_ds_read_tr16_b64_v4i16((LAS v4i16_t*)(q + 4 * 96));
  return (bf16x8){lo[0], lo[1], lo[2], lo[3], hi[0], hi[1], hi[2], hi[3]};
}
__device__ __forceinline__ bf16x8 sel_frag(int c, int base) {
  bf16x8 r;
#pragma unroll
  for (int i = 0; i < 8; ++i) r[i] = (c == base + i) ? (short)0x3f80 : (short)0;
  return r;
}
__device__ __forceinline__ bf16x8 qk_frag(const char* p4, const char* p2, const char* p2b) {
  f32x4 a0 = *(const f32x4*)(p4), a1 = *(const f32x4*)(p4 + 1792), a2 = *(const f32x4*)(p4 + 3584), a3 = *(const f32x4*)(p4 + 5376);
  f32x2 b0 = *(const f32x2*)(p2), b1 = *(const f32x2*)(p2 + 1792), b2 = *(const f32x2*)(p2b), b3 = *(const f32x2*)(p2b + 1792);
  f32x4 va = (a0 + a1) + (a2 + a3);
  f32x2 vb = (b0 + b1) + (b2 + b3);
  return mk8(pack2(va[0], va[1]), pack2(va[2], va[3]), pack2(vb[0], vb[1]), 0u);
}
__device__ __forceinline__ void x_fetch(const u16* src, unsigned lds0, int seg, int p, int lane) {
  if (EXP_NODMA) return;
  glds16_sc1(src + (size_t)p * 512 + lane * 8, lds0 + L_XN + (seg & 1) * 40960 + p * 1024);
}
template <int N> __device__ __forceinline__ void x_fetch_n(const u16* src, unsigned lds0, int seg, int p0, unsigned voff) {
  if (EXP_NODMA) return;
#pragma unroll
  for (int i = 0; i < N; i += 4) {
    unsigned keep;
    asm volatile("s_mov_b32 %0, m0\n\ts_mov_b32 m0, %3\n\ts_nop 0\n\tglobal_load_lds_dwordx4 %1, %2 sc1\n\t"
                 ".if %4 > 1\n\tglobal_load_lds_dwordx4 %1, %2 offset:1024 sc1\n\t.endif\n\t.if %4 > 2\n\tglobal_load_lds_dwordx4 %1, %2 offset:2048 sc1\n\t.endif\n\t"
                 ".if %4 > 3\n\tglobal_load_lds_dwordx4 %1, %2 offset:3072 sc1\n\t.endif\n\ts_mov_b32 m0, %0"
                 : "=&s"(keep) : "v"(voff), "s"(src + (size_t)(p0 + i) * 512), "s"(lds0 + L_XN + (seg & 1) * 40960 + (p0 + i) * 1024), "n"(N - i < 4 ? N - i : 4) : "memory");
  }
}
__device__ __forceinline__ int qkv_row(int b) { return (b & 7) / QR * (2 * QR) + (b >> 3) / (16 / QR); }
__device__ __forceinline__ int qkv_col(int b) { return (b & 7) % QR * (16 / QR) + (b >> 3) % (16 / QR); }
__device__ __forceinline__ const u16* x_src(const RecArgs& a, int seg, int b) {
  return seg < NSEG ? (const u16*)(a.ws + OFF_XN) + (size_t)seg * 32 * DM : a.xnew + (size_t)(qkv_row(b) + 16 * (seg - NSEG)) * 32 * DM;
}
template <int N>
__device__ __forceinline__ void img_fetch(const RecArgs& a, unsigned lds0, int h, int j, int part, int lane) {
#pragma unroll
  for (int i = 0; i < N; ++i) {
    const int n = part * N + i, idx = n * 64 + lane, row = idx / 6, pc = idx - row * 6;
    if (FR_PACE && N == 20) asm volatile("s_waitcnt vmcnt(%0)" :: "n"(FR_PACE) : "memory");
    glds16((pc < 3 ? a.wq : a.wk - 24) + (size_t)row * HD + h * DH + j * 24 + pc * 8, lds0 + S_IMG + n * 1024);
  }
}
constexpr int P2B = F4 + F5 + 2 * F67 + F6 + F7, PZ = P2B + F2B, PA = PZ, P0C = PA + 2 * F0A, F3C = 40 - F3A - F3 - P0C - 2 * F0C - F2C;
static_assert(F3C >= 0, "fetch plan");
template <int OFF> __device__ __forceinline__ void lds_inc(unsigned lds0) {
  asm volatile("s_mov_b64 exec, 1\n\tds_add_u32 %0, %1 offset:%2\n\ts_mov_b64 exec, -1" :: "v"(lds0), "v"(1u), "n"(OFF) : "memory");
}
__device__ __forceinline__ void lds_arrive(unsigned* p, int lane) {
  asm volatile("s_waitcnt lgkmcnt(0)" ::: "memory");
  if (lane == 0) __hip_atomic_fetch_add(p, 1u, __ATOMIC_RELAXED, __HIP_MEMORY_SCOPE_WORKGROUP);
}
__device__ __forceinline__ void lds_wait(unsigned* p, unsigned want) {
  while (__hip_atomic_load(p, __ATOMIC_RELAXED, __HIP_MEMORY_SCOPE_WORKGROUP) < want) __builtin_amdgcn_s_sleep(1);
  asm volatile("" ::: "memory");
}
constexpr float KEXP = 0.07216878364870322f * 1.4426950408889634f;
#define FRESH_LANE() int lane = lane0; asm volatile("" : "+v"(lane)); const int c = lane & 15, g = lane >> 4; (void)c; (void)g

struct OutTask {
  u32x4 y[4]; u32x2 xo, out;
  bf16x8 sel;
  unsigned t5f, t5o;
  __device__ __forceinline__ void init(int b, int lane) {
    const int um = b / 10, uq = (b / 5) & 1, up = b % 5, tok = lane & 15, k = up < 4 ? (up >> 1) * 256 + um * 16 + (up & 1) * 8 : 512 + um * 8, q4 = ((lane >> 4) & 1) * 4;
    t5f = (unsigned)(((uq * 20 + (k >> 5)) * 64 + ((k & 31) >> 3) * 16 + tok) * 8 + q4);
    t5o = (unsigned)(((um * 2 + uq) * 16 + (lane >> 4)) * 1280 + up * 256 + tok * 16);
    if (b >= 160) { t5f = 0u; t5o = 0x7ffffff0u; }
    sel = sel_frag(tok, 0);
    PIN_V(sel);
  }
  static __device__ __forceinline__ bool ok(int sl, int b) { return sl >= 0 && b < 160; }
  __device__ __forceinline__ void load(const RecArgs& a, const __amdgpu_buffer_rsrc_t rs, int sl, int b, int part = 3) {
    const unsigned o = t5o | (sl < 0 ? 0x7ff00000u : 0u);
    sl = imax(sl, 0);
#pragma unroll
    for (int k = 0; k < 4; ++k) if (part & 1) y[k] = SC1_LOADS(o, op_slot(sl) + k * 5120);
    if (part & 2) xo = __builtin_bit_cast(u32x2, __hip_atomic_load((const gu64*)(a.xold + (size_t)sl * 32 * DM + t5f), RLX_AGENT));
  }
  __device__ __forceinline__ bool ready(int b) { return b >= 160 || wave_all(umin(umin(min4(y[0]), min4(y[1])), umin(min4(y[2]), min4(y[3]))) != 0u); }
  __device__ __forceinline__ void run(int lane) {
    f32x4 d = {0.f, 0.f, 0.f, 0.f};
#pragma unroll
    for (int k = 0; k < 4; ++k) d = mfma32(sel, as8(y[k]), d);
#pragma unroll
    for (int i = 0; i < 2; ++i) {
      const unsigned r = pack2(d[2 * i], d[2 * i + 1]), x = xo[i];
      out[i] = pack2(u2f(x << 16) + u2f(r << 16), u2f(x & 0xffff0000u) + u2f(r & 0xffff0000u));
    }
    PIN_V(out);
  }
  __device__ __forceinline__ void store(const RecArgs& a, int sl, int b, int lane) {
    __builtin_amdgcn_raw_buffer_store_b64(out, __builtin_amdgcn_make_buffer_rsrc(a.xnew, 0, ok(sl, b) ? S_ * DM * 2 : 0, 0x00020000), (t5f + (unsigned)sl * 32 * DM) * 2u | (unsigned)(lane & 32) << 25, 0, 16);
  }
};

struct PvTask2 {
  u32x4 y[2][2], pa[2], pb[2];
  unsigned lo, lm, vr, ma, mz, so, sz;
  __device__ __forceinline__ void init(int gp, int lane) {
    const int hi = lane >> 5;
    lo = pt_tok(2 * gp) + (unsigned)(lane * 16); lm = pt_mem(2 * gp + hi) + (unsigned)((lane & 31) * 8);
    vr = (unsigned)(L_VOX + 2048 + (lane ^ 8) * 16);
    ma = (unsigned)(L_VOM + hi * 768 + (lane & 31) * 8); mz = (unsigned)(L_VOM + 512 + ((lane & 31) ^ (hi * 8)) * 8);
    so = (unsigned)(L_STG + 2048 + (hi * 16 + (lane & 15)) * 16 + ((lane >> 4) & 1) * 8);
    sz = hi ? (unsigned)(L_RED1 + 8704 + lane * 8) : so + 1024;
    PIN_V(lo); PIN_V(lm); PIN_V(vr); PIN_V(ma); PIN_V(mz); PIN_V(so); PIN_V(sz);
#pragma unroll
    for (int q = 0; q < 2; ++q) { pa[q] = pb[q] = y[q][0] = y[q][1] = (u32x4){0u, 0u, 0u, 0u}; PIN_V(pa[q]); PIN_V(pb[q]); }
  }
  __device__ __forceinline__ void load(const __amdgpu_buffer_rsrc_t rs, unsigned o) {
#pragma unroll
    for (int q = 0; q < 2; ++q) {
      y[q][0] = SC1_LOADS(lo + q * 1024, o); y[q][1] = SC1_LOADS(lo + 2048 + q * 1024, o);
      const u32x2 p = __builtin_amdgcn_raw_buffer_load_b64(rs, lm + q * 256, o, 16);
      pa[q][0] = p[0]; pa[q][1] = p[1];
    }
  }
  __device__ __forceinline__ bool ready() {
    return wave_all(umin(umin(umin(min4(y[0][0]), min4(y[0][1])), umin(min4(y[1][0]), min4(y[1][1]))), umin(umin(pa[0][0], pa[0][1]), umin(pa[1][0], pa[1][1]))) != 0u);
  }
  template <bool DIRECT> __device__ __forceinline__ void run(char* lds, int sv, int lane, const __amdgpu_buffer_rsrc_t rs, unsigned og) {
    const f32x4 Z4 = {0.f, 0.f, 0.f, 0.f};
    const char* vp = lds + L_VOX + sv * 5120 + lane * 16;
    bf16x8 vx[6];
#pragma unroll
    for (int t = 0; t < 5; ++t) vx[t] = *(const bf16x8*)(vp + t * 1024);
    vx[5] = *(const bf16x8*)(lds + vr + sv * 5120);
    const u32x2 a0 = *(const u32x2*)(lds + ma + sv * 1280), a1 = *(const u32x2*)(lds + ma + sv * 1280 + 256);
    const u32x2 z0 = *(const u32x2*)(lds + mz + sv * 1280), z1 = *(const u32x2*)(lds + mz + sv * 1280 + 256);
    const bf16x8 vm = mk8(a0[0], a0[1], a1[0], a1[1]), vz = mk8(z0[0], z0[1], z1[0], z1[1]);
#pragma unroll
    for (int q = 0; q < 2; ++q) { PIN_V(pa[q]); pb[q][2] = pa[q][0]; pb[q][3] = pa[q][1]; PIN_V(pb[q]); }
    __builtin_amdgcn_sched_barrier(0);
    f32x4 o[2][3];
#pragma unroll
    for (int q = 0; q < 2; ++q) {
#pragma unroll
      for (int t = 0; t < 3; ++t) o[q][t] = mfma32(vx[t], as8(y[q][0]), Z4);
#pragma unroll
      for (int t = 0; t < 3; ++t) o[q][t] = mfma32(vx[3 + t], as8(y[q][1]), o[q][t]);
      o[q][0] = mfma32(vm, as8(pa[q]), o[q][0]); o[q][1] = mfma32(vm, as8(pb[q]), o[q][1]); o[q][2] = mfma32(vz, as8(pa[q]), o[q][2]);
    }
#pragma unroll
    for (int q = 0; q < 2; ++q) {
#pragma unroll
      for (int t = 0; t < 3; ++t) {
        const u32x2 v = {pack2n(o[q][t][0], o[q][t][1]), pack2n(o[q][t][2], o[q][t][3])};
        if (DIRECT) SC1_STORE8S(v, (so - (L_STG + 2048) + t * 512) | (t == 2 ? (unsigned)(lane & 32) << 25 : 0u), og + q * 20480);
        else *(u32x2*)(lds + (t < 2 ? so + t * 512 : sz) + q * 1280) = v;
      }
    }
  }
};

struct TokScores {
  bf16x8 k[2], q[2]; u32x4 v[2];
  __device__ __forceinline__ void get(const char* lds, int seg, int lane) {
#pragma unroll
    for (int i = 0; i < 2; ++i) { k[i] = *(const bf16x8*)(lds + L_KX + (seg & 1) * 2048 + i * 1024 + lane * 16); q[i] = *(const bf16x8*)(lds + L_QX + (seg & 1) * 2048 + i * 1024 + lane * 16); }
  }
  __device__ __forceinline__ void run(int qt) {
    const f32x4 Z4 = {0.f, 0.f, 0.f, 0.f};
    const f32x4 d1 = mfma32(k[0], q[qt], Z4), d2 = mfma32(k[1], q[qt], Z4);
    v[qt] = (u32x4){pack2n(d1[0], d1[1]), pack2n(d1[2], d1[3]), pack2n(d2[0], d2[1]), pack2n(d2[2], d2[3])};
    PIN_V(v[qt]);
  }
  __device__ __forceinline__ void store(const __amdgpu_buffer_rsrc_t rs, unsigned off, unsigned o) {
    __builtin_amdgcn_raw_buffer_store_b128(v[0], rs, off, o, 16); __builtin_amdgcn_raw_buffer_store_b128(v[1], rs, off + 1024, o, 16);
    asm volatile("s_nop 1" ::: "memory");
  }
};

__device__ __forceinline__ void touch(const char* base, unsigned off, unsigned lds0) {
  unsigned keep;
  asm volatile("s_mov_b32 %0, m0\n\ts_mov_b32 m0, %3\n\ts_nop 0\n\tglobal_load_lds_dword %1, %2 sc1\n\ts_mov_b32 m0, %0" : "=&s"(keep) : "v"(off), "s"(base), "s"(lds0 + L_RED1 + 8192) : "memory");
}
template <int N> __device__ __forceinline__ void stouch(const char* p, unsigned& d) {
  asm volatile(".set pf_o, 0\n\t.rept %2\n\ts_load_dword %0, %1, pf_o\n\t.set pf_o, pf_o + 128\n\t.endr" : "+s"(d) : "s"(p), "n"(N) : "memory");
}
__device__ __forceinline__ void code_touch(int b, int lane, unsigned lds0) {
  const char* pc; int d0, d1;
  asm volatile("s_getpc_b64 %0\n\ts_mov_b32 %1, _Z10rec_kernel7RecArgs@rel32@lo+4\n\ts_mov_b32 %2, _Z11attb_kernelPKtS0_Pt@rel32@lo+12" : "=s"(pc), "=s"(d0), "=s"(d1));
  const int o = (lane * 32 + (b >> 3)) * 128;
  touch(pc + d0, (unsigned)(o < d1 - d0 ? o : 0), lds0);
}
__device__ __forceinline__ unsigned zero_base(int b, int h, int j, int ln) {
  return ln < 32 ? (unsigned)((h & 3) * 32768 + j * 512 + ln * 16) : ln < 40 ? (unsigned)((h & 2) * 1024 + j * 256 + (ln & 4) * 32 + (h & 1) * 64 + (ln & 3) * 16)
                 : (unsigned)OFF_ACC + (b < ACC_NB ? (unsigned)(b * 256 + (ln - 40) * 16) : ts_piece((b - ACC_NB) * 8 + ln - 40));
}
__device__ __forceinline__ unsigned sct_slot(unsigned ck, unsigned sl) { return sl * SCT_STEP + ((ck >> (sl & 4u)) & 7u) * 4096u; }
__device__ __forceinline__ unsigned scm_slot(unsigned ck, unsigned sl) { return OFF_SCM + sl * SCM_STEP + ((ck >> 8) & 7u) * 4096u; }
__device__ __forceinline__ int ck_rot(int x) { return ((x & 1) << 2) | (x >> 1); }
struct Place {
  unsigned t[8];
  __device__ __forceinline__ void load(const char* ws, int lane) {
#pragma unroll
    for (int x = 0; x < 8; ++x) t[x] = *(const unsigned*)(ws + OFF_LAT + ((imin(lane >> 3, 5) * 8 + (ck_rot(x) ^ (lane & 7))) * 8 + x) * 4);
  }
  __device__ __forceinline__ unsigned get(int b, int lane) {
    int c = (int)((((t[0] + t[1]) + (t[2] + t[3])) + ((t[4] + t[5]) + (t[6] + t[7]))) * 8u) + (lane & 7);
    c = imin(c, dppi<DPP_XOR1>(c)); c = imin(c, dppi<DPP_XOR2>(c)); c = imin(c, dppi<0x141>(c));
    unsigned k = 0u;
#pragma unroll
    for (int r = 0; r < 6; ++r) k |= (unsigned)(__builtin_amdgcn_readlane(c, 8 * r) & 7) << (4 * r);
    return (k ^ PL_X) ^ (unsigned)ck_rot(b & 7) * 0x111111u;
  }
};

struct Sched {
  u32x4 tsv, rec;
  unsigned side, o;
  __device__ __forceinline__ void pre(int s) { o = (unsigned)OFF_ACC + (unsigned)((s + SR - 1) % SR) * (unsigned)ACC_STEP + ACC_TS + side; PIN_S(o); }
  __device__ __forceinline__ void load(const __amdgpu_buffer_rsrc_t rs, int lane) {
    tsv = SC1_LOADS(ts_piece(lane & 63) - ACC_TS, o); rec = SC1_LOADS(128, o);
  }
  __device__ __forceinline__ unsigned last(unsigned ref) {
    const unsigned o = 0x40000000u - ref;
    int l = (int)umax(umax(tsv[0] ? tsv[0] + o : 0u, tsv[1] ? tsv[1] + o : 0u), umax(tsv[2] ? tsv[2] + o : 0u, tsv[3] ? tsv[3] + o : 0u));
    return (unsigned)wave_imax(l) - o;
  }
  __device__ __forceinline__ unsigned run(const RecArgs& a, const __amdgpu_buffer_rsrc_t rs, int s, int b, int lane) {
    constexpr int w = 3; (void)w;
    unsigned acur = __builtin_amdgcn_readfirstlane(rec[0]), aprev = __builtin_amdgcn_readfirstlane(rec[1]), hist = __builtin_amdgcn_readfirstlane(rec[3]);
    int D8 = (int)__builtin_amdgcn_readfirstlane(rec[2]);
    const int d = wave_imax((int)(umax(umax(tsv[0], tsv[1]), umax(tsv[2], tsv[3])) - aprev)), dd = 8 * d - D8;
    int Dn = D8 + (dd >> (dd > 0 ? SCH_GU : SCH_GD));
    unsigned anext = (acur + (unsigned)((Dn + 4) >> 3) + SCH_V + (s < EARLY_N ? EARLY_V : (s >= WPF0 ? (s == NSEG - 2 ? LAST_V : LATE_V) : 0))) | 1u;
    if (__builtin_expect(hist < 2u || aprev == 0u || d < 1 || d + SCH_VMIN > (int)(acur - aprev), 0)) {
      if (s == 0) {
        for (int it = 0; it < 40 && !wave_all(min4(tsv) != 0u); ++it) {
          __builtin_amdgcn_s_sleep(ST_ALL);
          asm volatile("" ::: "memory");
          load(rs, lane);
        }
      }
      const unsigned now = now32(), L = last(now);
      if (acur == 0u) { D8 = 8 * SCH_D0; hist = 0u; }
      const bool late = acur == 0u || (int)(L + SCH_VMIN - acur) > 0;
      if (late) { if (b == 0) FC(3); if (acur) D8 += 8 * (hist ? SCH_UP : 3 * SCH_UP); }
      else if (aprev) { if (hist < 2u) D8 = 8 * imin(imax((int)(L - aprev), 1), 4000); hist = umin(hist + 1u, 2u); }
      const unsigned base = acur == 0u ? L + SCH_FIRST : (late ? L + SCH_V + ((int)(L + SCH_VMIN2 - acur) <= 0 ? SCH_PEN2 : SCH_PEN) : acur);
      anext = (base + (unsigned)((D8 + 4) >> 3) + SCH_V + (s == 1 ? EARLY_V1 : (s < EARLY_N ? EARLY_V : 0))) | 1u;
      if (s == 0) anext = (L + ST_X) | 1u;
      if ((int)(now - L) > 50000) anext = 0u;
      if (late) acur = 0u;
      Dn = D8;
    }
    if (__builtin_expect(b == 0, 0))
      __builtin_amdgcn_raw_buffer_store_b128((u32x4){anext, acur, (unsigned)Dn, hist}, rs, lane < 2 ? lane * 8192 : 0x7ffffff0, (unsigned)OFF_ACC + (unsigned)(s % SR) * (unsigned)ACC_STEP + ACC_TS + 128u, 16);
    LOGV(0, anext); LOGV(1, acur == 0u); LOGV(2, (Dn + 4) >> 3); LOGV(3, d); LOGV(4, acur - aprev - d); LOGV(5, aprev);
    return anext;
  }
};

struct TokTask {
  u32x4 y[2]; u32x2 ym; u32x4 res;
  unsigned l0, l1, lm, s0, s1;
  static __device__ __forceinline__ bool ok(int sl) { return sl >= 0 && sl < NSEG; }
  __device__ __forceinline__ void init(int h, int j, int lane) {
    const int c = lane & 15, g = lane >> 4;
    l0 = ts_tok(h, g) + (unsigned)(j * 256 + c * 16); l1 = l0 + 16384u;
    lm = ts_mem(h, g + 4 * (c >> 3)) + (unsigned)(j * 64 + (c & 7) * 8);
    res = (u32x4){0u, 0u, 0u, 0u};
    const int qi = 4 * j + (c & 3);
    s0 = g < 2 ? pt_tok(h) + (unsigned)((qi >> 4) * 1024 + ((c >> 2) * 16 + (qi & 15)) * 16 + g * 8) : 0x7ffffff0u;
    s1 = lane < 8 ? pt_mem(h) + (unsigned)((qi >> 4) * 256 + ((c >> 2) * 16 + (qi & 15)) * 8) : 0x7ffffff0u;
    PIN_V(l0); PIN_V(l1); PIN_V(lm); PIN_V(s0); PIN_V(s1);
  }
  __device__ __forceinline__ void load(const __amdgpu_buffer_rsrc_t rs, unsigned o) {
    y[0] = SC1_LOADS(l0, o); y[1] = SC1_LOADS(l1, o);
    ym = __builtin_amdgcn_raw_buffer_load_b64(rs, lm, o, 16);
  }
  __device__ __forceinline__ bool ready() { return wave_all(umin(umin(min4(y[0]), min4(y[1])), umin(ym[0], ym[1])) != 0u); }
  __device__ __forceinline__ void run(int j, int lane, const bf16x8 SelLo) {
    const int c = lane & 15, g = lane >> 4;
    const f32x4 Z4 = {0.f, 0.f, 0.f, 0.f};
    const f32x4 d1 = mfma32(SelLo, as8(y[1]), mfma32(SelLo, as8(y[0]), Z4));
    const f32x4 d0 = mfma32(SelLo, mk8(ym[0], ym[1], 0u, 0u), Z4);
    const int qi = 4 * j + (c & 3), k0 = 16 * g + 4 * (c >> 2);
    float t[4], mv[4];
    float mx = -INFINITY;
#pragma unroll
    for (int r = 0; r < 4; ++r) {
      t[r] = (g < 2 && k0 + r <= qi) ? d1[r] : -INFINITY;
      const float mt = d0[r] + dppf<DPP_ROR8>(d0[r]);
      mv[r] = lane < 8 ? mt : -INFINITY;
      mx = fmx(mx, fmx(t[r], mv[r]));
    }
    mx = fmx(mx, dppf<DPP_ROR8>(mx)); mx = fmx(mx, dppf<DPP_ROR4>(mx));
    mx = x16mx(mx);
    float sum = 0.f;
#pragma unroll
    for (int r = 0; r < 4; ++r) {
      t[r] = __builtin_amdgcn_exp2f(t[r] - mx); mv[r] = __builtin_amdgcn_exp2f(mv[r] - mx);
      sum += t[r] + mv[r];
    }
    sum += dppf<DPP_ROR8>(sum); sum += dppf<DPP_ROR4>(sum);
    sum = x16sum(sum);
    const float rs_ = __builtin_amdgcn_rcpf(sum);
    res = (u32x4){pack2n(t[0] * rs_, t[1] * rs_), pack2n(t[2] * rs_, t[3] * rs_), pack2n(mv[0] * rs_, mv[1] * rs_), pack2n(mv[2] * rs_, mv[3] * rs_)};
    PIN_V(res);
  }
  __device__ __forceinline__ void store(const __amdgpu_buffer_rsrc_t rs, unsigned o, unsigned no = 0u) {
    SC1_STORE8S(((u32x2){res[0], res[1]}), s0 | no, o);
    SC1_STORE8S(((u32x2){res[2], res[3]}), s1 | no, o);
  }
};

constexpr int L_DRN = L_VOX + (NSEG % NV) * 5120;
__device__ __forceinline__ void drain_pre(char* lds, int b, int lane) {
  const int gp = (b & 7) * 2 + (b >> 7), m = (b >> 3) & 15;
  TokTask tk; PvTask2 pv; OutTask ot;
  tk.init(2 * gp + (m >> 3), m & 7, lane); pv.init(gp, lane); ot.init(b, lane);
  u32x4* p = (u32x4*)(lds + L_DRN) + lane;
  p[0] = (u32x4){tk.l0, tk.lm, tk.s0, tk.s1}; p[64] = (u32x4){pv.lo, pv.lm, pv.vr, pv.ma}; p[128] = (u32x4){pv.mz, pv.so, ot.t5f, ot.t5o};
  p[192] = __builtin_bit_cast(u32x4, ot.sel);
}
__device__ __forceinline__ void qkv_pre(unsigned (&vo)[2][2], int b, int w, int lane) {
  const int cb = qkv_col(b), c = lane & 15, g = lane >> 4, t0 = w & 1, col0 = (cb % 5) * 128 + w * 32;
  const bool SW = cb < 10;
#pragma unroll
  for (int i = 0; i < 2; ++i)
#pragma unroll
    for (int tt = 0; tt < 2; ++tt) {
      const int tq = tt ^ t0, tk = tq * 16 + c, cc = col0 + i * 16 + (SW ? 4 * g : c), hd = cc / BD, e = cc - hd * BD;
      const int kv = (int)(OFF_KV - OFF_QB) + hd * 64 * KVT;
      vo[i][tt] = (unsigned)(!SW ? kv + 1280 + tq * 672 + ((g >> 1) * 21 + e) * 16 + (g & 1) * 8
                                 : (cb < 5 ? (hd * S_ + tk) * 2 * QST + e * 2 : kv + (e < 16 ? (tk + 32 * (e >> 3)) * 16 + (e & 7) * 2 : 1024 + tk * 8)));
      PIN_V(vo[i][tt]);
    }
}

template <int w>
__device__ __forceinline__ void token_waves(const RecArgs& a, char* lds, const __amdgpu_buffer_rsrc_t rs, unsigned (&vo)[2][2]) {
  const int lane0 = __builtin_amdgcn_workitem_id_x() & 63, b = __builtin_amdgcn_workgroup_id_x(), wid = w + 4; (void)wid;
  const int gp = (b & 7) * 2 + (b >> 7), m = (b >> 3) & 15, h0 = 2 * gp, h = h0 + (m >> 3), j = m & 7;
  float* const misc = (float*)(lds + L_MISC);
  const f32x4 Z4 = {0.f, 0.f, 0.f, 0.f};
  const unsigned lds0 = (unsigned)(uintptr_t)(LAS char*)lds;
  unsigned* const cnt = (unsigned*)(misc + 6);
  const float* const xnrm = (const float*)(a.ws + OFF_XNRM);
  bf16x16 W[2][10];
  auto btile = [&](int i) { return w == 1 ? 0 : (w == 2 ? 1 + i : 3 + i); };
  if (w == 1) code_touch(b, lane0, lds0);
  Place pl;
  if (w == 3) pl.load(a.ws, lane0);
  if (a.layer) {
    if (w != 0) img_fetch<20>(a, lds0, h, j, w - 1, lane0);
  } else {
    if (w == 0) img_fetch<12>(a, lds0, h, j, 4, lane0);
#pragma unroll
    for (int i = 0; i < 10; ++i) x_fetch(x_src(a, 0, b), lds0, 0, 10 * w + i, lane0);
  }
#pragma unroll
  for (int i = 0; i < 2; ++i)
#pragma unroll
    for (int p = 0; p < 10; ++p)
      if (w >= 2 || (w == 1 && i == 1)) {
        const char* wp = a.ws + OFF_WB + ((size_t)(b * 5 + btile(i)) * 20 + 2 * p) * 1024 + lane0 * 16;
        if (FR_PACE && a.layer) asm volatile("s_waitcnt vmcnt(%0)" :: "n"(FR_PACE) : "memory");
        W[i][p] = cat16(*(const bf16x8*)wp, *(const bf16x8*)(wp + 1024));
      }
  f32x4 bn = Z4;
  if (w == 0) {
    bn = *(const f32x4*)(a.ws + OFF_BNRM + lane0 * 16);
    if (!a.layer) asm volatile("s_waitcnt vmcnt(0)" ::: "memory");
  } else asm volatile("s_waitcnt vmcnt(%0)" :: "n"(w == 1 ? 20 : 40) : "memory");
  unsigned ck = 0u;
  if (w == 3) { ck = pl.get(b, lane0); *(unsigned*)(misc + 11) = ck; }
  BAR();
#pragma unroll
  for (int p = 0; p < 10; ++p) {
    if (w < 2) W[0][p] = cat16(wqk_frag(lds, 2 * w, 2 * p, lane0), wqk_frag(lds, 2 * w, 2 * p + 1, lane0));
    if (w == 0) W[1][p] = cat16(wqk_frag(lds, 1, 2 * p, lane0), wqk_frag(lds, 1, 2 * p + 1, lane0));
  }
  float cb2 = 0.f;
  if (w == 0) {
    float n = __builtin_amdgcn_sqrtf(bn[0] + bn[1]) + __builtin_amdgcn_sqrtf(bn[2] + bn[3]);
    n += dppf<DPP_XOR1>(n); n += dppf<DPP_XOR2>(n); n += dppf<DPP_ROR4>(n); n += dppf<DPP_ROR8>(n);
    n = x32mx(x16mx(n));
    cb2 = u2f(__builtin_amdgcn_readfirstlane(f2u(1.15f * n * n)));
  }
  const float cm2 = u2f((f2u(cb2) & 0x7fffffu) | 0x40000000u);
  const int ek = imax((int)((f2u(cb2) >> 23) & 0xffu), 1) - 252;
  if (w >= 2) {
#pragma unroll
    for (int i = 0; i < 4; ++i) *(unsigned*)(lds + L_KX + (w - 2) * 4096 + i * 1024 + lane0 * 16 + 12) = 0u;
  }
  OutTask ot;
  const int zl = lane0 - 16;
  const bool zon = w == 3 && zl >= 0 && (zl < 40 || b < ACC_NB + 17);
  unsigned zo = zon ? zero_base(b, h, j, zl) + (zl < 32 ? sct_slot(ck, 0u) : (zl < 40 ? scm_slot(ck, 0u) : 0u)) : 0x7ffffff0u;
  unsigned zm = zon ? (zl < 32 ? SCT_STEP + 1u : (zl < 40 ? SCM_STEP : (unsigned)ACC_STEP)) : 0u;
  const unsigned zx = ((ck ^ (ck >> 4)) & 7u) * 4096u;
  if (w == 3) { PIN_V(zo); PIN_V(zm); }
  auto st_out = [&](int s, int lane, auto go) {
    if (w < 2) return;
    if (ST_E) lds_spin<L_MISC + 112, 1>(lds0, (unsigned)imax(s, 0)); else go();
    const int sl = s - 2 - LAG4;
    const char* sp = lds + L_STG + 2048 + (w & 1) * 1280;
    u32x4 v0 = *(const u32x4*)(sp + lane * 16), v1 = *(const u32x4*)(sp + 1024 + (lane & 15) * 16);
    const u32x4 z = {0u, 0u, 0u, 0u};
    unsigned oo = (sl < 0 ? 0x7ff00000u : op_slot(sl) + (unsigned)((m * 32 + (w & 1) * 16 + gp) * 1280)) + (unsigned)(lane * 16), za = 0x7ffffff0u;
    if (w == 3) {
      const unsigned zs = (unsigned)(s + 2) % SR;
      za = (zo + (s < 0 ? 1024u : zs) * (zm & ~1u)) ^ ((zm & 1u) * (zs & 4u ? zx : 0u));
#pragma unroll
      for (int i = 0; i < 4; ++i) { v1[i] = lane < 16 ? v1[i] : z[i]; PIN_V(v1[i]); }
    }
    za = lane < 16 ? oo + 1024 : za;
    PIN_V(za); PIN_V(oo); PIN_V(v0); PIN_V(v1);
    __builtin_amdgcn_sched_barrier(0);
    if (ST_E) go();
    SC1_STORE(v0, oo);
    SC1_STORE(v1, za);
    __builtin_amdgcn_sched_barrier(0);
    if (w == 2) ot.store(a, s - 2 - LAG5, b, lane);
    else {
      const unsigned xo_ = (unsigned)((s - 8) * 1024 + lane * 16);
      if (__builtin_expect((unsigned)(s - 8) < 20u, 0)) SC1_STORE(z, xo_ < X_BYTES / 256 ? (unsigned)(OFF_X + b * (X_BYTES / 256)) + xo_ : 0x7ffffff0u);
    }
  };
  STAMP_DECL(); ARRIVE_DECL();
  auto mma_seg = [&](int seg, int sv, int lane, auto pause) {
    const int c = lane & 15, g = lane >> 4;
    const char* xp = lds + L_XN + (seg & 1) * 40960 + lane * 16;
    f32x4 acc[2][2] = {{Z4, Z4}, {Z4, Z4}};
    bf16x8 x[4][2];
#pragma unroll
    for (int p = 0; p < 3; ++p) { x[p][0] = *(const bf16x8*)(xp + p * 1024); x[p][1] = *(const bf16x8*)(xp + (20 + p) * 1024); }
    unsigned eo[2];
#pragma unroll
    for (int ks = 0; ks < 20; ++ks) {
      if (ks == (w == 0 ? KP0 : (w == 1 ? KP1 : KP2))) { pause(); __builtin_amdgcn_sched_barrier(0); }
      if (ks == 17) {
#pragma unroll
        for (int i = 0; i < 2; ++i) {
          eo[i] = (unsigned)(w == 0 ? (i ? L_KX : L_QX) + (seg % NKQ) * 2048 + lane * 16
                                    : (w == 1 && i == 0 ? ((g >> 1) ? L_KX : L_QX) + (seg % NKQ) * 2048 + (2 * (g & 1) * 16 + c) * 16 + 8 : L_VOX + sv * 5120 + btile(i) * 1024 + lane * 16));
          if (i == 0 || w == 1) PIN_V(eo[i]);
        }
        if (w == 0) eo[1] = eo[0] + (L_KX - L_QX);
        if (w >= 2) eo[1] = eo[0] + 1024;
      }
      __builtin_amdgcn_sched_barrier(0);
#pragma unroll
      for (int q = 0; q < 4; ++q) {
        const int tt = 1 - (q >> 1), i = q & 1;
        const bool isqk = w == 0 || (w == 1 && i == 0);
        const bf16x8 wf = half8(W[i][ks >> 1], ks & 1);
        constexpr int q0 = UNIT && w < 2;
        if (q < q0) continue;
        acc[i][tt] = isqk ? mfma32(wf, x[ks & 3][tt], acc[i][tt]) : mfma32(x[ks & 3][tt], wf, acc[i][tt]);
        __builtin_amdgcn_sched_barrier(0);
        if (q - q0 < 2 && ks + 3 < 20) x[(ks + 3) & 3][q - q0] = *(const bf16x8*)(xp + ((q - q0) * 20 + ks + 3) * 1024);
        __builtin_amdgcn_sched_barrier(0);
      }
    }
#pragma unroll
    for (int i = 0; i < 2; ++i) {
      if (w + i == 1) { const float ks_ = (w == 0 || (g >> 1)) ? KEXP : 1.0f; acc[i][0] *= ks_; acc[i][1] *= ks_; }
      const u32x4 v = {pack2(acc[i][0][0], acc[i][0][1]), pack2(acc[i][0][2], acc[i][0][3]), pack2(acc[i][1][0], acc[i][1][1]), pack2(acc[i][1][2], acc[i][1][3])};
      char* p = lds + eo[i];
      if (w == 0) { *(u32x2*)(p) = (u32x2){v[0], v[1]}; if (i || !UNIT) *(u32x2*)(p + 1024) = (u32x2){v[2], v[3]}; }
      else if (w == 1 && i == 0) { *(unsigned*)(p) = v[0]; *(unsigned*)(p + 256) = v[1]; if (!UNIT) { *(unsigned*)(p + 1024) = v[2]; *(unsigned*)(p + 1280) = v[3]; } }
      else *(u32x4*)p = v;
    }
  };
  unsigned vp, mp, ip;
  int sidx, ei = 127;
  float inv;
  f32x4 acc[2];
  auto vo_pre = [&](int s, int sv, int lane) {
    vp = (unsigned)(w == 0 ? L_MISC + (s & 3) * 4 : L_VOM + sv * 1280 + (btile(0) * 32 + (w == 1 ? lane & 31 : lane)) * 8);
    mp = (unsigned)(L_BM + lane * 16);
    PIN_V(vp); PIN_V(mp);
    if (w == 0) { ip = (unsigned)(L_MISC + 16 + ((s + 1) & 1) * 4); PIN_V(ip); }
    else {
      inv = misc[4 + (s & 1)];
      sidx = (lane & 8) ? 0xEEEE : 0x4444;
      acc[0] = acc[1] = Z4;
      PIN_V(sidx); PIN_V(acc[1]);
      if (w >= 2) { PIN_V(acc[0]); PIN_V(inv); }
    }
    __builtin_amdgcn_sched_barrier(0);
  };
  auto vo_mem = [&](int s, auto last) {
    __builtin_amdgcn_s_setprio(2);
    asm volatile(".rept %0\n\ts_nop 3\n\t.endr" :: "n"(w == 0 ? VO_A0 : (w == 1 ? VO_A1 : VO_A2)) : "memory");
    float xmax = 0.f;
    if (w == 0) xmax = *(const float*)(lds + vp);
    constexpr int NX = w == 2 ? VO_N2 : (w == 3 ? VO_N3 : 10);
    bf16x8 x[10];
#pragma unroll
    for (int p = 0; p < NX; ++p) x[p] = *(const bf16x8*)(lds + mp + p * 1024);
    __builtin_amdgcn_sched_barrier(0);
    if (w < 2) asm volatile(".rept %0\n\ts_nop 3\n\t.endr" :: "n"(w == 0 ? VO_B0 : VO_B1) : "memory");
    if (VO_W >= 0) asm volatile("s_waitcnt lgkmcnt(%0)" :: "n"(VO_W < 0 ? 0 : VO_W) : "memory");
    STAMP(5);
    __builtin_amdgcn_sched_barrier(0);
    int ex = 0;
#pragma unroll
    for (int p = 0; p < 10; ++p) {
      if (w == 0) acc[0] = mfma32(x[p], x[p], p ? acc[0] : Z4);
      if (w == 0 && p == 9) { ex = imax((int)((__builtin_amdgcn_readfirstlane(f2u(__builtin_amdgcn_sqrtf(cb2 * xmax))) >> 23) & 0xffu) + 1, 32); PIN_S(ex); }
#pragma unroll
      for (int i = (w == 1); i < 2 && w != 0; ++i) acc[i] = smfma64(x[p], W[i][p], acc[i], sidx);
      __builtin_amdgcn_sched_barrier(0);
      if (p + NX < 10) x[p + NX] = *(const bf16x8*)(lds + mp + (p + NX) * 1024);
      __builtin_amdgcn_sched_barrier(0);
    }
    STAMP(7);
    if (w != 0) {
      float v[4];
#pragma unroll
      for (int r = 0; r < 4; ++r) {
        const auto sw = __builtin_amdgcn_permlane32_swap(f2u(acc[w == 1][r]), f2u(acc[1][r]), false, false);
        v[r] = (u2f(sw[0]) + u2f(sw[1])) * inv;
      }
      *(u32x2*)(lds + vp) = (u32x2){pack2(v[0], v[1]), pack2(v[2], v[3])};
    } else {
      const int t = (int)f2u(fmx(fmx(acc[0][0], acc[0][1]), fmx(acc[0][2], acc[0][3])) * cm2);
      const int et = wave_imax(t) >> 23, em = ((et + ek) >> 1) + ei;
      int e = imax(et == 255 ? 256 : em, ex);
      PIN_S(e);
      e = imin(e, 256);
      ei = e - 31;
      *(unsigned*)(lds + ip) = (unsigned)ei << 23;
      const u32x3 sr = {(unsigned)(285 - e) << 23, (unsigned)(e - 87) < 79u ? REC_RNG << 23 : 0u, (unsigned)(s + 1)};
      asm volatile("ds_write_b96 %0, %1 offset:%2" :: "v"(lds0), "v"(sr), "n"(L_MISC + 32) : "memory");
    }
    if (last == 0 && w >= 2) asm volatile("s_waitcnt vmcnt(%0)" :: "n"(w == SCH_W ? 2 : 0) : "memory");
    if (w != 0) lds_inc<L_MISC + 28>(lds0);
    STAMP(8);
    if (last) __builtin_amdgcn_s_setprio(0);
    if (last && w == 2) { VM_DRAIN(); lds_inc<L_MISC + 28>(lds0); }
  };
  unsigned* const cntq = (unsigned*)(misc + 12);
  unsigned* const cntt = (unsigned*)(misc + 26 + (w & 1));
  unsigned* const cntp = (unsigned*)(misc + 23);
  unsigned* const cntx2 = (unsigned*)(misc + 28);
  float xn4 = 0.f;
  auto wpf = [&](int s, int lane) {
    constexpr int PER = 81920 / QR / WPF_N;
    static_assert(PER % WPF_S == 0 && PER / WPF_S <= 64, "prefetch");
    touch(a.ws + OFF_WQKVBT + (size_t)imin(qkv_col(b), 14) * 163840 + (qkv_row(b) % (2 * QR)) * (81920 / QR), (s - WPF0) * PER + imin(lane, PER / WPF_S - 1) * WPF_S, lds0);
  };
  auto t5_get = [&](int s, int lane) {
    ot.load(a, rs, s - 1 - LAG5, b);
    xn4 = u2f(__hip_atomic_load((const GAS unsigned*)xnrm + (imax(s + 1, 0) * 32 + (lane & 31)) * 4, RLX_AGENT));
    __builtin_amdgcn_sched_barrier(0);
  };
  Sched sch;
  auto late = [&](int s, int lane) {
    if (w == SCH_W && SCH_LATE && __builtin_expect(s >= 0, 1)) *(u32x2*)(misc + 24) = (u32x2){sch.run(a, rs, s, b, lane), (unsigned)(s + 1)};
    if ((w == 2 && F6) || (w == 0 && F4) || (w == 1 && F5) || (w == 3 && F7)) { lds_spin<L_MISC + 48, 1>(lds0, 2u * (unsigned)imax(s + 1, 0)); if (FB_SL && w != 2) __builtin_amdgcn_s_sleep(FB_SL); }
    if (w >= 2) { x_fetch_n<w == 2 ? F6 : F7>(x_src(a, s + 3, b), lds0, s + 3, F4 + F5 + 2 * F67 + (w == 3 ? F6 : 0), lane * 16); return; }
    unsigned sd = 0u;
    if ((w == 0 && XPF == 3) || XPF == 4 || (w == 1 && XPF == 5)) stouch<10>(a.ws + OFF_XN + (size_t)imin(s + 4, NSEG - 1) * (s < 1 ? 0 : 40960) + (b >> 3) * 1280 + w * 64, sd);
    x_fetch_n<w == 0 ? F4 : F5>(x_src(a, s + 3, b), lds0, s + 3, w == 0 ? 0 : F4, lane * 16);
    if (WPF_W == 0 && w == 0 && __builtin_expect(s >= WPF0 && s < WPF0 + WPF_N, 0)) wpf(s, lane);
    if (w == 1 && __builtin_expect(s == CPF_S, 0)) code_touch(b, lane, lds0);
    lds_spin<L_MISC + 104 + 4 * (w & 1), PS_SL>(lds0, (unsigned)imax(s + 1, 0));
    asm volatile("" :: "s"(sd));
  };
  auto xn_put = [&](int s) {
    float xn2 = xn4;
    xn2 = fmx(xn2, dppf<DPP_XOR1>(xn2)); xn2 = fmx(xn2, dppf<DPP_XOR2>(xn2));
    xn2 = fmx(xn2, dppf<DPP_ROR4>(xn2)); xn2 = fmx(xn2, dppf<DPP_ROR8>(xn2));
    misc[s & 3] = x16mx(xn2);
  };
  if (w == 2) { ot.init(b, lane0); ot.out = (u32x2){0u, 0u}; ot.xo = ot.out; ot.y[0] = ot.y[1] = ot.y[2] = ot.y[3] = (u32x4){0u, 0u, 0u, 0u}; }
  VM_DRAIN();
  lds_arrive(cnt, lane0);
  lds_wait(cnt, 8u);
  sch.tsv = sch.rec = (u32x4){0u, 0u, 0u, 0u};
  sch.side = __builtin_amdgcn_readfirstlane(*(const unsigned*)(misc + 11)) & 8192u;
  sch.o = 0u;
  int sv = NV - 1;
  CODE_PAD((PADT >> (4 * w)) & 15);
#pragma unroll 1
  for (int s = -1; s < NSEG - 1; ++s) {
    const int sv1 = sv == NV - 1 ? 0 : sv + 1;
    FRESH_LANE();
    STAMP(0);
    mma_seg(s + 1, sv1, lane, [&]() { STAMP(1); late(s - 1, lane); if (w == SCH_W) sch.pre(s); STAMP(9); });
    STAMP(2);
    __builtin_amdgcn_sched_barrier(0);
    VM_DRAIN();
    if (w == 2) { ot.run(lane); xn_put(s); }
    STAMP(10);
    st_out(s, lane, [&]() { lds_spin<L_MISC + 92, W1_SL>(lds0, 4u * (unsigned)imax(s, 0)); });
    if (w == 2) t5_get(s, lane);
    __builtin_amdgcn_sched_barrier(0);
    if (w >= 2) vo_pre(s, sv, lane);
    STAMP(3);
    BAR();
    STAMP(4);
    if (__builtin_expect(s >= 0, 1)) {
      if (w == SCH_W) { sch.load(rs, lane0); __builtin_amdgcn_sched_barrier(0); }
      if (w < 2) vo_pre(s, sv, lane);
      vo_mem(s, std::integral_constant<int, 0>{});
      STAMP(6);
      if (w == SCH_W && !SCH_LATE) {
        __builtin_amdgcn_s_setprio(SCH_P);
        *(u32x2*)(misc + 24) = (u32x2){sch.run(a, rs, s, b, lane0), (unsigned)(s + 1)};
      }
      STAMP(11);
    } else BAR();
    __builtin_amdgcn_s_setprio(MMA_P);
    __builtin_amdgcn_sched_barrier(0);
    sv = sv1;
  }
  __builtin_amdgcn_s_setprio(0);
  STAMP_OUT();
  late(NSEG - 2, lane0);
  if (w == 2) ot.run(lane0);
  { FRESH_LANE(); if (w == 0) drain_pre(lds, b, lane); qkv_pre(vo, b, w, lane); }
  st_out(NSEG - 1, lane0, [&]() { lds_spin<L_MISC + 92, 1>(lds0, 4u * (NSEG - 1)); });
  if (w != 0) vo_pre(NSEG - 1, (NSEG - 1) % NV, lane0);
  BAR();
  if (w != 0) vo_mem(NSEG - 1, std::integral_constant<int, 1>{});
}

__device__ __forceinline__ void qkv_tail(const RecArgs& a, char* lds, const int w, const unsigned (&vo)[2][2]) {
  const int lane0 = __builtin_amdgcn_workitem_id_x() & 63, b = __builtin_amdgcn_workgroup_id_x();
  const int r = qkv_row(b), cb = qkv_col(b);
  if (cb >= 15) return;
  const unsigned lds0 = (unsigned)(uintptr_t)(LAS char*)lds;
  unsigned* const stage = (unsigned*)(lds + L_MISC) + 17;
  unsigned* const cnty = stage + 1;
  const f32x4 Z4 = {0.f, 0.f, 0.f, 0.f};
  auto fetch = [&](int i) {
    x_fetch_n<10>(x_src(a, NSEG + i, b), lds0, i, 10 * w, lane0 * 16);
  };
  unsigned want = 0;
  auto sync = [&]() { VM_DRAIN(); want += 4u; lds_inc<L_MISC + 72>(lds0); while (__hip_atomic_load(cnty, __ATOMIC_RELAXED, __HIP_MEMORY_SCOPE_WORKGROUP) < want) { } asm volatile("" ::: "memory"); };
  LOGQ(0);
  bf16x8 W[2][20];
  const __amdgpu_buffer_rsrc_t rw = __builtin_amdgcn_make_buffer_rsrc(a.ws + OFF_WQKVBT + (size_t)(cb * 8 + 2 * w) * 20480, 0, 40960, 0x00020000);
  auto w_get = [&](int ks) {
#pragma unroll
    for (int i = 0; i < 2; ++i) W[i][ks] = as8(__builtin_amdgcn_raw_buffer_load_b128(rw, lane0 * 16 + (ks & 3) * 1024, (i * 20 + (ks & ~3)) * 1024, 0));
    __builtin_amdgcn_sched_barrier(0);
  };
#pragma unroll
  for (int ks = 0; ks < QW; ++ks) w_get(ks);
  bf16x8 ones = mk8(0x3f803f80u, 0x3f803f80u, 0x3f803f80u, 0x3f803f80u);
  PIN_V(ones);
  const __amdgpu_buffer_rsrc_t ro = __builtin_amdgcn_make_buffer_rsrc(a.ws + OFF_QB, 0, (int)(OFF_OB - OFF_QB), 0x00020000);
  const int t0 = w & 1;
  auto body = [&](auto SW, auto SQ) {
    const float qs = cb < 5 ? 0.22360679774997896f * 1.4426950408889634f : 1.0f;
    f32x4 cs[2] = {Z4, Z4};
    LOGQ(1);
    f32x4 acc[2][2], st;
    auto mma = [&](int it, auto FIRST) {
      const char* xp0 = lds + L_XN + (it & 1) * 40960 + t0 * 20480 + lane0 * 16;
      const char* xp1 = lds + L_XN + (it & 1) * 40960 + (1 - t0) * 20480 + lane0 * 16;
      acc[0][0] = acc[0][1] = acc[1][0] = acc[1][1] = st = Z4;
      bf16x8 x[4][2];
#pragma unroll
      for (int p = 0; p < 3; ++p) { x[p][0] = *(const bf16x8*)(xp0 + p * 1024); x[p][1] = *(const bf16x8*)(xp1 + p * 1024); }
      __builtin_amdgcn_sched_barrier(0);
#pragma unroll
      for (int ks = 0; ks < 20; ++ks) {
        if (FIRST && ks + QW < 20) w_get(ks + QW);
#pragma unroll
        for (int q = 0; q < 4; ++q) {
          const int tt = 1 - (q >> 1), i = 1 - (q & 1);
          acc[i][tt] = SW ? mfma32(W[i][ks], x[ks & 3][tt], acc[i][tt]) : mfma32(x[ks & 3][tt], W[i][ks], acc[i][tt]);
          __builtin_amdgcn_sched_barrier(0);
          if (q < 2 && ks + 3 < 20) x[(ks + 3) & 3][q] = *(const bf16x8*)((q ? xp1 : xp0) + (ks + 3) * 1024);
          __builtin_amdgcn_sched_barrier(0);
        }
        st = mfma32(SQ ? x[ks & 3][0] : ones, x[ks & 3][0], st);
        if (FIRST) {
#pragma unroll
          for (int i = 0; i < 2; ++i) cs[i] = SW ? mfma32(W[i][ks], ones, cs[i]) : mfma32(ones, W[i][ks], cs[i]);
        }
        __builtin_amdgcn_sched_barrier(0);
      }
    };
    mma(0, std::integral_constant<bool, true>{});
#pragma unroll 1
    for (int it = 0; it < 4; ++it) {
      if (it) mma(it, std::integral_constant<bool, false>{});
      LOGQ(2 + 3 * it);
      FRESH_LANE();
      float* const sp = (float*)(lds + L_ST) + (it & 1) * 64;
      if (SQ) { if ((c >> 2) == g) sp[32 + t0 * 16 + c] = (c & 3) == 0 ? st[0] : ((c & 3) == 1 ? st[1] : ((c & 3) == 2 ? st[2] : st[3])); }
      else if (g == 0) sp[t0 * 16 + c] = st[0];
      sync();
      LOGQ(3 + 3 * it);
      if (it == 0) fetch(2);
      const bool late = r + 48 >= 55;
      if (it == 1 && !late) fetch(3);
      const unsigned so = (unsigned)((r + 16 * it) * (cb < 5 ? 64 * QST : KVT));
      auto stat = [&](float sm, float sq, float& mean, float& rstd) {
        mean = sm * (1.0f / DM);
        rstd = __builtin_amdgcn_rsqf(__builtin_fmaxf(sq * (1.0f / DM) - mean * mean, 0.f) + 1e-5f);
      };
#pragma unroll
      for (int tt = 0; tt < 2; ++tt) {
        const int tq = tt ^ t0;
        float mean[4], rstd[4];
        if (SW) {
          stat(sp[tq * 16 + c], sp[32 + tq * 16 + c], mean[0], rstd[0]);
          rstd[0] *= qs;
        } else {
          const f32x4 sm = *(const f32x4*)(sp + tq * 16 + 4 * g), sq = *(const f32x4*)(sp + 32 + tq * 16 + 4 * g);
#pragma unroll
          for (int k = 0; k < 4; ++k) stat(sm[k], sq[k], mean[k], rstd[k]);
        }
#pragma unroll
        for (int i = 0; i < 2; ++i) {
          float v[4];
#pragma unroll
          for (int k = 0; k < 4; ++k) v[k] = (acc[i][tt][k] - mean[SW ? 0 : k] * cs[i][SW ? k : 0]) * rstd[SW ? 0 : k];
          __builtin_amdgcn_raw_buffer_store_b64((u32x2){pack2(v[0], v[1]), pack2(v[2], v[3])}, ro, vo[i][tt], so, 0);
        }
      }
      LOGQ(4 + 3 * it);
      if (it == 2 && late) lds_spin<L_MISC + 68, 0>(lds0, 5u);
    }
  };
  typedef std::integral_constant<bool, false> F_; typedef std::integral_constant<bool, true> T_;
  if (cb < 10) { if (w >> 1) body(T_{}, T_{}); else body(T_{}, F_{}); }
  else { if (w >> 1) body(F_{}, T_{}); else body(F_{}, F_{}); }
}

__device__ __forceinline__ void t2_mem(const bf16x8 fr, const bf16x8 q0, const bf16x8 q1, const __amdgpu_buffer_rsrc_t rs, unsigned o, int h, int j, float inv, int lane) {
  const f32x4 Z4 = {0.f, 0.f, 0.f, 0.f};
  const f32x4 d0 = mfma32(fr, q0, Z4) * inv, d1 = mfma32(fr, q1, Z4) * inv;
  const unsigned a0 = pack2n(d0[0], d0[1]), a1 = pack2n(d0[2], d0[3]), b0 = pack2n(d1[0], d1[1]), b1 = pack2n(d1[2], d1[3]);
  const auto r0 = __builtin_amdgcn_permlane32_swap(a0, b0, false, false), r1 = __builtin_amdgcn_permlane32_swap(a1, b1, false, false);
  SC1_STORE8S(((u32x2){r0[1], r1[1]}), ts_mem(h, j) + (lane >> 5) * 256 + (lane & 12) * 16 + (lane & 16) * 2 + (lane & 3) * 8, o);
}

template <int w>
__device__ __forceinline__ void rec_waves(const RecArgs& a, char* lds, __amdgpu_buffer_rsrc_t rs, unsigned& tlast) {
  PIN_S(rs);
  const int lane = __builtin_amdgcn_workitem_id_x() & 63, b = __builtin_amdgcn_workgroup_id_x(), c = lane & 15, g = lane >> 4, wid = w; (void)wid;
  const int gp = (b & 7) * 2 + (b >> 7), m = (b >> 3) & 15, h0 = 2 * gp, h = h0 + (m >> 3), j = m & 7;
  float* const misc = (float*)(lds + L_MISC);
  const f32x4 Z4 = {0.f, 0.f, 0.f, 0.f};
  LOGP(0);
  __builtin_amdgcn_s_setprio(3);
  const unsigned lds0 = (unsigned)(uintptr_t)(LAS char*)lds;
  if (!a.layer) img_fetch<12>(a, lds0, h, j, w, lane);
  constexpr int NC = w < 2 ? 2 : 3;
  const int far = ((b >> 2) & 1) ^ 1;
  auto chunk = [&](int f) { return f == 0 ? 4 * far + w : (w == 2 && f == 1 ? 8 + far : (w == 3 && f == 2 ? 9 - far : 4 - 4 * far + w)); };
  bf16x16 Wqk[3][NC];
  u32x4 bm0[NC][2];
#pragma unroll
  for (int f = 0; f < 2 * NC; ++f) bm0[f >> 1][f & 1] = *(const u32x4*)(a.mem0 + (size_t)(c & 7) * DM + 64 * chunk(f >> 1) + 16 * g + 8 * (f & 1));
  if (!a.layer) asm volatile("s_waitcnt vmcnt(0)" ::: "memory");
  BAR();
  LOGP(9);
  const unsigned ck = __builtin_amdgcn_readfirstlane(*(const unsigned*)(misc + 11));
  const unsigned flip = (ck ^ (unsigned)(b << 11)) & 8192u;
#pragma unroll
  for (int t = 0; t < 3; ++t)
#pragma unroll
    for (int f = 0; f < NC; ++f) Wqk[t][f] = cat16(wqk_frag(lds, t, 2 * chunk(f), lane), wqk_frag(lds, t, 2 * chunk(f) + 1, lane));
  bf16x16 We[7];
  auto echunk = [&](int f) { return f < 6 ? (f / 3) * 4 + f % 3 + (f % 3 >= w) : (f == 6 ? 17 - chunk(w - 1) : chunk(f - 7)); };
  if (UNIT && w >= 2) {
#pragma unroll
    for (int f = 0; f < 7; ++f) We[f] = cat16(wqk_frag(lds, 2 * w - 4, 2 * echunk(f), lane), wqk_frag(lds, 2 * w - 4, 2 * echunk(f) + 1, lane));
  }
  auto unit = [&](int seg) {
    if (!UNIT || w < 2) return;
    const char* xp = lds + L_XN + (seg & 1) * 40960 + 20480 + lane * 16;
    bf16x8 x[20];
    f32x4 d = Z4;
#pragma unroll
    for (int k = 0; k < 26; ++k) {
      if (k >= 6) d = mfma32(half8(k - 6 < 14 ? We[(k - 6) >> 1] : Wqk[w < 2 ? 0 : 2 * w - 4][k - 6 < 14 ? 0 : ((k - 6) >> 1) - 7], k & 1), x[k - 6], d);
      __builtin_amdgcn_sched_barrier(0);
      if (k < 20) x[k] = *(const bf16x8*)(xp + echunk(k >> 1) * 2048 + (k & 1) * 1024);
      __builtin_amdgcn_sched_barrier(0);
    }
    if (w == 2) *(u32x2*)(lds + L_QX + (seg % NKQ) * 2048 + 1024 + lane * 16) = (u32x2){pack2(d[0], d[1]), pack2(d[2], d[3])};
    else {
      d *= (g >> 1) ? KEXP : 1.0f;
      char* p = lds + ((g >> 1) ? L_KX : L_QX) + (seg % NKQ) * 2048 + 1024 + (2 * (g & 1) * 16 + c) * 16 + 8;
      *(unsigned*)p = pack2(d[0], d[1]); *(unsigned*)(p + 256) = pack2(d[2], d[3]);
    }
    __builtin_amdgcn_sched_barrier(0);
  };
  if (!a.layer) lds_arrive((unsigned*)(misc + 6), lane);
  else {
    if (w == 0) {
      for (unsigned spins = 0; spins < SPIN_CAP; ++spins) {
        if (__builtin_amdgcn_raw_buffer_load_b32(rs, (b & 7) * 256, (unsigned)OFF_RDY, 16) >= 16u) break;
        __builtin_amdgcn_s_sleep(4);
        asm volatile("" ::: "memory");
      }
      lds_inc<L_MISC + 84>(lds0);
    }
    lds_wait((unsigned*)(misc + 21), 2u);
#pragma unroll
    for (int f = 0; f < 2 * NC; ++f) PIN_V(bm0[f >> 1][f & 1]);
#pragma unroll
    for (int i = 0; i < 10; ++i) x_fetch(x_src(a, 0, b), lds0, 0, 10 * w + i, lane);
  }
  const bf16x8 SelLo = sel_frag(c, 0), SelHi = sel_frag(c, 8);
  bf16x8 ONES = mk8(0x3f803f80u, 0x3f803f80u, 0x3f803f80u, 0x3f803f80u);
  if (w < 2) PIN_V(ONES);
  bool dead = false;
  float inv = 1.0f;
  unsigned* const cnt2 = (unsigned*)(misc + 7);
  unsigned l2off[2 * NC];
#pragma unroll
  for (int f = 0; f < 2 * NC; ++f) { l2off[f] = acc_off(16 * chunk(f >> 1) + 4 * g + 2 * (f & 1) + (c >> 3), c & 7) ^ flip; PIN_V(l2off[f]); }
  int sidx = (c >> 3) ? 0xEEEE : 0x4444;
  PIN_V(sidx);
  const unsigned p1off = (unsigned)((w == 0 ? (h & 2) * 1024 + j * 256 + (h & 1) * 64 + (g & 1) * 8 : (h & 3) * 32768 + j * 512 + (g >> 1) * 256 + (g & 1) * 64) + (c & 4) * 32 + (c & 3) * 16);
  unsigned p1v = (c < 8) && (w != 0 || g >= 2) ? p1off : 0x7ffffff0u, p1cur = 0x7ffffff0u;
  PIN_V(p1v); PIN_V(p1cur);
  const int hd = (c >> 2) & 1, mr = hd ? 3 - (c & 3) : (c & 3);
  const unsigned l1off = (unsigned)(((h0 + hd) & 3) * 32768 + g * 512 + (w & 1) * 128 + (c >> 3) * 64 + mr * 16);
  const unsigned l1offm = (unsigned)((h0 & 2) * 1024 + (g + 4 * (c >> 3)) * 256 + (w & 1) * 128 + hd * 64 + mr * 16);
  const int plane = (lane & ~6) | ((lane & 2) << 1) | ((lane & 4) >> 1);
  unsigned acco = c < 8 ? (acc_off(64 * far + m * 4 + g, 4 * (w & 1) + mr) + (unsigned)(hd * 8)) ^ flip : 0x7ffffff0u;
  unsigned acco2 = c >= 8 ? (acc_off(64 - 64 * far + m * 4 + g, 4 * (w & 1) + mr) + (unsigned)(hd * 8)) ^ flip
                          : (c < 4 ? (acc_off(128 + m * 2 + (g & 1), 4 * (w & 1) + mr) + (unsigned)((g >> 1) * 8)) ^ flip : 0x7ffffff0u);
  if (w < 2) { PIN_V(acco); PIN_V(acco2); }
  unsigned bmo[NC];
#pragma unroll
  for (int f = 0; f < NC; ++f) { bmo[f] = (unsigned)(chunk(f) * 1024 + lane * 16); PIN_V(bmo[f]); }
  bf16x8 bm[NC];
  auto bm_put = [&](int f) { *(bf16x8*)(lds + L_BM + bmo[f]) = bm[f]; __builtin_amdgcn_sched_barrier(0); };
  f32x4 acc[3] = {Z4, Z4, Z4};
  const int rt2 = ((c >> 3) * 8 + 4 * (g & 1)) * 28 + 16 + (c & 7);
  unsigned redo[3] = {(unsigned)(4 * g * 28 + c), (unsigned)(g < 2 ? (4 * g + 2) * 28 + c : rt2), (unsigned)(g < 2 ? (4 * g + 10) * 28 + c : rt2 + 56)};
#pragma unroll
  for (int k = 0; k < 3; ++k) { redo[k] = L_RED1 + w * 1792 + redo[k] * 4; PIN_V(redo[k]); }
  auto red_put = [&]() {
    auto hs = [&](float x, float y) { const auto sw = __builtin_amdgcn_permlane32_swap(f2u(x), f2u(y), false, false); return u2f(sw[0]) + u2f(sw[1]); };
#pragma unroll
    for (int k = 0; k < 3; ++k) {
#pragma unroll
      for (int i = 0; i < 2; ++i) *(float*)(lds + redo[k] + i * 112) = k == 0 ? hs(acc[0][i], acc[1][i]) : hs(acc[k - 1][2 + i], acc[2][2 * k - 2 + i]);
      __builtin_amdgcn_sched_barrier(0);
    }
  };
#pragma unroll
  for (int f = 0; f < NC; ++f) {
    const u32x4 u = bm0[f][0], v = bm0[f][1];
    bm[f] = c < 8 ? mk8(u[0], u[2], v[0], v[2]) : mk8(u[1], u[3], v[1], v[3]);
    bm_put(f);
#pragma unroll
    for (int t = 0; t < 3; ++t) acc[t] = smfma64(bm[f], Wqk[t][f], acc[t], sidx);
  }
  red_put();
  VM_DRAIN();
  if (a.layer) lds_arrive((unsigned*)(misc + 6), lane);
  lds_wait((unsigned*)(misc + 6), 8u);
#pragma unroll
  for (int i = 0; i < 10; ++i) x_fetch(x_src(a, 1, b), lds0, 1, 10 * w + i, lane);
  LOGP(1);
  unit(0);
  BAR();
  STAMP_DECL(); ARRIVE_DECL();
  bf16x8 fr, qx[2];
  auto kq_get = [&](int seg) {
    const char* p = lds + (w == 1 ? L_KX : L_QX) + (seg % NKQ) * 2048 + lane * 16;
    if (w == 1 || w == 2) { qx[0] = *(const bf16x8*)p; qx[1] = *(const bf16x8*)(p + 1024); }
  };
  unsigned qo4 = (unsigned)(L_RED1 + (w == 1 ? (c & 7) * 112 + (c >> 3) * 3584 : c * 112) + g * 16), qo2 = qo4 + 64u - (unsigned)(g * 8), qo2b = qo2 + 3584u;
  if (w < 3) { PIN_V(qo4); PIN_V(qo2); if (w != 1) PIN_V(qo2b); }
  auto pub1 = [&](unsigned off, unsigned o1) {
    if (w == 0) fr = qk_frag(lds + qo4, lds + qo2, lds + qo2b);
    else {
      const f32x4 a0 = *(const f32x4*)(lds + qo4), a1 = *(const f32x4*)(lds + qo4 + 1792);
      const f32x2 b0 = *(const f32x2*)(lds + qo2), b1 = *(const f32x2*)(lds + qo2 + 1792);
      float v[6];
#pragma unroll
      for (int i = 0; i < 6; ++i) v[i] = i < 4 ? a0[i] + a1[i] : b0[i - 4] + b1[i - 4];
#pragma unroll
      for (int i = 0; i < 6; ++i) v[i] += dppf<DPP_ROR8>(v[i]);
      fr = mk8(pack2(v[0], v[1]), pack2(v[2], v[3]), pack2(v[4], v[5]), 0u);
    }
    const f32x4 sc = mfma32(w == 0 ? fr : qx[0], fr, Z4);
    if (w == 0) SC1_STORE8S(((u32x2){pack2n(sc[0], sc[1]), pack2n(sc[2], sc[3])}), off, o1);
    else {
      const f32x4 s2 = mfma32(qx[1], fr, Z4);
      __builtin_amdgcn_raw_buffer_store_b128((u32x4){pack2n(sc[0], sc[1]), pack2n(sc[2], sc[3]), pack2n(s2[0], s2[1]), pack2n(s2[2], s2[3])}, rs, off, o1, 16);
    }
  };
  VM_DRAIN();
  kq_get(0);
  unsigned o1 = sct_slot(ck, 0u), o1m = scm_slot(ck, 0u);
  unsigned t2o = ts_tok(h, j) + (unsigned)((c >> 2) * 256 + g * 64 + (c & 3) * 16);
  TokScores t2;
  if (w == 3) { PIN_V(t2o); t2.get(lds, 0, lane); t2.run(0); t2.run(1); t2.store(rs, t2o, ts_slot(ck, 0)); }
  if (w < 2) pub1(p1v, w == 0 ? o1m : o1);
  const unsigned tst = now32() | 1u;
  if (w == 3 && ST_X) __builtin_amdgcn_raw_buffer_store_b32(tst, rs, lane < 2 ? ts_piece(b >> 2) - ACC_TS + (b & 3) * 4 + lane * 8192 : 0x7ffffff0, (unsigned)(OFF_ACC + (SR - 1) * ACC_STEP) + ACC_TS, 16);
  LOGP(2);
  BAR();
  LOGP(3);
#pragma unroll
  for (int i = 0; i < 10; ++i) x_fetch(x_src(a, 2, b), lds0, 2, 10 * w + i, lane);
  unit(1);
  VM_DRAIN();
  unsigned o2 = (unsigned)OFF_ACC, o2p = 0u, tw = 0u;
  asm volatile("" : "+s"(o2p));
  unsigned tc0 = 0u, tstar = 0u;
  int dmin = 0, dmax = 0;
  int e1 = 100000;
  unsigned offs = 100u;
  TokTask tk;
  PvTask2 pv2;
  if (w == 3) pv2.init(gp, lane);
  if (w == 2) tk.init(h, j, lane);
  unsigned o4 = 0u;
  unsigned o3, o3p;
  u32x16 P = {0u, 0u, 0u, 0u, 0u, 0u, 0u, 0u, 0u, 0u, 0u, 0u, 0u, 0u, 0u, 0u}, V = P;
  u32x8 V8 = {0u, 0u, 0u, 0u, 0u, 0u, 0u, 0u};
  if (w < 2) asm volatile("" : "+{v[208:223]}"(P), "+{v[184:191]}"(V8), "+{v[192:207]}"(V));
  unsigned tso = lane < 2 ? ts_piece(b >> 2) + (unsigned)((b & 3) * 4 + lane * 8192) : 0x7ffffff0u;
  if (w < 2) PIN_V(tso);
  float minf = g >= 2 ? -INFINITY : 0.f;
  if (w < 2) PIN_V(minf);
  int sv = NV - 1;
  bf16x8 vox[5];
  unsigned vma[3], need2;
  u32x4 slo, shi;
  float k1;
  const u16* xsrc;
  float invv = 1.0f;
  auto prep0 = [&](int s) {
    xsrc = x_src(a, s + 3, b);
    if (w == 2) { o3p = ts_slot(ck, (s + 1 - LAG3) & 3); o3 = ts_slot(ck, imax(s, 0)); o4 = pt_slot(ck, imax(s - LAG3, 0)); PIN_S(o3); }
    if (w == 3) { o3p = pt_slot(ck, (s - LAG4) & 3); o4 = ts_slot(ck, s + 1); }
    PIN_S(xsrc); PIN_S(o3p); PIN_S(o4);
    __builtin_amdgcn_sched_barrier(0);
  };
  auto prep = [&](int s) {
    o2 = (unsigned)OFF_ACC + (unsigned)(s % SR) * (unsigned)ACC_STEP;
    sv = sv == NV - 1 ? 0 : sv + 1;
    PIN_S(o2); PIN_S(sv);
    if (w < 2) {
      PIN_S(tc0);
      o1 = sct_slot(ck, s % SR); o1m = scm_slot(ck, s % SR);
      need2 = 4u * (unsigned)(s + 1) + 2u * ((unsigned)(s + 2) / NSEG);
      PIN_S(o1); PIN_S(o1m); PIN_S(need2);
      __builtin_amdgcn_sched_barrier(0);
      kq_get(s);
    }
    invv = misc[4 + (s & 1)];
  };
  auto prep2 = [&](int s) {
#pragma unroll
    for (int k = 0; k < 3; ++k) {
      const int t = k == 0 ? 2 : (k == 1 ? far : 1 - far);
      const char* p = lds + L_VOX + sv * 5120 + t * 1024 + (plane & 47) * 16 + (g & 1) * 8;
      auto rd = [&](const char* q) { const u32x2 a0 = *(const u32x2*)q, a1 = *(const u32x2*)(q + 256); return mk8(a0[0], a0[1], a1[0], a1[1]); };
      vox[k == 0 ? 0 : 2 * k - 1] = rd(p);
      if (k) vox[2 * k] = rd(p + 3072);
      vma[k] = lds0 + (unsigned)(L_VOM + sv * 1280 + t * 256 + (plane & 31) * 8);
      PIN_V(vma[k]);
    }
    __builtin_amdgcn_sched_barrier(0);
    inv = u2f(__builtin_amdgcn_readfirstlane(f2u(invv)));
    slo = __builtin_bit_cast(u32x4, SelLo); shi = __builtin_bit_cast(u32x4, SelHi);
#pragma unroll
    for (int i = 0; i < 4; ++i) { slo[i] = ((slo[i] >> 7) & 0x10001u) * (f2u(inv) >> 16); shi[i] = ((shi[i] >> 7) & 0x10001u) * (f2u(inv) >> 16); }
    if (F0A) { xsrc = x_src(a, s + 3, b); PIN_S(xsrc); }
    k1 = KEXP * inv;
    PIN_V(k1); PIN_V(slo); PIN_V(shi);
    asm volatile("" : "+v"(vox[0]), "+v"(vox[1]), "+v"(vox[2]), "+v"(vox[3]), "+v"(vox[4]));
    __builtin_amdgcn_sched_barrier(0);
  };
  auto rows = [&](int s) {
    const int dl = spin_until(tw);
    if (w < 2) tc0 = cyc32();
    __builtin_amdgcn_sched_barrier(0);
    STAMP(1);
    unsigned spins = 0;
    auto pass = [&](bool first) {
      u32x4 x[2 * NC];
#pragma unroll
      for (int f = 0; f < NC; ++f) {
        if (f && w < 2 && first) { prep(s); asm volatile(".rept %0\n\ts_nop 3\n\t.endr" :: "n"(P2_G1)); }
        else if (f) asm volatile(".rept %0\n\ts_nop 3\n\t.endr" :: "n"(w < 2 ? P2_G0 : (f == 1 ? P2_G2 : P2_G3)));
        __builtin_amdgcn_sched_barrier(0);
        x[2 * f] = SC1_LOADS(l2off[2 * f], o2p); x[2 * f + 1] = SC1_LOADS(l2off[2 * f + 1], o2p);
        __builtin_amdgcn_sched_barrier(0);
      }
      if (first) lds_inc<L_MISC + 92>(lds0);
      if (first) { if (w >= 2) prep(s); else prep2(s); }
      if (w >= 2 && first && P2_W) lds_spin<L_MISC + 92, 0>(lds0, 4u * (unsigned)s);
      if (w == 2 && first) tk.store(rs, o4, s > LAG3 ? 0u : 0x7ff00000u);
      if (w == 3 && first) t2.store(rs, t2o, o4);
      if (first && w < 2) {
        p1cur = p1v; offs = (offs - (unsigned)dl) & ~3u;
        PIN_V(p1cur); PIN_S(offs);
        tstar = tc0 + (unsigned)(e1 + D1_M + (w == 1 ? D1_W1 : 0)) - offs * 20u;
        dmin = offs < 40u ? D1_MIN : REC_D1;
        dmin = s < EARLY_N ? REC_D1 + EARLY_D1 : dmin;
        dmax = REC_D1;
        PIN_S(tstar); PIN_S(dmin); PIN_S(dmax);
      }
      __builtin_amdgcn_sched_barrier(0);
      unsigned ok = 32u;
#pragma unroll
      for (int t = 0; t < 3; ++t) { acc[t] = Z4; PIN_V(acc[t]); }
      __builtin_amdgcn_sched_barrier(0);
      bool r = true;
#pragma unroll
      for (int f = 0; f < NC; ++f) {
        unsigned p[4];
#pragma unroll
        for (int k = 0; k < 2; ++k) {
          const u32x4 v = x[2 * f + k];
          p[2 * k] = pack2((float)(int)v[0], (float)(int)v[1]); p[2 * k + 1] = pack2((float)(int)v[2], (float)(int)v[3]);
          asm volatile("" : "+v"(p[2 * k]), "+v"(p[2 * k + 1]));
          __builtin_amdgcn_sched_barrier(0);
        }
        ok = (ok & x[2 * f][0] & x[2 * f][2]) & (x[2 * f + 1][0] & x[2 * f + 1][2]);
        PIN_V(ok);
        __builtin_amdgcn_sched_barrier(0);
        bm[f] = mk8(p[0], p[1], p[2], p[3]);
        acc[0] = smfma64(bm[f], Wqk[0][f], acc[0], sidx);
        __builtin_amdgcn_sched_barrier(0);
        bm_put(f);
        acc[1] = smfma64(bm[f], Wqk[1][f], acc[1], sidx);
        __builtin_amdgcn_sched_barrier(0);
        if (f == NC - 1) { u64 bad = __builtin_amdgcn_ballot_w64(ok == 0u); PIN_S(bad); r = bad == 0ull; }
        __builtin_amdgcn_sched_barrier(0);
        acc[2] = smfma64(bm[f], Wqk[2][f], acc[2], sidx);
        __builtin_amdgcn_sched_barrier(0);
      }
      red_put();
      return r;
    };
    if (__builtin_expect(!pass(true), 0)) {
      FC(0);
      do {
        if (dead || ++spins > SPIN_CAP) { dead = true; break; }
        asm volatile("" ::: "memory");
      } while (!pass(false));
    }
    STAMPV(12, spins); LOGF(1, spins);
    TSTAMP(2, now32()); TSTAMP(3, spins);
  };
  if (w >= 2) prep0(-1);
  prep(0);
  if (w < 2) prep2(0);
  CODE_PAD((PADR >> (4 * w)) & 15);
#pragma unroll 1
  for (int s = 0;; ++s) {
    if (__builtin_expect(o2p != 0u, 1)) rows(s);
    if (w < 2 && __builtin_expect(s == NSEG - 1, 0)) break;
    STAMP(2);
    BAR();
    STAMP(3);
    if (w < 2) pub1(p1cur, w == 0 ? o1m : o1);
    if (w == 2) tk.load(rs, o3p);
    if (w == 3) pv2.load(rs, o3p);
    const unsigned tp1 = cyc32();
    __builtin_amdgcn_sched_barrier(0);
    if (w == 2) {
      __builtin_amdgcn_s_setprio(W2_P);
      prep0(s);
      if (W2_D) spin_cyc(tp1 + W2_D);
      fr = qk_frag(lds + qo4, lds + qo2, lds + qo2b);
      t2_mem(fr, qx[0], qx[1], rs, o3, h, j, invv * KEXP, lane);
      __builtin_amdgcn_sched_barrier(0);
      asm volatile("s_waitcnt vmcnt(1)" ::: "memory");
      lds_inc<L_MISC + 28>(lds0);
      kq_get(s + 1);
    }
    if (w == 3) {
      __builtin_amdgcn_s_setprio(T4_P);
      prep0(s);
      t2.get(lds, s + 1, lane); t2.run(0); t2.run(1);
      __builtin_amdgcn_sched_barrier(0);
    }
    if (w >= 2 && __builtin_expect(s == NSEG - 2, 0)) { VM_DRAIN(); lds_inc<L_MISC + 28>(lds0); }
    if (w >= 2) { __builtin_amdgcn_s_setprio(UN_P); unit(s + 2); }
    TSTAMP(0, now32());
    STAMP(4);
    __builtin_amdgcn_sched_barrier(0);
    unsigned tpub = 0;
    u32x2 sw;
    if (w == 2) {
      STAMP(5);
      __builtin_amdgcn_sched_barrier(0);
      STAMP(6);
      ARRIVE(0);
      tk.run(j, lane, SelLo);
      if (__builtin_expect(s == NSEG - 1, 0)) { tlast = tp1; break; }
      if (F2B) { lds_spin<L_MISC + 48, 1>(lds0, 2u * (unsigned)(s + 1)); x_fetch_n<F2B>(xsrc, lds0, s + 3, P2B, lane * 16); }
      __builtin_amdgcn_s_setprio(SP_P);
    }
    if (w == 3) {
      pv2.run<false>(lds, sv < 1 + LAG4 ? sv + NV - 1 - LAG4 : sv - 1 - LAG4, lane, rs, 0u);
      if (__builtin_expect(s == NSEG - 1, 0)) { tlast = tp1; break; }
      if (ST_E) lds_inc<L_MISC + 112>(lds0);
      STAMP(11);
      __builtin_amdgcn_s_setprio(SP_P);
      STAMP(5);
      ARRIVE(0);
      if (F3) { lds_spin<L_MISC + 48, 1>(lds0, 2u * (unsigned)(s + 1)); x_fetch_n<F3>(xsrc, lds0, s + 3, 40 - F3C - F3, lane * 16); }
      STAMP(6);
    }
    if (w < 2) {
      __builtin_amdgcn_s_setprio(D1_P);
      x_fetch_n<F0A>(xsrc, lds0, s + 3, PA + w * F0A, lane * 16);
      int dq = imin((int)(tstar - tp1), dmax);
      dq = dq < -D1_LATE ? dmax : dq;
      PIN_S(dq);
      spin_exact(tp1 + (unsigned)imax(dq, dmin));
      STAMP(5);
      u32x32 S;
      auto sc_load = [&]() {
        asm volatile("buffer_load_dwordx4 v[224:227], %2, %4, %5 offen sc1\n\t.rept %7\n\ts_nop 3\n\t.endr\n\t"
                     "buffer_load_dwordx4 v[228:231], %3, %4, %6 offen sc1\n\tbuffer_load_dwordx4 v[232:235], %3, %4, %6 offen offset:2048 sc1\n\t"
                     "buffer_load_dwordx4 v[236:239], %3, %4, %6 offen offset:256 sc1\n\tbuffer_load_dwordx4 v[240:243], %3, %4, %6 offen offset:2304 sc1\n\t"
                     "v_mov_b32 v220, 0x42000000\n\tv_mov_b32 v221, 0\n\tv_mov_b32 v222, 0x42000000\n\tv_mov_b32 v223, 0"
                     : "=&{v[224:255]}"(S), "+{v[208:223]}"(P) : "v"(l1offm), "v"(l1off), "s"(rs), "s"(o1m), "s"(o1), "n"(P1_G) : "memory");
      };
      sc_load();
      __builtin_amdgcn_sched_barrier(0);
      lds_inc<L_MISC + 48>(lds0);
      STAMP(6);
      ARRIVE(0);
      lds_spin<L_MISC + 28, 0>(lds0, need2);
      __builtin_amdgcn_s_setprio(3);
      asm volatile("ds_read_b64 v[192:193], %3\n\tds_read_b64 v[196:197], %3 offset:768\n\tds_read_b64 v[200:201], %4\n\tds_read_b64 v[204:205], %4 offset:768\n\tds_read_b64 v[184:185], %2"
                   : "+{v[184:191]}"(V8), "+{v[192:207]}"(V) : "v"(vma[0]), "v"(vma[1]), "v"(vma[2]) : "memory");
      __builtin_amdgcn_sched_barrier(0);
      {
        const int de = imin((int)(tp1 - tc0) - e1, D1_C);
        e1 += s == 0 ? 0 : de >> (s < EARLY_N && de < -D1_C ? 0 : D1_G);
        PIN_S(e1);
      }
      STAMP(11);
      __builtin_amdgcn_sched_barrier(0);
      {
        unsigned spins = 0;
        u64 miss, bad;
#define MM(d, a, b, c) "v_mfma_f32_16x16x32_bf16 " d ", " a ", " b ", " c "\n\t"
#define FM(d, x, r, ctrl) "v_fmac_f32_dpp " d ", " x ", " r " " ctrl "\n\t"
#define ID_ "quad_perm:[0,1,2,3] row_mask:0xf bank_mask:"
#define HM_ "row_half_mirror row_mask:0xf bank_mask:"
#define R8_ "row_ror:8 row_mask:0xf bank_mask:"
#define MI_ "row_mirror row_mask:0xf bank_mask:"
        auto soft = [&]() {
          asm volatile(
            "s_waitcnt vmcnt(4)\n\t" MM("v[244:247]", "%6", "v[224:227]", "0") "v_min3_u32 v214, v224, v225, v226\n\t"
            "s_waitcnt vmcnt(3)\n\t" MM("v[248:251]", "%6", "v[228:231]", "0") "v_min3_u32 v214, v214, v227, v228\n\tv_min3_u32 v214, v214, v229, v230\n\t"
            "s_waitcnt vmcnt(2)\n\t" MM("v[248:251]", "%6", "v[232:235]", "v[248:251]") "v_min3_u32 v214, v214, v231, v232\n\t"
            "v_add_f32_dpp v212, v244, v244 " R8_ "0x3\n\tv_add_f32_dpp v213, v245, v245 " R8_ "0x3\n\t"
            "s_waitcnt vmcnt(1)\n\t" MM("v[248:251]", "%7", "v[236:239]", "v[248:251]")
            "v_add_f32_dpp v212, v246, v246 " R8_ "0xc\n\tv_add_f32_dpp v213, v247, v247 " R8_ "0xc\n\t"
            "s_waitcnt vmcnt(0) lgkmcnt(0)\n\t" MM("v[248:251]", "%7", "v[240:243]", "v[248:251]")
            "v_fma_f32 v212, v212, %14, %15\n\tv_fma_f32 v213, v213, %14, %15\n\tv_exp_f32 v216, v212\n\tv_exp_f32 v217, v213\n\t"
            "v_min3_u32 v214, v214, v233, v234\n\tv_cvt_pk_bf16_f32 v208, v216, v217\n\tv_min3_u32 v214, v214, v235, v236\n\tds_read_b96 v[188:190], %16 offset:%18\n\t"
            "v_exp_f32 v216, v248\n\tv_mov_b32_dpp v209, v208 " R8_ "0xf\n\tv_exp_f32 v217, v249\n\tv_exp_f32 v218, v250\n\t"
            MM("v[252:255]", "%8", "v[208:211]", "0") "v_exp_f32 v219, v251\n\tv_cvt_pk_bf16_f32 v232, v216, v217\n\t"
            MM("v[224:227]", "v[192:195]", "v[208:211]", "0") "v_cvt_pk_bf16_f32 v233, v218, v219\n\tv_min3_u32 v214, v214, v237, v238\n\tv_mov_b32_dpp v234, v232 " R8_ "0xf\n\t"
            MM("v[228:231]", "v[196:199]", "v[208:211]", "0") "v_mov_b32_dpp v235, v233 " R8_ "0xf\n\t"
            MM("v[244:247]", "v[204:207]", "v[208:211]", "0") "v_min3_u32 v214, v214, v239, v240\n\t"
            MM("v[252:255]", "%8", "v[232:235]", "v[252:255]") MM("v[224:227]", "%10", "v[232:235]", "v[224:227]") "v_min3_u32 v214, v214, v241, v242\n\t"
            MM("v[228:231]", "%11", "v[232:235]", "v[228:231]") "s_waitcnt lgkmcnt(0)\n\tv_sub_u32 v215, v190, %17 clamp\n\t"
            MM("v[236:239]", "v[200:203]", "v[208:211]", "0") "v_min3_u32 v214, v214, v243, v215\n\tv_cmp_eq_u32 %4, 0, v214\n\tv_rcp_f32 v216, v252\n\t"
            MM("v[240:243]", "v[184:187]", "v[208:211]", "0") "v_mul_f32 v216, v216, v188\n\tv_subrev_u32 v215, 0x2f800000, v252\n\t"
            MM("v[236:239]", "%12", "v[232:235]", "v[236:239]") FM("v220", "v224", "v216", ID_ "0x1") FM("v221", "v225", "v216", ID_ "0x1")
            "v_mov_b32_dpp v217, v216 " HM_ "0xf\n\t"
            MM("v[244:247]", "%13", "v[232:235]", "v[244:247]") FM("v220", "v230", "v216", ID_ "0x2") FM("v221", "v231", "v216", ID_ "0x2")
            MM("v[240:243]", "%9", "v[232:235]", "v[240:243]") FM("v220", "v228", "v217", HM_ "0x1") FM("v221", "v229", "v217", HM_ "0x1")
            "v_cmp_ge_u32 vcc, v215, v189\n\t" FM("v220", "v226", "v217", HM_ "0x2") FM("v221", "v227", "v217", HM_ "0x2")
            "s_or_b64 %5, %4, vcc"
            : "+{v[224:255]}"(S), "+{v[208:223]}"(P), "+{v[184:191]}"(V8), "+{v[192:207]}"(V), "=&s"(miss), "=&s"(bad)
            : "v"(slo), "v"(shi), "v"(ONES), "v"(vox[0]), "v"(vox[1]), "v"(vox[2]), "v"(vox[3]), "v"(vox[4]), "v"(k1), "v"(minf), "v"(lds0), "s"(s), "n"(L_MISC + 32)
            : "vcc", "scc", "memory");
        };
        soft();
        if (__builtin_expect(bad != 0ull, 0)) {
          if (miss) FC(1); else FC(2);
          while (miss != 0ull && !dead) {
            if (++spins > SPIN_CAP) dead = true;
            asm volatile("s_nop 7\n\ts_nop 7" ::: "memory");
            sc_load(); soft();
          }
          if (bad != 0ull) {
            u32x4 y[5];
#pragma unroll
            for (int k = 0; k < 4; ++k) y[k] = SC1_LOADS(l1off + (k & 1) * 2048 + (k >> 1) * 256, o1);
            y[4] = SC1_LOADS(l1offm, o1m);
            const f32x4 d0 = mfma32(as8(slo), as8(y[4]), Z4);
            f32x4 d1 = mfma32(as8(shi), as8(y[3]), mfma32(as8(shi), as8(y[2]), mfma32(as8(slo), as8(y[1]), mfma32(as8(slo), as8(y[0]), Z4))));
            const bool lo = c < 8;
            const float m0 = __builtin_fmaf((lo ? d0[0] : d0[2]) + dppf<DPP_ROR8>(lo ? d0[2] : d0[0]), k1, minf);
            const float m1 = __builtin_fmaf((lo ? d0[1] : d0[3]) + dppf<DPP_ROR8>(lo ? d0[3] : d0[1]), k1, minf);
            float mx = fmx(fmx(fmx(d1[0], d1[1]), d1[2]), fmx(fmx(d1[3], m0), m1));
            mx = fmx(mx, dppf<DPP_ROR8>(mx));
            mx = x32mx(x16mx(mx));
            d1 -= mx;
            const unsigned mm = pack2(__builtin_amdgcn_exp2f(m0 - mx), __builtin_amdgcn_exp2f(m1 - mx));
            const unsigned t01 = pack2(__builtin_amdgcn_exp2f(d1[0]), __builtin_amdgcn_exp2f(d1[1])), t23 = pack2(__builtin_amdgcn_exp2f(d1[2]), __builtin_amdgcn_exp2f(d1[3]));
            const bf16x8 pm = mk8(mm, dpp_ror8(mm), 0u, 0u), pt = mk8(t01, t23, dpp_ror8(t01), dpp_ror8(t23));
            const float r = __builtin_amdgcn_rcpf(mfma32(ONES, pt, mfma32(ONES, pm, Z4))[0]) * u2f(V8[4]), rm = dppf<0x141>(r);
            f32x4 o[5];
#pragma unroll
            for (int k = 0; k < 5; ++k) o[k] = mfma32(vox[k], pt, mfma32(k ? mk8(V[4 * k - 4], V[4 * k - 3], 0u, 0u) : mk8(V8[0], V8[1], 0u, 0u), pm, Z4));
#pragma unroll
            for (int i = 0; i < 2; ++i) {
              const float oa = dppf<0x141>(o[2][i]), ob = dppf<0x141>(o[1][2 + i]);
              P[12 + i] = f2u(__builtin_fmaf(hd ? ob : oa, rm, __builtin_fmaf(hd ? o[2][2 + i] : o[1][i], r, i ? 0.f : 32.0f)));
            }
            P[8] = f2u(r); P[9] = f2u(rm);
#pragma unroll
            for (int i = 0; i < 4; ++i) { S[12 + i] = f2u(o[3][i]); S[16 + i] = f2u(o[0][i]); S[20 + i] = f2u(o[4][i]); }
            asm volatile("s_nop 7\n\ts_nop 7" : "+{v[224:255]}"(S), "+{v[208:223]}"(P));
          }
        }
        STAMP(7); STAMPV(13, spins); LOGF(0, spins);
        {
          asm volatile("v_cvt_i32_f32 v218, v220\n\tv_cvt_i32_f32 v219, v221\n\tv_and_or_b32 v218, v218, %7, 2\n\tbuffer_atomic_add_x2 v[218:219], %3, %5, %6 offen\n\t"
                       FM("v222", "v236", "v216", R8_ "0x4") FM("v223", "v237", "v216", R8_ "0x4") "ds_read_b64 %2, %8 offset:%9\n\t"
                       FM("v222", "v238", "v217", MI_ "0x8") FM("v223", "v239", "v217", MI_ "0x8") "v_permlane32_swap_b32 v240, v242\n\t"
                       FM("v222", "v244", "v217", MI_ "0x4") FM("v223", "v245", "v217", MI_ "0x4") "v_permlane32_swap_b32 v241, v243\n\t"
                       FM("v222", "v246", "v216", R8_ "0x8") FM("v223", "v247", "v216", R8_ "0x8")
                       FM("v222", "v240", "v216", ID_ "0x1") FM("v223", "v241", "v216", ID_ "0x1") FM("v222", "v242", "v217", HM_ "0x1") FM("v223", "v243", "v217", HM_ "0x1")
                       "v_cvt_i32_f32 v212, v222\n\tv_cvt_i32_f32 v213, v223\n\tv_and_or_b32 v212, v212, %7, 2\n\tbuffer_atomic_add_x2 v[212:213], %4, %5, %6 offen\n\t"
                       "s_waitcnt lgkmcnt(0)"
                       : "+{v[224:255]}"(S), "+{v[208:223]}"(P), "=&v"(sw) : "v"(acco), "v"(acco2), "s"(rs), "s"(o2), "s"(~63u), "v"(lds0), "n"(L_MISC + 96) : "memory");
          __builtin_amdgcn_sched_barrier(0);
          tpub = now32();
          lds_inc<L_MISC + 104 + 4 * (w & 1)>(lds0);
          asm volatile("buffer_atomic_umax %0, %1, %2, %3 offen" :: "v"(tpub | 1u), "v"(tso), "s"(rs), "s"(o2) : "memory");
          STAMP(10);
        }
      }
    }
    STAMP(8);
    ARRIVE(1);
    o2p = o2;
    PIN_S(o2p);
    if (w < 2) {
      if (__builtin_expect(__builtin_amdgcn_readfirstlane(sw[1]) <= (unsigned)s, 0)) {
        lds_spin<L_MISC + 100, 0>(lds0, (unsigned)s + 1u);
        sw[0] = __hip_atomic_load((unsigned*)(misc + 24), __ATOMIC_RELAXED, __HIP_MEMORY_SCOPE_WORKGROUP);
      }
    } else {
      u32x4 q;
      asm volatile("1:\n\tds_read_b128 %0, %1 offset:%3\n\t.if %4\n\ts_sleep 1\n\t.endif\n\ts_waitcnt lgkmcnt(0)\n\tv_min3_u32 v201, v201, v202, v203\n\tv_cmp_ge_u32 vcc, %2, v201\n\ts_cbranch_vccnz 1b"
                   : "=&{v[200:203]}"(q) : "v"(lds0), "s"(s), "n"(L_MISC + 96), "n"(SP_SL) : "vcc", "memory");
      __builtin_amdgcn_s_setprio(3);
      tpub = now32();
      sw[0] = q[0];
    }
    STAMP(9);
    static_assert(F3C == 0 && F2C == 0 && F0C == 0, "nothing behind the atomics");
    LOGT(0, tpub); LOGT(1, tb2_);
    TSTAMP(1, tpub);
    ARRIVE_OUT();
    const unsigned an = __builtin_amdgcn_readfirstlane(sw[0]);
    const int ahead = (int)(an - tpub);
    tw = tpub + (unsigned)(an == 0u || ahead > 1000 ? SCH_FIRST : imax(ahead, w < 2 ? SCH_DMIN : 0));
    offs = tw - an;
  }
  STAMP_OUT();
  LOGP(4);
  __builtin_amdgcn_s_setprio(3);
  if (w < 2) { BAR(); tlast = cyc32(); }
  if (w == 2) tk.store(rs, o4);
  if (w == 3) {
    const unsigned oo = op_slot(NSEG - 2 - LAG4) + (unsigned)((m * 32 + gp) * 1280);
    const char* sp = lds + L_STG + 2048;
    const u32x4 v0 = *(const u32x4*)(sp + lane * 16), v1 = *(const u32x4*)(sp + 1280 + lane * 16), v2 = *(const u32x4*)(sp + (g & 1) * 1280 + 1024 + c * 16);
    SC1_STORE(v0, oo + lane * 16); SC1_STORE(v1, oo + 20480 + lane * 16); SC1_STORE(v2, lane < 32 ? oo + g * 20480 + 1024 + c * 16 : 0x7ffffff0u);
  }
  if (dead) tlast |= 1u; else tlast &= ~1u;
}

__device__ __forceinline__ void drain(const RecArgs& a, char* lds, const __amdgpu_buffer_rsrc_t rs, const int w, const unsigned tp1) {
  static_assert(LAG3 == 2 && LAG4 == 3 && LAG5 == 6, "drain schedule");
  const int lane = __builtin_amdgcn_workitem_id_x() & 63, b = __builtin_amdgcn_workgroup_id_x(), c = lane & 15;
  const int gp = (b & 7) * 2 + (b >> 7), m = (b >> 3) & 15, j = m & 7;
  unsigned* const misc = (unsigned*)(lds + L_MISC);
  const unsigned lds0 = (unsigned)(uintptr_t)(LAS char*)lds, ck = __builtin_amdgcn_readfirstlane(misc[11]);
  bool dead = tp1 & 1u;
  auto poll = [&](auto load, auto ready) {
    for (unsigned spins = 0;;) {
      asm volatile("" ::: "memory");
      load();
      if (ready() || dead) break;
      if (++spins > SPIN_CAP) dead = true;
      if (END_SL) __builtin_amdgcn_s_sleep(END_SL);
    }
  };
  LOGD(0);
#define TASK(ty, sg, tm) ((u64)((ty) | ((sg) - 56) << 2 | ((tm) / 100) << 5))
  u64 todo = w == 0 ? TASK(1, 63, END_E1) | TASK(2, 63, END_E2) << 12 | TASK(3, 62, END_E3) << 24
           : w == 1 ? TASK(1, 62, 0) | TASK(2, 62, END_E62) << 12 | TASK(3, 60, END_E3) << 24
           : w == 2 ? TASK(2, 60, 0) | TASK(3, 56, 0) << 12 : TASK(2, 61, END_E61) | TASK(3, 58, END_E59) << 12;
  TokTask tk;
  PvTask2 pv;
  OutTask ot;
  {
    const u32x4* p = (const u32x4*)(lds + L_DRN) + lane;
    const u32x4 q0 = p[0], q1 = p[64], q2 = p[128];
    tk.l0 = q0[0]; tk.l1 = q0[0] + 16384u; tk.lm = q0[1]; tk.s0 = q0[2]; tk.s1 = q0[3];
    pv.lo = q1[0]; pv.lm = q1[1]; pv.vr = q1[2]; pv.ma = q1[3]; pv.mz = q2[0]; pv.so = q2[1];
    pv.pa[0] = pv.pa[1] = pv.pb[0] = pv.pb[1] = (u32x4){0u, 0u, 0u, 0u};
    ot.t5f = q2[2]; ot.t5o = q2[3]; ot.sel = as8(p[192]);
  }
#pragma unroll 1
  for (int ti = 0; todo; todo >>= 12, ++ti) {
    const int ty = (int)todo & 3, sl = 56 + (((int)todo >> 2) & 7);
    const unsigned tm = tp1 + (((unsigned)todo >> 5) & 0x7fu) * 100u;
    if (ty == 1) {
      wait_cyc(tm);
      LOGD(3 * ti + 1);
      poll([&]() { tk.load(rs, ts_slot(ck, sl)); }, [&]() { return tk.ready(); });
      LOGD(3 * ti + 2);
      tk.run(j, lane, ot.sel); tk.store(rs, pt_slot(ck, sl));
    } else if (ty == 2) {
      const unsigned t0 = cyc32();
      if (sl >= NSEG - 2) { lds_wait(misc + 7, 4u * NSEG + 3u); wait_cyc(t0 + END_G2); }
      wait_cyc(tm);
      LOGD(3 * ti + 1);
      poll([&]() { pv.load(rs, pt_slot(ck, sl)); }, [&]() { return pv.ready(); });
      LOGD(3 * ti + 2);
      pv.run<true>(lds, sl % NV, lane, rs, op_slot(sl) + (unsigned)((m * 32 + gp) * 1280));
      if (sl == NSEG - 1) misc[14] = cyc32() | 1u;
    } else {
      if (sl >= NSEG - 4) { lds_wait(misc + 14, 1u); wait_cyc(__builtin_amdgcn_readfirstlane(misc[14]) + END_G3); }
      wait_cyc(tm);
      LOGD(3 * ti + 1);
      OutTask o2 = ot;
      poll([&]() { o2.load(a, rs, sl + 1, b); ot.load(a, rs, sl, b); }, [&]() { return ot.ready(b) & o2.ready(b); });
      LOGD(3 * ti + 2);
      o2.run(lane); o2.store(a, sl + 1, b, lane);
      ot.run(lane); ot.store(a, sl, b, lane);
    }
    LOGD(3 * ti + 3);
  }
  VM_DRAIN();
  if (w != 0) lds_inc<L_MISC + 64>(lds0);
  else {
    constexpr unsigned o2 = (unsigned)OFF_ACC + (unsigned)((NSEG - 1) % SR) * (unsigned)ACC_STEP;
    lds_wait(misc + 16, 3u);
    LOGD(10);
    if (lane == 0) __hip_atomic_fetch_add((gu64*)((GAS char*)(a.ws + o2) + acc_off(m * 4, 0)), (u64)1, RLX_AGENT);
    wait_until(now32() + SCH_DRAIN);
    wait_cyc(tp1 + END_E4);
    unsigned x;
    if (qkv_row(b) + 48 >= 55 && qkv_col(b) < 15) poll([&]() { x = __builtin_amdgcn_raw_buffer_load_b32(rs, acc_off(c * 4, 0), o2, 16); }, [&]() { return wave_all((x & 16u) != 0u); });
    lds_inc<L_MISC + 68>(lds0);
    LOGD(11);
  }
  if (qkv_row(b) + 48 >= 55 && qkv_col(b) < 15) {
    lds_wait(misc + 18, 8u);
    lds_spin<L_MISC + 68, 1>(lds0, 1u);
    x_fetch_n<10>(x_src(a, NSEG + 3, b), lds0, 3, 10 * w, lane * 16);
    VM_DRAIN();
    lds_inc<L_MISC + 68>(lds0);
  }
  LOGP(8);
}

__device__ __forceinline__ void proj_front(const RecArgs& a, char* lds, const __amdgpu_buffer_rsrc_t rs, const int v) {
  const int lane = __builtin_amdgcn_workitem_id_x() & 63, b = __builtin_amdgcn_workgroup_id_x(), c = lane & 15, g = lane >> 4;
  const int rb = (b & 7) + 8 * (b >> 5), cq = (b >> 3) & 3;
  const unsigned lds0 = (unsigned)(uintptr_t)(LAS char*)lds;
  unsigned* const cnt = (unsigned*)(lds + L_MISC) + 20;
  LOGF2(0);
  float* const red = (float*)(lds + L_RED1);
  float* const fin = red + 640;
  const f32x4 Z4 = {0.f, 0.f, 0.f, 0.f};
#pragma unroll
  for (int i = 0; i < 8; ++i) x_fetch((const u16*)(a.ws + OFF_OB) + (size_t)rb * 32 * DM, lds0, 0, 8 * v + i, lane);
  bf16x8 W[2][20];
#pragma unroll
  for (int k = 0; k < 40; ++k) W[k & 1][k >> 1] = *(const bf16x8*)(a.ws + OFF_WOBT + ((size_t)(cq * 10 + 2 * v + (k & 1)) * 20 + (k >> 1)) * 1024 + lane * 16);
  const unsigned fo = (unsigned)(rb * 40960 + (cq * 5 + v) * 1024 + ((g >> 1) * 16 + c) * 16), f8 = fo + (g & 1) * 8, f16 = fo + (g & 1) * 20480;
  u32x2 xo[2][2];
#pragma unroll
  for (int k = 0; k < 4; ++k)
    xo[k >> 1][k & 1] = __builtin_bit_cast(u32x2, __hip_atomic_load((const gu64*)((const char*)a.xnew + f8 + (k >> 1) * 512 + (k & 1) * 20480), RLX_AGENT));
  asm volatile("s_waitcnt vmcnt(44)" ::: "memory");
  lds_inc<L_MISC + 80>(lds0);
  lds_wait(cnt, 5u);
  LOGF2(1);
  f32x4 acc[2][2] = {{Z4, Z4}, {Z4, Z4}};
#pragma unroll
  for (int ks = 0; ks < 20; ++ks)
#pragma unroll
    for (int t = 0; t < 2; ++t) {
      const bf16x8 x = *(const bf16x8*)(lds + L_XN + (t * 20 + ks) * 1024 + lane * 16);
#pragma unroll
      for (int i = 0; i < 2; ++i) acc[i][t] = mfma32(W[i][ks], x, acc[i][t]);
    }
  u32x2 xp[2][2];
  PIN_V(acc[1][1]);
  LOGF2(2);
#pragma unroll
  for (int k = 0; k < 4; ++k) {
    const int i = k >> 1, t = k & 1;
#pragma unroll
    for (int q = 0; q < 2; ++q) {
      const unsigned r = pack2(acc[i][t][2 * q], acc[i][t][2 * q + 1]), x = xo[i][t][q];
      xp[i][t][q] = pack2(u2f(x << 16) + u2f(r << 16), u2f(x & 0xffff0000u) + u2f(r & 0xffff0000u));
      acc[i][t][2 * q] = u2f(xp[i][t][q] << 16); acc[i][t][2 * q + 1] = u2f(xp[i][t][q] & 0xffff0000u);
    }
    const float ml = x32sum(x16sum((acc[i][t][0] + acc[i][t][1]) + (acc[i][t][2] + acc[i][t][3]))) * (1.0f / 16.0f);
    float q = 0.f;
#pragma unroll
    for (int r = 0; r < 4; ++r) { const float d = acc[i][t][r] - ml; q += d * d; }
    q = x32sum(x16sum(q));
    if (g == 0) { red[(2 * v + i) * 32 + t * 16 + c] = ml; red[320 + (2 * v + i) * 32 + t * 16 + c] = q; }
  }
  lds_inc<L_MISC + 80>(lds0);
  if (v == 0) {
    lds_wait(cnt, 10u);
    LOGF2(3);
    float mw[10], ml = 0.f, m2 = 0.f;
#pragma unroll
    for (int i = 0; i < 10; ++i) { mw[i] = red[i * 32 + (lane & 31)]; ml += mw[i]; m2 += red[320 + i * 32 + (lane & 31)]; }
    ml *= 0.1f;
#pragma unroll
    for (int i = 0; i < 10; ++i) m2 += 16.0f * (mw[i] - ml) * (mw[i] - ml);
    const unsigned xs = (unsigned)OFF_LNX + rb * 1024;
    if (lane < 32) SC1_STORE8S(((u32x2){f2u(ml), nz(f2u(m2))}), (cq * 32 + lane) * 8, xs);
    wait_cyc(cyc32() + FR_D);
    u32x2 x0, x1;
    for (unsigned spins = 0; spins < SPIN_CAP; ++spins) {
      x0 = __builtin_amdgcn_raw_buffer_load_b64(rs, lane * 8, xs, 16); x1 = __builtin_amdgcn_raw_buffer_load_b64(rs, 512 + lane * 8, xs, 16);
      if (wave_all(x0[1] != 0u && x1[1] != 0u)) break;
      asm volatile("" ::: "memory");
    }
    const float m0 = u2f(x0[0]), m1 = u2f(x1[0]);
    const float mean = x32sum(m0 + m1) * 0.25f;
    const float M2 = x32sum((u2f(x0[1]) + u2f(x1[1])) + 160.0f * ((m0 - mean) * (m0 - mean) + (m1 - mean) * (m1 - mean)));
    const float mb = rbf(mean), den = rbf(__builtin_sqrtf(rbf(rbf(M2 * (1.0f / DM)) + 1e-5f)));
    if (lane < 32) {
      fin[lane] = mb; fin[32 + lane] = 1.0f / den;
      if (cq == 0) __hip_atomic_store((GAS unsigned*)(a.ws + OFF_XNRM) + (rb * 32 + lane) * 4, f2u(1.02f * (M2 + DM * (mean - mb) * (mean - mb)) / (den * den)), RLX_AGENT);
    }
    lds_inc<L_MISC + 84>(lds0);
  }
  lds_wait(cnt + 1, 1u);
  LOGF2(4);
  const __amdgpu_buffer_rsrc_t rx = __builtin_amdgcn_make_buffer_rsrc(a.ws + OFF_XN, 0, (int)(OFF_QB - OFF_XN), 0x00020000);
  auto put = [&](const u32x2 p0, const u32x2 p1, unsigned so) {
    const auto r0 = __builtin_amdgcn_permlane16_swap(p0[0], p1[0], false, false), r1 = __builtin_amdgcn_permlane16_swap(p0[1], p1[1], false, false);
    __builtin_amdgcn_raw_buffer_store_b128((u32x4){r0[0], r1[0], r0[1], r1[1]}, rx, f16, so, 16);
  };
#pragma unroll
  for (int i = 0; i < 2; ++i) {
    u32x2 y[2];
#pragma unroll
    for (int t = 0; t < 2; ++t) {
      const float mb = fin[t * 16 + c], rden = fin[32 + t * 16 + c];
      y[t] = (u32x2){pack2(rbf(acc[i][t][0] - mb) * rden, rbf(acc[i][t][1] - mb) * rden), pack2(rbf(acc[i][t][2] - mb) * rden, rbf(acc[i][t][3] - mb) * rden)};
    }
    put(y[0], y[1], i * 512);
  }
  VM_DRAIN();
  lds_inc<L_MISC + 80>(lds0);
#pragma unroll
  for (int i = 0; i < 2; ++i) put(xp[i][0], xp[i][1], (unsigned)(OFF_XB - OFF_XN) + i * 512);
  if (v == 0) {
    lds_wait(cnt, 15u);
    if (rb < 4 && lane < 8) __hip_atomic_fetch_add((GAS unsigned*)(a.ws + OFF_RDY) + lane * 64, 1u, RLX_AGENT);
  }
  LOGF2(5);
}

__global__ __launch_bounds__(512, 2) void rec_kernel(RecArgs a) {
  __shared__ __attribute__((aligned(16))) char lds[L_BYTES];
  const int tid = __builtin_amdgcn_workitem_id_x(), wid = __builtin_amdgcn_readfirstlane(tid >> 6), w = wid & 3;
  const __amdgpu_buffer_rsrc_t rs = __builtin_amdgcn_make_buffer_rsrc(a.ws, 0, (int)COMM_BYTES, 0x00020000);
  if (tid < 32) ((float*)(lds + L_MISC))[tid] = (tid == 4 || tid == 5) ? 1.0f : 0.0f;
  BAR();
  if (a.layer && wid < 5) proj_front(a, lds, rs, wid);
  if (wid < 4) {
    unsigned tlast;
    if (w == 0) rec_waves<0>(a, lds, rs, tlast);
    else if (w == 1) rec_waves<1>(a, lds, rs, tlast);
    else if (w == 2) rec_waves<2>(a, lds, rs, tlast);
    else rec_waves<3>(a, lds, rs, tlast);
    drain(a, lds, rs, w, tlast);
  } else {
    unsigned vo[2][2];
    if (w == 0) token_waves<0>(a, lds, rs, vo);
    else if (w == 1) token_waves<1>(a, lds, rs, vo);
    else if (w == 2) token_waves<2>(a, lds, rs, vo);
    else token_waves<3>(a, lds, rs, vo);
    qkv_tail(a, lds, w, vo);
  }
}

#if defined(__HIP_DEVICE_COMPILE__)
#pragma clang attribute pop
#endif

constexpr int KQ = S_ / 4;
#define ATT_P1 6
#define ATT_P2 10
#define ATT_P3 14
#define ATT_THR 6.0f
#define ATT_NQ 4
#define ATT_LMAX 1.0e12f
#if defined(__HIP_DEVICE_COMPILE__)
#pragma clang attribute push(__attribute__((target("no-packed-fp32-ops"))), apply_to = function)
#endif
__global__ __launch_bounds__(256 * ATT_NQ, 4) void attb_kernel(const u16* __restrict__ Qp, const u16* __restrict__ KV, u16* __restrict__ O) {
  __shared__ f32x4 mo_[ATT_NQ][4][2][3][64];
  __shared__ float mm_[ATT_NQ][4][2][64];
  const int tid = __builtin_amdgcn_workitem_id_x(), bid = __builtin_amdgcn_workgroup_id_x();
  __shared__ unsigned sink[64];
  own_code<5000>(tid, sink);
  const int lane = tid & 63, wid = __builtin_amdgcn_readfirstlane(tid >> 6), kh = wid / ATT_NQ, r = lane & 31, hh = lane >> 5;
  const int hd = (bid & 7) * 4 + ((bid >> 3) & 3), qb = (bid >> 5) * ATT_NQ + wid % ATT_NQ;
  const int q0 = qb * 64;
  bf16x8 qa[2]; u32x2 qb2[2];
#pragma unroll
  for (int qt = 0; qt < 2; ++qt) {
    { const u32x2* qp = (const u32x2*)(Qp + ((size_t)hd * S_ + q0 + qt * 32 + r) * QST + 8 * hh); const u32x2 q0_ = qp[0], q1_ = qp[1]; qa[qt] = mk8(q0_[0], q0_[1], q1_[0], q1_[1]); }
    const u32x2 t = *(const u32x2*)(Qp + ((size_t)hd * S_ + q0 + qt * 32 + r) * QST + 16);
    qb2[qt] = t;
  }
  const __amdgpu_buffer_rsrc_t rk = __builtin_amdgcn_make_buffer_rsrc((void*)(KV + ((size_t)hd * 64 + kh * 16) * (KVT / 2)), 0, 16 * KVT, 0x00020000);
  unsigned ok1 = lane * 16, ok2 = hh ? 1600 : 1024 + r * 8, ov = r > 20 ? 1u << 30 : 1280 + (hh * 21 + r) * 16;
  f32x16 o[2];
  float m[2];
  const f32x16 Z16 = {0.f, 0.f, 0.f, 0.f, 0.f, 0.f, 0.f, 0.f, 0.f, 0.f, 0.f, 0.f, 0.f, 0.f, 0.f, 0.f};
  int odd = 0xDDDD;
  asm volatile("" : "+v"(odd));
  struct KvT { u32x4 k1, v0, v1; u32x2 k2; };
  auto fetch_k = [&](KvT& x, int t) {
    x.k1 = __builtin_amdgcn_raw_buffer_load_b128(rk, ok1, imin(t, KQ / 32 - 1) * KVT, 0); x.k2 = __builtin_amdgcn_raw_buffer_load_b64(rk, ok2, imin(t, KQ / 32 - 1) * KVT, 0);
    __builtin_amdgcn_sched_barrier(0);
  };
  auto fetch_v = [&](KvT& x, int t) {
    x.v0 = __builtin_amdgcn_raw_buffer_load_b128(rk, ov, imin(t, KQ / 32 - 1) * KVT, 0); x.v1 = __builtin_amdgcn_raw_buffer_load_b128(rk, ov + 672, imin(t, KQ / 32 - 1) * KVT, 0);
    __builtin_amdgcn_sched_barrier(0);
  };
  auto fetch = [&](int t) { KvT x; __builtin_amdgcn_sched_barrier(0); fetch_k(x, t); fetch_v(x, t); return x; };
  auto qk1 = [&](const KvT& x, int qt) { return __builtin_amdgcn_mfma_f32_32x32x16_bf16(as8(x.k1), qa[qt], Z16, 0, 0, 0); };
  auto qk2 = [&](const KvT& x, int qt, f32x16 c) { return __builtin_amdgcn_mfma_f32_32x32x8bf16_1k(__builtin_bit_cast(v4i16_t, x.k2), __builtin_bit_cast(v4i16_t, qb2[qt]), c, 0, 0, 0); };
  auto pv = [&](const u32x4 v, const f32x16 p, int hi, f32x16 c) {
    const f32x8 ph = hi ? __builtin_shufflevector(p, p, 8, 9, 10, 11, 12, 13, 14, 15) : __builtin_shufflevector(p, p, 0, 1, 2, 3, 4, 5, 6, 7);
    return __builtin_amdgcn_smfmac_f32_32x32x32_bf16(__builtin_bit_cast(hbf16x8, v), __builtin_bit_cast(hbf16x16, ph), c, odd, 0, 0);
  };
  auto tile = [&](const KvT& x, auto MODE) {
    f32x16 sa[2];
#pragma unroll
    for (int qt = 0; qt < 2; ++qt) sa[qt] = qk1(x, qt);
#pragma unroll
    for (int qt = 0; qt < 2; ++qt) sa[qt] = qk2(x, qt, sa[qt]);
#pragma unroll
    for (int qt = 0; qt < 2; ++qt) {
      if (MODE != 0) {
        float mx = fmx(fmx(sa[qt][0], sa[qt][1]), sa[qt][2]);
#pragma unroll
        for (int i = 3; i < 15; i += 2) mx = fmx(fmx(mx, sa[qt][i]), sa[qt][i + 1]);
        mx = fmx(mx, sa[qt][15]);
        if (MODE == 1 || !wave_all(mx <= ATT_THR)) {
          mx = x32mx(mx);
          if (MODE != 1) mx = fmx(mx, 0.f);
          const float mn = m[qt] + mx;
          const unsigned h1 = f2bf(mn); const float r1 = mn - bf2f(h1);
          const unsigned h2 = f2bf(r1); const float r2 = r1 - bf2f(h2);
          const unsigned h3 = f2bf(r2);
          const float me = (bf2f(h1) + bf2f(h2)) + bf2f(h3);
          const float dm = me - m[qt];
          m[qt] = me;
          if (hh) qb2[qt] = (u32x2){(h1 | (h2 << 16)) ^ 0x80008000u, h3 ^ 0x8000u};
          const float alpha = __builtin_amdgcn_exp2f(-dm);
#pragma unroll
          for (int i = 0; i < 16; ++i) { if (MODE != 1) o[qt][i] *= alpha; sa[qt][i] -= dm; }
        }
      }
#pragma unroll
      for (int i = 0; i < 16; ++i) sa[qt][i] = __builtin_amdgcn_exp2f(sa[qt][i]);
      asm volatile("" : "+v"(sa[qt]));
      if (MODE == 1) { o[qt] = Z16; asm volatile("" : "+v"(o[qt])); }
      o[qt] = pv(x.v1, sa[qt], 1, pv(x.v0, sa[qt], 0, o[qt]));
      __builtin_amdgcn_sched_barrier(0);
    }
  };
  typedef std::integral_constant<int, 1> I1; typedef std::integral_constant<int, 2> I2;
  auto reset = [&]() {
    asm volatile("" : "+v"(ok1), "+v"(ok2), "+v"(ov) :: "memory");
    m[0] = m[1] = 0.f;
    if (hh) qb2[0] = qb2[1] = (u32x2){0u, 0u};
  };
  auto slow = [&]() {
    reset();
    tile(fetch(0), I1{});
#pragma unroll 1
    for (int t = 1; t < KQ / 32; ++t) tile(fetch(t), I2{});
  };
  f32x16 sc[2];
  auto phase = [&](int qt, const u32x4 vp, const KvT& xk, const u32x4 v0, auto mid) {
    auto ex = [&](int i0, int i1) {
#pragma unroll
      for (int i = i0; i < i1; ++i) sc[qt][i] = __builtin_amdgcn_exp2f(sc[qt][i]);
      asm volatile("" : "+v"(sc[qt]));
      __builtin_amdgcn_sched_barrier(0);
    };
    o[1 - qt] = pv(vp, sc[1 - qt], 1, o[1 - qt]);
    __builtin_amdgcn_sched_barrier(0);
    ex(0, 3);
    sc[1 - qt] = qk1(xk, 1 - qt);
    __builtin_amdgcn_sched_barrier(0);
    ex(3, 6);
    sc[1 - qt] = qk2(xk, 1 - qt, sc[1 - qt]);
    __builtin_amdgcn_sched_barrier(0);
    ex(6, 10);
    o[qt] = pv(v0, sc[qt], 0, o[qt]);
    __builtin_amdgcn_sched_barrier(0);
    mid();
    ex(10, 16);
  };
  auto fast = [&]() {
    reset();
    KvT xa = fetch(0), xb = fetch(1);
    tile(xa, I1{});
    fetch_k(xa, 2);
    sc[0] = qk2(xb, 0, qk1(xb, 0)); sc[1] = Z16;
    asm volatile("" : "+v"(sc[1]));
    auto step = [&](KvT& x, KvT& y, int t) {
      phase(0, y.v1, x, x.v0, [&]() { fetch_v(y, t + 1); fetch_k(x, t + 2); });
      phase(1, x.v1, y, x.v0, []() {});
    };
    step(xb, xa, 1);
#pragma unroll 1
    for (int t = 2; t < KQ / 32; t += 2) {
      if (t == ATT_P1) __builtin_amdgcn_s_setprio(2);
      if (t == ATT_P2) __builtin_amdgcn_s_setprio(1);
      if (t == ATT_P3) __builtin_amdgcn_s_setprio(0);
      step(xa, xb, t);
      step(xb, xa, t + 1);
    }
    o[1] = pv(xb.v1, sc[1], 1, o[1]);
  };
  if (ATT_P1) __builtin_amdgcn_s_setprio(3);
  fast();
  {
    float big = 0.f;
#pragma unroll
    for (int qt = 0; qt < 2; ++qt) {
      big = fmx(big, hh ? o[qt][8] * (3.0e38f / ATT_LMAX) : __builtin_fabsf(o[qt][8]));
#pragma unroll
      for (int i = 0; i < 12; ++i) if (i != 8) big = fmx(big, (i > 8 && hh) ? 0.f : __builtin_fabsf(o[qt][i]));
    }
    if (__builtin_expect(!wave_all(big <= 3.0e38f), 0)) slow();
  }
  f32x4 (*mo)[2][3][64] = mo_[wid % ATT_NQ];
  float (*mm)[2][64] = mm_[wid % ATT_NQ];
#pragma unroll
  for (int qt = 0; qt < 2; ++qt) {
#pragma unroll
    for (int k = 0; k < 3; ++k) mo[kh][qt][k][lane] = (f32x4){o[qt][4 * k], o[qt][4 * k + 1], o[qt][4 * k + 2], o[qt][4 * k + 3]};
    mm[kh][qt][lane] = m[qt];
  }
  const int qt = kh & 1, grp = kh >> 1, row = q0 + qt * 32 + r, k0 = hd * BD + 4 * hh + 8 * grp;
  auto oaddr = [&](int k) { return O + (((size_t)(row >> 4) * 20 + (k >> 5)) * 64 + ((k & 31) >> 3) * 16 + (row & 15)) * 8 + (k & 7); };
  u16 *op0 = oaddr(k0), *op1 = oaddr(k0 + 16);
  asm volatile("" : "+v"(op0), "+v"(op1));
  asm volatile("s_waitcnt lgkmcnt(0)\n\ts_barrier" ::: "memory");
  {
    float a[4], l[4];
    f32x4 u[4], x[4];
#pragma unroll
    for (int i = 0; i < 4; ++i) { a[i] = mm[i][qt][lane]; l[i] = mo[i][qt][2][lane | 32][0]; u[i] = mo[i][qt][grp][lane]; x[i] = mo[i][qt][2][lane]; }
    const float mn = fmx(fmx(a[0], a[1]), fmx(a[2], a[3]));
#pragma unroll
    for (int i = 0; i < 4; ++i) a[i] = __builtin_amdgcn_exp2f(a[i] - mn);
    const float inv = __builtin_amdgcn_rcpf((l[0] * a[0] + l[1] * a[1]) + (l[2] * a[2] + l[3] * a[3]));
    const f32x4 v = ((u[0] * a[0] + u[1] * a[1]) + (u[2] * a[2] + u[3] * a[3])) * inv;
    *(GAS u32x2*)op0 = (u32x2){pack2(v[0], v[1]), pack2(v[2], v[3])};
    if (grp == 0) {
      const f32x4 y = ((x[0] * a[0] + x[1] * a[1]) + (x[2] * a[2] + x[3] * a[3])) * inv;
      if (hh == 0) *(GAS u32x2*)op1 = (u32x2){pack2(y[0], y[1]), pack2(y[2], y[3])};
    }
  }
}
#if defined(__HIP_DEVICE_COMPILE__)
#pragma clang attribute pop
#endif

__global__ __launch_bounds__(640, 3) void proj_kernel(const u16* __restrict__ Of, const u16* __restrict__ Wf, const u16* __restrict__ xold,
                                                      u16* __restrict__ xnew) {
  __shared__ __attribute__((aligned(16))) char ob[40960];
  const int tid = threadIdx.x, lane = tid & 63, w = __builtin_amdgcn_readfirstlane(tid >> 6), g = lane >> 4, c = lane & 15;
  __shared__ unsigned sink[64];
  own_code<1500>(tid, sink);
  const int rb = (blockIdx.x & 7) + 8 * (blockIdx.x >> 5), cb = (blockIdx.x >> 3) & 3;
  const int col = cb * 160 + w * 16 + 4 * g;
  const u16* wp = Wf + (size_t)(cb * 10 + w) * 20 * 512 + lane * 8;
  u32x4 st[4];
#pragma unroll
  for (int i = 0; i < 4; ++i) st[i] = *(const u32x4*)(Of + (size_t)rb * 40 * 512 + (i * 640 + tid) * 8);
  bf16x8 wf[20];
#pragma unroll
  for (int ks = 0; ks < 20; ++ks) wf[ks] = *(const bf16x8*)(wp + (size_t)ks * 512);
  u32x2 xo[2];
#pragma unroll
  for (int t = 0; t < 2; ++t) xo[t] = *(const u32x2*)(xold + (((size_t)(rb * 2 + t) * 20 + (col >> 5)) * 64 + ((col & 31) >> 3) * 16 + c) * 8 + (col & 7));
#pragma unroll
  for (int i = 0; i < 4; ++i) *(u32x4*)(ob + (i * 640 + tid) * 16) = st[i];
  BAR();
  f32x4 acc[2] = {{0.f, 0.f, 0.f, 0.f}, {0.f, 0.f, 0.f, 0.f}};
#pragma unroll
  for (int ks = 0; ks < 20; ++ks)
#pragma unroll
    for (int t = 0; t < 2; ++t) acc[t] = mfma32(wf[ks], *(const bf16x8*)(ob + (t * 20 + ks) * 1024 + lane * 16), acc[t]);
#pragma unroll
  for (int t = 0; t < 2; ++t) {
    const unsigned r01 = pack2(acc[t][0], acc[t][1]), r23 = pack2(acc[t][2], acc[t][3]);
    const unsigned p01 = pack2(u2f(xo[t][0] << 16) + u2f(r01 << 16), u2f(xo[t][0] & 0xffff0000u) + u2f(r01 & 0xffff0000u));
    const unsigned p23 = pack2(u2f(xo[t][1] << 16) + u2f(r23 << 16), u2f(xo[t][1] & 0xffff0000u) + u2f(r23 & 0xffff0000u));
    *(u32x2*)(xnew + (size_t)(rb * 32 + t * 16 + c) * DM + col) = (u32x2){p01, p23};
  }
}

extern "C" void kernel_launch(void* const* d_in, const int* in_sizes, int n_in, void* d_out, int out_size, void* d_ws,
                              size_t ws_size, hipStream_t stream) {
  const u16* A_x = (const u16*)d_in[0];
  const u16* A_mem0 = (const u16*)d_in[1];
  const u16* A_Wq = (const u16*)d_in[2];
  const u16* A_Wk = (const u16*)d_in[3];
  const u16* A_Wv = (const u16*)d_in[4];
  const u16* A_Wo = (const u16*)d_in[5];
  const u16* B_wqkv = (const u16*)d_in[8];
  const u16* B_wo = (const u16*)d_in[9];
  char* ws = (char*)(((uintptr_t)d_ws + ((size_t)WS_OFF << 20) + WS_AL - 1) & ~(uintptr_t)(WS_AL - 1));
  u16* xa = (u16*)(ws + OFF_XA); u16* Ob = (u16*)(ws + OFF_OB);

  prologue_kernel<<<256, 640, 0, stream>>>(ProArgs{A_x, A_Wq, A_Wk, A_Wv, A_Wo, B_wqkv, B_wo, ws});
  for (int layer = 0; layer < 2; ++layer) {
    rec_kernel<<<256, 512, 0, stream>>>(RecArgs{A_mem0, A_Wq, A_Wk, (const u16*)(ws + OFF_XB), xa, ws, (unsigned)layer, 0u});
    attb_kernel<<<BH * 32 / ATT_NQ, 256 * ATT_NQ, 0, stream>>>((const u16*)(ws + OFF_QB), (const u16*)(ws + OFF_KV), Ob);
  }
  proj_kernel<<<256, 640, 0, stream>>>(Ob, (const u16*)(ws + OFF_WOBT), xa, (u16*)d_out);
}
```

```cpp
#include <hip/hip_runtime.h>
__device__ unsigned char g_fc[2 * 4 * 64];
#define FC(k) do { if (lane == 0) g_fc[(a.layer * 4 + (k)) * 64 + s] = 1; } while (0)
__global__ void spin_kernel(int L, int k, int lo, int hi) {
  unsigned t0 = (unsigned)__builtin_amdgcn_s_memrealtime(), n = 0;
  for (int s = lo; s <= hi; ++s) n += g_fc[(L * 4 + k) * 64 + s];
  for (int i = 0; i < 512; ++i) g_fc[i] = 0;
  while ((unsigned)__builtin_amdgcn_s_memrealtime() - t0 < 1000u + n * 2000u) { }
}
#include <hip/hip_runtime.h>
#include <hip/hip_bf16.h>
#include <stdint.h>
#include <type_traits>

typedef unsigned short u16;
typedef unsigned long long u64;
typedef __attribute__((ext_vector_type(8))) short bf16x8;
typedef __attribute__((ext_vector_type(16))) short bf16x16;
typedef __attribute__((ext_vector_type(4))) float f32x4;
typedef __attribute__((ext_vector_type(2))) float f32x2;
typedef __attribute__((ext_vector_type(16))) float f32x16;
typedef __attribute__((ext_vector_type(8))) float f32x8;
typedef __attribute__((ext_vector_type(4))) unsigned u32x4;
typedef __attribute__((ext_vector_type(2))) unsigned u32x2;
typedef __attribute__((ext_vector_type(3))) unsigned u32x3;
typedef __attribute__((ext_vector_type(8))) unsigned u32x8;
typedef __attribute__((ext_vector_type(16))) unsigned u32x16;
typedef __attribute__((ext_vector_type(32))) unsigned u32x32;
#define GAS __attribute__((address_space(1)))
#define LAS __attribute__((address_space(3)))
typedef GAS unsigned long long gu64;
#define BAR() do { __builtin_amdgcn_s_waitcnt(0xC07F); asm volatile("s_waitcnt lgkmcnt(0)\n\ts_barrier" ::: "memory"); } while (0)

constexpr int S_ = 2048, DM = 640;
constexpr int NH = 32, DH = 192, HD = NH * DH;
constexpr int MEM = 8, SEG = 32, NSEG = S_ / SEG;
constexpr int BH = 32, BD = 20;

constexpr size_t al256(size_t x) { return (x + 255) & ~(size_t)255; }
constexpr int LAG3 = 2, LAG4 = 3, LAG5 = 6;
#define SR_ 8
constexpr int SR = SR_;
constexpr unsigned SCT_STEP = 131072, OFF_SCM = SR * SCT_STEP, SCM_STEP = 32768;
constexpr int ACC_NB = 192;
constexpr size_t ACC_STEP = 65536;
constexpr unsigned ACC_TS = ACC_NB * 256;
constexpr size_t ACC_LAYER = SR * ACC_STEP;
constexpr size_t OFF_ACC = 3 * 524288;
static_assert(SR == 8, "regions");
__device__ __forceinline__ unsigned ts_piece(int i) { return ACC_TS + (unsigned)(i < 128 ? (i >> 1) * 256 + (i & 1) * 16 : 128 + (i & 1) * 8192); }
constexpr size_t OFF_LNX = OFF_SCM + SR * SCM_STEP;
constexpr size_t OFF_RDY = OFF_LNX + 65536;
constexpr size_t PG = 2097152;
#define RING_ 4
constexpr int RING = RING_;
constexpr size_t TS_SLOT = (size_t)NH * 8 * 2560, PT_SLOT = (size_t)NH * 2560, OP_SLOT = (size_t)256 * 2560;
__device__ __forceinline__ unsigned ts_tok(int h, int j) { return (unsigned)((h & 15) * 32768 + (j >> 2) * 16384 + (j & 3) * 2048); }
__device__ __forceinline__ unsigned ts_mem(int h, int j) { return (unsigned)(524288 + ((h & 15) >> 1) * 16384 + (h & 1) * 4096 + j * 512); }
__device__ __forceinline__ unsigned pt_tok(int h) { return (unsigned)(((h & 15) >> 2) * 16384 + (h & 3) * 2048); }
__device__ __forceinline__ unsigned pt_mem(int h) { return (unsigned)(65536 + (h & 15) * 512); }
constexpr size_t OFF_X = 2 * PG + TS_SLOT + 3 * PT_SLOT, X_BYTES = 7 * OP_SLOT + 4 * PT_SLOT;
constexpr size_t OFF_OP = OFF_X + TS_SLOT + 4 * PT_SLOT;
constexpr size_t COMM_BYTES = OFF_X + X_BYTES + RING * OP_SLOT;
__device__ __forceinline__ unsigned near_half(unsigned ck, unsigned o) { return o + ((ck >> (o >= 2 * PG ? 8 : 4)) & 8192u); }
__device__ __forceinline__ unsigned ts_slot(unsigned ck, int sl) {
  return near_half(ck, sl == NSEG - 1 ? (unsigned)OFF_X : ((sl & 3) == 3 ? (unsigned)(2 * PG) : (unsigned)PG + (unsigned)(sl & 3) * (unsigned)TS_SLOT));
}
__device__ __forceinline__ unsigned pt_slot(unsigned ck, int sl) {
  return near_half(ck, sl >= NSEG - 4 ? (unsigned)(OFF_X + TS_SLOT) + (unsigned)(sl - (NSEG - 4)) * (unsigned)PT_SLOT
                                      : ((sl & 3) == 0 ? (unsigned)(PG + 3 * TS_SLOT) : (unsigned)(2 * PG + TS_SLOT - PT_SLOT) + (unsigned)(sl & 3) * (unsigned)PT_SLOT));
}
__device__ __forceinline__ unsigned op_slot(int sl) { return (unsigned)OFF_OP + (unsigned)(sl >= NSEG - 6 ? sl - (NSEG - 6) : 6 + sl % RING) * (unsigned)OP_SLOT; }
static_assert(RING == 4 && OFF_OP <= 3 * PG && OFF_RDY + 2048 <= OFF_ACC && OFF_ACC + ACC_LAYER == PG && TS_SLOT % 16384 == 0 && PT_SLOT % 16384 == 0, "pages");
static_assert(TS_SLOT == OP_SLOT && RING >= 4 && NSEG % SR == 0 && SR >= 5 && X_BYTES % 4096 == 0 && X_BYTES <= 256 * 20 * 1024, "slots");
constexpr size_t OFF_WB = al256(COMM_BYTES);
constexpr size_t OFF_BNRM = OFF_WB + (size_t)256 * 100 * 1024;
constexpr size_t OFF_LAT = OFF_BNRM + 1024;
constexpr size_t OFF_XNRM = OFF_LAT + 2048;
constexpr size_t OFF_WQKVBT = OFF_XNRM + al256(S_ * 16);
constexpr size_t OFF_WOBT = OFF_WQKVBT + al256((size_t)3 * DM * DM * 2);
constexpr size_t OFF_XN = OFF_WOBT + al256((size_t)DM * DM * 2);
constexpr int QST = BD;
constexpr size_t OFF_XA = OFF_XN + al256((size_t)S_ * DM * 2);
constexpr size_t OFF_XB = OFF_XA + al256((size_t)S_ * DM * 2);
constexpr size_t OFF_QB = OFF_XB + al256((size_t)S_ * DM * 2);
constexpr int KVT = 2624;
constexpr size_t OFF_KV = OFF_QB + al256((size_t)BH * S_ * QST * 2);
constexpr size_t OFF_OB = OFF_KV + al256((size_t)BH * 64 * KVT);
constexpr size_t OFF_DBG = OFF_OB + al256((size_t)S_ * DM * 2);
constexpr size_t WS_TOTAL = OFF_DBG + (size_t)2 * 64 * 8 * DM * 2;
#define WS_AL 2097152
#define WS_OFF 0
static_assert(WS_TOTAL + WS_AL + ((size_t)WS_OFF << 20) <= ((size_t)256 << 20), "workspace budget");

#if defined(__HIP_DEVICE_COMPILE__)
#pragma clang attribute push(__attribute__((target("no-packed-fp32-ops"))), apply_to = function)
#endif
__device__ __forceinline__ float u2f(unsigned v) { return __builtin_bit_cast(float, v); }
__device__ __forceinline__ unsigned f2u(float v) { return __builtin_bit_cast(unsigned, v); }
__device__ __forceinline__ float bf2f(unsigned v) { return __builtin_bit_cast(float, v << 16); }
__device__ __forceinline__ unsigned f2bf(float f) { __bf16 b = (__bf16)f; return (unsigned)__builtin_bit_cast(u16, b); }
__device__ __forceinline__ float rbf(float f) { return bf2f(f2bf(f)); }
typedef __attribute__((ext_vector_type(2))) __bf16 hbf16x2;
__device__ __forceinline__ unsigned pack2(float lo, float hi) { f32x2 v = {lo, hi}; hbf16x2 b = __builtin_convertvector(v, hbf16x2); return __builtin_bit_cast(unsigned, b); }
__device__ __forceinline__ f32x4 mfma32(bf16x8 a, bf16x8 b, f32x4 c) { return __builtin_amdgcn_mfma_f32_16x16x32_bf16(a, b, c, 0, 0, 0); }
typedef __attribute__((ext_vector_type(8))) __bf16 hbf16x8;
typedef __attribute__((ext_vector_type(16))) __bf16 hbf16x16;
__device__ __forceinline__ f32x4 smfma64(bf16x8 a, bf16x16 b, f32x4 c, int idx) {
  return __builtin_amdgcn_smfmac_f32_16x16x64_bf16(__builtin_bit_cast(hbf16x8, a), __builtin_bit_cast(hbf16x16, b), c, idx, 0, 0);
}
__device__ __forceinline__ bf16x16 cat16(bf16x8 a, bf16x8 b) { return __builtin_shufflevector(a, b, 0, 1, 2, 3, 4, 5, 6, 7, 8, 9, 10, 11, 12, 13, 14, 15); }
__device__ __forceinline__ bf16x8 half8(bf16x16 v, int hi) { return hi ? __builtin_shufflevector(v, v, 8, 9, 10, 11, 12, 13, 14, 15) : __builtin_shufflevector(v, v, 0, 1, 2, 3, 4, 5, 6, 7); }
__device__ __forceinline__ bf16x8 mk8(unsigned a, unsigned b, unsigned c, unsigned d) { u32x4 v = {a, b, c, d}; return __builtin_bit_cast(bf16x8, v); }
__device__ __forceinline__ unsigned dpp_ror8(unsigned v) { return (unsigned)__builtin_amdgcn_mov_dpp((int)v, 0x128, 0xf, 0xf, true); }
__device__ __forceinline__ float fmx(float a, float b) { return __builtin_elementwise_maximum(a, b); }

__device__ __forceinline__ int imax(int a, int b) { return a > b ? a : b; }
__device__ __forceinline__ int imin(int a, int b) { return a < b ? a : b; }
__device__ __forceinline__ unsigned umin(unsigned a, unsigned b) { return a < b ? a : b; }
__device__ __forceinline__ unsigned umax(unsigned a, unsigned b) { return a > b ? a : b; }
__device__ __forceinline__ bool wave_all(bool p) { return __builtin_amdgcn_ballot_w64(!p) == 0ull; }
__device__ __forceinline__ float x16mx(float x) { unsigned u = __builtin_bit_cast(unsigned, x); auto r = __builtin_amdgcn_permlane16_swap(u, u, false, false); return fmx(u2f(r[0]), u2f(r[1])); }
__device__ __forceinline__ float x32mx(float x) { unsigned u = __builtin_bit_cast(unsigned, x); auto r = __builtin_amdgcn_permlane32_swap(u, u, false, false); return fmx(u2f(r[0]), u2f(r[1])); }
__device__ __forceinline__ float x16sum(float x) { unsigned u = __builtin_bit_cast(unsigned, x); auto r = __builtin_amdgcn_permlane16_swap(u, u, false, false); return u2f(r[0]) + u2f(r[1]); }
__device__ __forceinline__ float x32sum(float x) { unsigned u = __builtin_bit_cast(unsigned, x); auto r = __builtin_amdgcn_permlane32_swap(u, u, false, false); return u2f(r[0]) + u2f(r[1]); }
template <int CTRL> __device__ __forceinline__ float dppf(float v) { return u2f((unsigned)__builtin_amdgcn_mov_dpp((int)f2u(v), CTRL, 0xf, 0xf, true)); }
#define OWN_PF 1
template <int BYTES> __device__ __forceinline__ void own_code(int tid, unsigned* sink) {
  if (!OWN_PF || __builtin_amdgcn_readfirstlane(tid >> 6) * 4096 >= BYTES) return;
  const char* pc; unsigned keep;
  asm volatile("s_getpc_b64 %0" : "=s"(pc));
  asm volatile("s_mov_b32 %0, m0\n\ts_mov_b32 m0, %3\n\ts_nop 0\n\tglobal_load_lds_dword %1, %2\n\ts_mov_b32 m0, %0"
               : "=&s"(keep) : "v"(tid * 64 < BYTES ? tid * 64 : 0), "s"(pc), "s"((unsigned)(uintptr_t)(LAS unsigned*)sink) : "memory");
}
constexpr int DPP_XOR1 = 0xB1, DPP_XOR2 = 0x4E, DPP_ROR4 = 0x124, DPP_ROR8 = 0x128;
#if defined(__HIP_DEVICE_COMPILE__)
#pragma clang attribute pop
#endif

typedef short v4i16_t __attribute__((ext_vector_type(4)));
__device__ __forceinline__ bf16x8 tr_frag(const char* lo_, const char* hi_) {
  const v4i16_t lo = __builtin_amdgcn_ds_read_tr16_b64_v4i16((LAS v4i16_t*)lo_), hi = __builtin_amdgcn_ds_read_tr16_b64_v4i16((LAS v4i16_t*)hi_);
  return (bf16x8){lo[0], lo[1], lo[2], lo[3], hi[0], hi[1], hi[2], hi[3]};
}
__device__ __forceinline__ int rec_wg(int gp, int m) { return (((gp & 1) * 16 + m) << 3) | (gp >> 1); }
__device__ __forceinline__ float wsum(float v) {
  v += dppf<DPP_XOR1>(v); v += dppf<DPP_XOR2>(v); v += dppf<DPP_ROR4>(v); v += dppf<DPP_ROR8>(v);
  return x32sum(x16sum(v));
}
__device__ __forceinline__ float ln_row(int lane, u32x4 (&xo)[2]) {
  const bool two = lane < 16;
  float v[16];
#pragma unroll
  for (int hf = 0; hf < 2; ++hf)
#pragma unroll
    for (int i = 0; i < 4; ++i) { const unsigned u = xo[hf][i]; v[hf * 8 + 2 * i] = bf2f(u & 0xffff); v[hf * 8 + 2 * i + 1] = bf2f(u >> 16); }
  float s0 = 0.f, s1 = 0.f;
#pragma unroll
  for (int i = 0; i < 8; ++i) { s0 += v[i]; s1 += v[8 + i]; }
  const float mean = wsum(two ? s0 + s1 : s0) * (1.0f / DM);
  s0 = s1 = 0.f;
#pragma unroll
  for (int i = 0; i < 8; ++i) { const float d0 = v[i] - mean, d1 = v[8 + i] - mean; s0 += d0 * d0; s1 += d1 * d1; }
  const float var = rbf(wsum(two ? s0 + s1 : s0) * (1.0f / DM));
  const float mb = rbf(mean);
  const float rden = 1.0f / rbf(sqrtf(rbf(var + 1e-5f)));
  float n2[2] = {0.f, 0.f};
#pragma unroll
  for (int hf = 0; hf < 2; ++hf) {
    float y[8];
#pragma unroll
    for (int i = 0; i < 8; ++i) { y[i] = rbf(rbf(v[hf * 8 + i] - mb) * rden); n2[hf] += y[i] * y[i]; }
    xo[hf] = (u32x4){pack2(y[0], y[1]), pack2(y[2], y[3]), pack2(y[4], y[5]), pack2(y[6], y[7])};
  }
  return wsum(two ? n2[0] + n2[1] : n2[0]);
}

#define PROT(k)
#define WARM_CODE 1
#define B_AUX 16
#define X_AUX 16
#define T_AUX 16
#define PF_POS 0
#define PRB_N0 3
#define PRB_N1 0
#define PRB_LOG(i, v)
#define PLD(p) __builtin_nontemporal_load((const u32x4*)(p))
struct ProArgs { const u16* x; const u16* wq; const u16* wk; const u16* wv; const u16* wo; const u16* wqkvb; const u16* wob; char* ws; };
__global__ __launch_bounds__(640, 3) void prologue_kernel(ProArgs a) {
  __shared__ __attribute__((aligned(16))) char img[192 * 320];
  __shared__ __attribute__((aligned(16))) char slots[10 * 6144];
  __shared__ float red[10][160];
  __shared__ unsigned latw[8];
  __shared__ u32x4 xst[2][640];
  const int b = blockIdx.x, tid = threadIdx.x, lane = tid & 63, v = __builtin_amdgcn_readfirstlane(tid >> 6), c = lane & 15, g = lane >> 4;
  const int h = (b & 7) * 4 + ((b >> 3) & 3), dh = (b >> 5) & 1, cq = b >> 6, ks = dh * 10 + v;
  char* const slot = slots + v * 6144;
  PROT(0);
  __shared__ unsigned sink[64];
  own_code<9600>(tid, sink);
  const unsigned zo = v < 8 ? (unsigned)(((b >> 6) * 512 + ((b & 7) * 2 + (v >> 2)) * 32 + ((b >> 3) & 7) * 4 + (v & 3)) * 1024) : (unsigned)(((v - 7) * 2048 + (b & 7) * 32 + ((b >> 3) & 7) * 4 + (b >> 6)) * 1024);
  __builtin_amdgcn_raw_buffer_store_b128((u32x4){0u, 0u, 0u, 0u}, __builtin_amdgcn_make_buffer_rsrc(a.ws, 0, (int)(3 * PG), 0x00020000), zo + lane * 16, 0, 16);
  __builtin_amdgcn_sched_barrier(0);
  u32x4 wa[2][6], wb[6], tr[4], xo[2];
  const int xrow = b * 8 + (v & 7);
  xo[0] = *(const u32x4*)(a.x + (size_t)xrow * DM + lane * 8);
  xo[1] = *(const u32x4*)(a.x + (size_t)xrow * DM + 512 + c * 8);
  const int job = imin(b + 256 * (9 - v), 800);
  const int jn = job < 600 ? job / 20 : (job % 200) / 20, jk = job % 20;
  {
    const u16* src = job < 600 ? a.wqkvb + (size_t)(32 * jk) * 3 * DM : a.wob + (size_t)(32 * jk) * DM;
#pragma unroll
    for (int i = 0; i < 4; ++i) tr[i] = *(const u32x4*)(src + (size_t)(i * 8 + (lane >> 3)) * (job < 600 ? 3 * DM : DM) + jn * 64 + (lane & 7) * 8);
  }
#pragma unroll
  for (int i = 0; i < 6; ++i) {
    const int idx = (v * 6 + i) * 64 + lane, row = idx / 20;
    wb[i] = PLD(a.wo + (size_t)(h * DH + row) * DM + cq * 160 + (idx - row * 20) * 8);
  }
#pragma unroll
  for (int i = 0; i < 6; ++i) {
    const int idx = i * 64 + lane, r = idx / 24;
#pragma unroll
    for (int u = 0; u < 2; ++u) wa[u][i] = PLD(a.wv + (size_t)(32 * ks + 8 * (r >> 2) + (r & 3) + 4 * u) * HD + h * DH + (idx - r * 24) * 8);
  }
  unsigned pf[2] = {0u, 0u};
  auto prefetch = [&]() {
#pragma unroll
    for (int i = 0; i < 2; ++i) pf[i] = *(const unsigned*)((i ? a.wq : a.wk) + ((size_t)(b * 10 + v) * 48 + imin(lane, 47)) * 32);
  };
  if (PF_POS == 0) prefetch();
  __builtin_amdgcn_sched_barrier(0);
  unsigned lat = ~0u;
  auto probe = [&](int n0, int n) {
    const char* p = a.ws + zo;
#pragma unroll 1
    for (int i = n0; i < n0 + n; ++i) {
      u64 t0, t1; unsigned d;
      asm volatile("s_memtime %0\n\ts_waitcnt lgkmcnt(0)\n\ts_load_dword %2, %3, %4\n\ts_waitcnt lgkmcnt(0)\n\ts_memtime %1\n\ts_waitcnt lgkmcnt(0)"
                   : "=&s"(t0), "=&s"(t1), "=&s"(d) : "s"(p), "s"((i & 3) * 256 + (i >> 2) * 128) : "memory");
      PRB_LOG(i, (unsigned)t1 - (unsigned)t0);
      lat = umin(lat, (unsigned)t1 - (unsigned)t0);
    }
  };
  if (PRB_N0) { asm volatile("s_waitcnt vmcnt(26)" ::: "memory"); probe(0, PRB_N0); }
  __builtin_amdgcn_sched_barrier(0);
  auto xput = [&](int k) {
    if (v < 8) { xst[k][lane * 8 + (v ^ (lane & 7))] = xo[0]; if (lane < 16) xst[k][(64 + lane) * 8 + (v ^ (lane & 7))] = xo[1]; }
  };
  xput(0);
  const float xn2 = u2f(__builtin_amdgcn_readfirstlane(f2u(ln_row(lane, xo))));
  xput(1);
#pragma unroll
  for (int i = 0; i < 4; ++i) *(u32x4*)(slot + (i * 8 + (lane >> 3)) * 144 + (lane & 7) * 16) = tr[i];
  __builtin_amdgcn_fence(__ATOMIC_RELEASE, "wavefront"); __builtin_amdgcn_wave_barrier();
  bf16x8 tf[4];
#pragma unroll
  for (int t = 0; t < 4; ++t) {
    const char* bp = slot + (8 * g + (c >> 2)) * 144 + t * 32 + (c & 3) * 8;
    tf[t] = tr_frag(bp, bp + 4 * 144);
  }
  __builtin_amdgcn_fence(__ATOMIC_RELEASE, "wavefront"); __builtin_amdgcn_wave_barrier();
  __builtin_amdgcn_sched_barrier(0);
#pragma unroll
  for (int i = 0; i < 6; ++i) {
    const int idx = (v * 6 + i) * 64 + lane, row = idx / 20;
    *(u32x4*)(img + row * 320 + (((idx - row * 20) ^ ((row >> 2) & 2)) * 16)) = wb[i];
  }
  bf16x8 A[2][6];
#pragma unroll
  for (int u = 0; u < 2; ++u) {
#pragma unroll
    for (int i = 0; i < 6; ++i) {
      const int idx = i * 64 + lane, r = idx / 24;
      *(u32x4*)(slot + r * 384 + (((idx - r * 24) ^ (r >> 1)) * 16)) = wa[u][i];
    }
    __builtin_amdgcn_fence(__ATOMIC_RELEASE, "wavefront"); __builtin_amdgcn_wave_barrier();
#pragma unroll
    for (int e = 0; e < 6; ++e) A[u][e] = *(const bf16x8*)(slot + c * 384 + (((4 * e + g) ^ (c >> 1)) * 16));
    __builtin_amdgcn_fence(__ATOMIC_RELEASE, "wavefront"); __builtin_amdgcn_wave_barrier();
  }
  BAR();
  PROT(1);
  if (PF_POS == 0) asm volatile("" :: "v"(pf[0]), "v"(pf[1]));
  if (PF_POS == 1) prefetch();
  {
    const unsigned xf = (unsigned)((b >> 1) * 20480 + (tid >> 3) * 256 + (b & 1) * 128 + (tid & 7) * 16);
    const __amdgpu_buffer_rsrc_t rx = __builtin_amdgcn_make_buffer_rsrc(a.ws + OFF_XN, 0, (int)(OFF_QB - OFF_XN), 0x00020000);
    __builtin_amdgcn_raw_buffer_store_b128(xst[1][tid ^ ((tid >> 3) & 7)], rx, xf, 0, X_AUX);
    __builtin_amdgcn_raw_buffer_store_b128(xst[0][tid ^ ((tid >> 3) & 7)], rx, xf + (unsigned)(OFF_XB - OFF_XN), 0, X_AUX);
    if (v < 8 && lane < 4) ((float*)(a.ws + OFF_XNRM))[xrow * 4 + lane] = lane == 0 ? xn2 : 0.f;
  }
  if (job < 800) {
    const __amdgpu_buffer_rsrc_t rt = __builtin_amdgcn_make_buffer_rsrc(a.ws + (job < 600 ? OFF_WQKVBT : OFF_WOBT) + ((size_t)jn * 80 + jk) * 1024, 0, 81920, 0x00020000);
#pragma unroll
    for (int t = 0; t < 4; ++t) __builtin_amdgcn_raw_buffer_store_b128(__builtin_bit_cast(u32x4, tf[t]), rt, lane * 16 + t * 20480, 0, T_AUX);
  }
  const int gp = h >> 1, hh = h & 1;
  const __amdgpu_buffer_rsrc_t rwb = __builtin_amdgcn_make_buffer_rsrc(a.ws + OFF_WB, 0, 256 * 100 * 1024, 0x00020000);
#pragma unroll
  for (int n = 0; n < 10; ++n) {
    const int off = n * 32 + 8 * (c & 3), T = cq * 10 + n;
    const char* bp = img + (8 * g + (c >> 2)) * 320 + (off ^ ((g & 1) << 5));
    f32x4 ac[2] = {{0.f, 0.f, 0.f, 0.f}, {0.f, 0.f, 0.f, 0.f}};
#pragma unroll
    for (int e = 0; e < 6; ++e) {
      const bf16x8 bf = tr_frag(bp + e * 32 * 320, bp + (e * 32 + 4) * 320);
      ac[0] = mfma32(A[0][e], bf, ac[0]); ac[1] = mfma32(A[1][e], bf, ac[1]);
    }
    const u32x4 o = {pack2(ac[0][0], ac[0][1]), pack2(ac[0][2], ac[0][3]), pack2(ac[1][0], ac[1][1]), pack2(ac[1][2], ac[1][3])};
    const unsigned oo = T < 32 ? (unsigned)((rec_wg(gp, T & 15) * 100 + (hh * 3 + (T >> 4)) * 20 + ks) * 1024 + lane * 16)
                               : (unsigned)((rec_wg(gp, 2 * (T - 32) + (c >> 3)) * 100 + 40 + ks) * 1024 + (g * 16 + hh * 8 + (c & 7)) * 16);
    __builtin_amdgcn_raw_buffer_store_b128(o, rwb, oo, 0, B_AUX);
    float n2 = 0.f;
#pragma unroll
    for (int r = 0; r < 4; ++r) n2 += ac[0][r] * ac[0][r] + ac[1][r] * ac[1][r];
    n2 = x32sum(x16sum(n2));
    if (g == 0) red[v][n * 16 + c] = n2;
  }
  PROT(2);
  if (PF_POS == 1) asm volatile("" :: "v"(pf[0]), "v"(pf[1]));
  probe(PRB_N0, PRB_N1);
  if (v < 8) latw[v] = lat;
  else if (b < 64 && lane == 0) ((unsigned*)(a.ws + OFF_LAT))[(v - 4) * 64 + b] = umin(lat, 60000u);
  if (WARM_CODE && v == 7) {
    const char* pc; int d0, d1;
    asm("s_getpc_b64 %0\n\ts_mov_b32 %1, _Z10rec_kernel7RecArgs@rel32@lo+4\n\ts_mov_b32 %2, _Z11proj_kernelPKtS0_S0_Pt@rel32@lo+12" : "=s"(pc), "=s"(d0), "=s"(d1));
    const char *c0 = pc + d0, *c1 = pc + d1;
    const long o = ((long)b * 64 + lane) * 64;
    if (o < c1 - c0) asm volatile("" :: "v"(*(const volatile GAS unsigned*)(c0 + o)));
  }
  if (v == 9 && lane < 32) {
    const int q = b * 32 + lane;
    *(u32x4*)(a.ws + OFF_KV + (size_t)(q >> 2) * KVT + 1280 + (q & 3) * 336 + 320) = (u32x4){0x3f803f80u, 0x3f803f80u, 0x3f803f80u, 0x3f803f80u};
  }
  BAR();
  if (v == 1 && lane == 0) ((unsigned*)(a.ws + OFF_LAT))[b] = umin(umin(umin(latw[0], latw[4]) + umin(latw[1], latw[5]), 30000u) + umin(umin(latw[2], latw[6]) + umin(latw[3], latw[7]), 30000u), 60000u);
  if (v == 0) {
    float mx = 0.f;
#pragma unroll
    for (int k = 0; k < 3; ++k) {
      const int col = k * 64 + lane;
      float sm = 0.f;
#pragma unroll
      for (int i = 0; i < 10; ++i) sm += red[i][col < 160 ? col : 0];
      mx = fmx(mx, sm);
    }
    mx = fmx(mx, dppf<DPP_XOR1>(mx)); mx = fmx(mx, dppf<DPP_XOR2>(mx)); mx = fmx(mx, dppf<DPP_ROR4>(mx)); mx = fmx(mx, dppf<DPP_ROR8>(mx));
    mx = x32mx(x16mx(mx));
    if (lane == 0) ((float*)(a.ws + OFF_BNRM))[(cq * 32 + h) * 2 + dh] = mx;
  }
  PROT(3);
}

struct RecArgs {
  const u16* mem0; const u16* wq; const u16* wk; const u16* xold; u16* xnew; char* ws;
  unsigned layer; unsigned pad;
};
#define RLX_AGENT __ATOMIC_RELAXED, __HIP_MEMORY_SCOPE_AGENT
constexpr unsigned SPIN_CAP = 1u << 20;
#define TSTAMP(k, v)
#define ARRIVE(k)
#define ARRIVE_OUT()
#define ARRIVE_DECL()
#define LOGV(k, v)
#define LOGF(k, v)
#define LOGP(k)
#define LOGQ(k)
#define LOGF2(k)
#define LOGD(k)
#define LOGT(k, v)
#define STAMP_DECL()
#define STAMP(k)
#define STAMPV(k, v)
#define STAMP_OUT()
#define REC_D1 520
#define SCH_V 18
#define SCH_VMIN 0
#define SCH_DMIN 4
#define SCH_PEN 170
#define SCH_VMIN2 (-4)
#define SCH_PEN2 40
#define SCH_GU 1
#define SCH_GD 2
#define SCH_UP 6
#define SCH_D0 196
#define SCH_FIRST 120
#define EXP_NODMA 0
#define BG_HOLD 220
#define QBATCH 0
#define WPF_N 5
#define WPF_W 0
#define WPF0 (63 - WPF_N)
#define BG_HOLD4 250
#define D1_W1 20
#define F3_D 1200
#define T2W 2
#define AT_ASM 1
#define PM_Z 1
#define P2_X 1
#define ST_KS 14
#define ST_KS0 ST_KS
#define ST_KS1 ST_KS
#define PADR 0x6666
#define PADT 0x000
#define CODE_PAD(n) asm volatile(".p2align 6\n.rept %0\ns_nop 0\n.endr" :: "n"(n))
#define ST_X 300
#define ST_ALL 4
#define CPF_S 61
#define FR_D 700
#define FR_PACE 6
#define END_E60 600
#define END_E1 1300
#define END_E2 3400
#define END_E62 2800
#define END_E3 6800
#define END_E4 11400
#define EARLY_N 4
#define EARLY_D1 0
#define EARLY_V 4
#define EARLY_V1 10
#define F3 0
#define F3A 0
#define F3A_D 400
#define T4_D 300
#define T4_P 1
#define P2_W 1
#define PS_SL 1
#define F2B 4
#define F0C 0
#define F2C 0
#define W2_P 1
#define SCH_W 1
#define SP_SL 0
#define D1_M 300
#define D1_MIN 260
#define D1_C 96
#define D1_G 2
#define BG_HOLD6 0
#define W2_D 0
#define END_E61 2600
#define D1_LATE 200
#define P2_G0 10
#define P2_G2 6
#define P2_G3 6
#define SCH_RL 0
#define SCH_P 3
#define MMA_P 0
#define SP_P 0
#define F6 8
#define F7 10
#define XPF_W 2
#define SCH_LATE 0
#define LATE_V 6
#define QW 3
#define FB_SL 0
#define P2_G1 1
#define P1_G 0
#define F4Z 0
#define F5Z 0
#define T5_Z 2
#define F0A 3
#define FB_E 0
#define EXACT 1
#define D1_P 3
#define VO_A0 9
#define VO_A2 10
#define VO_B0 3
#define VO_B1 0
#define VO_N2 7
#define VO_N3 10
#define VO_W -1
#define UNIT 0
#define UN_P 2
#define VP_E 1
#define TS_B 0
#define ST_E 1
#define W1_SL 0
#define VO_A1 4
#define LAST_V 18
#define END_E59 3200
#define END_G2 600
#define END_G3 600
#define QR 4
#define WPF_S 128
#define END_SL 4
#define KP0 0
#define KP1 0
#define XPF 3
#define W1_D 0
#define F4 12
#define F5 0
#define F67 0
#define KP2 0
#define REC_RNG 70u
#define SCH_DRAIN 40
#define PL_X 0
#define PIN_V(x) asm volatile("" : "+v"(x))
#define PIN_S(x) asm volatile("" : "+s"(x))
#define VM_DRAIN() do { asm volatile("s_waitcnt vmcnt(0)" ::: "memory"); __builtin_amdgcn_s_waitcnt(0x0F70); } while (0)
#define SC1_LOAD(off) __builtin_amdgcn_raw_buffer_load_b128(rs, (off), 0, 16)
#define SC1_LOADS(off, soff) __builtin_amdgcn_raw_buffer_load_b128(rs, (off), (soff), 16)
#define SC1_STORE8S(v, off, soff) __builtin_amdgcn_raw_buffer_store_b64((v), rs, (off), (soff), 16)
#define SC1_LOAD8(off) __builtin_amdgcn_raw_buffer_load_b64(rs, (off), 0, 16)
#define SC1_STORE(v, off) __builtin_amdgcn_raw_buffer_store_b128((v), rs, (off), 0, 16)
#define SC1_STORE8(v, off) __builtin_amdgcn_raw_buffer_store_b64((v), rs, (off), 0, 16)
#if defined(__HIP_DEVICE_COMPILE__)
#pragma clang attribute push(__attribute__((target("no-packed-fp32-ops"))), apply_to = function)
#endif
__device__ __forceinline__ unsigned now32() { return (unsigned)__builtin_amdgcn_s_memrealtime(); }
__device__ __forceinline__ void wait_until(unsigned t) { for (;;) { const int d = (int)(t - now32()); if (d <= 0) break; if (d > 8) __builtin_amdgcn_s_sleep(1); } }
__device__ __forceinline__ unsigned cyc32() { return (unsigned)__builtin_amdgcn_s_memtime(); }
__device__ __forceinline__ void wait_cyc(unsigned t) { for (;;) { const int d = (int)(t - cyc32()); if (d <= 0) break; if (d > 200) __builtin_amdgcn_s_sleep(1); } }
#define WAIT_ASM(clk, far_) "1:\n\t" clk " vcc\n\ts_waitcnt lgkmcnt(0)\n\ts_sub_i32 %0, %1, vcc_lo\n\ts_cmp_lt_i32 %0, " far_ "\n\ts_cbranch_scc1 3f\n\ts_sleep 1\n\ts_branch 1b\n" \
                            "2:\n\t" clk " vcc\n\ts_waitcnt lgkmcnt(0)\n\ts_sub_i32 %0, %1, vcc_lo\n3:\n\ts_cmp_gt_i32 %0, 0\n\ts_cbranch_scc1 2b"
__device__ __forceinline__ int spin_until(unsigned t) { int d; asm volatile(WAIT_ASM("s_memrealtime", "9") : "=&s"(d) : "s"(t) : "vcc", "scc", "memory"); return d; }
__device__ __forceinline__ int spin_cyc(unsigned t) { int d; asm volatile(WAIT_ASM("s_memtime", "200") : "=&s"(d) : "s"(t) : "vcc", "scc", "memory"); return d; }
__device__ __forceinline__ void spin_exact(unsigned t) {
  if (!EXACT) { spin_cyc(t); return; }
  int d;
  asm volatile("1:\n\ts_memtime vcc\n\ts_waitcnt lgkmcnt(0)\n\ts_sub_i32 %0, %1, vcc_lo\n\ts_cmp_gt_i32 %0, 95\n\ts_cbranch_scc1 1b\n\t"
               "s_max_i32 %0, %0, 0\n\ts_andn2_b32 %0, %0, 3\n\ts_sub_i32 %0, 3f-2f, %0\n\ts_getpc_b64 vcc\n"
               "2:\n\ts_add_u32 vcc_lo, vcc_lo, %0\n\ts_addc_u32 vcc_hi, vcc_hi, 0\n\ts_setpc_b64 vcc\n\t.rept 24\n\ts_nop 0\n\t.endr\n3:"
               : "=&s"(d) : "s"(t) : "scc", "vcc", "memory");
}
template <int OFF, int SL> __device__ __forceinline__ void lds_spin(unsigned lds0, unsigned want) {
  unsigned v;
  asm volatile("1:\n\tds_read_b32 %0, %1 offset:%3\n\t.if %4\n\ts_sleep %4\n\t.endif\n\ts_waitcnt lgkmcnt(0)\n\tv_cmp_gt_u32 vcc, %2, %0\n\ts_cbranch_vccnz 1b"
               : "=&v"(v) : "v"(lds0), "s"(want), "n"(OFF), "n"(SL) : "vcc", "memory");
}
template <int CTRL> __device__ __forceinline__ int dppi(int v) { return __builtin_amdgcn_mov_dpp(v, CTRL, 0xf, 0xf, true); }
__device__ __forceinline__ int wave_imax(int l) {
  l = imax(l, dppi<DPP_XOR1>(l)); l = imax(l, dppi<DPP_XOR2>(l)); l = imax(l, dppi<DPP_ROR4>(l)); l = imax(l, dppi<DPP_ROR8>(l));
  int a = imax(__builtin_amdgcn_readlane(l, 0), __builtin_amdgcn_readlane(l, 16)), b = imax(__builtin_amdgcn_readlane(l, 32), __builtin_amdgcn_readlane(l, 48));
  PIN_S(a); PIN_S(b);
  return imax(a, b);
}
__device__ __forceinline__ void glds16(const void* gsrc, unsigned lds_dst) {
  unsigned keep;
  asm volatile("s_mov_b32 %0, m0\n\ts_mov_b32 m0, %2\n\ts_nop 0\n\tglobal_load_lds_dwordx4 %1, off\n\ts_mov_b32 m0, %0" : "=&s"(keep) : "v"(gsrc), "s"(lds_dst) : "memory");
}
__device__ __forceinline__ void glds16_sc1(const void* gsrc, unsigned lds_dst) {
  unsigned keep;
  asm volatile("s_mov_b32 %0, m0\n\ts_mov_b32 m0, %2\n\ts_nop 0\n\tglobal_load_lds_dwordx4 %1, off sc1\n\ts_mov_b32 m0, %0" : "=&s"(keep) : "v"(gsrc), "s"(lds_dst) : "memory");
}
__device__ __forceinline__ unsigned acc_off(int G, int row) {
  return (unsigned)(G < 128 ? ((G & 31) | ((G & 32) << 1) | ((G & 64) >> 1)) * 256 + row * 16 : (2 * G - 128 + (row >> 2)) * 256 + (row & 3) * 16);
}
__device__ __forceinline__ unsigned nz(unsigned v) { return v > 1u ? v : 1u; }
__device__ __forceinline__ unsigned pack2n(float lo, float hi) { return nz(pack2(lo, hi)); }
__device__ __forceinline__ unsigned min4(u32x4 v) { return umin(umin(v[0], v[1]), umin(v[2], v[3])); }
__device__ __forceinline__ bf16x8 as8(u32x4 v) { return __builtin_bit_cast(bf16x8, v); }

constexpr int NV = 7, NKQ = 2;
constexpr int L_RED1 = 0;
constexpr int L_BM = L_RED1 + 8 * 1536;
constexpr int L_STG = L_BM + 10240;
constexpr int L_ST = L_STG + 2048 + 2560;
constexpr int L_MISC = L_ST + 512;
constexpr int L_KX = L_MISC + 128;
constexpr int L_QX = L_KX + NKQ * 2048;
constexpr int L_XN = L_QX + NKQ * 2048;
constexpr int L_VOX = L_XN + 81920;
constexpr int L_VOM = L_VOX + NV * 5120;
constexpr int L_BYTES = L_VOM + NV * 1280;
constexpr int S_IMG = L_XN + 40960;
static_assert(S_IMG + 61440 <= L_VOM, "start-up image");
static_assert(L_BYTES <= 160 * 1024, "LDS budget");

__device__ __forceinline__ bf16x8 wqk_frag(const char* lds, int t, int ks, int lane) {
  const int c = lane & 15, g = lane >> 4, p = c & 3;
  const char* q = lds + S_IMG + (32 * ks + 8 * g + (c >> 2)) * 96 + 8 * p + (t == 0 ? 0 : (t == 1 ? 48 : (p < 2 ? 32 : 64)));
  const v4i16_t lo = __builtin_amdgcn_ds_read_tr16_b64_v4i16((LAS v4i16_t*)q), hi = __builtin_amdgcn_ds_read_tr16_b64_v4i16((LAS v4i16_t*)(q + 4 * 96));
  return (bf16x8){lo[0], lo[1], lo[2], lo[3], hi[0], hi[1], hi[2], hi[3]};
}
__device__ __forceinline__ bf16x8 sel_frag(int c, int base) {
  bf16x8 r;
#pragma unroll
  for (int i = 0; i < 8; ++i) r[i] = (c == base + i) ? (short)0x3f80 : (short)0;
  return r;
}
__device__ __forceinline__ bf16x8 qk_frag(const char* p4, const char* p2, const char* p2b) {
  f32x4 a0 = *(const f32x4*)(p4), a1 = *(const f32x4*)(p4 + 1792), a2 = *(const f32x4*)(p4 + 3584), a3 = *(const f32x4*)(p4 + 5376);
  f32x2 b0 = *(const f32x2*)(p2), b1 = *(const f32x2*)(p2 + 1792), b2 = *(const f32x2*)(p2b), b3 = *(const f32x2*)(p2b + 1792);
  f32x4 va = (a0 + a1) + (a2 + a3);
  f32x2 vb = (b0 + b1) + (b2 + b3);
  return mk8(pack2(va[0], va[1]), pack2(va[2], va[3]), pack2(vb[0], vb[1]), 0u);
}
__device__ __forceinline__ void x_fetch(const u16* src, unsigned lds0, int seg, int p, int lane) {
  if (EXP_NODMA) return;
  glds16_sc1(src + (size_t)p * 512 + lane * 8, lds0 + L_XN + (seg & 1) * 40960 + p * 1024);
}
template <int N> __device__ __forceinline__ void x_fetch_n(const u16* src, unsigned lds0, int seg, int p0, unsigned voff) {
  if (EXP_NODMA) return;
#pragma unroll
  for (int i = 0; i < N; i += 4) {
    unsigned keep;
    asm volatile("s_mov_b32 %0, m0\n\ts_mov_b32 m0, %3\n\ts_nop 0\n\tglobal_load_lds_dwordx4 %1, %2 sc1\n\t"
                 ".if %4 > 1\n\tglobal_load_lds_dwordx4 %1, %2 offset:1024 sc1\n\t.endif\n\t.if %4 > 2\n\tglobal_load_lds_dwordx4 %1, %2 offset:2048 sc1\n\t.endif\n\t"
                 ".if %4 > 3\n\tglobal_load_lds_dwordx4 %1, %2 offset:3072 sc1\n\t.endif\n\ts_mov_b32 m0, %0"
                 : "=&s"(keep) : "v"(voff), "s"(src + (size_t)(p0 + i) * 512), "s"(lds0 + L_XN + (seg & 1) * 40960 + (p0 + i) * 1024), "n"(N - i < 4 ? N - i : 4) : "memory");
  }
}
__device__ __forceinline__ int qkv_row(int b) { return (b & 7) / QR * (2 * QR) + (b >> 3) / (16 / QR); }
__device__ __forceinline__ int qkv_col(int b) { return (b & 7) % QR * (16 / QR) + (b >> 3) % (16 / QR); }
__device__ __forceinline__ const u16* x_src(const RecArgs& a, int seg, int b) {
  return seg < NSEG ? (const u16*)(a.ws + OFF_XN) + (size_t)seg * 32 * DM : a.xnew + (size_t)(qkv_row(b) + 16 * (seg - NSEG)) * 32 * DM;
}
template <int N>
__device__ __forceinline__ void img_fetch(const RecArgs& a, unsigned lds0, int h, int j, int part, int lane) {
#pragma unroll
  for (int i = 0; i < N; ++i) {
    const int n = part * N + i, idx = n * 64 + lane, row = idx / 6, pc = idx - row * 6;
    if (FR_PACE && N == 20) asm volatile("s_waitcnt vmcnt(%0)" :: "n"(FR_PACE) : "memory");
    glds16((pc < 3 ? a.wq : a.wk - 24) + (size_t)row * HD + h * DH + j * 24 + pc * 8, lds0 + S_IMG + n * 1024);
  }
}
constexpr int P2B = F4 + F5 + 2 * F67 + F6 + F7, PZ = P2B + F2B, PA = PZ, P0C = PA + 2 * F0A, F3C = 40 - F3A - F3 - P0C - 2 * F0C - F2C;
static_assert(F3C >= 0, "fetch plan");
template <int OFF> __device__ __forceinline__ void lds_inc(unsigned lds0) {
  asm volatile("s_mov_b64 exec, 1\n\tds_add_u32 %0, %1 offset:%2\n\ts_mov_b64 exec, -1" :: "v"(lds0), "v"(1u), "n"(OFF) : "memory");
}
__device__ __forceinline__ void lds_arrive(unsigned* p, int lane) {
  asm volatile("s_waitcnt lgkmcnt(0)" ::: "memory");
  if (lane == 0) __hip_atomic_fetch_add(p, 1u, __ATOMIC_RELAXED, __HIP_MEMORY_SCOPE_WORKGROUP);
}
__device__ __forceinline__ void lds_wait(unsigned* p, unsigned want) {
  while (__hip_atomic_load(p, __ATOMIC_RELAXED, __HIP_MEMORY_SCOPE_WORKGROUP) < want) __builtin_amdgcn_s_sleep(1);
  asm volatile("" ::: "memory");
}
constexpr float KEXP = 0.07216878364870322f * 1.4426950408889634f;
#define FRESH_LANE() int lane = lane0; asm volatile("" : "+v"(lane)); const int c = lane & 15, g = lane >> 4; (void)c; (void)g

struct OutTask {
  u32x4 y[4]; u32x2 xo, out;
  bf16x8 sel;
  unsigned t5f, t5o;
  __device__ __forceinline__ void init(int b, int lane) {
    const int um = b / 10, uq = (b / 5) & 1, up = b % 5, tok = lane & 15, k = up < 4 ? (up >> 1) * 256 + um * 16 + (up & 1) * 8 : 512 + um * 8, q4 = ((lane >> 4) & 1) * 4;
    t5f = (unsigned)(((uq * 20 + (k >> 5)) * 64 + ((k & 31) >> 3) * 16 + tok) * 8 + q4);
    t5o = (unsigned)(((um * 2 + uq) * 16 + (lane >> 4)) * 1280 + up * 256 + tok * 16);
    if (b >= 160) { t5f = 0u; t5o = 0x7ffffff0u; }
    sel = sel_frag(tok, 0);
    PIN_V(sel);
  }
  static __device__ __forceinline__ bool ok(int sl, int b) { return sl >= 0 && b < 160; }
  __device__ __forceinline__ void load(const RecArgs& a, const __amdgpu_buffer_rsrc_t rs, int sl, int b, int part = 3) {
    const unsigned o = t5o | (sl < 0 ? 0x7ff00000u : 0u);
    sl = imax(sl, 0);
#pragma unroll
    for (int k = 0; k < 4; ++k) if (part & 1) y[k] = SC1_LOADS(o, op_slot(sl) + k * 5120);
    if (part & 2) xo = __builtin_bit_cast(u32x2, __hip_atomic_load((const gu64*)(a.xold + (size_t)sl * 32 * DM + t5f), RLX_AGENT));
  }
  __device__ __forceinline__ bool ready(int b) { return b >= 160 || wave_all(umin(umin(min4(y[0]), min4(y[1])), umin(min4(y[2]), min4(y[3]))) != 0u); }
  __device__ __forceinline__ void run(int lane) {
    f32x4 d = {0.f, 0.f, 0.f, 0.f};
#pragma unroll
    for (int k = 0; k < 4; ++k) d = mfma32(sel, as8(y[k]), d);
#pragma unroll
    for (int i = 0; i < 2; ++i) {
      const unsigned r = pack2(d[2 * i], d[2 * i + 1]), x = xo[i];
      out[i] = pack2(u2f(x << 16) + u2f(r << 16), u2f(x & 0xffff0000u) + u2f(r & 0xffff0000u));
    }
    PIN_V(out);
  }
  __device__ __forceinline__ void store(const RecArgs& a, int sl, int b, int lane) {
    __builtin_amdgcn_raw_buffer_store_b64(out, __builtin_amdgcn_make_buffer_rsrc(a.xnew, 0, ok(sl, b) ? S_ * DM * 2 : 0, 0x00020000), (t5f + (unsigned)sl * 32 * DM) * 2u | (unsigned)(lane & 32) << 25, 0, 16);
  }
};

struct PvTask2 {
  u32x4 y[2][2], pa[2], pb[2];
  unsigned lo, lm, vr, ma, mz, so, sz;
  __device__ __forceinline__ void init(int gp, int lane) {
    const int hi = lane >> 5;
    lo = pt_tok(2 * gp) + (unsigned)(lane * 16); lm = pt_mem(2 * gp + hi) + (unsigned)((lane & 31) * 8);
    vr = (unsigned)(L_VOX + 2048 + (lane ^ 8) * 16);
    ma = (unsigned)(L_VOM + hi * 768 + (lane & 31) * 8); mz = (unsigned)(L_VOM + 512 + ((lane & 31) ^ (hi * 8)) * 8);
    so = (unsigned)(L_STG + 2048 + (hi * 16 + (lane & 15)) * 16 + ((lane >> 4) & 1) * 8);
    sz = hi ? (unsigned)(L_RED1 + 8704 + lane * 8) : so + 1024;
    PIN_V(lo); PIN_V(lm); PIN_V(vr); PIN_V(ma); PIN_V(mz); PIN_V(so); PIN_V(sz);
#pragma unroll
    for (int q = 0; q < 2; ++q) { pa[q] = pb[q] = y[q][0] = y[q][1] = (u32x4){0u, 0u, 0u, 0u}; PIN_V(pa[q]); PIN_V(pb[q]); }
  }
  __device__ __forceinline__ void load(const __amdgpu_buffer_rsrc_t rs, unsigned o) {
#pragma unroll
    for (int q = 0; q < 2; ++q) {
      y[q][0] = SC1_LOADS(lo + q * 1024, o); y[q][1] = SC1_LOADS(lo + 2048 + q * 1024, o);
      const u32x2 p = __builtin_amdgcn_raw_buffer_load_b64(rs, lm + q * 256, o, 16);
      pa[q][0] = p[0]; pa[q][1] = p[1];
    }
  }
  __device__ __forceinline__ bool ready() {
    return wave_all(umin(umin(umin(min4(y[0][0]), min4(y[0][1])), umin(min4(y[1][0]), min4(y[1][1]))), umin(umin(pa[0][0], pa[0][1]), umin(pa[1][0], pa[1][1]))) != 0u);
  }
  template <bool DIRECT> __device__ __forceinline__ void run(char* lds, int sv, int lane, const __amdgpu_buffer_rsrc_t rs, unsigned og) {
    const f32x4 Z4 = {0.f, 0.f, 0.f, 0.f};
    const char* vp = lds + L_VOX + sv * 5120 + lane * 16;
    bf16x8 vx[6];
#pragma unroll
    for (int t = 0; t < 5; ++t) vx[t] = *(const bf16x8*)(vp + t * 1024);
    vx[5] = *(const bf16x8*)(lds + vr + sv * 5120);
    const u32x2 a0 = *(const u32x2*)(lds + ma + sv * 1280), a1 = *(const u32x2*)(lds + ma + sv * 1280 + 256);
    const u32x2 z0 = *(const u32x2*)(lds + mz + sv * 1280), z1 = *(const u32x2*)(lds + mz + sv * 1280 + 256);
    const bf16x8 vm = mk8(a0[0], a0[1], a1[0], a1[1]), vz = mk8(z0[0], z0[1], z1[0], z1[1]);
#pragma unroll
    for (int q = 0; q < 2; ++q) { PIN_V(pa[q]); pb[q][2] = pa[q][0]; pb[q][3] = pa[q][1]; PIN_V(pb[q]); }
    __builtin_amdgcn_sched_barrier(0);
    f32x4 o[2][3];
#pragma unroll
    for (int q = 0; q < 2; ++q) {
#pragma unroll
      for (int t = 0; t < 3; ++t) o[q][t] = mfma32(vx[t], as8(y[q][0]), Z4);
#pragma unroll
      for (int t = 0; t < 3; ++t) o[q][t] = mfma32(vx[3 + t], as8(y[q][1]), o[q][t]);
      o[q][0] = mfma32(vm, as8(pa[q]), o[q][0]); o[q][1] = mfma32(vm, as8(pb[q]), o[q][1]); o[q][2] = mfma32(vz, as8(pa[q]), o[q][2]);
    }
#pragma unroll
    for (int q = 0; q < 2; ++q) {
#pragma unroll
      for (int t = 0; t < 3; ++t) {
        const u32x2 v = {pack2n(o[q][t][0], o[q][t][1]), pack2n(o[q][t][2], o[q][t][3])};
        if (DIRECT) SC1_STORE8S(v, (so - (L_STG + 2048) + t * 512) | (t == 2 ? (unsigned)(lane & 32) << 25 : 0u), og + q * 20480);
        else *(u32x2*)(lds + (t < 2 ? so + t * 512 : sz) + q * 1280) = v;
      }
    }
  }
};

struct TokScores {
  bf16x8 k[2], q[2]; u32x4 v[2];
  __device__ __forceinline__ void get(const char* lds, int seg, int lane) {
#pragma unroll
    for (int i = 0; i < 2; ++i) { k[i] = *(const bf16x8*)(lds + L_KX + (seg & 1) * 2048 + i * 1024 + lane * 16); q[i] = *(const bf16x8*)(lds + L_QX + (seg & 1) * 2048 + i * 1024 + lane * 16); }
  }
  __device__ __forceinline__ void run(int qt) {
    const f32x4 Z4 = {0.f, 0.f, 0.f, 0.f};
    const f32x4 d1 = mfma32(k[0], q[qt], Z4), d2 = mfma32(k[1], q[qt], Z4);
    v[qt] = (u32x4){pack2n(d1[0], d1[1]), pack2n(d1[2], d1[3]), pack2n(d2[0], d2[1]), pack2n(d2[2], d2[3])};
    PIN_V(v[qt]);
  }
  __device__ __forceinline__ void store(const __amdgpu_buffer_rsrc_t rs, unsigned off, unsigned o) {
    __builtin_amdgcn_raw_buffer_store_b128(v[0], rs, off, o, 16); __builtin_amdgcn_raw_buffer_store_b128(v[1], rs, off + 1024, o, 16);
    asm volatile("s_nop 1" ::: "memory");
  }
};

__device__ __forceinline__ void touch(const char* base, unsigned off, unsigned lds0) {
  unsigned keep;
  asm volatile("s_mov_b32 %0, m0\n\ts_mov_b32 m0, %3\n\ts_nop 0\n\tglobal_load_lds_dword %1, %2 sc1\n\ts_mov_b32 m0, %0" : "=&s"(keep) : "v"(off), "s"(base), "s"(lds0 + L_RED1 + 8192) : "memory");
}
template <int N> __device__ __forceinline__ void stouch(const char* p, unsigned& d) {
  asm volatile(".set pf_o, 0\n\t.rept %2\n\ts_load_dword %0, %1, pf_o\n\t.set pf_o, pf_o + 128\n\t.endr" : "+s"(d) : "s"(p), "n"(N) : "memory");
}
__device__ __forceinline__ void code_touch(int b, int lane, unsigned lds0) {
  const char* pc; int d0, d1;
  asm volatile("s_getpc_b64 %0\n\ts_mov_b32 %1, _Z10rec_kernel7RecArgs@rel32@lo+4\n\ts_mov_b32 %2, _Z11attb_kernelPKtS0_Pt@rel32@lo+12" : "=s"(pc), "=s"(d0), "=s"(d1));
  const int o = (lane * 32 + (b >> 3)) * 128;
  touch(pc + d0, (unsigned)(o < d1 - d0 ? o : 0), lds0);
}
__device__ __forceinline__ unsigned zero_base(int b, int h, int j, int ln) {
  return ln < 32 ? (unsigned)((h & 3) * 32768 + j * 512 + ln * 16) : ln < 40 ? (unsigned)((h & 2) * 1024 + j * 256 + (ln & 4) * 32 + (h & 1) * 64 + (ln & 3) * 16)
                 : (unsigned)OFF_ACC + (b < ACC_NB ? (unsigned)(b * 256 + (ln - 40) * 16) : ts_piece((b - ACC_NB) * 8 + ln - 40));
}
__device__ __forceinline__ unsigned sct_slot(unsigned ck, unsigned sl) { return sl * SCT_STEP + ((ck >> (sl & 4u)) & 7u) * 4096u; }
__device__ __forceinline__ unsigned scm_slot(unsigned ck, unsigned sl) { return OFF_SCM + sl * SCM_STEP + ((ck >> 8) & 7u) * 4096u; }
__device__ __forceinline__ int ck_rot(int x) { return ((x & 1) << 2) | (x >> 1); }
struct Place {
  unsigned t[8];
  __device__ __forceinline__ void load(const char* ws, int lane) {
#pragma unroll
    for (int x = 0; x < 8; ++x) t[x] = *(const unsigned*)(ws + OFF_LAT + ((imin(lane >> 3, 5) * 8 + (ck_rot(x) ^ (lane & 7))) * 8 + x) * 4);
  }
  __device__ __forceinline__ unsigned get(int b, int lane) {
    int c = (int)((((t[0] + t[1]) + (t[2] + t[3])) + ((t[4] + t[5]) + (t[6] + t[7]))) * 8u) + (lane & 7);
    c = imin(c, dppi<DPP_XOR1>(c)); c = imin(c, dppi<DPP_XOR2>(c)); c = imin(c, dppi<0x141>(c));
    unsigned k = 0u;
#pragma unroll
    for (int r = 0; r < 6; ++r) k |= (unsigned)(__builtin_amdgcn_readlane(c, 8 * r) & 7) << (4 * r);
    return (k ^ PL_X) ^ (unsigned)ck_rot(b & 7) * 0x111111u;
  }
};

struct Sched {
  u32x4 tsv, rec;
  unsigned side, o;
  __device__ __forceinline__ void pre(int s) { o = (unsigned)OFF_ACC + (unsigned)((s + SR - 1) % SR) * (unsigned)ACC_STEP + ACC_TS + side; PIN_S(o); }
  __device__ __forceinline__ void load(const __amdgpu_buffer_rsrc_t rs, int lane) {
    tsv = SC1_LOADS(ts_piece(lane & 63) - ACC_TS, o); rec = SC1_LOADS(128, o);
  }
  __device__ __forceinline__ unsigned last(unsigned ref) {
    const unsigned o = 0x40000000u - ref;
    int l = (int)umax(umax(tsv[0] ? tsv[0] + o : 0u, tsv[1] ? tsv[1] + o : 0u), umax(tsv[2] ? tsv[2] + o : 0u, tsv[3] ? tsv[3] + o : 0u));
    return (unsigned)wave_imax(l) - o;
  }
  __device__ __forceinline__ unsigned run(const RecArgs& a, const __amdgpu_buffer_rsrc_t rs, int s, int b, int lane) {
    constexpr int w = 3; (void)w;
    unsigned acur = __builtin_amdgcn_readfirstlane(rec[0]), aprev = __builtin_amdgcn_readfirstlane(rec[1]), hist = __builtin_amdgcn_readfirstlane(rec[3]);
    int D8 = (int)__builtin_amdgcn_readfirstlane(rec[2]);
    const int d = wave_imax((int)(umax(umax(tsv[0], tsv[1]), umax(tsv[2], tsv[3])) - aprev)), dd = 8 * d - D8;
    int Dn = D8 + (dd >> (dd > 0 ? SCH_GU : SCH_GD));
    unsigned anext = (acur + (unsigned)((Dn + 4) >> 3) + SCH_V + (s < EARLY_N ? EARLY_V : (s >= WPF0 ? (s == NSEG - 2 ? LAST_V : LATE_V) : 0))) | 1u;
    if (__builtin_expect(hist < 2u || aprev == 0u || d < 1 || d + SCH_VMIN > (int)(acur - aprev), 0)) {
      if (s == 0) {
        for (int it = 0; it < 40 && !wave_all(min4(tsv) != 0u); ++it) {
          __builtin_amdgcn_s_sleep(ST_ALL);
          asm volatile("" ::: "memory");
          load(rs, lane);
        }
      }
      const unsigned now = now32(), L = last(now);
      if (acur == 0u) { D8 = 8 * SCH_D0; hist = 0u; }
      const bool late = acur == 0u || (int)(L + SCH_VMIN - acur) > 0;
      if (late) { if (b == 0) FC(3); if (acur) D8 += 8 * (hist ? SCH_UP : 3 * SCH_UP); }
      else if (aprev) { if (hist < 2u) D8 = 8 * imin(imax((int)(L - aprev), 1), 4000); hist = umin(hist + 1u, 2u); }
      const unsigned base = acur == 0u ? L + SCH_FIRST : (late ? L + SCH_V + ((int)(L + SCH_VMIN2 - acur) <= 0 ? SCH_PEN2 : SCH_PEN) : acur);
      anext = (base + (unsigned)((D8 + 4) >> 3) + SCH_V + (s == 1 ? EARLY_V1 : (s < EARLY_N ? EARLY_V : 0))) | 1u;
      if (s == 0) anext = (L + ST_X) | 1u;
      if ((int)(now - L) > 50000) anext = 0u;
      if (late) acur = 0u;
      Dn = D8;
    }
    if (__builtin_expect(b == 0, 0))
      __builtin_amdgcn_raw_buffer_store_b128((u32x4){anext, acur, (unsigned)Dn, hist}, rs, lane < 2 ? lane * 8192 : 0x7ffffff0, (unsigned)OFF_ACC + (unsigned)(s % SR) * (unsigned)ACC_STEP + ACC_TS + 128u, 16);
    LOGV(0, anext); LOGV(1, acur == 0u); LOGV(2, (Dn + 4) >> 3); LOGV(3, d); LOGV(4, acur - aprev - d); LOGV(5, aprev);
    return anext;
  }
};

struct TokTask {
  u32x4 y[2]; u32x2 ym; u32x4 res;
  unsigned l0, l1, lm, s0, s1;
  static __device__ __forceinline__ bool ok(int sl) { return sl >= 0 && sl < NSEG; }
  __device__ __forceinline__ void init(int h, int j, int lane) {
    const int c = lane & 15, g = lane >> 4;
    l0 = ts_tok(h, g) + (unsigned)(j * 256 + c * 16); l1 = l0 + 16384u;
    lm = ts_mem(h, g + 4 * (c >> 3)) + (unsigned)(j * 64 + (c & 7) * 8);
    res = (u32x4){0u, 0u, 0u, 0u};
    const int qi = 4 * j + (c & 3);
    s0 = g < 2 ? pt_tok(h) + (unsigned)((qi >> 4) * 1024 + ((c >> 2) * 16 + (qi & 15)) * 16 + g * 8) : 0x7ffffff0u;
    s1 = lane < 8 ? pt_mem(h) + (unsigned)((qi >> 4) * 256 + ((c >> 2) * 16 + (qi & 15)) * 8) : 0x7ffffff0u;
    PIN_V(l0); PIN_V(l1); PIN_V(lm); PIN_V(s0); PIN_V(s1);
  }
  __device__ __forceinline__ void load(const __amdgpu_buffer_rsrc_t rs, unsigned o) {
    y[0] = SC1_LOADS(l0, o); y[1] = SC1_LOADS(l1, o);
    ym = __builtin_amdgcn_raw_buffer_load_b64(rs, lm, o, 16);
  }
  __device__ __forceinline__ bool ready() { return wave_all(umin(umin(min4(y[0]), min4(y[1])), umin(ym[0], ym[1])) != 0u); }
  __device__ __forceinline__ void run(int j, int lane, const bf16x8 SelLo) {
    const int c = lane & 15, g = lane >> 4;
    const f32x4 Z4 = {0.f, 0.f, 0.f, 0.f};
    const f32x4 d1 = mfma32(SelLo, as8(y[1]), mfma32(SelLo, as8(y[0]), Z4));
    const f32x4 d0 = mfma32(SelLo, mk8(ym[0], ym[1], 0u, 0u), Z4);
    const int qi = 4 * j + (c & 3), k0 = 16 * g + 4 * (c >> 2);
    float t[4], mv[4];
    float mx = -INFINITY;
#pragma unroll
    for (int r = 0; r < 4; ++r) {
      t[r] = (g < 2 && k0 + r <= qi) ? d1[r] : -INFINITY;
      const float mt = d0[r] + dppf<DPP_ROR8>(d0[r]);
      mv[r] = lane < 8 ? mt : -INFINITY;
      mx = fmx(mx, fmx(t[r], mv[r]));
    }
    mx = fmx(mx, dppf<DPP_ROR8>(mx)); mx = fmx(mx, dppf<DPP_ROR4>(mx));
    mx = x16mx(mx);
    float sum = 0.f;
#pragma unroll
    for (int r = 0; r < 4; ++r) {
      t[r] = __builtin_amdgcn_exp2f(t[r] - mx); mv[r] = __builtin_amdgcn_exp2f(mv[r] - mx);
      sum += t[r] + mv[r];
    }
    sum += dppf<DPP_ROR8>(sum); sum += dppf<DPP_ROR4>(sum);
    sum = x16sum(sum);
    const float rs_ = __builtin_amdgcn_rcpf(sum);
    res = (u32x4){pack2n(t[0] * rs_, t[1] * rs_), pack2n(t[2] * rs_, t[3] * rs_), pack2n(mv[0] * rs_, mv[1] * rs_), pack2n(mv[2] * rs_, mv[3] * rs_)};
    PIN_V(res);
  }
  __device__ __forceinline__ void store(const __amdgpu_buffer_rsrc_t rs, unsigned o, unsigned no = 0u) {
    SC1_STORE8S(((u32x2){res[0], res[1]}), s0 | no, o);
    SC1_STORE8S(((u32x2){res[2], res[3]}), s1 | no, o);
  }
};

constexpr int L_DRN = L_VOX + (NSEG % NV) * 5120;
__device__ __forceinline__ void drain_pre(char* lds, int b, int lane) {
  const int gp = (b & 7) * 2 + (b >> 7), m = (b >> 3) & 15;
  TokTask tk; PvTask2 pv; OutTask ot;
  tk.init(2 * gp + (m >> 3), m & 7, lane); pv.init(gp, lane); ot.init(b, lane);
  u32x4* p = (u32x4*)(lds + L_DRN) + lane;
  p[0] = (u32x4){tk.l0, tk.lm, tk.s0, tk.s1}; p[64] = (u32x4){pv.lo, pv.lm, pv.vr, pv.ma}; p[128] = (u32x4){pv.mz, pv.so, ot.t5f, ot.t5o};
  p[192] = __builtin_bit_cast(u32x4, ot.sel);
}
__device__ __forceinline__ void qkv_pre(unsigned (&vo)[2][2], int b, int w, int lane) {
  const int cb = qkv_col(b), c = lane & 15, g = lane >> 4, t0 = w & 1, col0 = (cb % 5) * 128 + w * 32;
  const bool SW = cb < 10;
#pragma unroll
  for (int i = 0; i < 2; ++i)
#pragma unroll
    for (int tt = 0; tt < 2; ++tt) {
      const int tq = tt ^ t0, tk = tq * 16 + c, cc = col0 + i * 16 + (SW ? 4 * g : c), hd = cc / BD, e = cc - hd * BD;
      const int kv = (int)(OFF_KV - OFF_QB) + hd * 64 * KVT;
      vo[i][tt] = (unsigned)(!SW ? kv + 1280 + tq * 672 + ((g >> 1) * 21 + e) * 16 + (g & 1) * 8
                                 : (cb < 5 ? (hd * S_ + tk) * 2 * QST + e * 2 : kv + (e < 16 ? (tk + 32 * (e >> 3)) * 16 + (e & 7) * 2 : 1024 + tk * 8)));
      PIN_V(vo[i][tt]);
    }
}

template <int w>
__device__ __forceinline__ void token_waves(const RecArgs& a, char* lds, const __amdgpu_buffer_rsrc_t rs, unsigned (&vo)[2][2]) {
  const int lane0 = __builtin_amdgcn_workitem_id_x() & 63, b = __builtin_amdgcn_workgroup_id_x(), wid = w + 4; (void)wid;
  const int gp = (b & 7) * 2 + (b >> 7), m = (b >> 3) & 15, h0 = 2 * gp, h = h0 + (m >> 3), j = m & 7;
  float* const misc = (float*)(lds + L_MISC);
  const f32x4 Z4 = {0.f, 0.f, 0.f, 0.f};
  const unsigned lds0 = (unsigned)(uintptr_t)(LAS char*)lds;
  unsigned* const cnt = (unsigned*)(misc + 6);
  const float* const xnrm = (const float*)(a.ws + OFF_XNRM);
  bf16x16 W[2][10];
  auto btile = [&](int i) { return w == 1 ? 0 : (w == 2 ? 1 + i : 3 + i); };
  if (w == 1) code_touch(b, lane0, lds0);
  Place pl;
  if (w == 3) pl.load(a.ws, lane0);
  if (a.layer) {
    if (w != 0) img_fetch<20>(a, lds0, h, j, w - 1, lane0);
  } else {
    if (w == 0) img_fetch<12>(a, lds0, h, j, 4, lane0);
#pragma unroll
    for (int i = 0; i < 10; ++i) x_fetch(x_src(a, 0, b), lds0, 0, 10 * w + i, lane0);
  }
#pragma unroll
  for (int i = 0; i < 2; ++i)
#pragma unroll
    for (int p = 0; p < 10; ++p)
      if (w >= 2 || (w == 1 && i == 1)) {
        const char* wp = a.ws + OFF_WB + ((size_t)(b * 5 + btile(i)) * 20 + 2 * p) * 1024 + lane0 * 16;
        if (FR_PACE && a.layer) asm volatile("s_waitcnt vmcnt(%0)" :: "n"(FR_PACE) : "memory");
        W[i][p] = cat16(*(const bf16x8*)wp, *(const bf16x8*)(wp + 1024));
      }
  f32x4 bn = Z4;
  if (w == 0) {
    bn = *(const f32x4*)(a.ws + OFF_BNRM + lane0 * 16);
    if (!a.layer) asm volatile("s_waitcnt vmcnt(0)" ::: "memory");
  } else asm volatile("s_waitcnt vmcnt(%0)" :: "n"(w == 1 ? 20 : 40) : "memory");
  unsigned ck = 0u;
  if (w == 3) { ck = pl.get(b, lane0); *(unsigned*)(misc + 11) = ck; }
  BAR();
#pragma unroll
  for (int p = 0; p < 10; ++p) {
    if (w < 2) W[0][p] = cat16(wqk_frag(lds, 2 * w, 2 * p, lane0), wqk_frag(lds, 2 * w, 2 * p + 1, lane0));
    if (w == 0) W[1][p] = cat16(wqk_frag(lds, 1, 2 * p, lane0), wqk_frag(lds, 1, 2 * p + 1, lane0));
  }
  float cb2 = 0.f;
  if (w == 0) {
    float n = __builtin_amdgcn_sqrtf(bn[0] + bn[1]) + __builtin_amdgcn_sqrtf(bn[2] + bn[3]);
    n += dppf<DPP_XOR1>(n); n += dppf<DPP_XOR2>(n); n += dppf<DPP_ROR4>(n); n += dppf<DPP_ROR8>(n);
    n = x32mx(x16mx(n));
    cb2 = u2f(__builtin_amdgcn_readfirstlane(f2u(1.15f * n * n)));
  }
  const float cm2 = u2f((f2u(cb2) & 0x7fffffu) | 0x40000000u);
  const int ek = imax((int)((f2u(cb2) >> 23) & 0xffu), 1) - 252;
  if (w >= 2) {
#pragma unroll
    for (int i = 0; i < 4; ++i) *(unsigned*)(lds + L_KX + (w - 2) * 4096 + i * 1024 + lane0 * 16 + 12) = 0u;
  }
  OutTask ot;
  const int zl = lane0 - 16;
  const bool zon = w == 3 && zl >= 0 && (zl < 40 || b < ACC_NB + 17);
  unsigned zo = zon ? zero_base(b, h, j, zl) + (zl < 32 ? sct_slot(ck, 0u) : (zl < 40 ? scm_slot(ck, 0u) : 0u)) : 0x7ffffff0u;
  unsigned zm = zon ? (zl < 32 ? SCT_STEP + 1u : (zl < 40 ? SCM_STEP : (unsigned)ACC_STEP)) : 0u;
  const unsigned zx = ((ck ^ (ck >> 4)) & 7u) * 4096u;
  if (w == 3) { PIN_V(zo); PIN_V(zm); }
  auto st_out = [&](int s, int lane, auto go) {
    if (w < 2) return;
    if (ST_E) lds_spin<L_MISC + 112, 1>(lds0, (unsigned)imax(s, 0)); else go();
    const int sl = s - 2 - LAG4;
    const char* sp = lds + L_STG + 2048 + (w & 1) * 1280;
    u32x4 v0 = *(const u32x4*)(sp + lane * 16), v1 = *(const u32x4*)(sp + 1024 + (lane & 15) * 16);
    const u32x4 z = {0u, 0u, 0u, 0u};
    unsigned oo = (sl < 0 ? 0x7ff00000u : op_slot(sl) + (unsigned)((m * 32 + (w & 1) * 16 + gp) * 1280)) + (unsigned)(lane * 16), za = 0x7ffffff0u;
    if (w == 3) {
      const unsigned zs = (unsigned)(s + 2) % SR;
      za = (zo + (s < 0 ? 1024u : zs) * (zm & ~1u)) ^ ((zm & 1u) * (zs & 4u ? zx : 0u));
#pragma unroll
      for (int i = 0; i < 4; ++i) { v1[i] = lane < 16 ? v1[i] : z[i]; PIN_V(v1[i]); }
    }
    za = lane < 16 ? oo + 1024 : za;
    PIN_V(za); PIN_V(oo); PIN_V(v0); PIN_V(v1);
    __builtin_amdgcn_sched_barrier(0);
    if (ST_E) go();
    SC1_STORE(v0, oo);
    SC1_STORE(v1, za);
    __builtin_amdgcn_sched_barrier(0);
    if (w == 2) ot.store(a, s - 2 - LAG5, b, lane);
    else {
      const unsigned xo_ = (unsigned)((s - 8) * 1024 + lane * 16);
      if (__builtin_expect((unsigned)(s - 8) < 20u, 0)) SC1_STORE(z, xo_ < X_BYTES / 256 ? (unsigned)(OFF_X + b * (X_BYTES / 256)) + xo_ : 0x7ffffff0u);
    }
  };
  STAMP_DECL(); ARRIVE_DECL();
  auto mma_seg = [&](int seg, int sv, int lane, auto pause) {
    const int c = lane & 15, g = lane >> 4;
    const char* xp = lds + L_XN + (seg & 1) * 40960 + lane * 16;
    f32x4 acc[2][2] = {{Z4, Z4}, {Z4, Z4}};
    bf16x8 x[4][2];
#pragma unroll
    for (int p = 0; p < 3; ++p) { x[p][0] = *(const bf16x8*)(xp + p * 1024); x[p][1] = *(const bf16x8*)(xp + (20 + p) * 1024); }
    unsigned eo[2];
#pragma unroll
    for (int ks = 0; ks < 20; ++ks) {
      if (ks == (w == 0 ? KP0 : (w == 1 ? KP1 : KP2))) { pause(); __builtin_amdgcn_sched_barrier(0); }
      if (ks == 17) {
#pragma unroll
        for (int i = 0; i < 2; ++i) {
          eo[i] = (unsigned)(w == 0 ? (i ? L_KX : L_QX) + (seg % NKQ) * 2048 + lane * 16
                                    : (w == 1 && i == 0 ? ((g >> 1) ? L_KX : L_QX) + (seg % NKQ) * 2048 + (2 * (g & 1) * 16 + c) * 16 + 8 : L_VOX + sv * 5120 + btile(i) * 1024 + lane * 16));
          if (i == 0 || w == 1) PIN_V(eo[i]);
        }
        if (w == 0) eo[1] = eo[0] + (L_KX - L_QX);
        if (w >= 2) eo[1] = eo[0] + 1024;
      }
      __builtin_amdgcn_sched_barrier(0);
#pragma unroll
      for (int q = 0; q < 4; ++q) {
        const int tt = 1 - (q >> 1), i = q & 1;
        const bool isqk = w == 0 || (w == 1 && i == 0);
        const bf16x8 wf = half8(W[i][ks >> 1], ks & 1);
        constexpr int q0 = UNIT && w < 2;
        if (q < q0) continue;
        acc[i][tt] = isqk ? mfma32(wf, x[ks & 3][tt], acc[i][tt]) : mfma32(x[ks & 3][tt], wf, acc[i][tt]);
        __builtin_amdgcn_sched_barrier(0);
        if (q - q0 < 2 && ks + 3 < 20) x[(ks + 3) & 3][q - q0] = *(const bf16x8*)(xp + ((q - q0) * 20 + ks + 3) * 1024);
        __builtin_amdgcn_sched_barrier(0);
      }
    }
#pragma unroll
    for (int i = 0; i < 2; ++i) {
      if (w + i == 1) { const float ks_ = (w == 0 || (g >> 1)) ? KEXP : 1.0f; acc[i][0] *= ks_; acc[i][1] *= ks_; }
      const u32x4 v = {pack2(acc[i][0][0], acc[i][0][1]), pack2(acc[i][0][2], acc[i][0][3]), pack2(acc[i][1][0], acc[i][1][1]), pack2(acc[i][1][2], acc[i][1][3])};
      char* p = lds + eo[i];
      if (w == 0) { *(u32x2*)(p) = (u32x2){v[0], v[1]}; if (i || !UNIT) *(u32x2*)(p + 1024) = (u32x2){v[2], v[3]}; }
      else if (w == 1 && i == 0) { *(unsigned*)(p) = v[0]; *(unsigned*)(p + 256) = v[1]; if (!UNIT) { *(unsigned*)(p + 1024) = v[2]; *(unsigned*)(p + 1280) = v[3]; } }
      else *(u32x4*)p = v;
    }
  };
  unsigned vp, mp, ip;
  int sidx, ei = 127;
  float inv;
  f32x4 acc[2];
  auto vo_pre = [&](int s, int sv, int lane) {
    vp = (unsigned)(w == 0 ? L_MISC + (s & 3) * 4 : L_VOM + sv * 1280 + (btile(0) * 32 + (w == 1 ? lane & 31 : lane)) * 8);
    mp = (unsigned)(L_BM + lane * 16);
    PIN_V(vp); PIN_V(mp);
    if (w == 0) { ip = (unsigned)(L_MISC + 16 + ((s + 1) & 1) * 4); PIN_V(ip); }
    else {
      inv = misc[4 + (s & 1)];
      sidx = (lane & 8) ? 0xEEEE : 0x4444;
      acc[0] = acc[1] = Z4;
      PIN_V(sidx); PIN_V(acc[1]);
      if (w >= 2) { PIN_V(acc[0]); PIN_V(inv); }
    }
    __builtin_amdgcn_sched_barrier(0);
  };
  auto vo_mem = [&](int s, auto last) {
    __builtin_amdgcn_s_setprio(2);
    asm volatile(".rept %0\n\ts_nop 3\n\t.endr" :: "n"(w == 0 ? VO_A0 : (w == 1 ? VO_A1 : VO_A2)) : "memory");
    float xmax = 0.f;
    if (w == 0) xmax = *(const float*)(lds + vp);
    constexpr int NX = w == 2 ? VO_N2 : (w == 3 ? VO_N3 : 10);
    bf16x8 x[10];
#pragma unroll
    for (int p = 0; p < NX; ++p) x[p] = *(const bf16x8*)(lds + mp + p * 1024);
    __builtin_amdgcn_sched_barrier(0);
    if (w < 2) asm volatile(".rept %0\n\ts_nop 3\n\t.endr" :: "n"(w == 0 ? VO_B0 : VO_B1) : "memory");
    if (VO_W >= 0) asm volatile("s_waitcnt lgkmcnt(%0)" :: "n"(VO_W < 0 ? 0 : VO_W) : "memory");
    STAMP(5);
    __builtin_amdgcn_sched_barrier(0);
    int ex = 0;
#pragma unroll
    for (int p = 0; p < 10; ++p) {
      if (w == 0) acc[0] = mfma32(x[p], x[p], p ? acc[0] : Z4);
      if (w == 0 && p == 9) { ex = imax((int)((__builtin_amdgcn_readfirstlane(f2u(__builtin_amdgcn_sqrtf(cb2 * xmax))) >> 23) & 0xffu) + 1, 32); PIN_S(ex); }
#pragma unroll
      for (int i = (w == 1); i < 2 && w != 0; ++i) acc[i] = smfma64(x[p], W[i][p], acc[i], sidx);
      __builtin_amdgcn_sched_barrier(0);
      if (p + NX < 10) x[p + NX] = *(const bf16x8*)(lds + mp + (p + NX) * 1024);
      __builtin_amdgcn_sched_barrier(0);
    }
    STAMP(7);
    if (w != 0) {
      float v[4];
#pragma unroll
      for (int r = 0; r < 4; ++r) {
        const auto sw = __builtin_amdgcn_permlane32_swap(f2u(acc[w == 1][r]), f2u(acc[1][r]), false, false);
        v[r] = (u2f(sw[0]) + u2f(sw[1])) * inv;
      }
      *(u32x2*)(lds + vp) = (u32x2){pack2(v[0], v[1]), pack2(v[2], v[3])};
    } else {
      const int t = (int)f2u(fmx(fmx(acc[0][0], acc[0][1]), fmx(acc[0][2], acc[0][3])) * cm2);
      const int et = wave_imax(t) >> 23, em = ((et + ek) >> 1) + ei;
      int e = imax(et == 255 ? 256 : em, ex);
      PIN_S(e);
      e = imin(e, 256);
      ei = e - 31;
      *(unsigned*)(lds + ip) = (unsigned)ei << 23;
      const u32x3 sr = {(unsigned)(285 - e) << 23, (unsigned)(e - 87) < 79u ? REC_RNG << 23 : 0u, (unsigned)(s + 1)};
      asm volatile("ds_write_b96 %0, %1 offset:%2" :: "v"(lds0), "v"(sr), "n"(L_MISC + 32) : "memory");
    }
    if (last == 0 && w >= 2) asm volatile("s_waitcnt vmcnt(%0)" :: "n"(w == SCH_W ? 2 : 0) : "memory");
    if (w != 0) lds_inc<L_MISC + 28>(lds0);
    STAMP(8);
    if (last) __builtin_amdgcn_s_setprio(0);
    if (last && w == 2) { VM_DRAIN(); lds_inc<L_MISC + 28>(lds0); }
  };
  unsigned* const cntq = (unsigned*)(misc + 12);
  unsigned* const cntt = (unsigned*)(misc + 26 + (w & 1));
  unsigned* const cntp = (unsigned*)(misc + 23);
  unsigned* const cntx2 = (unsigned*)(misc + 28);
  float xn4 = 0.f;
  auto wpf = [&](int s, int lane) {
    constexpr int PER = 81920 / QR / WPF_N;
    static_assert(PER % WPF_S == 0 && PER / WPF_S <= 64, "prefetch");
    touch(a.ws + OFF_WQKVBT + (size_t)imin(qkv_col(b), 14) * 163840 + (qkv_row(b) % (2 * QR)) * (81920 / QR), (s - WPF0) * PER + imin(lane, PER / WPF_S - 1) * WPF_S, lds0);
  };
  auto t5_get = [&](int s, int lane) {
    ot.load(a, rs, s - 1 - LAG5, b);
    xn4 = u2f(__hip_atomic_load((const GAS unsigned*)xnrm + (imax(s + 1, 0) * 32 + (lane & 31)) * 4, RLX_AGENT));
    __builtin_amdgcn_sched_barrier(0);
  };
  Sched sch;
  auto late = [&](int s, int lane) {
    if (w == SCH_W && SCH_LATE && __builtin_expect(s >= 0, 1)) *(u32x2*)(misc + 24) = (u32x2){sch.run(a, rs, s, b, lane), (unsigned)(s + 1)};
    if ((w == 2 && F6) || (w == 0 && F4) || (w == 1 && F5) || (w == 3 && F7)) { lds_spin<L_MISC + 48, 1>(lds0, 2u * (unsigned)imax(s + 1, 0)); if (FB_SL && w != 2) __builtin_amdgcn_s_sleep(FB_SL); }
    if (w >= 2) { x_fetch_n<w == 2 ? F6 : F7>(x_src(a, s + 3, b), lds0, s + 3, F4 + F5 + 2 * F67 + (w == 3 ? F6 : 0), lane * 16); return; }
    unsigned sd = 0u;
    if ((w == 0 && XPF == 3) || XPF == 4 || (w == 1 && XPF == 5)) stouch<10>(a.ws + OFF_XN + (size_t)imin(s + 4, NSEG - 1) * (s < 1 ? 0 : 40960) + (b >> 3) * 1280 + w * 64, sd);
    x_fetch_n<w == 0 ? F4 : F5>(x_src(a, s + 3, b), lds0, s + 3, w == 0 ? 0 : F4, lane * 16);
    if (WPF_W == 0 && w == 0 && __builtin_expect(s >= WPF0 && s < WPF0 + WPF_N, 0)) wpf(s, lane);
    if (w == 1 && __builtin_expect(s == CPF_S, 0)) code_touch(b, lane, lds0);
    lds_spin<L_MISC + 104 + 4 * (w & 1), PS_SL>(lds0, (unsigned)imax(s + 1, 0));
    asm volatile("" :: "s"(sd));
  };
  auto xn_put = [&](int s) {
    float xn2 = xn4;
    xn2 = fmx(xn2, dppf<DPP_XOR1>(xn2)); xn2 = fmx(xn2, dppf<DPP_XOR2>(xn2));
    xn2 = fmx(xn2, dppf<DPP_ROR4>(xn2)); xn2 = fmx(xn2, dppf<DPP_ROR8>(xn2));
    misc[s & 3] = x16mx(xn2);
  };
  if (w == 2) { ot.init(b, lane0); ot.out = (u32x2){0u, 0u}; ot.xo = ot.out; ot.y[0] = ot.y[1] = ot.y[2] = ot.y[3] = (u32x4){0u, 0u, 0u, 0u}; }
  VM_DRAIN();
  lds_arrive(cnt, lane0);
  lds_wait(cnt, 8u);
  sch.tsv = sch.rec = (u32x4){0u, 0u, 0u, 0u};
  sch.side = __builtin_amdgcn_readfirstlane(*(const unsigned*)(misc + 11)) & 8192u;
  sch.o = 0u;
  int sv = NV - 1;
  CODE_PAD((PADT >> (4 * w)) & 15);
#pragma unroll 1
  for (int s = -1; s < NSEG - 1; ++s) {
    const int sv1 = sv == NV - 1 ? 0 : sv + 1;
    FRESH_LANE();
    STAMP(0);
    mma_seg(s + 1, sv1, lane, [&]() { STAMP(1); late(s - 1, lane); if (w == SCH_W) sch.pre(s); STAMP(9); });
    STAMP(2);
    __builtin_amdgcn_sched_barrier(0);
    VM_DRAIN();
    if (w == 2) { ot.run(lane); xn_put(s); }
    STAMP(10);
    st_out(s, lane, [&]() { lds_spin<L_MISC + 92, W1_SL>(lds0, 4u * (unsigned)imax(s, 0)); });
    if (w == 2) t5_get(s, lane);
    __builtin_amdgcn_sched_barrier(0);
    if (w >= 2) vo_pre(s, sv, lane);
    STAMP(3);
    BAR();
    STAMP(4);
    if (__builtin_expect(s >= 0, 1)) {
      if (w == SCH_W) { sch.load(rs, lane0); __builtin_amdgcn_sched_barrier(0); }
      if (w < 2) vo_pre(s, sv, lane);
      vo_mem(s, std::integral_constant<int, 0>{});
      STAMP(6);
      if (w == SCH_W && !SCH_LATE) {
        __builtin_amdgcn_s_setprio(SCH_P);
        *(u32x2*)(misc + 24) = (u32x2){sch.run(a, rs, s, b, lane0), (unsigned)(s + 1)};
      }
      STAMP(11);
    } else BAR();
    __builtin_amdgcn_s_setprio(MMA_P);
    __builtin_amdgcn_sched_barrier(0);
    sv = sv1;
  }
  __builtin_amdgcn_s_setprio(0);
  STAMP_OUT();
  late(NSEG - 2, lane0);
  if (w == 2) ot.run(lane0);
  { FRESH_LANE(); if (w == 0) drain_pre(lds, b, lane); qkv_pre(vo, b, w, lane); }
  st_out(NSEG - 1, lane0, [&]() { lds_spin<L_MISC + 92, 1>(lds0, 4u * (NSEG - 1)); });
  if (w != 0) vo_pre(NSEG - 1, (NSEG - 1) % NV, lane0);
  BAR();
  if (w != 0) vo_mem(NSEG - 1, std::integral_constant<int, 1>{});
}

__device__ __forceinline__ void qkv_tail(const RecArgs& a, char* lds, const int w, const unsigned (&vo)[2][2]) {
  const int lane0 = __builtin_amdgcn_workitem_id_x() & 63, b = __builtin_amdgcn_workgroup_id_x();
  const int r = qkv_row(b), cb = qkv_col(b);
  if (cb >= 15) return;
  const unsigned lds0 = (unsigned)(uintptr_t)(LAS char*)lds;
  unsigned* const stage = (unsigned*)(lds + L_MISC) + 17;
  unsigned* const cnty = stage + 1;
  const f32x4 Z4 = {0.f, 0.f, 0.f, 0.f};
  auto fetch = [&](int i) {
    x_fetch_n<10>(x_src(a, NSEG + i, b), lds0, i, 10 * w, lane0 * 16);
  };
  unsigned want = 0;
  auto sync = [&]() { VM_DRAIN(); want += 4u; lds_inc<L_MISC + 72>(lds0); while (__hip_atomic_load(cnty, __ATOMIC_RELAXED, __HIP_MEMORY_SCOPE_WORKGROUP) < want) { } asm volatile("" ::: "memory"); };
  LOGQ(0);
  bf16x8 W[2][20];
  const __amdgpu_buffer_rsrc_t rw = __builtin_amdgcn_make_buffer_rsrc(a.ws + OFF_WQKVBT + (size_t)(cb * 8 + 2 * w) * 20480, 0, 40960, 0x00020000);
  auto w_get = [&](int ks) {
#pragma unroll
    for (int i = 0; i < 2; ++i) W[i][ks] = as8(__builtin_amdgcn_raw_buffer_load_b128(rw, lane0 * 16 + (ks & 3) * 1024, (i * 20 + (ks & ~3)) * 1024, 0));
    __builtin_amdgcn_sched_barrier(0);
  };
#pragma unroll
  for (int ks = 0; ks < QW; ++ks) w_get(ks);
  bf16x8 ones = mk8(0x3f803f80u, 0x3f803f80u, 0x3f803f80u, 0x3f803f80u);
  PIN_V(ones);
  const __amdgpu_buffer_rsrc_t ro = __builtin_amdgcn_make_buffer_rsrc(a.ws + OFF_QB, 0, (int)(OFF_OB - OFF_QB), 0x00020000);
  const int t0 = w & 1;
  auto body = [&](auto SW, auto SQ) {
    const float qs = cb < 5 ? 0.22360679774997896f * 1.4426950408889634f : 1.0f;
    f32x4 cs[2] = {Z4, Z4};
    LOGQ(1);
    f32x4 acc[2][2], st;
    auto mma = [&](int it, auto FIRST) {
      const char* xp0 = lds + L_XN + (it & 1) * 40960 + t0 * 20480 + lane0 * 16;
      const char* xp1 = lds + L_XN + (it & 1) * 40960 + (1 - t0) * 20480 + lane0 * 16;
      acc[0][0] = acc[0][1] = acc[1][0] = acc[1][1] = st = Z4;
      bf16x8 x[4][2];
#pragma unroll
      for (int p = 0; p < 3; ++p) { x[p][0] = *(const bf16x8*)(xp0 + p * 1024); x[p][1] = *(const bf16x8*)(xp1 + p * 1024); }
      __builtin_amdgcn_sched_barrier(0);
#pragma unroll
      for (int ks = 0; ks < 20; ++ks) {
        if (FIRST && ks + QW < 20) w_get(ks + QW);
#pragma unroll
        for (int q = 0; q < 4; ++q) {
          const int tt = 1 - (q >> 1), i = 1 - (q & 1);
          acc[i][tt] = SW ? mfma32(W[i][ks], x[ks & 3][tt], acc[i][tt]) : mfma32(x[ks & 3][tt], W[i][ks], acc[i][tt]);
          __builtin_amdgcn_sched_barrier(0);
          if (q < 2 && ks + 3 < 20) x[(ks + 3) & 3][q] = *(const bf16x8*)((q ? xp1 : xp0) + (ks + 3) * 1024);
          __builtin_amdgcn_sched_barrier(0);
        }
        st = mfma32(SQ ? x[ks & 3][0] : ones, x[ks & 3][0], st);
        if (FIRST) {
#pragma unroll
          for (int i = 0; i < 2; ++i) cs[i] = SW ? mfma32(W[i][ks], ones, cs[i]) : mfma32(ones, W[i][ks], cs[i]);
        }
        __builtin_amdgcn_sched_barrier(0);
      }
    };
    mma(0, std::integral_constant<bool, true>{});
#pragma unroll 1
    for (int it = 0; it < 4; ++it) {
      if (it) mma(it, std::integral_constant<bool, false>{});
      LOGQ(2 + 3 * it);
      FRESH_LANE();
      float* const sp = (float*)(lds + L_ST) + (it & 1) * 64;
      if (SQ) { if ((c >> 2) == g) sp[32 + t0 * 16 + c] = (c & 3) == 0 ? st[0] : ((c & 3) == 1 ? st[1] : ((c & 3) == 2 ? st[2] : st[3])); }
      else if (g == 0) sp[t0 * 16 + c] = st[0];
      sync();
      LOGQ(3 + 3 * it);
      if (it == 0) fetch(2);
      const bool late = r + 48 >= 55;
      if (it == 1 && !late) fetch(3);
      const unsigned so = (unsigned)((r + 16 * it) * (cb < 5 ? 64 * QST : KVT));
      auto stat = [&](float sm, float sq, float& mean, float& rstd) {
        mean = sm * (1.0f / DM);
        rstd = __builtin_amdgcn_rsqf(__builtin_fmaxf(sq * (1.0f / DM) - mean * mean, 0.f) + 1e-5f);
      };
#pragma unroll
      for (int tt = 0; tt < 2; ++tt) {
        const int tq = tt ^ t0;
        float mean[4], rstd[4];
        if (SW) {
          stat(sp[tq * 16 + c], sp[32 + tq * 16 + c], mean[0], rstd[0]);
          rstd[0] *= qs;
        } else {
          const f32x4 sm = *(const f32x4*)(sp + tq * 16 + 4 * g), sq = *(const f32x4*)(sp + 32 + tq * 16 + 4 * g);
#pragma unroll
          for (int k = 0; k < 4; ++k) stat(sm[k], sq[k], mean[k], rstd[k]);
        }
#pragma unroll
        for (int i = 0; i < 2; ++i) {
          float v[4];
#pragma unroll
          for (int k = 0; k < 4; ++k) v[k] = (acc[i][tt][k] - mean[SW ? 0 : k] * cs[i][SW ? k : 0]) * rstd[SW ? 0 : k];
          __builtin_amdgcn_raw_buffer_store_b64((u32x2){pack2(v[0], v[1]), pack2(v[2], v[3])}, ro, vo[i][tt], so, 0);
        }
      }
      LOGQ(4 + 3 * it);
      if (it == 2 && late) lds_spin<L_MISC + 68, 0>(lds0, 5u);
    }
  };
  typedef std::integral_constant<bool, false> F_; typedef std::integral_constant<bool, true> T_;
  if (cb < 10) { if (w >> 1) body(T_{}, T_{}); else body(T_{}, F_{}); }
  else { if (w >> 1) body(F_{}, T_{}); else body(F_{}, F_{}); }
}

__device__ __forceinline__ void t2_mem(const bf16x8 fr, const bf16x8 q0, const bf16x8 q1, const __amdgpu_buffer_rsrc_t rs, unsigned o, int h, int j, float inv, int lane) {
  const f32x4 Z4 = {0.f, 0.f, 0.f, 0.f};
  const f32x4 d0 = mfma32(fr, q0, Z4) * inv, d1 = mfma32(fr, q1, Z4) * inv;
  const unsigned a0 = pack2n(d0[0], d0[1]), a1 = pack2n(d0[2], d0[3]), b0 = pack2n(d1[0], d1[1]), b1 = pack2n(d1[2], d1[3]);
  const auto r0 = __builtin_amdgcn_permlane32_swap(a0, b0, false, false), r1 = __builtin_amdgcn_permlane32_swap(a1, b1, false, false);
  SC1_STORE8S(((u32x2){r0[1], r1[1]}), ts_mem(h, j) + (lane >> 5) * 256 + (lane & 12) * 16 + (lane & 16) * 2 + (lane & 3) * 8, o);
}

template <int w>
__device__ __forceinline__ void rec_waves(const RecArgs& a, char* lds, __amdgpu_buffer_rsrc_t rs, unsigned& tlast) {
  PIN_S(rs);
  const int lane = __builtin_amdgcn_workitem_id_x() & 63, b = __builtin_amdgcn_workgroup_id_x(), c = lane & 15, g = lane >> 4, wid = w; (void)wid;
  const int gp = (b & 7) * 2 + (b >> 7), m = (b >> 3) & 15, h0 = 2 * gp, h = h0 + (m >> 3), j = m & 7;
  float* const misc = (float*)(lds + L_MISC);
  const f32x4 Z4 = {0.f, 0.f, 0.f, 0.f};
  LOGP(0);
  __builtin_amdgcn_s_setprio(3);
  const unsigned lds0 = (unsigned)(uintptr_t)(LAS char*)lds;
  if (!a.layer) img_fetch<12>(a, lds0, h, j, w, lane);
  constexpr int NC = w < 2 ? 2 : 3;
  const int far = ((b >> 2) & 1) ^ 1;
  auto chunk = [&](int f) { return f == 0 ? 4 * far + w : (w == 2 && f == 1 ? 8 + far : (w == 3 && f == 2 ? 9 - far : 4 - 4 * far + w)); };
  bf16x16 Wqk[3][NC];
  u32x4 bm0[NC][2];
#pragma unroll
  for (int f = 0; f < 2 * NC; ++f) bm0[f >> 1][f & 1] = *(const u32x4*)(a.mem0 + (size_t)(c & 7) * DM + 64 * chunk(f >> 1) + 16 * g + 8 * (f & 1));
  if (!a.layer) asm volatile("s_waitcnt vmcnt(0)" ::: "memory");
  BAR();
  LOGP(9);
  const unsigned ck = __builtin_amdgcn_readfirstlane(*(const unsigned*)(misc + 11));
  const unsigned flip = (ck ^ (unsigned)(b << 11)) & 8192u;
#pragma unroll
  for (int t = 0; t < 3; ++t)
#pragma unroll
    for (int f = 0; f < NC; ++f) Wqk[t][f] = cat16(wqk_frag(lds, t, 2 * chunk(f), lane), wqk_frag(lds, t, 2 * chunk(f) + 1, lane));
  bf16x16 We[7];
  auto echunk = [&](int f) { return f < 6 ? (f / 3) * 4 + f % 3 + (f % 3 >= w) : (f == 6 ? 17 - chunk(w - 1) : chunk(f - 7)); };
  if (UNIT && w >= 2) {
#pragma unroll
    for (int f = 0; f < 7; ++f) We[f] = cat16(wqk_frag(lds, 2 * w - 4, 2 * echunk(f), lane), wqk_frag(lds, 2 * w - 4, 2 * echunk(f) + 1, lane));
  }
  auto unit = [&](int seg) {
    if (!UNIT || w < 2) return;
    const char* xp = lds + L_XN + (seg & 1) * 40960 + 20480 + lane * 16;
    bf16x8 x[20];
    f32x4 d = Z4;
#pragma unroll
    for (int k = 0; k < 26; ++k) {
      if (k >= 6) d = mfma32(half8(k - 6 < 14 ? We[(k - 6) >> 1] : Wqk[w < 2 ? 0 : 2 * w - 4][k - 6 < 14 ? 0 : ((k - 6) >> 1) - 7], k & 1), x[k - 6], d);
      __builtin_amdgcn_sched_barrier(0);
      if (k < 20) x[k] = *(const bf16x8*)(xp + echunk(k >> 1) * 2048 + (k & 1) * 1024);
      __builtin_amdgcn_sched_barrier(0);
    }
    if (w == 2) *(u32x2*)(lds + L_QX + (seg % NKQ) * 2048 + 1024 + lane * 16) = (u32x2){pack2(d[0], d[1]), pack2(d[2], d[3])};
    else {
      d *= (g >> 1) ? KEXP : 1.0f;
      char* p = lds + ((g >> 1) ? L_KX : L_QX) + (seg % NKQ) * 2048 + 1024 + (2 * (g & 1) * 16 + c) * 16 + 8;
      *(unsigned*)p = pack2(d[0], d[1]); *(unsigned*)(p + 256) = pack2(d[2], d[3]);
    }
    __builtin_amdgcn_sched_barrier(0);
  };
  if (!a.layer) lds_arrive((unsigned*)(misc + 6), lane);
  else {
    if (w == 0) {
      for (unsigned spins = 0; spins < SPIN_CAP; ++spins) {
        if (__builtin_amdgcn_raw_buffer_load_b32(rs, (b & 7) * 256, (unsigned)OFF_RDY, 16) >= 16u) break;
        __builtin_amdgcn_s_sleep(4);
        asm volatile("" ::: "memory");
      }
      lds_inc<L_MISC + 84>(lds0);
    }
    lds_wait((unsigned*)(misc + 21), 2u);
#pragma unroll
    for (int f = 0; f < 2 * NC; ++f) PIN_V(bm0[f >> 1][f & 1]);
#pragma unroll
    for (int i = 0; i < 10; ++i) x_fetch(x_src(a, 0, b), lds0, 0, 10 * w + i, lane);
  }
  const bf16x8 SelLo = sel_frag(c, 0), SelHi = sel_frag(c, 8);
  bf16x8 ONES = mk8(0x3f803f80u, 0x3f803f80u, 0x3f803f80u, 0x3f803f80u);
  if (w < 2) PIN_V(ONES);
  bool dead = false;
  float inv = 1.0f;
  unsigned* const cnt2 = (unsigned*)(misc + 7);
  unsigned l2off[2 * NC];
#pragma unroll
  for (int f = 0; f < 2 * NC; ++f) { l2off[f] = acc_off(16 * chunk(f >> 1) + 4 * g + 2 * (f & 1) + (c >> 3), c & 7) ^ flip; PIN_V(l2off[f]); }
  int sidx = (c >> 3) ? 0xEEEE : 0x4444;
  PIN_V(sidx);
  const unsigned p1off = (unsigned)((w == 0 ? (h & 2) * 1024 + j * 256 + (h & 1) * 64 + (g & 1) * 8 : (h & 3) * 32768 + j * 512 + (g >> 1) * 256 + (g & 1) * 64) + (c & 4) * 32 + (c & 3) * 16);
  unsigned p1v = (c < 8) && (w != 0 || g >= 2) ? p1off : 0x7ffffff0u, p1cur = 0x7ffffff0u;
  PIN_V(p1v); PIN_V(p1cur);
  const int hd = (c >> 2) & 1, mr = hd ? 3 - (c & 3) : (c & 3);
  const unsigned l1off = (unsigned)(((h0 + hd) & 3) * 32768 + g * 512 + (w & 1) * 128 + (c >> 3) * 64 + mr * 16);
  const unsigned l1offm = (unsigned)((h0 & 2) * 1024 + (g + 4 * (c >> 3)) * 256 + (w & 1) * 128 + hd * 64 + mr * 16);
  const int plane = (lane & ~6) | ((lane & 2) << 1) | ((lane & 4) >> 1);
  unsigned acco = c < 8 ? (acc_off(64 * far + m * 4 + g, 4 * (w & 1) + mr) + (unsigned)(hd * 8)) ^ flip : 0x7ffffff0u;
  unsigned acco2 = c >= 8 ? (acc_off(64 - 64 * far + m * 4 + g, 4 * (w & 1) + mr) + (unsigned)(hd * 8)) ^ flip
                          : (c < 4 ? (acc_off(128 + m * 2 + (g & 1), 4 * (w & 1) + mr) + (unsigned)((g >> 1) * 8)) ^ flip : 0x7ffffff0u);
  if (w < 2) { PIN_V(acco); PIN_V(acco2); }
  unsigned bmo[NC];
#pragma unroll
  for (int f = 0; f < NC; ++f) { bmo[f] = (unsigned)(chunk(f) * 1024 + lane * 16); PIN_V(bmo[f]); }
  bf16x8 bm[NC];
  auto bm_put = [&](int f) { *(bf16x8*)(lds + L_BM + bmo[f]) = bm[f]; __builtin_amdgcn_sched_barrier(0); };
  f32x4 acc[3] = {Z4, Z4, Z4};
  const int rt2 = ((c >> 3) * 8 + 4 * (g & 1)) * 28 + 16 + (c & 7);
  unsigned redo[3] = {(unsigned)(4 * g * 28 + c), (unsigned)(g < 2 ? (4 * g + 2) * 28 + c : rt2), (unsigned)(g < 2 ? (4 * g + 10) * 28 + c : rt2 + 56)};
#pragma unroll
  for (int k = 0; k < 3; ++k) { redo[k] = L_RED1 + w * 1792 + redo[k] * 4; PIN_V(redo[k]); }
  auto red_put = [&]() {
    auto hs = [&](float x, float y) { const auto sw = __builtin_amdgcn_permlane32_swap(f2u(x), f2u(y), false, false); return u2f(sw[0]) + u2f(sw[1]); };
#pragma unroll
    for (int k = 0; k < 3; ++k) {
#pragma unroll
      for (int i = 0; i < 2; ++i) *(float*)(lds + redo[k] + i * 112) = k == 0 ? hs(acc[0][i], acc[1][i]) : hs(acc[k - 1][2 + i], acc[2][2 * k - 2 + i]);
      __builtin_amdgcn_sched_barrier(0);
    }
  };
#pragma unroll
  for (int f = 0; f < NC; ++f) {
    const u32x4 u = bm0[f][0], v = bm0[f][1];
    bm[f] = c < 8 ? mk8(u[0], u[2], v[0], v[2]) : mk8(u[1], u[3], v[1], v[3]);
    bm_put(f);
#pragma unroll
    for (int t = 0; t < 3; ++t) acc[t] = smfma64(bm[f], Wqk[t][f], acc[t], sidx);
  }
  red_put();
  VM_DRAIN();
  if (a.layer) lds_arrive((unsigned*)(misc + 6), lane);
  lds_wait((unsigned*)(misc + 6), 8u);
#pragma unroll
  for (int i = 0; i < 10; ++i) x_fetch(x_src(a, 1, b), lds0, 1, 10 * w + i, lane);
  LOGP(1);
  unit(0);
  BAR();
  STAMP_DECL(); ARRIVE_DECL();
  bf16x8 fr, qx[2];
  auto kq_get = [&](int seg) {
    const char* p = lds + (w == 1 ? L_KX : L_QX) + (seg % NKQ) * 2048 + lane * 16;
    if (w == 1 || w == 2) { qx[0] = *(const bf16x8*)p; qx[1] = *(const bf16x8*)(p + 1024); }
  };
  unsigned qo4 = (unsigned)(L_RED1 + (w == 1 ? (c & 7) * 112 + (c >> 3) * 3584 : c * 112) + g * 16), qo2 = qo4 + 64u - (unsigned)(g * 8), qo2b = qo2 + 3584u;
  if (w < 3) { PIN_V(qo4); PIN_V(qo2); if (w != 1) PIN_V(qo2b); }
  auto pub1 = [&](unsigned off, unsigned o1) {
    if (w == 0) fr = qk_frag(lds + qo4, lds + qo2, lds + qo2b);
    else {
      const f32x4 a0 = *(const f32x4*)(lds + qo4), a1 = *(const f32x4*)(lds + qo4 + 1792);
      const f32x2 b0 = *(const f32x2*)(lds + qo2), b1 = *(const f32x2*)(lds + qo2 + 1792);
      float v[6];
#pragma unroll
      for (int i = 0; i < 6; ++i) v[i] = i < 4 ? a0[i] + a1[i] : b0[i - 4] + b1[i - 4];
#pragma unroll
      for (int i = 0; i < 6; ++i) v[i] += dppf<DPP_ROR8>(v[i]);
      fr = mk8(pack2(v[0], v[1]), pack2(v[2], v[3]), pack2(v[4], v[5]), 0u);
    }
    const f32x4 sc = mfma32(w == 0 ? fr : qx[0], fr, Z4);
    if (w == 0) SC1_STORE8S(((u32x2){pack2n(sc[0], sc[1]), pack2n(sc[2], sc[3])}), off, o1);
    else {
      const f32x4 s2 = mfma32(qx[1], fr, Z4);
      __builtin_amdgcn_raw_buffer_store_b128((u32x4){pack2n(sc[0], sc[1]), pack2n(sc[2], sc[3]), pack2n(s2[0], s2[1]), pack2n(s2[2], s2[3])}, rs, off, o1, 16);
    }
  };
  VM_DRAIN();
  kq_get(0);
  unsigned o1 = sct_slot(ck, 0u), o1m = scm_slot(ck, 0u);
  unsigned t2o = ts_tok(h, j) + (unsigned)((c >> 2) * 256 + g * 64 + (c & 3) * 16);
  TokScores t2;
  if (w == 3) { PIN_V(t2o); t2.get(lds, 0, lane); t2.run(0); t2.run(1); t2.store(rs, t2o, ts_slot(ck, 0)); }
  if (w < 2) pub1(p1v, w == 0 ? o1m : o1);
  const unsigned tst = now32() | 1u;
  if (w == 3 && ST_X) __builtin_amdgcn_raw_buffer_store_b32(tst, rs, lane < 2 ? ts_piece(b >> 2) - ACC_TS + (b & 3) * 4 + lane * 8192 : 0x7ffffff0, (unsigned)(OFF_ACC + (SR - 1) * ACC_STEP) + ACC_TS, 16);
  LOGP(2);
  BAR();
  LOGP(3);
#pragma unroll
  for (int i = 0; i < 10; ++i) x_fetch(x_src(a, 2, b), lds0, 2, 10 * w + i, lane);
  unit(1);
  VM_DRAIN();
  unsigned o2 = (unsigned)OFF_ACC, o2p = 0u, tw = 0u;
  asm volatile("" : "+s"(o2p));
  unsigned tc0 = 0u, tstar = 0u;
  int dmin = 0, dmax = 0;
  int e1 = 100000;
  unsigned offs = 100u;
  TokTask tk;
  PvTask2 pv2;
  if (w == 3) pv2.init(gp, lane);
  if (w == 2) tk.init(h, j, lane);
  unsigned o4 = 0u;
  unsigned o3, o3p;
  u32x16 P = {0u, 0u, 0u, 0u, 0u, 0u, 0u, 0u, 0u, 0u, 0u, 0u, 0u, 0u, 0u, 0u}, V = P;
  u32x8 V8 = {0u, 0u, 0u, 0u, 0u, 0u, 0u, 0u};
  if (w < 2) asm volatile("" : "+{v[208:223]}"(P), "+{v[184:191]}"(V8), "+{v[192:207]}"(V));
  unsigned tso = lane < 2 ? ts_piece(b >> 2) + (unsigned)((b & 3) * 4 + lane * 8192) : 0x7ffffff0u;
  if (w < 2) PIN_V(tso);
  float minf = g >= 2 ? -INFINITY : 0.f;
  if (w < 2) PIN_V(minf);
  int sv = NV - 1;
  bf16x8 vox[5];
  unsigned vma[3], need2;
  u32x4 slo, shi;
  float k1;
  const u16* xsrc;
  float invv = 1.0f;
  auto prep0 = [&](int s) {
    xsrc = x_src(a, s + 3, b);
    if (w == 2) { o3p = ts_slot(ck, (s + 1 - LAG3) & 3); o3 = ts_slot(ck, imax(s, 0)); o4 = pt_slot(ck, imax(s - LAG3, 0)); PIN_S(o3); }
    if (w == 3) { o3p = pt_slot(ck, (s - LAG4) & 3); o4 = ts_slot(ck, s + 1); }
    PIN_S(xsrc); PIN_S(o3p); PIN_S(o4);
    __builtin_amdgcn_sched_barrier(0);
  };
  auto prep = [&](int s) {
    o2 = (unsigned)OFF_ACC + (unsigned)(s % SR) * (unsigned)ACC_STEP;
    sv = sv == NV - 1 ? 0 : sv + 1;
    PIN_S(o2); PIN_S(sv);
    if (w < 2) {
      PIN_S(tc0);
      o1 = sct_slot(ck, s % SR); o1m = scm_slot(ck, s % SR);
      need2 = 4u * (unsigned)(s + 1) + 2u * ((unsigned)(s + 2) / NSEG);
      PIN_S(o1); PIN_S(o1m); PIN_S(need2);
      __builtin_amdgcn_sched_barrier(0);
      kq_get(s);
    }
    invv = misc[4 + (s & 1)];
  };
  auto prep2 = [&](int s) {
#pragma unroll
    for (int k = 0; k < 3; ++k) {
      const int t = k == 0 ? 2 : (k == 1 ? far : 1 - far);
      const char* p = lds + L_VOX + sv * 5120 + t * 1024 + (plane & 47) * 16 + (g & 1) * 8;
      auto rd = [&](const char* q) { const u32x2 a0 = *(const u32x2*)q, a1 = *(const u32x2*)(q + 256); return mk8(a0[0], a0[1], a1[0], a1[1]); };
      vox[k == 0 ? 0 : 2 * k - 1] = rd(p);
      if (k) vox[2 * k] = rd(p + 3072);
      vma[k] = lds0 + (unsigned)(L_VOM + sv * 1280 + t * 256 + (plane & 31) * 8);
      PIN_V(vma[k]);
    }
    __builtin_amdgcn_sched_barrier(0);
    inv = u2f(__builtin_amdgcn_readfirstlane(f2u(invv)));
    slo = __builtin_bit_cast(u32x4, SelLo); shi = __builtin_bit_cast(u32x4, SelHi);
#pragma unroll
    for (int i = 0; i < 4; ++i) { slo[i] = ((slo[i] >> 7) & 0x10001u) * (f2u(inv) >> 16); shi[i] = ((shi[i] >> 7) & 0x10001u) * (f2u(inv) >> 16); }
    if (F0A) { xsrc = x_src(a, s + 3, b); PIN_S(xsrc); }
    k1 = KEXP * inv;
    PIN_V(k1); PIN_V(slo); PIN_V(shi);
    asm volatile("" : "+v"(vox[0]), "+v"(vox[1]), "+v"(vox[2]), "+v"(vox[3]), "+v"(vox[4]));
    __builtin_amdgcn_sched_barrier(0);
  };
  auto rows = [&](int s) {
    const int dl = spin_until(tw);
    if (w < 2) tc0 = cyc32();
    __builtin_amdgcn_sched_barrier(0);
    STAMP(1);
    unsigned spins = 0;
    auto pass = [&](bool first) {
      u32x4 x[2 * NC];
#pragma unroll
      for (int f = 0; f < NC; ++f) {
        if (f && w < 2 && first) { prep(s); asm volatile(".rept %0\n\ts_nop 3\n\t.endr" :: "n"(P2_G1)); }
        else if (f) asm volatile(".rept %0\n\ts_nop 3\n\t.endr" :: "n"(w < 2 ? P2_G0 : (f == 1 ? P2_G2 : P2_G3)));
        __builtin_amdgcn_sched_barrier(0);
        x[2 * f] = SC1_LOADS(l2off[2 * f], o2p); x[2 * f + 1] = SC1_LOADS(l2off[2 * f + 1], o2p);
        __builtin_amdgcn_sched_barrier(0);
      }
      if (first) lds_inc<L_MISC + 92>(lds0);
      if (first) { if (w >= 2) prep(s); else prep2(s); }
      if (w >= 2 && first && P2_W) lds_spin<L_MISC + 92, 0>(lds0, 4u * (unsigned)s);
      if (w == 2 && first) tk.store(rs, o4, s > LAG3 ? 0u : 0x7ff00000u);
      if (w == 3 && first) t2.store(rs, t2o, o4);
      if (first && w < 2) {
        p1cur = p1v; offs = (offs - (unsigned)dl) & ~3u;
        PIN_V(p1cur); PIN_S(offs);
        tstar = tc0 + (unsigned)(e1 + D1_M + (w == 1 ? D1_W1 : 0)) - offs * 20u;
        dmin = offs < 40u ? D1_MIN : REC_D1;
        dmin = s < EARLY_N ? REC_D1 + EARLY_D1 : dmin;
        dmax = REC_D1;
        PIN_S(tstar); PIN_S(dmin); PIN_S(dmax);
      }
      __builtin_amdgcn_sched_barrier(0);
      unsigned ok = 32u;
#pragma unroll
      for (int t = 0; t < 3; ++t) { acc[t] = Z4; PIN_V(acc[t]); }
      __builtin_amdgcn_sched_barrier(0);
      bool r = true;
#pragma unroll
      for (int f = 0; f < NC; ++f) {
        unsigned p[4];
#pragma unroll
        for (int k = 0; k < 2; ++k) {
          const u32x4 v = x[2 * f + k];
          p[2 * k] = pack2((float)(int)v[0], (float)(int)v[1]); p[2 * k + 1] = pack2((float)(int)v[2], (float)(int)v[3]);
          asm volatile("" : "+v"(p[2 * k]), "+v"(p[2 * k + 1]));
          __builtin_amdgcn_sched_barrier(0);
        }
        ok = (ok & x[2 * f][0] & x[2 * f][2]) & (x[2 * f + 1][0] & x[2 * f + 1][2]);
        PIN_V(ok);
        __builtin_amdgcn_sched_barrier(0);
        bm[f] = mk8(p[0], p[1], p[2], p[3]);
        acc[0] = smfma64(bm[f], Wqk[0][f], acc[0], sidx);
        __builtin_amdgcn_sched_barrier(0);
        bm_put(f);
        acc[1] = smfma64(bm[f], Wqk[1][f], acc[1], sidx);
        __builtin_amdgcn_sched_barrier(0);
        if (f == NC - 1) { u64 bad = __builtin_amdgcn_ballot_w64(ok == 0u); PIN_S(bad); r = bad == 0ull; }
        __builtin_amdgcn_sched_barrier(0);
        acc[2] = smfma64(bm[f], Wqk[2][f], acc[2], sidx);
        __builtin_amdgcn_sched_barrier(0);
      }
      red_put();
      return r;
    };
    if (__builtin_expect(!pass(true), 0)) {
      FC(0);
      do {
        if (dead || ++spins > SPIN_CAP) { dead = true; break; }
        asm volatile("" ::: "memory");
      } while (!pass(false));
    }
    STAMPV(12, spins); LOGF(1, spins);
    TSTAMP(2, now32()); TSTAMP(3, spins);
  };
  if (w >= 2) prep0(-1);
  prep(0);
  if (w < 2) prep2(0);
  CODE_PAD((PADR >> (4 * w)) & 15);
#pragma unroll 1
  for (int s = 0;; ++s) {
    if (__builtin_expect(o2p != 0u, 1)) rows(s);
    if (w < 2 && __builtin_expect(s == NSEG - 1, 0)) break;
    STAMP(2);
    BAR();
    STAMP(3);
    if (w < 2) pub1(p1cur, w == 0 ? o1m : o1);
    if (w == 2) tk.load(rs, o3p);
    if (w == 3) pv2.load(rs, o3p);
    const unsigned tp1 = cyc32();
    __builtin_amdgcn_sched_barrier(0);
    if (w == 2) {
      __builtin_amdgcn_s_setprio(W2_P);
      prep0(s);
      if (W2_D) spin_cyc(tp1 + W2_D);
      fr = qk_frag(lds + qo4, lds + qo2, lds + qo2b);
      t2_mem(fr, qx[0], qx[1], rs, o3, h, j, invv * KEXP, lane);
      __builtin_amdgcn_sched_barrier(0);
      asm volatile("s_waitcnt vmcnt(1)" ::: "memory");
      lds_inc<L_MISC + 28>(lds0);
      kq_get(s + 1);
    }
    if (w == 3) {
      __builtin_amdgcn_s_setprio(T4_P);
      prep0(s);
      t2.get(lds, s + 1, lane); t2.run(0); t2.run(1);
      __builtin_amdgcn_sched_barrier(0);
    }
    if (w >= 2 && __builtin_expect(s == NSEG - 2, 0)) { VM_DRAIN(); lds_inc<L_MISC + 28>(lds0); }
    if (w >= 2) { __builtin_amdgcn_s_setprio(UN_P); unit(s + 2); }
    TSTAMP(0, now32());
    STAMP(4);
    __builtin_amdgcn_sched_barrier(0);
    unsigned tpub = 0;
    u32x2 sw;
    if (w == 2) {
      STAMP(5);
      __builtin_amdgcn_sched_barrier(0);
      STAMP(6);
      ARRIVE(0);
      tk.run(j, lane, SelLo);
      if (__builtin_expect(s == NSEG - 1, 0)) { tlast = tp1; break; }
      if (F2B) { lds_spin<L_MISC + 48, 1>(lds0, 2u * (unsigned)(s + 1)); x_fetch_n<F2B>(xsrc, lds0, s + 3, P2B, lane * 16); }
      __builtin_amdgcn_s_setprio(SP_P);
    }
    if (w == 3) {
      pv2.run<false>(lds, sv < 1 + LAG4 ? sv + NV - 1 - LAG4 : sv - 1 - LAG4, lane, rs, 0u);
      if (__builtin_expect(s == NSEG - 1, 0)) { tlast = tp1; break; }
      if (ST_E) lds_inc<L_MISC + 112>(lds0);
      STAMP(11);
      __builtin_amdgcn_s_setprio(SP_P);
      STAMP(5);
      ARRIVE(0);
      if (F3) { lds_spin<L_MISC + 48, 1>(lds0, 2u * (unsigned)(s + 1)); x_fetch_n<F3>(xsrc, lds0, s + 3, 40 - F3C - F3, lane * 16); }
      STAMP(6);
    }
    if (w < 2) {
      __builtin_amdgcn_s_setprio(D1_P);
      x_fetch_n<F0A>(xsrc, lds0, s + 3, PA + w * F0A, lane * 16);
      int dq = imin((int)(tstar - tp1), dmax);
      dq = dq < -D1_LATE ? dmax : dq;
      PIN_S(dq);
      spin_exact(tp1 + (unsigned)imax(dq, dmin));
      STAMP(5);
      u32x32 S;
      auto sc_load = [&]() {
        asm volatile("buffer_load_dwordx4 v[224:227], %2, %4, %5 offen sc1\n\t.rept %7\n\ts_nop 3\n\t.endr\n\t"
                     "buffer_load_dwordx4 v[228:231], %3, %4, %6 offen sc1\n\tbuffer_load_dwordx4 v[232:235], %3, %4, %6 offen offset:2048 sc1\n\t"
                     "buffer_load_dwordx4 v[236:239], %3, %4, %6 offen offset:256 sc1\n\tbuffer_load_dwordx4 v[240:243], %3, %4, %6 offen offset:2304 sc1\n\t"
                     "v_mov_b32 v220, 0x42000000\n\tv_mov_b32 v221, 0\n\tv_mov_b32 v222, 0x42000000\n\tv_mov_b32 v223, 0"
                     : "=&{v[224:255]}"(S), "+{v[208:223]}"(P) : "v"(l1offm), "v"(l1off), "s"(rs), "s"(o1m), "s"(o1), "n"(P1_G) : "memory");
      };
      sc_load();
      __builtin_amdgcn_sched_barrier(0);
      lds_inc<L_MISC + 48>(lds0);
      STAMP(6);
      ARRIVE(0);
      lds_spin<L_MISC + 28, 0>(lds0, need2);
      __builtin_amdgcn_s_setprio(3);
      asm volatile("ds_read_b64 v[192:193], %3\n\tds_read_b64 v[196:197], %3 offset:768\n\tds_read_b64 v[200:201], %4\n\tds_read_b64 v[204:205], %4 offset:768\n\tds_read_b64 v[184:185], %2"
                   : "+{v[184:191]}"(V8), "+{v[192:207]}"(V) : "v"(vma[0]), "v"(vma[1]), "v"(vma[2]) : "memory");
      __builtin_amdgcn_sched_barrier(0);
      {
        const int de = imin((int)(tp1 - tc0) - e1, D1_C);
        e1 += s == 0 ? 0 : de >> (s < EARLY_N && de < -D1_C ? 0 : D1_G);
        PIN_S(e1);
      }
      STAMP(11);
      __builtin_amdgcn_sched_barrier(0);
      {
        unsigned spins = 0;
        u64 miss, bad;
#define MM(d, a, b, c) "v_mfma_f32_16x16x32_bf16 " d ", " a ", " b ", " c "\n\t"
#define FM(d, x, r, ctrl) "v_fmac_f32_dpp " d ", " x ", " r " " ctrl "\n\t"
#define ID_ "quad_perm:[0,1,2,3] row_mask:0xf bank_mask:"
#define HM_ "row_half_mirror row_mask:0xf bank_mask:"
#define R8_ "row_ror:8 row_mask:0xf bank_mask:"
#define MI_ "row_mirror row_mask:0xf bank_mask:"
        auto soft = [&]() {
          asm volatile(
            "s_waitcnt vmcnt(4)\n\t" MM("v[244:247]", "%6", "v[224:227]", "0") "v_min3_u32 v214, v224, v225, v226\n\t"
            "s_waitcnt vmcnt(3)\n\t" MM("v[248:251]", "%6", "v[228:231]", "0") "v_min3_u32 v214, v214, v227, v228\n\tv_min3_u32 v214, v214, v229, v230\n\t"
            "s_waitcnt vmcnt(2)\n\t" MM("v[248:251]", "%6", "v[232:235]", "v[248:251]") "v_min3_u32 v214, v214, v231, v232\n\t"
            "v_add_f32_dpp v212, v244, v244 " R8_ "0x3\n\tv_add_f32_dpp v213, v245, v245 " R8_ "0x3\n\t"
            "s_waitcnt vmcnt(1)\n\t" MM("v[248:251]", "%7", "v[236:239]", "v[248:251]")
            "v_add_f32_dpp v212, v246, v246 " R8_ "0xc\n\tv_add_f32_dpp v213, v247, v247 " R8_ "0xc\n\t"
            "s_waitcnt vmcnt(0) lgkmcnt(0)\n\t" MM("v[248:251]", "%7", "v[240:243]", "v[248:251]")
            "v_fma_f32 v212, v212, %14, %15\n\tv_fma_f32 v213, v213, %14, %15\n\tv_exp_f32 v216, v212\n\tv_exp_f32 v217, v213\n\t"
            "v_min3_u32 v214, v214, v233, v234\n\tv_cvt_pk_bf16_f32 v208, v216, v217\n\tv_min3_u32 v214, v214, v235, v236\n\tds_read_b96 v[188:190], %16 offset:%18\n\t"
            "v_exp_f32 v216, v248\n\tv_mov_b32_dpp v209, v208 " R8_ "0xf\n\tv_exp_f32 v217, v249\n\tv_exp_f32 v218, v250\n\t"
            MM("v[252:255]", "%8", "v[208:211]", "0") "v_exp_f32 v219, v251\n\tv_cvt_pk_bf16_f32 v232, v216, v217\n\t"
            MM("v[224:227]", "v[192:195]", "v[208:211]", "0") "v_cvt_pk_bf16_f32 v233, v218, v219\n\tv_min3_u32 v214, v214, v237, v238\n\tv_mov_b32_dpp v234, v232 " R8_ "0xf\n\t"
            MM("v[228:231]", "v[196:199]", "v[208:211]", "0") "v_mov_b32_dpp v235, v233 " R8_ "0xf\n\t"
            MM("v[244:247]", "v[204:207]", "v[208:211]", "0") "v_min3_u32 v214, v214, v239, v240\n\t"
            MM("v[252:255]", "%8", "v[232:235]", "v[252:255]") MM("v[224:227]", "%10", "v[232:235]", "v[224:227]") "v_min3_u32 v214, v214, v241, v242\n\t"
            MM("v[228:231]", "%11", "v[232:235]", "v[228:231]") "s_waitcnt lgkmcnt(0)\n\tv_sub_u32 v215, v190, %17 clamp\n\t"
            MM("v[236:239]", "v[200:203]", "v[208:211]", "0") "v_min3_u32 v214, v214, v243, v215\n\tv_cmp_eq_u32 %4, 0, v214\n\tv_rcp_f32 v216, v252\n\t"
            MM("v[240:243]", "v[184:187]", "v[208:211]", "0") "v_mul_f32 v216, v216, v188\n\tv_subrev_u32 v215, 0x2f800000, v252\n\t"
            MM("v[236:239]", "%12", "v[232:235]", "v[236:239]") FM("v220", "v224", "v216", ID_ "0x1") FM("v221", "v225", "v216", ID_ "0x1")
            "v_mov_b32_dpp v217, v216 " HM_ "0xf\n\t"
            MM("v[244:247]", "%13", "v[232:235]", "v[244:247]") FM("v220", "v230", "v216", ID_ "0x2") FM("v221", "v231", "v216", ID_ "0x2")
            MM("v[240:243]", "%9", "v[232:235]", "v[240:243]") FM("v220", "v228", "v217", HM_ "0x1") FM("v221", "v229", "v217", HM_ "0x1")
            "v_cmp_ge_u32 vcc, v215, v189\n\t" FM("v220", "v226", "v217", HM_ "0x2") FM("v221", "v227", "v217", HM_ "0x2")
            "s_or_b64 %5, %4, vcc"
            : "+{v[224:255]}"(S), "+{v[208:223]}"(P), "+{v[184:191]}"(V8), "+{v[192:207]}"(V), "=&s"(miss), "=&s"(bad)
            : "v"(slo), "v"(shi), "v"(ONES), "v"(vox[0]), "v"(vox[1]), "v"(vox[2]), "v"(vox[3]), "v"(vox[4]), "v"(k1), "v"(minf), "v"(lds0), "s"(s), "n"(L_MISC + 32)
            : "vcc", "scc", "memory");
        };
        soft();
        if (__builtin_expect(bad != 0ull, 0)) {
          if (miss) FC(1); else FC(2);
          while (miss != 0ull && !dead) {
            if (++spins > SPIN_CAP) dead = true;
            asm volatile("s_nop 7\n\ts_nop 7" ::: "memory");
            sc_load(); soft();
          }
          if (bad != 0ull) {
            u32x4 y[5];
#pragma unroll
            for (int k = 0; k < 4; ++k) y[k] = SC1_LOADS(l1off + (k & 1) * 2048 + (k >> 1) * 256, o1);
            y[4] = SC1_LOADS(l1offm, o1m);
            const f32x4 d0 = mfma32(as8(slo), as8(y[4]), Z4);
            f32x4 d1 = mfma32(as8(shi), as8(y[3]), mfma32(as8(shi), as8(y[2]), mfma32(as8(slo), as8(y[1]), mfma32(as8(slo), as8(y[0]), Z4))));
            const bool lo = c < 8;
            const float m0 = __builtin_fmaf((lo ? d0[0] : d0[2]) + dppf<DPP_ROR8>(lo ? d0[2] : d0[0]), k1, minf);
            const float m1 = __builtin_fmaf((lo ? d0[1] : d0[3]) + dppf<DPP_ROR8>(lo ? d0[3] : d0[1]), k1, minf);
            float mx = fmx(fmx(fmx(d1[0], d1[1]), d1[2]), fmx(fmx(d1[3], m0), m1));
            mx = fmx(mx, dppf<DPP_ROR8>(mx));
            mx = x32mx(x16mx(mx));
            d1 -= mx;
            const unsigned mm = pack2(__builtin_amdgcn_exp2f(m0 - mx), __builtin_amdgcn_exp2f(m1 - mx));
            const unsigned t01 = pack2(__builtin_amdgcn_exp2f(d1[0]), __builtin_amdgcn_exp2f(d1[1])), t23 = pack2(__builtin_amdgcn_exp2f(d1[2]), __builtin_amdgcn_exp2f(d1[3]));
            const bf16x8 pm = mk8(mm, dpp_ror8(mm), 0u, 0u), pt = mk8(t01, t23, dpp_ror8(t01), dpp_ror8(t23));
            const float r = __builtin_amdgcn_rcpf(mfma32(ONES, pt, mfma32(ONES, pm, Z4))[0]) * u2f(V8[4]), rm = dppf<0x141>(r);
            f32x4 o[5];
#pragma unroll
            for (int k = 0; k < 5; ++k) o[k] = mfma32(vox[k], pt, mfma32(k ? mk8(V[4 * k - 4], V[4 * k - 3], 0u, 0u) : mk8(V8[0], V8[1], 0u, 0u), pm, Z4));
#pragma unroll
            for (int i = 0; i < 2; ++i) {
              const float oa = dppf<0x141>(o[2][i]), ob = dppf<0x141>(o[1][2 + i]);
              P[12 + i] = f2u(__builtin_fmaf(hd ? ob : oa, rm, __builtin_fmaf(hd ? o[2][2 + i] : o[1][i], r, i ? 0.f : 32.0f)));
            }
            P[8] = f2u(r); P[9] = f2u(rm);
#pragma unroll
            for (int i = 0; i < 4; ++i) { S[12 + i] = f2u(o[3][i]); S[16 + i] = f2u(o[0][i]); S[20 + i] = f2u(o[4][i]); }
            asm volatile("s_nop 7\n\ts_nop 7" : "+{v[224:255]}"(S), "+{v[208:223]}"(P));
          }
        }
        STAMP(7); STAMPV(13, spins); LOGF(0, spins);
        {
          asm volatile("v_cvt_i32_f32 v218, v220\n\tv_cvt_i32_f32 v219, v221\n\tv_and_or_b32 v218, v218, %7, 2\n\tbuffer_atomic_add_x2 v[218:219], %3, %5, %6 offen\n\t"
                       FM("v222", "v236", "v216", R8_ "0x4") FM("v223", "v237", "v216", R8_ "0x4") "ds_read_b64 %2, %8 offset:%9\n\t"
                       FM("v222", "v238", "v217", MI_ "0x8") FM("v223", "v239", "v217", MI_ "0x8") "v_permlane32_swap_b32 v240, v242\n\t"
                       FM("v222", "v244", "v217", MI_ "0x4") FM("v223", "v245", "v217", MI_ "0x4") "v_permlane32_swap_b32 v241, v243\n\t"
                       FM("v222", "v246", "v216", R8_ "0x8") FM("v223", "v247", "v216", R8_ "0x8")
                       FM("v222", "v240", "v216", ID_ "0x1") FM("v223", "v241", "v216", ID_ "0x1") FM("v222", "v242", "v217", HM_ "0x1") FM("v223", "v243", "v217", HM_ "0x1")
                       "v_cvt_i32_f32 v212, v222\n\tv_cvt_i32_f32 v213, v223\n\tv_and_or_b32 v212, v212, %7, 2\n\tbuffer_atomic_add_x2 v[212:213], %4, %5, %6 offen\n\t"
                       "s_waitcnt lgkmcnt(0)"
                       : "+{v[224:255]}"(S), "+{v[208:223]}"(P), "=&v"(sw) : "v"(acco), "v"(acco2), "s"(rs), "s"(o2), "s"(~63u), "v"(lds0), "n"(L_MISC + 96) : "memory");
          __builtin_amdgcn_sched_barrier(0);
          tpub = now32();
          lds_inc<L_MISC + 104 + 4 * (w & 1)>(lds0);
          asm volatile("buffer_atomic_umax %0, %1, %2, %3 offen" :: "v"(tpub | 1u), "v"(tso), "s"(rs), "s"(o2) : "memory");
          STAMP(10);
        }
      }
    }
    STAMP(8);
    ARRIVE(1);
    o2p = o2;
    PIN_S(o2p);
    if (w < 2) {
      if (__builtin_expect(__builtin_amdgcn_readfirstlane(sw[1]) <= (unsigned)s, 0)) {
        lds_spin<L_MISC + 100, 0>(lds0, (unsigned)s + 1u);
        sw[0] = __hip_atomic_load((unsigned*)(misc + 24), __ATOMIC_RELAXED, __HIP_MEMORY_SCOPE_WORKGROUP);
      }
    } else {
      u32x4 q;
      asm volatile("1:\n\tds_read_b128 %0, %1 offset:%3\n\t.if %4\n\ts_sleep 1\n\t.endif\n\ts_waitcnt lgkmcnt(0)\n\tv_min3_u32 v201, v201, v202, v203\n\tv_cmp_ge_u32 vcc, %2, v201\n\ts_cbranch_vccnz 1b"
                   : "=&{v[200:203]}"(q) : "v"(lds0), "s"(s), "n"(L_MISC + 96), "n"(SP_SL) : "vcc", "memory");
      __builtin_amdgcn_s_setprio(3);
      tpub = now32();
      sw[0] = q[0];
    }
    STAMP(9);
    static_assert(F3C == 0 && F2C == 0 && F0C == 0, "nothing behind the atomics");
    LOGT(0, tpub); LOGT(1, tb2_);
    TSTAMP(1, tpub);
    ARRIVE_OUT();
    const unsigned an = __builtin_amdgcn_readfirstlane(sw[0]);
    const int ahead = (int)(an - tpub);
    tw = tpub + (unsigned)(an == 0u || ahead > 1000 ? SCH_FIRST : imax(ahead, w < 2 ? SCH_DMIN : 0));
    offs = tw - an;
  }
  STAMP_OUT();
  LOGP(4);
  __builtin_amdgcn_s_setprio(3);
  if (w < 2) { BAR(); tlast = cyc32(); }
  if (w == 2) tk.store(rs, o4);
  if (w == 3) {
    const unsigned oo = op_slot(NSEG - 2 - LAG4) + (unsigned)((m * 32 + gp) * 1280);
    const char* sp = lds + L_STG + 2048;
    const u32x4 v0 = *(const u32x4*)(sp + lane * 16), v1 = *(const u32x4*)(sp + 1280 + lane * 16), v2 = *(const u32x4*)(sp + (g & 1) * 1280 + 1024 + c * 16);
    SC1_STORE(v0, oo + lane * 16); SC1_STORE(v1, oo + 20480 + lane * 16); SC1_STORE(v2, lane < 32 ? oo + g * 20480 + 1024 + c * 16 : 0x7ffffff0u);
  }
  if (dead) tlast |= 1u; else tlast &= ~1u;
}

__device__ __forceinline__ void drain(const RecArgs& a, char* lds, const __amdgpu_buffer_rsrc_t rs, const int w, const unsigned tp1) {
  static_assert(LAG3 == 2 && LAG4 == 3 && LAG5 == 6, "drain schedule");
  const int lane = __builtin_amdgcn_workitem_id_x() & 63, b = __builtin_amdgcn_workgroup_id_x(), c = lane & 15;
  const int gp = (b & 7) * 2 + (b >> 7), m = (b >> 3) & 15, j = m & 7;
  unsigned* const misc = (unsigned*)(lds + L_MISC);
  const unsigned lds0 = (unsigned)(uintptr_t)(LAS char*)lds, ck = __builtin_amdgcn_readfirstlane(misc[11]);
  bool dead = tp1 & 1u;
  auto poll = [&](auto load, auto ready) {
    for (unsigned spins = 0;;) {
      asm volatile("" ::: "memory");
      load();
      if (ready() || dead) break;
      if (++spins > SPIN_CAP) dead = true;
      if (END_SL) __builtin_amdgcn_s_sleep(END_SL);
    }
  };
  LOGD(0);
#define TASK(ty, sg, tm) ((u64)((ty) | ((sg) - 56) << 2 | ((tm) / 100) << 5))
  u64 todo = w == 0 ? TASK(1, 63, END_E1) | TASK(2, 63, END_E2) << 12 | TASK(3, 62, END_E3) << 24
           : w == 1 ? TASK(1, 62, 0) | TASK(2, 62, END_E62) << 12 | TASK(3, 60, END_E3) << 24
           : w == 2 ? TASK(2, 60, 0) | TASK(3, 56, 0) << 12 : TASK(2, 61, END_E61) | TASK(3, 58, END_E59) << 12;
  TokTask tk;
  PvTask2 pv;
  OutTask ot;
  {
    const u32x4* p = (const u32x4*)(lds + L_DRN) + lane;
    const u32x4 q0 = p[0], q1 = p[64], q2 = p[128];
    tk.l0 = q0[0]; tk.l1 = q0[0] + 16384u; tk.lm = q0[1]; tk.s0 = q0[2]; tk.s1 = q0[3];
    pv.lo = q1[0]; pv.lm = q1[1]; pv.vr = q1[2]; pv.ma = q1[3]; pv.mz = q2[0]; pv.so = q2[1];
    pv.pa[0] = pv.pa[1] = pv.pb[0] = pv.pb[1] = (u32x4){0u, 0u, 0u, 0u};
    ot.t5f = q2[2]; ot.t5o = q2[3]; ot.sel = as8(p[192]);
  }
#pragma unroll 1
  for (int ti = 0; todo; todo >>= 12, ++ti) {
    const int ty = (int)todo & 3, sl = 56 + (((int)todo >> 2) & 7);
    const unsigned tm = tp1 + (((unsigned)todo >> 5) & 0x7fu) * 100u;
    if (ty == 1) {
      wait_cyc(tm);
      LOGD(3 * ti + 1);
      poll([&]() { tk.load(rs, ts_slot(ck, sl)); }, [&]() { return tk.ready(); });
      LOGD(3 * ti + 2);
      tk.run(j, lane, ot.sel); tk.store(rs, pt_slot(ck, sl));
    } else if (ty == 2) {
      const unsigned t0 = cyc32();
      if (sl >= NSEG - 2) { lds_wait(misc + 7, 4u * NSEG + 3u); wait_cyc(t0 + END_G2); }
      wait_cyc(tm);
      LOGD(3 * ti + 1);
      poll([&]() { pv.load(rs, pt_slot(ck, sl)); }, [&]() { return pv.ready(); });
      LOGD(3 * ti + 2);
      pv.run<true>(lds, sl % NV, lane, rs, op_slot(sl) + (unsigned)((m * 32 + gp) * 1280));
      if (sl == NSEG - 1) misc[14] = cyc32() | 1u;
    } else {
      if (sl >= NSEG - 4) { lds_wait(misc + 14, 1u); wait_cyc(__builtin_amdgcn_readfirstlane(misc[14]) + END_G3); }
      wait_cyc(tm);
      LOGD(3 * ti + 1);
      OutTask o2 = ot;
      poll([&]() { o2.load(a, rs, sl + 1, b); ot.load(a, rs, sl, b); }, [&]() { return ot.ready(b) & o2.ready(b); });
      LOGD(3 * ti + 2);
      o2.run(lane); o2.store(a, sl + 1, b, lane);
      ot.run(lane); ot.store(a, sl, b, lane);
    }
    LOGD(3 * ti + 3);
  }
  VM_DRAIN();
  if (w != 0) lds_inc<L_MISC + 64>(lds0);
  else {
    constexpr unsigned o2 = (unsigned)OFF_ACC + (unsigned)((NSEG - 1) % SR) * (unsigned)ACC_STEP;
    lds_wait(misc + 16, 3u);
    LOGD(10);
    if (lane == 0) __hip_atomic_fetch_add((gu64*)((GAS char*)(a.ws + o2) + acc_off(m * 4, 0)), (u64)1, RLX_AGENT);
    wait_until(now32() + SCH_DRAIN);
    wait_cyc(tp1 + END_E4);
    unsigned x;
    if (qkv_row(b) + 48 >= 55 && qkv_col(b) < 15) poll([&]() { x = __builtin_amdgcn_raw_buffer_load_b32(rs, acc_off(c * 4, 0), o2, 16); }, [&]() { return wave_all((x & 16u) != 0u); });
    lds_inc<L_MISC + 68>(lds0);
    LOGD(11);
  }
  if (qkv_row(b) + 48 >= 55 && qkv_col(b) < 15) {
    lds_wait(misc + 18, 8u);
    lds_spin<L_MISC + 68, 1>(lds0, 1u);
    x_fetch_n<10>(x_src(a, NSEG + 3, b), lds0, 3, 10 * w, lane * 16);
    VM_DRAIN();
    lds_inc<L_MISC + 68>(lds0);
  }
  LOGP(8);
}

__device__ __forceinline__ void proj_front(const RecArgs& a, char* lds, const __amdgpu_buffer_rsrc_t rs, const int v) {
  const int lane = __builtin_amdgcn_workitem_id_x() & 63, b = __builtin_amdgcn_workgroup_id_x(), c = lane & 15, g = lane >> 4;
  const int rb = (b & 7) + 8 * (b >> 5), cq = (b >> 3) & 3;
  const unsigned lds0 = (unsigned)(uintptr_t)(LAS char*)lds;
  unsigned* const cnt = (unsigned*)(lds + L_MISC) + 20;
  LOGF2(0);
  float* const red = (float*)(lds + L_RED1);
  float* const fin = red + 640;
  const f32x4 Z4 = {0.f, 0.f, 0.f, 0.f};
#pragma unroll
  for (int i = 0; i < 8; ++i) x_fetch((const u16*)(a.ws + OFF_OB) + (size_t)rb * 32 * DM, lds0, 0, 8 * v + i, lane);
  bf16x8 W[2][20];
#pragma unroll
  for (int k = 0; k < 40; ++k) W[k & 1][k >> 1] = *(const bf16x8*)(a.ws + OFF_WOBT + ((size_t)(cq * 10 + 2 * v + (k & 1)) * 20 + (k >> 1)) * 1024 + lane * 16);
  const unsigned fo = (unsigned)(rb * 40960 + (cq * 5 + v) * 1024 + ((g >> 1) * 16 + c) * 16), f8 = fo + (g & 1) * 8, f16 = fo + (g & 1) * 20480;
  u32x2 xo[2][2];
#pragma unroll
  for (int k = 0; k < 4; ++k)
    xo[k >> 1][k & 1] = __builtin_bit_cast(u32x2, __hip_atomic_load((const gu64*)((const char*)a.xnew + f8 + (k >> 1) * 512 + (k & 1) * 20480), RLX_AGENT));
  asm volatile("s_waitcnt vmcnt(44)" ::: "memory");
  lds_inc<L_MISC + 80>(lds0);
  lds_wait(cnt, 5u);
  LOGF2(1);
  f32x4 acc[2][2] = {{Z4, Z4}, {Z4, Z4}};
#pragma unroll
  for (int ks = 0; ks < 20; ++ks)
#pragma unroll
    for (int t = 0; t < 2; ++t) {
      const bf16x8 x = *(const bf16x8*)(lds + L_XN + (t * 20 + ks) * 1024 + lane * 16);
#pragma unroll
      for (int i = 0; i < 2; ++i) acc[i][t] = mfma32(W[i][ks], x, acc[i][t]);
    }
  u32x2 xp[2][2];
  PIN_V(acc[1][1]);
  LOGF2(2);
#pragma unroll
  for (int k = 0; k < 4; ++k) {
    const int i = k >> 1, t = k & 1;
#pragma unroll
    for (int q = 0; q < 2; ++q) {
      const unsigned r = pack2(acc[i][t][2 * q], acc[i][t][2 * q + 1]), x = xo[i][t][q];
      xp[i][t][q] = pack2(u2f(x << 16) + u2f(r << 16), u2f(x & 0xffff0000u) + u2f(r & 0xffff0000u));
      acc[i][t][2 * q] = u2f(xp[i][t][q] << 16); acc[i][t][2 * q + 1] = u2f(xp[i][t][q] & 0xffff0000u);
    }
    const float ml = x32sum(x16sum((acc[i][t][0] + acc[i][t][1]) + (acc[i][t][2] + acc[i][t][3]))) * (1.0f / 16.0f);
    float q = 0.f;
#pragma unroll
    for (int r = 0; r < 4; ++r) { const float d = acc[i][t][r] - ml; q += d * d; }
    q = x32sum(x16sum(q));
    if (g == 0) { red[(2 * v + i) * 32 + t * 16 + c] = ml; red[320 + (2 * v + i) * 32 + t * 16 + c] = q; }
  }
  lds_inc<L_MISC + 80>(lds0);
  if (v == 0) {
    lds_wait(cnt, 10u);
    LOGF2(3);
    float mw[10], ml = 0.f, m2 = 0.f;
#pragma unroll
    for (int i = 0; i < 10; ++i) { mw[i] = red[i * 32 + (lane & 31)]; ml += mw[i]; m2 += red[320 + i * 32 + (lane & 31)]; }
    ml *= 0.1f;
#pragma unroll
    for (int i = 0; i < 10; ++i) m2 += 16.0f * (mw[i] - ml) * (mw[i] - ml);
    const unsigned xs = (unsigned)OFF_LNX + rb * 1024;
    if (lane < 32) SC1_STORE8S(((u32x2){f2u(ml), nz(f2u(m2))}), (cq * 32 + lane) * 8, xs);
    wait_cyc(cyc32() + FR_D);
    u32x2 x0, x1;
    for (unsigned spins = 0; spins < SPIN_CAP; ++spins) {
      x0 = __builtin_amdgcn_raw_buffer_load_b64(rs, lane * 8, xs, 16); x1 = __builtin_amdgcn_raw_buffer_load_b64(rs, 512 + lane * 8, xs, 16);
      if (wave_all(x0[1] != 0u && x1[1] != 0u)) break;
      asm volatile("" ::: "memory");
    }
    const float m0 = u2f(x0[0]), m1 = u2f(x1[0]);
    const float mean = x32sum(m0 + m1) * 0.25f;
    const float M2 = x32sum((u2f(x0[1]) + u2f(x1[1])) + 160.0f * ((m0 - mean) * (m0 - mean) + (m1 - mean) * (m1 - mean)));
    const float mb = rbf(mean), den = rbf(__builtin_sqrtf(rbf(rbf(M2 * (1.0f / DM)) + 1e-5f)));
    if (lane < 32) {
      fin[lane] = mb; fin[32 + lane] = 1.0f / den;
      if (cq == 0) __hip_atomic_store((GAS unsigned*)(a.ws + OFF_XNRM) + (rb * 32 + lane) * 4, f2u(1.02f * (M2 + DM * (mean - mb) * (mean - mb)) / (den * den)), RLX_AGENT);
    }
    lds_inc<L_MISC + 84>(lds0);
  }
  lds_wait(cnt + 1, 1u);
  LOGF2(4);
  const __amdgpu_buffer_rsrc_t rx = __builtin_amdgcn_make_buffer_rsrc(a.ws + OFF_XN, 0, (int)(OFF_QB - OFF_XN), 0x00020000);
  auto put = [&](const u32x2 p0, const u32x2 p1, unsigned so) {
    const auto r0 = __builtin_amdgcn_permlane16_swap(p0[0], p1[0], false, false), r1 = __builtin_amdgcn_permlane16_swap(p0[1], p1[1], false, false);
    __builtin_amdgcn_raw_buffer_store_b128((u32x4){r0[0], r1[0], r0[1], r1[1]}, rx, f16, so, 16);
  };
#pragma unroll
  for (int i = 0; i < 2; ++i) {
    u32x2 y[2];
#pragma unroll
    for (int t = 0; t < 2; ++t) {
      const float mb = fin[t * 16 + c], rden = fin[32 + t * 16 + c];
      y[t] = (u32x2){pack2(rbf(acc[i][t][0] - mb) * rden, rbf(acc[i][t][1] - mb) * rden), pack2(rbf(acc[i][t][2] - mb) * rden, rbf(acc[i][t][3] - mb) * rden)};
    }
    put(y[0], y[1], i * 512);
  }
  VM_DRAIN();
  lds_inc<L_MISC + 80>(lds0);
#pragma unroll
  for (int i = 0; i < 2; ++i) put(xp[i][0], xp[i][1], (unsigned)(OFF_XB - OFF_XN) + i * 512);
  if (v == 0) {
    lds_wait(cnt, 15u);
    if (rb < 4 && lane < 8) __hip_atomic_fetch_add((GAS unsigned*)(a.ws + OFF_RDY) + lane * 64, 1u, RLX_AGENT);
  }
  LOGF2(5);
}

__global__ __launch_bounds__(512, 2) void rec_kernel(RecArgs a) {
  __shared__ __attribute__((aligned(16))) char lds[L_BYTES];
  const int tid = __builtin_amdgcn_workitem_id_x(), wid = __builtin_amdgcn_readfirstlane(tid >> 6), w = wid & 3;
  const __amdgpu_buffer_rsrc_t rs = __builtin_amdgcn_make_buffer_rsrc(a.ws, 0, (int)COMM_BYTES, 0x00020000);
  if (tid < 32) ((float*)(lds + L_MISC))[tid] = (tid == 4 || tid == 5) ? 1.0f : 0.0f;
  BAR();
  if (a.layer && wid < 5) proj_front(a, lds, rs, wid);
  if (wid < 4) {
    unsigned tlast;
    if (w == 0) rec_waves<0>(a, lds, rs, tlast);
    else if (w == 1) rec_waves<1>(a, lds, rs, tlast);
    else if (w == 2) rec_waves<2>(a, lds, rs, tlast);
    else rec_waves<3>(a, lds, rs, tlast);
    drain(a, lds, rs, w, tlast);
  } else {
    unsigned vo[2][2];
    if (w == 0) token_waves<0>(a, lds, rs, vo);
    else if (w == 1) token_waves<1>(a, lds, rs, vo);
    else if (w == 2) token_waves<2>(a, lds, rs, vo);
    else token_waves<3>(a, lds, rs, vo);
    qkv_tail(a, lds, w, vo);
  }
}

#if defined(__HIP_DEVICE_COMPILE__)
#pragma clang attribute pop
#endif

constexpr int KQ = S_ / 4;
#define ATT_P1 6
#define ATT_P2 10
#define ATT_P3 14
#define ATT_THR 6.0f
#define ATT_NQ 4
#define ATT_LMAX 1.0e12f
#if defined(__HIP_DEVICE_COMPILE__)
#pragma clang attribute push(__attribute__((target("no-packed-fp32-ops"))), apply_to = function)
#endif
__global__ __launch_bounds__(256 * ATT_NQ, 4) void attb_kernel(const u16* __restrict__ Qp, const u16* __restrict__ KV, u16* __restrict__ O) {
  __shared__ f32x4 mo_[ATT_NQ][4][2][3][64];
  __shared__ float mm_[ATT_NQ][4][2][64];
  const int tid = __builtin_amdgcn_workitem_id_x(), bid = __builtin_amdgcn_workgroup_id_x();
  __shared__ unsigned sink[64];
  own_code<5000>(tid, sink);
  const int lane = tid & 63, wid = __builtin_amdgcn_readfirstlane(tid >> 6), kh = wid / ATT_NQ, r = lane & 31, hh = lane >> 5;
  const int hd = (bid & 7) * 4 + ((bid >> 3) & 3), qb = (bid >> 5) * ATT_NQ + wid % ATT_NQ;
  const int q0 = qb * 64;
  bf16x8 qa[2]; u32x2 qb2[2];
#pragma unroll
  for (int qt = 0; qt < 2; ++qt) {
    { const u32x2* qp = (const u32x2*)(Qp + ((size_t)hd * S_ + q0 + qt * 32 + r) * QST + 8 * hh); const u32x2 q0_ = qp[0], q1_ = qp[1]; qa[qt] = mk8(q0_[0], q0_[1], q1_[0], q1_[1]); }
    const u32x2 t = *(const u32x2*)(Qp + ((size_t)hd * S_ + q0 + qt * 32 + r) * QST + 16);
    qb2[qt] = t;
  }
  const __amdgpu_buffer_rsrc_t rk = __builtin_amdgcn_make_buffer_rsrc((void*)(KV + ((size_t)hd * 64 + kh * 16) * (KVT / 2)), 0, 16 * KVT, 0x00020000);
  unsigned ok1 = lane * 16, ok2 = hh ? 1600 : 1024 + r * 8, ov = r > 20 ? 1u << 30 : 1280 + (hh * 21 + r) * 16;
  f32x16 o[2];
  float m[2];
  const f32x16 Z16 = {0.f, 0.f, 0.f, 0.f, 0.f, 0.f, 0.f, 0.f, 0.f, 0.f, 0.f, 0.f, 0.f, 0.f, 0.f, 0.f};
  int odd = 0xDDDD;
  asm volatile("" : "+v"(odd));
  struct KvT { u32x4 k1, v0, v1; u32x2 k2; };
  auto fetch_k = [&](KvT& x, int t) {
    x.k1 = __builtin_amdgcn_raw_buffer_load_b128(rk, ok1, imin(t, KQ / 32 - 1) * KVT, 0); x.k2 = __builtin_amdgcn_raw_buffer_load_b64(rk, ok2, imin(t, KQ / 32 - 1) * KVT, 0);
    __builtin_amdgcn_sched_barrier(0);
  };
  auto fetch_v = [&](KvT& x, int t) {
    x.v0 = __builtin_amdgcn_raw_buffer_load_b128(rk, ov, imin(t, KQ / 32 - 1) * KVT, 0); x.v1 = __builtin_amdgcn_raw_buffer_load_b128(rk, ov + 672, imin(t, KQ / 32 - 1) * KVT, 0);
    __builtin_amdgcn_sched_barrier(0);
  };
  auto fetch = [&](int t) { KvT x; __builtin_amdgcn_sched_barrier(0); fetch_k(x, t); fetch_v(x, t); return x; };
  auto qk1 = [&](const KvT& x, int qt) { return __builtin_amdgcn_mfma_f32_32x32x16_bf16(as8(x.k1), qa[qt], Z16, 0, 0, 0); };
  auto qk2 = [&](const KvT& x, int qt, f32x16 c) { return __builtin_amdgcn_mfma_f32_32x32x8bf16_1k(__builtin_bit_cast(v4i16_t, x.k2), __builtin_bit_cast(v4i16_t, qb2[qt]), c, 0, 0, 0); };
  auto pv = [&](const u32x4 v, const f32x16 p, int hi, f32x16 c) {
    const f32x8 ph = hi ? __builtin_shufflevector(p, p, 8, 9, 10, 11, 12, 13, 14, 15) : __builtin_shufflevector(p, p, 0, 1, 2, 3, 4, 5, 6, 7);
    return __builtin_amdgcn_smfmac_f32_32x32x32_bf16(__builtin_bit_cast(hbf16x8, v), __builtin_bit_cast(hbf16x16, ph), c, odd, 0, 0);
  };
  auto tile = [&](const KvT& x, auto MODE) {
    f32x16 sa[2];
#pragma unroll
    for (int qt = 0; qt < 2; ++qt) sa[qt] = qk1(x, qt);
#pragma unroll
    for (int qt = 0; qt < 2; ++qt) sa[qt] = qk2(x, qt, sa[qt]);
#pragma unroll
    for (int qt = 0; qt < 2; ++qt) {
      if (MODE != 0) {
        float mx = fmx(fmx(sa[qt][0], sa[qt][1]), sa[qt][2]);
#pragma unroll
        for (int i = 3; i < 15; i += 2) mx = fmx(fmx(mx, sa[qt][i]), sa[qt][i + 1]);
        mx = fmx(mx, sa[qt][15]);
        if (MODE == 1 || !wave_all(mx <= ATT_THR)) {
          mx = x32mx(mx);
          if (MODE != 1) mx = fmx(mx, 0.f);
          const float mn = m[qt] + mx;
          const unsigned h1 = f2bf(mn); const float r1 = mn - bf2f(h1);
          const unsigned h2 = f2bf(r1); const float r2 = r1 - bf2f(h2);
          const unsigned h3 = f2bf(r2);
          const float me = (bf2f(h1) + bf2f(h2)) + bf2f(h3);
          const float dm = me - m[qt];
          m[qt] = me;
          if (hh) qb2[qt] = (u32x2){(h1 | (h2 << 16)) ^ 0x80008000u, h3 ^ 0x8000u};
          const float alpha = __builtin_amdgcn_exp2f(-dm);
#pragma unroll
          for (int i = 0; i < 16; ++i) { if (MODE != 1) o[qt][i] *= alpha; sa[qt][i] -= dm; }
        }
      }
#pragma unroll
      for (int i = 0; i < 16; ++i) sa[qt][i] = __builtin_amdgcn_exp2f(sa[qt][i]);
      asm volatile("" : "+v"(sa[qt]));
      if (MODE == 1) { o[qt] = Z16; asm volatile("" : "+v"(o[qt])); }
      o[qt] = pv(x.v1, sa[qt], 1, pv(x.v0, sa[qt], 0, o[qt]));
      __builtin_amdgcn_sched_barrier(0);
    }
  };
  typedef std::integral_constant<int, 1> I1; typedef std::integral_constant<int, 2> I2;
  auto reset = [&]() {
    asm volatile("" : "+v"(ok1), "+v"(ok2), "+v"(ov) :: "memory");
    m[0] = m[1] = 0.f;
    if (hh) qb2[0] = qb2[1] = (u32x2){0u, 0u};
  };
  auto slow = [&]() {
    reset();
    tile(fetch(0), I1{});
#pragma unroll 1
    for (int t = 1; t < KQ / 32; ++t) tile(fetch(t), I2{});
  };
  f32x16 sc[2];
  auto phase = [&](int qt, const u32x4 vp, const KvT& xk, const u32x4 v0, auto mid) {
    auto ex = [&](int i0, int i1) {
#pragma unroll
      for (int i = i0; i < i1; ++i) sc[qt][i] = __builtin_amdgcn_exp2f(sc[qt][i]);
      asm volatile("" : "+v"(sc[qt]));
      __builtin_amdgcn_sched_barrier(0);
    };
    o[1 - qt] = pv(vp, sc[1 - qt], 1, o[1 - qt]);
    __builtin_amdgcn_sched_barrier(0);
    ex(0, 3);
    sc[1 - qt] = qk1(xk, 1 - qt);
    __builtin_amdgcn_sched_barrier(0);
    ex(3, 6);
    sc[1 - qt] = qk2(xk, 1 - qt, sc[1 - qt]);
    __builtin_amdgcn_sched_barrier(0);
    ex(6, 10);
    o[qt] = pv(v0, sc[qt], 0, o[qt]);
    __builtin_amdgcn_sched_barrier(0);
    mid();
    ex(10, 16);
  };
  auto fast = [&]() {
    reset();
    KvT xa = fetch(0), xb = fetch(1);
    tile(xa, I1{});
    fetch_k(xa, 2);
    sc[0] = qk2(xb, 0, qk1(xb, 0)); sc[1] = Z16;
    asm volatile("" : "+v"(sc[1]));
    auto step = [&](KvT& x, KvT& y, int t) {
      phase(0, y.v1, x, x.v0, [&]() { fetch_v(y, t + 1); fetch_k(x, t + 2); });
      phase(1, x.v1, y, x.v0, []() {});
    };
    step(xb, xa, 1);
#pragma unroll 1
    for (int t = 2; t < KQ / 32; t += 2) {
      if (t == ATT_P1) __builtin_amdgcn_s_setprio(2);
      if (t == ATT_P2) __builtin_amdgcn_s_setprio(1);
      if (t == ATT_P3) __builtin_amdgcn_s_setprio(0);
      step(xa, xb, t);
      step(xb, xa, t + 1);
    }
    o[1] = pv(xb.v1, sc[1], 1, o[1]);
  };
  if (ATT_P1) __builtin_amdgcn_s_setprio(3);
  fast();
  {
    float big = 0.f;
#pragma unroll
    for (int qt = 0; qt < 2; ++qt) {
      big = fmx(big, hh ? o[qt][8] * (3.0e38f / ATT_LMAX) : __builtin_fabsf(o[qt][8]));
#pragma unroll
      for (int i = 0; i < 12; ++i) if (i != 8) big = fmx(big, (i > 8 && hh) ? 0.f : __builtin_fabsf(o[qt][i]));
    }
    if (__builtin_expect(!wave_all(big <= 3.0e38f), 0)) slow();
  }
  f32x4 (*mo)[2][3][64] = mo_[wid % ATT_NQ];
  float (*mm)[2][64] = mm_[wid % ATT_NQ];
#pragma unroll
  for (int qt = 0; qt < 2; ++qt) {
#pragma unroll
    for (int k = 0; k < 3; ++k) mo[kh][qt][k][lane] = (f32x4){o[qt][4 * k], o[qt][4 * k + 1], o[qt][4 * k + 2], o[qt][4 * k + 3]};
    mm[kh][qt][lane] = m[qt];
  }
  const int qt = kh & 1, grp = kh >> 1, row = q0 + qt * 32 + r, k0 = hd * BD + 4 * hh + 8 * grp;
  auto oaddr = [&](int k) { return O + (((size_t)(row >> 4) * 20 + (k >> 5)) * 64 + ((k & 31) >> 3) * 16 + (row & 15)) * 8 + (k & 7); };
  u16 *op0 = oaddr(k0), *op1 = oaddr(k0 + 16);
  asm volatile("" : "+v"(op0), "+v"(op1));
  asm volatile("s_waitcnt lgkmcnt(0)\n\ts_barrier" ::: "memory");
  {
    float a[4], l[4];
    f32x4 u[4], x[4];
#pragma unroll
    for (int i = 0; i < 4; ++i) { a[i] = mm[i][qt][lane]; l[i] = mo[i][qt][2][lane | 32][0]; u[i] = mo[i][qt][grp][lane]; x[i] = mo[i][qt][2][lane]; }
    const float mn = fmx(fmx(a[0], a[1]), fmx(a[2], a[3]));
#pragma unroll
    for (int i = 0; i < 4; ++i) a[i] = __builtin_amdgcn_exp2f(a[i] - mn);
    const float inv = __builtin_amdgcn_rcpf((l[0] * a[0] + l[1] * a[1]) + (l[2] * a[2] + l[3] * a[3]));
    const f32x4 v = ((u[0] * a[0] + u[1] * a[1]) + (u[2] * a[2] + u[3] * a[3])) * inv;
    *(GAS u32x2*)op0 = (u32x2){pack2(v[0], v[1]), pack2(v[2], v[3])};
    if (grp == 0) {
      const f32x4 y = ((x[0] * a[0] + x[1] * a[1]) + (x[2] * a[2] + x[3] * a[3])) * inv;
      if (hh == 0) *(GAS u32x2*)op1 = (u32x2){pack2(y[0], y[1]), pack2(y[2], y[3])};
    }
  }
}
#if defined(__HIP_DEVICE_COMPILE__)
#pragma clang attribute pop
#endif

__global__ __launch_bounds__(640, 3) void proj_kernel(const u16* __restrict__ Of, const u16* __restrict__ Wf, const u16* __restrict__ xold,
                                                      u16* __restrict__ xnew) {
  __shared__ __attribute__((aligned(16))) char ob[40960];
  const int tid = threadIdx.x, lane = tid & 63, w = __builtin_amdgcn_readfirstlane(tid >> 6), g = lane >> 4, c = lane & 15;
  __shared__ unsigned sink[64];
  own_code<1500>(tid, sink);
  const int rb = (blockIdx.x & 7) + 8 * (blockIdx.x >> 5), cb = (blockIdx.x >> 3) & 3;
  const int col = cb * 160 + w * 16 + 4 * g;
  const u16* wp = Wf + (size_t)(cb * 10 + w) * 20 * 512 + lane * 8;
  u32x4 st[4];
#pragma unroll
  for (int i = 0; i < 4; ++i) st[i] = *(const u32x4*)(Of + (size_t)rb * 40 * 512 + (i * 640 + tid) * 8);
  bf16x8 wf[20];
#pragma unroll
  for (int ks = 0; ks < 20; ++ks) wf[ks] = *(const bf16x8*)(wp + (size_t)ks * 512);
  u32x2 xo[2];
#pragma unroll
  for (int t = 0; t < 2; ++t) xo[t] = *(const u32x2*)(xold + (((size_t)(rb * 2 + t) * 20 + (col >> 5)) * 64 + ((col & 31) >> 3) * 16 + c) * 8 + (col & 7));
#pragma unroll
  for (int i = 0; i < 4; ++i) *(u32x4*)(ob + (i * 640 + tid) * 16) = st[i];
  BAR();
  f32x4 acc[2] = {{0.f, 0.f, 0.f, 0.f}, {0.f, 0.f, 0.f, 0.f}};
#pragma unroll
  for (int ks = 0; ks < 20; ++ks)
#pragma unroll
    for (int t = 0; t < 2; ++t) acc[t] = mfma32(wf[ks], *(const bf16x8*)(ob + (t * 20 + ks) * 1024 + lane * 16), acc[t]);
#pragma unroll
  for (int t = 0; t < 2; ++t) {
    const unsigned r01 = pack2(acc[t][0], acc[t][1]), r23 = pack2(acc[t][2], acc[t][3]);
    const unsigned p01 = pack2(u2f(xo[t][0] << 16) + u2f(r01 << 16), u2f(xo[t][0] & 0xffff0000u) + u2f(r01 & 0xffff0000u));
    const unsigned p23 = pack2(u2f(xo[t][1] << 16) + u2f(r23 << 16), u2f(xo[t][1] & 0xffff0000u) + u2f(r23 & 0xffff0000u));
    *(u32x2*)(xnew + (size_t)(rb * 32 + t * 16 + c) * DM + col) = (u32x2){p01, p23};
  }
}

extern "C" void kernel_launch(void* const* d_in, const int* in_sizes, int n_in, void* d_out, int out_size, void* d_ws,
                              size_t ws_size, hipStream_t stream) {
  const u16* A_x = (const u16*)d_in[0];
  const u16* A_mem0 = (const u16*)d_in[1];
  const u16* A_Wq = (const u16*)d_in[2];
  const u16* A_Wk = (const u16*)d_in[3];
  const u16* A_Wv = (const u16*)d_in[4];
  const u16* A_Wo = (const u16*)d_in[5];
  const u16* B_wqkv = (const u16*)d_in[8];
  const u16* B_wo = (const u16*)d_in[9];
  char* ws = (char*)(((uintptr_t)d_ws + ((size_t)WS_OFF << 20) + WS_AL - 1) & ~(uintptr_t)(WS_AL - 1));
  u16* xa = (u16*)(ws + OFF_XA); u16* Ob = (u16*)(ws + OFF_OB);

  prologue_kernel<<<256, 640, 0, stream>>>(ProArgs{A_x, A_Wq, A_Wk, A_Wv, A_Wo, B_wqkv, B_wo, ws});
  for (int layer = 0; layer < 2; ++layer) {
    rec_kernel<<<256, 512, 0, stream>>>(RecArgs{A_mem0, A_Wq, A_Wk, (const u16*)(ws + OFF_XB), xa, ws, (unsigned)layer, 0u});
    attb_kernel<<<BH * 32 / ATT_NQ, 256 * ATT_NQ, 0, stream>>>((const u16*)(ws + OFF_QB), (const u16*)(ws + OFF_KV), Ob);
  }
  proj_kernel<<<256, 640, 0, stream>>>(Ob, (const u16*)(ws + OFF_WOBT), xa, (u16*)d_out);
  spin_kernel<<<1, 1, 0, stream>>>(0, 0, 1, 0);
}
```

```cpp
#include <hip/hip_runtime.h>
#include <hip/hip_bf16.h>
#include <stdint.h>
#include <type_traits>

typedef unsigned short u16;
typedef unsigned long long u64;
typedef __attribute__((ext_vector_type(8))) short bf16x8;
typedef __attribute__((ext_vector_type(16))) short bf16x16;
typedef __attribute__((ext_vector_type(4))) float f32x4;
typedef __attribute__((ext_vector_type(2))) float f32x2;
typedef __attribute__((ext_vector_type(16))) float f32x16;
typedef __attribute__((ext_vector_type(8))) float f32x8;
typedef __attribute__((ext_vector_type(4))) unsigned u32x4;
typedef __attribute__((ext_vector_type(2))) unsigned u32x2;
typedef __attribute__((ext_vector_type(3))) unsigned u32x3;
typedef __attribute__((ext_vector_type(8))) unsigned u32x8;
typedef __attribute__((ext_vector_type(16))) unsigned u32x16;
typedef __attribute__((ext_vector_type(32))) unsigned u32x32;
#define GAS __attribute__((address_space(1)))
#define LAS __attribute__((address_space(3)))
typedef GAS unsigned long long gu64;
#define BAR() do { __builtin_amdgcn_s_waitcnt(0xC07F); asm volatile("s_waitcnt lgkmcnt(0)\n\ts_barrier" ::: "memory"); } while (0)

constexpr int S_ = 2048, DM = 640;
constexpr int NH = 32, DH = 192, HD = NH * DH;
constexpr int MEM = 8, SEG = 32, NSEG = S_ / SEG;
constexpr int BH = 32, BD = 20;

constexpr size_t al256(size_t x) { return (x + 255) & ~(size_t)255; }
constexpr int LAG3 = 2, LAG4 = 3, LAG5 = 6;
#define SR_ 8
constexpr int SR = SR_;
constexpr unsigned SCT_STEP = 131072, OFF_SCM = SR * SCT_STEP, SCM_STEP = 32768;
constexpr int ACC_NB = 192;
constexpr size_t ACC_STEP = 65536;
constexpr unsigned ACC_TS = ACC_NB * 256;
constexpr size_t ACC_LAYER = SR * ACC_STEP;
constexpr size_t OFF_ACC = 3 * 524288;
static_assert(SR == 8, "regions");
__device__ __forceinline__ unsigned ts_piece(int i) { return ACC_TS + (unsigned)(i < 128 ? (i >> 1) * 256 + (i & 1) * 16 : 128 + (i & 1) * 8192); }
constexpr size_t OFF_LNX = OFF_SCM + SR * SCM_STEP;
constexpr size_t OFF_RDY = OFF_LNX + 65536;
constexpr size_t PG = 2097152;
#define RING_ 4
constexpr int RING = RING_;
constexpr size_t TS_SLOT = (size_t)NH * 8 * 2560, PT_SLOT = (size_t)NH * 2560, OP_SLOT = (size_t)256 * 2560;
__device__ __forceinline__ unsigned ts_tok(int h, int j) { return (unsigned)((h & 15) * 32768 + (j >> 2) * 16384 + (j & 3) * 2048); }
__device__ __forceinline__ unsigned ts_mem(int h, int j) { return (unsigned)(524288 + ((h & 15) >> 1) * 16384 + (h & 1) * 4096 + j * 512); }
__device__ __forceinline__ unsigned pt_tok(int h) { return (unsigned)(((h & 15) >> 2) * 16384 + (h & 3) * 2048); }
__device__ __forceinline__ unsigned pt_mem(int h) { return (unsigned)(65536 + (h & 15) * 512); }
constexpr size_t OFF_X = 2 * PG + TS_SLOT + 3 * PT_SLOT, X_BYTES = 7 * OP_SLOT + 4 * PT_SLOT;
constexpr size_t OFF_OP = OFF_X + TS_SLOT + 4 * PT_SLOT;
constexpr size_t COMM_BYTES = OFF_X + X_BYTES + RING * OP_SLOT;
__device__ __forceinline__ unsigned near_half(unsigned ck, unsigned o) { return o + ((ck >> (o >= 2 * PG ? 8 : 4)) & 8192u); }
__device__ __forceinline__ unsigned ts_slot(unsigned ck, int sl) {
  return near_half(ck, sl == NSEG - 1 ? (unsigned)OFF_X : ((sl & 3) == 3 ? (unsigned)(2 * PG) : (unsigned)PG + (unsigned)(sl & 3) * (unsigned)TS_SLOT));
}
__device__ __forceinline__ unsigned pt_slot(unsigned ck, int sl) {
  return near_half(ck, sl >= NSEG - 4 ? (unsigned)(OFF_X + TS_SLOT) + (unsigned)(sl - (NSEG - 4)) * (unsigned)PT_SLOT
                                      : ((sl & 3) == 0 ? (unsigned)(PG + 3 * TS_SLOT) : (unsigned)(2 * PG + TS_SLOT - PT_SLOT) + (unsigned)(sl & 3) * (unsigned)PT_SLOT));
}
__device__ __forceinline__ unsigned op_slot(int sl) { return (unsigned)OFF_OP + (unsigned)(sl >= NSEG - 6 ? sl - (NSEG - 6) : 6 + sl % RING) * (unsigned)OP_SLOT; }
static_assert(RING == 4 && OFF_OP <= 3 * PG && OFF_RDY + 2048 <= OFF_ACC && OFF_ACC + ACC_LAYER == PG && TS_SLOT % 16384 == 0 && PT_SLOT % 16384 == 0, "pages");
static_assert(TS_SLOT == OP_SLOT && RING >= 4 && NSEG % SR == 0 && SR >= 5 && X_BYTES % 4096 == 0 && X_BYTES <= 256 * 20 * 1024, "slots");
constexpr size_t OFF_WB = al256(COMM_BYTES);
constexpr size_t OFF_BNRM = OFF_WB + (size_t)256 * 100 * 1024;
constexpr size_t OFF_LAT = OFF_BNRM + 1024;
constexpr size_t OFF_XNRM = OFF_LAT + 2048;
constexpr size_t OFF_WQKVBT = OFF_XNRM + al256(S_ * 16);
constexpr size_t OFF_WOBT = OFF_WQKVBT + al256((size_t)3 * DM * DM * 2);
constexpr size_t OFF_XN = OFF_WOBT + al256((size_t)DM * DM * 2);
constexpr int QST = BD;
constexpr size_t OFF_XA = OFF_XN + al256((size_t)S_ * DM * 2);
constexpr size_t OFF_XB = OFF_XA + al256((size_t)S_ * DM * 2);
constexpr size_t OFF_QB = OFF_XB + al256((size_t)S_ * DM * 2);
constexpr int KVT = 2624;
constexpr size_t OFF_KV = OFF_QB + al256((size_t)BH * S_ * QST * 2);
constexpr size_t OFF_OB = OFF_KV + al256((size_t)BH * 64 * KVT);
constexpr size_t OFF_DBG = OFF_OB + al256((size_t)S_ * DM * 2);
constexpr size_t WS_TOTAL = OFF_DBG + (size_t)2 * 64 * 8 * DM * 2;
#define WS_AL 2097152
#define WS_OFF 0
static_assert(WS_TOTAL + WS_AL + ((size_t)WS_OFF << 20) <= ((size_t)256 << 20), "workspace budget");

#if defined(__HIP_DEVICE_COMPILE__)
#pragma clang attribute push(__attribute__((target("no-packed-fp32-ops"))), apply_to = function)
#endif
__device__ __forceinline__ float u2f(unsigned v) { return __builtin_bit_cast(float, v); }
__device__ __forceinline__ unsigned f2u(float v) { return __builtin_bit_cast(unsigned, v); }
__device__ __forceinline__ float bf2f(unsigned v) { return __builtin_bit_cast(float, v << 16); }
__device__ __forceinline__ unsigned f2bf(float f) { __bf16 b = (__bf16)f; return (unsigned)__builtin_bit_cast(u16, b); }
__device__ __forceinline__ float rbf(float f) { return bf2f(f2bf(f)); }
typedef __attribute__((ext_vector_type(2))) __bf16 hbf16x2;
__device__ __forceinline__ unsigned pack2(float lo, float hi) { f32x2 v = {lo, hi}; hbf16x2 b = __builtin_convertvector(v, hbf16x2); return __builtin_bit_cast(unsigned, b); }
__device__ __forceinline__ f32x4 mfma32(bf16x8 a, bf16x8 b, f32x4 c) { return __builtin_amdgcn_mfma_f32_16x16x32_bf16(a, b, c, 0, 0, 0); }
typedef __attribute__((ext_vector_type(8))) __bf16 hbf16x8;
typedef __attribute__((ext_vector_type(16))) __bf16 hbf16x16;
__device__ __forceinline__ f32x4 smfma64(bf16x8 a, bf16x16 b, f32x4 c, int idx) {
  return __builtin_amdgcn_smfmac_f32_16x16x64_bf16(__builtin_bit_cast(hbf16x8, a), __builtin_bit_cast(hbf16x16, b), c, idx, 0, 0);
}
__device__ __forceinline__ bf16x16 cat16(bf16x8 a, bf16x8 b) { return __builtin_shufflevector(a, b, 0, 1, 2, 3, 4, 5, 6, 7, 8, 9, 10, 11, 12, 13, 14, 15); }
__device__ __forceinline__ bf16x8 half8(bf16x16 v, int hi) { return hi ? __builtin_shufflevector(v, v, 8, 9, 10, 11, 12, 13, 14, 15) : __builtin_shufflevector(v, v, 0, 1, 2, 3, 4, 5, 6, 7); }
__device__ __forceinline__ bf16x8 mk8(unsigned a, unsigned b, unsigned c, unsigned d) { u32x4 v = {a, b, c, d}; return __builtin_bit_cast(bf16x8, v); }
__device__ __forceinline__ unsigned dpp_ror8(unsigned v) { return (unsigned)__builtin_amdgcn_mov_dpp((int)v, 0x128, 0xf, 0xf, true); }
__device__ __forceinline__ float fmx(float a, float b) { return __builtin_elementwise_maximum(a, b); }

__device__ __forceinline__ int imax(int a, int b) { return a > b ? a : b; }
__device__ __forceinline__ int imin(int a, int b) { return a < b ? a : b; }
__device__ __forceinline__ unsigned umin(unsigned a, unsigned b) { return a < b ? a : b; }
__device__ __forceinline__ unsigned umax(unsigned a, unsigned b) { return a > b ? a : b; }
__device__ __forceinline__ bool wave_all(bool p) { return __builtin_amdgcn_ballot_w64(!p) == 0ull; }
__device__ __forceinline__ float x16mx(float x) { unsigned u = __builtin_bit_cast(unsigned, x); auto r = __builtin_amdgcn_permlane16_swap(u, u, false, false); return fmx(u2f(r[0]), u2f(r[1])); }
__device__ __forceinline__ float x32mx(float x) { unsigned u = __builtin_bit_cast(unsigned, x); auto r = __builtin_amdgcn_permlane32_swap(u, u, false, false); return fmx(u2f(r[0]), u2f(r[1])); }
__device__ __forceinline__ float x16sum(float x) { unsigned u = __builtin_bit_cast(unsigned, x); auto r = __builtin_amdgcn_permlane16_swap(u, u, false, false); return u2f(r[0]) + u2f(r[1]); }
__device__ __forceinline__ float x32sum(float x) { unsigned u = __builtin_bit_cast(unsigned, x); auto r = __builtin_amdgcn_permlane32_swap(u, u, false, false); return u2f(r[0]) + u2f(r[1]); }
template <int CTRL> __device__ __forceinline__ float dppf(float v) { return u2f((unsigned)__builtin_amdgcn_mov_dpp((int)f2u(v), CTRL, 0xf, 0xf, true)); }
#define OWN_PF 1
template <int BYTES> __device__ __forceinline__ void own_code(int tid, unsigned* sink) {
  if (!OWN_PF || __builtin_amdgcn_readfirstlane(tid >> 6) * 4096 >= BYTES) return;
  const char* pc; unsigned keep;
  asm volatile("s_getpc_b64 %0" : "=s"(pc));
  asm volatile("s_mov_b32 %0, m0\n\ts_mov_b32 m0, %3\n\ts_nop 0\n\tglobal_load_lds_dword %1, %2\n\ts_mov_b32 m0, %0"
               : "=&s"(keep) : "v"(tid * 64 < BYTES ? tid * 64 : 0), "s"(pc), "s"((unsigned)(uintptr_t)(LAS unsigned*)sink) : "memory");
}
constexpr int DPP_XOR1 = 0xB1, DPP_XOR2 = 0x4E, DPP_ROR4 = 0x124, DPP_ROR8 = 0x128;
#if defined(__HIP_DEVICE_COMPILE__)
#pragma clang attribute pop
#endif

typedef short v4i16_t __attribute__((ext_vector_type(4)));
__device__ __forceinline__ bf16x8 tr_frag(const char* lo_, const char* hi_) {
  const v4i16_t lo = __builtin_amdgcn_ds_read_tr16_b64_v4i16((LAS v4i16_t*)lo_), hi = __builtin_amdgcn_ds_read_tr16_b64_v4i16((LAS v4i16_t*)hi_);
  return (bf16x8){lo[0], lo[1], lo[2], lo[3], hi[0], hi[1], hi[2], hi[3]};
}
__device__ __forceinline__ int rec_wg(int gp, int m) { return (((gp & 1) * 16 + m) << 3) | (gp >> 1); }
__device__ __forceinline__ float wsum(float v) {
  v += dppf<DPP_XOR1>(v); v += dppf<DPP_XOR2>(v); v += dppf<DPP_ROR4>(v); v += dppf<DPP_ROR8>(v);
  return x32sum(x16sum(v));
}
__device__ __forceinline__ float ln_row(int lane, u32x4 (&xo)[2]) {
  const bool two = lane < 16;
  float v[16];
#pragma unroll
  for (int hf = 0; hf < 2; ++hf)
#pragma unroll
    for (int i = 0; i < 4; ++i) { const unsigned u = xo[hf][i]; v[hf * 8 + 2 * i] = bf2f(u & 0xffff); v[hf * 8 + 2 * i + 1] = bf2f(u >> 16); }
  float s0 = 0.f, s1 = 0.f;
#pragma unroll
  for (int i = 0; i < 8; ++i) { s0 += v[i]; s1 += v[8 + i]; }
  const float mean = wsum(two ? s0 + s1 : s0) * (1.0f / DM);
  s0 = s1 = 0.f;
#pragma unroll
  for (int i = 0; i < 8; ++i) { const float d0 = v[i] - mean, d1 = v[8 + i] - mean; s0 += d0 * d0; s1 += d1 * d1; }
  const float var = rbf(wsum(two ? s0 + s1 : s0) * (1.0f / DM));
  const float mb = rbf(mean);
  const float rden = 1.0f / rbf(sqrtf(rbf(var + 1e-5f)));
  float n2[2] = {0.f, 0.f};
#pragma unroll
  for (int hf = 0; hf < 2; ++hf) {
    float y[8];
#pragma unroll
    for (int i = 0; i < 8; ++i) { y[i] = rbf(rbf(v[hf * 8 + i] - mb) * rden); n2[hf] += y[i] * y[i]; }
    xo[hf] = (u32x4){pack2(y[0], y[1]), pack2(y[2], y[3]), pack2(y[4], y[5]), pack2(y[6], y[7])};
  }
  return wsum(two ? n2[0] + n2[1] : n2[0]);
}

#define PROT(k)
#define WARM_CODE 1
#define B_AUX 16
#define X_AUX 16
#define T_AUX 16
#define PF_POS 0
#define PRB_N0 3
#define PRB_N1 0
#define PRB_LOG(i, v)
#define PLD(p) __builtin_nontemporal_load((const u32x4*)(p))
struct ProArgs { const u16* x; const u16* wq; const u16* wk; const u16* wv; const u16* wo; const u16* wqkvb; const u16* wob; char* ws; };
__global__ __launch_bounds__(640, 3) void prologue_kernel(ProArgs a) {
  __shared__ __attribute__((aligned(16))) char img[192 * 320];
  __shared__ __attribute__((aligned(16))) char slots[10 * 6144];
  __shared__ float red[10][160];
  __shared__ unsigned latw[8];
  __shared__ u32x4 xst[2][640];
  const int b = blockIdx.x, tid = threadIdx.x, lane = tid & 63, v = __builtin_amdgcn_readfirstlane(tid >> 6), c = lane & 15, g = lane >> 4;
  const int h = (b & 7) * 4 + ((b >> 3) & 3), dh = (b >> 5) & 1, cq = b >> 6, ks = dh * 10 + v;
  char* const slot = slots + v * 6144;
  PROT(0);
  __shared__ unsigned sink[64];
  own_code<9600>(tid, sink);
  const unsigned zo = v < 8 ? (unsigned)(((b >> 6) * 512 + ((b & 7) * 2 + (v >> 2)) * 32 + ((b >> 3) & 7) * 4 + (v & 3)) * 1024) : (unsigned)(((v - 7) * 2048 + (b & 7) * 32 + ((b >> 3) & 7) * 4 + (b >> 6)) * 1024);
  __builtin_amdgcn_raw_buffer_store_b128((u32x4){0u, 0u, 0u, 0u}, __builtin_amdgcn_make_buffer_rsrc(a.ws, 0, (int)(3 * PG), 0x00020000), zo + lane * 16, 0, 16);
  __builtin_amdgcn_sched_barrier(0);
  u32x4 wa[2][6], wb[6], tr[4], xo[2];
  const int xrow = b * 8 + (v & 7);
  xo[0] = *(const u32x4*)(a.x + (size_t)xrow * DM + lane * 8);
  xo[1] = *(const u32x4*)(a.x + (size_t)xrow * DM + 512 + c * 8);
  const int job = imin(b + 256 * (9 - v), 800);
  const int jn = job < 600 ? job / 20 : (job % 200) / 20, jk = job % 20;
  {
    const u16* src = job < 600 ? a.wqkvb + (size_t)(32 * jk) * 3 * DM : a.wob + (size_t)(32 * jk) * DM;
#pragma unroll
    for (int i = 0; i < 4; ++i) tr[i] = *(const u32x4*)(src + (size_t)(i * 8 + (lane >> 3)) * (job < 600 ? 3 * DM : DM) + jn * 64 + (lane & 7) * 8);
  }
#pragma unroll
  for (int i = 0; i < 6; ++i) {
    const int idx = (v * 6 + i) * 64 + lane, row = idx / 20;
    wb[i] = PLD(a.wo + (size_t)(h * DH + row) * DM + cq * 160 + (idx - row * 20) * 8);
  }
#pragma unroll
  for (int i = 0; i < 6; ++i) {
    const int idx = i * 64 + lane, r = idx / 24;
#pragma unroll
    for (int u = 0; u < 2; ++u) wa[u][i] = PLD(a.wv + (size_t)(32 * ks + 8 * (r >> 2) + (r & 3) + 4 * u) * HD + h * DH + (idx - r * 24) * 8);
  }
  unsigned pf[2] = {0u, 0u};
  auto prefetch = [&]() {
#pragma unroll
    for (int i = 0; i < 2; ++i) pf[i] = *(const unsigned*)((i ? a.wq : a.wk) + ((size_t)(b * 10 + v) * 48 + imin(lane, 47)) * 32);
  };
  if (PF_POS == 0) prefetch();
  __builtin_amdgcn_sched_barrier(0);
  unsigned lat = ~0u;
  auto probe = [&](int n0, int n) {
    const char* p = a.ws + zo;
#pragma unroll 1
    for (int i = n0; i < n0 + n; ++i) {
      u64 t0, t1; unsigned d;
      asm volatile("s_memtime %0\n\ts_waitcnt lgkmcnt(0)\n\ts_load_dword %2, %3, %4\n\ts_waitcnt lgkmcnt(0)\n\ts_memtime %1\n\ts_waitcnt lgkmcnt(0)"
                   : "=&s"(t0), "=&s"(t1), "=&s"(d) : "s"(p), "s"((i & 3) * 256 + (i >> 2) * 128) : "memory");
      PRB_LOG(i, (unsigned)t1 - (unsigned)t0);
      lat = umin(lat, (unsigned)t1 - (unsigned)t0);
    }
  };
  if (PRB_N0) { asm volatile("s_waitcnt vmcnt(26)" ::: "memory"); probe(0, PRB_N0); }
  __builtin_amdgcn_sched_barrier(0);
  auto xput = [&](int k) {
    if (v < 8) { xst[k][lane * 8 + (v ^ (lane & 7))] = xo[0]; if (lane < 16) xst[k][(64 + lane) * 8 + (v ^ (lane & 7))] = xo[1]; }
  };
  xput(0);
  const float xn2 = u2f(__builtin_amdgcn_readfirstlane(f2u(ln_row(lane, xo))));
  xput(1);
#pragma unroll
  for (int i = 0; i < 4; ++i) *(u32x4*)(slot + (i * 8 + (lane >> 3)) * 144 + (lane & 7) * 16) = tr[i];
  __builtin_amdgcn_fence(__ATOMIC_RELEASE, "wavefront"); __builtin_amdgcn_wave_barrier();
  bf16x8 tf[4];
#pragma unroll
  for (int t = 0; t < 4; ++t) {
    const char* bp = slot + (8 * g + (c >> 2)) * 144 + t * 32 + (c & 3) * 8;
    tf[t] = tr_frag(bp, bp + 4 * 144);
  }
  __builtin_amdgcn_fence(__ATOMIC_RELEASE, "wavefront"); __builtin_amdgcn_wave_barrier();
  __builtin_amdgcn_sched_barrier(0);
#pragma unroll
  for (int i = 0; i < 6; ++i) {
    const int idx = (v * 6 + i) * 64 + lane, row = idx / 20;
    *(u32x4*)(img + row * 320 + (((idx - row * 20) ^ ((row >> 2) & 2)) * 16)) = wb[i];
  }
  bf16x8 A[2][6];
#pragma unroll
  for (int u = 0; u < 2; ++u) {
#pragma unroll
    for (int i = 0; i < 6; ++i) {
      const int idx = i * 64 + lane, r = idx / 24;
      *(u32x4*)(slot + r * 384 + (((idx - r * 24) ^ (r >> 1)) * 16)) = wa[u][i];
    }
    __builtin_amdgcn_fence(__ATOMIC_RELEASE, "wavefront"); __builtin_amdgcn_wave_barrier();
#pragma unroll
    for (int e = 0; e < 6; ++e) A[u][e] = *(const bf16x8*)(slot + c * 384 + (((4 * e + g) ^ (c >> 1)) * 16));
    __builtin_amdgcn_fence(__ATOMIC_RELEASE, "wavefront"); __builtin_amdgcn_wave_barrier();
  }
  BAR();
  PROT(1);
  if (PF_POS == 0) asm volatile("" :: "v"(pf[0]), "v"(pf[1]));
  if (PF_POS == 1) prefetch();
  {
    const unsigned xf = (unsigned)((b >> 1) * 20480 + (tid >> 3) * 256 + (b & 1) * 128 + (tid & 7) * 16);
    const __amdgpu_buffer_rsrc_t rx = __builtin_amdgcn_make_buffer_rsrc(a.ws + OFF_XN, 0, (int)(OFF_QB - OFF_XN), 0x00020000);
    __builtin_amdgcn_raw_buffer_store_b128(xst[1][tid ^ ((tid >> 3) & 7)], rx, xf, 0, X_AUX);
    __builtin_amdgcn_raw_buffer_store_b128(xst[0][tid ^ ((tid >> 3) & 7)], rx, xf + (unsigned)(OFF_XB - OFF_XN), 0, X_AUX);
    if (v < 8 && lane < 4) ((float*)(a.ws + OFF_XNRM))[xrow * 4 + lane] = lane == 0 ? xn2 : 0.f;
  }
  if (job < 800) {
    const __amdgpu_buffer_rsrc_t rt = __builtin_amdgcn_make_buffer_rsrc(a.ws + (job < 600 ? OFF_WQKVBT : OFF_WOBT) + ((size_t)jn * 80 + jk) * 1024, 0, 81920, 0x00020000);
#pragma unroll
    for (int t = 0; t < 4; ++t) __builtin_amdgcn_raw_buffer_store_b128(__builtin_bit_cast(u32x4, tf[t]), rt, lane * 16 + t * 20480, 0, T_AUX);
  }
  const int gp = h >> 1, hh = h & 1;
  const __amdgpu_buffer_rsrc_t rwb = __builtin_amdgcn_make_buffer_rsrc(a.ws + OFF_WB, 0, 256 * 100 * 1024, 0x00020000);
#pragma unroll
  for (int n = 0; n < 10; ++n) {
    const int off = n * 32 + 8 * (c & 3), T = cq * 10 + n;
    const char* bp = img + (8 * g + (c >> 2)) * 320 + (off ^ ((g & 1) << 5));
    f32x4 ac[2] = {{0.f, 0.f, 0.f, 0.f}, {0.f, 0.f, 0.f, 0.f}};
#pragma unroll
    for (int e = 0; e < 6; ++e) {
      const bf16x8 bf = tr_frag(bp + e * 32 * 320, bp + (e * 32 + 4) * 320);
      ac[0] = mfma32(A[0][e], bf, ac[0]); ac[1] = mfma32(A[1][e], bf, ac[1]);
    }
    const u32x4 o = {pack2(ac[0][0], ac[0][1]), pack2(ac[0][2], ac[0][3]), pack2(ac[1][0], ac[1][1]), pack2(ac[1][2], ac[1][3])};
    const unsigned oo = T < 32 ? (unsigned)((rec_wg(gp, T & 15) * 100 + (hh * 3 + (T >> 4)) * 20 + ks) * 1024 + lane * 16)
                               : (unsigned)((rec_wg(gp, 2 * (T - 32) + (c >> 3)) * 100 + 40 + ks) * 1024 + (g * 16 + hh * 8 + (c & 7)) * 16);
    __builtin_amdgcn_raw_buffer_store_b128(o, rwb, oo, 0, B_AUX);
    float n2 = 0.f;
#pragma unroll
    for (int r = 0; r < 4; ++r) n2 += ac[0][r] * ac[0][r] + ac[1][r] * ac[1][r];
    n2 = x32sum(x16sum(n2));
    if (g == 0) red[v][n * 16 + c] = n2;
  }
  PROT(2);
  if (PF_POS == 1) asm volatile("" :: "v"(pf[0]), "v"(pf[1]));
  probe(PRB_N0, PRB_N1);
  if (v < 8) latw[v] = lat;
  else if (b < 64 && lane == 0) ((unsigned*)(a.ws + OFF_LAT))[(v - 4) * 64 + b] = umin(lat, 60000u);
  if (WARM_CODE && v == 7) {
    const char* pc; int d0, d1;
    asm("s_getpc_b64 %0\n\ts_mov_b32 %1, _Z10rec_kernel7RecArgs@rel32@lo+4\n\ts_mov_b32 %2, _Z11proj_kernelPKtS0_S0_Pt@rel32@lo+12" : "=s"(pc), "=s"(d0), "=s"(d1));
    const char *c0 = pc + d0, *c1 = pc + d1;
    const long o = ((long)b * 64 + lane) * 64;
    if (o < c1 - c0) asm volatile("" :: "v"(*(const volatile GAS unsigned*)(c0 + o)));
  }
  if (v == 9 && lane < 32) {
    const int q = b * 32 + lane;
    *(u32x4*)(a.ws + OFF_KV + (size_t)(q >> 2) * KVT + 1280 + (q & 3) * 336 + 320) = (u32x4){0x3f803f80u, 0x3f803f80u, 0x3f803f80u, 0x3f803f80u};
  }
  BAR();
  if (v == 1 && lane == 0) ((unsigned*)(a.ws + OFF_LAT))[b] = umin(umin(umin(latw[0], latw[4]) + umin(latw[1], latw[5]), 30000u) + umin(umin(latw[2], latw[6]) + umin(latw[3], latw[7]), 30000u), 60000u);
  if (v == 0) {
    float mx = 0.f;
#pragma unroll
    for (int k = 0; k < 3; ++k) {
      const int col = k * 64 + lane;
      float sm = 0.f;
#pragma unroll
      for (int i = 0; i < 10; ++i) sm += red[i][col < 160 ? col : 0];
      mx = fmx(mx, sm);
    }
    mx = fmx(mx, dppf<DPP_XOR1>(mx)); mx = fmx(mx, dppf<DPP_XOR2>(mx)); mx = fmx(mx, dppf<DPP_ROR4>(mx)); mx = fmx(mx, dppf<DPP_ROR8>(mx));
    mx = x32mx(x16mx(mx));
    if (lane == 0) ((float*)(a.ws + OFF_BNRM))[(cq * 32 + h) * 2 + dh] = mx;
  }
  PROT(3);
}

struct RecArgs {
  const u16* mem0; const u16* wq; const u16* wk; const u16* xold; u16* xnew; char* ws;
  unsigned layer; unsigned pad;
};
#define RLX_AGENT __ATOMIC_RELAXED, __HIP_MEMORY_SCOPE_AGENT
constexpr unsigned SPIN_CAP = 1u << 20;
#define TSTAMP(k, v)
#define ARRIVE(k)
#define ARRIVE_OUT()
#define ARRIVE_DECL()
#define LOGV(k, v)
#define LOGF(k, v)
#define LOGP(k)
#define LOGQ(k)
#define LOGF2(k)
#define LOGD(k)
#define LOGT(k, v)
#define STAMP_DECL()
#define STAMP(k)
#define STAMPV(k, v)
#define STAMP_OUT()
#define REC_D1 520
#define SCH_V 16
#define SCH_VMIN 0
#define SCH_DMIN 4
#define SCH_PEN 20
#define SCH_VMIN2 (-4)
#define SCH_PEN2 40
#define SCH_GU 1
#define SCH_GD 2
#define SCH_UP 6
#define SCH_D0 196
#define SCH_FIRST 120
#define EXP_NODMA 0
#define BG_HOLD 220
#define QBATCH 0
#define WPF_N 5
#define WPF_W 0
#define WPF0 (63 - WPF_N)
#define BG_HOLD4 250
#define D1_W1 20
#define F3_D 1200
#define T2W 2
#define AT_ASM 1
#define PM_Z 1
#define P2_X 1
#define ST_KS 14
#define ST_KS0 ST_KS
#define ST_KS1 ST_KS
#define PADR 0x6666
#define PADT 0x000
#define CODE_PAD(n) asm volatile(".p2align 6\n.rept %0\ns_nop 0\n.endr" :: "n"(n))
#define ST_X 300
#define ST_ALL 4
#define CPF_S 61
#define FR_D 700
#define FR_PACE 6
#define END_E60 600
#define END_E1 1300
#define END_E2 3400
#define END_E62 2800
#define END_E3 6800
#define END_E4 11400
#define EARLY_N 4
#define EARLY_D1 0
#define EARLY_V 4
#define EARLY_V1 10
#define F3 0
#define F3A 0
#define F3A_D 400
#define T4_D 300
#define T4_P 1
#define P2_W 1
#define PS_SL 1
#define F2B 4
#define F0C 0
#define F2C 0
#define W2_P 1
#define SCH_W 1
#define SP_SL 0
#define D1_M 300
#define D1_MIN 260
#define D1_C 96
#define D1_G 2
#define BG_HOLD6 0
#define W2_D 0
#define END_E61 2600
#define D1_LATE 200
#define P2_G0 10
#define P2_G2 6
#define P2_G3 6
#define SCH_RL 0
#define SCH_P 3
#define MMA_P 0
#define SP_P 0
#define F6 8
#define F7 10
#define XPF_W 2
#define SCH_LATE 0
#define LATE_V 6
#define QW 3
#define FB_SL 0
#define P2_G1 1
#define P1_G 0
#define F4Z 0
#define F5Z 0
#define T5_Z 2
#define F0A 3
#define FB_E 0
#define EXACT 1
#define D1_P 3
#define VO_A0 9
#define VO_A2 10
#define VO_B0 3
#define VO_B1 0
#define VO_N2 7
#define VO_N3 10
#define VO_W -1
#define UNIT 0
#define UN_P 2
#define VP_E 1
#define TS_B 0
#define ST_E 1
#define W1_SL 0
#define VO_A1 4
#define LAST_V 18
#define END_E59 3200
#define END_G2 600
#define END_G3 600
#define QR 4
#define WPF_S 128
#define END_SL 4
#define SCH_BX 0
#define SCH_BH 40
#define RETRY_T 115
#define RETRY_G 12
#define RETRY_L 20
#define RETRY_D 300
#define GO_T 10
#define KP0 0
#define KP1 0
#define XPF 3
#define W1_D 0
#define F4 12
#define F5 0
#define F67 0
#define KP2 0
#define REC_RNG 70u
#define SCH_DRAIN 40
#define PL_X 0
#define FC(k)
#define PIN_V(x) asm volatile("" : "+v"(x))
#define PIN_S(x) asm volatile("" : "+s"(x))
#define VM_DRAIN() do { asm volatile("s_waitcnt vmcnt(0)" ::: "memory"); __builtin_amdgcn_s_waitcnt(0x0F70); } while (0)
#define SC1_LOAD(off) __builtin_amdgcn_raw_buffer_load_b128(rs, (off), 0, 16)
#define SC1_LOADS(off, soff) __builtin_amdgcn_raw_buffer_load_b128(rs, (off), (soff), 16)
#define SC1_STORE8S(v, off, soff) __builtin_amdgcn_raw_buffer_store_b64((v), rs, (off), (soff), 16)
#define SC1_LOAD8(off) __builtin_amdgcn_raw_buffer_load_b64(rs, (off), 0, 16)
#define SC1_STORE(v, off) __builtin_amdgcn_raw_buffer_store_b128((v), rs, (off), 0, 16)
#define SC1_STORE8(v, off) __builtin_amdgcn_raw_buffer_store_b64((v), rs, (off), 0, 16)
#if defined(__HIP_DEVICE_COMPILE__)
#pragma clang attribute push(__attribute__((target("no-packed-fp32-ops"))), apply_to = function)
#endif
__device__ __forceinline__ unsigned now32() { return (unsigned)__builtin_amdgcn_s_memrealtime(); }
__device__ __forceinline__ void wait_until(unsigned t) { for (;;) { const int d = (int)(t - now32()); if (d <= 0) break; if (d > 8) __builtin_amdgcn_s_sleep(1); } }
__device__ __forceinline__ unsigned cyc32() { return (unsigned)__builtin_amdgcn_s_memtime(); }
__device__ __forceinline__ void wait_cyc(unsigned t) { for (;;) { const int d = (int)(t - cyc32()); if (d <= 0) break; if (d > 200) __builtin_amdgcn_s_sleep(1); } }
#define WAIT_ASM(clk, far_) "1:\n\t" clk " vcc\n\ts_waitcnt lgkmcnt(0)\n\ts_sub_i32 %0, %1, vcc_lo\n\ts_cmp_lt_i32 %0, " far_ "\n\ts_cbranch_scc1 3f\n\ts_sleep 1\n\ts_branch 1b\n" \
                            "2:\n\t" clk " vcc\n\ts_waitcnt lgkmcnt(0)\n\ts_sub_i32 %0, %1, vcc_lo\n3:\n\ts_cmp_gt_i32 %0, 0\n\ts_cbranch_scc1 2b"
__device__ __forceinline__ int spin_until(unsigned t) { int d; asm volatile(WAIT_ASM("s_memrealtime", "9") : "=&s"(d) : "s"(t) : "vcc", "scc", "memory"); return d; }
__device__ __forceinline__ int spin_cyc(unsigned t) { int d; asm volatile(WAIT_ASM("s_memtime", "200") : "=&s"(d) : "s"(t) : "vcc", "scc", "memory"); return d; }
__device__ __forceinline__ void spin_exact(unsigned t) {
  if (!EXACT) { spin_cyc(t); return; }
  int d;
  asm volatile("1:\n\ts_memtime vcc\n\ts_waitcnt lgkmcnt(0)\n\ts_sub_i32 %0, %1, vcc_lo\n\ts_cmp_gt_i32 %0, 95\n\ts_cbranch_scc1 1b\n\t"
               "s_max_i32 %0, %0, 0\n\ts_andn2_b32 %0, %0, 3\n\ts_sub_i32 %0, 3f-2f, %0\n\ts_getpc_b64 vcc\n"
               "2:\n\ts_add_u32 vcc_lo, vcc_lo, %0\n\ts_addc_u32 vcc_hi, vcc_hi, 0\n\ts_setpc_b64 vcc\n\t.rept 24\n\ts_nop 0\n\t.endr\n3:"
               : "=&s"(d) : "s"(t) : "scc", "vcc", "memory");
}
template <int OFF, int SL> __device__ __forceinline__ void lds_spin(unsigned lds0, unsigned want) {
  unsigned v;
  asm volatile("1:\n\tds_read_b32 %0, %1 offset:%3\n\t.if %4\n\ts_sleep %4\n\t.endif\n\ts_waitcnt lgkmcnt(0)\n\tv_cmp_gt_u32 vcc, %2, %0\n\ts_cbranch_vccnz 1b"
               : "=&v"(v) : "v"(lds0), "s"(want), "n"(OFF), "n"(SL) : "vcc", "memory");
}
template <int CTRL> __device__ __forceinline__ int dppi(int v) { return __builtin_amdgcn_mov_dpp(v, CTRL, 0xf, 0xf, true); }
__device__ __forceinline__ int wave_imax(int l) {
  l = imax(l, dppi<DPP_XOR1>(l)); l = imax(l, dppi<DPP_XOR2>(l)); l = imax(l, dppi<DPP_ROR4>(l)); l = imax(l, dppi<DPP_ROR8>(l));
  int a = imax(__builtin_amdgcn_readlane(l, 0), __builtin_amdgcn_readlane(l, 16)), b = imax(__builtin_amdgcn_readlane(l, 32), __builtin_amdgcn_readlane(l, 48));
  PIN_S(a); PIN_S(b);
  return imax(a, b);
}
__device__ __forceinline__ void glds16(const void* gsrc, unsigned lds_dst) {
  unsigned keep;
  asm volatile("s_mov_b32 %0, m0\n\ts_mov_b32 m0, %2\n\ts_nop 0\n\tglobal_load_lds_dwordx4 %1, off\n\ts_mov_b32 m0, %0" : "=&s"(keep) : "v"(gsrc), "s"(lds_dst) : "memory");
}
__device__ __forceinline__ void glds16_sc1(const void* gsrc, unsigned lds_dst) {
  unsigned keep;
  asm volatile("s_mov_b32 %0, m0\n\ts_mov_b32 m0, %2\n\ts_nop 0\n\tglobal_load_lds_dwordx4 %1, off sc1\n\ts_mov_b32 m0, %0" : "=&s"(keep) : "v"(gsrc), "s"(lds_dst) : "memory");
}
__device__ __forceinline__ unsigned acc_off(int G, int row) {
  return (unsigned)(G < 128 ? ((G & 31) | ((G & 32) << 1) | ((G & 64) >> 1)) * 256 + row * 16 : (2 * G - 128 + (row >> 2)) * 256 + (row & 3) * 16);
}
__device__ __forceinline__ unsigned nz(unsigned v) { return v > 1u ? v : 1u; }
__device__ __forceinline__ unsigned pack2n(float lo, float hi) { return nz(pack2(lo, hi)); }
__device__ __forceinline__ unsigned min4(u32x4 v) { return umin(umin(v[0], v[1]), umin(v[2], v[3])); }
__device__ __forceinline__ bf16x8 as8(u32x4 v) { return __builtin_bit_cast(bf16x8, v); }

constexpr int NV = 7, NKQ = 2;
constexpr int L_RED1 = 0;
constexpr int L_BM = L_RED1 + 8 * 1536;
constexpr int L_STG = L_BM + 10240;
constexpr int L_ST = L_STG + 2048 + 2560;
constexpr int L_MISC = L_ST + 512;
constexpr int L_KX = L_MISC + 128;
constexpr int L_QX = L_KX + NKQ * 2048;
constexpr int L_XN = L_QX + NKQ * 2048;
constexpr int L_VOX = L_XN + 81920;
constexpr int L_VOM = L_VOX + NV * 5120;
constexpr int L_BYTES = L_VOM + NV * 1280;
constexpr int S_IMG = L_XN + 40960;
static_assert(S_IMG + 61440 <= L_VOM, "start-up image");
static_assert(L_BYTES <= 160 * 1024, "LDS budget");

__device__ __forceinline__ bf16x8 wqk_frag(const char* lds, int t, int ks, int lane) {
  const int c = lane & 15, g = lane >> 4, p = c & 3;
  const char* q = lds + S_IMG + (32 * ks + 8 * g + (c >> 2)) * 96 + 8 * p + (t == 0 ? 0 : (t == 1 ? 48 : (p < 2 ? 32 : 64)));
  const v4i16_t lo = __builtin_amdgcn_ds_read_tr16_b64_v4i16((LAS v4i16_t*)q), hi = __builtin_amdgcn_ds_read_tr16_b64_v4i16((LAS v4i16_t*)(q + 4 * 96));
  return (bf16x8){lo[0], lo[1], lo[2], lo[3], hi[0], hi[1], hi[2], hi[3]};
}
__device__ __forceinline__ bf16x8 sel_frag(int c, int base) {
  bf16x8 r;
#pragma unroll
  for (int i = 0; i < 8; ++i) r[i] = (c == base + i) ? (short)0x3f80 : (short)0;
  return r;
}
__device__ __forceinline__ bf16x8 qk_frag(const char* p4, const char* p2, const char* p2b) {
  f32x4 a0 = *(const f32x4*)(p4), a1 = *(const f32x4*)(p4 + 1792), a2 = *(const f32x4*)(p4 + 3584), a3 = *(const f32x4*)(p4 + 5376);
  f32x2 b0 = *(const f32x2*)(p2), b1 = *(const f32x2*)(p2 + 1792), b2 = *(const f32x2*)(p2b), b3 = *(const f32x2*)(p2b + 1792);
  f32x4 va = (a0 + a1) + (a2 + a3);
  f32x2 vb = (b0 + b1) + (b2 + b3);
  return mk8(pack2(va[0], va[1]), pack2(va[2], va[3]), pack2(vb[0], vb[1]), 0u);
}
__device__ __forceinline__ void x_fetch(const u16* src, unsigned lds0, int seg, int p, int lane) {
  if (EXP_NODMA) return;
  glds16_sc1(src + (size_t)p * 512 + lane * 8, lds0 + L_XN + (seg & 1) * 40960 + p * 1024);
}
template <int N> __device__ __forceinline__ void x_fetch_n(const u16* src, unsigned lds0, int seg, int p0, unsigned voff) {
  if (EXP_NODMA) return;
#pragma unroll
  for (int i = 0; i < N; i += 4) {
    unsigned keep;
    asm volatile("s_mov_b32 %0, m0\n\ts_mov_b32 m0, %3\n\ts_nop 0\n\tglobal_load_lds_dwordx4 %1, %2 sc1\n\t"
                 ".if %4 > 1\n\tglobal_load_lds_dwordx4 %1, %2 offset:1024 sc1\n\t.endif\n\t.if %4 > 2\n\tglobal_load_lds_dwordx4 %1, %2 offset:2048 sc1\n\t.endif\n\t"
                 ".if %4 > 3\n\tglobal_load_lds_dwordx4 %1, %2 offset:3072 sc1\n\t.endif\n\ts_mov_b32 m0, %0"
                 : "=&s"(keep) : "v"(voff), "s"(src + (size_t)(p0 + i) * 512), "s"(lds0 + L_XN + (seg & 1) * 40960 + (p0 + i) * 1024), "n"(N - i < 4 ? N - i : 4) : "memory");
  }
}
__device__ __forceinline__ int qkv_row(int b) { return (b & 7) / QR * (2 * QR) + (b >> 3) / (16 / QR); }
__device__ __forceinline__ int qkv_col(int b) { return (b & 7) % QR * (16 / QR) + (b >> 3) % (16 / QR); }
__device__ __forceinline__ const u16* x_src(const RecArgs& a, int seg, int b) {
  return seg < NSEG ? (const u16*)(a.ws + OFF_XN) + (size_t)seg * 32 * DM : a.xnew + (size_t)(qkv_row(b) + 16 * (seg - NSEG)) * 32 * DM;
}
template <int N>
__device__ __forceinline__ void img_fetch(const RecArgs& a, unsigned lds0, int h, int j, int part, int lane) {
#pragma unroll
  for (int i = 0; i < N; ++i) {
    const int n = part * N + i, idx = n * 64 + lane, row = idx / 6, pc = idx - row * 6;
    if (FR_PACE && N == 20) asm volatile("s_waitcnt vmcnt(%0)" :: "n"(FR_PACE) : "memory");
    glds16((pc < 3 ? a.wq : a.wk - 24) + (size_t)row * HD + h * DH + j * 24 + pc * 8, lds0 + S_IMG + n * 1024);
  }
}
constexpr int P2B = F4 + F5 + 2 * F67 + F6 + F7, PZ = P2B + F2B, PA = PZ, P0C = PA + 2 * F0A, F3C = 40 - F3A - F3 - P0C - 2 * F0C - F2C;
static_assert(F3C >= 0, "fetch plan");
template <int OFF> __device__ __forceinline__ void lds_inc(unsigned lds0) {
  asm volatile("s_mov_b64 exec, 1\n\tds_add_u32 %0, %1 offset:%2\n\ts_mov_b64 exec, -1" :: "v"(lds0), "v"(1u), "n"(OFF) : "memory");
}
__device__ __forceinline__ void lds_arrive(unsigned* p, int lane) {
  asm volatile("s_waitcnt lgkmcnt(0)" ::: "memory");
  if (lane == 0) __hip_atomic_fetch_add(p, 1u, __ATOMIC_RELAXED, __HIP_MEMORY_SCOPE_WORKGROUP);
}
__device__ __forceinline__ void lds_wait(unsigned* p, unsigned want) {
  while (__hip_atomic_load(p, __ATOMIC_RELAXED, __HIP_MEMORY_SCOPE_WORKGROUP) < want) __builtin_amdgcn_s_sleep(1);
  asm volatile("" ::: "memory");
}
constexpr float KEXP = 0.07216878364870322f * 1.4426950408889634f;
#define FRESH_LANE() int lane = lane0; asm volatile("" : "+v"(lane)); const int c = lane & 15, g = lane >> 4; (void)c; (void)g

struct OutTask {
  u32x4 y[4]; u32x2 xo, out;
  bf16x8 sel;
  unsigned t5f, t5o;
  __device__ __forceinline__ void init(int b, int lane) {
    const int um = b / 10, uq = (b / 5) & 1, up = b % 5, tok = lane & 15, k = up < 4 ? (up >> 1) * 256 + um * 16 + (up & 1) * 8 : 512 + um * 8, q4 = ((lane >> 4) & 1) * 4;
    t5f = (unsigned)(((uq * 20 + (k >> 5)) * 64 + ((k & 31) >> 3) * 16 + tok) * 8 + q4);
    t5o = (unsigned)(((um * 2 + uq) * 16 + (lane >> 4)) * 1280 + up * 256 + tok * 16);
    if (b >= 160) { t5f = 0u; t5o = 0x7ffffff0u; }
    sel = sel_frag(tok, 0);
    PIN_V(sel);
  }
  static __device__ __forceinline__ bool ok(int sl, int b) { return sl >= 0 && b < 160; }
  __device__ __forceinline__ void load(const RecArgs& a, const __amdgpu_buffer_rsrc_t rs, int sl, int b, int part = 3) {
    const unsigned o = t5o | (sl < 0 ? 0x7ff00000u : 0u);
    sl = imax(sl, 0);
#pragma unroll
    for (int k = 0; k < 4; ++k) if (part & 1) y[k] = SC1_LOADS(o, op_slot(sl) + k * 5120);
    if (part & 2) xo = __builtin_bit_cast(u32x2, __hip_atomic_load((const gu64*)(a.xold + (size_t)sl * 32 * DM + t5f), RLX_AGENT));
  }
  __device__ __forceinline__ bool ready(int b) { return b >= 160 || wave_all(umin(umin(min4(y[0]), min4(y[1])), umin(min4(y[2]), min4(y[3]))) != 0u); }
  __device__ __forceinline__ void run(int lane) {
    f32x4 d = {0.f, 0.f, 0.f, 0.f};
#pragma unroll
    for (int k = 0; k < 4; ++k) d = mfma32(sel, as8(y[k]), d);
#pragma unroll
    for (int i = 0; i < 2; ++i) {
      const unsigned r = pack2(d[2 * i], d[2 * i + 1]), x = xo[i];
      out[i] = pack2(u2f(x << 16) + u2f(r << 16), u2f(x & 0xffff0000u) + u2f(r & 0xffff0000u));
    }
    PIN_V(out);
  }
  __device__ __forceinline__ void store(const RecArgs& a, int sl, int b, int lane) {
    __builtin_amdgcn_raw_buffer_store_b64(out, __builtin_amdgcn_make_buffer_rsrc(a.xnew, 0, ok(sl, b) ? S_ * DM * 2 : 0, 0x00020000), (t5f + (unsigned)sl * 32 * DM) * 2u | (unsigned)(lane & 32) << 25, 0, 16);
  }
};

struct PvTask2 {
  u32x4 y[2][2], pa[2], pb[2];
  unsigned lo, lm, vr, ma, mz, so, sz;
  __device__ __forceinline__ void init(int gp, int lane) {
    const int hi = lane >> 5;
    lo = pt_tok(2 * gp) + (unsigned)(lane * 16); lm = pt_mem(2 * gp + hi) + (unsigned)((lane & 31) * 8);
    vr = (unsigned)(L_VOX + 2048 + (lane ^ 8) * 16);
    ma = (unsigned)(L_VOM + hi * 768 + (lane & 31) * 8); mz = (unsigned)(L_VOM + 512 + ((lane & 31) ^ (hi * 8)) * 8);
    so = (unsigned)(L_STG + 2048 + (hi * 16 + (lane & 15)) * 16 + ((lane >> 4) & 1) * 8);
    sz = hi ? (unsigned)(L_RED1 + 8704 + lane * 8) : so + 1024;
    PIN_V(lo); PIN_V(lm); PIN_V(vr); PIN_V(ma); PIN_V(mz); PIN_V(so); PIN_V(sz);
#pragma unroll
    for (int q = 0; q < 2; ++q) { pa[q] = pb[q] = y[q][0] = y[q][1] = (u32x4){0u, 0u, 0u, 0u}; PIN_V(pa[q]); PIN_V(pb[q]); }
  }
  __device__ __forceinline__ void load(const __amdgpu_buffer_rsrc_t rs, unsigned o) {
#pragma unroll
    for (int q = 0; q < 2; ++q) {
      y[q][0] = SC1_LOADS(lo + q * 1024, o); y[q][1] = SC1_LOADS(lo + 2048 + q * 1024, o);
      const u32x2 p = __builtin_amdgcn_raw_buffer_load_b64(rs, lm + q * 256, o, 16);
      pa[q][0] = p[0]; pa[q][1] = p[1];
    }
  }
  __device__ __forceinline__ bool ready() {
    return wave_all(umin(umin(umin(min4(y[0][0]), min4(y[0][1])), umin(min4(y[1][0]), min4(y[1][1]))), umin(umin(pa[0][0], pa[0][1]), umin(pa[1][0], pa[1][1]))) != 0u);
  }
  template <bool DIRECT> __device__ __forceinline__ void run(char* lds, int sv, int lane, const __amdgpu_buffer_rsrc_t rs, unsigned og) {
    const f32x4 Z4 = {0.f, 0.f, 0.f, 0.f};
    const char* vp = lds + L_VOX + sv * 5120 + lane * 16;
    bf16x8 vx[6];
#pragma unroll
    for (int t = 0; t < 5; ++t) vx[t] = *(const bf16x8*)(vp + t * 1024);
    vx[5] = *(const bf16x8*)(lds + vr + sv * 5120);
    const u32x2 a0 = *(const u32x2*)(lds + ma + sv * 1280), a1 = *(const u32x2*)(lds + ma + sv * 1280 + 256);
    const u32x2 z0 = *(const u32x2*)(lds + mz + sv * 1280), z1 = *(const u32x2*)(lds + mz + sv * 1280 + 256);
    const bf16x8 vm = mk8(a0[0], a0[1], a1[0], a1[1]), vz = mk8(z0[0], z0[1], z1[0], z1[1]);
#pragma unroll
    for (int q = 0; q < 2; ++q) { PIN_V(pa[q]); pb[q][2] = pa[q][0]; pb[q][3] = pa[q][1]; PIN_V(pb[q]); }
    __builtin_amdgcn_sched_barrier(0);
    f32x4 o[2][3];
#pragma unroll
    for (int q = 0; q < 2; ++q) {
#pragma unroll
      for (int t = 0; t < 3; ++t) o[q][t] = mfma32(vx[t], as8(y[q][0]), Z4);
#pragma unroll
      for (int t = 0; t < 3; ++t) o[q][t] = mfma32(vx[3 + t], as8(y[q][1]), o[q][t]);
      o[q][0] = mfma32(vm, as8(pa[q]), o[q][0]); o[q][1] = mfma32(vm, as8(pb[q]), o[q][1]); o[q][2] = mfma32(vz, as8(pa[q]), o[q][2]);
    }
#pragma unroll
    for (int q = 0; q < 2; ++q) {
#pragma unroll
      for (int t = 0; t < 3; ++t) {
        const u32x2 v = {pack2n(o[q][t][0], o[q][t][1]), pack2n(o[q][t][2], o[q][t][3])};
        if (DIRECT) SC1_STORE8S(v, (so - (L_STG + 2048) + t * 512) | (t == 2 ? (unsigned)(lane & 32) << 25 : 0u), og + q * 20480);
        else *(u32x2*)(lds + (t < 2 ? so + t * 512 : sz) + q * 1280) = v;
      }
    }
  }
};

struct TokScores {
  bf16x8 k[2], q[2]; u32x4 v[2];
  __device__ __forceinline__ void get(const char* lds, int seg, int lane) {
#pragma unroll
    for (int i = 0; i < 2; ++i) { k[i] = *(const bf16x8*)(lds + L_KX + (seg & 1) * 2048 + i * 1024 + lane * 16); q[i] = *(const bf16x8*)(lds + L_QX + (seg & 1) * 2048 + i * 1024 + lane * 16); }
  }
  __device__ __forceinline__ void run(int qt) {
    const f32x4 Z4 = {0.f, 0.f, 0.f, 0.f};
    const f32x4 d1 = mfma32(k[0], q[qt], Z4), d2 = mfma32(k[1], q[qt], Z4);
    v[qt] = (u32x4){pack2n(d1[0], d1[1]), pack2n(d1[2], d1[3]), pack2n(d2[0], d2[1]), pack2n(d2[2], d2[3])};
    PIN_V(v[qt]);
  }
  __device__ __forceinline__ void store(const __amdgpu_buffer_rsrc_t rs, unsigned off, unsigned o) {
    __builtin_amdgcn_raw_buffer_store_b128(v[0], rs, off, o, 16); __builtin_amdgcn_raw_buffer_store_b128(v[1], rs, off + 1024, o, 16);
    asm volatile("s_nop 1" ::: "memory");
  }
};

__device__ __forceinline__ void touch(const char* base, unsigned off, unsigned lds0) {
  unsigned keep;
  asm volatile("s_mov_b32 %0, m0\n\ts_mov_b32 m0, %3\n\ts_nop 0\n\tglobal_load_lds_dword %1, %2 sc1\n\ts_mov_b32 m0, %0" : "=&s"(keep) : "v"(off), "s"(base), "s"(lds0 + L_RED1 + 8192) : "memory");
}
template <int N> __device__ __forceinline__ void stouch(const char* p, unsigned& d) {
  asm volatile(".set pf_o, 0\n\t.rept %2\n\ts_load_dword %0, %1, pf_o\n\t.set pf_o, pf_o + 128\n\t.endr" : "+s"(d) : "s"(p), "n"(N) : "memory");
}
__device__ __forceinline__ void code_touch(int b, int lane, unsigned lds0) {
  const char* pc; int d0, d1;
  asm volatile("s_getpc_b64 %0\n\ts_mov_b32 %1, _Z10rec_kernel7RecArgs@rel32@lo+4\n\ts_mov_b32 %2, _Z11attb_kernelPKtS0_Pt@rel32@lo+12" : "=s"(pc), "=s"(d0), "=s"(d1));
  const int o = (lane * 32 + (b >> 3)) * 128;
  touch(pc + d0, (unsigned)(o < d1 - d0 ? o : 0), lds0);
}
__device__ __forceinline__ unsigned zero_base(int b, int h, int j, int ln) {
  return ln < 32 ? (unsigned)((h & 3) * 32768 + j * 512 + ln * 16) : ln < 40 ? (unsigned)((h & 2) * 1024 + j * 256 + (ln & 4) * 32 + (h & 1) * 64 + (ln & 3) * 16)
                 : (unsigned)OFF_ACC + (b < ACC_NB ? (unsigned)(b * 256 + (ln - 40) * 16) : ts_piece((b - ACC_NB) * 8 + ln - 40));
}
__device__ __forceinline__ unsigned sct_slot(unsigned ck, unsigned sl) { return sl * SCT_STEP + ((ck >> (sl & 4u)) & 7u) * 4096u; }
__device__ __forceinline__ unsigned scm_slot(unsigned ck, unsigned sl) { return OFF_SCM + sl * SCM_STEP + ((ck >> 8) & 7u) * 4096u; }
__device__ __forceinline__ int ck_rot(int x) { return ((x & 1) << 2) | (x >> 1); }
struct Place {
  unsigned t[8];
  __device__ __forceinline__ void load(const char* ws, int lane) {
#pragma unroll
    for (int x = 0; x < 8; ++x) t[x] = *(const unsigned*)(ws + OFF_LAT + ((imin(lane >> 3, 5) * 8 + (ck_rot(x) ^ (lane & 7))) * 8 + x) * 4);
  }
  __device__ __forceinline__ unsigned get(int b, int lane) {
    int c = (int)((((t[0] + t[1]) + (t[2] + t[3])) + ((t[4] + t[5]) + (t[6] + t[7]))) * 8u) + (lane & 7);
    c = imin(c, dppi<DPP_XOR1>(c)); c = imin(c, dppi<DPP_XOR2>(c)); c = imin(c, dppi<0x141>(c));
    unsigned k = 0u;
#pragma unroll
    for (int r = 0; r < 6; ++r) k |= (unsigned)(__builtin_amdgcn_readlane(c, 8 * r) & 7) << (4 * r);
    return (k ^ PL_X) ^ (unsigned)ck_rot(b & 7) * 0x111111u;
  }
};

struct Sched {
  u32x4 tsv, rec;
  unsigned side, o;
  __device__ __forceinline__ void pre(int s) { o = (unsigned)OFF_ACC + (unsigned)((s + SR - 1) % SR) * (unsigned)ACC_STEP + ACC_TS + side; PIN_S(o); }
  __device__ __forceinline__ void load(const __amdgpu_buffer_rsrc_t rs, int lane) {
    tsv = SC1_LOADS(ts_piece(lane & 63) - ACC_TS, o); rec = SC1_LOADS(128, o);
  }
  __device__ __forceinline__ unsigned last(unsigned ref) {
    const unsigned o = 0x40000000u - ref;
    int l = (int)umax(umax(tsv[0] ? tsv[0] + o : 0u, tsv[1] ? tsv[1] + o : 0u), umax(tsv[2] ? tsv[2] + o : 0u, tsv[3] ? tsv[3] + o : 0u));
    return (unsigned)wave_imax(l) - o;
  }
  __device__ __forceinline__ unsigned run(const RecArgs& a, const __amdgpu_buffer_rsrc_t rs, int s, int b, int lane) {
    constexpr int w = 3; (void)w;
    unsigned acur = __builtin_amdgcn_readfirstlane(rec[0]), aprev = __builtin_amdgcn_readfirstlane(rec[1]), hist = __builtin_amdgcn_readfirstlane(rec[3]);
    int D8 = (int)__builtin_amdgcn_readfirstlane(rec[2]);
    const int d = wave_imax((int)(umax(umax(tsv[0], tsv[1]), umax(tsv[2], tsv[3])) - aprev)), dd = 8 * d - D8;
    int Dn = D8 + (dd >> (dd > 0 ? SCH_GU : SCH_GD));
    unsigned anext = (acur + (unsigned)((Dn + 4) >> 3) + SCH_V + (s < EARLY_N ? EARLY_V : (s >= WPF0 ? (s == NSEG - 2 ? LAST_V : LATE_V) : 0))) | 1u;
    if (__builtin_expect(hist < 2u || aprev == 0u || d < 1 || d + SCH_VMIN > (int)(acur - aprev), 0)) {
      if (s == 0) {
        for (int it = 0; it < 40 && !wave_all(min4(tsv) != 0u); ++it) {
          __builtin_amdgcn_s_sleep(ST_ALL);
          asm volatile("" ::: "memory");
          load(rs, lane);
        }
      }
      const unsigned now = now32(), L = last(now);
      if (acur == 0u) { D8 = 8 * SCH_D0; hist = 0u; }
      const bool late = acur == 0u || (int)(L + SCH_VMIN - acur) > 0;
      if (late) { if (b == 0) FC(3); if (acur) D8 += 8 * (hist ? SCH_UP : 3 * SCH_UP); }
      else if (aprev) { if (hist < 2u) D8 = 8 * imin(imax((int)(L - aprev), 1), 4000); hist = umin(hist + 1u, 2u); }
      const unsigned base = acur == 0u ? L + SCH_FIRST : (late ? acur + (L + RETRY_G - acur + RETRY_T - 1) / RETRY_T * RETRY_T + SCH_PEN : acur);
      anext = (base + (unsigned)((D8 + 4) >> 3) + SCH_V + (s == 1 ? EARLY_V1 : (s < EARLY_N ? EARLY_V : 0))) | 1u;
      if (s == 0) anext = (L + ST_X) | 1u;
      if ((int)(now - L) > 50000) anext = 0u;
      if (late) acur = 0u;
      Dn = D8;
    }
    if (__builtin_expect(b == 0, 0))
      __builtin_amdgcn_raw_buffer_store_b128((u32x4){anext, acur, (unsigned)Dn, hist}, rs, lane < 2 ? lane * 8192 : 0x7ffffff0, (unsigned)OFF_ACC + (unsigned)(s % SR) * (unsigned)ACC_STEP + ACC_TS + 128u, 16);
    LOGV(0, anext); LOGV(1, acur == 0u); LOGV(2, (Dn + 4) >> 3); LOGV(3, d); LOGV(4, acur - aprev - d); LOGV(5, aprev);
    return anext;
  }
};

struct TokTask {
  u32x4 y[2]; u32x2 ym; u32x4 res;
  unsigned l0, l1, lm, s0, s1;
  static __device__ __forceinline__ bool ok(int sl) { return sl >= 0 && sl < NSEG; }
  __device__ __forceinline__ void init(int h, int j, int lane) {
    const int c = lane & 15, g = lane >> 4;
    l0 = ts_tok(h, g) + (unsigned)(j * 256 + c * 16); l1 = l0 + 16384u;
    lm = ts_mem(h, g + 4 * (c >> 3)) + (unsigned)(j * 64 + (c & 7) * 8);
    res = (u32x4){0u, 0u, 0u, 0u};
    const int qi = 4 * j + (c & 3);
    s0 = g < 2 ? pt_tok(h) + (unsigned)((qi >> 4) * 1024 + ((c >> 2) * 16 + (qi & 15)) * 16 + g * 8) : 0x7ffffff0u;
    s1 = lane < 8 ? pt_mem(h) + (unsigned)((qi >> 4) * 256 + ((c >> 2) * 16 + (qi & 15)) * 8) : 0x7ffffff0u;
    PIN_V(l0); PIN_V(l1); PIN_V(lm); PIN_V(s0); PIN_V(s1);
  }
  __device__ __forceinline__ void load(const __amdgpu_buffer_rsrc_t rs, unsigned o) {
    y[0] = SC1_LOADS(l0, o); y[1] = SC1_LOADS(l1, o);
    ym = __builtin_amdgcn_raw_buffer_load_b64(rs, lm, o, 16);
  }
  __device__ __forceinline__ bool ready() { return wave_all(umin(umin(min4(y[0]), min4(y[1])), umin(ym[0], ym[1])) != 0u); }
  __device__ __forceinline__ void run(int j, int lane, const bf16x8 SelLo) {
    const int c = lane & 15, g = lane >> 4;
    const f32x4 Z4 = {0.f, 0.f, 0.f, 0.f};
    const f32x4 d1 = mfma32(SelLo, as8(y[1]), mfma32(SelLo, as8(y[0]), Z4));
    const f32x4 d0 = mfma32(SelLo, mk8(ym[0], ym[1], 0u, 0u), Z4);
    const int qi = 4 * j + (c & 3), k0 = 16 * g + 4 * (c >> 2);
    float t[4], mv[4];
    float mx = -INFINITY;
#pragma unroll
    for (int r = 0; r < 4; ++r) {
      t[r] = (g < 2 && k0 + r <= qi) ? d1[r] : -INFINITY;
      const float mt = d0[r] + dppf<DPP_ROR8>(d0[r]);
      mv[r] = lane < 8 ? mt : -INFINITY;
      mx = fmx(mx, fmx(t[r], mv[r]));
    }
    mx = fmx(mx, dppf<DPP_ROR8>(mx)); mx = fmx(mx, dppf<DPP_ROR4>(mx));
    mx = x16mx(mx);
    float sum = 0.f;
#pragma unroll
    for (int r = 0; r < 4; ++r) {
      t[r] = __builtin_amdgcn_exp2f(t[r] - mx); mv[r] = __builtin_amdgcn_exp2f(mv[r] - mx);
      sum += t[r] + mv[r];
    }
    sum += dppf<DPP_ROR8>(sum); sum += dppf<DPP_ROR4>(sum);
    sum = x16sum(sum);
    const float rs_ = __builtin_amdgcn_rcpf(sum);
    res = (u32x4){pack2n(t[0] * rs_, t[1] * rs_), pack2n(t[2] * rs_, t[3] * rs_), pack2n(mv[0] * rs_, mv[1] * rs_), pack2n(mv[2] * rs_, mv[3] * rs_)};
    PIN_V(res);
  }
  __device__ __forceinline__ void store(const __amdgpu_buffer_rsrc_t rs, unsigned o, unsigned no = 0u) {
    SC1_STORE8S(((u32x2){res[0], res[1]}), s0 | no, o);
    SC1_STORE8S(((u32x2){res[2], res[3]}), s1 | no, o);
  }
};

constexpr int L_DRN = L_VOX + (NSEG % NV) * 5120;
__device__ __forceinline__ void drain_pre(char* lds, int b, int lane) {
  const int gp = (b & 7) * 2 + (b >> 7), m = (b >> 3) & 15;
  TokTask tk; PvTask2 pv; OutTask ot;
  tk.init(2 * gp + (m >> 3), m & 7, lane); pv.init(gp, lane); ot.init(b, lane);
  u32x4* p = (u32x4*)(lds + L_DRN) + lane;
  p[0] = (u32x4){tk.l0, tk.lm, tk.s0, tk.s1}; p[64] = (u32x4){pv.lo, pv.lm, pv.vr, pv.ma}; p[128] = (u32x4){pv.mz, pv.so, ot.t5f, ot.t5o};
  p[192] = __builtin_bit_cast(u32x4, ot.sel);
}
__device__ __forceinline__ void qkv_pre(unsigned (&vo)[2][2], int b, int w, int lane) {
  const int cb = qkv_col(b), c = lane & 15, g = lane >> 4, t0 = w & 1, col0 = (cb % 5) * 128 + w * 32;
  const bool SW = cb < 10;
#pragma unroll
  for (int i = 0; i < 2; ++i)
#pragma unroll
    for (int tt = 0; tt < 2; ++tt) {
      const int tq = tt ^ t0, tk = tq * 16 + c, cc = col0 + i * 16 + (SW ? 4 * g : c), hd = cc / BD, e = cc - hd * BD;
      const int kv = (int)(OFF_KV - OFF_QB) + hd * 64 * KVT;
      vo[i][tt] = (unsigned)(!SW ? kv + 1280 + tq * 672 + ((g >> 1) * 21 + e) * 16 + (g & 1) * 8
                                 : (cb < 5 ? (hd * S_ + tk) * 2 * QST + e * 2 : kv + (e < 16 ? (tk + 32 * (e >> 3)) * 16 + (e & 7) * 2 : 1024 + tk * 8)));
      PIN_V(vo[i][tt]);
    }
}

template <int w>
__device__ __forceinline__ void token_waves(const RecArgs& a, char* lds, const __amdgpu_buffer_rsrc_t rs, unsigned (&vo)[2][2]) {
  const int lane0 = __builtin_amdgcn_workitem_id_x() & 63, b = __builtin_amdgcn_workgroup_id_x(), wid = w + 4; (void)wid;
  const int gp = (b & 7) * 2 + (b >> 7), m = (b >> 3) & 15, h0 = 2 * gp, h = h0 + (m >> 3), j = m & 7;
  float* const misc = (float*)(lds + L_MISC);
  const f32x4 Z4 = {0.f, 0.f, 0.f, 0.f};
  const unsigned lds0 = (unsigned)(uintptr_t)(LAS char*)lds;
  unsigned* const cnt = (unsigned*)(misc + 6);
  const float* const xnrm = (const float*)(a.ws + OFF_XNRM);
  bf16x16 W[2][10];
  auto btile = [&](int i) { return w == 1 ? 0 : (w == 2 ? 1 + i : 3 + i); };
  if (w == 1) code_touch(b, lane0, lds0);
  Place pl;
  if (w == 3) pl.load(a.ws, lane0);
  if (a.layer) {
    if (w != 0) img_fetch<20>(a, lds0, h, j, w - 1, lane0);
  } else {
    if (w == 0) img_fetch<12>(a, lds0, h, j, 4, lane0);
#pragma unroll
    for (int i = 0; i < 10; ++i) x_fetch(x_src(a, 0, b), lds0, 0, 10 * w + i, lane0);
  }
#pragma unroll
  for (int i = 0; i < 2; ++i)
#pragma unroll
    for (int p = 0; p < 10; ++p)
      if (w >= 2 || (w == 1 && i == 1)) {
        const char* wp = a.ws + OFF_WB + ((size_t)(b * 5 + btile(i)) * 20 + 2 * p) * 1024 + lane0 * 16;
        if (FR_PACE && a.layer) asm volatile("s_waitcnt vmcnt(%0)" :: "n"(FR_PACE) : "memory");
        W[i][p] = cat16(*(const bf16x8*)wp, *(const bf16x8*)(wp + 1024));
      }
  f32x4 bn = Z4;
  if (w == 0) {
    bn = *(const f32x4*)(a.ws + OFF_BNRM + lane0 * 16);
    if (!a.layer) asm volatile("s_waitcnt vmcnt(0)" ::: "memory");
  } else asm volatile("s_waitcnt vmcnt(%0)" :: "n"(w == 1 ? 20 : 40) : "memory");
  unsigned ck = 0u;
  if (w == 3) { ck = pl.get(b, lane0); *(unsigned*)(misc + 11) = ck; }
  BAR();
#pragma unroll
  for (int p = 0; p < 10; ++p) {
    if (w < 2) W[0][p] = cat16(wqk_frag(lds, 2 * w, 2 * p, lane0), wqk_frag(lds, 2 * w, 2 * p + 1, lane0));
    if (w == 0) W[1][p] = cat16(wqk_frag(lds, 1, 2 * p, lane0), wqk_frag(lds, 1, 2 * p + 1, lane0));
  }
  float cb2 = 0.f;
  if (w == 0) {
    float n = __builtin_amdgcn_sqrtf(bn[0] + bn[1]) + __builtin_amdgcn_sqrtf(bn[2] + bn[3]);
    n += dppf<DPP_XOR1>(n); n += dppf<DPP_XOR2>(n); n += dppf<DPP_ROR4>(n); n += dppf<DPP_ROR8>(n);
    n = x32mx(x16mx(n));
    cb2 = u2f(__builtin_amdgcn_readfirstlane(f2u(1.15f * n * n)));
  }
  const float cm2 = u2f((f2u(cb2) & 0x7fffffu) | 0x40000000u);
  const int ek = imax((int)((f2u(cb2) >> 23) & 0xffu), 1) - 252;
  if (w >= 2) {
#pragma unroll
    for (int i = 0; i < 4; ++i) *(unsigned*)(lds + L_KX + (w - 2) * 4096 + i * 1024 + lane0 * 16 + 12) = 0u;
  }
  OutTask ot;
  const int zl = lane0 - 16;
  const bool zon = w == 3 && zl >= 0 && (zl < 40 || b < ACC_NB + 17);
  unsigned zo = zon ? zero_base(b, h, j, zl) + (zl < 32 ? sct_slot(ck, 0u) : (zl < 40 ? scm_slot(ck, 0u) : 0u)) : 0x7ffffff0u;
  unsigned zm = zon ? (zl < 32 ? SCT_STEP + 1u : (zl < 40 ? SCM_STEP : (unsigned)ACC_STEP)) : 0u;
  const unsigned zx = ((ck ^ (ck >> 4)) & 7u) * 4096u;
  if (w == 3) { PIN_V(zo); PIN_V(zm); }
  auto st_out = [&](int s, int lane, auto go) {
    if (w < 2) return;
    if (ST_E) lds_spin<L_MISC + 112, 1>(lds0, (unsigned)imax(s, 0)); else go();
    const int sl = s - 2 - LAG4;
    const char* sp = lds + L_STG + 2048 + (w & 1) * 1280;
    u32x4 v0 = *(const u32x4*)(sp + lane * 16), v1 = *(const u32x4*)(sp + 1024 + (lane & 15) * 16);
    const u32x4 z = {0u, 0u, 0u, 0u};
    unsigned oo = (sl < 0 ? 0x7ff00000u : op_slot(sl) + (unsigned)((m * 32 + (w & 1) * 16 + gp) * 1280)) + (unsigned)(lane * 16), za = 0x7ffffff0u;
    if (w == 3) {
      const unsigned zs = (unsigned)(s + 2) % SR;
      za = (zo + (s < 0 ? 1024u : zs) * (zm & ~1u)) ^ ((zm & 1u) * (zs & 4u ? zx : 0u));
#pragma unroll
      for (int i = 0; i < 4; ++i) { v1[i] = lane < 16 ? v1[i] : z[i]; PIN_V(v1[i]); }
    }
    za = lane < 16 ? oo + 1024 : za;
    PIN_V(za); PIN_V(oo); PIN_V(v0); PIN_V(v1);
    __builtin_amdgcn_sched_barrier(0);
    if (ST_E) go();
    SC1_STORE(v0, oo);
    SC1_STORE(v1, za);
    __builtin_amdgcn_sched_barrier(0);
    if (w == 2) ot.store(a, s - 2 - LAG5, b, lane);
    else {
      const unsigned xo_ = (unsigned)((s - 8) * 1024 + lane * 16);
      if (__builtin_expect((unsigned)(s - 8) < 20u, 0)) SC1_STORE(z, xo_ < X_BYTES / 256 ? (unsigned)(OFF_X + b * (X_BYTES / 256)) + xo_ : 0x7ffffff0u);
    }
  };
  STAMP_DECL(); ARRIVE_DECL();
  auto mma_seg = [&](int seg, int sv, int lane, auto pause) {
    const int c = lane & 15, g = lane >> 4;
    const char* xp = lds + L_XN + (seg & 1) * 40960 + lane * 16;
    f32x4 acc[2][2] = {{Z4, Z4}, {Z4, Z4}};
    bf16x8 x[4][2];
#pragma unroll
    for (int p = 0; p < 3; ++p) { x[p][0] = *(const bf16x8*)(xp + p * 1024); x[p][1] = *(const bf16x8*)(xp + (20 + p) * 1024); }
    unsigned eo[2];
#pragma unroll
    for (int ks = 0; ks < 20; ++ks) {
      if (ks == (w == 0 ? KP0 : (w == 1 ? KP1 : KP2))) { pause(); __builtin_amdgcn_sched_barrier(0); }
      if (ks == 17) {
#pragma unroll
        for (int i = 0; i < 2; ++i) {
          eo[i] = (unsigned)(w == 0 ? (i ? L_KX : L_QX) + (seg % NKQ) * 2048 + lane * 16
                                    : (w == 1 && i == 0 ? ((g >> 1) ? L_KX : L_QX) + (seg % NKQ) * 2048 + (2 * (g & 1) * 16 + c) * 16 + 8 : L_VOX + sv * 5120 + btile(i) * 1024 + lane * 16));
          if (i == 0 || w == 1) PIN_V(eo[i]);
        }
        if (w == 0) eo[1] = eo[0] + (L_KX - L_QX);
        if (w >= 2) eo[1] = eo[0] + 1024;
      }
      __builtin_amdgcn_sched_barrier(0);
#pragma unroll
      for (int q = 0; q < 4; ++q) {
        const int tt = 1 - (q >> 1), i = q & 1;
        const bool isqk = w == 0 || (w == 1 && i == 0);
        const bf16x8 wf = half8(W[i][ks >> 1], ks & 1);
        constexpr int q0 = UNIT && w < 2;
        if (q < q0) continue;
        acc[i][tt] = isqk ? mfma32(wf, x[ks & 3][tt], acc[i][tt]) : mfma32(x[ks & 3][tt], wf, acc[i][tt]);
        __builtin_amdgcn_sched_barrier(0);
        if (q - q0 < 2 && ks + 3 < 20) x[(ks + 3) & 3][q - q0] = *(const bf16x8*)(xp + ((q - q0) * 20 + ks + 3) * 1024);
        __builtin_amdgcn_sched_barrier(0);
      }
    }
#pragma unroll
    for (int i = 0; i < 2; ++i) {
      if (w + i == 1) { const float ks_ = (w == 0 || (g >> 1)) ? KEXP : 1.0f; acc[i][0] *= ks_; acc[i][1] *= ks_; }
      const u32x4 v = {pack2(acc[i][0][0], acc[i][0][1]), pack2(acc[i][0][2], acc[i][0][3]), pack2(acc[i][1][0], acc[i][1][1]), pack2(acc[i][1][2], acc[i][1][3])};
      char* p = lds + eo[i];
      if (w == 0) { *(u32x2*)(p) = (u32x2){v[0], v[1]}; if (i || !UNIT) *(u32x2*)(p + 1024) = (u32x2){v[2], v[3]}; }
      else if (w == 1 && i == 0) { *(unsigned*)(p) = v[0]; *(unsigned*)(p + 256) = v[1]; if (!UNIT) { *(unsigned*)(p + 1024) = v[2]; *(unsigned*)(p + 1280) = v[3]; } }
      else *(u32x4*)p = v;
    }
  };
  unsigned vp, mp, ip;
  int sidx, ei = 127;
  float inv;
  f32x4 acc[2];
  auto vo_pre = [&](int s, int sv, int lane) {
    vp = (unsigned)(w == 0 ? L_MISC + (s & 3) * 4 : L_VOM + sv * 1280 + (btile(0) * 32 + (w == 1 ? lane & 31 : lane)) * 8);
    mp = (unsigned)(L_BM + lane * 16);
    PIN_V(vp); PIN_V(mp);
    if (w == 0) { ip = (unsigned)(L_MISC + 16 + ((s + 1) & 1) * 4); PIN_V(ip); }
    else {
      inv = misc[4 + (s & 1)];
      sidx = (lane & 8) ? 0xEEEE : 0x4444;
      acc[0] = acc[1] = Z4;
      PIN_V(sidx); PIN_V(acc[1]);
      if (w >= 2) { PIN_V(acc[0]); PIN_V(inv); }
    }
    __builtin_amdgcn_sched_barrier(0);
  };
  auto vo_mem = [&](int s, auto last) {
    __builtin_amdgcn_s_setprio(2);
    asm volatile(".rept %0\n\ts_nop 3\n\t.endr" :: "n"(w == 0 ? VO_A0 : (w == 1 ? VO_A1 : VO_A2)) : "memory");
    float xmax = 0.f;
    if (w == 0) xmax = *(const float*)(lds + vp);
    constexpr int NX = w == 2 ? VO_N2 : (w == 3 ? VO_N3 : 10);
    bf16x8 x[10];
#pragma unroll
    for (int p = 0; p < NX; ++p) x[p] = *(const bf16x8*)(lds + mp + p * 1024);
    __builtin_amdgcn_sched_barrier(0);
    if (w < 2) asm volatile(".rept %0\n\ts_nop 3\n\t.endr" :: "n"(w == 0 ? VO_B0 : VO_B1) : "memory");
    if (VO_W >= 0) asm volatile("s_waitcnt lgkmcnt(%0)" :: "n"(VO_W < 0 ? 0 : VO_W) : "memory");
    STAMP(5);
    __builtin_amdgcn_sched_barrier(0);
    int ex = 0;
#pragma unroll
    for (int p = 0; p < 10; ++p) {
      if (w == 0) acc[0] = mfma32(x[p], x[p], p ? acc[0] : Z4);
      if (w == 0 && p == 9) { ex = imax((int)((__builtin_amdgcn_readfirstlane(f2u(__builtin_amdgcn_sqrtf(cb2 * xmax))) >> 23) & 0xffu) + 1, 32); PIN_S(ex); }
#pragma unroll
      for (int i = (w == 1); i < 2 && w != 0; ++i) acc[i] = smfma64(x[p], W[i][p], acc[i], sidx);
      __builtin_amdgcn_sched_barrier(0);
      if (p + NX < 10) x[p + NX] = *(const bf16x8*)(lds + mp + (p + NX) * 1024);
      __builtin_amdgcn_sched_barrier(0);
    }
    STAMP(7);
    if (w != 0) {
      float v[4];
#pragma unroll
      for (int r = 0; r < 4; ++r) {
        const auto sw = __builtin_amdgcn_permlane32_swap(f2u(acc[w == 1][r]), f2u(acc[1][r]), false, false);
        v[r] = (u2f(sw[0]) + u2f(sw[1])) * inv;
      }
      *(u32x2*)(lds + vp) = (u32x2){pack2(v[0], v[1]), pack2(v[2], v[3])};
    } else {
      const int t = (int)f2u(fmx(fmx(acc[0][0], acc[0][1]), fmx(acc[0][2], acc[0][3])) * cm2);
      const int et = wave_imax(t) >> 23, em = ((et + ek) >> 1) + ei;
      int e = imax(et == 255 ? 256 : em, ex);
      PIN_S(e);
      e = imin(e, 256);
      ei = e - 31;
      *(unsigned*)(lds + ip) = (unsigned)ei << 23;
      const u32x3 sr = {(unsigned)(285 - e) << 23, (unsigned)(e - 87) < 79u ? REC_RNG << 23 : 0u, (unsigned)(s + 1)};
      asm volatile("ds_write_b96 %0, %1 offset:%2" :: "v"(lds0), "v"(sr), "n"(L_MISC + 32) : "memory");
    }
    if (last == 0 && w >= 2) asm volatile("s_waitcnt vmcnt(%0)" :: "n"(w == SCH_W ? 2 : 0) : "memory");
    if (w != 0) lds_inc<L_MISC + 28>(lds0);
    STAMP(8);
    if (last) __builtin_amdgcn_s_setprio(0);
    if (last && w == 2) { VM_DRAIN(); lds_inc<L_MISC + 28>(lds0); }
  };
  unsigned* const cntq = (unsigned*)(misc + 12);
  unsigned* const cntt = (unsigned*)(misc + 26 + (w & 1));
  unsigned* const cntp = (unsigned*)(misc + 23);
  unsigned* const cntx2 = (unsigned*)(misc + 28);
  float xn4 = 0.f;
  auto wpf = [&](int s, int lane) {
    constexpr int PER = 81920 / QR / WPF_N;
    static_assert(PER % WPF_S == 0 && PER / WPF_S <= 64, "prefetch");
    touch(a.ws + OFF_WQKVBT + (size_t)imin(qkv_col(b), 14) * 163840 + (qkv_row(b) % (2 * QR)) * (81920 / QR), (s - WPF0) * PER + imin(lane, PER / WPF_S - 1) * WPF_S, lds0);
  };
  auto t5_get = [&](int s, int lane) {
    ot.load(a, rs, s - 1 - LAG5, b);
    xn4 = u2f(__hip_atomic_load((const GAS unsigned*)xnrm + (imax(s + 1, 0) * 32 + (lane & 31)) * 4, RLX_AGENT));
    __builtin_amdgcn_sched_barrier(0);
  };
  Sched sch;
  auto late = [&](int s, int lane) {
    if (w == SCH_W && SCH_LATE && __builtin_expect(s >= 0, 1)) *(u32x2*)(misc + 24) = (u32x2){sch.run(a, rs, s, b, lane), (unsigned)(s + 1)};
    if ((w == 2 && F6) || (w == 0 && F4) || (w == 1 && F5) || (w == 3 && F7)) { lds_spin<L_MISC + 48, 1>(lds0, 2u * (unsigned)imax(s + 1, 0)); if (FB_SL && w != 2) __builtin_amdgcn_s_sleep(FB_SL); }
    if (w >= 2) { x_fetch_n<w == 2 ? F6 : F7>(x_src(a, s + 3, b), lds0, s + 3, F4 + F5 + 2 * F67 + (w == 3 ? F6 : 0), lane * 16); return; }
    unsigned sd = 0u;
    if ((w == 0 && XPF == 3) || XPF == 4 || (w == 1 && XPF == 5)) stouch<10>(a.ws + OFF_XN + (size_t)imin(s + 4, NSEG - 1) * (s < 1 ? 0 : 40960) + (b >> 3) * 1280 + w * 64, sd);
    x_fetch_n<w == 0 ? F4 : F5>(x_src(a, s + 3, b), lds0, s + 3, w == 0 ? 0 : F4, lane * 16);
    if (WPF_W == 0 && w == 0 && __builtin_expect(s >= WPF0 && s < WPF0 + WPF_N, 0)) wpf(s, lane);
    if (w == 1 && __builtin_expect(s == CPF_S, 0)) code_touch(b, lane, lds0);
    lds_spin<L_MISC + 104 + 4 * (w & 1), PS_SL>(lds0, (unsigned)imax(s + 1, 0));
    asm volatile("" :: "s"(sd));
  };
  auto xn_put = [&](int s) {
    float xn2 = xn4;
    xn2 = fmx(xn2, dppf<DPP_XOR1>(xn2)); xn2 = fmx(xn2, dppf<DPP_XOR2>(xn2));
    xn2 = fmx(xn2, dppf<DPP_ROR4>(xn2)); xn2 = fmx(xn2, dppf<DPP_ROR8>(xn2));
    misc[s & 3] = x16mx(xn2);
  };
  if (w == 2) { ot.init(b, lane0); ot.out = (u32x2){0u, 0u}; ot.xo = ot.out; ot.y[0] = ot.y[1] = ot.y[2] = ot.y[3] = (u32x4){0u, 0u, 0u, 0u}; }
  VM_DRAIN();
  lds_arrive(cnt, lane0);
  lds_wait(cnt, 8u);
  sch.tsv = sch.rec = (u32x4){0u, 0u, 0u, 0u};
  sch.side = __builtin_amdgcn_readfirstlane(*(const unsigned*)(misc + 11)) & 8192u;
  sch.o = 0u;
  int sv = NV - 1;
  CODE_PAD((PADT >> (4 * w)) & 15);
#pragma unroll 1
  for (int s = -1; s < NSEG - 1; ++s) {
    const int sv1 = sv == NV - 1 ? 0 : sv + 1;
    FRESH_LANE();
    STAMP(0);
    mma_seg(s + 1, sv1, lane, [&]() { STAMP(1); late(s - 1, lane); if (w == SCH_W) sch.pre(s); STAMP(9); });
    STAMP(2);
    __builtin_amdgcn_sched_barrier(0);
    VM_DRAIN();
    if (w == 2) { ot.run(lane); xn_put(s); }
    STAMP(10);
    st_out(s, lane, [&]() { lds_spin<L_MISC + 92, W1_SL>(lds0, 4u * (unsigned)imax(s, 0)); });
    if (w == 2) t5_get(s, lane);
    __builtin_amdgcn_sched_barrier(0);
    if (w >= 2) vo_pre(s, sv, lane);
    STAMP(3);
    BAR();
    STAMP(4);
    if (__builtin_expect(s >= 0, 1)) {
      if (w == SCH_W) { sch.load(rs, lane0); __builtin_amdgcn_sched_barrier(0); }
      if (w < 2) vo_pre(s, sv, lane);
      vo_mem(s, std::integral_constant<int, 0>{});
      STAMP(6);
      if (w == SCH_W && !SCH_LATE) {
        __builtin_amdgcn_s_setprio(SCH_P);
        *(u32x2*)(misc + 24) = (u32x2){sch.run(a, rs, s, b, lane0), (unsigned)(s + 1)};
      }
      STAMP(11);
    } else BAR();
    __builtin_amdgcn_s_setprio(MMA_P);
    __builtin_amdgcn_sched_barrier(0);
    sv = sv1;
  }
  __builtin_amdgcn_s_setprio(0);
  STAMP_OUT();
  late(NSEG - 2, lane0);
  if (w == 2) ot.run(lane0);
  { FRESH_LANE(); if (w == 0) drain_pre(lds, b, lane); qkv_pre(vo, b, w, lane); }
  st_out(NSEG - 1, lane0, [&]() { lds_spin<L_MISC + 92, 1>(lds0, 4u * (NSEG - 1)); });
  if (w != 0) vo_pre(NSEG - 1, (NSEG - 1) % NV, lane0);
  BAR();
  if (w != 0) vo_mem(NSEG - 1, std::integral_constant<int, 1>{});
}

__device__ __forceinline__ void qkv_tail(const RecArgs& a, char* lds, const int w, const unsigned (&vo)[2][2]) {
  const int lane0 = __builtin_amdgcn_workitem_id_x() & 63, b = __builtin_amdgcn_workgroup_id_x();
  const int r = qkv_row(b), cb = qkv_col(b);
  if (cb >= 15) return;
  const unsigned lds0 = (unsigned)(uintptr_t)(LAS char*)lds;
  unsigned* const stage = (unsigned*)(lds + L_MISC) + 17;
  unsigned* const cnty = stage + 1;
  const f32x4 Z4 = {0.f, 0.f, 0.f, 0.f};
  auto fetch = [&](int i) {
    x_fetch_n<10>(x_src(a, NSEG + i, b), lds0, i, 10 * w, lane0 * 16);
  };
  unsigned want = 0;
  auto sync = [&]() { VM_DRAIN(); want += 4u; lds_inc<L_MISC + 72>(lds0); while (__hip_atomic_load(cnty, __ATOMIC_RELAXED, __HIP_MEMORY_SCOPE_WORKGROUP) < want) { } asm volatile("" ::: "memory"); };
  LOGQ(0);
  bf16x8 W[2][20];
  const __amdgpu_buffer_rsrc_t rw = __builtin_amdgcn_make_buffer_rsrc(a.ws + OFF_WQKVBT + (size_t)(cb * 8 + 2 * w) * 20480, 0, 40960, 0x00020000);
  auto w_get = [&](int ks) {
#pragma unroll
    for (int i = 0; i < 2; ++i) W[i][ks] = as8(__builtin_amdgcn_raw_buffer_load_b128(rw, lane0 * 16 + (ks & 3) * 1024, (i * 20 + (ks & ~3)) * 1024, 0));
    __builtin_amdgcn_sched_barrier(0);
  };
#pragma unroll
  for (int ks = 0; ks < QW; ++ks) w_get(ks);
  bf16x8 ones = mk8(0x3f803f80u, 0x3f803f80u, 0x3f803f80u, 0x3f803f80u);
  PIN_V(ones);
  const __amdgpu_buffer_rsrc_t ro = __builtin_amdgcn_make_buffer_rsrc(a.ws + OFF_QB, 0, (int)(OFF_OB - OFF_QB), 0x00020000);
  const int t0 = w & 1;
  auto body = [&](auto SW, auto SQ) {
    const float qs = cb < 5 ? 0.22360679774997896f * 1.4426950408889634f : 1.0f;
    f32x4 cs[2] = {Z4, Z4};
    LOGQ(1);
    f32x4 acc[2][2], st;
    auto mma = [&](int it, auto FIRST) {
      const char* xp0 = lds + L_XN + (it & 1) * 40960 + t0 * 20480 + lane0 * 16;
      const char* xp1 = lds + L_XN + (it & 1) * 40960 + (1 - t0) * 20480 + lane0 * 16;
      acc[0][0] = acc[0][1] = acc[1][0] = acc[1][1] = st = Z4;
      bf16x8 x[4][2];
#pragma unroll
      for (int p = 0; p < 3; ++p) { x[p][0] = *(const bf16x8*)(xp0 + p * 1024); x[p][1] = *(const bf16x8*)(xp1 + p * 1024); }
      __builtin_amdgcn_sched_barrier(0);
#pragma unroll
      for (int ks = 0; ks < 20; ++ks) {
        if (FIRST && ks + QW < 20) w_get(ks + QW);
#pragma unroll
        for (int q = 0; q < 4; ++q) {
          const int tt = 1 - (q >> 1), i = 1 - (q & 1);
          acc[i][tt] = SW ? mfma32(W[i][ks], x[ks & 3][tt], acc[i][tt]) : mfma32(x[ks & 3][tt], W[i][ks], acc[i][tt]);
          __builtin_amdgcn_sched_barrier(0);
          if (q < 2 && ks + 3 < 20) x[(ks + 3) & 3][q] = *(const bf16x8*)((q ? xp1 : xp0) + (ks + 3) * 1024);
          __builtin_amdgcn_sched_barrier(0);
        }
        st = mfma32(SQ ? x[ks & 3][0] : ones, x[ks & 3][0], st);
        if (FIRST) {
#pragma unroll
          for (int i = 0; i < 2; ++i) cs[i] = SW ? mfma32(W[i][ks], ones, cs[i]) : mfma32(ones, W[i][ks], cs[i]);
        }
        __builtin_amdgcn_sched_barrier(0);
      }
    };
    mma(0, std::integral_constant<bool, true>{});
#pragma unroll 1
    for (int it = 0; it < 4; ++it) {
      if (it) mma(it, std::integral_constant<bool, false>{});
      LOGQ(2 + 3 * it);
      FRESH_LANE();
      float* const sp = (float*)(lds + L_ST) + (it & 1) * 64;
      if (SQ) { if ((c >> 2) == g) sp[32 + t0 * 16 + c] = (c & 3) == 0 ? st[0] : ((c & 3) == 1 ? st[1] : ((c & 3) == 2 ? st[2] : st[3])); }
      else if (g == 0) sp[t0 * 16 + c] = st[0];
      sync();
      LOGQ(3 + 3 * it);
      if (it == 0) fetch(2);
      const bool late = r + 48 >= 55;
      if (it == 1 && !late) fetch(3);
      const unsigned so = (unsigned)((r + 16 * it) * (cb < 5 ? 64 * QST : KVT));
      auto stat = [&](float sm, float sq, float& mean, float& rstd) {
        mean = sm * (1.0f / DM);
        rstd = __builtin_amdgcn_rsqf(__builtin_fmaxf(sq * (1.0f / DM) - mean * mean, 0.f) + 1e-5f);
      };
#pragma unroll
      for (int tt = 0; tt < 2; ++tt) {
        const int tq = tt ^ t0;
        float mean[4], rstd[4];
        if (SW) {
          stat(sp[tq * 16 + c], sp[32 + tq * 16 + c], mean[0], rstd[0]);
          rstd[0] *= qs;
        } else {
          const f32x4 sm = *(const f32x4*)(sp + tq * 16 + 4 * g), sq = *(const f32x4*)(sp + 32 + tq * 16 + 4 * g);
#pragma unroll
          for (int k = 0; k < 4; ++k) stat(sm[k], sq[k], mean[k], rstd[k]);
        }
#pragma unroll
        for (int i = 0; i < 2; ++i) {
          float v[4];
#pragma unroll
          for (int k = 0; k < 4; ++k) v[k] = (acc[i][tt][k] - mean[SW ? 0 : k] * cs[i][SW ? k : 0]) * rstd[SW ? 0 : k];
          __builtin_amdgcn_raw_buffer_store_b64((u32x2){pack2(v[0], v[1]), pack2(v[2], v[3])}, ro, vo[i][tt], so, 0);
        }
      }
      LOGQ(4 + 3 * it);
      if (it == 2 && late) lds_spin<L_MISC + 68, 0>(lds0, 5u);
    }
  };
  typedef std::integral_constant<bool, false> F_; typedef std::integral_constant<bool, true> T_;
  if (cb < 10) { if (w >> 1) body(T_{}, T_{}); else body(T_{}, F_{}); }
  else { if (w >> 1) body(F_{}, T_{}); else body(F_{}, F_{}); }
}

__device__ __forceinline__ void t2_mem(const bf16x8 fr, const bf16x8 q0, const bf16x8 q1, const __amdgpu_buffer_rsrc_t rs, unsigned o, int h, int j, float inv, int lane) {
  const f32x4 Z4 = {0.f, 0.f, 0.f, 0.f};
  const f32x4 d0 = mfma32(fr, q0, Z4) * inv, d1 = mfma32(fr, q1, Z4) * inv;
  const unsigned a0 = pack2n(d0[0], d0[1]), a1 = pack2n(d0[2], d0[3]), b0 = pack2n(d1[0], d1[1]), b1 = pack2n(d1[2], d1[3]);
  const auto r0 = __builtin_amdgcn_permlane32_swap(a0, b0, false, false), r1 = __builtin_amdgcn_permlane32_swap(a1, b1, false, false);
  SC1_STORE8S(((u32x2){r0[1], r1[1]}), ts_mem(h, j) + (lane >> 5) * 256 + (lane & 12) * 16 + (lane & 16) * 2 + (lane & 3) * 8, o);
}

template <int w>
__device__ __forceinline__ void rec_waves(const RecArgs& a, char* lds, __amdgpu_buffer_rsrc_t rs, unsigned& tlast) {
  PIN_S(rs);
  const int lane = __builtin_amdgcn_workitem_id_x() & 63, b = __builtin_amdgcn_workgroup_id_x(), c = lane & 15, g = lane >> 4, wid = w; (void)wid;
  const int gp = (b & 7) * 2 + (b >> 7), m = (b >> 3) & 15, h0 = 2 * gp, h = h0 + (m >> 3), j = m & 7;
  float* const misc = (float*)(lds + L_MISC);
  const f32x4 Z4 = {0.f, 0.f, 0.f, 0.f};
  LOGP(0);
  __builtin_amdgcn_s_setprio(3);
  const unsigned lds0 = (unsigned)(uintptr_t)(LAS char*)lds;
  if (!a.layer) img_fetch<12>(a, lds0, h, j, w, lane);
  constexpr int NC = w < 2 ? 2 : 3;
  const int far = ((b >> 2) & 1) ^ 1;
  auto chunk = [&](int f) { return f == 0 ? 4 * far + w : (w == 2 && f == 1 ? 8 + far : (w == 3 && f == 2 ? 9 - far : 4 - 4 * far + w)); };
  bf16x16 Wqk[3][NC];
  u32x4 bm0[NC][2];
#pragma unroll
  for (int f = 0; f < 2 * NC; ++f) bm0[f >> 1][f & 1] = *(const u32x4*)(a.mem0 + (size_t)(c & 7) * DM + 64 * chunk(f >> 1) + 16 * g + 8 * (f & 1));
  if (!a.layer) asm volatile("s_waitcnt vmcnt(0)" ::: "memory");
  BAR();
  LOGP(9);
  const unsigned ck = __builtin_amdgcn_readfirstlane(*(const unsigned*)(misc + 11));
  const unsigned flip = (ck ^ (unsigned)(b << 11)) & 8192u;
#pragma unroll
  for (int t = 0; t < 3; ++t)
#pragma unroll
    for (int f = 0; f < NC; ++f) Wqk[t][f] = cat16(wqk_frag(lds, t, 2 * chunk(f), lane), wqk_frag(lds, t, 2 * chunk(f) + 1, lane));
  bf16x16 We[7];
  auto echunk = [&](int f) { return f < 6 ? (f / 3) * 4 + f % 3 + (f % 3 >= w) : (f == 6 ? 17 - chunk(w - 1) : chunk(f - 7)); };
  if (UNIT && w >= 2) {
#pragma unroll
    for (int f = 0; f < 7; ++f) We[f] = cat16(wqk_frag(lds, 2 * w - 4, 2 * echunk(f), lane), wqk_frag(lds, 2 * w - 4, 2 * echunk(f) + 1, lane));
  }
  auto unit = [&](int seg) {
    if (!UNIT || w < 2) return;
    const char* xp = lds + L_XN + (seg & 1) * 40960 + 20480 + lane * 16;
    bf16x8 x[20];
    f32x4 d = Z4;
#pragma unroll
    for (int k = 0; k < 26; ++k) {
      if (k >= 6) d = mfma32(half8(k - 6 < 14 ? We[(k - 6) >> 1] : Wqk[w < 2 ? 0 : 2 * w - 4][k - 6 < 14 ? 0 : ((k - 6) >> 1) - 7], k & 1), x[k - 6], d);
      __builtin_amdgcn_sched_barrier(0);
      if (k < 20) x[k] = *(const bf16x8*)(xp + echunk(k >> 1) * 2048 + (k & 1) * 1024);
      __builtin_amdgcn_sched_barrier(0);
    }
    if (w == 2) *(u32x2*)(lds + L_QX + (seg % NKQ) * 2048 + 1024 + lane * 16) = (u32x2){pack2(d[0], d[1]), pack2(d[2], d[3])};
    else {
      d *= (g >> 1) ? KEXP : 1.0f;
      char* p = lds + ((g >> 1) ? L_KX : L_QX) + (seg % NKQ) * 2048 + 1024 + (2 * (g & 1) * 16 + c) * 16 + 8;
      *(unsigned*)p = pack2(d[0], d[1]); *(unsigned*)(p + 256) = pack2(d[2], d[3]);
    }
    __builtin_amdgcn_sched_barrier(0);
  };
  if (!a.layer) lds_arrive((unsigned*)(misc + 6), lane);
  else {
    if (w == 0) {
      for (unsigned spins = 0; spins < SPIN_CAP; ++spins) {
        if (__builtin_amdgcn_raw_buffer_load_b32(rs, (b & 7) * 256, (unsigned)OFF_RDY, 16) >= 16u) break;
        __builtin_amdgcn_s_sleep(4);
        asm volatile("" ::: "memory");
      }
      lds_inc<L_MISC + 84>(lds0);
    }
    lds_wait((unsigned*)(misc + 21), 2u);
#pragma unroll
    for (int f = 0; f < 2 * NC; ++f) PIN_V(bm0[f >> 1][f & 1]);
#pragma unroll
    for (int i = 0; i < 10; ++i) x_fetch(x_src(a, 0, b), lds0, 0, 10 * w + i, lane);
  }
  const bf16x8 SelLo = sel_frag(c, 0), SelHi = sel_frag(c, 8);
  bf16x8 ONES = mk8(0x3f803f80u, 0x3f803f80u, 0x3f803f80u, 0x3f803f80u);
  if (w < 2) PIN_V(ONES);
  bool dead = false;
  float inv = 1.0f;
  unsigned* const cnt2 = (unsigned*)(misc + 7);
  unsigned l2off[2 * NC];
#pragma unroll
  for (int f = 0; f < 2 * NC; ++f) { l2off[f] = acc_off(16 * chunk(f >> 1) + 4 * g + 2 * (f & 1) + (c >> 3), c & 7) ^ flip; PIN_V(l2off[f]); }
  int sidx = (c >> 3) ? 0xEEEE : 0x4444;
  PIN_V(sidx);
  const unsigned p1off = (unsigned)((w == 0 ? (h & 2) * 1024 + j * 256 + (h & 1) * 64 + (g & 1) * 8 : (h & 3) * 32768 + j * 512 + (g >> 1) * 256 + (g & 1) * 64) + (c & 4) * 32 + (c & 3) * 16);
  unsigned p1v = (c < 8) && (w != 0 || g >= 2) ? p1off : 0x7ffffff0u, p1cur = 0x7ffffff0u;
  PIN_V(p1v); PIN_V(p1cur);
  const int hd = (c >> 2) & 1, mr = hd ? 3 - (c & 3) : (c & 3);
  const unsigned l1off = (unsigned)(((h0 + hd) & 3) * 32768 + g * 512 + (w & 1) * 128 + (c >> 3) * 64 + mr * 16);
  const unsigned l1offm = (unsigned)((h0 & 2) * 1024 + (g + 4 * (c >> 3)) * 256 + (w & 1) * 128 + hd * 64 + mr * 16);
  const int plane = (lane & ~6) | ((lane & 2) << 1) | ((lane & 4) >> 1);
  unsigned acco = c < 8 ? (acc_off(64 * far + m * 4 + g, 4 * (w & 1) + mr) + (unsigned)(hd * 8)) ^ flip : 0x7ffffff0u;
  unsigned acco2 = c >= 8 ? (acc_off(64 - 64 * far + m * 4 + g, 4 * (w & 1) + mr) + (unsigned)(hd * 8)) ^ flip
                          : (c < 4 ? (acc_off(128 + m * 2 + (g & 1), 4 * (w & 1) + mr) + (unsigned)((g >> 1) * 8)) ^ flip : 0x7ffffff0u);
  if (w < 2) { PIN_V(acco); PIN_V(acco2); }
  unsigned bmo[NC];
#pragma unroll
  for (int f = 0; f < NC; ++f) { bmo[f] = (unsigned)(chunk(f) * 1024 + lane * 16); PIN_V(bmo[f]); }
  bf16x8 bm[NC];
  auto bm_put = [&](int f) { *(bf16x8*)(lds + L_BM + bmo[f]) = bm[f]; __builtin_amdgcn_sched_barrier(0); };
  f32x4 acc[3] = {Z4, Z4, Z4};
  const int rt2 = ((c >> 3) * 8 + 4 * (g & 1)) * 28 + 16 + (c & 7);
  unsigned redo[3] = {(unsigned)(4 * g * 28 + c), (unsigned)(g < 2 ? (4 * g + 2) * 28 + c : rt2), (unsigned)(g < 2 ? (4 * g + 10) * 28 + c : rt2 + 56)};
#pragma unroll
  for (int k = 0; k < 3; ++k) { redo[k] = L_RED1 + w * 1792 + redo[k] * 4; PIN_V(redo[k]); }
  auto red_put = [&]() {
    auto hs = [&](float x, float y) { const auto sw = __builtin_amdgcn_permlane32_swap(f2u(x), f2u(y), false, false); return u2f(sw[0]) + u2f(sw[1]); };
#pragma unroll
    for (int k = 0; k < 3; ++k) {
#pragma unroll
      for (int i = 0; i < 2; ++i) *(float*)(lds + redo[k] + i * 112) = k == 0 ? hs(acc[0][i], acc[1][i]) : hs(acc[k - 1][2 + i], acc[2][2 * k - 2 + i]);
      __builtin_amdgcn_sched_barrier(0);
    }
  };
#pragma unroll
  for (int f = 0; f < NC; ++f) {
    const u32x4 u = bm0[f][0], v = bm0[f][1];
    bm[f] = c < 8 ? mk8(u[0], u[2], v[0], v[2]) : mk8(u[1], u[3], v[1], v[3]);
    bm_put(f);
#pragma unroll
    for (int t = 0; t < 3; ++t) acc[t] = smfma64(bm[f], Wqk[t][f], acc[t], sidx);
  }
  red_put();
  VM_DRAIN();
  if (a.layer) lds_arrive((unsigned*)(misc + 6), lane);
  lds_wait((unsigned*)(misc + 6), 8u);
#pragma unroll
  for (int i = 0; i < 10; ++i) x_fetch(x_src(a, 1, b), lds0, 1, 10 * w + i, lane);
  LOGP(1);
  unit(0);
  BAR();
  STAMP_DECL(); ARRIVE_DECL();
  bf16x8 fr, qx[2];
  auto kq_get = [&](int seg) {
    const char* p = lds + (w == 1 ? L_KX : L_QX) + (seg % NKQ) * 2048 + lane * 16;
    if (w == 1 || w == 2) { qx[0] = *(const bf16x8*)p; qx[1] = *(const bf16x8*)(p + 1024); }
  };
  unsigned qo4 = (unsigned)(L_RED1 + (w == 1 ? (c & 7) * 112 + (c >> 3) * 3584 : c * 112) + g * 16), qo2 = qo4 + 64u - (unsigned)(g * 8), qo2b = qo2 + 3584u;
  if (w < 3) { PIN_V(qo4); PIN_V(qo2); if (w != 1) PIN_V(qo2b); }
  auto pub1 = [&](unsigned off, unsigned o1) {
    if (w == 0) fr = qk_frag(lds + qo4, lds + qo2, lds + qo2b);
    else {
      const f32x4 a0 = *(const f32x4*)(lds + qo4), a1 = *(const f32x4*)(lds + qo4 + 1792);
      const f32x2 b0 = *(const f32x2*)(lds + qo2), b1 = *(const f32x2*)(lds + qo2 + 1792);
      float v[6];
#pragma unroll
      for (int i = 0; i < 6; ++i) v[i] = i < 4 ? a0[i] + a1[i] : b0[i - 4] + b1[i - 4];
#pragma unroll
      for (int i = 0; i < 6; ++i) v[i] += dppf<DPP_ROR8>(v[i]);
      fr = mk8(pack2(v[0], v[1]), pack2(v[2], v[3]), pack2(v[4], v[5]), 0u);
    }
    const f32x4 sc = mfma32(w == 0 ? fr : qx[0], fr, Z4);
    if (w == 0) SC1_STORE8S(((u32x2){pack2n(sc[0], sc[1]), pack2n(sc[2], sc[3])}), off, o1);
    else {
      const f32x4 s2 = mfma32(qx[1], fr, Z4);
      __builtin_amdgcn_raw_buffer_store_b128((u32x4){pack2n(sc[0], sc[1]), pack2n(sc[2], sc[3]), pack2n(s2[0], s2[1]), pack2n(s2[2], s2[3])}, rs, off, o1, 16);
    }
  };
  VM_DRAIN();
  kq_get(0);
  unsigned o1 = sct_slot(ck, 0u), o1m = scm_slot(ck, 0u);
  unsigned t2o = ts_tok(h, j) + (unsigned)((c >> 2) * 256 + g * 64 + (c & 3) * 16);
  TokScores t2;
  if (w == 3) { PIN_V(t2o); t2.get(lds, 0, lane); t2.run(0); t2.run(1); t2.store(rs, t2o, ts_slot(ck, 0)); }
  if (w < 2) pub1(p1v, w == 0 ? o1m : o1);
  const unsigned tst = now32() | 1u;
  if (w == 3 && ST_X) __builtin_amdgcn_raw_buffer_store_b32(tst, rs, lane < 2 ? ts_piece(b >> 2) - ACC_TS + (b & 3) * 4 + lane * 8192 : 0x7ffffff0, (unsigned)(OFF_ACC + (SR - 1) * ACC_STEP) + ACC_TS, 16);
  LOGP(2);
  BAR();
  LOGP(3);
#pragma unroll
  for (int i = 0; i < 10; ++i) x_fetch(x_src(a, 2, b), lds0, 2, 10 * w + i, lane);
  unit(1);
  VM_DRAIN();
  unsigned o2 = (unsigned)OFF_ACC, o2p = 0u, tw = 0u;
  asm volatile("" : "+s"(o2p));
  unsigned tc0 = 0u, tstar = 0u, tg = 0u;
  int dmin = 0, dmax = 0;
  int e1 = 100000;
  unsigned offs = 100u;
  TokTask tk;
  PvTask2 pv2;
  if (w == 3) pv2.init(gp, lane);
  if (w == 2) tk.init(h, j, lane);
  unsigned o4 = 0u;
  unsigned o3, o3p;
  u32x16 P = {0u, 0u, 0u, 0u, 0u, 0u, 0u, 0u, 0u, 0u, 0u, 0u, 0u, 0u, 0u, 0u}, V = P;
  u32x8 V8 = {0u, 0u, 0u, 0u, 0u, 0u, 0u, 0u};
  if (w < 2) asm volatile("" : "+{v[208:223]}"(P), "+{v[184:191]}"(V8), "+{v[192:207]}"(V));
  unsigned tso = lane < 2 ? ts_piece(b >> 2) + (unsigned)((b & 3) * 4 + lane * 8192) : 0x7ffffff0u;
  if (w < 2) PIN_V(tso);
  float minf = g >= 2 ? -INFINITY : 0.f;
  if (w < 2) PIN_V(minf);
  int sv = NV - 1;
  bf16x8 vox[5];
  unsigned vma[3], need2;
  u32x4 slo, shi;
  float k1;
  const u16* xsrc;
  float invv = 1.0f;
  auto prep0 = [&](int s) {
    xsrc = x_src(a, s + 3, b);
    if (w == 2) { o3p = ts_slot(ck, (s + 1 - LAG3) & 3); o3 = ts_slot(ck, imax(s, 0)); o4 = pt_slot(ck, imax(s - LAG3, 0)); PIN_S(o3); }
    if (w == 3) { o3p = pt_slot(ck, (s - LAG4) & 3); o4 = ts_slot(ck, s + 1); }
    PIN_S(xsrc); PIN_S(o3p); PIN_S(o4);
    __builtin_amdgcn_sched_barrier(0);
  };
  auto prep = [&](int s) {
    o2 = (unsigned)OFF_ACC + (unsigned)(s % SR) * (unsigned)ACC_STEP;
    sv = sv == NV - 1 ? 0 : sv + 1;
    PIN_S(o2); PIN_S(sv);
    if (w < 2) {
      PIN_S(tc0);
      o1 = sct_slot(ck, s % SR); o1m = scm_slot(ck, s % SR);
      need2 = 4u * (unsigned)(s + 1) + 2u * ((unsigned)(s + 2) / NSEG);
      PIN_S(o1); PIN_S(o1m); PIN_S(need2);
      __builtin_amdgcn_sched_barrier(0);
      kq_get(s);
    }
    invv = misc[4 + (s & 1)];
  };
  auto prep2 = [&](int s) {
#pragma unroll
    for (int k = 0; k < 3; ++k) {
      const int t = k == 0 ? 2 : (k == 1 ? far : 1 - far);
      const char* p = lds + L_VOX + sv * 5120 + t * 1024 + (plane & 47) * 16 + (g & 1) * 8;
      auto rd = [&](const char* q) { const u32x2 a0 = *(const u32x2*)q, a1 = *(const u32x2*)(q + 256); return mk8(a0[0], a0[1], a1[0], a1[1]); };
      vox[k == 0 ? 0 : 2 * k - 1] = rd(p);
      if (k) vox[2 * k] = rd(p + 3072);
      vma[k] = lds0 + (unsigned)(L_VOM + sv * 1280 + t * 256 + (plane & 31) * 8);
      PIN_V(vma[k]);
    }
    __builtin_amdgcn_sched_barrier(0);
    inv = u2f(__builtin_amdgcn_readfirstlane(f2u(invv)));
    slo = __builtin_bit_cast(u32x4, SelLo); shi = __builtin_bit_cast(u32x4, SelHi);
#pragma unroll
    for (int i = 0; i < 4; ++i) { slo[i] = ((slo[i] >> 7) & 0x10001u) * (f2u(inv) >> 16); shi[i] = ((shi[i] >> 7) & 0x10001u) * (f2u(inv) >> 16); }
    if (F0A) { xsrc = x_src(a, s + 3, b); PIN_S(xsrc); }
    k1 = KEXP * inv;
    PIN_V(k1); PIN_V(slo); PIN_V(shi);
    asm volatile("" : "+v"(vox[0]), "+v"(vox[1]), "+v"(vox[2]), "+v"(vox[3]), "+v"(vox[4]));
    __builtin_amdgcn_sched_barrier(0);
  };
  auto rows = [&](int s) {
    const int dl = spin_until(tw);
    if (w < 2) tc0 = cyc32();
    __builtin_amdgcn_sched_barrier(0);
    STAMP(1);
    unsigned spins = 0;
    auto pass = [&](bool first) {
      u32x4 x[2 * NC];
#pragma unroll
      for (int f = 0; f < NC; ++f) {
        if (f && w < 2 && first) { prep(s); asm volatile(".rept %0\n\ts_nop 3\n\t.endr" :: "n"(P2_G1)); }
        else if (f) asm volatile(".rept %0\n\ts_nop 3\n\t.endr" :: "n"(w < 2 ? P2_G0 : (f == 1 ? P2_G2 : P2_G3)));
        __builtin_amdgcn_sched_barrier(0);
        x[2 * f] = SC1_LOADS(l2off[2 * f], o2p); x[2 * f + 1] = SC1_LOADS(l2off[2 * f + 1], o2p);
        __builtin_amdgcn_sched_barrier(0);
      }
      if (first) lds_inc<L_MISC + 92>(lds0);
      if (first) { if (w >= 2) prep(s); else prep2(s); }
      if (w >= 2 && first && P2_W) lds_spin<L_MISC + 92, 0>(lds0, 4u * (unsigned)s);
      if (w == 2 && first) tk.store(rs, o4, s > LAG3 ? 0u : 0x7ff00000u);
      if (w == 3 && first) t2.store(rs, t2o, o4);
      if (first && w < 2) {
        p1cur = p1v; offs = (offs - (unsigned)dl) & ~3u;
        PIN_V(p1cur); PIN_S(offs);
        tstar = tc0 + (unsigned)(e1 + D1_M + (w == 1 ? D1_W1 : 0)) - offs * 20u;
        dmin = offs < 40u ? D1_MIN : REC_D1;
        dmin = s < EARLY_N ? REC_D1 + EARLY_D1 : dmin;
        dmax = REC_D1;
        PIN_S(tstar); PIN_S(dmin); PIN_S(dmax);
      }
      __builtin_amdgcn_sched_barrier(0);
      unsigned ok = 32u;
#pragma unroll
      for (int t = 0; t < 3; ++t) { acc[t] = Z4; PIN_V(acc[t]); }
      __builtin_amdgcn_sched_barrier(0);
      bool r = true;
#pragma unroll
      for (int f = 0; f < NC; ++f) {
        unsigned p[4];
#pragma unroll
        for (int k = 0; k < 2; ++k) {
          const u32x4 v = x[2 * f + k];
          p[2 * k] = pack2((float)(int)v[0], (float)(int)v[1]); p[2 * k + 1] = pack2((float)(int)v[2], (float)(int)v[3]);
          asm volatile("" : "+v"(p[2 * k]), "+v"(p[2 * k + 1]));
          __builtin_amdgcn_sched_barrier(0);
        }
        ok = (ok & x[2 * f][0] & x[2 * f][2]) & (x[2 * f + 1][0] & x[2 * f + 1][2]);
        PIN_V(ok);
        __builtin_amdgcn_sched_barrier(0);
        bm[f] = mk8(p[0], p[1], p[2], p[3]);
        acc[0] = smfma64(bm[f], Wqk[0][f], acc[0], sidx);
        __builtin_amdgcn_sched_barrier(0);
        bm_put(f);
        acc[1] = smfma64(bm[f], Wqk[1][f], acc[1], sidx);
        __builtin_amdgcn_sched_barrier(0);
        if (f == NC - 1) { u64 bad = __builtin_amdgcn_ballot_w64(ok == 0u); PIN_S(bad); r = bad == 0ull; }
        __builtin_amdgcn_sched_barrier(0);
        acc[2] = smfma64(bm[f], Wqk[2][f], acc[2], sidx);
        __builtin_amdgcn_sched_barrier(0);
      }
      red_put();
      return r;
    };
    if (__builtin_expect(!pass(true), 0)) {
      FC(0);
      do {
        if (dead || ++spins > SPIN_CAP) { dead = true; break; }
        do tg += RETRY_T; while ((int)(now32() - tg) > RETRY_L);
        wait_until(tg);
        if (w < 2) { tc0 = cyc32(); e1 += 24; tstar = tc0 + (unsigned)(e1 + D1_M + RETRY_D); }
        asm volatile("" ::: "memory");
      } while (!pass(false));
    }
    STAMPV(12, spins); LOGF(1, spins);
    TSTAMP(2, now32()); TSTAMP(3, spins);
  };
  if (w >= 2) prep0(-1);
  prep(0);
  if (w < 2) prep2(0);
  CODE_PAD((PADR >> (4 * w)) & 15);
#pragma unroll 1
  for (int s = 0;; ++s) {
    if (__builtin_expect(o2p != 0u, 1)) rows(s);
    if (w < 2 && __builtin_expect(s == NSEG - 1, 0)) break;
    STAMP(2);
    BAR();
    STAMP(3);
    if (w < 2) pub1(p1cur, w == 0 ? o1m : o1);
    if (w == 2) tk.load(rs, o3p);
    if (w == 3) pv2.load(rs, o3p);
    const unsigned tp1 = cyc32();
    __builtin_amdgcn_sched_barrier(0);
    if (w == 2) {
      __builtin_amdgcn_s_setprio(W2_P);
      prep0(s);
      if (W2_D) spin_cyc(tp1 + W2_D);
      fr = qk_frag(lds + qo4, lds + qo2, lds + qo2b);
      t2_mem(fr, qx[0], qx[1], rs, o3, h, j, invv * KEXP, lane);
      __builtin_amdgcn_sched_barrier(0);
      asm volatile("s_waitcnt vmcnt(1)" ::: "memory");
      lds_inc<L_MISC + 28>(lds0);
      kq_get(s + 1);
    }
    if (w == 3) {
      __builtin_amdgcn_s_setprio(T4_P);
      prep0(s);
      t2.get(lds, s + 1, lane); t2.run(0); t2.run(1);
      __builtin_amdgcn_sched_barrier(0);
    }
    if (w >= 2 && __builtin_expect(s == NSEG - 2, 0)) { VM_DRAIN(); lds_inc<L_MISC + 28>(lds0); }
    if (w >= 2) { __builtin_amdgcn_s_setprio(UN_P); unit(s + 2); }
    TSTAMP(0, now32());
    STAMP(4);
    __builtin_amdgcn_sched_barrier(0);
    unsigned tpub = 0;
    u32x2 sw;
    if (w == 2) {
      STAMP(5);
      __builtin_amdgcn_sched_barrier(0);
      STAMP(6);
      ARRIVE(0);
      tk.run(j, lane, SelLo);
      if (__builtin_expect(s == NSEG - 1, 0)) { tlast = tp1; break; }
      if (F2B) { lds_spin<L_MISC + 48, 1>(lds0, 2u * (unsigned)(s + 1)); x_fetch_n<F2B>(xsrc, lds0, s + 3, P2B, lane * 16); }
      __builtin_amdgcn_s_setprio(SP_P);
    }
    if (w == 3) {
      pv2.run<false>(lds, sv < 1 + LAG4 ? sv + NV - 1 - LAG4 : sv - 1 - LAG4, lane, rs, 0u);
      if (__builtin_expect(s == NSEG - 1, 0)) { tlast = tp1; break; }
      if (ST_E) lds_inc<L_MISC + 112>(lds0);
      STAMP(11);
      __builtin_amdgcn_s_setprio(SP_P);
      STAMP(5);
      ARRIVE(0);
      if (F3) { lds_spin<L_MISC + 48, 1>(lds0, 2u * (unsigned)(s + 1)); x_fetch_n<F3>(xsrc, lds0, s + 3, 40 - F3C - F3, lane * 16); }
      STAMP(6);
    }
    if (w < 2) {
      __builtin_amdgcn_s_setprio(D1_P);
      x_fetch_n<F0A>(xsrc, lds0, s + 3, PA + w * F0A, lane * 16);
      int dq = imin((int)(tstar - tp1), dmax);
      dq = dq < -D1_LATE ? dmax : dq;
      PIN_S(dq);
      spin_exact(tp1 + (unsigned)imax(dq, dmin));
      STAMP(5);
      u32x32 S;
      auto sc_load = [&]() {
        asm volatile("buffer_load_dwordx4 v[224:227], %2, %4, %5 offen sc1\n\t.rept %7\n\ts_nop 3\n\t.endr\n\t"
                     "buffer_load_dwordx4 v[228:231], %3, %4, %6 offen sc1\n\tbuffer_load_dwordx4 v[232:235], %3, %4, %6 offen offset:2048 sc1\n\t"
                     "buffer_load_dwordx4 v[236:239], %3, %4, %6 offen offset:256 sc1\n\tbuffer_load_dwordx4 v[240:243], %3, %4, %6 offen offset:2304 sc1\n\t"
                     "v_mov_b32 v220, 0x42000000\n\tv_mov_b32 v221, 0\n\tv_mov_b32 v222, 0x42000000\n\tv_mov_b32 v223, 0"
                     : "=&{v[224:255]}"(S), "+{v[208:223]}"(P) : "v"(l1offm), "v"(l1off), "s"(rs), "s"(o1m), "s"(o1), "n"(P1_G) : "memory");
      };
      sc_load();
      __builtin_amdgcn_sched_barrier(0);
      lds_inc<L_MISC + 48>(lds0);
      STAMP(6);
      ARRIVE(0);
      lds_spin<L_MISC + 28, 0>(lds0, need2);
      __builtin_amdgcn_s_setprio(3);
      asm volatile("ds_read_b64 v[192:193], %3\n\tds_read_b64 v[196:197], %3 offset:768\n\tds_read_b64 v[200:201], %4\n\tds_read_b64 v[204:205], %4 offset:768\n\tds_read_b64 v[184:185], %2"
                   : "+{v[184:191]}"(V8), "+{v[192:207]}"(V) : "v"(vma[0]), "v"(vma[1]), "v"(vma[2]) : "memory");
      __builtin_amdgcn_sched_barrier(0);
      {
        const int de = imin((int)(tp1 - tc0) - e1, D1_C);
        e1 += s == 0 ? 0 : de >> (s < EARLY_N && de < -D1_C ? 0 : D1_G);
        PIN_S(e1);
      }
      STAMP(11);
      __builtin_amdgcn_sched_barrier(0);
      {
        unsigned spins = 0;
        u64 miss, bad;
#define MM(d, a, b, c) "v_mfma_f32_16x16x32_bf16 " d ", " a ", " b ", " c "\n\t"
#define FM(d, x, r, ctrl) "v_fmac_f32_dpp " d ", " x ", " r " " ctrl "\n\t"
#define ID_ "quad_perm:[0,1,2,3] row_mask:0xf bank_mask:"
#define HM_ "row_half_mirror row_mask:0xf bank_mask:"
#define R8_ "row_ror:8 row_mask:0xf bank_mask:"
#define MI_ "row_mirror row_mask:0xf bank_mask:"
        auto soft = [&]() {
          asm volatile(
            "s_waitcnt vmcnt(4)\n\t" MM("v[244:247]", "%6", "v[224:227]", "0") "v_min3_u32 v214, v224, v225, v226\n\t"
            "s_waitcnt vmcnt(3)\n\t" MM("v[248:251]", "%6", "v[228:231]", "0") "v_min3_u32 v214, v214, v227, v228\n\tv_min3_u32 v214, v214, v229, v230\n\t"
            "s_waitcnt vmcnt(2)\n\t" MM("v[248:251]", "%6", "v[232:235]", "v[248:251]") "v_min3_u32 v214, v214, v231, v232\n\t"
            "v_add_f32_dpp v212, v244, v244 " R8_ "0x3\n\tv_add_f32_dpp v213, v245, v245 " R8_ "0x3\n\t"
            "s_waitcnt vmcnt(1)\n\t" MM("v[248:251]", "%7", "v[236:239]", "v[248:251]")
            "v_add_f32_dpp v212, v246, v246 " R8_ "0xc\n\tv_add_f32_dpp v213, v247, v247 " R8_ "0xc\n\t"
            "s_waitcnt vmcnt(0) lgkmcnt(0)\n\t" MM("v[248:251]", "%7", "v[240:243]", "v[248:251]")
            "v_fma_f32 v212, v212, %14, %15\n\tv_fma_f32 v213, v213, %14, %15\n\tv_exp_f32 v216, v212\n\tv_exp_f32 v217, v213\n\t"
            "v_min3_u32 v214, v214, v233, v234\n\tv_cvt_pk_bf16_f32 v208, v216, v217\n\tv_min3_u32 v214, v214, v235, v236\n\tds_read_b96 v[188:190], %16 offset:%18\n\t"
            "v_exp_f32 v216, v248\n\tv_mov_b32_dpp v209, v208 " R8_ "0xf\n\tv_exp_f32 v217, v249\n\tv_exp_f32 v218, v250\n\t"
            MM("v[252:255]", "%8", "v[208:211]", "0") "v_exp_f32 v219, v251\n\tv_cvt_pk_bf16_f32 v232, v216, v217\n\t"
            MM("v[224:227]", "v[192:195]", "v[208:211]", "0") "v_cvt_pk_bf16_f32 v233, v218, v219\n\tv_min3_u32 v214, v214, v237, v238\n\tv_mov_b32_dpp v234, v232 " R8_ "0xf\n\t"
            MM("v[228:231]", "v[196:199]", "v[208:211]", "0") "v_mov_b32_dpp v235, v233 " R8_ "0xf\n\t"
            MM("v[244:247]", "v[204:207]", "v[208:211]", "0") "v_min3_u32 v214, v214, v239, v240\n\t"
            MM("v[252:255]", "%8", "v[232:235]", "v[252:255]") MM("v[224:227]", "%10", "v[232:235]", "v[224:227]") "v_min3_u32 v214, v214, v241, v242\n\t"
            MM("v[228:231]", "%11", "v[232:235]", "v[228:231]") "s_waitcnt lgkmcnt(0)\n\tv_sub_u32 v215, v190, %17 clamp\n\t"
            MM("v[236:239]", "v[200:203]", "v[208:211]", "0") "v_min3_u32 v214, v214, v243, v215\n\tv_cmp_eq_u32 %4, 0, v214\n\tv_rcp_f32 v216, v252\n\t"
            MM("v[240:243]", "v[184:187]", "v[208:211]", "0") "v_mul_f32 v216, v216, v188\n\tv_subrev_u32 v215, 0x2f800000, v252\n\t"
            MM("v[236:239]", "%12", "v[232:235]", "v[236:239]") FM("v220", "v224", "v216", ID_ "0x1") FM("v221", "v225", "v216", ID_ "0x1")
            "v_mov_b32_dpp v217, v216 " HM_ "0xf\n\t"
            MM("v[244:247]", "%13", "v[232:235]", "v[244:247]") FM("v220", "v230", "v216", ID_ "0x2") FM("v221", "v231", "v216", ID_ "0x2")
            MM("v[240:243]", "%9", "v[232:235]", "v[240:243]") FM("v220", "v228", "v217", HM_ "0x1") FM("v221", "v229", "v217", HM_ "0x1")
            "v_cmp_ge_u32 vcc, v215, v189\n\t" FM("v220", "v226", "v217", HM_ "0x2") FM("v221", "v227", "v217", HM_ "0x2")
            "s_or_b64 %5, %4, vcc"
            : "+{v[224:255]}"(S), "+{v[208:223]}"(P), "+{v[184:191]}"(V8), "+{v[192:207]}"(V), "=&s"(miss), "=&s"(bad)
            : "v"(slo), "v"(shi), "v"(ONES), "v"(vox[0]), "v"(vox[1]), "v"(vox[2]), "v"(vox[3]), "v"(vox[4]), "v"(k1), "v"(minf), "v"(lds0), "s"(s), "n"(L_MISC + 32)
            : "vcc", "scc", "memory");
        };
        soft();
        if (__builtin_expect(bad != 0ull, 0)) {
          if (miss) FC(1); else FC(2);
          while (miss != 0ull && !dead) {
            if (++spins > SPIN_CAP) dead = true;
            asm volatile("s_nop 7\n\ts_nop 7" ::: "memory");
            sc_load(); soft();
          }
          if (bad != 0ull) {
            u32x4 y[5];
#pragma unroll
            for (int k = 0; k < 4; ++k) y[k] = SC1_LOADS(l1off + (k & 1) * 2048 + (k >> 1) * 256, o1);
            y[4] = SC1_LOADS(l1offm, o1m);
            const f32x4 d0 = mfma32(as8(slo), as8(y[4]), Z4);
            f32x4 d1 = mfma32(as8(shi), as8(y[3]), mfma32(as8(shi), as8(y[2]), mfma32(as8(slo), as8(y[1]), mfma32(as8(slo), as8(y[0]), Z4))));
            const bool lo = c < 8;
            const float m0 = __builtin_fmaf((lo ? d0[0] : d0[2]) + dppf<DPP_ROR8>(lo ? d0[2] : d0[0]), k1, minf);
            const float m1 = __builtin_fmaf((lo ? d0[1] : d0[3]) + dppf<DPP_ROR8>(lo ? d0[3] : d0[1]), k1, minf);
            float mx = fmx(fmx(fmx(d1[0], d1[1]), d1[2]), fmx(fmx(d1[3], m0), m1));
            mx = fmx(mx, dppf<DPP_ROR8>(mx));
            mx = x32mx(x16mx(mx));
            d1 -= mx;
            const unsigned mm = pack2(__builtin_amdgcn_exp2f(m0 - mx), __builtin_amdgcn_exp2f(m1 - mx));
            const unsigned t01 = pack2(__builtin_amdgcn_exp2f(d1[0]), __builtin_amdgcn_exp2f(d1[1])), t23 = pack2(__builtin_amdgcn_exp2f(d1[2]), __builtin_amdgcn_exp2f(d1[3]));
            const bf16x8 pm = mk8(mm, dpp_ror8(mm), 0u, 0u), pt = mk8(t01, t23, dpp_ror8(t01), dpp_ror8(t23));
            const float r = __builtin_amdgcn_rcpf(mfma32(ONES, pt, mfma32(ONES, pm, Z4))[0]) * u2f(V8[4]), rm = dppf<0x141>(r);
            f32x4 o[5];
#pragma unroll
            for (int k = 0; k < 5; ++k) o[k] = mfma32(vox[k], pt, mfma32(k ? mk8(V[4 * k - 4], V[4 * k - 3], 0u, 0u) : mk8(V8[0], V8[1], 0u, 0u), pm, Z4));
#pragma unroll
            for (int i = 0; i < 2; ++i) {
              const float oa = dppf<0x141>(o[2][i]), ob = dppf<0x141>(o[1][2 + i]);
              P[12 + i] = f2u(__builtin_fmaf(hd ? ob : oa, rm, __builtin_fmaf(hd ? o[2][2 + i] : o[1][i], r, i ? 0.f : 32.0f)));
            }
            P[8] = f2u(r); P[9] = f2u(rm);
#pragma unroll
            for (int i = 0; i < 4; ++i) { S[12 + i] = f2u(o[3][i]); S[16 + i] = f2u(o[0][i]); S[20 + i] = f2u(o[4][i]); }
            asm volatile("s_nop 7\n\ts_nop 7" : "+{v[224:255]}"(S), "+{v[208:223]}"(P));
          }
        }
        STAMP(7); STAMPV(13, spins); LOGF(0, spins);
        {
          asm volatile("v_cvt_i32_f32 v218, v220\n\tv_cvt_i32_f32 v219, v221\n\tv_and_or_b32 v218, v218, %7, 2\n\tbuffer_atomic_add_x2 v[218:219], %3, %5, %6 offen\n\t"
                       FM("v222", "v236", "v216", R8_ "0x4") FM("v223", "v237", "v216", R8_ "0x4") "ds_read_b64 %2, %8 offset:%9\n\t"
                       FM("v222", "v238", "v217", MI_ "0x8") FM("v223", "v239", "v217", MI_ "0x8") "v_permlane32_swap_b32 v240, v242\n\t"
                       FM("v222", "v244", "v217", MI_ "0x4") FM("v223", "v245", "v217", MI_ "0x4") "v_permlane32_swap_b32 v241, v243\n\t"
                       FM("v222", "v246", "v216", R8_ "0x8") FM("v223", "v247", "v216", R8_ "0x8")
                       FM("v222", "v240", "v216", ID_ "0x1") FM("v223", "v241", "v216", ID_ "0x1") FM("v222", "v242", "v217", HM_ "0x1") FM("v223", "v243", "v217", HM_ "0x1")
                       "v_cvt_i32_f32 v212, v222\n\tv_cvt_i32_f32 v213, v223\n\tv_and_or_b32 v212, v212, %7, 2\n\tbuffer_atomic_add_x2 v[212:213], %4, %5, %6 offen\n\t"
                       "s_waitcnt lgkmcnt(0)"
                       : "+{v[224:255]}"(S), "+{v[208:223]}"(P), "=&v"(sw) : "v"(acco), "v"(acco2), "s"(rs), "s"(o2), "s"(~63u), "v"(lds0), "n"(L_MISC + 96) : "memory");
          __builtin_amdgcn_sched_barrier(0);
          tpub = now32();
          lds_inc<L_MISC + 104 + 4 * (w & 1)>(lds0);
          asm volatile("buffer_atomic_umax %0, %1, %2, %3 offen" :: "v"(tpub | 1u), "v"(tso), "s"(rs), "s"(o2) : "memory");
          STAMP(10);
        }
      }
    }
    STAMP(8);
    ARRIVE(1);
    o2p = o2;
    PIN_S(o2p);
    if (w < 2) {
      if (__builtin_expect(__builtin_amdgcn_readfirstlane(sw[1]) <= (unsigned)s, 0)) {
        lds_spin<L_MISC + 100, 0>(lds0, (unsigned)s + 1u);
        sw[0] = __hip_atomic_load((unsigned*)(misc + 24), __ATOMIC_RELAXED, __HIP_MEMORY_SCOPE_WORKGROUP);
      }
    } else {
      u32x4 q;
      asm volatile("1:\n\tds_read_b128 %0, %1 offset:%3\n\t.if %4\n\ts_sleep 1\n\t.endif\n\ts_waitcnt lgkmcnt(0)\n\tv_min3_u32 v201, v201, v202, v203\n\tv_cmp_ge_u32 vcc, %2, v201\n\ts_cbranch_vccnz 1b"
                   : "=&{v[200:203]}"(q) : "v"(lds0), "s"(s), "n"(L_MISC + 96), "n"(SP_SL) : "vcc", "memory");
      __builtin_amdgcn_s_setprio(3);
      tpub = now32();
      sw[0] = q[0];
    }
    STAMP(9);
    static_assert(F3C == 0 && F2C == 0 && F0C == 0, "nothing behind the atomics");
    LOGT(0, tpub); LOGT(1, tb2_);
    TSTAMP(1, tpub);
    ARRIVE_OUT();
    const unsigned an = __builtin_amdgcn_readfirstlane(sw[0]);
    const int ahead = (int)(an - tpub);
    tw = tpub + (unsigned)imax(ahead, w < 2 ? SCH_DMIN : 0);
    offs = tw - an;
    tg = an;
    if (__builtin_expect((unsigned)(ahead + SCH_BX + (w < 2 ? 0 : GO_T)) > 1000u, 0)) {
      tw = tg = ahead > 0 || ahead < -20000 ? tpub + SCH_FIRST : an + (unsigned)(RETRY_G - (w < 2 ? 0 : GO_T) - ahead + RETRY_T - 1) / RETRY_T * RETRY_T;
      offs = 0u;
    }
  }
  STAMP_OUT();
  LOGP(4);
  __builtin_amdgcn_s_setprio(3);
  if (w < 2) { BAR(); tlast = cyc32(); }
  if (w == 2) tk.store(rs, o4);
  if (w == 3) {
    const unsigned oo = op_slot(NSEG - 2 - LAG4) + (unsigned)((m * 32 + gp) * 1280);
    const char* sp = lds + L_STG + 2048;
    const u32x4 v0 = *(const u32x4*)(sp + lane * 16), v1 = *(const u32x4*)(sp + 1280 + lane * 16), v2 = *(const u32x4*)(sp + (g & 1) * 1280 + 1024 + c * 16);
    SC1_STORE(v0, oo + lane * 16); SC1_STORE(v1, oo + 20480 + lane * 16); SC1_STORE(v2, lane < 32 ? oo + g * 20480 + 1024 + c * 16 : 0x7ffffff0u);
  }
  if (dead) tlast |= 1u; else tlast &= ~1u;
}

__device__ __forceinline__ void drain(const RecArgs& a, char* lds, const __amdgpu_buffer_rsrc_t rs, const int w, const unsigned tp1) {
  static_assert(LAG3 == 2 && LAG4 == 3 && LAG5 == 6, "drain schedule");
  const int lane = __builtin_amdgcn_workitem_id_x() & 63, b = __builtin_amdgcn_workgroup_id_x(), c = lane & 15;
  const int gp = (b & 7) * 2 + (b >> 7), m = (b >> 3) & 15, j = m & 7;
  unsigned* const misc = (unsigned*)(lds + L_MISC);
  const unsigned lds0 = (unsigned)(uintptr_t)(LAS char*)lds, ck = __builtin_amdgcn_readfirstlane(misc[11]);
  bool dead = tp1 & 1u;
  auto poll = [&](auto load, auto ready) {
    for (unsigned spins = 0;;) {
      asm volatile("" ::: "memory");
      load();
      if (ready() || dead) break;
      if (++spins > SPIN_CAP) dead = true;
      if (END_SL) __builtin_amdgcn_s_sleep(END_SL);
    }
  };
  LOGD(0);
#define TASK(ty, sg, tm) ((u64)((ty) | ((sg) - 56) << 2 | ((tm) / 100) << 5))
  u64 todo = w == 0 ? TASK(1, 63, END_E1) | TASK(2, 63, END_E2) << 12 | TASK(3, 62, END_E3) << 24
           : w == 1 ? TASK(1, 62, 0) | TASK(2, 62, END_E62) << 12 | TASK(3, 60, END_E3) << 24
           : w == 2 ? TASK(2, 60, 0) | TASK(3, 56, 0) << 12 : TASK(2, 61, END_E61) | TASK(3, 58, END_E59) << 12;
  TokTask tk;
  PvTask2 pv;
  OutTask ot;
  {
    const u32x4* p = (const u32x4*)(lds + L_DRN) + lane;
    const u32x4 q0 = p[0], q1 = p[64], q2 = p[128];
    tk.l0 = q0[0]; tk.l1 = q0[0] + 16384u; tk.lm = q0[1]; tk.s0 = q0[2]; tk.s1 = q0[3];
    pv.lo = q1[0]; pv.lm = q1[1]; pv.vr = q1[2]; pv.ma = q1[3]; pv.mz = q2[0]; pv.so = q2[1];
    pv.pa[0] = pv.pa[1] = pv.pb[0] = pv.pb[1] = (u32x4){0u, 0u, 0u, 0u};
    ot.t5f = q2[2]; ot.t5o = q2[3]; ot.sel = as8(p[192]);
  }
#pragma unroll 1
  for (int ti = 0; todo; todo >>= 12, ++ti) {
    const int ty = (int)todo & 3, sl = 56 + (((int)todo >> 2) & 7);
    const unsigned tm = tp1 + (((unsigned)todo >> 5) & 0x7fu) * 100u;
    if (ty == 1) {
      wait_cyc(tm);
      LOGD(3 * ti + 1);
      poll([&]() { tk.load(rs, ts_slot(ck, sl)); }, [&]() { return tk.ready(); });
      LOGD(3 * ti + 2);
      tk.run(j, lane, ot.sel); tk.store(rs, pt_slot(ck, sl));
    } else if (ty == 2) {
      const unsigned t0 = cyc32();
      if (sl >= NSEG - 2) { lds_wait(misc + 7, 4u * NSEG + 3u); wait_cyc(t0 + END_G2); }
      wait_cyc(tm);
      LOGD(3 * ti + 1);
      poll([&]() { pv.load(rs, pt_slot(ck, sl)); }, [&]() { return pv.ready(); });
      LOGD(3 * ti + 2);
      pv.run<true>(lds, sl % NV, lane, rs, op_slot(sl) + (unsigned)((m * 32 + gp) * 1280));
      if (sl == NSEG - 1) misc[14] = cyc32() | 1u;
    } else {
      if (sl >= NSEG - 4) { lds_wait(misc + 14, 1u); wait_cyc(__builtin_amdgcn_readfirstlane(misc[14]) + END_G3); }
      wait_cyc(tm);
      LOGD(3 * ti + 1);
      OutTask o2 = ot;
      poll([&]() { o2.load(a, rs, sl + 1, b); ot.load(a, rs, sl, b); }, [&]() { return ot.ready(b) & o2.ready(b); });
      LOGD(3 * ti + 2);
      o2.run(lane); o2.store(a, sl + 1, b, lane);
      ot.run(lane); ot.store(a, sl, b, lane);
    }
    LOGD(3 * ti + 3);
  }
  VM_DRAIN();
  if (w != 0) lds_inc<L_MISC + 64>(lds0);
  else {
    constexpr unsigned o2 = (unsigned)OFF_ACC + (unsigned)((NSEG - 1) % SR) * (unsigned)ACC_STEP;
    lds_wait(misc + 16, 3u);
    LOGD(10);
    if (lane == 0) __hip_atomic_fetch_add((gu64*)((GAS char*)(a.ws + o2) + acc_off(m * 4, 0)), (u64)1, RLX_AGENT);
    wait_until(now32() + SCH_DRAIN);
    wait_cyc(tp1 + END_E4);
    unsigned x;
    if (qkv_row(b) + 48 >= 55 && qkv_col(b) < 15) poll([&]() { x = __builtin_amdgcn_raw_buffer_load_b32(rs, acc_off(c * 4, 0), o2, 16); }, [&]() { return wave_all((x & 16u) != 0u); });
    lds_inc<L_MISC + 68>(lds0);
    LOGD(11);
  }
  if (qkv_row(b) + 48 >= 55 && qkv_col(b) < 15) {
    lds_wait(misc + 18, 8u);
    lds_spin<L_MISC + 68, 1>(lds0, 1u);
    x_fetch_n<10>(x_src(a, NSEG + 3, b), lds0, 3, 10 * w, lane * 16);
    VM_DRAIN();
    lds_inc<L_MISC + 68>(lds0);
  }
  LOGP(8);
}

__device__ __forceinline__ void proj_front(const RecArgs& a, char* lds, const __amdgpu_buffer_rsrc_t rs, const int v) {
  const int lane = __builtin_amdgcn_workitem_id_x() & 63, b = __builtin_amdgcn_workgroup_id_x(), c = lane & 15, g = lane >> 4;
  const int rb = (b & 7) + 8 * (b >> 5), cq = (b >> 3) & 3;
  const unsigned lds0 = (unsigned)(uintptr_t)(LAS char*)lds;
  unsigned* const cnt = (unsigned*)(lds + L_MISC) + 20;
  LOGF2(0);
  float* const red = (float*)(lds + L_RED1);
  float* const fin = red + 640;
  const f32x4 Z4 = {0.f, 0.f, 0.f, 0.f};
#pragma unroll
  for (int i = 0; i < 8; ++i) x_fetch((const u16*)(a.ws + OFF_OB) + (size_t)rb * 32 * DM, lds0, 0, 8 * v + i, lane);
  bf16x8 W[2][20];
#pragma unroll
  for (int k = 0; k < 40; ++k) W[k & 1][k >> 1] = *(const bf16x8*)(a.ws + OFF_WOBT + ((size_t)(cq * 10 + 2 * v + (k & 1)) * 20 + (k >> 1)) * 1024 + lane * 16);
  const unsigned fo = (unsigned)(rb * 40960 + (cq * 5 + v) * 1024 + ((g >> 1) * 16 + c) * 16), f8 = fo + (g & 1) * 8, f16 = fo + (g & 1) * 20480;
  u32x2 xo[2][2];
#pragma unroll
  for (int k = 0; k < 4; ++k)
    xo[k >> 1][k & 1] = __builtin_bit_cast(u32x2, __hip_atomic_load((const gu64*)((const char*)a.xnew + f8 + (k >> 1) * 512 + (k & 1) * 20480), RLX_AGENT));
  asm volatile("s_waitcnt vmcnt(44)" ::: "memory");
  lds_inc<L_MISC + 80>(lds0);
  lds_wait(cnt, 5u);
  LOGF2(1);
  f32x4 acc[2][2] = {{Z4, Z4}, {Z4, Z4}};
#pragma unroll
  for (int ks = 0; ks < 20; ++ks)
#pragma unroll
    for (int t = 0; t < 2; ++t) {
      const bf16x8 x = *(const bf16x8*)(lds + L_XN + (t * 20 + ks) * 1024 + lane * 16);
#pragma unroll
      for (int i = 0; i < 2; ++i) acc[i][t] = mfma32(W[i][ks], x, acc[i][t]);
    }
  u32x2 xp[2][2];
  PIN_V(acc[1][1]);
  LOGF2(2);
#pragma unroll
  for (int k = 0; k < 4; ++k) {
    const int i = k >> 1, t = k & 1;
#pragma unroll
    for (int q = 0; q < 2; ++q) {
      const unsigned r = pack2(acc[i][t][2 * q], acc[i][t][2 * q + 1]), x = xo[i][t][q];
      xp[i][t][q] = pack2(u2f(x << 16) + u2f(r << 16), u2f(x & 0xffff0000u) + u2f(r & 0xffff0000u));
      acc[i][t][2 * q] = u2f(xp[i][t][q] << 16); acc[i][t][2 * q + 1] = u2f(xp[i][t][q] & 0xffff0000u);
    }
    const float ml = x32sum(x16sum((acc[i][t][0] + acc[i][t][1]) + (acc[i][t][2] + acc[i][t][3]))) * (1.0f / 16.0f);
    float q = 0.f;
#pragma unroll
    for (int r = 0; r < 4; ++r) { const float d = acc[i][t][r] - ml; q += d * d; }
    q = x32sum(x16sum(q));
    if (g == 0) { red[(2 * v + i) * 32 + t * 16 + c] = ml; red[320 + (2 * v + i) * 32 + t * 16 + c] = q; }
  }
  lds_inc<L_MISC + 80>(lds0);
  if (v == 0) {
    lds_wait(cnt, 10u);
    LOGF2(3);
    float mw[10], ml = 0.f, m2 = 0.f;
#pragma unroll
    for (int i = 0; i < 10; ++i) { mw[i] = red[i * 32 + (lane & 31)]; ml += mw[i]; m2 += red[320 + i * 32 + (lane & 31)]; }
    ml *= 0.1f;
#pragma unroll
    for (int i = 0; i < 10; ++i) m2 += 16.0f * (mw[i] - ml) * (mw[i] - ml);
    const unsigned xs = (unsigned)OFF_LNX + rb * 1024;
    if (lane < 32) SC1_STORE8S(((u32x2){f2u(ml), nz(f2u(m2))}), (cq * 32 + lane) * 8, xs);
    wait_cyc(cyc32() + FR_D);
    u32x2 x0, x1;
    for (unsigned spins = 0; spins < SPIN_CAP; ++spins) {
      x0 = __builtin_amdgcn_raw_buffer_load_b64(rs, lane * 8, xs, 16); x1 = __builtin_amdgcn_raw_buffer_load_b64(rs, 512 + lane * 8, xs, 16);
      if (wave_all(x0[1] != 0u && x1[1] != 0u)) break;
      asm volatile("" ::: "memory");
    }
    const float m0 = u2f(x0[0]), m1 = u2f(x1[0]);
    const float mean = x32sum(m0 + m1) * 0.25f;
    const float M2 = x32sum((u2f(x0[1]) + u2f(x1[1])) + 160.0f * ((m0 - mean) * (m0 - mean) + (m1 - mean) * (m1 - mean)));
    const float mb = rbf(mean), den = rbf(__builtin_sqrtf(rbf(rbf(M2 * (1.0f / DM)) + 1e-5f)));
    if (lane < 32) {
      fin[lane] = mb; fin[32 + lane] = 1.0f / den;
      if (cq == 0) __hip_atomic_store((GAS unsigned*)(a.ws + OFF_XNRM) + (rb * 32 + lane) * 4, f2u(1.02f * (M2 + DM * (mean - mb) * (mean - mb)) / (den * den)), RLX_AGENT);
    }
    lds_inc<L_MISC + 84>(lds0);
  }
  lds_wait(cnt + 1, 1u);
  LOGF2(4);
  const __amdgpu_buffer_rsrc_t rx = __builtin_amdgcn_make_buffer_rsrc(a.ws + OFF_XN, 0, (int)(OFF_QB - OFF_XN), 0x00020000);
  auto put = [&](const u32x2 p0, const u32x2 p1, unsigned so) {
    const auto r0 = __builtin_amdgcn_permlane16_swap(p0[0], p1[0], false, false), r1 = __builtin_amdgcn_permlane16_swap(p0[1], p1[1], false, false);
    __builtin_amdgcn_raw_buffer_store_b128((u32x4){r0[0], r1[0], r0[1], r1[1]}, rx, f16, so, 16);
  };
#pragma unroll
  for (int i = 0; i < 2; ++i) {
    u32x2 y[2];
#pragma unroll
    for (int t = 0; t < 2; ++t) {
      const float mb = fin[t * 16 + c], rden = fin[32 + t * 16 + c];
      y[t] = (u32x2){pack2(rbf(acc[i][t][0] - mb) * rden, rbf(acc[i][t][1] - mb) * rden), pack2(rbf(acc[i][t][2] - mb) * rden, rbf(acc[i][t][3] - mb) * rden)};
    }
    put(y[0], y[1], i * 512);
  }
  VM_DRAIN();
  lds_inc<L_MISC + 80>(lds0);
#pragma unroll
  for (int i = 0; i < 2; ++i) put(xp[i][0], xp[i][1], (unsigned)(OFF_XB - OFF_XN) + i * 512);
  if (v == 0) {
    lds_wait(cnt, 15u);
    if (rb < 4 && lane < 8) __hip_atomic_fetch_add((GAS unsigned*)(a.ws + OFF_RDY) + lane * 64, 1u, RLX_AGENT);
  }
  LOGF2(5);
}

__global__ __launch_bounds__(512, 2) void rec_kernel(RecArgs a) {
  __shared__ __attribute__((aligned(16))) char lds[L_BYTES];
  const int tid = __builtin_amdgcn_workitem_id_x(), wid = __builtin_amdgcn_readfirstlane(tid >> 6), w = wid & 3;
  const __amdgpu_buffer_rsrc_t rs = __builtin_amdgcn_make_buffer_rsrc(a.ws, 0, (int)COMM_BYTES, 0x00020000);
  if (tid < 32) ((float*)(lds + L_MISC))[tid] = (tid == 4 || tid == 5) ? 1.0f : 0.0f;
  BAR();
  if (a.layer && wid < 5) proj_front(a, lds, rs, wid);
  if (wid < 4) {
    unsigned tlast;
    if (w == 0) rec_waves<0>(a, lds, rs, tlast);
    else if (w == 1) rec_waves<1>(a, lds, rs, tlast);
    else if (w == 2) rec_waves<2>(a, lds, rs, tlast);
    else rec_waves<3>(a, lds, rs, tlast);
    drain(a, lds, rs, w, tlast);
  } else {
    unsigned vo[2][2];
    if (w == 0) token_waves<0>(a, lds, rs, vo);
    else if (w == 1) token_waves<1>(a, lds, rs, vo);
    else if (w == 2) token_waves<2>(a, lds, rs, vo);
    else token_waves<3>(a, lds, rs, vo);
    qkv_tail(a, lds, w, vo);
  }
}

#if defined(__HIP_DEVICE_COMPILE__)
#pragma clang attribute pop
#endif

constexpr int KQ = S_ / 4;
#define ATT_P1 6
#define ATT_P2 10
#define ATT_P3 14
#define ATT_THR 6.0f
#define ATT_NQ 4
#define ATT_LMAX 1.0e12f
#if defined(__HIP_DEVICE_COMPILE__)
#pragma clang attribute push(__attribute__((target("no-packed-fp32-ops"))), apply_to = function)
#endif
__global__ __launch_bounds__(256 * ATT_NQ, 4) void attb_kernel(const u16* __restrict__ Qp, const u16* __restrict__ KV, u16* __restrict__ O) {
  __shared__ f32x4 mo_[ATT_NQ][4][2][3][64];
  __shared__ float mm_[ATT_NQ][4][2][64];
  const int tid = __builtin_amdgcn_workitem_id_x(), bid = __builtin_amdgcn_workgroup_id_x();
  __shared__ unsigned sink[64];
  own_code<5000>(tid, sink);
  const int lane = tid & 63, wid = __builtin_amdgcn_readfirstlane(tid >> 6), kh = wid / ATT_NQ, r = lane & 31, hh = lane >> 5;
  const int hd = (bid & 7) * 4 + ((bid >> 3) & 3), qb = (bid >> 5) * ATT_NQ + wid % ATT_NQ;
  const int q0 = qb * 64;
  bf16x8 qa[2]; u32x2 qb2[2];
#pragma unroll
  for (int qt = 0; qt < 2; ++qt) {
    { const u32x2* qp = (const u32x2*)(Qp + ((size_t)hd * S_ + q0 + qt * 32 + r) * QST + 8 * hh); const u32x2 q0_ = qp[0], q1_ = qp[1]; qa[qt] = mk8(q0_[0], q0_[1], q1_[0], q1_[1]); }
    const u32x2 t = *(const u32x2*)(Qp + ((size_t)hd * S_ + q0 + qt * 32 + r) * QST + 16);
    qb2[qt] = t;
  }
  const __amdgpu_buffer_rsrc_t rk = __builtin_amdgcn_make_buffer_rsrc((void*)(KV + ((size_t)hd * 64 + kh * 16) * (KVT / 2)), 0, 16 * KVT, 0x00020000);
  unsigned ok1 = lane * 16, ok2 = hh ? 1600 : 1024 + r * 8, ov = r > 20 ? 1u << 30 : 1280 + (hh * 21 + r) * 16;
  f32x16 o[2];
  float m[2];
  const f32x16 Z16 = {0.f, 0.f, 0.f, 0.f, 0.f, 0.f, 0.f, 0.f, 0.f, 0.f, 0.f, 0.f, 0.f, 0.f, 0.f, 0.f};
  int odd = 0xDDDD;
  asm volatile("" : "+v"(odd));
  struct KvT { u32x4 k1, v0, v1; u32x2 k2; };
  auto fetch_k = [&](KvT& x, int t) {
    x.k1 = __builtin_amdgcn_raw_buffer_load_b128(rk, ok1, imin(t, KQ / 32 - 1) * KVT, 0); x.k2 = __builtin_amdgcn_raw_buffer_load_b64(rk, ok2, imin(t, KQ / 32 - 1) * KVT, 0);
    __builtin_amdgcn_sched_barrier(0);
  };
  auto fetch_v = [&](KvT& x, int t) {
    x.v0 = __builtin_amdgcn_raw_buffer_load_b128(rk, ov, imin(t, KQ / 32 - 1) * KVT, 0); x.v1 = __builtin_amdgcn_raw_buffer_load_b128(rk, ov + 672, imin(t, KQ / 32 - 1) * KVT, 0);
    __builtin_amdgcn_sched_barrier(0);
  };
  auto fetch = [&](int t) { KvT x; __builtin_amdgcn_sched_barrier(0); fetch_k(x, t); fetch_v(x, t); return x; };
  auto qk1 = [&](const KvT& x, int qt) { return __builtin_amdgcn_mfma_f32_32x32x16_bf16(as8(x.k1), qa[qt], Z16, 0, 0, 0); };
  auto qk2 = [&](const KvT& x, int qt, f32x16 c) { return __builtin_amdgcn_mfma_f32_32x32x8bf16_1k(__builtin_bit_cast(v4i16_t, x.k2), __builtin_bit_cast(v4i16_t, qb2[qt]), c, 0, 0, 0); };
  auto pv = [&](const u32x4 v, const f32x16 p, int hi, f32x16 c) {
    const f32x8 ph = hi ? __builtin_shufflevector(p, p, 8, 9, 10, 11, 12, 13, 14, 15) : __builtin_shufflevector(p, p, 0, 1, 2, 3, 4, 5, 6, 7);
    return __builtin_amdgcn_smfmac_f32_32x32x32_bf16(__builtin_bit_cast(hbf16x8, v), __builtin_bit_cast(hbf16x16, ph), c, odd, 0, 0);
  };
  auto tile = [&](const KvT& x, auto MODE) {
    f32x16 sa[2];
#pragma unroll
    for (int qt = 0; qt < 2; ++qt) sa[qt] = qk1(x, qt);
#pragma unroll
    for (int qt = 0; qt < 2; ++qt) sa[qt] = qk2(x, qt, sa[qt]);
#pragma unroll
    for (int qt = 0; qt < 2; ++qt) {
      if (MODE != 0) {
        float mx = fmx(fmx(sa[qt][0], sa[qt][1]), sa[qt][2]);
#pragma unroll
        for (int i = 3; i < 15; i += 2) mx = fmx(fmx(mx, sa[qt][i]), sa[qt][i + 1]);
        mx = fmx(mx, sa[qt][15]);
        if (MODE == 1 || !wave_all(mx <= ATT_THR)) {
          mx = x32mx(mx);
          if (MODE != 1) mx = fmx(mx, 0.f);
          const float mn = m[qt] + mx;
          const unsigned h1 = f2bf(mn); const float r1 = mn - bf2f(h1);
          const unsigned h2 = f2bf(r1); const float r2 = r1 - bf2f(h2);
          const unsigned h3 = f2bf(r2);
          const float me = (bf2f(h1) + bf2f(h2)) + bf2f(h3);
          const float dm = me - m[qt];
          m[qt] = me;
          if (hh) qb2[qt] = (u32x2){(h1 | (h2 << 16)) ^ 0x80008000u, h3 ^ 0x8000u};
          const float alpha = __builtin_amdgcn_exp2f(-dm);
#pragma unroll
          for (int i = 0; i < 16; ++i) { if (MODE != 1) o[qt][i] *= alpha; sa[qt][i] -= dm; }
        }
      }
#pragma unroll
      for (int i = 0; i < 16; ++i) sa[qt][i] = __builtin_amdgcn_exp2f(sa[qt][i]);
      asm volatile("" : "+v"(sa[qt]));
      if (MODE == 1) { o[qt] = Z16; asm volatile("" : "+v"(o[qt])); }
      o[qt] = pv(x.v1, sa[qt], 1, pv(x.v0, sa[qt], 0, o[qt]));
      __builtin_amdgcn_sched_barrier(0);
    }
  };
  typedef std::integral_constant<int, 1> I1; typedef std::integral_constant<int, 2> I2;
  auto reset = [&]() {
    asm volatile("" : "+v"(ok1), "+v"(ok2), "+v"(ov) :: "memory");
    m[0] = m[1] = 0.f;
    if (hh) qb2[0] = qb2[1] = (u32x2){0u, 0u};
  };
  auto slow = [&]() {
    reset();
    tile(fetch(0), I1{});
#pragma unroll 1
    for (int t = 1; t < KQ / 32; ++t) tile(fetch(t), I2{});
  };
  f32x16 sc[2];
  auto phase = [&](int qt, const u32x4 vp, const KvT& xk, const u32x4 v0, auto mid) {
    auto ex = [&](int i0, int i1) {
#pragma unroll
      for (int i = i0; i < i1; ++i) sc[qt][i] = __builtin_amdgcn_exp2f(sc[qt][i]);
      asm volatile("" : "+v"(sc[qt]));
      __builtin_amdgcn_sched_barrier(0);
    };
    o[1 - qt] = pv(vp, sc[1 - qt], 1, o[1 - qt]);
    __builtin_amdgcn_sched_barrier(0);
    ex(0, 3);
    sc[1 - qt] = qk1(xk, 1 - qt);
    __builtin_amdgcn_sched_barrier(0);
    ex(3, 6);
    sc[1 - qt] = qk2(xk, 1 - qt, sc[1 - qt]);
    __builtin_amdgcn_sched_barrier(0);
    ex(6, 10);
    o[qt] = pv(v0, sc[qt], 0, o[qt]);
    __builtin_amdgcn_sched_barrier(0);
    mid();
    ex(10, 16);
  };
  auto fast = [&]() {
    reset();
    KvT xa = fetch(0), xb = fetch(1);
    tile(xa, I1{});
    fetch_k(xa, 2);
    sc[0] = qk2(xb, 0, qk1(xb, 0)); sc[1] = Z16;
    asm volatile("" : "+v"(sc[1]));
    auto step = [&](KvT& x, KvT& y, int t) {
      phase(0, y.v1, x, x.v0, [&]() { fetch_v(y, t + 1); fetch_k(x, t + 2); });
      phase(1, x.v1, y, x.v0, []() {});
    };
    step(xb, xa, 1);
#pragma unroll 1
    for (int t = 2; t < KQ / 32; t += 2) {
      if (t == ATT_P1) __builtin_amdgcn_s_setprio(2);
      if (t == ATT_P2) __builtin_amdgcn_s_setprio(1);
      if (t == ATT_P3) __builtin_amdgcn_s_setprio(0);
      step(xa, xb, t);
      step(xb, xa, t + 1);
    }
    o[1] = pv(xb.v1, sc[1], 1, o[1]);
  };
  if (ATT_P1) __builtin_amdgcn_s_setprio(3);
  fast();
  {
    float big = 0.f;
#pragma unroll
    for (int qt = 0; qt < 2; ++qt) {
      big = fmx(big, hh ? o[qt][8] * (3.0e38f / ATT_LMAX) : __builtin_fabsf(o[qt][8]));
#pragma unroll
      for (int i = 0; i < 12; ++i) if (i != 8) big = fmx(big, (i > 8 && hh) ? 0.f : __builtin_fabsf(o[qt][i]));
    }
    if (__builtin_expect(!wave_all(big <= 3.0e38f), 0)) slow();
  }
  f32x4 (*mo)[2][3][64] = mo_[wid % ATT_NQ];
  float (*mm)[2][64] = mm_[wid % ATT_NQ];
#pragma unroll
  for (int qt = 0; qt < 2; ++qt) {
#pragma unroll
    for (int k = 0; k < 3; ++k) mo[kh][qt][k][lane] = (f32x4){o[qt][4 * k], o[qt][4 * k + 1], o[qt][4 * k + 2], o[qt][4 * k + 3]};
    mm[kh][qt][lane] = m[qt];
  }
  const int qt = kh & 1, grp = kh >> 1, row = q0 + qt * 32 + r, k0 = hd * BD + 4 * hh + 8 * grp;
  auto oaddr = [&](int k) { return O + (((size_t)(row >> 4) * 20 + (k >> 5)) * 64 + ((k & 31) >> 3) * 16 + (row & 15)) * 8 + (k & 7); };
  u16 *op0 = oaddr(k0), *op1 = oaddr(k0 + 16);
  asm volatile("" : "+v"(op0), "+v"(op1));
  asm volatile("s_waitcnt lgkmcnt(0)\n\ts_barrier" ::: "memory");
  {
    float a[4], l[4];
    f32x4 u[4], x[4];
#pragma unroll
    for (int i = 0; i < 4; ++i) { a[i] = mm[i][qt][lane]; l[i] = mo[i][qt][2][lane | 32][0]; u[i] = mo[i][qt][grp][lane]; x[i] = mo[i][qt][2][lane]; }
    const float mn = fmx(fmx(a[0], a[1]), fmx(a[2], a[3]));
#pragma unroll
    for (int i = 0; i < 4; ++i) a[i] = __builtin_amdgcn_exp2f(a[i] - mn);
    const float inv = __builtin_amdgcn_rcpf((l[0] * a[0] + l[1] * a[1]) + (l[2] * a[2] + l[3] * a[3]));
    const f32x4 v = ((u[0] * a[0] + u[1] * a[1]) + (u[2] * a[2] + u[3] * a[3])) * inv;
    *(GAS u32x2*)op0 = (u32x2){pack2(v[0], v[1]), pack2(v[2], v[3])};
    if (grp == 0) {
      const f32x4 y = ((x[0] * a[0] + x[1] * a[1]) + (x[2] * a[2] + x[3] * a[3])) * inv;
      if (hh == 0) *(GAS u32x2*)op1 = (u32x2){pack2(y[0], y[1]), pack2(y[2], y[3])};
    }
  }
}
#if defined(__HIP_DEVICE_COMPILE__)
#pragma clang attribute pop
#endif

__global__ __launch_bounds__(640, 3) void proj_kernel(const u16* __restrict__ Of, const u16* __restrict__ Wf, const u16* __restrict__ xold,
                                                      u16* __restrict__ xnew) {
  __shared__ __attribute__((aligned(16))) char ob[40960];
  const int tid = threadIdx.x, lane = tid & 63, w = __builtin_amdgcn_readfirstlane(tid >> 6), g = lane >> 4, c = lane & 15;
  __shared__ unsigned sink[64];
  own_code<1500>(tid, sink);
  const int rb = (blockIdx.x & 7) + 8 * (blockIdx.x >> 5), cb = (blockIdx.x >> 3) & 3;
  const int col = cb * 160 + w * 16 + 4 * g;
  const u16* wp = Wf + (size_t)(cb * 10 + w) * 20 * 512 + lane * 8;
  u32x4 st[4];
#pragma unroll
  for (int i = 0; i < 4; ++i) st[i] = *(const u32x4*)(Of + (size_t)rb * 40 * 512 + (i * 640 + tid) * 8);
  bf16x8 wf[20];
#pragma unroll
  for (int ks = 0; ks < 20; ++ks) wf[ks] = *(const bf16x8*)(wp + (size_t)ks * 512);
  u32x2 xo[2];
#pragma unroll
  for (int t = 0; t < 2; ++t) xo[t] = *(const u32x2*)(xold + (((size_t)(rb * 2 + t) * 20 + (col >> 5)) * 64 + ((col & 31) >> 3) * 16 + c) * 8 + (col & 7));
#pragma unroll
  for (int i = 0; i < 4; ++i) *(u32x4*)(ob + (i * 640 + tid) * 16) = st[i];
  BAR();
  f32x4 acc[2] = {{0.f, 0.f, 0.f, 0.f}, {0.f, 0.f, 0.f, 0.f}};
#pragma unroll
  for (int ks = 0; ks < 20; ++ks)
#pragma unroll
    for (int t = 0; t < 2; ++t) acc[t] = mfma32(wf[ks], *(const bf16x8*)(ob + (t * 20 + ks) * 1024 + lane * 16), acc[t]);
#pragma unroll
  for (int t = 0; t < 2; ++t) {
    const unsigned r01 = pack2(acc[t][0], acc[t][1]), r23 = pack2(acc[t][2], acc[t][3]);
    const unsigned p01 = pack2(u2f(xo[t][0] << 16) + u2f(r01 << 16), u2f(xo[t][0] & 0xffff0000u) + u2f(r01 & 0xffff0000u));
    const unsigned p23 = pack2(u2f(xo[t][1] << 16) + u2f(r23 << 16), u2f(xo[t][1] & 0xffff0000u) + u2f(r23 & 0xffff0000u));
    *(u32x2*)(xnew + (size_t)(rb * 32 + t * 16 + c) * DM + col) = (u32x2){p01, p23};
  }
}

extern "C" void kernel_launch(void* const* d_in, const int* in_sizes, int n_in, void* d_out, int out_size, void* d_ws,
                              size_t ws_size, hipStream_t stream) {
  const u16* A_x = (const u16*)d_in[0];
  const u16* A_mem0 = (const u16*)d_in[1];
  const u16* A_Wq = (const u16*)d_in[2];
  const u16* A_Wk = (const u16*)d_in[3];
  const u16* A_Wv = (const u16*)d_in[4];
  const u16* A_Wo = (const u16*)d_in[5];
  const u16* B_wqkv = (const u16*)d_in[8];
  const u16* B_wo = (const u16*)d_in[9];
  char* ws = (char*)(((uintptr_t)d_ws + ((size_t)WS_OFF << 20) + WS_AL - 1) & ~(uintptr_t)(WS_AL - 1));
  u16* xa = (u16*)(ws + OFF_XA); u16* Ob = (u16*)(ws + OFF_OB);

  prologue_kernel<<<256, 640, 0, stream>>>(ProArgs{A_x, A_Wq, A_Wk, A_Wv, A_Wo, B_wqkv, B_wo, ws});
  for (int layer = 0; layer < 2; ++layer) {
    rec_kernel<<<256, 512, 0, stream>>>(RecArgs{A_mem0, A_Wq, A_Wk, (const u16*)(ws + OFF_XB), xa, ws, (unsigned)layer, 0u});
    attb_kernel<<<BH * 32 / ATT_NQ, 256 * ATT_NQ, 0, stream>>>((const u16*)(ws + OFF_QB), (const u16*)(ws + OFF_KV), Ob);
  }
  proj_kernel<<<256, 640, 0, stream>>>(Ob, (const u16*)(ws + OFF_WOBT), xa, (u16*)d_out);
}
```

```cpp
#include <hip/hip_runtime.h>
#include <hip/hip_bf16.h>
#include <stdint.h>
#include <type_traits>

typedef unsigned short u16;
typedef unsigned long long u64;
typedef __attribute__((ext_vector_type(8))) short bf16x8;
typedef __attribute__((ext_vector_type(16))) short bf16x16;
typedef __attribute__((ext_vector_type(4))) float f32x4;
typedef __attribute__((ext_vector_type(2))) float f32x2;
typedef __attribute__((ext_vector_type(16))) float f32x16;
typedef __attribute__((ext_vector_type(8))) float f32x8;
typedef __attribute__((ext_vector_type(4))) unsigned u32x4;
typedef __attribute__((ext_vector_type(2))) unsigned u32x2;
typedef __attribute__((ext_vector_type(3))) unsigned u32x3;
typedef __attribute__((ext_vector_type(8))) unsigned u32x8;
typedef __attribute__((ext_vector_type(16))) unsigned u32x16;
typedef __attribute__((ext_vector_type(32))) unsigned u32x32;
#define GAS __attribute__((address_space(1)))
#define LAS __attribute__((address_space(3)))
typedef GAS unsigned long long gu64;
#define BAR() do { __builtin_amdgcn_s_waitcnt(0xC07F); asm volatile("s_waitcnt lgkmcnt(0)\n\ts_barrier" ::: "memory"); } while (0)

constexpr int S_ = 2048, DM = 640;
constexpr int NH = 32, DH = 192, HD = NH * DH;
constexpr int MEM = 8, SEG = 32, NSEG = S_ / SEG;
constexpr int BH = 32, BD = 20;

constexpr size_t al256(size_t x) { return (x + 255) & ~(size_t)255; }
constexpr int LAG3 = 2, LAG4 = 3, LAG5 = 6;
#define SR_ 8
constexpr int SR = SR_;
constexpr unsigned SCT_STEP = 131072, OFF_SCM = SR * SCT_STEP, SCM_STEP = 32768;
constexpr int ACC_NB = 192;
constexpr size_t ACC_STEP = 65536;
constexpr unsigned ACC_TS = ACC_NB * 256;
constexpr size_t ACC_LAYER = SR * ACC_STEP;
constexpr size_t OFF_ACC = 3 * 524288;
static_assert(SR == 8, "regions");
__device__ __forceinline__ unsigned ts_piece(int i) { return ACC_TS + (unsigned)(i < 128 ? (i >> 1) * 256 + (i & 1) * 16 : 128 + (i & 1) * 8192); }
constexpr size_t OFF_LNX = OFF_SCM + SR * SCM_STEP;
constexpr size_t OFF_RDY = OFF_LNX + 65536;
constexpr size_t PG = 2097152;
#define RING_ 4
constexpr int RING = RING_;
constexpr size_t TS_SLOT = (size_t)NH * 8 * 2560, PT_SLOT = (size_t)NH * 2560, OP_SLOT = (size_t)256 * 2560;
__device__ __forceinline__ unsigned ts_tok(int h, int j) { return (unsigned)((h & 15) * 32768 + (j >> 2) * 16384 + (j & 3) * 2048); }
__device__ __forceinline__ unsigned ts_mem(int h, int j) { return (unsigned)(524288 + ((h & 15) >> 1) * 16384 + (h & 1) * 4096 + j * 512); }
__device__ __forceinline__ unsigned pt_tok(int h) { return (unsigned)(((h & 15) >> 2) * 16384 + (h & 3) * 2048); }
__device__ __forceinline__ unsigned pt_mem(int h) { return (unsigned)(65536 + (h & 15) * 512); }
constexpr size_t OFF_X = 2 * PG + TS_SLOT + 3 * PT_SLOT, X_BYTES = 7 * OP_SLOT + 4 * PT_SLOT;
constexpr size_t OFF_OP = OFF_X + TS_SLOT + 4 * PT_SLOT;
constexpr size_t COMM_BYTES = OFF_X + X_BYTES + RING * OP_SLOT;
__device__ __forceinline__ unsigned near_half(unsigned ck, unsigned o) { return o + ((ck >> (o >= 2 * PG ? 8 : 4)) & 8192u); }
__device__ __forceinline__ unsigned ts_slot(unsigned ck, int sl) {
  return near_half(ck, sl == NSEG - 1 ? (unsigned)OFF_X : ((sl & 3) == 3 ? (unsigned)(2 * PG) : (unsigned)PG + (unsigned)(sl & 3) * (unsigned)TS_SLOT));
}
__device__ __forceinline__ unsigned pt_slot(unsigned ck, int sl) {
  return near_half(ck, sl >= NSEG - 4 ? (unsigned)(OFF_X + TS_SLOT) + (unsigned)(sl - (NSEG - 4)) * (unsigned)PT_SLOT
                                      : ((sl & 3) == 0 ? (unsigned)(PG + 3 * TS_SLOT) : (unsigned)(2 * PG + TS_SLOT - PT_SLOT) + (unsigned)(sl & 3) * (unsigned)PT_SLOT));
}
__device__ __forceinline__ unsigned op_slot(int sl) { return (unsigned)OFF_OP + (unsigned)(sl >= NSEG - 6 ? sl - (NSEG - 6) : 6 + sl % RING) * (unsigned)OP_SLOT; }
static_assert(RING == 4 && OFF_OP <= 3 * PG && OFF_RDY + 2048 <= OFF_ACC && OFF_ACC + ACC_LAYER == PG && TS_SLOT % 16384 == 0 && PT_SLOT % 16384 == 0, "pages");
static_assert(TS_SLOT == OP_SLOT && RING >= 4 && NSEG % SR == 0 && SR >= 5 && X_BYTES % 4096 == 0 && X_BYTES <= 256 * 20 * 1024, "slots");
constexpr size_t OFF_WB = al256(COMM_BYTES);
constexpr size_t OFF_BNRM = OFF_WB + (size_t)256 * 100 * 1024;
constexpr size_t OFF_LAT = OFF_BNRM + 1024;
constexpr size_t OFF_XNRM = OFF_LAT + 2048;
constexpr size_t OFF_WQKVBT = OFF_XNRM + al256(S_ * 16);
constexpr size_t OFF_WOBT = OFF_WQKVBT + al256((size_t)3 * DM * DM * 2);
constexpr size_t OFF_XN = OFF_WOBT + al256((size_t)DM * DM * 2);
constexpr int QST = BD;
constexpr size_t OFF_XA = OFF_XN + al256((size_t)S_ * DM * 2);
constexpr size_t OFF_XB = OFF_XA + al256((size_t)S_ * DM * 2);
constexpr size_t OFF_QB = OFF_XB + al256((size_t)S_ * DM * 2);
constexpr int KVT = 2624;
constexpr size_t OFF_KV = OFF_QB + al256((size_t)BH * S_ * QST * 2);
constexpr size_t OFF_OB = OFF_KV + al256((size_t)BH * 64 * KVT);
constexpr size_t OFF_DBG = OFF_OB + al256((size_t)S_ * DM * 2);
constexpr size_t WS_TOTAL = OFF_DBG + (size_t)2 * 64 * 8 * DM * 2;
#define WS_AL 2097152
#define WS_OFF 0
static_assert(WS_TOTAL + WS_AL + ((size_t)WS_OFF << 20) <= ((size_t)256 << 20), "workspace budget");

#if defined(__HIP_DEVICE_COMPILE__)
#pragma clang attribute push(__attribute__((target("no-packed-fp32-ops"))), apply_to = function)
#endif
__device__ __forceinline__ float u2f(unsigned v) { return __builtin_bit_cast(float, v); }
__device__ __forceinline__ unsigned f2u(float v) { return __builtin_bit_cast(unsigned, v); }
__device__ __forceinline__ float bf2f(unsigned v) { return __builtin_bit_cast(float, v << 16); }
__device__ __forceinline__ unsigned f2bf(float f) { __bf16 b = (__bf16)f; return (unsigned)__builtin_bit_cast(u16, b); }
__device__ __forceinline__ float rbf(float f) { return bf2f(f2bf(f)); }
typedef __attribute__((ext_vector_type(2))) __bf16 hbf16x2;
__device__ __forceinline__ unsigned pack2(float lo, float hi) { f32x2 v = {lo, hi}; hbf16x2 b = __builtin_convertvector(v, hbf16x2); return __builtin_bit_cast(unsigned, b); }
__device__ __forceinline__ f32x4 mfma32(bf16x8 a, bf16x8 b, f32x4 c) { return __builtin_amdgcn_mfma_f32_16x16x32_bf16(a, b, c, 0, 0, 0); }
typedef __attribute__((ext_vector_type(8))) __bf16 hbf16x8;
typedef __attribute__((ext_vector_type(16))) __bf16 hbf16x16;
__device__ __forceinline__ f32x4 smfma64(bf16x8 a, bf16x16 b, f32x4 c, int idx) {
  return __builtin_amdgcn_smfmac_f32_16x16x64_bf16(__builtin_bit_cast(hbf16x8, a), __builtin_bit_cast(hbf16x16, b), c, idx, 0, 0);
}
__device__ __forceinline__ bf16x16 cat16(bf16x8 a, bf16x8 b) { return __builtin_shufflevector(a, b, 0, 1, 2, 3, 4, 5, 6, 7, 8, 9, 10, 11, 12, 13, 14, 15); }
__device__ __forceinline__ bf16x8 half8(bf16x16 v, int hi) { return hi ? __builtin_shufflevector(v, v, 8, 9, 10, 11, 12, 13, 14, 15) : __builtin_shufflevector(v, v, 0, 1, 2, 3, 4, 5, 6, 7); }
__device__ __forceinline__ bf16x8 mk8(unsigned a, unsigned b, unsigned c, unsigned d) { u32x4 v = {a, b, c, d}; return __builtin_bit_cast(bf16x8, v); }
__device__ __forceinline__ unsigned dpp_ror8(unsigned v) { return (unsigned)__builtin_amdgcn_mov_dpp((int)v, 0x128, 0xf, 0xf, true); }
__device__ __forceinline__ float fmx(float a, float b) { return __builtin_elementwise_maximum(a, b); }

__device__ __forceinline__ int imax(int a, int b) { return a > b ? a : b; }
__device__ __forceinline__ int imin(int a, int b) { return a < b ? a : b; }
__device__ __forceinline__ unsigned umin(unsigned a, unsigned b) { return a < b ? a : b; }
__device__ __forceinline__ unsigned umax(unsigned a, unsigned b) { return a > b ? a : b; }
__device__ __forceinline__ bool wave_all(bool p) { return __builtin_amdgcn_ballot_w64(!p) == 0ull; }
__device__ __forceinline__ float x16mx(float x) { unsigned u = __builtin_bit_cast(unsigned, x); auto r = __builtin_amdgcn_permlane16_swap(u, u, false, false); return fmx(u2f(r[0]), u2f(r[1])); }
__device__ __forceinline__ float x32mx(float x) { unsigned u = __builtin_bit_cast(unsigned, x); auto r = __builtin_amdgcn_permlane32_swap(u, u, false, false); return fmx(u2f(r[0]), u2f(r[1])); }
__device__ __forceinline__ float x16sum(float x) { unsigned u = __builtin_bit_cast(unsigned, x); auto r = __builtin_amdgcn_permlane16_swap(u, u, false, false); return u2f(r[0]) + u2f(r[1]); }
__device__ __forceinline__ float x32sum(float x) { unsigned u = __builtin_bit_cast(unsigned, x); auto r = __builtin_amdgcn_permlane32_swap(u, u, false, false); return u2f(r[0]) + u2f(r[1]); }
template <int CTRL> __device__ __forceinline__ float dppf(float v) { return u2f((unsigned)__builtin_amdgcn_mov_dpp((int)f2u(v), CTRL, 0xf, 0xf, true)); }
#define OWN_PF 1
template <int BYTES> __device__ __forceinline__ void own_code(int tid, unsigned* sink) {
  if (!OWN_PF || __builtin_amdgcn_readfirstlane(tid >> 6) * 4096 >= BYTES) return;
  const char* pc; unsigned keep;
  asm volatile("s_getpc_b64 %0" : "=s"(pc));
  asm volatile("s_mov_b32 %0, m0\n\ts_mov_b32 m0, %3\n\ts_nop 0\n\tglobal_load_lds_dword %1, %2\n\ts_mov_b32 m0, %0"
               : "=&s"(keep) : "v"(tid * 64 < BYTES ? tid * 64 : 0), "s"(pc), "s"((unsigned)(uintptr_t)(LAS unsigned*)sink) : "memory");
}
constexpr int DPP_XOR1 = 0xB1, DPP_XOR2 = 0x4E, DPP_ROR4 = 0x124, DPP_ROR8 = 0x128;
#if defined(__HIP_DEVICE_COMPILE__)
#pragma clang attribute pop
#endif

typedef short v4i16_t __attribute__((ext_vector_type(4)));
__device__ __forceinline__ bf16x8 tr_frag(const char* lo_, const char* hi_) {
  const v4i16_t lo = __builtin_amdgcn_ds_read_tr16_b64_v4i16((LAS v4i16_t*)lo_), hi = __builtin_amdgcn_ds_read_tr16_b64_v4i16((LAS v4i16_t*)hi_);
  return (bf16x8){lo[0], lo[1], lo[2], lo[3], hi[0], hi[1], hi[2], hi[3]};
}
__device__ __forceinline__ int rec_wg(int gp, int m) { return (((gp & 1) * 16 + m) << 3) | (gp >> 1); }
__device__ __forceinline__ float wsum(float v) {
  v += dppf<DPP_XOR1>(v); v += dppf<DPP_XOR2>(v); v += dppf<DPP_ROR4>(v); v += dppf<DPP_ROR8>(v);
  return x32sum(x16sum(v));
}
__device__ __forceinline__ float ln_row(int lane, u32x4 (&xo)[2]) {
  const bool two = lane < 16;
  float v[16];
#pragma unroll
  for (int hf = 0; hf < 2; ++hf)
#pragma unroll
    for (int i = 0; i < 4; ++i) { const unsigned u = xo[hf][i]; v[hf * 8 + 2 * i] = bf2f(u & 0xffff); v[hf * 8 + 2 * i + 1] = bf2f(u >> 16); }
  float s0 = 0.f, s1 = 0.f;
#pragma unroll
  for (int i = 0; i < 8; ++i) { s0 += v[i]; s1 += v[8 + i]; }
  const float mean = wsum(two ? s0 + s1 : s0) * (1.0f / DM);
  s0 = s1 = 0.f;
#pragma unroll
  for (int i = 0; i < 8; ++i) { const float d0 = v[i] - mean, d1 = v[8 + i] - mean; s0 += d0 * d0; s1 += d1 * d1; }
  const float var = rbf(wsum(two ? s0 + s1 : s0) * (1.0f / DM));
  const float mb = rbf(mean);
  const float rden = 1.0f / rbf(sqrtf(rbf(var + 1e-5f)));
  float n2[2] = {0.f, 0.f};
#pragma unroll
  for (int hf = 0; hf < 2; ++hf) {
    float y[8];
#pragma unroll
    for (int i = 0; i < 8; ++i) { y[i] = rbf(rbf(v[hf * 8 + i] - mb) * rden); n2[hf] += y[i] * y[i]; }
    xo[hf] = (u32x4){pack2(y[0], y[1]), pack2(y[2], y[3]), pack2(y[4], y[5]), pack2(y[6], y[7])};
  }
  return wsum(two ? n2[0] + n2[1] : n2[0]);
}

#define PROT(k)
#define WARM_CODE 1
#define B_AUX 16
#define X_AUX 16
#define T_AUX 16
#define PF_POS 0
#define PRB_N0 3
#define PRB_N1 0
#define PRB_LOG(i, v)
#define PLD(p) __builtin_nontemporal_load((const u32x4*)(p))
struct ProArgs { const u16* x; const u16* wq; const u16* wk; const u16* wv; const u16* wo; const u16* wqkvb; const u16* wob; char* ws; };
__global__ __launch_bounds__(640, 3) void prologue_kernel(ProArgs a) {
  __shared__ __attribute__((aligned(16))) char img[192 * 320];
  __shared__ __attribute__((aligned(16))) char slots[10 * 6144];
  __shared__ float red[10][160];
  __shared__ unsigned latw[8];
  __shared__ u32x4 xst[2][640];
  const int b = blockIdx.x, tid = threadIdx.x, lane = tid & 63, v = __builtin_amdgcn_readfirstlane(tid >> 6), c = lane & 15, g = lane >> 4;
  const int h = (b & 7) * 4 + ((b >> 3) & 3), dh = (b >> 5) & 1, cq = b >> 6, ks = dh * 10 + v;
  char* const slot = slots + v * 6144;
  PROT(0);
  __shared__ unsigned sink[64];
  own_code<9600>(tid, sink);
  const unsigned zo = v < 8 ? (unsigned)(((b >> 6) * 512 + ((b & 7) * 2 + (v >> 2)) * 32 + ((b >> 3) & 7) * 4 + (v & 3)) * 1024) : (unsigned)(((v - 7) * 2048 + (b & 7) * 32 + ((b >> 3) & 7) * 4 + (b >> 6)) * 1024);
  __builtin_amdgcn_raw_buffer_store_b128((u32x4){0u, 0u, 0u, 0u}, __builtin_amdgcn_make_buffer_rsrc(a.ws, 0, (int)(3 * PG), 0x00020000), zo + lane * 16, 0, 16);
  __builtin_amdgcn_sched_barrier(0);
  u32x4 wa[2][6], wb[6], tr[4], xo[2];
  const int xrow = b * 8 + (v & 7);
  xo[0] = *(const u32x4*)(a.x + (size_t)xrow * DM + lane * 8);
  xo[1] = *(const u32x4*)(a.x + (size_t)xrow * DM + 512 + c * 8);
  const int job = imin(b + 256 * (9 - v), 800);
  const int jn = job < 600 ? job / 20 : (job % 200) / 20, jk = job % 20;
  {
    const u16* src = job < 600 ? a.wqkvb + (size_t)(32 * jk) * 3 * DM : a.wob + (size_t)(32 * jk) * DM;
#pragma unroll
    for (int i = 0; i < 4; ++i) tr[i] = *(const u32x4*)(src + (size_t)(i * 8 + (lane >> 3)) * (job < 600 ? 3 * DM : DM) + jn * 64 + (lane & 7) * 8);
  }
#pragma unroll
  for (int i = 0; i < 6; ++i) {
    const int idx = (v * 6 + i) * 64 + lane, row = idx / 20;
    wb[i] = PLD(a.wo + (size_t)(h * DH + row) * DM + cq * 160 + (idx - row * 20) * 8);
  }
#pragma unroll
  for (int i = 0; i < 6; ++i) {
    const int idx = i * 64 + lane, r = idx / 24;
#pragma unroll
    for (int u = 0; u < 2; ++u) wa[u][i] = PLD(a.wv + (size_t)(32 * ks + 8 * (r >> 2) + (r & 3) + 4 * u) * HD + h * DH + (idx - r * 24) * 8);
  }
  unsigned pf[2] = {0u, 0u};
  auto prefetch = [&]() {
#pragma unroll
    for (int i = 0; i < 2; ++i) pf[i] = *(const unsigned*)((i ? a.wq : a.wk) + ((size_t)(b * 10 + v) * 48 + imin(lane, 47)) * 32);
  };
  if (PF_POS == 0) prefetch();
  __builtin_amdgcn_sched_barrier(0);
  unsigned lat = ~0u;
  auto probe = [&](int n0, int n) {
    const char* p = a.ws + zo;
#pragma unroll 1
    for (int i = n0; i < n0 + n; ++i) {
      u64 t0, t1; unsigned d;
      asm volatile("s_memtime %0\n\ts_waitcnt lgkmcnt(0)\n\ts_load_dword %2, %3, %4\n\ts_waitcnt lgkmcnt(0)\n\ts_memtime %1\n\ts_waitcnt lgkmcnt(0)"
                   : "=&s"(t0), "=&s"(t1), "=&s"(d) : "s"(p), "s"((i & 3) * 256 + (i >> 2) * 128) : "memory");
      PRB_LOG(i, (unsigned)t1 - (unsigned)t0);
      lat = umin(lat, (unsigned)t1 - (unsigned)t0);
    }
  };
  if (PRB_N0) { asm volatile("s_waitcnt vmcnt(26)" ::: "memory"); probe(0, PRB_N0); }
  __builtin_amdgcn_sched_barrier(0);
  auto xput = [&](int k) {
    if (v < 8) { xst[k][lane * 8 + (v ^ (lane & 7))] = xo[0]; if (lane < 16) xst[k][(64 + lane) * 8 + (v ^ (lane & 7))] = xo[1]; }
  };
  xput(0);
  const float xn2 = u2f(__builtin_amdgcn_readfirstlane(f2u(ln_row(lane, xo))));
  xput(1);
#pragma unroll
  for (int i = 0; i < 4; ++i) *(u32x4*)(slot + (i * 8 + (lane >> 3)) * 144 + (lane & 7) * 16) = tr[i];
  __builtin_amdgcn_fence(__ATOMIC_RELEASE, "wavefront"); __builtin_amdgcn_wave_barrier();
  bf16x8 tf[4];
#pragma unroll
  for (int t = 0; t < 4; ++t) {
    const char* bp = slot + (8 * g + (c >> 2)) * 144 + t * 32 + (c & 3) * 8;
    tf[t] = tr_frag(bp, bp + 4 * 144);
  }
  __builtin_amdgcn_fence(__ATOMIC_RELEASE, "wavefront"); __builtin_amdgcn_wave_barrier();
  __builtin_amdgcn_sched_barrier(0);
#pragma unroll
  for (int i = 0; i < 6; ++i) {
    const int idx = (v * 6 + i) * 64 + lane, row = idx / 20;
    *(u32x4*)(img + row * 320 + (((idx - row * 20) ^ ((row >> 2) & 2)) * 16)) = wb[i];
  }
  bf16x8 A[2][6];
#pragma unroll
  for (int u = 0; u < 2; ++u) {
#pragma unroll
    for (int i = 0; i < 6; ++i) {
      const int idx = i * 64 + lane, r = idx / 24;
      *(u32x4*)(slot + r * 384 + (((idx - r * 24) ^ (r >> 1)) * 16)) = wa[u][i];
    }
    __builtin_amdgcn_fence(__ATOMIC_RELEASE, "wavefront"); __builtin_amdgcn_wave_barrier();
#pragma unroll
    for (int e = 0; e < 6; ++e) A[u][e] = *(const bf16x8*)(slot + c * 384 + (((4 * e + g) ^ (c >> 1)) * 16));
    __builtin_amdgcn_fence(__ATOMIC_RELEASE, "wavefront"); __builtin_amdgcn_wave_barrier();
  }
  BAR();
  PROT(1);
  if (PF_POS == 0) asm volatile("" :: "v"(pf[0]), "v"(pf[1]));
  if (PF_POS == 1) prefetch();
  {
    const unsigned xf = (unsigned)((b >> 1) * 20480 + (tid >> 3) * 256 + (b & 1) * 128 + (tid & 7) * 16);
    const __amdgpu_buffer_rsrc_t rx = __builtin_amdgcn_make_buffer_rsrc(a.ws + OFF_XN, 0, (int)(OFF_QB - OFF_XN), 0x00020000);
    __builtin_amdgcn_raw_buffer_store_b128(xst[1][tid ^ ((tid >> 3) & 7)], rx, xf, 0, X_AUX);
    __builtin_amdgcn_raw_buffer_store_b128(xst[0][tid ^ ((tid >> 3) & 7)], rx, xf + (unsigned)(OFF_XB - OFF_XN), 0, X_AUX);
    if (v < 8 && lane < 4) ((float*)(a.ws + OFF_XNRM))[xrow * 4 + lane] = lane == 0 ? xn2 : 0.f;
  }
  if (job < 800) {
    const __amdgpu_buffer_rsrc_t rt = __builtin_amdgcn_make_buffer_rsrc(a.ws + (job < 600 ? OFF_WQKVBT : OFF_WOBT) + ((size_t)jn * 80 + jk) * 1024, 0, 81920, 0x00020000);
#pragma unroll
    for (int t = 0; t < 4; ++t) __builtin_amdgcn_raw_buffer_store_b128(__builtin_bit_cast(u32x4, tf[t]), rt, lane * 16 + t * 20480, 0, T_AUX);
  }
  const int gp = h >> 1, hh = h & 1;
  const __amdgpu_buffer_rsrc_t rwb = __builtin_amdgcn_make_buffer_rsrc(a.ws + OFF_WB, 0, 256 * 100 * 1024, 0x00020000);
#pragma unroll
  for (int n = 0; n < 10; ++n) {
    const int off = n * 32 + 8 * (c & 3), T = cq * 10 + n;
    const char* bp = img + (8 * g + (c >> 2)) * 320 + (off ^ ((g & 1) << 5));
    f32x4 ac[2] = {{0.f, 0.f, 0.f, 0.f}, {0.f, 0.f, 0.f, 0.f}};
#pragma unroll
    for (int e = 0; e < 6; ++e) {
      const bf16x8 bf = tr_frag(bp + e * 32 * 320, bp + (e * 32 + 4) * 320);
      ac[0] = mfma32(A[0][e], bf, ac[0]); ac[1] = mfma32(A[1][e], bf, ac[1]);
    }
    const u32x4 o = {pack2(ac[0][0], ac[0][1]), pack2(ac[0][2], ac[0][3]), pack2(ac[1][0], ac[1][1]), pack2(ac[1][2], ac[1][3])};
    const unsigned oo = T < 32 ? (unsigned)((rec_wg(gp, T & 15) * 100 + (hh * 3 + (T >> 4)) * 20 + ks) * 1024 + lane * 16)
                               : (unsigned)((rec_wg(gp, 2 * (T - 32) + (c >> 3)) * 100 + 40 + ks) * 1024 + (g * 16 + hh * 8 + (c & 7)) * 16);
    __builtin_amdgcn_raw_buffer_store_b128(o, rwb, oo, 0, B_AUX);
    float n2 = 0.f;
#pragma unroll
    for (int r = 0; r < 4; ++r) n2 += ac[0][r] * ac[0][r] + ac[1][r] * ac[1][r];
    n2 = x32sum(x16sum(n2));
    if (g == 0) red[v][n * 16 + c] = n2;
  }
  PROT(2);
  if (PF_POS == 1) asm volatile("" :: "v"(pf[0]), "v"(pf[1]));
  probe(PRB_N0, PRB_N1);
  if (v < 8) latw[v] = lat;
  else if (b < 64 && lane == 0) ((unsigned*)(a.ws + OFF_LAT))[(v - 4) * 64 + b] = umin(lat, 60000u);
  if (WARM_CODE && v == 7) {
    const char* pc; int d0, d1;
    asm("s_getpc_b64 %0\n\ts_mov_b32 %1, _Z10rec_kernel7RecArgs@rel32@lo+4\n\ts_mov_b32 %2, _Z11proj_kernelPKtS0_S0_Pt@rel32@lo+12" : "=s"(pc), "=s"(d0), "=s"(d1));
    const char *c0 = pc + d0, *c1 = pc + d1;
    const long o = ((long)b * 64 + lane) * 64;
    if (o < c1 - c0) asm volatile("" :: "v"(*(const volatile GAS unsigned*)(c0 + o)));
  }
  if (v == 9 && lane < 32) {
    const int q = b * 32 + lane;
    *(u32x4*)(a.ws + OFF_KV + (size_t)(q >> 2) * KVT + 1280 + (q & 3) * 336 + 320) = (u32x4){0x3f803f80u, 0x3f803f80u, 0x3f803f80u, 0x3f803f80u};
  }
  BAR();
  if (v == 1 && lane == 0) ((unsigned*)(a.ws + OFF_LAT))[b] = umin(umin(umin(latw[0], latw[4]) + umin(latw[1], latw[5]), 30000u) + umin(umin(latw[2], latw[6]) + umin(latw[3], latw[7]), 30000u), 60000u);
  if (v == 0) {
    float mx = 0.f;
#pragma unroll
    for (int k = 0; k < 3; ++k) {
      const int col = k * 64 + lane;
      float sm = 0.f;
#pragma unroll
      for (int i = 0; i < 10; ++i) sm += red[i][col < 160 ? col : 0];
      mx = fmx(mx, sm);
    }
    mx = fmx(mx, dppf<DPP_XOR1>(mx)); mx = fmx(mx, dppf<DPP_XOR2>(mx)); mx = fmx(mx, dppf<DPP_ROR4>(mx)); mx = fmx(mx, dppf<DPP_ROR8>(mx));
    mx = x32mx(x16mx(mx));
    if (lane == 0) ((float*)(a.ws + OFF_BNRM))[(cq * 32 + h) * 2 + dh] = mx;
  }
  PROT(3);
}

struct RecArgs {
  const u16* mem0; const u16* wq; const u16* wk; const u16* xold; u16* xnew; char* ws;
  unsigned layer; unsigned pad;
};
#define RLX_AGENT __ATOMIC_RELAXED, __HIP_MEMORY_SCOPE_AGENT
constexpr unsigned SPIN_CAP = 1u << 20;
#define TSTAMP(k, v)
#define ARRIVE(k)
#define ARRIVE_OUT()
#define ARRIVE_DECL()
#define LOGV(k, v)
#define LOGF(k, v)
#define LOGP(k)
#define LOGQ(k)
#define LOGF2(k)
#define LOGD(k)
#define LOGT(k, v)
#define STAMP_DECL()
#define STAMP(k)
#define STAMPV(k, v)
#define STAMP_OUT()
#define REC_D1 520
#define SCH_V 16
#define SCH_VMIN 0
#define SCH_DMIN 4
#define SCH_PEN 20
#define SCH_VMIN2 (-4)
#define SCH_PEN2 40
#define SCH_GU 1
#define SCH_GD 2
#define SCH_UP 6
#define SCH_D0 196
#define SCH_FIRST 120
#define EXP_NODMA 0
#define BG_HOLD 220
#define QBATCH 0
#define WPF_N 5
#define WPF_W 0
#define WPF0 (63 - WPF_N)
#define BG_HOLD4 250
#define D1_W1 20
#define F3_D 1200
#define T2W 2
#define AT_ASM 1
#define PM_Z 1
#define P2_X 1
#define ST_KS 14
#define ST_KS0 ST_KS
#define ST_KS1 ST_KS
#define PADR 0x6666
#define PADT 0x000
#define CODE_PAD(n) asm volatile(".p2align 6\n.rept %0\ns_nop 0\n.endr" :: "n"(n))
#define ST_X 300
#define ST_ALL 4
#define CPF_S 61
#define FR_D 700
#define FR_PACE 6
#define END_E60 600
#define END_E1 1300
#define END_E2 3400
#define END_E62 2800
#define END_E3 6800
#define END_E4 11400
#define EARLY_N 4
#define EARLY_D1 0
#define EARLY_V 4
#define EARLY_V1 10
#define F3 0
#define F3A 0
#define F3A_D 400
#define T4_D 300
#define T4_P 1
#define P2_W 1
#define PS_SL 1
#define F2B 4
#define F0C 0
#define F2C 0
#define W2_P 1
#define SCH_W 1
#define SP_SL 0
#define D1_M 300
#define D1_MIN 260
#define D1_C 96
#define D1_G 2
#define BG_HOLD6 0
#define W2_D 0
#define END_E61 2600
#define D1_LATE 200
#define P2_G0 10
#define P2_G2 6
#define P2_G3 6
#define SCH_RL 0
#define SCH_P 3
#define MMA_P 0
#define SP_P 0
#define F6 8
#define F7 10
#define XPF_W 2
#define SCH_LATE 0
#define LATE_V 6
#define QW 3
#define FB_SL 0
#define P2_G1 1
#define P1_G 0
#define F4Z 0
#define F5Z 0
#define T5_Z 2
#define F0A 3
#define FB_E 0
#define EXACT 1
#define D1_P 3
#define VO_A0 9
#define VO_A2 10
#define VO_B0 3
#define VO_B1 0
#define VO_N2 7
#define VO_N3 10
#define VO_W -1
#define UN_P 2
#define VP_E 1
#define TS_B 0
#define ST_E 1
#define W1_SL 0
#define VO_A1 4
#define LAST_V 18
#define END_E59 3200
#define END_G2 600
#define END_G3 600
#define QR 4
#define WPF_S 128
#define END_SL 4
#define SCH_BX 6
#define SCH_BH 40
#define RETRY_T 115
#define RETRY_G 12
#define RETRY_L 20
#define RETRY_D 300
#define GO_T 10
#define SCH_BX2 45
#define SCH_DR 184
#define SCH_DK 0
#define KP0 0
#define KP1 0
#define XPF 3
#define W1_D 0
#define F4 12
#define F5 0
#define F67 0
#define KP2 0
#define REC_RNG 70u
#define SCH_DRAIN 40
#define PL_X 0
#define FC(k)
#define PIN_V(x) asm volatile("" : "+v"(x))
#define PIN_S(x) asm volatile("" : "+s"(x))
#define VM_DRAIN() do { asm volatile("s_waitcnt vmcnt(0)" ::: "memory"); __builtin_amdgcn_s_waitcnt(0x0F70); } while (0)
#define SC1_LOAD(off) __builtin_amdgcn_raw_buffer_load_b128(rs, (off), 0, 16)
#define SC1_LOADS(off, soff) __builtin_amdgcn_raw_buffer_load_b128(rs, (off), (soff), 16)
#define SC1_STORE8S(v, off, soff) __builtin_amdgcn_raw_buffer_store_b64((v), rs, (off), (soff), 16)
#define SC1_LOAD8(off) __builtin_amdgcn_raw_buffer_load_b64(rs, (off), 0, 16)
#define SC1_STORE(v, off) __builtin_amdgcn_raw_buffer_store_b128((v), rs, (off), 0, 16)
#define SC1_STORE8(v, off) __builtin_amdgcn_raw_buffer_store_b64((v), rs, (off), 0, 16)
#if defined(__HIP_DEVICE_COMPILE__)
#pragma clang attribute push(__attribute__((target("no-packed-fp32-ops"))), apply_to = function)
#endif
__device__ __forceinline__ unsigned now32() { return (unsigned)__builtin_amdgcn_s_memrealtime(); }
__device__ __forceinline__ void wait_until(unsigned t) { for (;;) { const int d = (int)(t - now32()); if (d <= 0) break; if (d > 8) __builtin_amdgcn_s_sleep(1); } }
__device__ __forceinline__ unsigned cyc32() { return (unsigned)__builtin_amdgcn_s_memtime(); }
__device__ __forceinline__ void wait_cyc(unsigned t) { for (;;) { const int d = (int)(t - cyc32()); if (d <= 0) break; if (d > 200) __builtin_amdgcn_s_sleep(1); } }
#define WAIT_ASM(clk, far_) "1:\n\t" clk " vcc\n\ts_waitcnt lgkmcnt(0)\n\ts_sub_i32 %0, %1, vcc_lo\n\ts_cmp_lt_i32 %0, " far_ "\n\ts_cbranch_scc1 3f\n\ts_sleep 1\n\ts_branch 1b\n" \
                            "2:\n\t" clk " vcc\n\ts_waitcnt lgkmcnt(0)\n\ts_sub_i32 %0, %1, vcc_lo\n3:\n\ts_cmp_gt_i32 %0, 0\n\ts_cbranch_scc1 2b"
__device__ __forceinline__ int spin_until(unsigned t) { int d; asm volatile(WAIT_ASM("s_memrealtime", "9") : "=&s"(d) : "s"(t) : "vcc", "scc", "memory"); return d; }
__device__ __forceinline__ int spin_cyc(unsigned t) { int d; asm volatile(WAIT_ASM("s_memtime", "200") : "=&s"(d) : "s"(t) : "vcc", "scc", "memory"); return d; }
__device__ __forceinline__ void spin_exact(unsigned t) {
  if (!EXACT) { spin_cyc(t); return; }
  int d;
  asm volatile("1:\n\ts_memtime vcc\n\ts_waitcnt lgkmcnt(0)\n\ts_sub_i32 %0, %1, vcc_lo\n\ts_cmp_gt_i32 %0, 95\n\ts_cbranch_scc1 1b\n\t"
               "s_max_i32 %0, %0, 0\n\ts_andn2_b32 %0, %0, 3\n\ts_sub_i32 %0, 3f-2f, %0\n\ts_getpc_b64 vcc\n"
               "2:\n\ts_add_u32 vcc_lo, vcc_lo, %0\n\ts_addc_u32 vcc_hi, vcc_hi, 0\n\ts_setpc_b64 vcc\n\t.rept 24\n\ts_nop 0\n\t.endr\n3:"
               : "=&s"(d) : "s"(t) : "scc", "vcc", "memory");
}
template <int OFF, int SL> __device__ __forceinline__ void lds_spin(unsigned lds0, unsigned want) {
  unsigned v;
  asm volatile("1:\n\tds_read_b32 %0, %1 offset:%3\n\t.if %4\n\ts_sleep %4\n\t.endif\n\ts_waitcnt lgkmcnt(0)\n\tv_cmp_gt_u32 vcc, %2, %0\n\ts_cbranch_vccnz 1b"
               : "=&v"(v) : "v"(lds0), "s"(want), "n"(OFF), "n"(SL) : "vcc", "memory");
}
template <int CTRL> __device__ __forceinline__ int dppi(int v) { return __builtin_amdgcn_mov_dpp(v, CTRL, 0xf, 0xf, true); }
__device__ __forceinline__ int wave_imax(int l) {
  l = imax(l, dppi<DPP_XOR1>(l)); l = imax(l, dppi<DPP_XOR2>(l)); l = imax(l, dppi<DPP_ROR4>(l)); l = imax(l, dppi<DPP_ROR8>(l));
  int a = imax(__builtin_amdgcn_readlane(l, 0), __builtin_amdgcn_readlane(l, 16)), b = imax(__builtin_amdgcn_readlane(l, 32), __builtin_amdgcn_readlane(l, 48));
  PIN_S(a); PIN_S(b);
  return imax(a, b);
}
__device__ __forceinline__ void glds16(const void* gsrc, unsigned lds_dst) {
  unsigned keep;
  asm volatile("s_mov_b32 %0, m0\n\ts_mov_b32 m0, %2\n\ts_nop 0\n\tglobal_load_lds_dwordx4 %1, off\n\ts_mov_b32 m0, %0" : "=&s"(keep) : "v"(gsrc), "s"(lds_dst) : "memory");
}
__device__ __forceinline__ void glds16_sc1(const void* gsrc, unsigned lds_dst) {
  unsigned keep;
  asm volatile("s_mov_b32 %0, m0\n\ts_mov_b32 m0, %2\n\ts_nop 0\n\tglobal_load_lds_dwordx4 %1, off sc1\n\ts_mov_b32 m0, %0" : "=&s"(keep) : "v"(gsrc), "s"(lds_dst) : "memory");
}
__device__ __forceinline__ unsigned acc_off(int G, int row) {
  return (unsigned)(G < 128 ? ((G & 31) | ((G & 32) << 1) | ((G & 64) >> 1)) * 256 + row * 16 : (2 * G - 128 + (row >> 2)) * 256 + (row & 3) * 16);
}
__device__ __forceinline__ unsigned nz(unsigned v) { return v > 1u ? v : 1u; }
__device__ __forceinline__ unsigned pack2n(float lo, float hi) { return nz(pack2(lo, hi)); }
__device__ __forceinline__ unsigned min4(u32x4 v) { return umin(umin(v[0], v[1]), umin(v[2], v[3])); }
__device__ __forceinline__ bf16x8 as8(u32x4 v) { return __builtin_bit_cast(bf16x8, v); }

constexpr int NV = 7, NKQ = 2;
constexpr int L_RED1 = 0;
constexpr int L_BM = L_RED1 + 8 * 1536;
constexpr int L_STG = L_BM + 10240;
constexpr int L_ST = L_STG + 2048 + 2560;
constexpr int L_MISC = L_ST + 512;
constexpr int L_KX = L_MISC + 128;
constexpr int L_QX = L_KX + NKQ * 2048;
constexpr int L_XN = L_QX + NKQ * 2048;
constexpr int L_VOX = L_XN + 81920;
constexpr int L_VOM = L_VOX + NV * 5120;
constexpr int L_BYTES = L_VOM + NV * 1280;
constexpr int S_IMG = L_XN + 40960;
static_assert(S_IMG + 61440 <= L_VOM, "start-up image");
static_assert(L_BYTES <= 160 * 1024, "LDS budget");

__device__ __forceinline__ bf16x8 wqk_frag(const char* lds, int t, int ks, int lane) {
  const int c = lane & 15, g = lane >> 4, p = c & 3;
  const char* q = lds + S_IMG + (32 * ks + 8 * g + (c >> 2)) * 96 + 8 * p + (t == 0 ? 0 : (t == 1 ? 48 : (p < 2 ? 32 : 64)));
  const v4i16_t lo = __builtin_amdgcn_ds_read_tr16_b64_v4i16((LAS v4i16_t*)q), hi = __builtin_amdgcn_ds_read_tr16_b64_v4i16((LAS v4i16_t*)(q + 4 * 96));
  return (bf16x8){lo[0], lo[1], lo[2], lo[3], hi[0], hi[1], hi[2], hi[3]};
}
__device__ __forceinline__ bf16x8 sel_frag(int c, int base) {
  bf16x8 r;
#pragma unroll
  for (int i = 0; i < 8; ++i) r[i] = (c == base + i) ? (short)0x3f80 : (short)0;
  return r;
}
__device__ __forceinline__ bf16x8 qk_frag(const char* p4, const char* p2, const char* p2b) {
  f32x4 a0 = *(const f32x4*)(p4), a1 = *(const f32x4*)(p4 + 1792), a2 = *(const f32x4*)(p4 + 3584), a3 = *(const f32x4*)(p4 + 5376);
  f32x2 b0 = *(const f32x2*)(p2), b1 = *(const f32x2*)(p2 + 1792), b2 = *(const f32x2*)(p2b), b3 = *(const f32x2*)(p2b + 1792);
  f32x4 va = (a0 + a1) + (a2 + a3);
  f32x2 vb = (b0 + b1) + (b2 + b3);
  return mk8(pack2(va[0], va[1]), pack2(va[2], va[3]), pack2(vb[0], vb[1]), 0u);
}
__device__ __forceinline__ void x_fetch(const u16* src, unsigned lds0, int seg, int p, int lane) {
  if (EXP_NODMA) return;
  glds16_sc1(src + (size_t)p * 512 + lane * 8, lds0 + L_XN + (seg & 1) * 40960 + p * 1024);
}
template <int N> __device__ __forceinline__ void x_fetch_n(const u16* src, unsigned lds0, int seg, int p0, unsigned voff) {
  if (EXP_NODMA) return;
#pragma unroll
  for (int i = 0; i < N; i += 4) {
    unsigned keep;
    asm volatile("s_mov_b32 %0, m0\n\ts_mov_b32 m0, %3\n\ts_nop 0\n\tglobal_load_lds_dwordx4 %1, %2 sc1\n\t"
                 ".if %4 > 1\n\tglobal_load_lds_dwordx4 %1, %2 offset:1024 sc1\n\t.endif\n\t.if %4 > 2\n\tglobal_load_lds_dwordx4 %1, %2 offset:2048 sc1\n\t.endif\n\t"
                 ".if %4 > 3\n\tglobal_load_lds_dwordx4 %1, %2 offset:3072 sc1\n\t.endif\n\ts_mov_b32 m0, %0"
                 : "=&s"(keep) : "v"(voff), "s"(src + (size_t)(p0 + i) * 512), "s"(lds0 + L_XN + (seg & 1) * 40960 + (p0 + i) * 1024), "n"(N - i < 4 ? N - i : 4) : "memory");
  }
}
__device__ __forceinline__ int qkv_row(int b) { return (b & 7) / QR * (2 * QR) + (b >> 3) / (16 / QR); }
__device__ __forceinline__ int qkv_col(int b) { return (b & 7) % QR * (16 / QR) + (b >> 3) % (16 / QR); }
__device__ __forceinline__ const u16* x_src(const RecArgs& a, int seg, int b) {
  return seg < NSEG ? (const u16*)(a.ws + OFF_XN) + (size_t)seg * 32 * DM : a.xnew + (size_t)(qkv_row(b) + 16 * (seg - NSEG)) * 32 * DM;
}
template <int N>
__device__ __forceinline__ void img_fetch(const RecArgs& a, unsigned lds0, int h, int j, int part, int lane) {
#pragma unroll
  for (int i = 0; i < N; ++i) {
    const int n = part * N + i, idx = n * 64 + lane, row = idx / 6, pc = idx - row * 6;
    if (FR_PACE && N == 20) asm volatile("s_waitcnt vmcnt(%0)" :: "n"(FR_PACE) : "memory");
    glds16((pc < 3 ? a.wq : a.wk - 24) + (size_t)row * HD + h * DH + j * 24 + pc * 8, lds0 + S_IMG + n * 1024);
  }
}
constexpr int P2B = F4 + F5 + 2 * F67 + F6 + F7, PZ = P2B + F2B, PA = PZ, P0C = PA + 2 * F0A, F3C = 40 - F3A - F3 - P0C - 2 * F0C - F2C;
static_assert(F3C >= 0, "fetch plan");
template <int OFF> __device__ __forceinline__ void lds_inc(unsigned lds0) {
  asm volatile("s_mov_b64 exec, 1\n\tds_add_u32 %0, %1 offset:%2\n\ts_mov_b64 exec, -1" :: "v"(lds0), "v"(1u), "n"(OFF) : "memory");
}
__device__ __forceinline__ void lds_arrive(unsigned* p, int lane) {
  asm volatile("s_waitcnt lgkmcnt(0)" ::: "memory");
  if (lane == 0) __hip_atomic_fetch_add(p, 1u, __ATOMIC_RELAXED, __HIP_MEMORY_SCOPE_WORKGROUP);
}
__device__ __forceinline__ void lds_wait(unsigned* p, unsigned want) {
  while (__hip_atomic_load(p, __ATOMIC_RELAXED, __HIP_MEMORY_SCOPE_WORKGROUP) < want) __builtin_amdgcn_s_sleep(1);
  asm volatile("" ::: "memory");
}
constexpr float KEXP = 0.07216878364870322f * 1.4426950408889634f;
#define FRESH_LANE() int lane = lane0; asm volatile("" : "+v"(lane)); const int c = lane & 15, g = lane >> 4; (void)c; (void)g

struct OutTask {
  u32x4 y[4]; u32x2 xo, out;
  bf16x8 sel;
  unsigned t5f, t5o;
  __device__ __forceinline__ void init(int b, int lane) {
    const int um = b / 10, uq = (b / 5) & 1, up = b % 5, tok = lane & 15, k = up < 4 ? (up >> 1) * 256 + um * 16 + (up & 1) * 8 : 512 + um * 8, q4 = ((lane >> 4) & 1) * 4;
    t5f = (unsigned)(((uq * 20 + (k >> 5)) * 64 + ((k & 31) >> 3) * 16 + tok) * 8 + q4);
    t5o = (unsigned)(((um * 2 + uq) * 16 + (lane >> 4)) * 1280 + up * 256 + tok * 16);
    if (b >= 160) { t5f = 0u; t5o = 0x7ffffff0u; }
    sel = sel_frag(tok, 0);
    PIN_V(sel);
  }
  static __device__ __forceinline__ bool ok(int sl, int b) { return sl >= 0 && b < 160; }
  __device__ __forceinline__ void load(const RecArgs& a, const __amdgpu_buffer_rsrc_t rs, int sl, int b, int part = 3) {
    const unsigned o = t5o | (sl < 0 ? 0x7ff00000u : 0u);
    sl = imax(sl, 0);
#pragma unroll
    for (int k = 0; k < 4; ++k) if (part & 1) y[k] = SC1_LOADS(o, op_slot(sl) + k * 5120);
    if (part & 2) xo = __builtin_bit_cast(u32x2, __hip_atomic_load((const gu64*)(a.xold + (size_t)sl * 32 * DM + t5f), RLX_AGENT));
  }
  __device__ __forceinline__ bool ready(int b) { return b >= 160 || wave_all(umin(umin(min4(y[0]), min4(y[1])), umin(min4(y[2]), min4(y[3]))) != 0u); }
  __device__ __forceinline__ void run(int lane) {
    f32x4 d = {0.f, 0.f, 0.f, 0.f};
#pragma unroll
    for (int k = 0; k < 4; ++k) d = mfma32(sel, as8(y[k]), d);
#pragma unroll
    for (int i = 0; i < 2; ++i) {
      const unsigned r = pack2(d[2 * i], d[2 * i + 1]), x = xo[i];
      out[i] = pack2(u2f(x << 16) + u2f(r << 16), u2f(x & 0xffff0000u) + u2f(r & 0xffff0000u));
    }
    PIN_V(out);
  }
  __device__ __forceinline__ void store(const RecArgs& a, int sl, int b, int lane) {
    __builtin_amdgcn_raw_buffer_store_b64(out, __builtin_amdgcn_make_buffer_rsrc(a.xnew, 0, ok(sl, b) ? S_ * DM * 2 : 0, 0x00020000), (t5f + (unsigned)sl * 32 * DM) * 2u | (unsigned)(lane & 32) << 25, 0, 16);
  }
};

struct PvTask2 {
  u32x4 y[2][2], pa[2], pb[2];
  unsigned lo, lm, vr, ma, mz, so, sz;
  __device__ __forceinline__ void init(int gp, int lane) {
    const int hi = lane >> 5;
    lo = pt_tok(2 * gp) + (unsigned)(lane * 16); lm = pt_mem(2 * gp + hi) + (unsigned)((lane & 31) * 8);
    vr = (unsigned)(L_VOX + 2048 + (lane ^ 8) * 16);
    ma = (unsigned)(L_VOM + hi * 768 + (lane & 31) * 8); mz = (unsigned)(L_VOM + 512 + ((lane & 31) ^ (hi * 8)) * 8);
    so = (unsigned)(L_STG + 2048 + (hi * 16 + (lane & 15)) * 16 + ((lane >> 4) & 1) * 8);
    sz = hi ? (unsigned)(L_RED1 + 8704 + lane * 8) : so + 1024;
    PIN_V(lo); PIN_V(lm); PIN_V(vr); PIN_V(ma); PIN_V(mz); PIN_V(so); PIN_V(sz);
#pragma unroll
    for (int q = 0; q < 2; ++q) { pa[q] = pb[q] = y[q][0] = y[q][1] = (u32x4){0u, 0u, 0u, 0u}; PIN_V(pa[q]); PIN_V(pb[q]); }
  }
  __device__ __forceinline__ void load(const __amdgpu_buffer_rsrc_t rs, unsigned o) {
#pragma unroll
    for (int q = 0; q < 2; ++q) {
      y[q][0] = SC1_LOADS(lo + q * 1024, o); y[q][1] = SC1_LOADS(lo + 2048 + q * 1024, o);
      const u32x2 p = __builtin_amdgcn_raw_buffer_load_b64(rs, lm + q * 256, o, 16);
      pa[q][0] = p[0]; pa[q][1] = p[1];
    }
  }
  __device__ __forceinline__ bool ready() {
    return wave_all(umin(umin(umin(min4(y[0][0]), min4(y[0][1])), umin(min4(y[1][0]), min4(y[1][1]))), umin(umin(pa[0][0], pa[0][1]), umin(pa[1][0], pa[1][1]))) != 0u);
  }
  template <bool DIRECT> __device__ __forceinline__ void run(char* lds, int sv, int lane, const __amdgpu_buffer_rsrc_t rs, unsigned og) {
    const f32x4 Z4 = {0.f, 0.f, 0.f, 0.f};
    const char* vp = lds + L_VOX + sv * 5120 + lane * 16;
    bf16x8 vx[6];
#pragma unroll
    for (int t = 0; t < 5; ++t) vx[t] = *(const bf16x8*)(vp + t * 1024);
    vx[5] = *(const bf16x8*)(lds + vr + sv * 5120);
    const u32x2 a0 = *(const u32x2*)(lds + ma + sv * 1280), a1 = *(const u32x2*)(lds + ma + sv * 1280 + 256);
    const u32x2 z0 = *(const u32x2*)(lds + mz + sv * 1280), z1 = *(const u32x2*)(lds + mz + sv * 1280 + 256);
    const bf16x8 vm = mk8(a0[0], a0[1], a1[0], a1[1]), vz = mk8(z0[0], z0[1], z1[0], z1[1]);
#pragma unroll
    for (int q = 0; q < 2; ++q) { PIN_V(pa[q]); pb[q][2] = pa[q][0]; pb[q][3] = pa[q][1]; PIN_V(pb[q]); }
    __builtin_amdgcn_sched_barrier(0);
    f32x4 o[2][3];
#pragma unroll
    for (int q = 0; q < 2; ++q) {
#pragma unroll
      for (int t = 0; t < 3; ++t) o[q][t] = mfma32(vx[t], as8(y[q][0]), Z4);
#pragma unroll
      for (int t = 0; t < 3; ++t) o[q][t] = mfma32(vx[3 + t], as8(y[q][1]), o[q][t]);
      o[q][0] = mfma32(vm, as8(pa[q]), o[q][0]); o[q][1] = mfma32(vm, as8(pb[q]), o[q][1]); o[q][2] = mfma32(vz, as8(pa[q]), o[q][2]);
    }
#pragma unroll
    for (int q = 0; q < 2; ++q) {
#pragma unroll
      for (int t = 0; t < 3; ++t) {
        const u32x2 v = {pack2n(o[q][t][0], o[q][t][1]), pack2n(o[q][t][2], o[q][t][3])};
        if (DIRECT) SC1_STORE8S(v, (so - (L_STG + 2048) + t * 512) | (t == 2 ? (unsigned)(lane & 32) << 25 : 0u), og + q * 20480);
        else *(u32x2*)(lds + (t < 2 ? so + t * 512 : sz) + q * 1280) = v;
      }
    }
  }
};

struct TokScores {
  bf16x8 k[2], q[2]; u32x4 v[2];
  __device__ __forceinline__ void get(const char* lds, int seg, int lane) {
#pragma unroll
    for (int i = 0; i < 2; ++i) { k[i] = *(const bf16x8*)(lds + L_KX + (seg & 1) * 2048 + i * 1024 + lane * 16); q[i] = *(const bf16x8*)(lds + L_QX + (seg & 1) * 2048 + i * 1024 + lane * 16); }
  }
  __device__ __forceinline__ void run(int qt) {
    const f32x4 Z4 = {0.f, 0.f, 0.f, 0.f};
    const f32x4 d1 = mfma32(k[0], q[qt], Z4), d2 = mfma32(k[1], q[qt], Z4);
    v[qt] = (u32x4){pack2n(d1[0], d1[1]), pack2n(d1[2], d1[3]), pack2n(d2[0], d2[1]), pack2n(d2[2], d2[3])};
    PIN_V(v[qt]);
  }
  __device__ __forceinline__ void store(const __amdgpu_buffer_rsrc_t rs, unsigned off, unsigned o) {
    __builtin_amdgcn_raw_buffer_store_b128(v[0], rs, off, o, 16); __builtin_amdgcn_raw_buffer_store_b128(v[1], rs, off + 1024, o, 16);
    asm volatile("s_nop 1" ::: "memory");
  }
};

__device__ __forceinline__ void touch(const char* base, unsigned off, unsigned lds0) {
  unsigned keep;
  asm volatile("s_mov_b32 %0, m0\n\ts_mov_b32 m0, %3\n\ts_nop 0\n\tglobal_load_lds_dword %1, %2 sc1\n\ts_mov_b32 m0, %0" : "=&s"(keep) : "v"(off), "s"(base), "s"(lds0 + L_RED1 + 8192) : "memory");
}
template <int N> __device__ __forceinline__ void stouch(const char* p, unsigned& d) {
  asm volatile(".set pf_o, 0\n\t.rept %2\n\ts_load_dword %0, %1, pf_o\n\t.set pf_o, pf_o + 128\n\t.endr" : "+s"(d) : "s"(p), "n"(N) : "memory");
}
__device__ __forceinline__ void code_touch(int b, int lane, unsigned lds0) {
  const char* pc; int d0, d1;
  asm volatile("s_getpc_b64 %0\n\ts_mov_b32 %1, _Z10rec_kernel7RecArgs@rel32@lo+4\n\ts_mov_b32 %2, _Z11attb_kernelPKtS0_Pt@rel32@lo+12" : "=s"(pc), "=s"(d0), "=s"(d1));
  const int o = (lane * 32 + (b >> 3)) * 128;
  touch(pc + d0, (unsigned)(o < d1 - d0 ? o : 0), lds0);
}
__device__ __forceinline__ unsigned zero_base(int b, int h, int j, int ln) {
  return ln < 32 ? (unsigned)((h & 3) * 32768 + j * 512 + ln * 16) : ln < 40 ? (unsigned)((h & 2) * 1024 + j * 256 + (ln & 4) * 32 + (h & 1) * 64 + (ln & 3) * 16)
                 : (unsigned)OFF_ACC + (b < ACC_NB ? (unsigned)(b * 256 + (ln - 40) * 16) : ts_piece((b - ACC_NB) * 8 + ln - 40));
}
__device__ __forceinline__ unsigned sct_slot(unsigned ck, unsigned sl) { return sl * SCT_STEP + ((ck >> (sl & 4u)) & 7u) * 4096u; }
__device__ __forceinline__ unsigned scm_slot(unsigned ck, unsigned sl) { return OFF_SCM + sl * SCM_STEP + ((ck >> 8) & 7u) * 4096u; }
__device__ __forceinline__ int ck_rot(int x) { return ((x & 1) << 2) | (x >> 1); }
struct Place {
  unsigned t[8];
  __device__ __forceinline__ void load(const char* ws, int lane) {
#pragma unroll
    for (int x = 0; x < 8; ++x) t[x] = *(const unsigned*)(ws + OFF_LAT + ((imin(lane >> 3, 5) * 8 + (ck_rot(x) ^ (lane & 7))) * 8 + x) * 4);
  }
  __device__ __forceinline__ unsigned get(int b, int lane) {
    int c = (int)((((t[0] + t[1]) + (t[2] + t[3])) + ((t[4] + t[5]) + (t[6] + t[7]))) * 8u) + (lane & 7);
    c = imin(c, dppi<DPP_XOR1>(c)); c = imin(c, dppi<DPP_XOR2>(c)); c = imin(c, dppi<0x141>(c));
    unsigned k = 0u;
#pragma unroll
    for (int r = 0; r < 6; ++r) k |= (unsigned)(__builtin_amdgcn_readlane(c, 8 * r) & 7) << (4 * r);
    return (k ^ PL_X) ^ (unsigned)ck_rot(b & 7) * 0x111111u;
  }
};

struct Sched {
  u32x4 tsv, rec;
  unsigned side, o;
  __device__ __forceinline__ void pre(int s) { o = (unsigned)OFF_ACC + (unsigned)((s + SR - 1) % SR) * (unsigned)ACC_STEP + ACC_TS + side; PIN_S(o); }
  __device__ __forceinline__ void load(const __amdgpu_buffer_rsrc_t rs, int lane) {
    tsv = SC1_LOADS(ts_piece(lane & 63) - ACC_TS, o); rec = SC1_LOADS(128, o);
  }
  __device__ __forceinline__ unsigned last(unsigned ref) {
    const unsigned o = 0x40000000u - ref;
    int l = (int)umax(umax(tsv[0] ? tsv[0] + o : 0u, tsv[1] ? tsv[1] + o : 0u), umax(tsv[2] ? tsv[2] + o : 0u, tsv[3] ? tsv[3] + o : 0u));
    return (unsigned)wave_imax(l) - o;
  }
  __device__ __forceinline__ unsigned run(const RecArgs& a, const __amdgpu_buffer_rsrc_t rs, int s, int b, int lane) {
    constexpr int w = 3; (void)w;
    unsigned acur = __builtin_amdgcn_readfirstlane(rec[0]), aprev = __builtin_amdgcn_readfirstlane(rec[1]), hist = __builtin_amdgcn_readfirstlane(rec[3]);
    int D8 = (int)__builtin_amdgcn_readfirstlane(rec[2]);
    const int d = wave_imax((int)(umax(umax(tsv[0], tsv[1]), umax(tsv[2], tsv[3])) - aprev)), dd = 8 * d - D8;
    int Dn = D8 + (dd >> (dd > 0 ? SCH_GU : SCH_GD));
    unsigned anext = (acur + (unsigned)((Dn + 4 + imax(Dn - 8 * SCH_DR, 0) * SCH_DK / 8) >> 3) + SCH_V + (s < EARLY_N ? EARLY_V : (s >= WPF0 ? (s == NSEG - 2 ? LAST_V : LATE_V) : 0))) | 1u;
    if (__builtin_expect(hist < 2u || aprev == 0u || d < 1 || d + SCH_VMIN > (int)(acur - aprev), 0)) {
      if (s == 0) {
        for (int it = 0; it < 40 && !wave_all(min4(tsv) != 0u); ++it) {
          __builtin_amdgcn_s_sleep(ST_ALL);
          asm volatile("" ::: "memory");
          load(rs, lane);
        }
      }
      const unsigned now = now32(), L = last(now);
      if (acur == 0u) { D8 = 8 * SCH_D0; hist = 0u; }
      const bool late = acur == 0u || (int)(L + SCH_VMIN - acur) > 0;
      if (late) { if (b == 0) FC(3); if (acur) D8 += 8 * (hist ? SCH_UP : 3 * SCH_UP); }
      else if (aprev) { if (hist < 2u) D8 = 8 * imin(imax((int)(L - aprev), 1), 4000); hist = umin(hist + 1u, 2u); }
      const unsigned base = acur == 0u ? L + SCH_FIRST : (late ? acur + (L + RETRY_G - acur + RETRY_T - 1) / RETRY_T * RETRY_T + SCH_PEN : acur);
      anext = (base + (unsigned)((D8 + 4) >> 3) + SCH_V + (s == 1 ? EARLY_V1 : (s < EARLY_N ? EARLY_V : 0))) | 1u;
      if (s == 0) anext = (L + ST_X) | 1u;
      if ((int)(now - L) > 50000) anext = 0u;
      if (late) acur = 0u;
      Dn = D8;
    }
    if (__builtin_expect(b == 0, 0))
      __builtin_amdgcn_raw_buffer_store_b128((u32x4){anext, acur, (unsigned)Dn, hist}, rs, lane < 2 ? lane * 8192 : 0x7ffffff0, (unsigned)OFF_ACC + (unsigned)(s % SR) * (unsigned)ACC_STEP + ACC_TS + 128u, 16);
    LOGV(0, anext); LOGV(1, acur == 0u); LOGV(2, (Dn + 4) >> 3); LOGV(3, d); LOGV(4, acur - aprev - d); LOGV(5, aprev);
    return anext;
  }
};

struct TokTask {
  u32x4 y[2]; u32x2 ym; u32x4 res;
  unsigned l0, l1, lm, s0, s1;
  static __device__ __forceinline__ bool ok(int sl) { return sl >= 0 && sl < NSEG; }
  __device__ __forceinline__ void init(int h, int j, int lane) {
    const int c = lane & 15, g = lane >> 4;
    l0 = ts_tok(h, g) + (unsigned)(j * 256 + c * 16); l1 = l0 + 16384u;
    lm = ts_mem(h, g + 4 * (c >> 3)) + (unsigned)(j * 64 + (c & 7) * 8);
    res = (u32x4){0u, 0u, 0u, 0u};
    const int qi = 4 * j + (c & 3);
    s0 = g < 2 ? pt_tok(h) + (unsigned)((qi >> 4) * 1024 + ((c >> 2) * 16 + (qi & 15)) * 16 + g * 8) : 0x7ffffff0u;
    s1 = lane < 8 ? pt_mem(h) + (unsigned)((qi >> 4) * 256 + ((c >> 2) * 16 + (qi & 15)) * 8) : 0x7ffffff0u;
    PIN_V(l0); PIN_V(l1); PIN_V(lm); PIN_V(s0); PIN_V(s1);
  }
  __device__ __forceinline__ void load(const __amdgpu_buffer_rsrc_t rs, unsigned o) {
    y[0] = SC1_LOADS(l0, o); y[1] = SC1_LOADS(l1, o);
    ym = __builtin_amdgcn_raw_buffer_load_b64(rs, lm, o, 16);
  }
  __device__ __forceinline__ bool ready() { return wave_all(umin(umin(min4(y[0]), min4(y[1])), umin(ym[0], ym[1])) != 0u); }
  __device__ __forceinline__ void run(int j, int lane, const bf16x8 SelLo) {
    const int c = lane & 15, g = lane >> 4;
    const f32x4 Z4 = {0.f, 0.f, 0.f, 0.f};
    const f32x4 d1 = mfma32(SelLo, as8(y[1]), mfma32(SelLo, as8(y[0]), Z4));
    const f32x4 d0 = mfma32(SelLo, mk8(ym[0], ym[1], 0u, 0u), Z4);
    const int qi = 4 * j + (c & 3), k0 = 16 * g + 4 * (c >> 2);
    float t[4], mv[4];
    float mx = -INFINITY;
#pragma unroll
    for (int r = 0; r < 4; ++r) {
      t[r] = (g < 2 && k0 + r <= qi) ? d1[r] : -INFINITY;
      const float mt = d0[r] + dppf<DPP_ROR8>(d0[r]);
      mv[r] = lane < 8 ? mt : -INFINITY;
      mx = fmx(mx, fmx(t[r], mv[r]));
    }
    mx = fmx(mx, dppf<DPP_ROR8>(mx)); mx = fmx(mx, dppf<DPP_ROR4>(mx));
    mx = x16mx(mx);
    float sum = 0.f;
#pragma unroll
    for (int r = 0; r < 4; ++r) {
      t[r] = __builtin_amdgcn_exp2f(t[r] - mx); mv[r] = __builtin_amdgcn_exp2f(mv[r] - mx);
      sum += t[r] + mv[r];
    }
    sum += dppf<DPP_ROR8>(sum); sum += dppf<DPP_ROR4>(sum);
    sum = x16sum(sum);
    const float rs_ = __builtin_amdgcn_rcpf(sum);
    res = (u32x4){pack2n(t[0] * rs_, t[1] * rs_), pack2n(t[2] * rs_, t[3] * rs_), pack2n(mv[0] * rs_, mv[1] * rs_), pack2n(mv[2] * rs_, mv[3] * rs_)};
    PIN_V(res);
  }
  __device__ __forceinline__ void store(const __amdgpu_buffer_rsrc_t rs, unsigned o, unsigned no = 0u) {
    SC1_STORE8S(((u32x2){res[0], res[1]}), s0 | no, o);
    SC1_STORE8S(((u32x2){res[2], res[3]}), s1 | no, o);
  }
};

constexpr int L_DRN = L_VOX + (NSEG % NV) * 5120;
__device__ __forceinline__ void drain_pre(char* lds, int b, int lane) {
  const int gp = (b & 7) * 2 + (b >> 7), m = (b >> 3) & 15;
  TokTask tk; PvTask2 pv; OutTask ot;
  tk.init(2 * gp + (m >> 3), m & 7, lane); pv.init(gp, lane); ot.init(b, lane);
  u32x4* p = (u32x4*)(lds + L_DRN) + lane;
  p[0] = (u32x4){tk.l0, tk.lm, tk.s0, tk.s1}; p[64] = (u32x4){pv.lo, pv.lm, pv.vr, pv.ma}; p[128] = (u32x4){pv.mz, pv.so, ot.t5f, ot.t5o};
  p[192] = __builtin_bit_cast(u32x4, ot.sel);
}
__device__ __forceinline__ void qkv_pre(unsigned (&vo)[2][2], int b, int w, int lane) {
  const int cb = qkv_col(b), c = lane & 15, g = lane >> 4, t0 = w & 1, col0 = (cb % 5) * 128 + w * 32;
  const bool SW = cb < 10;
#pragma unroll
  for (int i = 0; i < 2; ++i)
#pragma unroll
    for (int tt = 0; tt < 2; ++tt) {
      const int tq = tt ^ t0, tk = tq * 16 + c, cc = col0 + i * 16 + (SW ? 4 * g : c), hd = cc / BD, e = cc - hd * BD;
      const int kv = (int)(OFF_KV - OFF_QB) + hd * 64 * KVT;
      vo[i][tt] = (unsigned)(!SW ? kv + 1280 + tq * 672 + ((g >> 1) * 21 + e) * 16 + (g & 1) * 8
                                 : (cb < 5 ? (hd * S_ + tk) * 2 * QST + e * 2 : kv + (e < 16 ? (tk + 32 * (e >> 3)) * 16 + (e & 7) * 2 : 1024 + tk * 8)));
      PIN_V(vo[i][tt]);
    }
}

template <int w>
__device__ __forceinline__ void token_waves(const RecArgs& a, char* lds, const __amdgpu_buffer_rsrc_t rs, unsigned (&vo)[2][2]) {
  const int lane0 = __builtin_amdgcn_workitem_id_x() & 63, b = __builtin_amdgcn_workgroup_id_x(), wid = w + 4; (void)wid;
  const int gp = (b & 7) * 2 + (b >> 7), m = (b >> 3) & 15, h0 = 2 * gp, h = h0 + (m >> 3), j = m & 7;
  float* const misc = (float*)(lds + L_MISC);
  const f32x4 Z4 = {0.f, 0.f, 0.f, 0.f};
  const unsigned lds0 = (unsigned)(uintptr_t)(LAS char*)lds;
  unsigned* const cnt = (unsigned*)(misc + 6);
  const float* const xnrm = (const float*)(a.ws + OFF_XNRM);
  bf16x16 W[2][10];
  auto btile = [&](int i) { return w == 1 ? 0 : (w == 2 ? 1 + i : 3 + i); };
  if (w == 1) code_touch(b, lane0, lds0);
  Place pl;
  if (w == 3) pl.load(a.ws, lane0);
  if (a.layer) {
    if (w != 0) img_fetch<20>(a, lds0, h, j, w - 1, lane0);
  } else {
    if (w == 0) img_fetch<12>(a, lds0, h, j, 4, lane0);
#pragma unroll
    for (int i = 0; i < 10; ++i) x_fetch(x_src(a, 0, b), lds0, 0, 10 * w + i, lane0);
  }
#pragma unroll
  for (int i = 0; i < 2; ++i)
#pragma unroll
    for (int p = 0; p < 10; ++p)
      if (w >= 2 || (w == 1 && i == 1)) {
        const char* wp = a.ws + OFF_WB + ((size_t)(b * 5 + btile(i)) * 20 + 2 * p) * 1024 + lane0 * 16;
        if (FR_PACE && a.layer) asm volatile("s_waitcnt vmcnt(%0)" :: "n"(FR_PACE) : "memory");
        W[i][p] = cat16(*(const bf16x8*)wp, *(const bf16x8*)(wp + 1024));
      }
  f32x4 bn = Z4;
  if (w == 0) {
    bn = *(const f32x4*)(a.ws + OFF_BNRM + lane0 * 16);
    if (!a.layer) asm volatile("s_waitcnt vmcnt(0)" ::: "memory");
  } else asm volatile("s_waitcnt vmcnt(%0)" :: "n"(w == 1 ? 20 : 40) : "memory");
  unsigned ck = 0u;
  if (w == 3) { ck = pl.get(b, lane0); *(unsigned*)(misc + 11) = ck; }
  BAR();
#pragma unroll
  for (int p = 0; p < 10; ++p) {
    if (w < 2) W[0][p] = cat16(wqk_frag(lds, 2 * w, 2 * p, lane0), wqk_frag(lds, 2 * w, 2 * p + 1, lane0));
    if (w == 0) W[1][p] = cat16(wqk_frag(lds, 1, 2 * p, lane0), wqk_frag(lds, 1, 2 * p + 1, lane0));
  }
  float cb2 = 0.f;
  if (w == 0) {
    float n = __builtin_amdgcn_sqrtf(bn[0] + bn[1]) + __builtin_amdgcn_sqrtf(bn[2] + bn[3]);
    n += dppf<DPP_XOR1>(n); n += dppf<DPP_XOR2>(n); n += dppf<DPP_ROR4>(n); n += dppf<DPP_ROR8>(n);
    n = x32mx(x16mx(n));
    cb2 = u2f(__builtin_amdgcn_readfirstlane(f2u(1.15f * n * n)));
  }
  const float cm2 = u2f((f2u(cb2) & 0x7fffffu) | 0x40000000u);
  const int ek = imax((int)((f2u(cb2) >> 23) & 0xffu), 1) - 252;
  if (w >= 2) {
#pragma unroll
    for (int i = 0; i < 4; ++i) *(unsigned*)(lds + L_KX + (w - 2) * 4096 + i * 1024 + lane0 * 16 + 12) = 0u;
  }
  OutTask ot;
  const int zl = lane0 - 16;
  const bool zon = w == 3 && zl >= 0 && (zl < 40 || b < ACC_NB + 17);
  unsigned zo = zon ? zero_base(b, h, j, zl) + (zl < 32 ? sct_slot(ck, 0u) : (zl < 40 ? scm_slot(ck, 0u) : 0u)) : 0x7ffffff0u;
  unsigned zm = zon ? (zl < 32 ? SCT_STEP + 1u : (zl < 40 ? SCM_STEP : (unsigned)ACC_STEP)) : 0u;
  const unsigned zx = ((ck ^ (ck >> 4)) & 7u) * 4096u;
  if (w == 3) { PIN_V(zo); PIN_V(zm); }
  auto st_out = [&](int s, int lane, auto go) {
    if (w < 2) return;
    if (ST_E) lds_spin<L_MISC + 112, 1>(lds0, (unsigned)imax(s, 0)); else go();
    const int sl = s - 2 - LAG4;
    const char* sp = lds + L_STG + 2048 + (w & 1) * 1280;
    u32x4 v0 = *(const u32x4*)(sp + lane * 16), v1 = *(const u32x4*)(sp + 1024 + (lane & 15) * 16);
    const u32x4 z = {0u, 0u, 0u, 0u};
    unsigned oo = (sl < 0 ? 0x7ff00000u : op_slot(sl) + (unsigned)((m * 32 + (w & 1) * 16 + gp) * 1280)) + (unsigned)(lane * 16), za = 0x7ffffff0u;
    if (w == 3) {
      const unsigned zs = (unsigned)(s + 2) % SR;
      za = (zo + (s < 0 ? 1024u : zs) * (zm & ~1u)) ^ ((zm & 1u) * (zs & 4u ? zx : 0u));
#pragma unroll
      for (int i = 0; i < 4; ++i) { v1[i] = lane < 16 ? v1[i] : z[i]; PIN_V(v1[i]); }
    }
    za = lane < 16 ? oo + 1024 : za;
    PIN_V(za); PIN_V(oo); PIN_V(v0); PIN_V(v1);
    __builtin_amdgcn_sched_barrier(0);
    if (ST_E) go();
    SC1_STORE(v0, oo);
    SC1_STORE(v1, za);
    __builtin_amdgcn_sched_barrier(0);
    if (w == 2) ot.store(a, s - 2 - LAG5, b, lane);
    else {
      const unsigned xo_ = (unsigned)((s - 8) * 1024 + lane * 16);
      if (__builtin_expect((unsigned)(s - 8) < 20u, 0)) SC1_STORE(z, xo_ < X_BYTES / 256 ? (unsigned)(OFF_X + b * (X_BYTES / 256)) + xo_ : 0x7ffffff0u);
    }
  };
  STAMP_DECL(); ARRIVE_DECL();
  auto mma_seg = [&](int seg, int sv, int lane, auto pause) {
    const int c = lane & 15, g = lane >> 4;
    const char* xp = lds + L_XN + (seg & 1) * 40960 + lane * 16;
    f32x4 acc[2][2] = {{Z4, Z4}, {Z4, Z4}};
    bf16x8 x[4][2];
#pragma unroll
    for (int p = 0; p < 3; ++p) { x[p][0] = *(const bf16x8*)(xp + p * 1024); x[p][1] = *(const bf16x8*)(xp + (20 + p) * 1024); }
    unsigned eo[2];
#pragma unroll
    for (int ks = 0; ks < 20; ++ks) {
      if (ks == (w == 0 ? KP0 : (w == 1 ? KP1 : KP2))) { pause(); __builtin_amdgcn_sched_barrier(0); }
      if (ks == 17) {
#pragma unroll
        for (int i = 0; i < 2; ++i) {
          eo[i] = (unsigned)(w == 0 ? (i ? L_KX : L_QX) + (seg % NKQ) * 2048 + lane * 16
                                    : (w == 1 && i == 0 ? ((g >> 1) ? L_KX : L_QX) + (seg % NKQ) * 2048 + (2 * (g & 1) * 16 + c) * 16 + 8 : L_VOX + sv * 5120 + btile(i) * 1024 + lane * 16));
          if (i == 0 || w == 1) PIN_V(eo[i]);
        }
        if (w == 0) eo[1] = eo[0] + (L_KX - L_QX);
        if (w >= 2) eo[1] = eo[0] + 1024;
      }
      __builtin_amdgcn_sched_barrier(0);
#pragma unroll
      for (int q = 0; q < 4; ++q) {
        const int tt = 1 - (q >> 1), i = q & 1;
        const bool isqk = w == 0 || (w == 1 && i == 0);
        const bf16x8 wf = half8(W[i][ks >> 1], ks & 1);
        acc[i][tt] = isqk ? mfma32(wf, x[ks & 3][tt], acc[i][tt]) : mfma32(x[ks & 3][tt], wf, acc[i][tt]);
        __builtin_amdgcn_sched_barrier(0);
        if (q < 2 && ks + 3 < 20) x[(ks + 3) & 3][q] = *(const bf16x8*)(xp + (q * 20 + ks + 3) * 1024);
        __builtin_amdgcn_sched_barrier(0);
      }
    }
#pragma unroll
    for (int i = 0; i < 2; ++i) {
      if (w + i == 1) { const float ks_ = (w == 0 || (g >> 1)) ? KEXP : 1.0f; acc[i][0] *= ks_; acc[i][1] *= ks_; }
      const u32x4 v = {pack2(acc[i][0][0], acc[i][0][1]), pack2(acc[i][0][2], acc[i][0][3]), pack2(acc[i][1][0], acc[i][1][1]), pack2(acc[i][1][2], acc[i][1][3])};
      char* p = lds + eo[i];
      if (w == 0) { *(u32x2*)(p) = (u32x2){v[0], v[1]}; *(u32x2*)(p + 1024) = (u32x2){v[2], v[3]}; }
      else if (w == 1 && i == 0) { *(unsigned*)(p) = v[0]; *(unsigned*)(p + 256) = v[1]; *(unsigned*)(p + 1024) = v[2]; *(unsigned*)(p + 1280) = v[3]; }
      else *(u32x4*)p = v;
    }
  };
  unsigned vp, mp, ip;
  int sidx, ei = 127;
  float inv;
  f32x4 acc[2];
  auto vo_pre = [&](int s, int sv, int lane) {
    vp = (unsigned)(w == 0 ? L_MISC + (s & 3) * 4 : L_VOM + sv * 1280 + (btile(0) * 32 + (w == 1 ? lane & 31 : lane)) * 8);
    mp = (unsigned)(L_BM + lane * 16);
    PIN_V(vp); PIN_V(mp);
    if (w == 0) { ip = (unsigned)(L_MISC + 16 + ((s + 1) & 1) * 4); PIN_V(ip); }
    else {
      inv = misc[4 + (s & 1)];
      sidx = (lane & 8) ? 0xEEEE : 0x4444;
      acc[0] = acc[1] = Z4;
      PIN_V(sidx); PIN_V(acc[1]);
      if (w >= 2) { PIN_V(acc[0]); PIN_V(inv); }
    }
    __builtin_amdgcn_sched_barrier(0);
  };
  auto vo_mem = [&](int s, auto last) {
    __builtin_amdgcn_s_setprio(2);
    asm volatile(".rept %0\n\ts_nop 3\n\t.endr" :: "n"(w == 0 ? VO_A0 : (w == 1 ? VO_A1 : VO_A2)) : "memory");
    float xmax = 0.f;
    if (w == 0) xmax = *(const float*)(lds + vp);
    constexpr int NX = w == 2 ? VO_N2 : (w == 3 ? VO_N3 : 10);
    bf16x8 x[10];
#pragma unroll
    for (int p = 0; p < NX; ++p) x[p] = *(const bf16x8*)(lds + mp + p * 1024);
    __builtin_amdgcn_sched_barrier(0);
    if (w < 2) asm volatile(".rept %0\n\ts_nop 3\n\t.endr" :: "n"(w == 0 ? VO_B0 : VO_B1) : "memory");
    if (VO_W >= 0) asm volatile("s_waitcnt lgkmcnt(%0)" :: "n"(VO_W < 0 ? 0 : VO_W) : "memory");
    STAMP(5);
    __builtin_amdgcn_sched_barrier(0);
    int ex = 0;
#pragma unroll
    for (int p = 0; p < 10; ++p) {
      if (w == 0) acc[0] = mfma32(x[p], x[p], p ? acc[0] : Z4);
      if (w == 0 && p == 9) { ex = imax((int)((__builtin_amdgcn_readfirstlane(f2u(__builtin_amdgcn_sqrtf(cb2 * xmax))) >> 23) & 0xffu) + 1, 32); PIN_S(ex); }
#pragma unroll
      for (int i = (w == 1); i < 2 && w != 0; ++i) acc[i] = smfma64(x[p], W[i][p], acc[i], sidx);
      __builtin_amdgcn_sched_barrier(0);
      if (p + NX < 10) x[p + NX] = *(const bf16x8*)(lds + mp + (p + NX) * 1024);
      __builtin_amdgcn_sched_barrier(0);
    }
    STAMP(7);
    if (w != 0) {
      float v[4];
#pragma unroll
      for (int r = 0; r < 4; ++r) {
        const auto sw = __builtin_amdgcn_permlane32_swap(f2u(acc[w == 1][r]), f2u(acc[1][r]), false, false);
        v[r] = (u2f(sw[0]) + u2f(sw[1])) * inv;
      }
      *(u32x2*)(lds + vp) = (u32x2){pack2(v[0], v[1]), pack2(v[2], v[3])};
    } else {
      const int t = (int)f2u(fmx(fmx(acc[0][0], acc[0][1]), fmx(acc[0][2], acc[0][3])) * cm2);
      const int et = wave_imax(t) >> 23, em = ((et + ek) >> 1) + ei;
      int e = imax(et == 255 ? 256 : em, ex);
      PIN_S(e);
      e = imin(e, 256);
      ei = e - 31;
      *(unsigned*)(lds + ip) = (unsigned)ei << 23;
      const u32x3 sr = {(unsigned)(285 - e) << 23, (unsigned)(e - 87) < 79u ? REC_RNG << 23 : 0u, (unsigned)(s + 1)};
      asm volatile("ds_write_b96 %0, %1 offset:%2" :: "v"(lds0), "v"(sr), "n"(L_MISC + 32) : "memory");
    }
    if (last == 0 && w >= 2) asm volatile("s_waitcnt vmcnt(%0)" :: "n"(w == SCH_W ? 2 : 0) : "memory");
    if (w != 0) lds_inc<L_MISC + 28>(lds0);
    STAMP(8);
    if (last) __builtin_amdgcn_s_setprio(0);
    if (last && w == 2) { VM_DRAIN(); lds_inc<L_MISC + 28>(lds0); }
  };
  unsigned* const cntq = (unsigned*)(misc + 12);
  unsigned* const cntt = (unsigned*)(misc + 26 + (w & 1));
  unsigned* const cntp = (unsigned*)(misc + 23);
  unsigned* const cntx2 = (unsigned*)(misc + 28);
  float xn4 = 0.f;
  auto wpf = [&](int s, int lane) {
    constexpr int PER = 81920 / QR / WPF_N;
    static_assert(PER % WPF_S == 0 && PER / WPF_S <= 64, "prefetch");
    touch(a.ws + OFF_WQKVBT + (size_t)imin(qkv_col(b), 14) * 163840 + (qkv_row(b) % (2 * QR)) * (81920 / QR), (s - WPF0) * PER + imin(lane, PER / WPF_S - 1) * WPF_S, lds0);
  };
  auto t5_get = [&](int s, int lane) {
    ot.load(a, rs, s - 1 - LAG5, b);
    xn4 = u2f(__hip_atomic_load((const GAS unsigned*)xnrm + (imax(s + 1, 0) * 32 + (lane & 31)) * 4, RLX_AGENT));
    __builtin_amdgcn_sched_barrier(0);
  };
  Sched sch;
  auto late = [&](int s, int lane) {
    if (w == SCH_W && SCH_LATE && __builtin_expect(s >= 0, 1)) *(u32x2*)(misc + 24) = (u32x2){sch.run(a, rs, s, b, lane), (unsigned)(s + 1)};
    if ((w == 2 && F6) || (w == 0 && F4) || (w == 1 && F5) || (w == 3 && F7)) { lds_spin<L_MISC + 48, 1>(lds0, 2u * (unsigned)imax(s + 1, 0)); if (FB_SL && w != 2) __builtin_amdgcn_s_sleep(FB_SL); }
    if (w >= 2) { x_fetch_n<w == 2 ? F6 : F7>(x_src(a, s + 3, b), lds0, s + 3, F4 + F5 + 2 * F67 + (w == 3 ? F6 : 0), lane * 16); return; }
    unsigned sd = 0u;
    if ((w == 0 && XPF == 3) || XPF == 4 || (w == 1 && XPF == 5)) stouch<10>(a.ws + OFF_XN + (size_t)imin(s + 4, NSEG - 1) * (s < 1 ? 0 : 40960) + (b >> 3) * 1280 + w * 64, sd);
    x_fetch_n<w == 0 ? F4 : F5>(x_src(a, s + 3, b), lds0, s + 3, w == 0 ? 0 : F4, lane * 16);
    if (WPF_W == 0 && w == 0 && __builtin_expect(s >= WPF0 && s < WPF0 + WPF_N, 0)) wpf(s, lane);
    if (w == 1 && __builtin_expect(s == CPF_S, 0)) code_touch(b, lane, lds0);
    lds_spin<L_MISC + 104 + 4 * (w & 1), PS_SL>(lds0, (unsigned)imax(s + 1, 0));
    asm volatile("" :: "s"(sd));
  };
  auto xn_put = [&](int s) {
    float xn2 = xn4;
    xn2 = fmx(xn2, dppf<DPP_XOR1>(xn2)); xn2 = fmx(xn2, dppf<DPP_XOR2>(xn2));
    xn2 = fmx(xn2, dppf<DPP_ROR4>(xn2)); xn2 = fmx(xn2, dppf<DPP_ROR8>(xn2));
    misc[s & 3] = x16mx(xn2);
  };
  if (w == 2) { ot.init(b, lane0); ot.out = (u32x2){0u, 0u}; ot.xo = ot.out; ot.y[0] = ot.y[1] = ot.y[2] = ot.y[3] = (u32x4){0u, 0u, 0u, 0u}; }
  VM_DRAIN();
  lds_arrive(cnt, lane0);
  lds_wait(cnt, 8u);
  sch.tsv = sch.rec = (u32x4){0u, 0u, 0u, 0u};
  sch.side = __builtin_amdgcn_readfirstlane(*(const unsigned*)(misc + 11)) & 8192u;
  sch.o = 0u;
  int sv = NV - 1;
  CODE_PAD((PADT >> (4 * w)) & 15);
#pragma unroll 1
  for (int s = -1; s < NSEG - 1; ++s) {
    const int sv1 = sv == NV - 1 ? 0 : sv + 1;
    FRESH_LANE();
    STAMP(0);
    mma_seg(s + 1, sv1, lane, [&]() { STAMP(1); late(s - 1, lane); if (w == SCH_W) sch.pre(s); STAMP(9); });
    STAMP(2);
    __builtin_amdgcn_sched_barrier(0);
    VM_DRAIN();
    if (w == 2) { ot.run(lane); xn_put(s); }
    STAMP(10);
    st_out(s, lane, [&]() { lds_spin<L_MISC + 92, W1_SL>(lds0, 4u * (unsigned)imax(s, 0)); });
    if (w == 2) t5_get(s, lane);
    __builtin_amdgcn_sched_barrier(0);
    if (w >= 2) vo_pre(s, sv, lane);
    STAMP(3);
    BAR();
    STAMP(4);
    if (__builtin_expect(s >= 0, 1)) {
      if (w == SCH_W) { sch.load(rs, lane0); __builtin_amdgcn_sched_barrier(0); }
      if (w < 2) vo_pre(s, sv, lane);
      vo_mem(s, std::integral_constant<int, 0>{});
      STAMP(6);
      if (w == SCH_W && !SCH_LATE) {
        __builtin_amdgcn_s_setprio(SCH_P);
        *(u32x2*)(misc + 24) = (u32x2){sch.run(a, rs, s, b, lane0), (unsigned)(s + 1)};
      }
      STAMP(11);
    } else BAR();
    __builtin_amdgcn_s_setprio(MMA_P);
    __builtin_amdgcn_sched_barrier(0);
    sv = sv1;
  }
  __builtin_amdgcn_s_setprio(0);
  STAMP_OUT();
  late(NSEG - 2, lane0);
  if (w == 2) ot.run(lane0);
  { FRESH_LANE(); if (w == 0) drain_pre(lds, b, lane); qkv_pre(vo, b, w, lane); }
  st_out(NSEG - 1, lane0, [&]() { lds_spin<L_MISC + 92, 1>(lds0, 4u * (NSEG - 1)); });
  if (w != 0) vo_pre(NSEG - 1, (NSEG - 1) % NV, lane0);
  BAR();
  if (w != 0) vo_mem(NSEG - 1, std::integral_constant<int, 1>{});
}

__device__ __forceinline__ void qkv_tail(const RecArgs& a, char* lds, const int w, const unsigned (&vo)[2][2]) {
  const int lane0 = __builtin_amdgcn_workitem_id_x() & 63, b = __builtin_amdgcn_workgroup_id_x();
  const int r = qkv_row(b), cb = qkv_col(b);
  if (cb >= 15) return;
  const unsigned lds0 = (unsigned)(uintptr_t)(LAS char*)lds;
  unsigned* const stage = (unsigned*)(lds + L_MISC) + 17;
  unsigned* const cnty = stage + 1;
  const f32x4 Z4 = {0.f, 0.f, 0.f, 0.f};
  auto fetch = [&](int i) {
    x_fetch_n<10>(x_src(a, NSEG + i, b), lds0, i, 10 * w, lane0 * 16);
  };
  unsigned want = 0;
  auto sync = [&]() { VM_DRAIN(); want += 4u; lds_inc<L_MISC + 72>(lds0); while (__hip_atomic_load(cnty, __ATOMIC_RELAXED, __HIP_MEMORY_SCOPE_WORKGROUP) < want) { } asm volatile("" ::: "memory"); };
  LOGQ(0);
  bf16x8 W[2][20];
  const __amdgpu_buffer_rsrc_t rw = __builtin_amdgcn_make_buffer_rsrc(a.ws + OFF_WQKVBT + (size_t)(cb * 8 + 2 * w) * 20480, 0, 40960, 0x00020000);
  auto w_get = [&](int ks) {
#pragma unroll
    for (int i = 0; i < 2; ++i) W[i][ks] = as8(__builtin_amdgcn_raw_buffer_load_b128(rw, lane0 * 16 + (ks & 3) * 1024, (i * 20 + (ks & ~3)) * 1024, 0));
    __builtin_amdgcn_sched_barrier(0);
  };
#pragma unroll
  for (int ks = 0; ks < QW; ++ks) w_get(ks);
  bf16x8 ones = mk8(0x3f803f80u, 0x3f803f80u, 0x3f803f80u, 0x3f803f80u);
  PIN_V(ones);
  const __amdgpu_buffer_rsrc_t ro = __builtin_amdgcn_make_buffer_rsrc(a.ws + OFF_QB, 0, (int)(OFF_OB - OFF_QB), 0x00020000);
  const int t0 = w & 1;
  auto body = [&](auto SW, auto SQ) {
    const float qs = cb < 5 ? 0.22360679774997896f * 1.4426950408889634f : 1.0f;
    f32x4 cs[2] = {Z4, Z4};
    LOGQ(1);
    f32x4 acc[2][2], st;
    auto mma = [&](int it, auto FIRST) {
      const char* xp0 = lds + L_XN + (it & 1) * 40960 + t0 * 20480 + lane0 * 16;
      const char* xp1 = lds + L_XN + (it & 1) * 40960 + (1 - t0) * 20480 + lane0 * 16;
      acc[0][0] = acc[0][1] = acc[1][0] = acc[1][1] = st = Z4;
      bf16x8 x[4][2];
#pragma unroll
      for (int p = 0; p < 3; ++p) { x[p][0] = *(const bf16x8*)(xp0 + p * 1024); x[p][1] = *(const bf16x8*)(xp1 + p * 1024); }
      __builtin_amdgcn_sched_barrier(0);
#pragma unroll
      for (int ks = 0; ks < 20; ++ks) {
        if (FIRST && ks + QW < 20) w_get(ks + QW);
#pragma unroll
        for (int q = 0; q < 4; ++q) {
          const int tt = 1 - (q >> 1), i = 1 - (q & 1);
          acc[i][tt] = SW ? mfma32(W[i][ks], x[ks & 3][tt], acc[i][tt]) : mfma32(x[ks & 3][tt], W[i][ks], acc[i][tt]);
          __builtin_amdgcn_sched_barrier(0);
          if (q < 2 && ks + 3 < 20) x[(ks + 3) & 3][q] = *(const bf16x8*)((q ? xp1 : xp0) + (ks + 3) * 1024);
          __builtin_amdgcn_sched_barrier(0);
        }
        st = mfma32(SQ ? x[ks & 3][0] : ones, x[ks & 3][0], st);
        if (FIRST) {
#pragma unroll
          for (int i = 0; i < 2; ++i) cs[i] = SW ? mfma32(W[i][ks], ones, cs[i]) : mfma32(ones, W[i][ks], cs[i]);
        }
        __builtin_amdgcn_sched_barrier(0);
      }
    };
    mma(0, std::integral_constant<bool, true>{});
#pragma unroll 1
    for (int it = 0; it < 4; ++it) {
      if (it) mma(it, std::integral_constant<bool, false>{});
      LOGQ(2 + 3 * it);
      FRESH_LANE();
      float* const sp = (float*)(lds + L_ST) + (it & 1) * 64;
      if (SQ) { if ((c >> 2) == g) sp[32 + t0 * 16 + c] = (c & 3) == 0 ? st[0] : ((c & 3) == 1 ? st[1] : ((c & 3) == 2 ? st[2] : st[3])); }
      else if (g == 0) sp[t0 * 16 + c] = st[0];
      sync();
      LOGQ(3 + 3 * it);
      if (it == 0) fetch(2);
      const bool late = r + 48 >= 55;
      if (it == 1 && !late) fetch(3);
      const unsigned so = (unsigned)((r + 16 * it) * (cb < 5 ? 64 * QST : KVT));
      auto stat = [&](float sm, float sq, float& mean, float& rstd) {
        mean = sm * (1.0f / DM);
        rstd = __builtin_amdgcn_rsqf(__builtin_fmaxf(sq * (1.0f / DM) - mean * mean, 0.f) + 1e-5f);
      };
#pragma unroll
      for (int tt = 0; tt < 2; ++tt) {
        const int tq = tt ^ t0;
        float mean[4], rstd[4];
        if (SW) {
          stat(sp[tq * 16 + c], sp[32 + tq * 16 + c], mean[0], rstd[0]);
          rstd[0] *= qs;
        } else {
          const f32x4 sm = *(const f32x4*)(sp + tq * 16 + 4 * g), sq = *(const f32x4*)(sp + 32 + tq * 16 + 4 * g);
#pragma unroll
          for (int k = 0; k < 4; ++k) stat(sm[k], sq[k], mean[k], rstd[k]);
        }
#pragma unroll
        for (int i = 0; i < 2; ++i) {
          float v[4];
#pragma unroll
          for (int k = 0; k < 4; ++k) v[k] = (acc[i][tt][k] - mean[SW ? 0 : k] * cs[i][SW ? k : 0]) * rstd[SW ? 0 : k];
          __builtin_amdgcn_raw_buffer_store_b64((u32x2){pack2(v[0], v[1]), pack2(v[2], v[3])}, ro, vo[i][tt], so, 0);
        }
      }
      LOGQ(4 + 3 * it);
      if (it == 2 && late) lds_spin<L_MISC + 68, 0>(lds0, 5u);
    }
  };
  typedef std::integral_constant<bool, false> F_; typedef std::integral_constant<bool, true> T_;
  if (cb < 10) { if (w >> 1) body(T_{}, T_{}); else body(T_{}, F_{}); }
  else { if (w >> 1) body(F_{}, T_{}); else body(F_{}, F_{}); }
}

__device__ __forceinline__ void t2_mem(const bf16x8 fr, const bf16x8 q0, const bf16x8 q1, const __amdgpu_buffer_rsrc_t rs, unsigned o, int h, int j, float inv, int lane) {
  const f32x4 Z4 = {0.f, 0.f, 0.f, 0.f};
  const f32x4 d0 = mfma32(fr, q0, Z4) * inv, d1 = mfma32(fr, q1, Z4) * inv;
  const unsigned a0 = pack2n(d0[0], d0[1]), a1 = pack2n(d0[2], d0[3]), b0 = pack2n(d1[0], d1[1]), b1 = pack2n(d1[2], d1[3]);
  const auto r0 = __builtin_amdgcn_permlane32_swap(a0, b0, false, false), r1 = __builtin_amdgcn_permlane32_swap(a1, b1, false, false);
  SC1_STORE8S(((u32x2){r0[1], r1[1]}), ts_mem(h, j) + (lane >> 5) * 256 + (lane & 12) * 16 + (lane & 16) * 2 + (lane & 3) * 8, o);
}

template <int w>
__device__ __forceinline__ void rec_waves(const RecArgs& a, char* lds, __amdgpu_buffer_rsrc_t rs, unsigned& tlast) {
  PIN_S(rs);
  const int lane = __builtin_amdgcn_workitem_id_x() & 63, b = __builtin_amdgcn_workgroup_id_x(), c = lane & 15, g = lane >> 4, wid = w; (void)wid;
  const int gp = (b & 7) * 2 + (b >> 7), m = (b >> 3) & 15, h0 = 2 * gp, h = h0 + (m >> 3), j = m & 7;
  float* const misc = (float*)(lds + L_MISC);
  const f32x4 Z4 = {0.f, 0.f, 0.f, 0.f};
  LOGP(0);
  __builtin_amdgcn_s_setprio(3);
  const unsigned lds0 = (unsigned)(uintptr_t)(LAS char*)lds;
  if (!a.layer) img_fetch<12>(a, lds0, h, j, w, lane);
  constexpr int NC = w < 2 ? 2 : 3;
  const int far = ((b >> 2) & 1) ^ 1;
  auto chunk = [&](int f) { return f == 0 ? 4 * far + w : (w == 2 && f == 1 ? 8 + far : (w == 3 && f == 2 ? 9 - far : 4 - 4 * far + w)); };
  bf16x16 Wqk[3][NC];
  u32x4 bm0[NC][2];
#pragma unroll
  for (int f = 0; f < 2 * NC; ++f) bm0[f >> 1][f & 1] = *(const u32x4*)(a.mem0 + (size_t)(c & 7) * DM + 64 * chunk(f >> 1) + 16 * g + 8 * (f & 1));
  if (!a.layer) asm volatile("s_waitcnt vmcnt(0)" ::: "memory");
  BAR();
  LOGP(9);
  const unsigned ck = __builtin_amdgcn_readfirstlane(*(const unsigned*)(misc + 11));
  const unsigned flip = (ck ^ (unsigned)(b << 11)) & 8192u;
#pragma unroll
  for (int t = 0; t < 3; ++t)
#pragma unroll
    for (int f = 0; f < NC; ++f) Wqk[t][f] = cat16(wqk_frag(lds, t, 2 * chunk(f), lane), wqk_frag(lds, t, 2 * chunk(f) + 1, lane));
  if (!a.layer) lds_arrive((unsigned*)(misc + 6), lane);
  else {
    if (w == 0) {
      for (unsigned spins = 0; spins < SPIN_CAP; ++spins) {
        if (__builtin_amdgcn_raw_buffer_load_b32(rs, (b & 7) * 256, (unsigned)OFF_RDY, 16) >= 16u) break;
        __builtin_amdgcn_s_sleep(4);
        asm volatile("" ::: "memory");
      }
      lds_inc<L_MISC + 84>(lds0);
    }
    lds_wait((unsigned*)(misc + 21), 2u);
#pragma unroll
    for (int f = 0; f < 2 * NC; ++f) PIN_V(bm0[f >> 1][f & 1]);
#pragma unroll
    for (int i = 0; i < 10; ++i) x_fetch(x_src(a, 0, b), lds0, 0, 10 * w + i, lane);
  }
  const bf16x8 SelLo = sel_frag(c, 0), SelHi = sel_frag(c, 8);
  bf16x8 ONES = mk8(0x3f803f80u, 0x3f803f80u, 0x3f803f80u, 0x3f803f80u);
  if (w < 2) PIN_V(ONES);
  bool dead = false;
  float inv = 1.0f;
  unsigned* const cnt2 = (unsigned*)(misc + 7);
  unsigned l2off[2 * NC];
#pragma unroll
  for (int f = 0; f < 2 * NC; ++f) { l2off[f] = acc_off(16 * chunk(f >> 1) + 4 * g + 2 * (f & 1) + (c >> 3), c & 7) ^ flip; PIN_V(l2off[f]); }
  int sidx = (c >> 3) ? 0xEEEE : 0x4444;
  PIN_V(sidx);
  const unsigned p1off = (unsigned)((w == 0 ? (h & 2) * 1024 + j * 256 + (h & 1) * 64 + (g & 1) * 8 : (h & 3) * 32768 + j * 512 + (g >> 1) * 256 + (g & 1) * 64) + (c & 4) * 32 + (c & 3) * 16);
  unsigned p1v = (c < 8) && (w != 0 || g >= 2) ? p1off : 0x7ffffff0u, p1cur = 0x7ffffff0u;
  PIN_V(p1v); PIN_V(p1cur);
  const int hd = (c >> 2) & 1, mr = hd ? 3 - (c & 3) : (c & 3);
  const unsigned l1off = (unsigned)(((h0 + hd) & 3) * 32768 + g * 512 + (w & 1) * 128 + (c >> 3) * 64 + mr * 16);
  const unsigned l1offm = (unsigned)((h0 & 2) * 1024 + (g + 4 * (c >> 3)) * 256 + (w & 1) * 128 + hd * 64 + mr * 16);
  const int plane = (lane & ~6) | ((lane & 2) << 1) | ((lane & 4) >> 1);
  unsigned acco = c < 8 ? (acc_off(64 * far + m * 4 + g, 4 * (w & 1) + mr) + (unsigned)(hd * 8)) ^ flip : 0x7ffffff0u;
  unsigned acco2 = c >= 8 ? (acc_off(64 - 64 * far + m * 4 + g, 4 * (w & 1) + mr) + (unsigned)(hd * 8)) ^ flip
                          : (c < 4 ? (acc_off(128 + m * 2 + (g & 1), 4 * (w & 1) + mr) + (unsigned)((g >> 1) * 8)) ^ flip : 0x7ffffff0u);
  if (w < 2) { PIN_V(acco); PIN_V(acco2); }
  unsigned bmo[NC];
#pragma unroll
  for (int f = 0; f < NC; ++f) { bmo[f] = (unsigned)(chunk(f) * 1024 + lane * 16); PIN_V(bmo[f]); }
  bf16x8 bm[NC];
  auto bm_put = [&](int f) { *(bf16x8*)(lds + L_BM + bmo[f]) = bm[f]; __builtin_amdgcn_sched_barrier(0); };
  f32x4 acc[3] = {Z4, Z4, Z4};
  const int rt2 = ((c >> 3) * 8 + 4 * (g & 1)) * 28 + 16 + (c & 7);
  unsigned redo[3] = {(unsigned)(4 * g * 28 + c), (unsigned)(g < 2 ? (4 * g + 2) * 28 + c : rt2), (unsigned)(g < 2 ? (4 * g + 10) * 28 + c : rt2 + 56)};
#pragma unroll
  for (int k = 0; k < 3; ++k) { redo[k] = L_RED1 + w * 1792 + redo[k] * 4; PIN_V(redo[k]); }
  auto red_put = [&]() {
    auto hs = [&](float x, float y) { const auto sw = __builtin_amdgcn_permlane32_swap(f2u(x), f2u(y), false, false); return u2f(sw[0]) + u2f(sw[1]); };
#pragma unroll
    for (int k = 0; k < 3; ++k) {
#pragma unroll
      for (int i = 0; i < 2; ++i) *(float*)(lds + redo[k] + i * 112) = k == 0 ? hs(acc[0][i], acc[1][i]) : hs(acc[k - 1][2 + i], acc[2][2 * k - 2 + i]);
      __builtin_amdgcn_sched_barrier(0);
    }
  };
#pragma unroll
  for (int f = 0; f < NC; ++f) {
    const u32x4 u = bm0[f][0], v = bm0[f][1];
    bm[f] = c < 8 ? mk8(u[0], u[2], v[0], v[2]) : mk8(u[1], u[3], v[1], v[3]);
    bm_put(f);
#pragma unroll
    for (int t = 0; t < 3; ++t) acc[t] = smfma64(bm[f], Wqk[t][f], acc[t], sidx);
  }
  red_put();
  VM_DRAIN();
  if (a.layer) lds_arrive((unsigned*)(misc + 6), lane);
  lds_wait((unsigned*)(misc + 6), 8u);
#pragma unroll
  for (int i = 0; i < 10; ++i) x_fetch(x_src(a, 1, b), lds0, 1, 10 * w + i, lane);
  LOGP(1);
  BAR();
  STAMP_DECL(); ARRIVE_DECL();
  bf16x8 fr, qx[2];
  auto kq_get = [&](int seg) {
    const char* p = lds + (w == 1 ? L_KX : L_QX) + (seg % NKQ) * 2048 + lane * 16;
    if (w == 1 || w == 2) { qx[0] = *(const bf16x8*)p; qx[1] = *(const bf16x8*)(p + 1024); }
  };
  unsigned qo4 = (unsigned)(L_RED1 + (w == 1 ? (c & 7) * 112 + (c >> 3) * 3584 : c * 112) + g * 16), qo2 = qo4 + 64u - (unsigned)(g * 8), qo2b = qo2 + 3584u;
  if (w < 3) { PIN_V(qo4); PIN_V(qo2); if (w != 1) PIN_V(qo2b); }
  auto pub1 = [&](unsigned off, unsigned o1) {
    if (w == 0) fr = qk_frag(lds + qo4, lds + qo2, lds + qo2b);
    else {
      const f32x4 a0 = *(const f32x4*)(lds + qo4), a1 = *(const f32x4*)(lds + qo4 + 1792);
      const f32x2 b0 = *(const f32x2*)(lds + qo2), b1 = *(const f32x2*)(lds + qo2 + 1792);
      float v[6];
#pragma unroll
      for (int i = 0; i < 6; ++i) v[i] = i < 4 ? a0[i] + a1[i] : b0[i - 4] + b1[i - 4];
#pragma unroll
      for (int i = 0; i < 6; ++i) v[i] += dppf<DPP_ROR8>(v[i]);
      fr = mk8(pack2(v[0], v[1]), pack2(v[2], v[3]), pack2(v[4], v[5]), 0u);
    }
    const f32x4 sc = mfma32(w == 0 ? fr : qx[0], fr, Z4);
    if (w == 0) SC1_STORE8S(((u32x2){pack2n(sc[0], sc[1]), pack2n(sc[2], sc[3])}), off, o1);
    else {
      const f32x4 s2 = mfma32(qx[1], fr, Z4);
      __builtin_amdgcn_raw_buffer_store_b128((u32x4){pack2n(sc[0], sc[1]), pack2n(sc[2], sc[3]), pack2n(s2[0], s2[1]), pack2n(s2[2], s2[3])}, rs, off, o1, 16);
    }
  };
  VM_DRAIN();
  kq_get(0);
  unsigned o1 = sct_slot(ck, 0u), o1m = scm_slot(ck, 0u);
  unsigned t2o = ts_tok(h, j) + (unsigned)((c >> 2) * 256 + g * 64 + (c & 3) * 16);
  TokScores t2;
  if (w == 3) { PIN_V(t2o); t2.get(lds, 0, lane); t2.run(0); t2.run(1); t2.store(rs, t2o, ts_slot(ck, 0)); }
  if (w < 2) pub1(p1v, w == 0 ? o1m : o1);
  const unsigned tst = now32() | 1u;
  if (w == 3 && ST_X) __builtin_amdgcn_raw_buffer_store_b32(tst, rs, lane < 2 ? ts_piece(b >> 2) - ACC_TS + (b & 3) * 4 + lane * 8192 : 0x7ffffff0, (unsigned)(OFF_ACC + (SR - 1) * ACC_STEP) + ACC_TS, 16);
  LOGP(2);
  BAR();
  LOGP(3);
#pragma unroll
  for (int i = 0; i < 10; ++i) x_fetch(x_src(a, 2, b), lds0, 2, 10 * w + i, lane);
  VM_DRAIN();
  unsigned o2 = (unsigned)OFF_ACC, o2p = 0u, tw = 0u;
  asm volatile("" : "+s"(o2p));
  unsigned tc0 = 0u, tstar = 0u, tg = 0u;
  int dmin = 0, dmax = 0;
  int e1 = 100000;
  unsigned offs = 100u;
  TokTask tk;
  PvTask2 pv2;
  if (w == 3) pv2.init(gp, lane);
  if (w == 2) tk.init(h, j, lane);
  unsigned o4 = 0u;
  unsigned o3, o3p;
  u32x16 P = {0u, 0u, 0u, 0u, 0u, 0u, 0u, 0u, 0u, 0u, 0u, 0u, 0u, 0u, 0u, 0u}, V = P;
  u32x8 V8 = {0u, 0u, 0u, 0u, 0u, 0u, 0u, 0u};
  if (w < 2) asm volatile("" : "+{v[208:223]}"(P), "+{v[184:191]}"(V8), "+{v[192:207]}"(V));
  unsigned tso = lane < 2 ? ts_piece(b >> 2) + (unsigned)((b & 3) * 4 + lane * 8192) : 0x7ffffff0u;
  if (w < 2) PIN_V(tso);
  float minf = g >= 2 ? -INFINITY : 0.f;
  if (w < 2) PIN_V(minf);
  int sv = NV - 1;
  bf16x8 vox[5];
  unsigned vma[3], need2;
  u32x4 slo, shi;
  float k1;
  const u16* xsrc;
  float invv = 1.0f;
  auto prep0 = [&](int s) {
    xsrc = x_src(a, s + 3, b);
    if (w == 2) { o3p = ts_slot(ck, (s + 1 - LAG3) & 3); o3 = ts_slot(ck, imax(s, 0)); o4 = pt_slot(ck, imax(s - LAG3, 0)); PIN_S(o3); }
    if (w == 3) { o3p = pt_slot(ck, (s - LAG4) & 3); o4 = ts_slot(ck, s + 1); }
    PIN_S(xsrc); PIN_S(o3p); PIN_S(o4);
    __builtin_amdgcn_sched_barrier(0);
  };
  auto prep = [&](int s) {
    o2 = (unsigned)OFF_ACC + (unsigned)(s % SR) * (unsigned)ACC_STEP;
    sv = sv == NV - 1 ? 0 : sv + 1;
    PIN_S(o2); PIN_S(sv);
    if (w < 2) {
      PIN_S(tc0);
      o1 = sct_slot(ck, s % SR); o1m = scm_slot(ck, s % SR);
      need2 = 4u * (unsigned)(s + 1) + 2u * ((unsigned)(s + 2) / NSEG);
      PIN_S(o1); PIN_S(o1m); PIN_S(need2);
      __builtin_amdgcn_sched_barrier(0);
      kq_get(s);
    }
    invv = misc[4 + (s & 1)];
  };
  auto prep2 = [&](int s) {
#pragma unroll
    for (int k = 0; k < 3; ++k) {
      const int t = k == 0 ? 2 : (k == 1 ? far : 1 - far);
      const char* p = lds + L_VOX + sv * 5120 + t * 1024 + (plane & 47) * 16 + (g & 1) * 8;
      auto rd = [&](const char* q) { const u32x2 a0 = *(const u32x2*)q, a1 = *(const u32x2*)(q + 256); return mk8(a0[0], a0[1], a1[0], a1[1]); };
      vox[k == 0 ? 0 : 2 * k - 1] = rd(p);
      if (k) vox[2 * k] = rd(p + 3072);
      vma[k] = lds0 + (unsigned)(L_VOM + sv * 1280 + t * 256 + (plane & 31) * 8);
      PIN_V(vma[k]);
    }
    __builtin_amdgcn_sched_barrier(0);
    inv = u2f(__builtin_amdgcn_readfirstlane(f2u(invv)));
    slo = __builtin_bit_cast(u32x4, SelLo); shi = __builtin_bit_cast(u32x4, SelHi);
#pragma unroll
    for (int i = 0; i < 4; ++i) { slo[i] = ((slo[i] >> 7) & 0x10001u) * (f2u(inv) >> 16); shi[i] = ((shi[i] >> 7) & 0x10001u) * (f2u(inv) >> 16); }
    if (F0A) { xsrc = x_src(a, s + 3, b); PIN_S(xsrc); }
    k1 = KEXP * inv;
    PIN_V(k1); PIN_V(slo); PIN_V(shi);
    asm volatile("" : "+v"(vox[0]), "+v"(vox[1]), "+v"(vox[2]), "+v"(vox[3]), "+v"(vox[4]));
    __builtin_amdgcn_sched_barrier(0);
  };
  auto rows = [&](int s) {
    const int dl = spin_until(tw);
    if (w < 2) tc0 = cyc32();
    __builtin_amdgcn_sched_barrier(0);
    STAMP(1);
    unsigned spins = 0;
    auto pass = [&](bool first) {
      u32x4 x[2 * NC];
#pragma unroll
      for (int f = 0; f < NC; ++f) {
        if (f && w < 2 && first) { prep(s); asm volatile(".rept %0\n\ts_nop 3\n\t.endr" :: "n"(P2_G1)); }
        else if (f) asm volatile(".rept %0\n\ts_nop 3\n\t.endr" :: "n"(w < 2 ? P2_G0 : (f == 1 ? P2_G2 : P2_G3)));
        __builtin_amdgcn_sched_barrier(0);
        x[2 * f] = SC1_LOADS(l2off[2 * f], o2p); x[2 * f + 1] = SC1_LOADS(l2off[2 * f + 1], o2p);
        __builtin_amdgcn_sched_barrier(0);
      }
      if (first) lds_inc<L_MISC + 92>(lds0);
      if (first) { if (w >= 2) prep(s); else prep2(s); }
      if (w >= 2 && first && P2_W) lds_spin<L_MISC + 92, 0>(lds0, 4u * (unsigned)s);
      if (w == 2 && first) tk.store(rs, o4, s > LAG3 ? 0u : 0x7ff00000u);
      if (w == 3 && first) t2.store(rs, t2o, o4);
      if (first && w < 2) {
        p1cur = p1v; offs = (offs - (unsigned)dl) & ~3u;
        PIN_V(p1cur); PIN_S(offs);
        tstar = tc0 + (unsigned)(e1 + D1_M + (w == 1 ? D1_W1 : 0)) - offs * 20u;
        dmin = offs < 40u ? D1_MIN : REC_D1;
        dmin = s < EARLY_N ? REC_D1 + EARLY_D1 : dmin;
        dmax = REC_D1;
        PIN_S(tstar); PIN_S(dmin); PIN_S(dmax);
      }
      __builtin_amdgcn_sched_barrier(0);
      unsigned ok = 32u;
#pragma unroll
      for (int t = 0; t < 3; ++t) { acc[t] = Z4; PIN_V(acc[t]); }
      __builtin_amdgcn_sched_barrier(0);
      bool r = true;
#pragma unroll
      for (int f = 0; f < NC; ++f) {
        unsigned p[4];
#pragma unroll
        for (int k = 0; k < 2; ++k) {
          const u32x4 v = x[2 * f + k];
          p[2 * k] = pack2((float)(int)v[0], (float)(int)v[1]); p[2 * k + 1] = pack2((float)(int)v[2], (float)(int)v[3]);
          asm volatile("" : "+v"(p[2 * k]), "+v"(p[2 * k + 1]));
          __builtin_amdgcn_sched_barrier(0);
        }
        ok = (ok & x[2 * f][0] & x[2 * f][2]) & (x[2 * f + 1][0] & x[2 * f + 1][2]);
        PIN_V(ok);
        __builtin_amdgcn_sched_barrier(0);
        bm[f] = mk8(p[0], p[1], p[2], p[3]);
        acc[0] = smfma64(bm[f], Wqk[0][f], acc[0], sidx);
        __builtin_amdgcn_sched_barrier(0);
        bm_put(f);
        acc[1] = smfma64(bm[f], Wqk[1][f], acc[1], sidx);
        __builtin_amdgcn_sched_barrier(0);
        if (f == NC - 1) { u64 bad = __builtin_amdgcn_ballot_w64(ok == 0u); PIN_S(bad); r = bad == 0ull; }
        __builtin_amdgcn_sched_barrier(0);
        acc[2] = smfma64(bm[f], Wqk[2][f], acc[2], sidx);
        __builtin_amdgcn_sched_barrier(0);
      }
      red_put();
      return r;
    };
    if (__builtin_expect(!pass(true), 0)) {
      FC(0);
      do {
        if (dead || ++spins > SPIN_CAP) { dead = true; break; }
        do tg += RETRY_T; while ((int)(now32() - tg) > RETRY_L);
        wait_until(tg);
        if (w < 2) { tc0 = cyc32(); e1 += 24; tstar = tc0 + (unsigned)(e1 + D1_M + RETRY_D); }
        asm volatile("" ::: "memory");
      } while (!pass(false));
    }
    STAMPV(12, spins); LOGF(1, spins);
    TSTAMP(2, now32()); TSTAMP(3, spins);
  };
  if (w >= 2) prep0(-1);
  prep(0);
  if (w < 2) prep2(0);
  CODE_PAD((PADR >> (4 * w)) & 15);
#pragma unroll 1
  for (int s = 0;; ++s) {
    if (__builtin_expect(o2p != 0u, 1)) rows(s);
    if (w < 2 && __builtin_expect(s == NSEG - 1, 0)) break;
    STAMP(2);
    BAR();
    STAMP(3);
    if (w < 2) pub1(p1cur, w == 0 ? o1m : o1);
    if (w == 2) tk.load(rs, o3p);
    if (w == 3) pv2.load(rs, o3p);
    const unsigned tp1 = cyc32();
    __builtin_amdgcn_sched_barrier(0);
    if (w == 2) {
      __builtin_amdgcn_s_setprio(W2_P);
      prep0(s);
      if (W2_D) spin_cyc(tp1 + W2_D);
      fr = qk_frag(lds + qo4, lds + qo2, lds + qo2b);
      t2_mem(fr, qx[0], qx[1], rs, o3, h, j, invv * KEXP, lane);
      __builtin_amdgcn_sched_barrier(0);
      asm volatile("s_waitcnt vmcnt(1)" ::: "memory");
      lds_inc<L_MISC + 28>(lds0);
      kq_get(s + 1);
    }
    if (w == 3) {
      __builtin_amdgcn_s_setprio(T4_P);
      prep0(s);
      t2.get(lds, s + 1, lane); t2.run(0); t2.run(1);
      __builtin_amdgcn_sched_barrier(0);
    }
    if (w >= 2 && __builtin_expect(s == NSEG - 2, 0)) { VM_DRAIN(); lds_inc<L_MISC + 28>(lds0); }
    if (w >= 2) __builtin_amdgcn_s_setprio(UN_P);
    TSTAMP(0, now32());
    STAMP(4);
    __builtin_amdgcn_sched_barrier(0);
    unsigned tpub = 0;
    u32x2 sw;
    if (w == 2) {
      STAMP(5);
      __builtin_amdgcn_sched_barrier(0);
      STAMP(6);
      ARRIVE(0);
      tk.run(j, lane, SelLo);
      if (__builtin_expect(s == NSEG - 1, 0)) { tlast = tp1; break; }
      if (F2B) { lds_spin<L_MISC + 48, 1>(lds0, 2u * (unsigned)(s + 1)); x_fetch_n<F2B>(xsrc, lds0, s + 3, P2B, lane * 16); }
      __builtin_amdgcn_s_setprio(SP_P);
    }
    if (w == 3) {
      pv2.run<false>(lds, sv < 1 + LAG4 ? sv + NV - 1 - LAG4 : sv - 1 - LAG4, lane, rs, 0u);
      if (__builtin_expect(s == NSEG - 1, 0)) { tlast = tp1; break; }
      if (ST_E) lds_inc<L_MISC + 112>(lds0);
      STAMP(11);
      __builtin_amdgcn_s_setprio(SP_P);
      STAMP(5);
      ARRIVE(0);
      if (F3) { lds_spin<L_MISC + 48, 1>(lds0, 2u * (unsigned)(s + 1)); x_fetch_n<F3>(xsrc, lds0, s + 3, 40 - F3C - F3, lane * 16); }
      STAMP(6);
    }
    if (w < 2) {
      __builtin_amdgcn_s_setprio(D1_P);
      x_fetch_n<F0A>(xsrc, lds0, s + 3, PA + w * F0A, lane * 16);
      int dq = imin((int)(tstar - tp1), dmax);
      dq = dq < -D1_LATE ? dmax : dq;
      PIN_S(dq);
      spin_exact(tp1 + (unsigned)imax(dq, dmin));
      STAMP(5);
      u32x32 S;
      auto sc_load = [&]() {
        asm volatile("buffer_load_dwordx4 v[224:227], %2, %4, %5 offen sc1\n\t.rept %7\n\ts_nop 3\n\t.endr\n\t"
                     "buffer_load_dwordx4 v[228:231], %3, %4, %6 offen sc1\n\tbuffer_load_dwordx4 v[232:235], %3, %4, %6 offen offset:2048 sc1\n\t"
                     "buffer_load_dwordx4 v[236:239], %3, %4, %6 offen offset:256 sc1\n\tbuffer_load_dwordx4 v[240:243], %3, %4, %6 offen offset:2304 sc1\n\t"
                     "v_mov_b32 v220, 0x42000000\n\tv_mov_b32 v221, 0\n\tv_mov_b32 v222, 0x42000000\n\tv_mov_b32 v223, 0"
                     : "=&{v[224:255]}"(S), "+{v[208:223]}"(P) : "v"(l1offm), "v"(l1off), "s"(rs), "s"(o1m), "s"(o1), "n"(P1_G) : "memory");
      };
      sc_load();
      __builtin_amdgcn_sched_barrier(0);
      lds_inc<L_MISC + 48>(lds0);
      STAMP(6);
      ARRIVE(0);
      lds_spin<L_MISC + 28, 0>(lds0, need2);
      __builtin_amdgcn_s_setprio(3);
      asm volatile("ds_read_b64 v[192:193], %3\n\tds_read_b64 v[196:197], %3 offset:768\n\tds_read_b64 v[200:201], %4\n\tds_read_b64 v[204:205], %4 offset:768\n\tds_read_b64 v[184:185], %2"
                   : "+{v[184:191]}"(V8), "+{v[192:207]}"(V) : "v"(vma[0]), "v"(vma[1]), "v"(vma[2]) : "memory");
      __builtin_amdgcn_sched_barrier(0);
      {
        const int de = imin((int)(tp1 - tc0) - e1, D1_C);
        e1 += s == 0 ? 0 : de >> (s < EARLY_N && de < -D1_C ? 0 : D1_G);
        PIN_S(e1);
      }
      STAMP(11);
      __builtin_amdgcn_sched_barrier(0);
      {
        unsigned spins = 0;
        u64 miss, bad;
#define MM(d, a, b, c) "v_mfma_f32_16x16x32_bf16 " d ", " a ", " b ", " c "\n\t"
#define FM(d, x, r, ctrl) "v_fmac_f32_dpp " d ", " x ", " r " " ctrl "\n\t"
#define ID_ "quad_perm:[0,1,2,3] row_mask:0xf bank_mask:"
#define HM_ "row_half_mirror row_mask:0xf bank_mask:"
#define R8_ "row_ror:8 row_mask:0xf bank_mask:"
#define MI_ "row_mirror row_mask:0xf bank_mask:"
        auto soft = [&]() {
          asm volatile(
            "s_waitcnt vmcnt(4)\n\t" MM("v[244:247]", "%6", "v[224:227]", "0") "v_min3_u32 v214, v224, v225, v226\n\t"
            "s_waitcnt vmcnt(3)\n\t" MM("v[248:251]", "%6", "v[228:231]", "0") "v_min3_u32 v214, v214, v227, v228\n\tv_min3_u32 v214, v214, v229, v230\n\t"
            "s_waitcnt vmcnt(2)\n\t" MM("v[248:251]", "%6", "v[232:235]", "v[248:251]") "v_min3_u32 v214, v214, v231, v232\n\t"
            "v_add_f32_dpp v212, v244, v244 " R8_ "0x3\n\tv_add_f32_dpp v213, v245, v245 " R8_ "0x3\n\t"
            "s_waitcnt vmcnt(1)\n\t" MM("v[248:251]", "%7", "v[236:239]", "v[248:251]")
            "v_add_f32_dpp v212, v246, v246 " R8_ "0xc\n\tv_add_f32_dpp v213, v247, v247 " R8_ "0xc\n\t"
            "s_waitcnt vmcnt(0) lgkmcnt(0)\n\t" MM("v[248:251]", "%7", "v[240:243]", "v[248:251]")
            "v_fma_f32 v212, v212, %14, %15\n\tv_fma_f32 v213, v213, %14, %15\n\tv_exp_f32 v216, v212\n\tv_exp_f32 v217, v213\n\t"
            "v_min3_u32 v214, v214, v233, v234\n\tv_cvt_pk_bf16_f32 v208, v216, v217\n\tv_min3_u32 v214, v214, v235, v236\n\tds_read_b96 v[188:190], %16 offset:%18\n\t"
            "v_exp_f32 v216, v248\n\tv_mov_b32_dpp v209, v208 " R8_ "0xf\n\tv_exp_f32 v217, v249\n\tv_exp_f32 v218, v250\n\t"
            MM("v[252:255]", "%8", "v[208:211]", "0") "v_exp_f32 v219, v251\n\tv_cvt_pk_bf16_f32 v232, v216, v217\n\t"
            MM("v[224:227]", "v[192:195]", "v[208:211]", "0") "v_cvt_pk_bf16_f32 v233, v218, v219\n\tv_min3_u32 v214, v214, v237, v238\n\tv_mov_b32_dpp v234, v232 " R8_ "0xf\n\t"
            MM("v[228:231]", "v[196:199]", "v[208:211]", "0") "v_mov_b32_dpp v235, v233 " R8_ "0xf\n\t"
            MM("v[244:247]", "v[204:207]", "v[208:211]", "0") "v_min3_u32 v214, v214, v239, v240\n\t"
            MM("v[252:255]", "%8", "v[232:235]", "v[252:255]") MM("v[224:227]", "%10", "v[232:235]", "v[224:227]") "v_min3_u32 v214, v214, v241, v242\n\t"
            MM("v[228:231]", "%11", "v[232:235]", "v[228:231]") "s_waitcnt lgkmcnt(0)\n\tv_sub_u32 v215, v190, %17 clamp\n\t"
            MM("v[236:239]", "v[200:203]", "v[208:211]", "0") "v_min3_u32 v214, v214, v243, v215\n\tv_cmp_eq_u32 %4, 0, v214\n\tv_rcp_f32 v216, v252\n\t"
            MM("v[240:243]", "v[184:187]", "v[208:211]", "0") "v_mul_f32 v216, v216, v188\n\tv_subrev_u32 v215, 0x2f800000, v252\n\t"
            MM("v[236:239]", "%12", "v[232:235]", "v[236:239]") FM("v220", "v224", "v216", ID_ "0x1") FM("v221", "v225", "v216", ID_ "0x1")
            "v_mov_b32_dpp v217, v216 " HM_ "0xf\n\t"
            MM("v[244:247]", "%13", "v[232:235]", "v[244:247]") FM("v220", "v230", "v216", ID_ "0x2") FM("v221", "v231", "v216", ID_ "0x2")
            MM("v[240:243]", "%9", "v[232:235]", "v[240:243]") FM("v220", "v228", "v217", HM_ "0x1") FM("v221", "v229", "v217", HM_ "0x1")
            "v_cmp_ge_u32 vcc, v215, v189\n\t" FM("v220", "v226", "v217", HM_ "0x2") FM("v221", "v227", "v217", HM_ "0x2")
            "s_or_b64 %5, %4, vcc"
            : "+{v[224:255]}"(S), "+{v[208:223]}"(P), "+{v[184:191]}"(V8), "+{v[192:207]}"(V), "=&s"(miss), "=&s"(bad)
            : "v"(slo), "v"(shi), "v"(ONES), "v"(vox[0]), "v"(vox[1]), "v"(vox[2]), "v"(vox[3]), "v"(vox[4]), "v"(k1), "v"(minf), "v"(lds0), "s"(s), "n"(L_MISC + 32)
            : "vcc", "scc", "memory");
        };
        soft();
        if (__builtin_expect(bad != 0ull, 0)) {
          if (miss) FC(1); else FC(2);
          while (miss != 0ull && !dead) {
            if (++spins > SPIN_CAP) dead = true;
            asm volatile("s_nop 7\n\ts_nop 7" ::: "memory");
            sc_load(); soft();
          }
          if (bad != 0ull) {
            u32x4 y[5];
#pragma unroll
            for (int k = 0; k < 4; ++k) y[k] = SC1_LOADS(l1off + (k & 1) * 2048 + (k >> 1) * 256, o1);
            y[4] = SC1_LOADS(l1offm, o1m);
            const f32x4 d0 = mfma32(as8(slo), as8(y[4]), Z4);
            f32x4 d1 = mfma32(as8(shi), as8(y[3]), mfma32(as8(shi), as8(y[2]), mfma32(as8(slo), as8(y[1]), mfma32(as8(slo), as8(y[0]), Z4))));
            const bool lo = c < 8;
            const float m0 = __builtin_fmaf((lo ? d0[0] : d0[2]) + dppf<DPP_ROR8>(lo ? d0[2] : d0[0]), k1, minf);
            const float m1 = __builtin_fmaf((lo ? d0[1] : d0[3]) + dppf<DPP_ROR8>(lo ? d0[3] : d0[1]), k1, minf);
            float mx = fmx(fmx(fmx(d1[0], d1[1]), d1[2]), fmx(fmx(d1[3], m0), m1));
            mx = fmx(mx, dppf<DPP_ROR8>(mx));
            mx = x32mx(x16mx(mx));
            d1 -= mx;
            const unsigned mm = pack2(__builtin_amdgcn_exp2f(m0 - mx), __builtin_amdgcn_exp2f(m1 - mx));
            const unsigned t01 = pack2(__builtin_amdgcn_exp2f(d1[0]), __builtin_amdgcn_exp2f(d1[1])), t23 = pack2(__builtin_amdgcn_exp2f(d1[2]), __builtin_amdgcn_exp2f(d1[3]));
            const bf16x8 pm = mk8(mm, dpp_ror8(mm), 0u, 0u), pt = mk8(t01, t23, dpp_ror8(t01), dpp_ror8(t23));
            const float r = __builtin_amdgcn_rcpf(mfma32(ONES, pt, mfma32(ONES, pm, Z4))[0]) * u2f(V8[4]), rm = dppf<0x141>(r);
            f32x4 o[5];
#pragma unroll
            for (int k = 0; k < 5; ++k) o[k] = mfma32(vox[k], pt, mfma32(k ? mk8(V[4 * k - 4], V[4 * k - 3], 0u, 0u) : mk8(V8[0], V8[1], 0u, 0u), pm, Z4));
#pragma unroll
            for (int i = 0; i < 2; ++i) {
              const float oa = dppf<0x141>(o[2][i]), ob = dppf<0x141>(o[1][2 + i]);
              P[12 + i] = f2u(__builtin_fmaf(hd ? ob : oa, rm, __builtin_fmaf(hd ? o[2][2 + i] : o[1][i], r, i ? 0.f : 32.0f)));
            }
            P[8] = f2u(r); P[9] = f2u(rm);
#pragma unroll
            for (int i = 0; i < 4; ++i) { S[12 + i] = f2u(o[3][i]); S[16 + i] = f2u(o[0][i]); S[20 + i] = f2u(o[4][i]); }
            asm volatile("s_nop 7\n\ts_nop 7" : "+{v[224:255]}"(S), "+{v[208:223]}"(P));
          }
        }
        STAMP(7); STAMPV(13, spins); LOGF(0, spins);
        {
          asm volatile("v_cvt_i32_f32 v218, v220\n\tv_cvt_i32_f32 v219, v221\n\tv_and_or_b32 v218, v218, %7, 2\n\tbuffer_atomic_add_x2 v[218:219], %3, %5, %6 offen\n\t"
                       FM("v222", "v236", "v216", R8_ "0x4") FM("v223", "v237", "v216", R8_ "0x4") "ds_read_b64 %2, %8 offset:%9\n\t"
                       FM("v222", "v238", "v217", MI_ "0x8") FM("v223", "v239", "v217", MI_ "0x8") "v_permlane32_swap_b32 v240, v242\n\t"
                       FM("v222", "v244", "v217", MI_ "0x4") FM("v223", "v245", "v217", MI_ "0x4") "v_permlane32_swap_b32 v241, v243\n\t"
                       FM("v222", "v246", "v216", R8_ "0x8") FM("v223", "v247", "v216", R8_ "0x8")
                       FM("v222", "v240", "v216", ID_ "0x1") FM("v223", "v241", "v216", ID_ "0x1") FM("v222", "v242", "v217", HM_ "0x1") FM("v223", "v243", "v217", HM_ "0x1")
                       "v_cvt_i32_f32 v212, v222\n\tv_cvt_i32_f32 v213, v223\n\tv_and_or_b32 v212, v212, %7, 2\n\tbuffer_atomic_add_x2 v[212:213], %4, %5, %6 offen\n\t"
                       "s_waitcnt lgkmcnt(0)"
                       : "+{v[224:255]}"(S), "+{v[208:223]}"(P), "=&v"(sw) : "v"(acco), "v"(acco2), "s"(rs), "s"(o2), "s"(~63u), "v"(lds0), "n"(L_MISC + 96) : "memory");
          __builtin_amdgcn_sched_barrier(0);
          tpub = now32();
          lds_inc<L_MISC + 104 + 4 * (w & 1)>(lds0);
          asm volatile("buffer_atomic_umax %0, %1, %2, %3 offen" :: "v"(tpub | 1u), "v"(tso), "s"(rs), "s"(o2) : "memory");
          STAMP(10);
        }
      }
    }
    STAMP(8);
    ARRIVE(1);
    o2p = o2;
    PIN_S(o2p);
    if (w < 2) {
      if (__builtin_expect(__builtin_amdgcn_readfirstlane(sw[1]) <= (unsigned)s, 0)) {
        lds_spin<L_MISC + 100, 0>(lds0, (unsigned)s + 1u);
        sw[0] = __hip_atomic_load((unsigned*)(misc + 24), __ATOMIC_RELAXED, __HIP_MEMORY_SCOPE_WORKGROUP);
      }
    } else {
      u32x4 q;
      asm volatile("1:\n\tds_read_b128 %0, %1 offset:%3\n\t.if %4\n\ts_sleep 1\n\t.endif\n\ts_waitcnt lgkmcnt(0)\n\tv_min3_u32 v201, v201, v202, v203\n\tv_cmp_ge_u32 vcc, %2, v201\n\ts_cbranch_vccnz 1b"
                   : "=&{v[200:203]}"(q) : "v"(lds0), "s"(s), "n"(L_MISC + 96), "n"(SP_SL) : "vcc", "memory");
      __builtin_amdgcn_s_setprio(3);
      tpub = now32();
      sw[0] = q[0];
    }
    STAMP(9);
    static_assert(F3C == 0 && F2C == 0 && F0C == 0, "nothing behind the atomics");
    LOGT(0, tpub); LOGT(1, tb2_);
    TSTAMP(1, tpub);
    ARRIVE_OUT();
    const unsigned an = __builtin_amdgcn_readfirstlane(sw[0]);
    const int ahead = (int)(an - tpub);
    tw = tpub + (unsigned)imax(ahead, w < 2 ? SCH_DMIN : 0);
    offs = tw - an;
    tg = an;
    if (__builtin_expect((unsigned)(ahead + (w < 2 ? SCH_BX : SCH_BX2)) > 1000u, 0)) {
      tw = tg = ahead > 0 || ahead < -20000 ? tpub + SCH_FIRST : an + (unsigned)(RETRY_G - (w < 2 ? 0 : GO_T) - ahead + RETRY_T - 1) / RETRY_T * RETRY_T;
      offs = 0u;
    }
  }
  STAMP_OUT();
  LOGP(4);
  __builtin_amdgcn_s_setprio(3);
  if (w < 2) { BAR(); tlast = cyc32(); }
  if (w == 2) tk.store(rs, o4);
  if (w == 3) {
    const unsigned oo = op_slot(NSEG - 2 - LAG4) + (unsigned)((m * 32 + gp) * 1280);
    const char* sp = lds + L_STG + 2048;
    const u32x4 v0 = *(const u32x4*)(sp + lane * 16), v1 = *(const u32x4*)(sp + 1280 + lane * 16), v2 = *(const u32x4*)(sp + (g & 1) * 1280 + 1024 + c * 16);
    SC1_STORE(v0, oo + lane * 16); SC1_STORE(v1, oo + 20480 + lane * 16); SC1_STORE(v2, lane < 32 ? oo + g * 20480 + 1024 + c * 16 : 0x7ffffff0u);
  }
  if (dead) tlast |= 1u; else tlast &= ~1u;
}

__device__ __forceinline__ void drain(const RecArgs& a, char* lds, const __amdgpu_buffer_rsrc_t rs, const int w, const unsigned tp1) {
  static_assert(LAG3 == 2 && LAG4 == 3 && LAG5 == 6, "drain schedule");
  const int lane = __builtin_amdgcn_workitem_id_x() & 63, b = __builtin_amdgcn_workgroup_id_x(), c = lane & 15;
  const int gp = (b & 7) * 2 + (b >> 7), m = (b >> 3) & 15, j = m & 7;
  unsigned* const misc = (unsigned*)(lds + L_MISC);
  const unsigned lds0 = (unsigned)(uintptr_t)(LAS char*)lds, ck = __builtin_amdgcn_readfirstlane(misc[11]);
  bool dead = tp1 & 1u;
  auto poll = [&](auto load, auto ready) {
    for (unsigned spins = 0;;) {
      asm volatile("" ::: "memory");
      load();
      if (ready() || dead) break;
      if (++spins > SPIN_CAP) dead = true;
      if (END_SL) __builtin_amdgcn_s_sleep(END_SL);
    }
  };
  LOGD(0);
#define TASK(ty, sg, tm) ((u64)((ty) | ((sg) - 56) << 2 | ((tm) / 100) << 5))
  u64 todo = w == 0 ? TASK(1, 63, END_E1) | TASK(2, 63, END_E2) << 12 | TASK(3, 62, END_E3) << 24
           : w == 1 ? TASK(1, 62, 0) | TASK(2, 62, END_E62) << 12 | TASK(3, 60, END_E3) << 24
           : w == 2 ? TASK(2, 60, 0) | TASK(3, 56, 0) << 12 : TASK(2, 61, END_E61) | TASK(3, 58, END_E59) << 12;
  TokTask tk;
  PvTask2 pv;
  OutTask ot;
  {
    const u32x4* p = (const u32x4*)(lds + L_DRN) + lane;
    const u32x4 q0 = p[0], q1 = p[64], q2 = p[128];
    tk.l0 = q0[0]; tk.l1 = q0[0] + 16384u; tk.lm = q0[1]; tk.s0 = q0[2]; tk.s1 = q0[3];
    pv.lo = q1[0]; pv.lm = q1[1]; pv.vr = q1[2]; pv.ma = q1[3]; pv.mz = q2[0]; pv.so = q2[1];
    pv.pa[0] = pv.pa[1] = pv.pb[0] = pv.pb[1] = (u32x4){0u, 0u, 0u, 0u};
    ot.t5f = q2[2]; ot.t5o = q2[3]; ot.sel = as8(p[192]);
  }
#pragma unroll 1
  for (int ti = 0; todo; todo >>= 12, ++ti) {
    const int ty = (int)todo & 3, sl = 56 + (((int)todo >> 2) & 7);
    const unsigned tm = tp1 + (((unsigned)todo >> 5) & 0x7fu) * 100u;
    if (ty == 1) {
      wait_cyc(tm);
      LOGD(3 * ti + 1);
      poll([&]() { tk.load(rs, ts_slot(ck, sl)); }, [&]() { return tk.ready(); });
      LOGD(3 * ti + 2);
      tk.run(j, lane, ot.sel); tk.store(rs, pt_slot(ck, sl));
    } else if (ty == 2) {
      const unsigned t0 = cyc32();
      if (sl >= NSEG - 2) { lds_wait(misc + 7, 4u * NSEG + 3u); wait_cyc(t0 + END_G2); }
      wait_cyc(tm);
      LOGD(3 * ti + 1);
      poll([&]() { pv.load(rs, pt_slot(ck, sl)); }, [&]() { return pv.ready(); });
      LOGD(3 * ti + 2);
      pv.run<true>(lds, sl % NV, lane, rs, op_slot(sl) + (unsigned)((m * 32 + gp) * 1280));
      if (sl == NSEG - 1) misc[14] = cyc32() | 1u;
    } else {
      if (sl >= NSEG - 4) { lds_wait(misc + 14, 1u); wait_cyc(__builtin_amdgcn_readfirstlane(misc[14]) + END_G3); }
      wait_cyc(tm);
      LOGD(3 * ti + 1);
      OutTask o2 = ot;
      poll([&]() { o2.load(a, rs, sl + 1, b); ot.load(a, rs, sl, b); }, [&]() { return ot.ready(b) & o2.ready(b); });
      LOGD(3 * ti + 2);
      o2.run(lane); o2.store(a, sl + 1, b, lane);
      ot.run(lane); ot.store(a, sl, b, lane);
    }
    LOGD(3 * ti + 3);
  }
  VM_DRAIN();
  if (w != 0) lds_inc<L_MISC + 64>(lds0);
  else {
    constexpr unsigned o2 = (unsigned)OFF_ACC + (unsigned)((NSEG - 1) % SR) * (unsigned)ACC_STEP;
    lds_wait(misc + 16, 3u);
    LOGD(10);
    if (lane == 0) __hip_atomic_fetch_add((gu64*)((GAS char*)(a.ws + o2) + acc_off(m * 4, 0)), (u64)1, RLX_AGENT);
    wait_until(now32() + SCH_DRAIN);
    wait_cyc(tp1 + END_E4);
    unsigned x;
    if (qkv_row(b) + 48 >= 55 && qkv_col(b) < 15) poll([&]() { x = __builtin_amdgcn_raw_buffer_load_b32(rs, acc_off(c * 4, 0), o2, 16); }, [&]() { return wave_all((x & 16u) != 0u); });
    lds_inc<L_MISC + 68>(lds0);
    LOGD(11);
  }
  if (qkv_row(b) + 48 >= 55 && qkv_col(b) < 15) {
    lds_wait(misc + 18, 8u);
    lds_spin<L_MISC + 68, 1>(lds0, 1u);
    x_fetch_n<10>(x_src(a, NSEG + 3, b), lds0, 3, 10 * w, lane * 16);
    VM_DRAIN();
    lds_inc<L_MISC + 68>(lds0);
  }
  LOGP(8);
}

__device__ __forceinline__ void proj_front(const RecArgs& a, char* lds, const __amdgpu_buffer_rsrc_t rs, const int v) {
  const int lane = __builtin_amdgcn_workitem_id_x() & 63, b = __builtin_amdgcn_workgroup_id_x(), c = lane & 15, g = lane >> 4;
  const int rb = (b & 7) + 8 * (b >> 5), cq = (b >> 3) & 3;
  const unsigned lds0 = (unsigned)(uintptr_t)(LAS char*)lds;
  unsigned* const cnt = (unsigned*)(lds + L_MISC) + 20;
  LOGF2(0);
  float* const red = (float*)(lds + L_RED1);
  float* const fin = red + 640;
  const f32x4 Z4 = {0.f, 0.f, 0.f, 0.f};
#pragma unroll
  for (int i = 0; i < 8; ++i) x_fetch((const u16*)(a.ws + OFF_OB) + (size_t)rb * 32 * DM, lds0, 0, 8 * v + i, lane);
  bf16x8 W[2][20];
#pragma unroll
  for (int k = 0; k < 40; ++k) W[k & 1][k >> 1] = *(const bf16x8*)(a.ws + OFF_WOBT + ((size_t)(cq * 10 + 2 * v + (k & 1)) * 20 + (k >> 1)) * 1024 + lane * 16);
  const unsigned fo = (unsigned)(rb * 40960 + (cq * 5 + v) * 1024 + ((g >> 1) * 16 + c) * 16), f8 = fo + (g & 1) * 8, f16 = fo + (g & 1) * 20480;
  u32x2 xo[2][2];
#pragma unroll
  for (int k = 0; k < 4; ++k)
    xo[k >> 1][k & 1] = __builtin_bit_cast(u32x2, __hip_atomic_load((const gu64*)((const char*)a.xnew + f8 + (k >> 1) * 512 + (k & 1) * 20480), RLX_AGENT));
  asm volatile("s_waitcnt vmcnt(44)" ::: "memory");
  lds_inc<L_MISC + 80>(lds0);
  lds_wait(cnt, 5u);
  LOGF2(1);
  f32x4 acc[2][2] = {{Z4, Z4}, {Z4, Z4}};
#pragma unroll
  for (int ks = 0; ks < 20; ++ks)
#pragma unroll
    for (int t = 0; t < 2; ++t) {
      const bf16x8 x = *(const bf16x8*)(lds + L_XN + (t * 20 + ks) * 1024 + lane * 16);
#pragma unroll
      for (int i = 0; i < 2; ++i) acc[i][t] = mfma32(W[i][ks], x, acc[i][t]);
    }
  u32x2 xp[2][2];
  PIN_V(acc[1][1]);
  LOGF2(2);
#pragma unroll
  for (int k = 0; k < 4; ++k) {
    const int i = k >> 1, t = k & 1;
#pragma unroll
    for (int q = 0; q < 2; ++q) {
      const unsigned r = pack2(acc[i][t][2 * q], acc[i][t][2 * q + 1]), x = xo[i][t][q];
      xp[i][t][q] = pack2(u2f(x << 16) + u2f(r << 16), u2f(x & 0xffff0000u) + u2f(r & 0xffff0000u));
      acc[i][t][2 * q] = u2f(xp[i][t][q] << 16); acc[i][t][2 * q + 1] = u2f(xp[i][t][q] & 0xffff0000u);
    }
    const float ml = x32sum(x16sum((acc[i][t][0] + acc[i][t][1]) + (acc[i][t][2] + acc[i][t][3]))) * (1.0f / 16.0f);
    float q = 0.f;
#pragma unroll
    for (int r = 0; r < 4; ++r) { const float d = acc[i][t][r] - ml; q += d * d; }
    q = x32sum(x16sum(q));
    if (g == 0) { red[(2 * v + i) * 32 + t * 16 + c] = ml; red[320 + (2 * v + i) * 32 + t * 16 + c] = q; }
  }
  lds_inc<L_MISC + 80>(lds0);
  if (v == 0) {
    lds_wait(cnt, 10u);
    LOGF2(3);
    float mw[10], ml = 0.f, m2 = 0.f;
#pragma unroll
    for (int i = 0; i < 10; ++i) { mw[i] = red[i * 32 + (lane & 31)]; ml += mw[i]; m2 += red[320 + i * 32 + (lane & 31)]; }
    ml *= 0.1f;
#pragma unroll
    for (int i = 0; i < 10; ++i) m2 += 16.0f * (mw[i] - ml) * (mw[i] - ml);
    const unsigned xs = (unsigned)OFF_LNX + rb * 1024;
    if (lane < 32) SC1_STORE8S(((u32x2){f2u(ml), nz(f2u(m2))}), (cq * 32 + lane) * 8, xs);
    wait_cyc(cyc32() + FR_D);
    u32x2 x0, x1;
    for (unsigned spins = 0; spins < SPIN_CAP; ++spins) {
      x0 = __builtin_amdgcn_raw_buffer_load_b64(rs, lane * 8, xs, 16); x1 = __builtin_amdgcn_raw_buffer_load_b64(rs, 512 + lane * 8, xs, 16);
      if (wave_all(x0[1] != 0u && x1[1] != 0u)) break;
      asm volatile("" ::: "memory");
    }
    const float m0 = u2f(x0[0]), m1 = u2f(x1[0]);
    const float mean = x32sum(m0 + m1) * 0.25f;
    const float M2 = x32sum((u2f(x0[1]) + u2f(x1[1])) + 160.0f * ((m0 - mean) * (m0 - mean) + (m1 - mean) * (m1 - mean)));
    const float mb = rbf(mean), den = rbf(__builtin_sqrtf(rbf(rbf(M2 * (1.0f / DM)) + 1e-5f)));
    if (lane < 32) {
      fin[lane] = mb; fin[32 + lane] = 1.0f / den;
      if (cq == 0) __hip_atomic_store((GAS unsigned*)(a.ws + OFF_XNRM) + (rb * 32 + lane) * 4, f2u(1.02f * (M2 + DM * (mean - mb) * (mean - mb)) / (den * den)), RLX_AGENT);
    }
    lds_inc<L_MISC + 84>(lds0);
  }
  lds_wait(cnt + 1, 1u);
  LOGF2(4);
  const __amdgpu_buffer_rsrc_t rx = __builtin_amdgcn_make_buffer_rsrc(a.ws + OFF_XN, 0, (int)(OFF_QB - OFF_XN), 0x00020000);
  auto put = [&](const u32x2 p0, const u32x2 p1, unsigned so) {
    const auto r0 = __builtin_amdgcn_permlane16_swap(p0[0], p1[0], false, false), r1 = __builtin_amdgcn_permlane16_swap(p0[1], p1[1], false, false);
    __builtin_amdgcn_raw_buffer_store_b128((u32x4){r0[0], r1[0], r0[1], r1[1]}, rx, f16, so, 16);
  };
#pragma unroll
  for (int i = 0; i < 2; ++i) {
    u32x2 y[2];
#pragma unroll
    for (int t = 0; t < 2; ++t) {
      const float mb = fin[t * 16 + c], rden = fin[32 + t * 16 + c];
      y[t] = (u32x2){pack2(rbf(acc[i][t][0] - mb) * rden, rbf(acc[i][t][1] - mb) * rden), pack2(rbf(acc[i][t][2] - mb) * rden, rbf(acc[i][t][3] - mb) * rden)};
    }
    put(y[0], y[1], i * 512);
  }
  VM_DRAIN();
  lds_inc<L_MISC + 80>(lds0);
#pragma unroll
  for (int i = 0; i < 2; ++i) put(xp[i][0], xp[i][1], (unsigned)(OFF_XB - OFF_XN) + i * 512);
  if (v == 0) {
    lds_wait(cnt, 15u);
    if (rb < 4 && lane < 8) __hip_atomic_fetch_add((GAS unsigned*)(a.ws + OFF_RDY) + lane * 64, 1u, RLX_AGENT);
  }
  LOGF2(5);
}

__global__ __launch_bounds__(512, 2) void rec_kernel(RecArgs a) {
  __shared__ __attribute__((aligned(16))) char lds[L_BYTES];
  const int tid = __builtin_amdgcn_workitem_id_x(), wid = __builtin_amdgcn_readfirstlane(tid >> 6), w = wid & 3;
  const __amdgpu_buffer_rsrc_t rs = __builtin_amdgcn_make_buffer_rsrc(a.ws, 0, (int)COMM_BYTES, 0x00020000);
  if (tid < 32) ((float*)(lds + L_MISC))[tid] = (tid == 4 || tid == 5) ? 1.0f : 0.0f;
  BAR();
  if (a.layer && wid < 5) proj_front(a, lds, rs, wid);
  if (wid < 4) {
    unsigned tlast;
    if (w == 0) rec_waves<0>(a, lds, rs, tlast);
    else if (w == 1) rec_waves<1>(a, lds, rs, tlast);
    else if (w == 2) rec_waves<2>(a, lds, rs, tlast);
    else rec_waves<3>(a, lds, rs, tlast);
    drain(a, lds, rs, w, tlast);
  } else {
    unsigned vo[2][2];
    if (w == 0) token_waves<0>(a, lds, rs, vo);
    else if (w == 1) token_waves<1>(a, lds, rs, vo);
    else if (w == 2) token_waves<2>(a, lds, rs, vo);
    else token_waves<3>(a, lds, rs, vo);
    qkv_tail(a, lds, w, vo);
  }
}

#if defined(__HIP_DEVICE_COMPILE__)
#pragma clang attribute pop
#endif

constexpr int KQ = S_ / 4;
#define ATT_P1 6
#define ATT_P2 10
#define ATT_P3 14
#define ATT_THR 6.0f
#define ATT_NQ 4
#define ATT_LMAX 1.0e12f
#if defined(__HIP_DEVICE_COMPILE__)
#pragma clang attribute push(__attribute__((target("no-packed-fp32-ops"))), apply_to = function)
#endif
__global__ __launch_bounds__(256 * ATT_NQ, 4) void attb_kernel(const u16* __restrict__ Qp, const u16* __restrict__ KV, u16* __restrict__ O) {
  __shared__ f32x4 mo_[ATT_NQ][4][2][3][64];
  __shared__ float mm_[ATT_NQ][4][2][64];
  const int tid = __builtin_amdgcn_workitem_id_x(), bid = __builtin_amdgcn_workgroup_id_x();
  __shared__ unsigned sink[64];
  own_code<5000>(tid, sink);
  const int lane = tid & 63, wid = __builtin_amdgcn_readfirstlane(tid >> 6), kh = wid / ATT_NQ, r = lane & 31, hh = lane >> 5;
  const int hd = (bid & 7) * 4 + ((bid >> 3) & 3), qb = (bid >> 5) * ATT_NQ + wid % ATT_NQ;
  const int q0 = qb * 64;
  bf16x8 qa[2]; u32x2 qb2[2];
#pragma unroll
  for (int qt = 0; qt < 2; ++qt) {
    { const u32x2* qp = (const u32x2*)(Qp + ((size_t)hd * S_ + q0 + qt * 32 + r) * QST + 8 * hh); const u32x2 q0_ = qp[0], q1_ = qp[1]; qa[qt] = mk8(q0_[0], q0_[1], q1_[0], q1_[1]); }
    const u32x2 t = *(const u32x2*)(Qp + ((size_t)hd * S_ + q0 + qt * 32 + r) * QST + 16);
    qb2[qt] = t;
  }
  const __amdgpu_buffer_rsrc_t rk = __builtin_amdgcn_make_buffer_rsrc((void*)(KV + ((size_t)hd * 64 + kh * 16) * (KVT / 2)), 0, 16 * KVT, 0x00020000);
  unsigned ok1 = lane * 16, ok2 = hh ? 1600 : 1024 + r * 8, ov = r > 20 ? 1u << 30 : 1280 + (hh * 21 + r) * 16;
  f32x16 o[2];
  float m[2];
  const f32x16 Z16 = {0.f, 0.f, 0.f, 0.f, 0.f, 0.f, 0.f, 0.f, 0.f, 0.f, 0.f, 0.f, 0.f, 0.f, 0.f, 0.f};
  int odd = 0xDDDD;
  asm volatile("" : "+v"(odd));
  struct KvT { u32x4 k1, v0, v1; u32x2 k2; };
  auto fetch_k = [&](KvT& x, int t) {
    x.k1 = __builtin_amdgcn_raw_buffer_load_b128(rk, ok1, imin(t, KQ / 32 - 1) * KVT, 0); x.k2 = __builtin_amdgcn_raw_buffer_load_b64(rk, ok2, imin(t, KQ / 32 - 1) * KVT, 0);
    __builtin_amdgcn_sched_barrier(0);
  };
  auto fetch_v = [&](KvT& x, int t) {
    x.v0 = __builtin_amdgcn_raw_buffer_load_b128(rk, ov, imin(t, KQ / 32 - 1) * KVT, 0); x.v1 = __builtin_amdgcn_raw_buffer_load_b128(rk, ov + 672, imin(t, KQ / 32 - 1) * KVT, 0);
    __builtin_amdgcn_sched_barrier(0);
  };
  auto fetch = [&](int t) { KvT x; __builtin_amdgcn_sched_barrier(0); fetch_k(x, t); fetch_v(x, t); return x; };
  auto qk1 = [&](const KvT& x, int qt) { return __builtin_amdgcn_mfma_f32_32x32x16_bf16(as8(x.k1), qa[qt], Z16, 0, 0, 0); };
  auto qk2 = [&](const KvT& x, int qt, f32x16 c) { return __builtin_amdgcn_mfma_f32_32x32x8bf16_1k(__builtin_bit_cast(v4i16_t, x.k2), __builtin_bit_cast(v4i16_t, qb2[qt]), c, 0, 0, 0); };
  auto pv = [&](const u32x4 v, const f32x16 p, int hi, f32x16 c) {
    const f32x8 ph = hi ? __builtin_shufflevector(p, p, 8, 9, 10, 11, 12, 13, 14, 15) : __builtin_shufflevector(p, p, 0, 1, 2, 3, 4, 5, 6, 7);
    return __builtin_amdgcn_smfmac_f32_32x32x32_bf16(__builtin_bit_cast(hbf16x8, v), __builtin_bit_cast(hbf16x16, ph), c, odd, 0, 0);
  };
  auto tile = [&](const KvT& x, auto MODE) {
    f32x16 sa[2];
#pragma unroll
    for (int qt = 0; qt < 2; ++qt) sa[qt] = qk1(x, qt);
#pragma unroll
    for (int qt = 0; qt < 2; ++qt) sa[qt] = qk2(x, qt, sa[qt]);
#pragma unroll
    for (int qt = 0; qt < 2; ++qt) {
      if (MODE != 0) {
        float mx = fmx(fmx(sa[qt][0], sa[qt][1]), sa[qt][2]);
#pragma unroll
        for (int i = 3; i < 15; i += 2) mx = fmx(fmx(mx, sa[qt][i]), sa[qt][i + 1]);
        mx = fmx(mx, sa[qt][15]);
        if (MODE == 1 || !wave_all(mx <= ATT_THR)) {
          mx = x32mx(mx);
          if (MODE != 1) mx = fmx(mx, 0.f);
          const float mn = m[qt] + mx;
          const unsigned h1 = f2bf(mn); const float r1 = mn - bf2f(h1);
          const unsigned h2 = f2bf(r1); const float r2 = r1 - bf2f(h2);
          const unsigned h3 = f2bf(r2);
          const float me = (bf2f(h1) + bf2f(h2)) + bf2f(h3);
          const float dm = me - m[qt];
          m[qt] = me;
          if (hh) qb2[qt] = (u32x2){(h1 | (h2 << 16)) ^ 0x80008000u, h3 ^ 0x8000u};
          const float alpha = __builtin_amdgcn_exp2f(-dm);
#pragma unroll
          for (int i = 0; i < 16; ++i) { if (MODE != 1) o[qt][i] *= alpha; sa[qt][i] -= dm; }
        }
      }
#pragma unroll
      for (int i = 0; i < 16; ++i) sa[qt][i] = __builtin_amdgcn_exp2f(sa[qt][i]);
      asm volatile("" : "+v"(sa[qt]));
      if (MODE == 1) { o[qt] = Z16; asm volatile("" : "+v"(o[qt])); }
      o[qt] = pv(x.v1, sa[qt], 1, pv(x.v0, sa[qt], 0, o[qt]));
      __builtin_amdgcn_sched_barrier(0);
    }
  };
  typedef std::integral_constant<int, 1> I1; typedef std::integral_constant<int, 2> I2;
  auto reset = [&]() {
    asm volatile("" : "+v"(ok1), "+v"(ok2), "+v"(ov) :: "memory");
    m[0] = m[1] = 0.f;
    if (hh) qb2[0] = qb2[1] = (u32x2){0u, 0u};
  };
  auto slow = [&]() {
    reset();
    tile(fetch(0), I1{});
#pragma unroll 1
    for (int t = 1; t < KQ / 32; ++t) tile(fetch(t), I2{});
  };
  f32x16 sc[2];
  auto phase = [&](int qt, const u32x4 vp, const KvT& xk, const u32x4 v0, auto mid) {
    auto ex = [&](int i0, int i1) {
#pragma unroll
      for (int i = i0; i < i1; ++i) sc[qt][i] = __builtin_amdgcn_exp2f(sc[qt][i]);
      asm volatile("" : "+v"(sc[qt]));
      __builtin_amdgcn_sched_barrier(0);
    };
    o[1 - qt] = pv(vp, sc[1 - qt], 1, o[1 - qt]);
    __builtin_amdgcn_sched_barrier(0);
    ex(0, 3);
    sc[1 - qt] = qk1(xk, 1 - qt);
    __builtin_amdgcn_sched_barrier(0);
    ex(3, 6);
    sc[1 - qt] = qk2(xk, 1 - qt, sc[1 - qt]);
    __builtin_amdgcn_sched_barrier(0);
    ex(6, 10);
    o[qt] = pv(v0, sc[qt], 0, o[qt]);
    __builtin_amdgcn_sched_barrier(0);
    mid();
    ex(10, 16);
  };
  auto fast = [&]() {
    reset();
    KvT xa = fetch(0), xb = fetch(1);
    tile(xa, I1{});
    fetch_k(xa, 2);
    sc[0] = qk2(xb, 0, qk1(xb, 0)); sc[1] = Z16;
    asm volatile("" : "+v"(sc[1]));
    auto step = [&](KvT& x, KvT& y, int t) {
      phase(0, y.v1, x, x.v0, [&]() { fetch_v(y, t + 1); fetch_k(x, t + 2); });
      phase(1, x.v1, y, x.v0, []() {});
    };
    step(xb, xa, 1);
#pragma unroll 1
    for (int t = 2; t < KQ / 32; t += 2) {
      if (t == ATT_P1) __builtin_amdgcn_s_setprio(2);
      if (t == ATT_P2) __builtin_amdgcn_s_setprio(1);
      if (t == ATT_P3) __builtin_amdgcn_s_setprio(0);
      step(xa, xb, t);
      step(xb, xa, t + 1);
    }
    o[1] = pv(xb.v1, sc[1], 1, o[1]);
  };
  if (ATT_P1) __builtin_amdgcn_s_setprio(3);
  fast();
  {
    float big = 0.f;
#pragma unroll
    for (int qt = 0; qt < 2; ++qt) {
      big = fmx(big, hh ? o[qt][8] * (3.0e38f / ATT_LMAX) : __builtin_fabsf(o[qt][8]));
#pragma unroll
      for (int i = 0; i < 12; ++i) if (i != 8) big = fmx(big, (i > 8 && hh) ? 0.f : __builtin_fabsf(o[qt][i]));
    }
    if (__builtin_expect(!wave_all(big <= 3.0e38f), 0)) slow();
  }
  f32x4 (*mo)[2][3][64] = mo_[wid % ATT_NQ];
  float (*mm)[2][64] = mm_[wid % ATT_NQ];
#pragma unroll
  for (int qt = 0; qt < 2; ++qt) {
#pragma unroll
    for (int k = 0; k < 3; ++k) mo[kh][qt][k][lane] = (f32x4){o[qt][4 * k], o[qt][4 * k + 1], o[qt][4 * k + 2], o[qt][4 * k + 3]};
    mm[kh][qt][lane] = m[qt];
  }
  const int qt = kh & 1, grp = kh >> 1, row = q0 + qt * 32 + r, k0 = hd * BD + 4 * hh + 8 * grp;
  auto oaddr = [&](int k) { return O + (((size_t)(row >> 4) * 20 + (k >> 5)) * 64 + ((k & 31) >> 3) * 16 + (row & 15)) * 8 + (k & 7); };
  u16 *op0 = oaddr(k0), *op1 = oaddr(k0 + 16);
  asm volatile("" : "+v"(op0), "+v"(op1));
  asm volatile("s_waitcnt lgkmcnt(0)\n\ts_barrier" ::: "memory");
  {
    float a[4], l[4];
    f32x4 u[4], x[4];
#pragma unroll
    for (int i = 0; i < 4; ++i) { a[i] = mm[i][qt][lane]; l[i] = mo[i][qt][2][lane | 32][0]; u[i] = mo[i][qt][grp][lane]; x[i] = mo[i][qt][2][lane]; }
    const float mn = fmx(fmx(a[0], a[1]), fmx(a[2], a[3]));
#pragma unroll
    for (int i = 0; i < 4; ++i) a[i] = __builtin_amdgcn_exp2f(a[i] - mn);
    const float inv = __builtin_amdgcn_rcpf((l[0] * a[0] + l[1] * a[1]) + (l[2] * a[2] + l[3] * a[3]));
    const f32x4 v = ((u[0] * a[0] + u[1] * a[1]) + (u[2] * a[2] + u[3] * a[3])) * inv;
    *(GAS u32x2*)op0 = (u32x2){pack2(v[0], v[1]), pack2(v[2], v[3])};
    if (grp == 0) {
      const f32x4 y = ((x[0] * a[0] + x[1] * a[1]) + (x[2] * a[2] + x[3] * a[3])) * inv;
      if (hh == 0) *(GAS u32x2*)op1 = (u32x2){pack2(y[0], y[1]), pack2(y[2], y[3])};
    }
  }
}
#if defined(__HIP_DEVICE_COMPILE__)
#pragma clang attribute pop
#endif

__global__ __launch_bounds__(640, 3) void proj_kernel(const u16* __restrict__ Of, const u16* __restrict__ Wf, const u16* __restrict__ xold,
                                                      u16* __restrict__ xnew) {
  __shared__ __attribute__((aligned(16))) char ob[40960];
  const int tid = threadIdx.x, lane = tid & 63, w = __builtin_amdgcn_readfirstlane(tid >> 6), g = lane >> 4, c = lane & 15;
  __shared__ unsigned sink[64];
  own_code<1500>(tid, sink);
  const int rb = (blockIdx.x & 7) + 8 * (blockIdx.x >> 5), cb = (blockIdx.x >> 3) & 3;
  const int col = cb * 160 + w * 16 + 4 * g;
  const u16* wp = Wf + (size_t)(cb * 10 + w) * 20 * 512 + lane * 8;
  u32x4 st[4];
#pragma unroll
  for (int i = 0; i < 4; ++i) st[i] = *(const u32x4*)(Of + (size_t)rb * 40 * 512 + (i * 640 + tid) * 8);
  bf16x8 wf[20];
#pragma unroll
  for (int ks = 0; ks < 20; ++ks) wf[ks] = *(const bf16x8*)(wp + (size_t)ks * 512);
  u32x2 xo[2];
#pragma unroll
  for (int t = 0; t < 2; ++t) xo[t] = *(const u32x2*)(xold + (((size_t)(rb * 2 + t) * 20 + (col >> 5)) * 64 + ((col & 31) >> 3) * 16 + c) * 8 + (col & 7));
#pragma unroll
  for (int i = 0; i < 4; ++i) *(u32x4*)(ob + (i * 640 + tid) * 16) = st[i];
  BAR();
  f32x4 acc[2] = {{0.f, 0.f, 0.f, 0.f}, {0.f, 0.f, 0.f, 0.f}};
#pragma unroll
  for (int ks = 0; ks < 20; ++ks)
#pragma unroll
    for (int t = 0; t < 2; ++t) acc[t] = mfma32(wf[ks], *(const bf16x8*)(ob + (t * 20 + ks) * 1024 + lane * 16), acc[t]);
#pragma unroll
  for (int t = 0; t < 2; ++t) {
    const unsigned r01 = pack2(acc[t][0], acc[t][1]), r23 = pack2(acc[t][2], acc[t][3]);
    const unsigned p01 = pack2(u2f(xo[t][0] << 16) + u2f(r01 << 16), u2f(xo[t][0] & 0xffff0000u) + u2f(r01 & 0xffff0000u));
    const unsigned p23 = pack2(u2f(xo[t][1] << 16) + u2f(r23 << 16), u2f(xo[t][1] & 0xffff0000u) + u2f(r23 & 0xffff0000u));
    *(u32x2*)(xnew + (size_t)(rb * 32 + t * 16 + c) * DM + col) = (u32x2){p01, p23};
  }
}

extern "C" void kernel_launch(void* const* d_in, const int* in_sizes, int n_in, void* d_out, int out_size, void* d_ws,
                              size_t ws_size, hipStream_t stream) {
  const u16* A_x = (const u16*)d_in[0];
  const u16* A_mem0 = (const u16*)d_in[1];
  const u16* A_Wq = (const u16*)d_in[2];
  const u16* A_Wk = (const u16*)d_in[3];
  const u16* A_Wv = (const u16*)d_in[4];
  const u16* A_Wo = (const u16*)d_in[5];
  const u16* B_wqkv = (const u16*)d_in[8];
  const u16* B_wo = (const u16*)d_in[9];
  char* ws = (char*)(((uintptr_t)d_ws + ((size_t)WS_OFF << 20) + WS_AL - 1) & ~(uintptr_t)(WS_AL - 1));
  u16* xa = (u16*)(ws + OFF_XA); u16* Ob = (u16*)(ws + OFF_OB);

  prologue_kernel<<<256, 640, 0, stream>>>(ProArgs{A_x, A_Wq, A_Wk, A_Wv, A_Wo, B_wqkv, B_wo, ws});
  for (int layer = 0; layer < 2; ++layer) {
    rec_kernel<<<256, 512, 0, stream>>>(RecArgs{A_mem0, A_Wq, A_Wk, (const u16*)(ws + OFF_XB), xa, ws, (unsigned)layer, 0u});
    attb_kernel<<<BH * 32 / ATT_NQ, 256 * ATT_NQ, 0, stream>>>((const u16*)(ws + OFF_QB), (const u16*)(ws + OFF_KV), Ob);
  }
  proj_kernel<<<256, 640, 0, stream>>>(Ob, (const u16*)(ws + OFF_WOBT), xa, (u16*)d_out);
}
```

```cpp
#include <hip/hip_runtime.h>
#include <hip/hip_bf16.h>
#include <stdint.h>
#include <type_traits>

typedef unsigned short u16;
typedef unsigned long long u64;
typedef __attribute__((ext_vector_type(8))) short bf16x8;
typedef __attribute__((ext_vector_type(16))) short bf16x16;
typedef __attribute__((ext_vector_type(4))) float f32x4;
typedef __attribute__((ext_vector_type(2))) float f32x2;
typedef __attribute__((ext_vector_type(16))) float f32x16;
typedef __attribute__((ext_vector_type(8))) float f32x8;
typedef __attribute__((ext_vector_type(4))) unsigned u32x4;
typedef __attribute__((ext_vector_type(2))) unsigned u32x2;
typedef __attribute__((ext_vector_type(3))) unsigned u32x3;
typedef __attribute__((ext_vector_type(8))) unsigned u32x8;
typedef __attribute__((ext_vector_type(16))) unsigned u32x16;
typedef __attribute__((ext_vector_type(32))) unsigned u32x32;
#define GAS __attribute__((address_space(1)))
#define LAS __attribute__((address_space(3)))
typedef GAS unsigned long long gu64;
#define BAR() do { __builtin_amdgcn_s_waitcnt(0xC07F); asm volatile("s_waitcnt lgkmcnt(0)\n\ts_barrier" ::: "memory"); } while (0)

constexpr int S_ = 2048, DM = 640;
constexpr int NH = 32, DH = 192, HD = NH * DH;
constexpr int MEM = 8, SEG = 32, NSEG = S_ / SEG;
constexpr int BH = 32, BD = 20;

constexpr size_t al256(size_t x) { return (x + 255) & ~(size_t)255; }
constexpr int LAG3 = 2, LAG4 = 3, LAG5 = 6;
#define SR_ 8
constexpr int SR = SR_;
constexpr unsigned SCT_STEP = 131072, OFF_SCM = SR * SCT_STEP, SCM_STEP = 32768;
constexpr int ACC_NB = 192;
constexpr size_t ACC_STEP = 65536;
constexpr unsigned ACC_TS = ACC_NB * 256;
constexpr size_t ACC_LAYER = SR * ACC_STEP;
constexpr size_t OFF_ACC = 3 * 524288;
static_assert(SR == 8, "regions");
__device__ __forceinline__ unsigned ts_piece(int i) { return ACC_TS + (unsigned)(i < 128 ? (i >> 1) * 256 + (i & 1) * 16 : 128 + (i & 1) * 8192); }
constexpr size_t OFF_LNX = OFF_SCM + SR * SCM_STEP;
constexpr size_t OFF_RDY = OFF_LNX + 65536;
constexpr size_t PG = 2097152;
#define RING_ 4
constexpr int RING = RING_;
constexpr size_t TS_SLOT = (size_t)NH * 8 * 2560, PT_SLOT = (size_t)NH * 2560, OP_SLOT = (size_t)256 * 2560;
__device__ __forceinline__ unsigned ts_tok(int h, int j) { return (unsigned)((h & 15) * 32768 + (j >> 2) * 16384 + (j & 3) * 2048); }
__device__ __forceinline__ unsigned ts_mem(int h, int j) { return (unsigned)(524288 + ((h & 15) >> 1) * 16384 + (h & 1) * 4096 + j * 512); }
__device__ __forceinline__ unsigned pt_tok(int h) { return (unsigned)(((h & 15) >> 2) * 16384 + (h & 3) * 2048); }
__device__ __forceinline__ unsigned pt_mem(int h) { return (unsigned)(65536 + (h & 15) * 512); }
constexpr size_t OFF_X = 2 * PG + TS_SLOT + 3 * PT_SLOT, X_BYTES = 7 * OP_SLOT + 4 * PT_SLOT;
constexpr size_t OFF_OP = OFF_X + TS_SLOT + 4 * PT_SLOT;
constexpr size_t COMM_BYTES = OFF_X + X_BYTES + RING * OP_SLOT;
__device__ __forceinline__ unsigned near_half(unsigned ck, unsigned o) { return o + ((ck >> (o >= 2 * PG ? 8 : 4)) & 8192u); }
__device__ __forceinline__ unsigned ts_slot(unsigned ck, int sl) {
  return near_half(ck, sl == NSEG - 1 ? (unsigned)OFF_X : ((sl & 3) == 3 ? (unsigned)(2 * PG) : (unsigned)PG + (unsigned)(sl & 3) * (unsigned)TS_SLOT));
}
__device__ __forceinline__ unsigned pt_slot(unsigned ck, int sl) {
  return near_half(ck, sl >= NSEG - 4 ? (unsigned)(OFF_X + TS_SLOT) + (unsigned)(sl - (NSEG - 4)) * (unsigned)PT_SLOT
                                      : ((sl & 3) == 0 ? (unsigned)(PG + 3 * TS_SLOT) : (unsigned)(2 * PG + TS_SLOT - PT_SLOT) + (unsigned)(sl & 3) * (unsigned)PT_SLOT));
}
__device__ __forceinline__ unsigned op_slot(int sl) { return (unsigned)OFF_OP + (unsigned)(sl >= NSEG - 6 ? sl - (NSEG - 6) : 6 + sl % RING) * (unsigned)OP_SLOT; }
static_assert(RING == 4 && OFF_OP <= 3 * PG && OFF_RDY + 2048 <= OFF_ACC && OFF_ACC + ACC_LAYER == PG && TS_SLOT % 16384 == 0 && PT_SLOT % 16384 == 0, "pages");
static_assert(TS_SLOT == OP_SLOT && RING >= 4 && NSEG % SR == 0 && SR >= 5 && X_BYTES % 4096 == 0 && X_BYTES <= 256 * 20 * 1024, "slots");
constexpr size_t OFF_WB = al256(COMM_BYTES);
constexpr size_t OFF_BNRM = OFF_WB + (size_t)256 * 100 * 1024;
constexpr size_t OFF_LAT = OFF_BNRM + 1024;
constexpr size_t OFF_XNRM = OFF_LAT + 2048;
constexpr size_t OFF_WQKVBT = OFF_XNRM + al256(S_ * 16);
constexpr size_t OFF_WOBT = OFF_WQKVBT + al256((size_t)3 * DM * DM * 2);
constexpr size_t OFF_XN = OFF_WOBT + al256((size_t)DM * DM * 2);
constexpr int QST = BD;
constexpr size_t OFF_XA = OFF_XN + al256((size_t)S_ * DM * 2);
constexpr size_t OFF_XB = OFF_XA + al256((size_t)S_ * DM * 2);
constexpr size_t OFF_QB = OFF_XB + al256((size_t)S_ * DM * 2);
constexpr int KVT = 2624;
constexpr size_t OFF_KV = OFF_QB + al256((size_t)BH * S_ * QST * 2);
constexpr size_t OFF_OB = OFF_KV + al256((size_t)BH * 64 * KVT);
constexpr size_t OFF_DBG = OFF_OB + al256((size_t)S_ * DM * 2);
constexpr size_t WS_TOTAL = OFF_DBG + (size_t)2 * 64 * 8 * DM * 2;
#define WS_AL 2097152
#define WS_OFF 0
static_assert(WS_TOTAL + WS_AL + ((size_t)WS_OFF << 20) <= ((size_t)256 << 20), "workspace budget");

#if defined(__HIP_DEVICE_COMPILE__)
#pragma clang attribute push(__attribute__((target("no-packed-fp32-ops"))), apply_to = function)
#endif
__device__ __forceinline__ float u2f(unsigned v) { return __builtin_bit_cast(float, v); }
__device__ __forceinline__ unsigned f2u(float v) { return __builtin_bit_cast(unsigned, v); }
__device__ __forceinline__ float bf2f(unsigned v) { return __builtin_bit_cast(float, v << 16); }
__device__ __forceinline__ unsigned f2bf(float f) { __bf16 b = (__bf16)f; return (unsigned)__builtin_bit_cast(u16, b); }
__device__ __forceinline__ float rbf(float f) { return bf2f(f2bf(f)); }
typedef __attribute__((ext_vector_type(2))) __bf16 hbf16x2;
__device__ __forceinline__ unsigned pack2(float lo, float hi) { f32x2 v = {lo, hi}; hbf16x2 b = __builtin_convertvector(v, hbf16x2); return __builtin_bit_cast(unsigned, b); }
__device__ __forceinline__ f32x4 mfma32(bf16x8 a, bf16x8 b, f32x4 c) { return __builtin_amdgcn_mfma_f32_16x16x32_bf16(a, b, c, 0, 0, 0); }
typedef __attribute__((ext_vector_type(8))) __bf16 hbf16x8;
typedef __attribute__((ext_vector_type(16))) __bf16 hbf16x16;
__device__ __forceinline__ f32x4 smfma64(bf16x8 a, bf16x16 b, f32x4 c, int idx) {
  return __builtin_amdgcn_smfmac_f32_16x16x64_bf16(__builtin_bit_cast(hbf16x8, a), __builtin_bit_cast(hbf16x16, b), c, idx, 0, 0);
}
__device__ __forceinline__ bf16x16 cat16(bf16x8 a, bf16x8 b) { return __builtin_shufflevector(a, b, 0, 1, 2, 3, 4, 5, 6, 7, 8, 9, 10, 11, 12, 13, 14, 15); }
__device__ __forceinline__ bf16x8 half8(bf16x16 v, int hi) { return hi ? __builtin_shufflevector(v, v, 8, 9, 10, 11, 12, 13, 14, 15) : __builtin_shufflevector(v, v, 0, 1, 2, 3, 4, 5, 6, 7); }
__device__ __forceinline__ bf16x8 mk8(unsigned a, unsigned b, unsigned c, unsigned d) { u32x4 v = {a, b, c, d}; return __builtin_bit_cast(bf16x8, v); }
__device__ __forceinline__ unsigned dpp_ror8(unsigned v) { return (unsigned)__builtin_amdgcn_mov_dpp((int)v, 0x128, 0xf, 0xf, true); }
__device__ __forceinline__ float fmx(float a, float b) { return __builtin_elementwise_maximum(a, b); }

__device__ __forceinline__ int imax(int a, int b) { return a > b ? a : b; }
__device__ __forceinline__ int imin(int a, int b) { return a < b ? a : b; }
__device__ __forceinline__ unsigned umin(unsigned a, unsigned b) { return a < b ? a : b; }
__device__ __forceinline__ unsigned umax(unsigned a, unsigned b) { return a > b ? a : b; }
__device__ __forceinline__ bool wave_all(bool p) { return __builtin_amdgcn_ballot_w64(!p) == 0ull; }
__device__ __forceinline__ float x16mx(float x) { unsigned u = __builtin_bit_cast(unsigned, x); auto r = __builtin_amdgcn_permlane16_swap(u, u, false, false); return fmx(u2f(r[0]), u2f(r[1])); }
__device__ __forceinline__ float x32mx(float x) { unsigned u = __builtin_bit_cast(unsigned, x); auto r = __builtin_amdgcn_permlane32_swap(u, u, false, false); return fmx(u2f(r[0]), u2f(r[1])); }
__device__ __forceinline__ float x16sum(float x) { unsigned u = __builtin_bit_cast(unsigned, x); auto r = __builtin_amdgcn_permlane16_swap(u, u, false, false); return u2f(r[0]) + u2f(r[1]); }
__device__ __forceinline__ float x32sum(float x) { unsigned u = __builtin_bit_cast(unsigned, x); auto r = __builtin_amdgcn_permlane32_swap(u, u, false, false); return u2f(r[0]) + u2f(r[1]); }
template <int CTRL> __device__ __forceinline__ float dppf(float v) { return u2f((unsigned)__builtin_amdgcn_mov_dpp((int)f2u(v), CTRL, 0xf, 0xf, true)); }
#define OWN_PF 1
template <int BYTES> __device__ __forceinline__ void own_code(int tid, unsigned* sink) {
  if (!OWN_PF || __builtin_amdgcn_readfirstlane(tid >> 6) * 4096 >= BYTES) return;
  const char* pc; unsigned keep;
  asm volatile("s_getpc_b64 %0" : "=s"(pc));
  asm volatile("s_mov_b32 %0, m0\n\ts_mov_b32 m0, %3\n\ts_nop 0\n\tglobal_load_lds_dword %1, %2\n\ts_mov_b32 m0, %0"
               : "=&s"(keep) : "v"(tid * 64 < BYTES ? tid * 64 : 0), "s"(pc), "s"((unsigned)(uintptr_t)(LAS unsigned*)sink) : "memory");
}
constexpr int DPP_XOR1 = 0xB1, DPP_XOR2 = 0x4E, DPP_ROR4 = 0x124, DPP_ROR8 = 0x128;
#if defined(__HIP_DEVICE_COMPILE__)
#pragma clang attribute pop
#endif

typedef short v4i16_t __attribute__((ext_vector_type(4)));
__device__ __forceinline__ bf16x8 tr_frag(const char* lo_, const char* hi_) {
  const v4i16_t lo = __builtin_amdgcn_ds_read_tr16_b64_v4i16((LAS v4i16_t*)lo_), hi = __builtin_amdgcn_ds_read_tr16_b64_v4i16((LAS v4i16_t*)hi_);
  return (bf16x8){lo[0], lo[1], lo[2], lo[3], hi[0], hi[1], hi[2], hi[3]};
}
__device__ __forceinline__ int rec_wg(int gp, int m) { return (((gp & 1) * 16 + m) << 3) | (gp >> 1); }
__device__ __forceinline__ float wsum(float v) {
  v += dppf<DPP_XOR1>(v); v += dppf<DPP_XOR2>(v); v += dppf<DPP_ROR4>(v); v += dppf<DPP_ROR8>(v);
  return x32sum(x16sum(v));
}
__device__ __forceinline__ float ln_row(int lane, u32x4 (&xo)[2]) {
  const bool two = lane < 16;
  float v[16];
#pragma unroll
  for (int hf = 0; hf < 2; ++hf)
#pragma unroll
    for (int i = 0; i < 4; ++i) { const unsigned u = xo[hf][i]; v[hf * 8 + 2 * i] = bf2f(u & 0xffff); v[hf * 8 + 2 * i + 1] = bf2f(u >> 16); }
  float s0 = 0.f, s1 = 0.f;
#pragma unroll
  for (int i = 0; i < 8; ++i) { s0 += v[i]; s1 += v[8 + i]; }
  const float mean = wsum(two ? s0 + s1 : s0) * (1.0f / DM);
  s0 = s1 = 0.f;
#pragma unroll
  for (int i = 0; i < 8; ++i) { const float d0 = v[i] - mean, d1 = v[8 + i] - mean; s0 += d0 * d0; s1 += d1 * d1; }
  const float var = rbf(wsum(two ? s0 + s1 : s0) * (1.0f / DM));
  const float mb = rbf(mean);
  const float rden = 1.0f / rbf(sqrtf(rbf(var + 1e-5f)));
  float n2[2] = {0.f, 0.f};
#pragma unroll
  for (int hf = 0; hf < 2; ++hf) {
    float y[8];
#pragma unroll
    for (int i = 0; i < 8; ++i) { y[i] = rbf(rbf(v[hf * 8 + i] - mb) * rden); n2[hf] += y[i] * y[i]; }
    xo[hf] = (u32x4){pack2(y[0], y[1]), pack2(y[2], y[3]), pack2(y[4], y[5]), pack2(y[6], y[7])};
  }
  return wsum(two ? n2[0] + n2[1] : n2[0]);
}

#define PROT(k)
#define WARM_CODE 1
#define B_AUX 16
#define X_AUX 16
#define T_AUX 16
#define PF_POS 0
#define PRB_N0 3
#define PRB_N1 0
#define PRB_LOG(i, v)
#define PLD(p) __builtin_nontemporal_load((const u32x4*)(p))
struct ProArgs { const u16* x; const u16* wq; const u16* wk; const u16* wv; const u16* wo; const u16* wqkvb; const u16* wob; char* ws; };
__global__ __launch_bounds__(640, 3) void prologue_kernel(ProArgs a) {
  __shared__ __attribute__((aligned(16))) char img[192 * 320];
  __shared__ __attribute__((aligned(16))) char slots[10 * 6144];
  __shared__ float red[10][160];
  __shared__ unsigned latw[8];
  __shared__ u32x4 xst[2][640];
  const int b = blockIdx.x, tid = threadIdx.x, lane = tid & 63, v = __builtin_amdgcn_readfirstlane(tid >> 6), c = lane & 15, g = lane >> 4;
  const int h = (b & 7) * 4 + ((b >> 3) & 3), dh = (b >> 5) & 1, cq = b >> 6, ks = dh * 10 + v;
  char* const slot = slots + v * 6144;
  PROT(0);
  __shared__ unsigned sink[64];
  own_code<9600>(tid, sink);
  const unsigned zo = v < 8 ? (unsigned)(((b >> 6) * 512 + ((b & 7) * 2 + (v >> 2)) * 32 + ((b >> 3) & 7) * 4 + (v & 3)) * 1024) : (unsigned)(((v - 7) * 2048 + (b & 7) * 32 + ((b >> 3) & 7) * 4 + (b >> 6)) * 1024);
  __builtin_amdgcn_raw_buffer_store_b128((u32x4){0u, 0u, 0u, 0u}, __builtin_amdgcn_make_buffer_rsrc(a.ws, 0, (int)(3 * PG), 0x00020000), zo + lane * 16, 0, 16);
  __builtin_amdgcn_sched_barrier(0);
  u32x4 wa[2][6], wb[6], tr[4], xo[2];
  const int xrow = b * 8 + (v & 7);
  xo[0] = *(const u32x4*)(a.x + (size_t)xrow * DM + lane * 8);
  xo[1] = *(const u32x4*)(a.x + (size_t)xrow * DM + 512 + c * 8);
  const int job = imin(b + 256 * (9 - v), 800);
  const int jn = job < 600 ? job / 20 : (job % 200) / 20, jk = job % 20;
  {
    const u16* src = job < 600 ? a.wqkvb + (size_t)(32 * jk) * 3 * DM : a.wob + (size_t)(32 * jk) * DM;
#pragma unroll
    for (int i = 0; i < 4; ++i) tr[i] = *(const u32x4*)(src + (size_t)(i * 8 + (lane >> 3)) * (job < 600 ? 3 * DM : DM) + jn * 64 + (lane & 7) * 8);
  }
#pragma unroll
  for (int i = 0; i < 6; ++i) {
    const int idx = (v * 6 + i) * 64 + lane, row = idx / 20;
    wb[i] = PLD(a.wo + (size_t)(h * DH + row) * DM + cq * 160 + (idx - row * 20) * 8);
  }
#pragma unroll
  for (int i = 0; i < 6; ++i) {
    const int idx = i * 64 + lane, r = idx / 24;
#pragma unroll
    for (int u = 0; u < 2; ++u) wa[u][i] = PLD(a.wv + (size_t)(32 * ks + 8 * (r >> 2) + (r & 3) + 4 * u) * HD + h * DH + (idx - r * 24) * 8);
  }
  unsigned pf[2] = {0u, 0u};
  auto prefetch = [&]() {
#pragma unroll
    for (int i = 0; i < 2; ++i) pf[i] = *(const unsigned*)((i ? a.wq : a.wk) + ((size_t)(b * 10 + v) * 48 + imin(lane, 47)) * 32);
  };
  if (PF_POS == 0) prefetch();
  __builtin_amdgcn_sched_barrier(0);
  unsigned lat = ~0u;
  auto probe = [&](int n0, int n) {
    const char* p = a.ws + zo;
#pragma unroll 1
    for (int i = n0; i < n0 + n; ++i) {
      u64 t0, t1; unsigned d;
      asm volatile("s_memtime %0\n\ts_waitcnt lgkmcnt(0)\n\ts_load_dword %2, %3, %4\n\ts_waitcnt lgkmcnt(0)\n\ts_memtime %1\n\ts_waitcnt lgkmcnt(0)"
                   : "=&s"(t0), "=&s"(t1), "=&s"(d) : "s"(p), "s"((i & 3) * 256 + (i >> 2) * 128) : "memory");
      PRB_LOG(i, (unsigned)t1 - (unsigned)t0);
      lat = umin(lat, (unsigned)t1 - (unsigned)t0);
    }
  };
  if (PRB_N0) { asm volatile("s_waitcnt vmcnt(26)" ::: "memory"); probe(0, PRB_N0); }
  __builtin_amdgcn_sched_barrier(0);
  auto xput = [&](int k) {
    if (v < 8) { xst[k][lane * 8 + (v ^ (lane & 7))] = xo[0]; if (lane < 16) xst[k][(64 + lane) * 8 + (v ^ (lane & 7))] = xo[1]; }
  };
  xput(0);
  const float xn2 = u2f(__builtin_amdgcn_readfirstlane(f2u(ln_row(lane, xo))));
  xput(1);
#pragma unroll
  for (int i = 0; i < 4; ++i) *(u32x4*)(slot + (i * 8 + (lane >> 3)) * 144 + (lane & 7) * 16) = tr[i];
  __builtin_amdgcn_fence(__ATOMIC_RELEASE, "wavefront"); __builtin_amdgcn_wave_barrier();
  bf16x8 tf[4];
#pragma unroll
  for (int t = 0; t < 4; ++t) {
    const char* bp = slot + (8 * g + (c >> 2)) * 144 + t * 32 + (c & 3) * 8;
    tf[t] = tr_frag(bp, bp + 4 * 144);
  }
  __builtin_amdgcn_fence(__ATOMIC_RELEASE, "wavefront"); __builtin_amdgcn_wave_barrier();
  __builtin_amdgcn_sched_barrier(0);
#pragma unroll
  for (int i = 0; i < 6; ++i) {
    const int idx = (v * 6 + i) * 64 + lane, row = idx / 20;
    *(u32x4*)(img + row * 320 + (((idx - row * 20) ^ ((row >> 2) & 2)) * 16)) = wb[i];
  }
  bf16x8 A[2][6];
#pragma unroll
  for (int u = 0; u < 2; ++u) {
#pragma unroll
    for (int i = 0; i < 6; ++i) {
      const int idx = i * 64 + lane, r = idx / 24;
      *(u32x4*)(slot + r * 384 + (((idx - r * 24) ^ (r >> 1)) * 16)) = wa[u][i];
    }
    __builtin_amdgcn_fence(__ATOMIC_RELEASE, "wavefront"); __builtin_amdgcn_wave_barrier();
#pragma unroll
    for (int e = 0; e < 6; ++e) A[u][e] = *(const bf16x8*)(slot + c * 384 + (((4 * e + g) ^ (c >> 1)) * 16));
    __builtin_amdgcn_fence(__ATOMIC_RELEASE, "wavefront"); __builtin_amdgcn_wave_barrier();
  }
  BAR();
  PROT(1);
  if (PF_POS == 0) asm volatile("" :: "v"(pf[0]), "v"(pf[1]));
  if (PF_POS == 1) prefetch();
  {
    const unsigned xf = (unsigned)((b >> 1) * 20480 + (tid >> 3) * 256 + (b & 1) * 128 + (tid & 7) * 16);
    const __amdgpu_buffer_rsrc_t rx = __builtin_amdgcn_make_buffer_rsrc(a.ws + OFF_XN, 0, (int)(OFF_QB - OFF_XN), 0x00020000);
    __builtin_amdgcn_raw_buffer_store_b128(xst[1][tid ^ ((tid >> 3) & 7)], rx, xf, 0, X_AUX);
    __builtin_amdgcn_raw_buffer_store_b128(xst[0][tid ^ ((tid >> 3) & 7)], rx, xf + (unsigned)(OFF_XB - OFF_XN), 0, X_AUX);
    if (v < 8 && lane < 4) ((float*)(a.ws + OFF_XNRM))[xrow * 4 + lane] = lane == 0 ? xn2 : 0.f;
  }
  if (job < 800) {
    const __amdgpu_buffer_rsrc_t rt = __builtin_amdgcn_make_buffer_rsrc(a.ws + (job < 600 ? OFF_WQKVBT : OFF_WOBT) + ((size_t)jn * 80 + jk) * 1024, 0, 81920, 0x00020000);
#pragma unroll
    for (int t = 0; t < 4; ++t) __builtin_amdgcn_raw_buffer_store_b128(__builtin_bit_cast(u32x4, tf[t]), rt, lane * 16 + t * 20480, 0, T_AUX);
  }
  const int gp = h >> 1, hh = h & 1;
  const __amdgpu_buffer_rsrc_t rwb = __builtin_amdgcn_make_buffer_rsrc(a.ws + OFF_WB, 0, 256 * 100 * 1024, 0x00020000);
#pragma unroll
  for (int n = 0; n < 10; ++n) {
    const int off = n * 32 + 8 * (c & 3), T = cq * 10 + n;
    const char* bp = img + (8 * g + (c >> 2)) * 320 + (off ^ ((g & 1) << 5));
    f32x4 ac[2] = {{0.f, 0.f, 0.f, 0.f}, {0.f, 0.f, 0.f, 0.f}};
#pragma unroll
    for (int e = 0; e < 6; ++e) {
      const bf16x8 bf = tr_frag(bp + e * 32 * 320, bp + (e * 32 + 4) * 320);
      ac[0] = mfma32(A[0][e], bf, ac[0]); ac[1] = mfma32(A[1][e], bf, ac[1]);
    }
    const u32x4 o = {pack2(ac[0][0], ac[0][1]), pack2(ac[0][2], ac[0][3]), pack2(ac[1][0], ac[1][1]), pack2(ac[1][2], ac[1][3])};
    const unsigned oo = T < 32 ? (unsigned)((rec_wg(gp, T & 15) * 100 + (hh * 3 + (T >> 4)) * 20 + ks) * 1024 + lane * 16)
                               : (unsigned)((rec_wg(gp, 2 * (T - 32) + (c >> 3)) * 100 + 40 + ks) * 1024 + (g * 16 + hh * 8 + (c & 7)) * 16);
    __builtin_amdgcn_raw_buffer_store_b128(o, rwb, oo, 0, B_AUX);
    float n2 = 0.f;
#pragma unroll
    for (int r = 0; r < 4; ++r) n2 += ac[0][r] * ac[0][r] + ac[1][r] * ac[1][r];
    n2 = x32sum(x16sum(n2));
    if (g == 0) red[v][n * 16 + c] = n2;
  }
  PROT(2);
  if (PF_POS == 1) asm volatile("" :: "v"(pf[0]), "v"(pf[1]));
  probe(PRB_N0, PRB_N1);
  if (v < 8) latw[v] = lat;
  else if (b < 64 && lane == 0) ((unsigned*)(a.ws + OFF_LAT))[(v - 4) * 64 + b] = umin(lat, 60000u);
  if (WARM_CODE && v == 7) {
    const char* pc; int d0, d1;
    asm("s_getpc_b64 %0\n\ts_mov_b32 %1, _Z10rec_kernel7RecArgs@rel32@lo+4\n\ts_mov_b32 %2, _Z11proj_kernelPKtS0_S0_Pt@rel32@lo+12" : "=s"(pc), "=s"(d0), "=s"(d1));
    const char *c0 = pc + d0, *c1 = pc + d1;
    const long o = ((long)b * 64 + lane) * 64;
    if (o < c1 - c0) asm volatile("" :: "v"(*(const volatile GAS unsigned*)(c0 + o)));
  }
  if (v == 9 && lane < 32) {
    const int q = b * 32 + lane;
    *(u32x4*)(a.ws + OFF_KV + (size_t)(q >> 2) * KVT + 1280 + (q & 3) * 336 + 320) = (u32x4){0x3f803f80u, 0x3f803f80u, 0x3f803f80u, 0x3f803f80u};
  }
  BAR();
  if (v == 1 && lane == 0) ((unsigned*)(a.ws + OFF_LAT))[b] = umin(umin(umin(latw[0], latw[4]) + umin(latw[1], latw[5]), 30000u) + umin(umin(latw[2], latw[6]) + umin(latw[3], latw[7]), 30000u), 60000u);
  if (v == 0) {
    float mx = 0.f;
#pragma unroll
    for (int k = 0; k < 3; ++k) {
      const int col = k * 64 + lane;
      float sm = 0.f;
#pragma unroll
      for (int i = 0; i < 10; ++i) sm += red[i][col < 160 ? col : 0];
      mx = fmx(mx, sm);
    }
    mx = fmx(mx, dppf<DPP_XOR1>(mx)); mx = fmx(mx, dppf<DPP_XOR2>(mx)); mx = fmx(mx, dppf<DPP_ROR4>(mx)); mx = fmx(mx, dppf<DPP_ROR8>(mx));
    mx = x32mx(x16mx(mx));
    if (lane == 0) ((float*)(a.ws + OFF_BNRM))[(cq * 32 + h) * 2 + dh] = mx;
  }
  PROT(3);
}

struct RecArgs {
  const u16* mem0; const u16* wq; const u16* wk; const u16* xold; u16* xnew; char* ws;
  unsigned layer; unsigned pad;
};
#define RLX_AGENT __ATOMIC_RELAXED, __HIP_MEMORY_SCOPE_AGENT
constexpr unsigned SPIN_CAP = 1u << 20;
#define TSTAMP(k, v)
#define ARRIVE(k)
#define ARRIVE_OUT()
#define ARRIVE_DECL()
#define LOGV(k, v)
#define LOGF(k, v)
#define LOGP(k)
#define LOGQ(k)
#define LOGF2(k)
#define LOGD(k)
#define LOGT(k, v)
#define STAMP_DECL()
#define STAMP(k)
#define STAMPV(k, v)
#define STAMP_OUT()
#define REC_D1 520
#define SCH_V 18
#define SCH_VMIN 0
#define SCH_DMIN 4
#define SCH_PEN 150
#define SCH_VMIN2 (-4)
#define SCH_PEN2 40
#define SCH_GU 1
#define SCH_GD 2
#define SCH_UP 6
#define SCH_D0 196
#define SCH_FIRST 120
#define EXP_NODMA 0
#define BG_HOLD 220
#define QBATCH 0
#define WPF_N 5
#define WPF_W 0
#define WPF0 (63 - WPF_N)
#define BG_HOLD4 250
#define D1_W1 20
#define F3_D 1200
#define T2W 2
#define AT_ASM 1
#define PM_Z 1
#define P2_X 1
#define ST_KS 14
#define ST_KS0 ST_KS
#define ST_KS1 ST_KS
#define PADR 0x6666
#define PADT 0x000
#define CODE_PAD(n) asm volatile(".p2align 6\n.rept %0\ns_nop 0\n.endr" :: "n"(n))
#define ST_X 300
#define ST_ALL 4
#define CPF_S 61
#define FR_D 700
#define FR_PACE 6
#define END_E60 600
#define END_E1 1300
#define END_E2 3400
#define END_E62 2800
#define END_E3 6800
#define END_E4 11400
#define EARLY_N 6
#define EARLY_D1 0
#define EARLY_V 4
#define EARLY_V1 10
#define F3 0
#define F3A 0
#define F3A_D 400
#define T4_D 300
#define T4_P 1
#define P2_W 1
#define PS_SL 1
#define F2B 4
#define F0C 0
#define F2C 0
#define W2_P 1
#define SCH_W 1
#define SP_SL 0
#define D1_M 300
#define D1_MIN 260
#define D1_C 96
#define D1_G 2
#define BG_HOLD6 0
#define W2_D 0
#define END_E61 2600
#define D1_LATE 200
#define P2_G0 10
#define P2_G2 6
#define P2_G3 6
#define SCH_RL 0
#define SCH_P 3
#define MMA_P 0
#define SP_P 0
#define F6 8
#define F7 10
#define XPF_W 2
#define SCH_LATE 0
#define LATE_V 6
#define QW 3
#define FB_SL 0
#define P2_G1 1
#define P1_G 0
#define F4Z 0
#define F5Z 0
#define T5_Z 2
#define F0A 3
#define FB_E 0
#define EXACT 1
#define D1_P 3
#define VO_A0 9
#define VO_A2 10
#define VO_B0 3
#define VO_B1 0
#define VO_N2 7
#define VO_N3 10
#define VO_W -1
#define UN_P 2
#define VP_E 1
#define TS_B 0
#define ST_E 1
#define W1_SL 0
#define VO_A1 4
#define LAST_V 18
#define END_E59 3200
#define END_G2 600
#define END_G3 600
#define QR 4
#define WPF_S 128
#define END_SL 4
#define SCH_DR 184
#define SCH_DK 0
#define KP0 0
#define KP1 0
#define XPF 3
#define W1_D 0
#define F4 12
#define F5 0
#define F67 0
#define KP2 0
#define REC_RNG 70u
#define SCH_DRAIN 40
#define PL_X 0
#define FC(k)
#define PIN_V(x) asm volatile("" : "+v"(x))
#define PIN_S(x) asm volatile("" : "+s"(x))
#define VM_DRAIN() do { asm volatile("s_waitcnt vmcnt(0)" ::: "memory"); __builtin_amdgcn_s_waitcnt(0x0F70); } while (0)
#define SC1_LOAD(off) __builtin_amdgcn_raw_buffer_load_b128(rs, (off), 0, 16)
#define SC1_LOADS(off, soff) __builtin_amdgcn_raw_buffer_load_b128(rs, (off), (soff), 16)
#define SC1_STORE8S(v, off, soff) __builtin_amdgcn_raw_buffer_store_b64((v), rs, (off), (soff), 16)
#define SC1_LOAD8(off) __builtin_amdgcn_raw_buffer_load_b64(rs, (off), 0, 16)
#define SC1_STORE(v, off) __builtin_amdgcn_raw_buffer_store_b128((v), rs, (off), 0, 16)
#define SC1_STORE8(v, off) __builtin_amdgcn_raw_buffer_store_b64((v), rs, (off), 0, 16)
#if defined(__HIP_DEVICE_COMPILE__)
#pragma clang attribute push(__attribute__((target("no-packed-fp32-ops"))), apply_to = function)
#endif
__device__ __forceinline__ unsigned now32() { return (unsigned)__builtin_amdgcn_s_memrealtime(); }
__device__ __forceinline__ void wait_until(unsigned t) { for (;;) { const int d = (int)(t - now32()); if (d <= 0) break; if (d > 8) __builtin_amdgcn_s_sleep(1); } }
__device__ __forceinline__ unsigned cyc32() { return (unsigned)__builtin_amdgcn_s_memtime(); }
__device__ __forceinline__ void wait_cyc(unsigned t) { for (;;) { const int d = (int)(t - cyc32()); if (d <= 0) break; if (d > 200) __builtin_amdgcn_s_sleep(1); } }
#define WAIT_ASM(clk, far_) "1:\n\t" clk " vcc\n\ts_waitcnt lgkmcnt(0)\n\ts_sub_i32 %0, %1, vcc_lo\n\ts_cmp_lt_i32 %0, " far_ "\n\ts_cbranch_scc1 3f\n\ts_sleep 1\n\ts_branch 1b\n" \
                            "2:\n\t" clk " vcc\n\ts_waitcnt lgkmcnt(0)\n\ts_sub_i32 %0, %1, vcc_lo\n3:\n\ts_cmp_gt_i32 %0, 0\n\ts_cbranch_scc1 2b"
__device__ __forceinline__ int spin_until(unsigned t) { int d; asm volatile(WAIT_ASM("s_memrealtime", "9") : "=&s"(d) : "s"(t) : "vcc", "scc", "memory"); return d; }
__device__ __forceinline__ int spin_cyc(unsigned t) { int d; asm volatile(WAIT_ASM("s_memtime", "200") : "=&s"(d) : "s"(t) : "vcc", "scc", "memory"); return d; }
__device__ __forceinline__ void spin_exact(unsigned t) {
  if (!EXACT) { spin_cyc(t); return; }
  int d;
  asm volatile("1:\n\ts_memtime vcc\n\ts_waitcnt lgkmcnt(0)\n\ts_sub_i32 %0, %1, vcc_lo\n\ts_cmp_gt_i32 %0, 95\n\ts_cbranch_scc1 1b\n\t"
               "s_max_i32 %0, %0, 0\n\ts_andn2_b32 %0, %0, 3\n\ts_sub_i32 %0, 3f-2f, %0\n\ts_getpc_b64 vcc\n"
               "2:\n\ts_add_u32 vcc_lo, vcc_lo, %0\n\ts_addc_u32 vcc_hi, vcc_hi, 0\n\ts_setpc_b64 vcc\n\t.rept 24\n\ts_nop 0\n\t.endr\n3:"
               : "=&s"(d) : "s"(t) : "scc", "vcc", "memory");
}
template <int OFF, int SL> __device__ __forceinline__ void lds_spin(unsigned lds0, unsigned want) {
  unsigned v;
  asm volatile("1:\n\tds_read_b32 %0, %1 offset:%3\n\t.if %4\n\ts_sleep %4\n\t.endif\n\ts_waitcnt lgkmcnt(0)\n\tv_cmp_gt_u32 vcc, %2, %0\n\ts_cbranch_vccnz 1b"
               : "=&v"(v) : "v"(lds0), "s"(want), "n"(OFF), "n"(SL) : "vcc", "memory");
}
template <int CTRL> __device__ __forceinline__ int dppi(int v) { return __builtin_amdgcn_mov_dpp(v, CTRL, 0xf, 0xf, true); }
__device__ __forceinline__ int wave_imax(int l) {
  l = imax(l, dppi<DPP_XOR1>(l)); l = imax(l, dppi<DPP_XOR2>(l)); l = imax(l, dppi<DPP_ROR4>(l)); l = imax(l, dppi<DPP_ROR8>(l));
  int a = imax(__builtin_amdgcn_readlane(l, 0), __builtin_amdgcn_readlane(l, 16)), b = imax(__builtin_amdgcn_readlane(l, 32), __builtin_amdgcn_readlane(l, 48));
  PIN_S(a); PIN_S(b);
  return imax(a, b);
}
__device__ __forceinline__ void glds16(const void* gsrc, unsigned lds_dst) {
  unsigned keep;
  asm volatile("s_mov_b32 %0, m0\n\ts_mov_b32 m0, %2\n\ts_nop 0\n\tglobal_load_lds_dwordx4 %1, off\n\ts_mov_b32 m0, %0" : "=&s"(keep) : "v"(gsrc), "s"(lds_dst) : "memory");
}
__device__ __forceinline__ void glds16_sc1(const void* gsrc, unsigned lds_dst) {
  unsigned keep;
  asm volatile("s_mov_b32 %0, m0\n\ts_mov_b32 m0, %2\n\ts_nop 0\n\tglobal_load_lds_dwordx4 %1, off sc1\n\ts_mov_b32 m0, %0" : "=&s"(keep) : "v"(gsrc), "s"(lds_dst) : "memory");
}
__device__ __forceinline__ unsigned acc_off(int G, int row) {
  return (unsigned)(G < 128 ? ((G & 31) | ((G & 32) << 1) | ((G & 64) >> 1)) * 256 + row * 16 : (2 * G - 128 + (row >> 2)) * 256 + (row & 3) * 16);
}
__device__ __forceinline__ unsigned nz(unsigned v) { return v > 1u ? v : 1u; }
__device__ __forceinline__ unsigned pack2n(float lo, float hi) { return nz(pack2(lo, hi)); }
__device__ __forceinline__ unsigned min4(u32x4 v) { return umin(umin(v[0], v[1]), umin(v[2], v[3])); }
__device__ __forceinline__ bf16x8 as8(u32x4 v) { return __builtin_bit_cast(bf16x8, v); }

constexpr int NV = 7, NKQ = 2;
constexpr int L_RED1 = 0;
constexpr int L_BM = L_RED1 + 8 * 1536;
constexpr int L_STG = L_BM + 10240;
constexpr int L_ST = L_STG + 2048 + 2560;
constexpr int L_MISC = L_ST + 512;
constexpr int L_KX = L_MISC + 128;
constexpr int L_QX = L_KX + NKQ * 2048;
constexpr int L_XN = L_QX + NKQ * 2048;
constexpr int L_VOX = L_XN + 81920;
constexpr int L_VOM = L_VOX + NV * 5120;
constexpr int L_BYTES = L_VOM + NV * 1280;
constexpr int S_IMG = L_XN + 40960;
static_assert(S_IMG + 61440 <= L_VOM, "start-up image");
static_assert(L_BYTES <= 160 * 1024, "LDS budget");

__device__ __forceinline__ bf16x8 wqk_frag(const char* lds, int t, int ks, int lane) {
  const int c = lane & 15, g = lane >> 4, p = c & 3;
  const char* q = lds + S_IMG + (32 * ks + 8 * g + (c >> 2)) * 96 + 8 * p + (t == 0 ? 0 : (t == 1 ? 48 : (p < 2 ? 32 : 64)));
  const v4i16_t lo = __builtin_amdgcn_ds_read_tr16_b64_v4i16((LAS v4i16_t*)q), hi = __builtin_amdgcn_ds_read_tr16_b64_v4i16((LAS v4i16_t*)(q + 4 * 96));
  return (bf16x8){lo[0], lo[1], lo[2], lo[3], hi[0], hi[1], hi[2], hi[3]};
}
__device__ __forceinline__ bf16x8 sel_frag(int c, int base) {
  bf16x8 r;
#pragma unroll
  for (int i = 0; i < 8; ++i) r[i] = (c == base + i) ? (short)0x3f80 : (short)0;
  return r;
}
__device__ __forceinline__ bf16x8 qk_frag(const char* p4, const char* p2, const char* p2b) {
  f32x4 a0 = *(const f32x4*)(p4), a1 = *(const f32x4*)(p4 + 1792), a2 = *(const f32x4*)(p4 + 3584), a3 = *(const f32x4*)(p4 + 5376);
  f32x2 b0 = *(const f32x2*)(p2), b1 = *(const f32x2*)(p2 + 1792), b2 = *(const f32x2*)(p2b), b3 = *(const f32x2*)(p2b + 1792);
  f32x4 va = (a0 + a1) + (a2 + a3);
  f32x2 vb = (b0 + b1) + (b2 + b3);
  return mk8(pack2(va[0], va[1]), pack2(va[2], va[3]), pack2(vb[0], vb[1]), 0u);
}
__device__ __forceinline__ void x_fetch(const u16* src, unsigned lds0, int seg, int p, int lane) {
  if (EXP_NODMA) return;
  glds16_sc1(src + (size_t)p * 512 + lane * 8, lds0 + L_XN + (seg & 1) * 40960 + p * 1024);
}
template <int N> __device__ __forceinline__ void x_fetch_n(const u16* src, unsigned lds0, int seg, int p0, unsigned voff) {
  if (EXP_NODMA) return;
#pragma unroll
  for (int i = 0; i < N; i += 4) {
    unsigned keep;
    asm volatile("s_mov_b32 %0, m0\n\ts_mov_b32 m0, %3\n\ts_nop 0\n\tglobal_load_lds_dwordx4 %1, %2 sc1\n\t"
                 ".if %4 > 1\n\tglobal_load_lds_dwordx4 %1, %2 offset:1024 sc1\n\t.endif\n\t.if %4 > 2\n\tglobal_load_lds_dwordx4 %1, %2 offset:2048 sc1\n\t.endif\n\t"
                 ".if %4 > 3\n\tglobal_load_lds_dwordx4 %1, %2 offset:3072 sc1\n\t.endif\n\ts_mov_b32 m0, %0"
                 : "=&s"(keep) : "v"(voff), "s"(src + (size_t)(p0 + i) * 512), "s"(lds0 + L_XN + (seg & 1) * 40960 + (p0 + i) * 1024), "n"(N - i < 4 ? N - i : 4) : "memory");
  }
}
__device__ __forceinline__ int qkv_row(int b) { return (b & 7) / QR * (2 * QR) + (b >> 3) / (16 / QR); }
__device__ __forceinline__ int qkv_col(int b) { return (b & 7) % QR * (16 / QR) + (b >> 3) % (16 / QR); }
__device__ __forceinline__ const u16* x_src(const RecArgs& a, int seg, int b) {
  return seg < NSEG ? (const u16*)(a.ws + OFF_XN) + (size_t)seg * 32 * DM : a.xnew + (size_t)(qkv_row(b) + 16 * (seg - NSEG)) * 32 * DM;
}
template <int N>
__device__ __forceinline__ void img_fetch(const RecArgs& a, unsigned lds0, int h, int j, int part, int lane) {
#pragma unroll
  for (int i = 0; i < N; ++i) {
    const int n = part * N + i, idx = n * 64 + lane, row = idx / 6, pc = idx - row * 6;
    if (FR_PACE && N == 20) asm volatile("s_waitcnt vmcnt(%0)" :: "n"(FR_PACE) : "memory");
    glds16((pc < 3 ? a.wq : a.wk - 24) + (size_t)row * HD + h * DH + j * 24 + pc * 8, lds0 + S_IMG + n * 1024);
  }
}
constexpr int P2B = F4 + F5 + 2 * F67 + F6 + F7, PZ = P2B + F2B, PA = PZ, P0C = PA + 2 * F0A, F3C = 40 - F3A - F3 - P0C - 2 * F0C - F2C;
static_assert(F3C >= 0, "fetch plan");
template <int OFF> __device__ __forceinline__ void lds_inc(unsigned lds0) {
  asm volatile("s_mov_b64 exec, 1\n\tds_add_u32 %0, %1 offset:%2\n\ts_mov_b64 exec, -1" :: "v"(lds0), "v"(1u), "n"(OFF) : "memory");
}
__device__ __forceinline__ void lds_arrive(unsigned* p, int lane) {
  asm volatile("s_waitcnt lgkmcnt(0)" ::: "memory");
  if (lane == 0) __hip_atomic_fetch_add(p, 1u, __ATOMIC_RELAXED, __HIP_MEMORY_SCOPE_WORKGROUP);
}
__device__ __forceinline__ void lds_wait(unsigned* p, unsigned want) {
  while (__hip_atomic_load(p, __ATOMIC_RELAXED, __HIP_MEMORY_SCOPE_WORKGROUP) < want) __builtin_amdgcn_s_sleep(1);
  asm volatile("" ::: "memory");
}
constexpr float KEXP = 0.07216878364870322f * 1.4426950408889634f;
#define FRESH_LANE() int lane = lane0; asm volatile("" : "+v"(lane)); const int c = lane & 15, g = lane >> 4; (void)c; (void)g

struct OutTask {
  u32x4 y[4]; u32x2 xo, out;
  bf16x8 sel;
  unsigned t5f, t5o;
  __device__ __forceinline__ void init(int b, int lane) {
    const int um = b / 10, uq = (b / 5) & 1, up = b % 5, tok = lane & 15, k = up < 4 ? (up >> 1) * 256 + um * 16 + (up & 1) * 8 : 512 + um * 8, q4 = ((lane >> 4) & 1) * 4;
    t5f = (unsigned)(((uq * 20 + (k >> 5)) * 64 + ((k & 31) >> 3) * 16 + tok) * 8 + q4);
    t5o = (unsigned)(((um * 2 + uq) * 16 + (lane >> 4)) * 1280 + up * 256 + tok * 16);
    if (b >= 160) { t5f = 0u; t5o = 0x7ffffff0u; }
    sel = sel_frag(tok, 0);
    PIN_V(sel);
  }
  static __device__ __forceinline__ bool ok(int sl, int b) { return sl >= 0 && b < 160; }
  __device__ __forceinline__ void load(const RecArgs& a, const __amdgpu_buffer_rsrc_t rs, int sl, int b, int part = 3) {
    const unsigned o = t5o | (sl < 0 ? 0x7ff00000u : 0u);
    sl = imax(sl, 0);
#pragma unroll
    for (int k = 0; k < 4; ++k) if (part & 1) y[k] = SC1_LOADS(o, op_slot(sl) + k * 5120);
    if (part & 2) xo = __builtin_bit_cast(u32x2, __hip_atomic_load((const gu64*)(a.xold + (size_t)sl * 32 * DM + t5f), RLX_AGENT));
  }
  __device__ __forceinline__ bool ready(int b) { return b >= 160 || wave_all(umin(umin(min4(y[0]), min4(y[1])), umin(min4(y[2]), min4(y[3]))) != 0u); }
  __device__ __forceinline__ void run(int lane) {
    f32x4 d = {0.f, 0.f, 0.f, 0.f};
#pragma unroll
    for (int k = 0; k < 4; ++k) d = mfma32(sel, as8(y[k]), d);
#pragma unroll
    for (int i = 0; i < 2; ++i) {
      const unsigned r = pack2(d[2 * i], d[2 * i + 1]), x = xo[i];
      out[i] = pack2(u2f(x << 16) + u2f(r << 16), u2f(x & 0xffff0000u) + u2f(r & 0xffff0000u));
    }
    PIN_V(out);
  }
  __device__ __forceinline__ void store(const RecArgs& a, int sl, int b, int lane) {
    __builtin_amdgcn_raw_buffer_store_b64(out, __builtin_amdgcn_make_buffer_rsrc(a.xnew, 0, ok(sl, b) ? S_ * DM * 2 : 0, 0x00020000), (t5f + (unsigned)sl * 32 * DM) * 2u | (unsigned)(lane & 32) << 25, 0, 16);
  }
};

struct PvTask2 {
  u32x4 y[2][2], pa[2], pb[2];
  unsigned lo, lm, vr, ma, mz, so, sz;
  __device__ __forceinline__ void init(int gp, int lane) {
    const int hi = lane >> 5;
    lo = pt_tok(2 * gp) + (unsigned)(lane * 16); lm = pt_mem(2 * gp + hi) + (unsigned)((lane & 31) * 8);
    vr = (unsigned)(L_VOX + 2048 + (lane ^ 8) * 16);
    ma = (unsigned)(L_VOM + hi * 768 + (lane & 31) * 8); mz = (unsigned)(L_VOM + 512 + ((lane & 31) ^ (hi * 8)) * 8);
    so = (unsigned)(L_STG + 2048 + (hi * 16 + (lane & 15)) * 16 + ((lane >> 4) & 1) * 8);
    sz = hi ? (unsigned)(L_RED1 + 8704 + lane * 8) : so + 1024;
    PIN_V(lo); PIN_V(lm); PIN_V(vr); PIN_V(ma); PIN_V(mz); PIN_V(so); PIN_V(sz);
#pragma unroll
    for (int q = 0; q < 2; ++q) { pa[q] = pb[q] = y[q][0] = y[q][1] = (u32x4){0u, 0u, 0u, 0u}; PIN_V(pa[q]); PIN_V(pb[q]); }
  }
  __device__ __forceinline__ void load(const __amdgpu_buffer_rsrc_t rs, unsigned o) {
#pragma unroll
    for (int q = 0; q < 2; ++q) {
      y[q][0] = SC1_LOADS(lo + q * 1024, o); y[q][1] = SC1_LOADS(lo + 2048 + q * 1024, o);
      const u32x2 p = __builtin_amdgcn_raw_buffer_load_b64(rs, lm + q * 256, o, 16);
      pa[q][0] = p[0]; pa[q][1] = p[1];
    }
  }
  __device__ __forceinline__ bool ready() {
    return wave_all(umin(umin(umin(min4(y[0][0]), min4(y[0][1])), umin(min4(y[1][0]), min4(y[1][1]))), umin(umin(pa[0][0], pa[0][1]), umin(pa[1][0], pa[1][1]))) != 0u);
  }
  template <bool DIRECT> __device__ __forceinline__ void run(char* lds, int sv, int lane, const __amdgpu_buffer_rsrc_t rs, unsigned og) {
    const f32x4 Z4 = {0.f, 0.f, 0.f, 0.f};
    const char* vp = lds + L_VOX + sv * 5120 + lane * 16;
    bf16x8 vx[6];
#pragma unroll
    for (int t = 0; t < 5; ++t) vx[t] = *(const bf16x8*)(vp + t * 1024);
    vx[5] = *(const bf16x8*)(lds + vr + sv * 5120);
    const u32x2 a0 = *(const u32x2*)(lds + ma + sv * 1280), a1 = *(const u32x2*)(lds + ma + sv * 1280 + 256);
    const u32x2 z0 = *(const u32x2*)(lds + mz + sv * 1280), z1 = *(const u32x2*)(lds + mz + sv * 1280 + 256);
    const bf16x8 vm = mk8(a0[0], a0[1], a1[0], a1[1]), vz = mk8(z0[0], z0[1], z1[0], z1[1]);
#pragma unroll
    for (int q = 0; q < 2; ++q) { PIN_V(pa[q]); pb[q][2] = pa[q][0]; pb[q][3] = pa[q][1]; PIN_V(pb[q]); }
    __builtin_amdgcn_sched_barrier(0);
    f32x4 o[2][3];
#pragma unroll
    for (int q = 0; q < 2; ++q) {
#pragma unroll
      for (int t = 0; t < 3; ++t) o[q][t] = mfma32(vx[t], as8(y[q][0]), Z4);
#pragma unroll
      for (int t = 0; t < 3; ++t) o[q][t] = mfma32(vx[3 + t], as8(y[q][1]), o[q][t]);
      o[q][0] = mfma32(vm, as8(pa[q]), o[q][0]); o[q][1] = mfma32(vm, as8(pb[q]), o[q][1]); o[q][2] = mfma32(vz, as8(pa[q]), o[q][2]);
    }
#pragma unroll
    for (int q = 0; q < 2; ++q) {
#pragma unroll
      for (int t = 0; t < 3; ++t) {
        const u32x2 v = {pack2n(o[q][t][0], o[q][t][1]), pack2n(o[q][t][2], o[q][t][3])};
        if (DIRECT) SC1_STORE8S(v, (so - (L_STG + 2048) + t * 512) | (t == 2 ? (unsigned)(lane & 32) << 25 : 0u), og + q * 20480);
        else *(u32x2*)(lds + (t < 2 ? so + t * 512 : sz) + q * 1280) = v;
      }
    }
  }
};

struct TokScores {
  bf16x8 k[2], q[2]; u32x4 v[2];
  __device__ __forceinline__ void get(const char* lds, int seg, int lane) {
#pragma unroll
    for (int i = 0; i < 2; ++i) { k[i] = *(const bf16x8*)(lds + L_KX + (seg & 1) * 2048 + i * 1024 + lane * 16); q[i] = *(const bf16x8*)(lds + L_QX + (seg & 1) * 2048 + i * 1024 + lane * 16); }
  }
  __device__ __forceinline__ void run(int qt) {
    const f32x4 Z4 = {0.f, 0.f, 0.f, 0.f};
    const f32x4 d1 = mfma32(k[0], q[qt], Z4), d2 = mfma32(k[1], q[qt], Z4);
    v[qt] = (u32x4){pack2n(d1[0], d1[1]), pack2n(d1[2], d1[3]), pack2n(d2[0], d2[1]), pack2n(d2[2], d2[3])};
    PIN_V(v[qt]);
  }
  __device__ __forceinline__ void store(const __amdgpu_buffer_rsrc_t rs, unsigned off, unsigned o) {
    __builtin_amdgcn_raw_buffer_store_b128(v[0], rs, off, o, 16); __builtin_amdgcn_raw_buffer_store_b128(v[1], rs, off + 1024, o, 16);
    asm volatile("s_nop 1" ::: "memory");
  }
};

__device__ __forceinline__ void touch(const char* base, unsigned off, unsigned lds0) {
  unsigned keep;
  asm volatile("s_mov_b32 %0, m0\n\ts_mov_b32 m0, %3\n\ts_nop 0\n\tglobal_load_lds_dword %1, %2 sc1\n\ts_mov_b32 m0, %0" : "=&s"(keep) : "v"(off), "s"(base), "s"(lds0 + L_RED1 + 8192) : "memory");
}
template <int N> __device__ __forceinline__ void stouch(const char* p, unsigned& d) {
  asm volatile(".set pf_o, 0\n\t.rept %2\n\ts_load_dword %0, %1, pf_o\n\t.set pf_o, pf_o + 128\n\t.endr" : "+s"(d) : "s"(p), "n"(N) : "memory");
}
__device__ __forceinline__ void code_touch(int b, int lane, unsigned lds0) {
  const char* pc; int d0, d1;
  asm volatile("s_getpc_b64 %0\n\ts_mov_b32 %1, _Z10rec_kernel7RecArgs@rel32@lo+4\n\ts_mov_b32 %2, _Z11attb_kernelPKtS0_Pt@rel32@lo+12" : "=s"(pc), "=s"(d0), "=s"(d1));
  const int o = (lane * 32 + (b >> 3)) * 128;
  touch(pc + d0, (unsigned)(o < d1 - d0 ? o : 0), lds0);
}
__device__ __forceinline__ unsigned zero_base(int b, int h, int j, int ln) {
  return ln < 32 ? (unsigned)((h & 3) * 32768 + j * 512 + ln * 16) : ln < 40 ? (unsigned)((h & 2) * 1024 + j * 256 + (ln & 4) * 32 + (h & 1) * 64 + (ln & 3) * 16)
                 : (unsigned)OFF_ACC + (b < ACC_NB ? (unsigned)(b * 256 + (ln - 40) * 16) : ts_piece((b - ACC_NB) * 8 + ln - 40));
}
__device__ __forceinline__ unsigned sct_slot(unsigned ck, unsigned sl) { return sl * SCT_STEP + ((ck >> (sl & 4u)) & 7u) * 4096u; }
__device__ __forceinline__ unsigned scm_slot(unsigned ck, unsigned sl) { return OFF_SCM + sl * SCM_STEP + ((ck >> 8) & 7u) * 4096u; }
__device__ __forceinline__ int ck_rot(int x) { return ((x & 1) << 2) | (x >> 1); }
struct Place {
  unsigned t[8];
  __device__ __forceinline__ void load(const char* ws, int lane) {
#pragma unroll
    for (int x = 0; x < 8; ++x) t[x] = *(const unsigned*)(ws + OFF_LAT + ((imin(lane >> 3, 5) * 8 + (ck_rot(x) ^ (lane & 7))) * 8 + x) * 4);
  }
  __device__ __forceinline__ unsigned get(int b, int lane) {
    int c = (int)((((t[0] + t[1]) + (t[2] + t[3])) + ((t[4] + t[5]) + (t[6] + t[7]))) * 8u) + (lane & 7);
    c = imin(c, dppi<DPP_XOR1>(c)); c = imin(c, dppi<DPP_XOR2>(c)); c = imin(c, dppi<0x141>(c));
    unsigned k = 0u;
#pragma unroll
    for (int r = 0; r < 6; ++r) k |= (unsigned)(__builtin_amdgcn_readlane(c, 8 * r) & 7) << (4 * r);
    return (k ^ PL_X) ^ (unsigned)ck_rot(b & 7) * 0x111111u;
  }
};

struct Sched {
  u32x4 tsv, rec;
  unsigned side, o;
  __device__ __forceinline__ void pre(int s) { o = (unsigned)OFF_ACC + (unsigned)((s + SR - 1) % SR) * (unsigned)ACC_STEP + ACC_TS + side; PIN_S(o); }
  __device__ __forceinline__ void load(const __amdgpu_buffer_rsrc_t rs, int lane) {
    tsv = SC1_LOADS(ts_piece(lane & 63) - ACC_TS, o); rec = SC1_LOADS(128, o);
  }
  __device__ __forceinline__ unsigned last(unsigned ref) {
    const unsigned o = 0x40000000u - ref;
    int l = (int)umax(umax(tsv[0] ? tsv[0] + o : 0u, tsv[1] ? tsv[1] + o : 0u), umax(tsv[2] ? tsv[2] + o : 0u, tsv[3] ? tsv[3] + o : 0u));
    return (unsigned)wave_imax(l) - o;
  }
  __device__ __forceinline__ unsigned run(const RecArgs& a, const __amdgpu_buffer_rsrc_t rs, int s, int b, int lane) {
    constexpr int w = 3; (void)w;
    unsigned acur = __builtin_amdgcn_readfirstlane(rec[0]), aprev = __builtin_amdgcn_readfirstlane(rec[1]), hist = __builtin_amdgcn_readfirstlane(rec[3]);
    int D8 = (int)__builtin_amdgcn_readfirstlane(rec[2]);
    const int d = wave_imax((int)(umax(umax(tsv[0], tsv[1]), umax(tsv[2], tsv[3])) - aprev)), dd = 8 * d - D8;
    int Dn = D8 + (dd >> (dd > 0 ? SCH_GU : SCH_GD));
    unsigned anext = (acur + (unsigned)((Dn + 4 + imax(Dn - 8 * SCH_DR, 0) * SCH_DK / 8) >> 3) + SCH_V + (s < EARLY_N ? EARLY_V : (s >= WPF0 ? (s == NSEG - 2 ? LAST_V : LATE_V) : 0))) | 1u;
    if (__builtin_expect(hist < 2u || aprev == 0u || d < 1 || d + SCH_VMIN > (int)(acur - aprev), 0)) {
      if (s == 0) {
        for (int it = 0; it < 40 && !wave_all(min4(tsv) != 0u); ++it) {
          __builtin_amdgcn_s_sleep(ST_ALL);
          asm volatile("" ::: "memory");
          load(rs, lane);
        }
      }
      const unsigned now = now32(), L = last(now);
      if (acur == 0u) { D8 = 8 * SCH_D0; hist = 0u; }
      const bool late = acur == 0u || (int)(L + SCH_VMIN - acur) > 0;
      if (late) { if (b == 0) FC(3); if (acur) D8 += 8 * (hist ? SCH_UP : 3 * SCH_UP); }
      else if (aprev) { if (hist < 2u) D8 = 8 * imin(imax((int)(L - aprev), 1), 4000); hist = umin(hist + 1u, 2u); }
      const unsigned base = acur == 0u ? L + SCH_FIRST : (late ? L + SCH_V + ((int)(L + SCH_VMIN2 - acur) <= 0 ? SCH_PEN2 : SCH_PEN) : acur);
      anext = (base + (unsigned)((D8 + 4) >> 3) + SCH_V + (s == 1 ? EARLY_V1 : (s < EARLY_N ? EARLY_V : 0))) | 1u;
      if (s == 0) anext = (L + ST_X) | 1u;
      if ((int)(now - L) > 50000) anext = 0u;
      if (late) acur = 0u;
      Dn = D8;
    }
    if (__builtin_expect(b == 0, 0))
      __builtin_amdgcn_raw_buffer_store_b128((u32x4){anext, acur, (unsigned)Dn, hist}, rs, lane < 2 ? lane * 8192 : 0x7ffffff0, (unsigned)OFF_ACC + (unsigned)(s % SR) * (unsigned)ACC_STEP + ACC_TS + 128u, 16);
    LOGV(0, anext); LOGV(1, acur == 0u); LOGV(2, (Dn + 4) >> 3); LOGV(3, d); LOGV(4, acur - aprev - d); LOGV(5, aprev);
    return anext;
  }
};

struct TokTask {
  u32x4 y[2]; u32x2 ym; u32x4 res;
  unsigned l0, l1, lm, s0, s1;
  static __device__ __forceinline__ bool ok(int sl) { return sl >= 0 && sl < NSEG; }
  __device__ __forceinline__ void init(int h, int j, int lane) {
    const int c = lane & 15, g = lane >> 4;
    l0 = ts_tok(h, g) + (unsigned)(j * 256 + c * 16); l1 = l0 + 16384u;
    lm = ts_mem(h, g + 4 * (c >> 3)) + (unsigned)(j * 64 + (c & 7) * 8);
    res = (u32x4){0u, 0u, 0u, 0u};
    const int qi = 4 * j + (c & 3);
    s0 = g < 2 ? pt_tok(h) + (unsigned)((qi >> 4) * 1024 + ((c >> 2) * 16 + (qi & 15)) * 16 + g * 8) : 0x7ffffff0u;
    s1 = lane < 8 ? pt_mem(h) + (unsigned)((qi >> 4) * 256 + ((c >> 2) * 16 + (qi & 15)) * 8) : 0x7ffffff0u;
    PIN_V(l0); PIN_V(l1); PIN_V(lm); PIN_V(s0); PIN_V(s1);
  }
  __device__ __forceinline__ void load(const __amdgpu_buffer_rsrc_t rs, unsigned o) {
    y[0] = SC1_LOADS(l0, o); y[1] = SC1_LOADS(l1, o);
    ym = __builtin_amdgcn_raw_buffer_load_b64(rs, lm, o, 16);
  }
  __device__ __forceinline__ bool ready() { return wave_all(umin(umin(min4(y[0]), min4(y[1])), umin(ym[0], ym[1])) != 0u); }
  __device__ __forceinline__ void run(int j, int lane, const bf16x8 SelLo) {
    const int c = lane & 15, g = lane >> 4;
    const f32x4 Z4 = {0.f, 0.f, 0.f, 0.f};
    const f32x4 d1 = mfma32(SelLo, as8(y[1]), mfma32(SelLo, as8(y[0]), Z4));
    const f32x4 d0 = mfma32(SelLo, mk8(ym[0], ym[1], 0u, 0u), Z4);
    const int qi = 4 * j + (c & 3), k0 = 16 * g + 4 * (c >> 2);
    float t[4], mv[4];
    float mx = -INFINITY;
#pragma unroll
    for (int r = 0; r < 4; ++r) {
      t[r] = (g < 2 && k0 + r <= qi) ? d1[r] : -INFINITY;
      const float mt = d0[r] + dppf<DPP_ROR8>(d0[r]);
      mv[r] = lane < 8 ? mt : -INFINITY;
      mx = fmx(mx, fmx(t[r], mv[r]));
    }
    mx = fmx(mx, dppf<DPP_ROR8>(mx)); mx = fmx(mx, dppf<DPP_ROR4>(mx));
    mx = x16mx(mx);
    float sum = 0.f;
#pragma unroll
    for (int r = 0; r < 4; ++r) {
      t[r] = __builtin_amdgcn_exp2f(t[r] - mx); mv[r] = __builtin_amdgcn_exp2f(mv[r] - mx);
      sum += t[r] + mv[r];
    }
    sum += dppf<DPP_ROR8>(sum); sum += dppf<DPP_ROR4>(sum);
    sum = x16sum(sum);
    const float rs_ = __builtin_amdgcn_rcpf(sum);
    res = (u32x4){pack2n(t[0] * rs_, t[1] * rs_), pack2n(t[2] * rs_, t[3] * rs_), pack2n(mv[0] * rs_, mv[1] * rs_), pack2n(mv[2] * rs_, mv[3] * rs_)};
    PIN_V(res);
  }
  __device__ __forceinline__ void store(const __amdgpu_buffer_rsrc_t rs, unsigned o, unsigned no = 0u) {
    SC1_STORE8S(((u32x2){res[0], res[1]}), s0 | no, o);
    SC1_STORE8S(((u32x2){res[2], res[3]}), s1 | no, o);
  }
};

constexpr int L_DRN = L_VOX + (NSEG % NV) * 5120;
__device__ __forceinline__ void drain_pre(char* lds, int b, int lane) {
  const int gp = (b & 7) * 2 + (b >> 7), m = (b >> 3) & 15;
  TokTask tk; PvTask2 pv; OutTask ot;
  tk.init(2 * gp + (m >> 3), m & 7, lane); pv.init(gp, lane); ot.init(b, lane);
  u32x4* p = (u32x4*)(lds + L_DRN) + lane;
  p[0] = (u32x4){tk.l0, tk.lm, tk.s0, tk.s1}; p[64] = (u32x4){pv.lo, pv.lm, pv.vr, pv.ma}; p[128] = (u32x4){pv.mz, pv.so, ot.t5f, ot.t5o};
  p[192] = __builtin_bit_cast(u32x4, ot.sel);
}
__device__ __forceinline__ void qkv_pre(unsigned (&vo)[2][2], int b, int w, int lane) {
  const int cb = qkv_col(b), c = lane & 15, g = lane >> 4, t0 = w & 1, col0 = (cb % 5) * 128 + w * 32;
  const bool SW = cb < 10;
#pragma unroll
  for (int i = 0; i < 2; ++i)
#pragma unroll
    for (int tt = 0; tt < 2; ++tt) {
      const int tq = tt ^ t0, tk = tq * 16 + c, cc = col0 + i * 16 + (SW ? 4 * g : c), hd = cc / BD, e = cc - hd * BD;
      const int kv = (int)(OFF_KV - OFF_QB) + hd * 64 * KVT;
      vo[i][tt] = (unsigned)(!SW ? kv + 1280 + tq * 672 + ((g >> 1) * 21 + e) * 16 + (g & 1) * 8
                                 : (cb < 5 ? (hd * S_ + tk) * 2 * QST + e * 2 : kv + (e < 16 ? (tk + 32 * (e >> 3)) * 16 + (e & 7) * 2 : 1024 + tk * 8)));
      PIN_V(vo[i][tt]);
    }
}

template <int w>
__device__ __forceinline__ void token_waves(const RecArgs& a, char* lds, const __amdgpu_buffer_rsrc_t rs, unsigned (&vo)[2][2]) {
  const int lane0 = __builtin_amdgcn_workitem_id_x() & 63, b = __builtin_amdgcn_workgroup_id_x(), wid = w + 4; (void)wid;
  const int gp = (b & 7) * 2 + (b >> 7), m = (b >> 3) & 15, h0 = 2 * gp, h = h0 + (m >> 3), j = m & 7;
  float* const misc = (float*)(lds + L_MISC);
  const f32x4 Z4 = {0.f, 0.f, 0.f, 0.f};
  const unsigned lds0 = (unsigned)(uintptr_t)(LAS char*)lds;
  unsigned* const cnt = (unsigned*)(misc + 6);
  const float* const xnrm = (const float*)(a.ws + OFF_XNRM);
  bf16x16 W[2][10];
  auto btile = [&](int i) { return w == 1 ? 0 : (w == 2 ? 1 + i : 3 + i); };
  if (w == 1) code_touch(b, lane0, lds0);
  Place pl;
  if (w == 3) pl.load(a.ws, lane0);
  if (a.layer) {
    if (w != 0) img_fetch<20>(a, lds0, h, j, w - 1, lane0);
  } else {
    if (w == 0) img_fetch<12>(a, lds0, h, j, 4, lane0);
#pragma unroll
    for (int i = 0; i < 10; ++i) x_fetch(x_src(a, 0, b), lds0, 0, 10 * w + i, lane0);
  }
#pragma unroll
  for (int i = 0; i < 2; ++i)
#pragma unroll
    for (int p = 0; p < 10; ++p)
      if (w >= 2 || (w == 1 && i == 1)) {
        const char* wp = a.ws + OFF_WB + ((size_t)(b * 5 + btile(i)) * 20 + 2 * p) * 1024 + lane0 * 16;
        if (FR_PACE && a.layer) asm volatile("s_waitcnt vmcnt(%0)" :: "n"(FR_PACE) : "memory");
        W[i][p] = cat16(*(const bf16x8*)wp, *(const bf16x8*)(wp + 1024));
      }
  f32x4 bn = Z4;
  if (w == 0) {
    bn = *(const f32x4*)(a.ws + OFF_BNRM + lane0 * 16);
    if (!a.layer) asm volatile("s_waitcnt vmcnt(0)" ::: "memory");
  } else asm volatile("s_waitcnt vmcnt(%0)" :: "n"(w == 1 ? 20 : 40) : "memory");
  unsigned ck = 0u;
  if (w == 3) { ck = pl.get(b, lane0); *(unsigned*)(misc + 11) = ck; }
  BAR();
#pragma unroll
  for (int p = 0; p < 10; ++p) {
    if (w < 2) W[0][p] = cat16(wqk_frag(lds, 2 * w, 2 * p, lane0), wqk_frag(lds, 2 * w, 2 * p + 1, lane0));
    if (w == 0) W[1][p] = cat16(wqk_frag(lds, 1, 2 * p, lane0), wqk_frag(lds, 1, 2 * p + 1, lane0));
  }
  float cb2 = 0.f;
  if (w == 0) {
    float n = __builtin_amdgcn_sqrtf(bn[0] + bn[1]) + __builtin_amdgcn_sqrtf(bn[2] + bn[3]);
    n += dppf<DPP_XOR1>(n); n += dppf<DPP_XOR2>(n); n += dppf<DPP_ROR4>(n); n += dppf<DPP_ROR8>(n);
    n = x32mx(x16mx(n));
    cb2 = u2f(__builtin_amdgcn_readfirstlane(f2u(1.15f * n * n)));
  }
  const float cm2 = u2f((f2u(cb2) & 0x7fffffu) | 0x40000000u);
  const int ek = imax((int)((f2u(cb2) >> 23) & 0xffu), 1) - 252;
  if (w >= 2) {
#pragma unroll
    for (int i = 0; i < 4; ++i) *(unsigned*)(lds + L_KX + (w - 2) * 4096 + i * 1024 + lane0 * 16 + 12) = 0u;
  }
  OutTask ot;
  const int zl = lane0 - 16;
  const bool zon = w == 3 && zl >= 0 && (zl < 40 || b < ACC_NB + 17);
  unsigned zo = zon ? zero_base(b, h, j, zl) + (zl < 32 ? sct_slot(ck, 0u) : (zl < 40 ? scm_slot(ck, 0u) : 0u)) : 0x7ffffff0u;
  unsigned zm = zon ? (zl < 32 ? SCT_STEP + 1u : (zl < 40 ? SCM_STEP : (unsigned)ACC_STEP)) : 0u;
  const unsigned zx = ((ck ^ (ck >> 4)) & 7u) * 4096u;
  if (w == 3) { PIN_V(zo); PIN_V(zm); }
  auto st_out = [&](int s, int lane, auto go) {
    if (w < 2) return;
    if (ST_E) lds_spin<L_MISC + 112, 1>(lds0, (unsigned)imax(s, 0)); else go();
    const int sl = s - 2 - LAG4;
    const char* sp = lds + L_STG + 2048 + (w & 1) * 1280;
    u32x4 v0 = *(const u32x4*)(sp + lane * 16), v1 = *(const u32x4*)(sp + 1024 + (lane & 15) * 16);
    const u32x4 z = {0u, 0u, 0u, 0u};
    unsigned oo = (sl < 0 ? 0x7ff00000u : op_slot(sl) + (unsigned)((m * 32 + (w & 1) * 16 + gp) * 1280)) + (unsigned)(lane * 16), za = 0x7ffffff0u;
    if (w == 3) {
      const unsigned zs = (unsigned)(s + 2) % SR;
      za = (zo + (s < 0 ? 1024u : zs) * (zm & ~1u)) ^ ((zm & 1u) * (zs & 4u ? zx : 0u));
#pragma unroll
      for (int i = 0; i < 4; ++i) { v1[i] = lane < 16 ? v1[i] : z[i]; PIN_V(v1[i]); }
    }
    za = lane < 16 ? oo + 1024 : za;
    PIN_V(za); PIN_V(oo); PIN_V(v0); PIN_V(v1);
    __builtin_amdgcn_sched_barrier(0);
    if (ST_E) go();
    SC1_STORE(v0, oo);
    SC1_STORE(v1, za);
    __builtin_amdgcn_sched_barrier(0);
    if (w == 2) ot.store(a, s - 2 - LAG5, b, lane);
    else {
      const unsigned xo_ = (unsigned)((s - 8) * 1024 + lane * 16);
      if (__builtin_expect((unsigned)(s - 8) < 20u, 0)) SC1_STORE(z, xo_ < X_BYTES / 256 ? (unsigned)(OFF_X + b * (X_BYTES / 256)) + xo_ : 0x7ffffff0u);
    }
  };
  STAMP_DECL(); ARRIVE_DECL();
  auto mma_seg = [&](int seg, int sv, int lane, auto pause) {
    const int c = lane & 15, g = lane >> 4;
    const char* xp = lds + L_XN + (seg & 1) * 40960 + lane * 16;
    f32x4 acc[2][2] = {{Z4, Z4}, {Z4, Z4}};
    bf16x8 x[4][2];
#pragma unroll
    for (int p = 0; p < 3; ++p) { x[p][0] = *(const bf16x8*)(xp + p * 1024); x[p][1] = *(const bf16x8*)(xp + (20 + p) * 1024); }
    unsigned eo[2];
#pragma unroll
    for (int ks = 0; ks < 20; ++ks) {
      if (ks == (w == 0 ? KP0 : (w == 1 ? KP1 : KP2))) { pause(); __builtin_amdgcn_sched_barrier(0); }
      if (ks == 17) {
#pragma unroll
        for (int i = 0; i < 2; ++i) {
          eo[i] = (unsigned)(w == 0 ? (i ? L_KX : L_QX) + (seg % NKQ) * 2048 + lane * 16
                                    : (w == 1 && i == 0 ? ((g >> 1) ? L_KX : L_QX) + (seg % NKQ) * 2048 + (2 * (g & 1) * 16 + c) * 16 + 8 : L_VOX + sv * 5120 + btile(i) * 1024 + lane * 16));
          if (i == 0 || w == 1) PIN_V(eo[i]);
        }
        if (w == 0) eo[1] = eo[0] + (L_KX - L_QX);
        if (w >= 2) eo[1] = eo[0] + 1024;
      }
      __builtin_amdgcn_sched_barrier(0);
#pragma unroll
      for (int q = 0; q < 4; ++q) {
        const int tt = 1 - (q >> 1), i = q & 1;
        const bool isqk = w == 0 || (w == 1 && i == 0);
        const bf16x8 wf = half8(W[i][ks >> 1], ks & 1);
        acc[i][tt] = isqk ? mfma32(wf, x[ks & 3][tt], acc[i][tt]) : mfma32(x[ks & 3][tt], wf, acc[i][tt]);
        __builtin_amdgcn_sched_barrier(0);
        if (q < 2 && ks + 3 < 20) x[(ks + 3) & 3][q] = *(const bf16x8*)(xp + (q * 20 + ks + 3) * 1024);
        __builtin_amdgcn_sched_barrier(0);
      }
    }
#pragma unroll
    for (int i = 0; i < 2; ++i) {
      if (w + i == 1) { const float ks_ = (w == 0 || (g >> 1)) ? KEXP : 1.0f; acc[i][0] *= ks_; acc[i][1] *= ks_; }
      const u32x4 v = {pack2(acc[i][0][0], acc[i][0][1]), pack2(acc[i][0][2], acc[i][0][3]), pack2(acc[i][1][0], acc[i][1][1]), pack2(acc[i][1][2], acc[i][1][3])};
      char* p = lds + eo[i];
      if (w == 0) { *(u32x2*)(p) = (u32x2){v[0], v[1]}; *(u32x2*)(p + 1024) = (u32x2){v[2], v[3]}; }
      else if (w == 1 && i == 0) { *(unsigned*)(p) = v[0]; *(unsigned*)(p + 256) = v[1]; *(unsigned*)(p + 1024) = v[2]; *(unsigned*)(p + 1280) = v[3]; }
      else *(u32x4*)p = v;
    }
  };
  unsigned vp, mp, ip;
  int sidx, ei = 127;
  float inv;
  f32x4 acc[2];
  auto vo_pre = [&](int s, int sv, int lane) {
    vp = (unsigned)(w == 0 ? L_MISC + (s & 3) * 4 : L_VOM + sv * 1280 + (btile(0) * 32 + (w == 1 ? lane & 31 : lane)) * 8);
    mp = (unsigned)(L_BM + lane * 16);
    PIN_V(vp); PIN_V(mp);
    if (w == 0) { ip = (unsigned)(L_MISC + 16 + ((s + 1) & 1) * 4); PIN_V(ip); }
    else {
      inv = misc[4 + (s & 1)];
      sidx = (lane & 8) ? 0xEEEE : 0x4444;
      acc[0] = acc[1] = Z4;
      PIN_V(sidx); PIN_V(acc[1]);
      if (w >= 2) { PIN_V(acc[0]); PIN_V(inv); }
    }
    __builtin_amdgcn_sched_barrier(0);
  };
  auto vo_mem = [&](int s, auto last) {
    __builtin_amdgcn_s_setprio(2);
    asm volatile(".rept %0\n\ts_nop 3\n\t.endr" :: "n"(w == 0 ? VO_A0 : (w == 1 ? VO_A1 : VO_A2)) : "memory");
    float xmax = 0.f;
    if (w == 0) xmax = *(const float*)(lds + vp);
    constexpr int NX = w == 2 ? VO_N2 : (w == 3 ? VO_N3 : 10);
    bf16x8 x[10];
#pragma unroll
    for (int p = 0; p < NX; ++p) x[p] = *(const bf16x8*)(lds + mp + p * 1024);
    __builtin_amdgcn_sched_barrier(0);
    if (w < 2) asm volatile(".rept %0\n\ts_nop 3\n\t.endr" :: "n"(w == 0 ? VO_B0 : VO_B1) : "memory");
    if (VO_W >= 0) asm volatile("s_waitcnt lgkmcnt(%0)" :: "n"(VO_W < 0 ? 0 : VO_W) : "memory");
    STAMP(5);
    __builtin_amdgcn_sched_barrier(0);
    int ex = 0;
#pragma unroll
    for (int p = 0; p < 10; ++p) {
      if (w == 0) acc[0] = mfma32(x[p], x[p], p ? acc[0] : Z4);
      if (w == 0 && p == 9) { ex = imax((int)((__builtin_amdgcn_readfirstlane(f2u(__builtin_amdgcn_sqrtf(cb2 * xmax))) >> 23) & 0xffu) + 1, 32); PIN_S(ex); }
#pragma unroll
      for (int i = (w == 1); i < 2 && w != 0; ++i) acc[i] = smfma64(x[p], W[i][p], acc[i], sidx);
      __builtin_amdgcn_sched_barrier(0);
      if (p + NX < 10) x[p + NX] = *(const bf16x8*)(lds + mp + (p + NX) * 1024);
      __builtin_amdgcn_sched_barrier(0);
    }
    STAMP(7);
    if (w != 0) {
      float v[4];
#pragma unroll
      for (int r = 0; r < 4; ++r) {
        const auto sw = __builtin_amdgcn_permlane32_swap(f2u(acc[w == 1][r]), f2u(acc[1][r]), false, false);
        v[r] = (u2f(sw[0]) + u2f(sw[1])) * inv;
      }
      *(u32x2*)(lds + vp) = (u32x2){pack2(v[0], v[1]), pack2(v[2], v[3])};
    } else {
      const int t = (int)f2u(fmx(fmx(acc[0][0], acc[0][1]), fmx(acc[0][2], acc[0][3])) * cm2);
      const int et = wave_imax(t) >> 23, em = ((et + ek) >> 1) + ei;
      int e = imax(et == 255 ? 256 : em, ex);
      PIN_S(e);
      e = imin(e, 256);
      ei = e - 31;
      *(unsigned*)(lds + ip) = (unsigned)ei << 23;
      const u32x3 sr = {(unsigned)(285 - e) << 23, (unsigned)(e - 87) < 79u ? REC_RNG << 23 : 0u, (unsigned)(s + 1)};
      asm volatile("ds_write_b96 %0, %1 offset:%2" :: "v"(lds0), "v"(sr), "n"(L_MISC + 32) : "memory");
    }
    if (last == 0 && w >= 2) asm volatile("s_waitcnt vmcnt(%0)" :: "n"(w == SCH_W ? 2 : 0) : "memory");
    if (w != 0) lds_inc<L_MISC + 28>(lds0);
    STAMP(8);
    if (last) __builtin_amdgcn_s_setprio(0);
    if (last && w == 2) { VM_DRAIN(); lds_inc<L_MISC + 28>(lds0); }
  };
  unsigned* const cntq = (unsigned*)(misc + 12);
  unsigned* const cntt = (unsigned*)(misc + 26 + (w & 1));
  unsigned* const cntp = (unsigned*)(misc + 23);
  unsigned* const cntx2 = (unsigned*)(misc + 28);
  float xn4 = 0.f;
  auto wpf = [&](int s, int lane) {
    constexpr int PER = 81920 / QR / WPF_N;
    static_assert(PER % WPF_S == 0 && PER / WPF_S <= 64, "prefetch");
    touch(a.ws + OFF_WQKVBT + (size_t)imin(qkv_col(b), 14) * 163840 + (qkv_row(b) % (2 * QR)) * (81920 / QR), (s - WPF0) * PER + imin(lane, PER / WPF_S - 1) * WPF_S, lds0);
  };
  auto t5_get = [&](int s, int lane) {
    ot.load(a, rs, s - 1 - LAG5, b);
    xn4 = u2f(__hip_atomic_load((const GAS unsigned*)xnrm + (imax(s + 1, 0) * 32 + (lane & 31)) * 4, RLX_AGENT));
    __builtin_amdgcn_sched_barrier(0);
  };
  Sched sch;
  auto late = [&](int s, int lane) {
    if (w == SCH_W && SCH_LATE && __builtin_expect(s >= 0, 1)) *(u32x2*)(misc + 24) = (u32x2){sch.run(a, rs, s, b, lane), (unsigned)(s + 1)};
    if ((w == 2 && F6) || (w == 0 && F4) || (w == 1 && F5) || (w == 3 && F7)) { lds_spin<L_MISC + 48, 1>(lds0, 2u * (unsigned)imax(s + 1, 0)); if (FB_SL && w != 2) __builtin_amdgcn_s_sleep(FB_SL); }
    if (w >= 2) { x_fetch_n<w == 2 ? F6 : F7>(x_src(a, s + 3, b), lds0, s + 3, F4 + F5 + 2 * F67 + (w == 3 ? F6 : 0), lane * 16); return; }
    unsigned sd = 0u;
    if ((w == 0 && XPF == 3) || XPF == 4 || (w == 1 && XPF == 5)) stouch<10>(a.ws + OFF_XN + (size_t)imin(s + 4, NSEG - 1) * (s < 1 ? 0 : 40960) + (b >> 3) * 1280 + w * 64, sd);
    x_fetch_n<w == 0 ? F4 : F5>(x_src(a, s + 3, b), lds0, s + 3, w == 0 ? 0 : F4, lane * 16);
    if (WPF_W == 0 && w == 0 && __builtin_expect(s >= WPF0 && s < WPF0 + WPF_N, 0)) wpf(s, lane);
    if (w == 1 && __builtin_expect(s == CPF_S, 0)) code_touch(b, lane, lds0);
    lds_spin<L_MISC + 104 + 4 * (w & 1), PS_SL>(lds0, (unsigned)imax(s + 1, 0));
    asm volatile("" :: "s"(sd));
  };
  auto xn_put = [&](int s) {
    float xn2 = xn4;
    xn2 = fmx(xn2, dppf<DPP_XOR1>(xn2)); xn2 = fmx(xn2, dppf<DPP_XOR2>(xn2));
    xn2 = fmx(xn2, dppf<DPP_ROR4>(xn2)); xn2 = fmx(xn2, dppf<DPP_ROR8>(xn2));
    misc[s & 3] = x16mx(xn2);
  };
  if (w == 2) { ot.init(b, lane0); ot.out = (u32x2){0u, 0u}; ot.xo = ot.out; ot.y[0] = ot.y[1] = ot.y[2] = ot.y[3] = (u32x4){0u, 0u, 0u, 0u}; }
  VM_DRAIN();
  lds_arrive(cnt, lane0);
  lds_wait(cnt, 8u);
  sch.tsv = sch.rec = (u32x4){0u, 0u, 0u, 0u};
  sch.side = __builtin_amdgcn_readfirstlane(*(const unsigned*)(misc + 11)) & 8192u;
  sch.o = 0u;
  int sv = NV - 1;
  CODE_PAD((PADT >> (4 * w)) & 15);
#pragma unroll 1
  for (int s = -1; s < NSEG - 1; ++s) {
    const int sv1 = sv == NV - 1 ? 0 : sv + 1;
    FRESH_LANE();
    STAMP(0);
    mma_seg(s + 1, sv1, lane, [&]() { STAMP(1); late(s - 1, lane); if (w == SCH_W) sch.pre(s); STAMP(9); });
    STAMP(2);
    __builtin_amdgcn_sched_barrier(0);
    VM_DRAIN();
    if (w == 2) { ot.run(lane); xn_put(s); }
    STAMP(10);
    st_out(s, lane, [&]() { lds_spin<L_MISC + 92, W1_SL>(lds0, 4u * (unsigned)imax(s, 0)); });
    if (w == 2) t5_get(s, lane);
    __builtin_amdgcn_sched_barrier(0);
    if (w >= 2) vo_pre(s, sv, lane);
    STAMP(3);
    BAR();
    STAMP(4);
    if (__builtin_expect(s >= 0, 1)) {
      if (w == SCH_W) { sch.load(rs, lane0); __builtin_amdgcn_sched_barrier(0); }
      if (w < 2) vo_pre(s, sv, lane);
      vo_mem(s, std::integral_constant<int, 0>{});
      STAMP(6);
      if (w == SCH_W && !SCH_LATE) {
        __builtin_amdgcn_s_setprio(SCH_P);
        *(u32x2*)(misc + 24) = (u32x2){sch.run(a, rs, s, b, lane0), (unsigned)(s + 1)};
      }
      STAMP(11);
    } else BAR();
    __builtin_amdgcn_s_setprio(MMA_P);
    __builtin_amdgcn_sched_barrier(0);
    sv = sv1;
  }
  __builtin_amdgcn_s_setprio(0);
  STAMP_OUT();
  late(NSEG - 2, lane0);
  if (w == 2) ot.run(lane0);
  { FRESH_LANE(); if (w == 0) drain_pre(lds, b, lane); qkv_pre(vo, b, w, lane); }
  st_out(NSEG - 1, lane0, [&]() { lds_spin<L_MISC + 92, 1>(lds0, 4u * (NSEG - 1)); });
  if (w != 0) vo_pre(NSEG - 1, (NSEG - 1) % NV, lane0);
  BAR();
  if (w != 0) vo_mem(NSEG - 1, std::integral_constant<int, 1>{});
}

__device__ __forceinline__ void qkv_tail(const RecArgs& a, char* lds, const int w, const unsigned (&vo)[2][2]) {
  const int lane0 = __builtin_amdgcn_workitem_id_x() & 63, b = __builtin_amdgcn_workgroup_id_x();
  const int r = qkv_row(b), cb = qkv_col(b);
  if (cb >= 15) return;
  const unsigned lds0 = (unsigned)(uintptr_t)(LAS char*)lds;
  unsigned* const stage = (unsigned*)(lds + L_MISC) + 17;
  unsigned* const cnty = stage + 1;
  const f32x4 Z4 = {0.f, 0.f, 0.f, 0.f};
  auto fetch = [&](int i) {
    x_fetch_n<10>(x_src(a, NSEG + i, b), lds0, i, 10 * w, lane0 * 16);
  };
  unsigned want = 0;
  auto sync = [&]() { VM_DRAIN(); want += 4u; lds_inc<L_MISC + 72>(lds0); while (__hip_atomic_load(cnty, __ATOMIC_RELAXED, __HIP_MEMORY_SCOPE_WORKGROUP) < want) { } asm volatile("" ::: "memory"); };
  LOGQ(0);
  bf16x8 W[2][20];
  const __amdgpu_buffer_rsrc_t rw = __builtin_amdgcn_make_buffer_rsrc(a.ws + OFF_WQKVBT + (size_t)(cb * 8 + 2 * w) * 20480, 0, 40960, 0x00020000);
  auto w_get = [&](int ks) {
#pragma unroll
    for (int i = 0; i < 2; ++i) W[i][ks] = as8(__builtin_amdgcn_raw_buffer_load_b128(rw, lane0 * 16 + (ks & 3) * 1024, (i * 20 + (ks & ~3)) * 1024, 0));
    __builtin_amdgcn_sched_barrier(0);
  };
#pragma unroll
  for (int ks = 0; ks < QW; ++ks) w_get(ks);
  bf16x8 ones = mk8(0x3f803f80u, 0x3f803f80u, 0x3f803f80u, 0x3f803f80u);
  PIN_V(ones);
  const __amdgpu_buffer_rsrc_t ro = __builtin_amdgcn_make_buffer_rsrc(a.ws + OFF_QB, 0, (int)(OFF_OB - OFF_QB), 0x00020000);
  const int t0 = w & 1;
  auto body = [&](auto SW, auto SQ) {
    const float qs = cb < 5 ? 0.22360679774997896f * 1.4426950408889634f : 1.0f;
    f32x4 cs[2] = {Z4, Z4};
    LOGQ(1);
    f32x4 acc[2][2], st;
    auto mma = [&](int it, auto FIRST) {
      const char* xp0 = lds + L_XN + (it & 1) * 40960 + t0 * 20480 + lane0 * 16;
      const char* xp1 = lds + L_XN + (it & 1) * 40960 + (1 - t0) * 20480 + lane0 * 16;
      acc[0][0] = acc[0][1] = acc[1][0] = acc[1][1] = st = Z4;
      bf16x8 x[4][2];
#pragma unroll
      for (int p = 0; p < 3; ++p) { x[p][0] = *(const bf16x8*)(xp0 + p * 1024); x[p][1] = *(const bf16x8*)(xp1 + p * 1024); }
      __builtin_amdgcn_sched_barrier(0);
#pragma unroll
      for (int ks = 0; ks < 20; ++ks) {
        if (FIRST && ks + QW < 20) w_get(ks + QW);
#pragma unroll
        for (int q = 0; q < 4; ++q) {
          const int tt = 1 - (q >> 1), i = 1 - (q & 1);
          acc[i][tt] = SW ? mfma32(W[i][ks], x[ks & 3][tt], acc[i][tt]) : mfma32(x[ks & 3][tt], W[i][ks], acc[i][tt]);
          __builtin_amdgcn_sched_barrier(0);
          if (q < 2 && ks + 3 < 20) x[(ks + 3) & 3][q] = *(const bf16x8*)((q ? xp1 : xp0) + (ks + 3) * 1024);
          __builtin_amdgcn_sched_barrier(0);
        }
        st = mfma32(SQ ? x[ks & 3][0] : ones, x[ks & 3][0], st);
        if (FIRST) {
#pragma unroll
          for (int i = 0; i < 2; ++i) cs[i] = SW ? mfma32(W[i][ks], ones, cs[i]) : mfma32(ones, W[i][ks], cs[i]);
        }
        __builtin_amdgcn_sched_barrier(0);
      }
    };
    mma(0, std::integral_constant<bool, true>{});
#pragma unroll 1
    for (int it = 0; it < 4; ++it) {
      if (it) mma(it, std::integral_constant<bool, false>{});
      LOGQ(2 + 3 * it);
      FRESH_LANE();
      float* const sp = (float*)(lds + L_ST) + (it & 1) * 64;
      if (SQ) { if ((c >> 2) == g) sp[32 + t0 * 16 + c] = (c & 3) == 0 ? st[0] : ((c & 3) == 1 ? st[1] : ((c & 3) == 2 ? st[2] : st[3])); }
      else if (g == 0) sp[t0 * 16 + c] = st[0];
      sync();
      LOGQ(3 + 3 * it);
      if (it == 0) fetch(2);
      const bool late = r + 48 >= 55;
      if (it == 1 && !late) fetch(3);
      const unsigned so = (unsigned)((r + 16 * it) * (cb < 5 ? 64 * QST : KVT));
      auto stat = [&](float sm, float sq, float& mean, float& rstd) {
        mean = sm * (1.0f / DM);
        rstd = __builtin_amdgcn_rsqf(__builtin_fmaxf(sq * (1.0f / DM) - mean * mean, 0.f) + 1e-5f);
      };
#pragma unroll
      for (int tt = 0; tt < 2; ++tt) {
        const int tq = tt ^ t0;
        float mean[4], rstd[4];
        if (SW) {
          stat(sp[tq * 16 + c], sp[32 + tq * 16 + c], mean[0], rstd[0]);
          rstd[0] *= qs;
        } else {
          const f32x4 sm = *(const f32x4*)(sp + tq * 16 + 4 * g), sq = *(const f32x4*)(sp + 32 + tq * 16 + 4 * g);
#pragma unroll
          for (int k = 0; k < 4; ++k) stat(sm[k], sq[k], mean[k], rstd[k]);
        }
#pragma unroll
        for (int i = 0; i < 2; ++i) {
          float v[4];
#pragma unroll
          for (int k = 0; k < 4; ++k) v[k] = (acc[i][tt][k] - mean[SW ? 0 : k] * cs[i][SW ? k : 0]) * rstd[SW ? 0 : k];
          __builtin_amdgcn_raw_buffer_store_b64((u32x2){pack2(v[0], v[1]), pack2(v[2], v[3])}, ro, vo[i][tt], so, 0);
        }
      }
      LOGQ(4 + 3 * it);
      if (it == 2 && late) lds_spin<L_MISC + 68, 0>(lds0, 5u);
    }
  };
  typedef std::integral_constant<bool, false> F_; typedef std::integral_constant<bool, true> T_;
  if (cb < 10) { if (w >> 1) body(T_{}, T_{}); else body(T_{}, F_{}); }
  else { if (w >> 1) body(F_{}, T_{}); else body(F_{}, F_{}); }
}

__device__ __forceinline__ void t2_mem(const bf16x8 fr, const bf16x8 q0, const bf16x8 q1, const __amdgpu_buffer_rsrc_t rs, unsigned o, int h, int j, float inv, int lane) {
  const f32x4 Z4 = {0.f, 0.f, 0.f, 0.f};
  const f32x4 d0 = mfma32(fr, q0, Z4) * inv, d1 = mfma32(fr, q1, Z4) * inv;
  const unsigned a0 = pack2n(d0[0], d0[1]), a1 = pack2n(d0[2], d0[3]), b0 = pack2n(d1[0], d1[1]), b1 = pack2n(d1[2], d1[3]);
  const auto r0 = __builtin_amdgcn_permlane32_swap(a0, b0, false, false), r1 = __builtin_amdgcn_permlane32_swap(a1, b1, false, false);
  SC1_STORE8S(((u32x2){r0[1], r1[1]}), ts_mem(h, j) + (lane >> 5) * 256 + (lane & 12) * 16 + (lane & 16) * 2 + (lane & 3) * 8, o);
}

template <int w>
__device__ __forceinline__ void rec_waves(const RecArgs& a, char* lds, __amdgpu_buffer_rsrc_t rs, unsigned& tlast) {
  PIN_S(rs);
  const int lane = __builtin_amdgcn_workitem_id_x() & 63, b = __builtin_amdgcn_workgroup_id_x(), c = lane & 15, g = lane >> 4, wid = w; (void)wid;
  const int gp = (b & 7) * 2 + (b >> 7), m = (b >> 3) & 15, h0 = 2 * gp, h = h0 + (m >> 3), j = m & 7;
  float* const misc = (float*)(lds + L_MISC);
  const f32x4 Z4 = {0.f, 0.f, 0.f, 0.f};
  LOGP(0);
  __builtin_amdgcn_s_setprio(3);
  const unsigned lds0 = (unsigned)(uintptr_t)(LAS char*)lds;
  if (!a.layer) img_fetch<12>(a, lds0, h, j, w, lane);
  constexpr int NC = w < 2 ? 2 : 3;
  const int far = ((b >> 2) & 1) ^ 1;
  auto chunk = [&](int f) { return f == 0 ? 4 * far + w : (w == 2 && f == 1 ? 8 + far : (w == 3 && f == 2 ? 9 - far : 4 - 4 * far + w)); };
  bf16x16 Wqk[3][NC];
  u32x4 bm0[NC][2];
#pragma unroll
  for (int f = 0; f < 2 * NC; ++f) bm0[f >> 1][f & 1] = *(const u32x4*)(a.mem0 + (size_t)(c & 7) * DM + 64 * chunk(f >> 1) + 16 * g + 8 * (f & 1));
  if (!a.layer) asm volatile("s_waitcnt vmcnt(0)" ::: "memory");
  BAR();
  LOGP(9);
  const unsigned ck = __builtin_amdgcn_readfirstlane(*(const unsigned*)(misc + 11));
  const unsigned flip = (ck ^ (unsigned)(b << 11)) & 8192u;
#pragma unroll
  for (int t = 0; t < 3; ++t)
#pragma unroll
    for (int f = 0; f < NC; ++f) Wqk[t][f] = cat16(wqk_frag(lds, t, 2 * chunk(f), lane), wqk_frag(lds, t, 2 * chunk(f) + 1, lane));
  if (!a.layer) lds_arrive((unsigned*)(misc + 6), lane);
  else {
    if (w == 0) {
      for (unsigned spins = 0; spins < SPIN_CAP; ++spins) {
        if (__builtin_amdgcn_raw_buffer_load_b32(rs, (b & 7) * 256, (unsigned)OFF_RDY, 16) >= 16u) break;
        __builtin_amdgcn_s_sleep(4);
        asm volatile("" ::: "memory");
      }
      lds_inc<L_MISC + 84>(lds0);
    }
    lds_wait((unsigned*)(misc + 21), 2u);
#pragma unroll
    for (int f = 0; f < 2 * NC; ++f) PIN_V(bm0[f >> 1][f & 1]);
#pragma unroll
    for (int i = 0; i < 10; ++i) x_fetch(x_src(a, 0, b), lds0, 0, 10 * w + i, lane);
  }
  const bf16x8 SelLo = sel_frag(c, 0), SelHi = sel_frag(c, 8);
  bf16x8 ONES = mk8(0x3f803f80u, 0x3f803f80u, 0x3f803f80u, 0x3f803f80u);
  if (w < 2) PIN_V(ONES);
  bool dead = false;
  float inv = 1.0f;
  unsigned* const cnt2 = (unsigned*)(misc + 7);
  unsigned l2off[2 * NC];
#pragma unroll
  for (int f = 0; f < 2 * NC; ++f) { l2off[f] = acc_off(16 * chunk(f >> 1) + 4 * g + 2 * (f & 1) + (c >> 3), c & 7) ^ flip; PIN_V(l2off[f]); }
  int sidx = (c >> 3) ? 0xEEEE : 0x4444;
  PIN_V(sidx);
  const unsigned p1off = (unsigned)((w == 0 ? (h & 2) * 1024 + j * 256 + (h & 1) * 64 + (g & 1) * 8 : (h & 3) * 32768 + j * 512 + (g >> 1) * 256 + (g & 1) * 64) + (c & 4) * 32 + (c & 3) * 16);
  unsigned p1v = (c < 8) && (w != 0 || g >= 2) ? p1off : 0x7ffffff0u, p1cur = 0x7ffffff0u;
  PIN_V(p1v); PIN_V(p1cur);
  const int hd = (c >> 2) & 1, mr = hd ? 3 - (c & 3) : (c & 3);
  const unsigned l1off = (unsigned)(((h0 + hd) & 3) * 32768 + g * 512 + (w & 1) * 128 + (c >> 3) * 64 + mr * 16);
  const unsigned l1offm = (unsigned)((h0 & 2) * 1024 + (g + 4 * (c >> 3)) * 256 + (w & 1) * 128 + hd * 64 + mr * 16);
  const int plane = (lane & ~6) | ((lane & 2) << 1) | ((lane & 4) >> 1);
  unsigned acco = c < 8 ? (acc_off(64 * far + m * 4 + g, 4 * (w & 1) + mr) + (unsigned)(hd * 8)) ^ flip : 0x7ffffff0u;
  unsigned acco2 = c >= 8 ? (acc_off(64 - 64 * far + m * 4 + g, 4 * (w & 1) + mr) + (unsigned)(hd * 8)) ^ flip
                          : (c < 4 ? (acc_off(128 + m * 2 + (g & 1), 4 * (w & 1) + mr) + (unsigned)((g >> 1) * 8)) ^ flip : 0x7ffffff0u);
  if (w < 2) { PIN_V(acco); PIN_V(acco2); }
  unsigned bmo[NC];
#pragma unroll
  for (int f = 0; f < NC; ++f) { bmo[f] = (unsigned)(chunk(f) * 1024 + lane * 16); PIN_V(bmo[f]); }
  bf16x8 bm[NC];
  auto bm_put = [&](int f) { *(bf16x8*)(lds + L_BM + bmo[f]) = bm[f]; __builtin_amdgcn_sched_barrier(0); };
  f32x4 acc[3] = {Z4, Z4, Z4};
  const int rt2 = ((c >> 3) * 8 + 4 * (g & 1)) * 28 + 16 + (c & 7);
  unsigned redo[3] = {(unsigned)(4 * g * 28 + c), (unsigned)(g < 2 ? (4 * g + 2) * 28 + c : rt2), (unsigned)(g < 2 ? (4 * g + 10) * 28 + c : rt2 + 56)};
#pragma unroll
  for (int k = 0; k < 3; ++k) { redo[k] = L_RED1 + w * 1792 + redo[k] * 4; PIN_V(redo[k]); }
  auto red_put = [&]() {
    auto hs = [&](float x, float y) { const auto sw = __builtin_amdgcn_permlane32_swap(f2u(x), f2u(y), false, false); return u2f(sw[0]) + u2f(sw[1]); };
#pragma unroll
    for (int k = 0; k < 3; ++k) {
#pragma unroll
      for (int i = 0; i < 2; ++i) *(float*)(lds + redo[k] + i * 112) = k == 0 ? hs(acc[0][i], acc[1][i]) : hs(acc[k - 1][2 + i], acc[2][2 * k - 2 + i]);
      __builtin_amdgcn_sched_barrier(0);
    }
  };
#pragma unroll
  for (int f = 0; f < NC; ++f) {
    const u32x4 u = bm0[f][0], v = bm0[f][1];
    bm[f] = c < 8 ? mk8(u[0], u[2], v[0], v[2]) : mk8(u[1], u[3], v[1], v[3]);
    bm_put(f);
#pragma unroll
    for (int t = 0; t < 3; ++t) acc[t] = smfma64(bm[f], Wqk[t][f], acc[t], sidx);
  }
  red_put();
  VM_DRAIN();
  if (a.layer) lds_arrive((unsigned*)(misc + 6), lane);
  lds_wait((unsigned*)(misc + 6), 8u);
#pragma unroll
  for (int i = 0; i < 10; ++i) x_fetch(x_src(a, 1, b), lds0, 1, 10 * w + i, lane);
  LOGP(1);
  BAR();
  STAMP_DECL(); ARRIVE_DECL();
  bf16x8 fr, qx[2];
  auto kq_get = [&](int seg) {
    const char* p = lds + (w == 1 ? L_KX : L_QX) + (seg % NKQ) * 2048 + lane * 16;
    if (w == 1 || w == 2) { qx[0] = *(const bf16x8*)p; qx[1] = *(const bf16x8*)(p + 1024); }
  };
  unsigned qo4 = (unsigned)(L_RED1 + (w == 1 ? (c & 7) * 112 + (c >> 3) * 3584 : c * 112) + g * 16), qo2 = qo4 + 64u - (unsigned)(g * 8), qo2b = qo2 + 3584u;
  if (w < 3) { PIN_V(qo4); PIN_V(qo2); if (w != 1) PIN_V(qo2b); }
  auto pub1 = [&](unsigned off, unsigned o1) {
    if (w == 0) fr = qk_frag(lds + qo4, lds + qo2, lds + qo2b);
    else {
      const f32x4 a0 = *(const f32x4*)(lds + qo4), a1 = *(const f32x4*)(lds + qo4 + 1792);
      const f32x2 b0 = *(const f32x2*)(lds + qo2), b1 = *(const f32x2*)(lds + qo2 + 1792);
      float v[6];
#pragma unroll
      for (int i = 0; i < 6; ++i) v[i] = i < 4 ? a0[i] + a1[i] : b0[i - 4] + b1[i - 4];
#pragma unroll
      for (int i = 0; i < 6; ++i) v[i] += dppf<DPP_ROR8>(v[i]);
      fr = mk8(pack2(v[0], v[1]), pack2(v[2], v[3]), pack2(v[4], v[5]), 0u);
    }
    const f32x4 sc = mfma32(w == 0 ? fr : qx[0], fr, Z4);
    if (w == 0) SC1_STORE8S(((u32x2){pack2n(sc[0], sc[1]), pack2n(sc[2], sc[3])}), off, o1);
    else {
      const f32x4 s2 = mfma32(qx[1], fr, Z4);
      __builtin_amdgcn_raw_buffer_store_b128((u32x4){pack2n(sc[0], sc[1]), pack2n(sc[2], sc[3]), pack2n(s2[0], s2[1]), pack2n(s2[2], s2[3])}, rs, off, o1, 16);
    }
  };
  VM_DRAIN();
  kq_get(0);
  unsigned o1 = sct_slot(ck, 0u), o1m = scm_slot(ck, 0u);
  unsigned t2o = ts_tok(h, j) + (unsigned)((c >> 2) * 256 + g * 64 + (c & 3) * 16);
  TokScores t2;
  if (w == 3) { PIN_V(t2o); t2.get(lds, 0, lane); t2.run(0); t2.run(1); t2.store(rs, t2o, ts_slot(ck, 0)); }
  if (w < 2) pub1(p1v, w == 0 ? o1m : o1);
  const unsigned tst = now32() | 1u;
  if (w == 3 && ST_X) __builtin_amdgcn_raw_buffer_store_b32(tst, rs, lane < 2 ? ts_piece(b >> 2) - ACC_TS + (b & 3) * 4 + lane * 8192 : 0x7ffffff0, (unsigned)(OFF_ACC + (SR - 1) * ACC_STEP) + ACC_TS, 16);
  LOGP(2);
  BAR();
  LOGP(3);
#pragma unroll
  for (int i = 0; i < 10; ++i) x_fetch(x_src(a, 2, b), lds0, 2, 10 * w + i, lane);
  VM_DRAIN();
  unsigned o2 = (unsigned)OFF_ACC, o2p = 0u, tw = 0u;
  asm volatile("" : "+s"(o2p));
  unsigned tc0 = 0u, tstar = 0u;
  int dmin = 0, dmax = 0;
  int e1 = 100000;
  unsigned offs = 100u;
  TokTask tk;
  PvTask2 pv2;
  if (w == 3) pv2.init(gp, lane);
  if (w == 2) tk.init(h, j, lane);
  unsigned o4 = 0u;
  unsigned o3, o3p;
  u32x16 P = {0u, 0u, 0u, 0u, 0u, 0u, 0u, 0u, 0u, 0u, 0u, 0u, 0u, 0u, 0u, 0u}, V = P;
  u32x8 V8 = {0u, 0u, 0u, 0u, 0u, 0u, 0u, 0u};
  if (w < 2) asm volatile("" : "+{v[208:223]}"(P), "+{v[184:191]}"(V8), "+{v[192:207]}"(V));
  unsigned tso = lane < 2 ? ts_piece(b >> 2) + (unsigned)((b & 3) * 4 + lane * 8192) : 0x7ffffff0u;
  if (w < 2) PIN_V(tso);
  float minf = g >= 2 ? -INFINITY : 0.f;
  if (w < 2) PIN_V(minf);
  int sv = NV - 1;
  bf16x8 vox[5];
  unsigned vma[3], need2;
  u32x4 slo, shi;
  float k1;
  const u16* xsrc;
  float invv = 1.0f;
  auto prep0 = [&](int s) {
    xsrc = x_src(a, s + 3, b);
    if (w == 2) { o3p = ts_slot(ck, (s + 1 - LAG3) & 3); o3 = ts_slot(ck, imax(s, 0)); o4 = pt_slot(ck, imax(s - LAG3, 0)); PIN_S(o3); }
    if (w == 3) { o3p = pt_slot(ck, (s - LAG4) & 3); o4 = ts_slot(ck, s + 1); }
    PIN_S(xsrc); PIN_S(o3p); PIN_S(o4);
    __builtin_amdgcn_sched_barrier(0);
  };
  auto prep = [&](int s) {
    o2 = (unsigned)OFF_ACC + (unsigned)(s % SR) * (unsigned)ACC_STEP;
    sv = sv == NV - 1 ? 0 : sv + 1;
    PIN_S(o2); PIN_S(sv);
    if (w < 2) {
      PIN_S(tc0);
      o1 = sct_slot(ck, s % SR); o1m = scm_slot(ck, s % SR);
      need2 = 4u * (unsigned)(s + 1) + 2u * ((unsigned)(s + 2) / NSEG);
      PIN_S(o1); PIN_S(o1m); PIN_S(need2);
      __builtin_amdgcn_sched_barrier(0);
      kq_get(s);
    }
    invv = misc[4 + (s & 1)];
  };
  auto prep2 = [&](int s) {
#pragma unroll
    for (int k = 0; k < 3; ++k) {
      const int t = k == 0 ? 2 : (k == 1 ? far : 1 - far);
      const char* p = lds + L_VOX + sv * 5120 + t * 1024 + (plane & 47) * 16 + (g & 1) * 8;
      auto rd = [&](const char* q) { const u32x2 a0 = *(const u32x2*)q, a1 = *(const u32x2*)(q + 256); return mk8(a0[0], a0[1], a1[0], a1[1]); };
      vox[k == 0 ? 0 : 2 * k - 1] = rd(p);
      if (k) vox[2 * k] = rd(p + 3072);
      vma[k] = lds0 + (unsigned)(L_VOM + sv * 1280 + t * 256 + (plane & 31) * 8);
      PIN_V(vma[k]);
    }
    __builtin_amdgcn_sched_barrier(0);
    inv = u2f(__builtin_amdgcn_readfirstlane(f2u(invv)));
    slo = __builtin_bit_cast(u32x4, SelLo); shi = __builtin_bit_cast(u32x4, SelHi);
#pragma unroll
    for (int i = 0; i < 4; ++i) { slo[i] = ((slo[i] >> 7) & 0x10001u) * (f2u(inv) >> 16); shi[i] = ((shi[i] >> 7) & 0x10001u) * (f2u(inv) >> 16); }
    if (F0A) { xsrc = x_src(a, s + 3, b); PIN_S(xsrc); }
    k1 = KEXP * inv;
    PIN_V(k1); PIN_V(slo); PIN_V(shi);
    asm volatile("" : "+v"(vox[0]), "+v"(vox[1]), "+v"(vox[2]), "+v"(vox[3]), "+v"(vox[4]));
    __builtin_amdgcn_sched_barrier(0);
  };
  auto rows = [&](int s) {
    const int dl = spin_until(tw);
    if (w < 2) tc0 = cyc32();
    __builtin_amdgcn_sched_barrier(0);
    STAMP(1);
    unsigned spins = 0;
    auto pass = [&](bool first) {
      u32x4 x[2 * NC];
#pragma unroll
      for (int f = 0; f < NC; ++f) {
        if (f && w < 2 && first) { prep(s); asm volatile(".rept %0\n\ts_nop 3\n\t.endr" :: "n"(P2_G1)); }
        else if (f) asm volatile(".rept %0\n\ts_nop 3\n\t.endr" :: "n"(w < 2 ? P2_G0 : (f == 1 ? P2_G2 : P2_G3)));
        __builtin_amdgcn_sched_barrier(0);
        x[2 * f] = SC1_LOADS(l2off[2 * f], o2p); x[2 * f + 1] = SC1_LOADS(l2off[2 * f + 1], o2p);
        __builtin_amdgcn_sched_barrier(0);
      }
      if (first) lds_inc<L_MISC + 92>(lds0);
      if (first) { if (w >= 2) prep(s); else prep2(s); }
      if (w >= 2 && first && P2_W) lds_spin<L_MISC + 92, 0>(lds0, 4u * (unsigned)s);
      if (w == 2 && first) tk.store(rs, o4, s > LAG3 ? 0u : 0x7ff00000u);
      if (w == 3 && first) t2.store(rs, t2o, o4);
      if (first && w < 2) {
        p1cur = p1v; offs = (offs - (unsigned)dl) & ~3u;
        PIN_V(p1cur); PIN_S(offs);
        tstar = tc0 + (unsigned)(e1 + D1_M + (w == 1 ? D1_W1 : 0)) - offs * 20u;
        dmin = offs < 40u ? D1_MIN : REC_D1;
        dmin = s < EARLY_N ? REC_D1 + EARLY_D1 : dmin;
        dmax = REC_D1;
        PIN_S(tstar); PIN_S(dmin); PIN_S(dmax);
      }
      __builtin_amdgcn_sched_barrier(0);
      unsigned ok = 32u;
#pragma unroll
      for (int t = 0; t < 3; ++t) { acc[t] = Z4; PIN_V(acc[t]); }
      __builtin_amdgcn_sched_barrier(0);
      bool r = true;
#pragma unroll
      for (int f = 0; f < NC; ++f) {
        unsigned p[4];
#pragma unroll
        for (int k = 0; k < 2; ++k) {
          const u32x4 v = x[2 * f + k];
          p[2 * k] = pack2((float)(int)v[0], (float)(int)v[1]); p[2 * k + 1] = pack2((float)(int)v[2], (float)(int)v[3]);
          asm volatile("" : "+v"(p[2 * k]), "+v"(p[2 * k + 1]));
          __builtin_amdgcn_sched_barrier(0);
        }
        ok = (ok & x[2 * f][0] & x[2 * f][2]) & (x[2 * f + 1][0] & x[2 * f + 1][2]);
        PIN_V(ok);
        __builtin_amdgcn_sched_barrier(0);
        bm[f] = mk8(p[0], p[1], p[2], p[3]);
        acc[0] = smfma64(bm[f], Wqk[0][f], acc[0], sidx);
        __builtin_amdgcn_sched_barrier(0);
        bm_put(f);
        acc[1] = smfma64(bm[f], Wqk[1][f], acc[1], sidx);
        __builtin_amdgcn_sched_barrier(0);
        if (f == NC - 1) { u64 bad = __builtin_amdgcn_ballot_w64(ok == 0u); PIN_S(bad); r = bad == 0ull; }
        __builtin_amdgcn_sched_barrier(0);
        acc[2] = smfma64(bm[f], Wqk[2][f], acc[2], sidx);
        __builtin_amdgcn_sched_barrier(0);
      }
      red_put();
      return r;
    };
    if (__builtin_expect(!pass(true), 0)) {
      FC(0);
      do {
        if (dead || ++spins > SPIN_CAP) { dead = true; break; }
        asm volatile("" ::: "memory");
      } while (!pass(false));
    }
    STAMPV(12, spins); LOGF(1, spins);
    TSTAMP(2, now32()); TSTAMP(3, spins);
  };
  if (w >= 2) prep0(-1);
  prep(0);
  if (w < 2) prep2(0);
  CODE_PAD((PADR >> (4 * w)) & 15);
#pragma unroll 1
  for (int s = 0;; ++s) {
    if (__builtin_expect(o2p != 0u, 1)) rows(s);
    if (w < 2 && __builtin_expect(s == NSEG - 1, 0)) break;
    STAMP(2);
    BAR();
    STAMP(3);
    if (w < 2) pub1(p1cur, w == 0 ? o1m : o1);
    if (w == 2) tk.load(rs, o3p);
    if (w == 3) pv2.load(rs, o3p);
    const unsigned tp1 = cyc32();
    __builtin_amdgcn_sched_barrier(0);
    if (w == 2) {
      __builtin_amdgcn_s_setprio(W2_P);
      prep0(s);
      if (W2_D) spin_cyc(tp1 + W2_D);
      fr = qk_frag(lds + qo4, lds + qo2, lds + qo2b);
      t2_mem(fr, qx[0], qx[1], rs, o3, h, j, invv * KEXP, lane);
      __builtin_amdgcn_sched_barrier(0);
      asm volatile("s_waitcnt vmcnt(1)" ::: "memory");
      lds_inc<L_MISC + 28>(lds0);
      kq_get(s + 1);
    }
    if (w == 3) {
      __builtin_amdgcn_s_setprio(T4_P);
      prep0(s);
      t2.get(lds, s + 1, lane); t2.run(0); t2.run(1);
      __builtin_amdgcn_sched_barrier(0);
    }
    if (w >= 2 && __builtin_expect(s == NSEG - 2, 0)) { VM_DRAIN(); lds_inc<L_MISC + 28>(lds0); }
    if (w >= 2) __builtin_amdgcn_s_setprio(UN_P);
    TSTAMP(0, now32());
    STAMP(4);
    __builtin_amdgcn_sched_barrier(0);
    unsigned tpub = 0;
    u32x2 sw;
    if (w == 2) {
      STAMP(5);
      __builtin_amdgcn_sched_barrier(0);
      STAMP(6);
      ARRIVE(0);
      tk.run(j, lane, SelLo);
      if (__builtin_expect(s == NSEG - 1, 0)) { tlast = tp1; break; }
      if (F2B) { lds_spin<L_MISC + 48, 1>(lds0, 2u * (unsigned)(s + 1)); x_fetch_n<F2B>(xsrc, lds0, s + 3, P2B, lane * 16); }
      __builtin_amdgcn_s_setprio(SP_P);
    }
    if (w == 3) {
      pv2.run<false>(lds, sv < 1 + LAG4 ? sv + NV - 1 - LAG4 : sv - 1 - LAG4, lane, rs, 0u);
      if (__builtin_expect(s == NSEG - 1, 0)) { tlast = tp1; break; }
      if (ST_E) lds_inc<L_MISC + 112>(lds0);
      STAMP(11);
      __builtin_amdgcn_s_setprio(SP_P);
      STAMP(5);
      ARRIVE(0);
      if (F3) { lds_spin<L_MISC + 48, 1>(lds0, 2u * (unsigned)(s + 1)); x_fetch_n<F3>(xsrc, lds0, s + 3, 40 - F3C - F3, lane * 16); }
      STAMP(6);
    }
    if (w < 2) {
      __builtin_amdgcn_s_setprio(D1_P);
      x_fetch_n<F0A>(xsrc, lds0, s + 3, PA + w * F0A, lane * 16);
      int dq = imin((int)(tstar - tp1), dmax);
      dq = dq < -D1_LATE ? dmax : dq;
      PIN_S(dq);
      spin_exact(tp1 + (unsigned)imax(dq, dmin));
      STAMP(5);
      u32x32 S;
      auto sc_load = [&]() {
        asm volatile("buffer_load_dwordx4 v[224:227], %2, %4, %5 offen sc1\n\t.rept %7\n\ts_nop 3\n\t.endr\n\t"
                     "buffer_load_dwordx4 v[228:231], %3, %4, %6 offen sc1\n\tbuffer_load_dwordx4 v[232:235], %3, %4, %6 offen offset:2048 sc1\n\t"
                     "buffer_load_dwordx4 v[236:239], %3, %4, %6 offen offset:256 sc1\n\tbuffer_load_dwordx4 v[240:243], %3, %4, %6 offen offset:2304 sc1\n\t"
                     "v_mov_b32 v220, 0x42000000\n\tv_mov_b32 v221, 0\n\tv_mov_b32 v222, 0x42000000\n\tv_mov_b32 v223, 0"
                     : "=&{v[224:255]}"(S), "+{v[208:223]}"(P) : "v"(l1offm), "v"(l1off), "s"(rs), "s"(o1m), "s"(o1), "n"(P1_G) : "memory");
      };
      sc_load();
      __builtin_amdgcn_sched_barrier(0);
      lds_inc<L_MISC + 48>(lds0);
      STAMP(6);
      ARRIVE(0);
      lds_spin<L_MISC + 28, 0>(lds0, need2);
      __builtin_amdgcn_s_setprio(3);
      asm volatile("ds_read_b64 v[192:193], %3\n\tds_read_b64 v[196:197], %3 offset:768\n\tds_read_b64 v[200:201], %4\n\tds_read_b64 v[204:205], %4 offset:768\n\tds_read_b64 v[184:185], %2"
                   : "+{v[184:191]}"(V8), "+{v[192:207]}"(V) : "v"(vma[0]), "v"(vma[1]), "v"(vma[2]) : "memory");
      __builtin_amdgcn_sched_barrier(0);
      {
        const int de = imin((int)(tp1 - tc0) - e1, D1_C);
        e1 += s == 0 ? 0 : de >> (s < EARLY_N && de < -D1_C ? 0 : D1_G);
        PIN_S(e1);
      }
      STAMP(11);
      __builtin_amdgcn_sched_barrier(0);
      {
        unsigned spins = 0;
        u64 miss, bad;
#define MM(d, a, b, c) "v_mfma_f32_16x16x32_bf16 " d ", " a ", " b ", " c "\n\t"
#define FM(d, x, r, ctrl) "v_fmac_f32_dpp " d ", " x ", " r " " ctrl "\n\t"
#define ID_ "quad_perm:[0,1,2,3] row_mask:0xf bank_mask:"
#define HM_ "row_half_mirror row_mask:0xf bank_mask:"
#define R8_ "row_ror:8 row_mask:0xf bank_mask:"
#define MI_ "row_mirror row_mask:0xf bank_mask:"
        auto soft = [&]() {
          asm volatile(
            "s_waitcnt vmcnt(4)\n\t" MM("v[244:247]", "%6", "v[224:227]", "0") "v_min3_u32 v214, v224, v225, v226\n\t"
            "s_waitcnt vmcnt(3)\n\t" MM("v[248:251]", "%6", "v[228:231]", "0") "v_min3_u32 v214, v214, v227, v228\n\tv_min3_u32 v214, v214, v229, v230\n\t"
            "s_waitcnt vmcnt(2)\n\t" MM("v[248:251]", "%6", "v[232:235]", "v[248:251]") "v_min3_u32 v214, v214, v231, v232\n\t"
            "v_add_f32_dpp v212, v244, v244 " R8_ "0x3\n\tv_add_f32_dpp v213, v245, v245 " R8_ "0x3\n\t"
            "s_waitcnt vmcnt(1)\n\t" MM("v[248:251]", "%7", "v[236:239]", "v[248:251]")
            "v_add_f32_dpp v212, v246, v246 " R8_ "0xc\n\tv_add_f32_dpp v213, v247, v247 " R8_ "0xc\n\t"
            "s_waitcnt vmcnt(0) lgkmcnt(0)\n\t" MM("v[248:251]", "%7", "v[240:243]", "v[248:251]")
            "v_fma_f32 v212, v212, %14, %15\n\tv_fma_f32 v213, v213, %14, %15\n\tv_exp_f32 v216, v212\n\tv_exp_f32 v217, v213\n\t"
            "v_min3_u32 v214, v214, v233, v234\n\tv_cvt_pk_bf16_f32 v208, v216, v217\n\tv_min3_u32 v214, v214, v235, v236\n\tds_read_b96 v[188:190], %16 offset:%18\n\t"
            "v_exp_f32 v216, v248\n\tv_mov_b32_dpp v209, v208 " R8_ "0xf\n\tv_exp_f32 v217, v249\n\tv_exp_f32 v218, v250\n\t"
            MM("v[252:255]", "%8", "v[208:211]", "0") "v_exp_f32 v219, v251\n\tv_cvt_pk_bf16_f32 v232, v216, v217\n\t"
            MM("v[224:227]", "v[192:195]", "v[208:211]", "0") "v_cvt_pk_bf16_f32 v233, v218, v219\n\tv_min3_u32 v214, v214, v237, v238\n\tv_mov_b32_dpp v234, v232 " R8_ "0xf\n\t"
            MM("v[228:231]", "v[196:199]", "v[208:211]", "0") "v_mov_b32_dpp v235, v233 " R8_ "0xf\n\t"
            MM("v[244:247]", "v[204:207]", "v[208:211]", "0") "v_min3_u32 v214, v214, v239, v240\n\t"
            MM("v[252:255]", "%8", "v[232:235]", "v[252:255]") MM("v[224:227]", "%10", "v[232:235]", "v[224:227]") "v_min3_u32 v214, v214, v241, v242\n\t"
            MM("v[228:231]", "%11", "v[232:235]", "v[228:231]") "s_waitcnt lgkmcnt(0)\n\tv_sub_u32 v215, v190, %17 clamp\n\t"
            MM("v[236:239]", "v[200:203]", "v[208:211]", "0") "v_min3_u32 v214, v214, v243, v215\n\tv_cmp_eq_u32 %4, 0, v214\n\tv_rcp_f32 v216, v252\n\t"
            MM("v[240:243]", "v[184:187]", "v[208:211]", "0") "v_mul_f32 v216, v216, v188\n\tv_subrev_u32 v215, 0x2f800000, v252\n\t"
            MM("v[236:239]", "%12", "v[232:235]", "v[236:239]") FM("v220", "v224", "v216", ID_ "0x1") FM("v221", "v225", "v216", ID_ "0x1")
            "v_mov_b32_dpp v217, v216 " HM_ "0xf\n\t"
            MM("v[244:247]", "%13", "v[232:235]", "v[244:247]") FM("v220", "v230", "v216", ID_ "0x2") FM("v221", "v231", "v216", ID_ "0x2")
            MM("v[240:243]", "%9", "v[232:235]", "v[240:243]") FM("v220", "v228", "v217", HM_ "0x1") FM("v221", "v229", "v217", HM_ "0x1")
            "v_cmp_ge_u32 vcc, v215, v189\n\t" FM("v220", "v226", "v217", HM_ "0x2") FM("v221", "v227", "v217", HM_ "0x2")
            "s_or_b64 %5, %4, vcc"
            : "+{v[224:255]}"(S), "+{v[208:223]}"(P), "+{v[184:191]}"(V8), "+{v[192:207]}"(V), "=&s"(miss), "=&s"(bad)
            : "v"(slo), "v"(shi), "v"(ONES), "v"(vox[0]), "v"(vox[1]), "v"(vox[2]), "v"(vox[3]), "v"(vox[4]), "v"(k1), "v"(minf), "v"(lds0), "s"(s), "n"(L_MISC + 32)
            : "vcc", "scc", "memory");
        };
        soft();
        if (__builtin_expect(bad != 0ull, 0)) {
          if (miss) FC(1); else FC(2);
          while (miss != 0ull && !dead) {
            if (++spins > SPIN_CAP) dead = true;
            asm volatile("s_nop 7\n\ts_nop 7" ::: "memory");
            sc_load(); soft();
          }
          if (bad != 0ull) {
            u32x4 y[5];
#pragma unroll
            for (int k = 0; k < 4; ++k) y[k] = SC1_LOADS(l1off + (k & 1) * 2048 + (k >> 1) * 256, o1);
            y[4] = SC1_LOADS(l1offm, o1m);
            const f32x4 d0 = mfma32(as8(slo), as8(y[4]), Z4);
            f32x4 d1 = mfma32(as8(shi), as8(y[3]), mfma32(as8(shi), as8(y[2]), mfma32(as8(slo), as8(y[1]), mfma32(as8(slo), as8(y[0]), Z4))));
            const bool lo = c < 8;
            const float m0 = __builtin_fmaf((lo ? d0[0] : d0[2]) + dppf<DPP_ROR8>(lo ? d0[2] : d0[0]), k1, minf);
            const float m1 = __builtin_fmaf((lo ? d0[1] : d0[3]) + dppf<DPP_ROR8>(lo ? d0[3] : d0[1]), k1, minf);
            float mx = fmx(fmx(fmx(d1[0], d1[1]), d1[2]), fmx(fmx(d1[3], m0), m1));
            mx = fmx(mx, dppf<DPP_ROR8>(mx));
            mx = x32mx(x16mx(mx));
            d1 -= mx;
            const unsigned mm = pack2(__builtin_amdgcn_exp2f(m0 - mx), __builtin_amdgcn_exp2f(m1 - mx));
            const unsigned t01 = pack2(__builtin_amdgcn_exp2f(d1[0]), __builtin_amdgcn_exp2f(d1[1])), t23 = pack2(__builtin_amdgcn_exp2f(d1[2]), __builtin_amdgcn_exp2f(d1[3]));
            const bf16x8 pm = mk8(mm, dpp_ror8(mm), 0u, 0u), pt = mk8(t01, t23, dpp_ror8(t01), dpp_ror8(t23));
            const float r = __builtin_amdgcn_rcpf(mfma32(ONES, pt, mfma32(ONES, pm, Z4))[0]) * u2f(V8[4]), rm = dppf<0x141>(r);
            f32x4 o[5];
#pragma unroll
            for (int k = 0; k < 5; ++k) o[k] = mfma32(vox[k], pt, mfma32(k ? mk8(V[4 * k - 4], V[4 * k - 3], 0u, 0u) : mk8(V8[0], V8[1], 0u, 0u), pm, Z4));
#pragma unroll
            for (int i = 0; i < 2; ++i) {
              const float oa = dppf<0x141>(o[2][i]), ob = dppf<0x141>(o[1][2 + i]);
              P[12 + i] = f2u(__builtin_fmaf(hd ? ob : oa, rm, __builtin_fmaf(hd ? o[2][2 + i] : o[1][i], r, i ? 0.f : 32.0f)));
            }
            P[8] = f2u(r); P[9] = f2u(rm);
#pragma unroll
            for (int i = 0; i < 4; ++i) { S[12 + i] = f2u(o[3][i]); S[16 + i] = f2u(o[0][i]); S[20 + i] = f2u(o[4][i]); }
            asm volatile("s_nop 7\n\ts_nop 7" : "+{v[224:255]}"(S), "+{v[208:223]}"(P));
          }
        }
        STAMP(7); STAMPV(13, spins); LOGF(0, spins);
        {
          asm volatile("v_cvt_i32_f32 v218, v220\n\tv_cvt_i32_f32 v219, v221\n\tv_and_or_b32 v218, v218, %7, 2\n\tbuffer_atomic_add_x2 v[218:219], %3, %5, %6 offen\n\t"
                       FM("v222", "v236", "v216", R8_ "0x4") FM("v223", "v237", "v216", R8_ "0x4") "ds_read_b64 %2, %8 offset:%9\n\t"
                       FM("v222", "v238", "v217", MI_ "0x8") FM("v223", "v239", "v217", MI_ "0x8") "v_permlane32_swap_b32 v240, v242\n\t"
                       FM("v222", "v244", "v217", MI_ "0x4") FM("v223", "v245", "v217", MI_ "0x4") "v_permlane32_swap_b32 v241, v243\n\t"
                       FM("v222", "v246", "v216", R8_ "0x8") FM("v223", "v247", "v216", R8_ "0x8")
                       FM("v222", "v240", "v216", ID_ "0x1") FM("v223", "v241", "v216", ID_ "0x1") FM("v222", "v242", "v217", HM_ "0x1") FM("v223", "v243", "v217", HM_ "0x1")
                       "v_cvt_i32_f32 v212, v222\n\tv_cvt_i32_f32 v213, v223\n\tv_and_or_b32 v212, v212, %7, 2\n\tbuffer_atomic_add_x2 v[212:213], %4, %5, %6 offen\n\t"
                       "s_waitcnt lgkmcnt(0)"
                       : "+{v[224:255]}"(S), "+{v[208:223]}"(P), "=&v"(sw) : "v"(acco), "v"(acco2), "s"(rs), "s"(o2), "s"(~63u), "v"(lds0), "n"(L_MISC + 96) : "memory");
          __builtin_amdgcn_sched_barrier(0);
          tpub = now32();
          lds_inc<L_MISC + 104 + 4 * (w & 1)>(lds0);
          asm volatile("buffer_atomic_umax %0, %1, %2, %3 offen" :: "v"(tpub | 1u), "v"(tso), "s"(rs), "s"(o2) : "memory");
          STAMP(10);
        }
      }
    }
    STAMP(8);
    ARRIVE(1);
    o2p = o2;
    PIN_S(o2p);
    if (w < 2) {
      if (__builtin_expect(__builtin_amdgcn_readfirstlane(sw[1]) <= (unsigned)s, 0)) {
        lds_spin<L_MISC + 100, 0>(lds0, (unsigned)s + 1u);
        sw[0] = __hip_atomic_load((unsigned*)(misc + 24), __ATOMIC_RELAXED, __HIP_MEMORY_SCOPE_WORKGROUP);
      }
    } else {
      u32x4 q;
      asm volatile("1:\n\tds_read_b128 %0, %1 offset:%3\n\t.if %4\n\ts_sleep 1\n\t.endif\n\ts_waitcnt lgkmcnt(0)\n\tv_min3_u32 v201, v201, v202, v203\n\tv_cmp_ge_u32 vcc, %2, v201\n\ts_cbranch_vccnz 1b"
                   : "=&{v[200:203]}"(q) : "v"(lds0), "s"(s), "n"(L_MISC + 96), "n"(SP_SL) : "vcc", "memory");
      __builtin_amdgcn_s_setprio(3);
      tpub = now32();
      sw[0] = q[0];
    }
    STAMP(9);
    static_assert(F3C == 0 && F2C == 0 && F0C == 0, "nothing behind the atomics");
    LOGT(0, tpub); LOGT(1, tb2_);
    TSTAMP(1, tpub);
    ARRIVE_OUT();
    const unsigned an = __builtin_amdgcn_readfirstlane(sw[0]);
    const int ahead = (int)(an - tpub);
    tw = tpub + (unsigned)(an == 0u || ahead > 1000 ? SCH_FIRST : imax(ahead, w < 2 ? SCH_DMIN : 0));
    offs = tw - an;
  }
  STAMP_OUT();
  LOGP(4);
  __builtin_amdgcn_s_setprio(3);
  if (w < 2) { BAR(); tlast = cyc32(); }
  if (w == 2) tk.store(rs, o4);
  if (w == 3) {
    const unsigned oo = op_slot(NSEG - 2 - LAG4) + (unsigned)((m * 32 + gp) * 1280);
    const char* sp = lds + L_STG + 2048;
    const u32x4 v0 = *(const u32x4*)(sp + lane * 16), v1 = *(const u32x4*)(sp + 1280 + lane * 16), v2 = *(const u32x4*)(sp + (g & 1) * 1280 + 1024 + c * 16);
    SC1_STORE(v0, oo + lane * 16); SC1_STORE(v1, oo + 20480 + lane * 16); SC1_STORE(v2, lane < 32 ? oo + g * 20480 + 1024 + c * 16 : 0x7ffffff0u);
  }
  if (dead) tlast |= 1u; else tlast &= ~1u;
}

__device__ __forceinline__ void drain(const RecArgs& a, char* lds, const __amdgpu_buffer_rsrc_t rs, const int w, const unsigned tp1) {
  static_assert(LAG3 == 2 && LAG4 == 3 && LAG5 == 6, "drain schedule");
  const int lane = __builtin_amdgcn_workitem_id_x() & 63, b = __builtin_amdgcn_workgroup_id_x(), c = lane & 15;
  const int gp = (b & 7) * 2 + (b >> 7), m = (b >> 3) & 15, j = m & 7;
  unsigned* const misc = (unsigned*)(lds + L_MISC);
  const unsigned lds0 = (unsigned)(uintptr_t)(LAS char*)lds, ck = __builtin_amdgcn_readfirstlane(misc[11]);
  bool dead = tp1 & 1u;
  auto poll = [&](auto load, auto ready) {
    for (unsigned spins = 0;;) {
      asm volatile("" ::: "memory");
      load();
      if (ready() || dead) break;
      if (++spins > SPIN_CAP) dead = true;
      if (END_SL) __builtin_amdgcn_s_sleep(END_SL);
    }
  };
  LOGD(0);
#define TASK(ty, sg, tm) ((u64)((ty) | ((sg) - 56) << 2 | ((tm) / 100) << 5))
  u64 todo = w == 0 ? TASK(1, 63, END_E1) | TASK(2, 63, END_E2) << 12 | TASK(3, 62, END_E3) << 24
           : w == 1 ? TASK(1, 62, 0) | TASK(2, 62, END_E62) << 12 | TASK(3, 60, END_E3) << 24
           : w == 2 ? TASK(2, 60, 0) | TASK(3, 56, 0) << 12 : TASK(2, 61, END_E61) | TASK(3, 58, END_E59) << 12;
  TokTask tk;
  PvTask2 pv;
  OutTask ot;
  {
    const u32x4* p = (const u32x4*)(lds + L_DRN) + lane;
    const u32x4 q0 = p[0], q1 = p[64], q2 = p[128];
    tk.l0 = q0[0]; tk.l1 = q0[0] + 16384u; tk.lm = q0[1]; tk.s0 = q0[2]; tk.s1 = q0[3];
    pv.lo = q1[0]; pv.lm = q1[1]; pv.vr = q1[2]; pv.ma = q1[3]; pv.mz = q2[0]; pv.so = q2[1];
    pv.pa[0] = pv.pa[1] = pv.pb[0] = pv.pb[1] = (u32x4){0u, 0u, 0u, 0u};
    ot.t5f = q2[2]; ot.t5o = q2[3]; ot.sel = as8(p[192]);
  }
#pragma unroll 1
  for (int ti = 0; todo; todo >>= 12, ++ti) {
    const int ty = (int)todo & 3, sl = 56 + (((int)todo >> 2) & 7);
    const unsigned tm = tp1 + (((unsigned)todo >> 5) & 0x7fu) * 100u;
    if (ty == 1) {
      wait_cyc(tm);
      LOGD(3 * ti + 1);
      poll([&]() { tk.load(rs, ts_slot(ck, sl)); }, [&]() { return tk.ready(); });
      LOGD(3 * ti + 2);
      tk.run(j, lane, ot.sel); tk.store(rs, pt_slot(ck, sl));
    } else if (ty == 2) {
      const unsigned t0 = cyc32();
      if (sl >= NSEG - 2) { lds_wait(misc + 7, 4u * NSEG + 3u); wait_cyc(t0 + END_G2); }
      wait_cyc(tm);
      LOGD(3 * ti + 1);
      poll([&]() { pv.load(rs, pt_slot(ck, sl)); }, [&]() { return pv.ready(); });
      LOGD(3 * ti + 2);
      pv.run<true>(lds, sl % NV, lane, rs, op_slot(sl) + (unsigned)((m * 32 + gp) * 1280));
      if (sl == NSEG - 1) misc[14] = cyc32() | 1u;
    } else {
      if (sl >= NSEG - 4) { lds_wait(misc + 14, 1u); wait_cyc(__builtin_amdgcn_readfirstlane(misc[14]) + END_G3); }
      wait_cyc(tm);
      LOGD(3 * ti + 1);
      OutTask o2 = ot;
      poll([&]() { o2.load(a, rs, sl + 1, b); ot.load(a, rs, sl, b); }, [&]() { return ot.ready(b) & o2.ready(b); });
      LOGD(3 * ti + 2);
      o2.run(lane); o2.store(a, sl + 1, b, lane);
      ot.run(lane); ot.store(a, sl, b, lane);
    }
    LOGD(3 * ti + 3);
  }
  VM_DRAIN();
  if (w != 0) lds_inc<L_MISC + 64>(lds0);
  else {
    constexpr unsigned o2 = (unsigned)OFF_ACC + (unsigned)((NSEG - 1) % SR) * (unsigned)ACC_STEP;
    lds_wait(misc + 16, 3u);
    LOGD(10);
    if (lane == 0) __hip_atomic_fetch_add((gu64*)((GAS char*)(a.ws + o2) + acc_off(m * 4, 0)), (u64)1, RLX_AGENT);
    wait_until(now32() + SCH_DRAIN);
    wait_cyc(tp1 + END_E4);
    unsigned x;
    if (qkv_row(b) + 48 >= 55 && qkv_col(b) < 15) poll([&]() { x = __builtin_amdgcn_raw_buffer_load_b32(rs, acc_off(c * 4, 0), o2, 16); }, [&]() { return wave_all((x & 16u) != 0u); });
    lds_inc<L_MISC + 68>(lds0);
    LOGD(11);
  }
  if (qkv_row(b) + 48 >= 55 && qkv_col(b) < 15) {
    lds_wait(misc + 18, 8u);
    lds_spin<L_MISC + 68, 1>(lds0, 1u);
    x_fetch_n<10>(x_src(a, NSEG + 3, b), lds0, 3, 10 * w, lane * 16);
    VM_DRAIN();
    lds_inc<L_MISC + 68>(lds0);
  }
  LOGP(8);
}

__device__ __forceinline__ void proj_front(const RecArgs& a, char* lds, const __amdgpu_buffer_rsrc_t rs, const int v) {
  const int lane = __builtin_amdgcn_workitem_id_x() & 63, b = __builtin_amdgcn_workgroup_id_x(), c = lane & 15, g = lane >> 4;
  const int rb = (b & 7) + 8 * (b >> 5), cq = (b >> 3) & 3;
  const unsigned lds0 = (unsigned)(uintptr_t)(LAS char*)lds;
  unsigned* const cnt = (unsigned*)(lds + L_MISC) + 20;
  LOGF2(0);
  float* const red = (float*)(lds + L_RED1);
  float* const fin = red + 640;
  const f32x4 Z4 = {0.f, 0.f, 0.f, 0.f};
#pragma unroll
  for (int i = 0; i < 8; ++i) x_fetch((const u16*)(a.ws + OFF_OB) + (size_t)rb * 32 * DM, lds0, 0, 8 * v + i, lane);
  bf16x8 W[2][20];
#pragma unroll
  for (int k = 0; k < 40; ++k) W[k & 1][k >> 1] = *(const bf16x8*)(a.ws + OFF_WOBT + ((size_t)(cq * 10 + 2 * v + (k & 1)) * 20 + (k >> 1)) * 1024 + lane * 16);
  const unsigned fo = (unsigned)(rb * 40960 + (cq * 5 + v) * 1024 + ((g >> 1) * 16 + c) * 16), f8 = fo + (g & 1) * 8, f16 = fo + (g & 1) * 20480;
  u32x2 xo[2][2];
#pragma unroll
  for (int k = 0; k < 4; ++k)
    xo[k >> 1][k & 1] = __builtin_bit_cast(u32x2, __hip_atomic_load((const gu64*)((const char*)a.xnew + f8 + (k >> 1) * 512 + (k & 1) * 20480), RLX_AGENT));
  asm volatile("s_waitcnt vmcnt(44)" ::: "memory");
  lds_inc<L_MISC + 80>(lds0);
  lds_wait(cnt, 5u);
  LOGF2(1);
  f32x4 acc[2][2] = {{Z4, Z4}, {Z4, Z4}};
#pragma unroll
  for (int ks = 0; ks < 20; ++ks)
#pragma unroll
    for (int t = 0; t < 2; ++t) {
      const bf16x8 x = *(const bf16x8*)(lds + L_XN + (t * 20 + ks) * 1024 + lane * 16);
#pragma unroll
      for (int i = 0; i < 2; ++i) acc[i][t] = mfma32(W[i][ks], x, acc[i][t]);
    }
  u32x2 xp[2][2];
  PIN_V(acc[1][1]);
  LOGF2(2);
#pragma unroll
  for (int k = 0; k < 4; ++k) {
    const int i = k >> 1, t = k & 1;
#pragma unroll
    for (int q = 0; q < 2; ++q) {
      const unsigned r = pack2(acc[i][t][2 * q], acc[i][t][2 * q + 1]), x = xo[i][t][q];
      xp[i][t][q] = pack2(u2f(x << 16) + u2f(r << 16), u2f(x & 0xffff0000u) + u2f(r & 0xffff0000u));
      acc[i][t][2 * q] = u2f(xp[i][t][q] << 16); acc[i][t][2 * q + 1] = u2f(xp[i][t][q] & 0xffff0000u);
    }
    const float ml = x32sum(x16sum((acc[i][t][0] + acc[i][t][1]) + (acc[i][t][2] + acc[i][t][3]))) * (1.0f / 16.0f);
    float q = 0.f;
#pragma unroll
    for (int r = 0; r < 4; ++r) { const float d = acc[i][t][r] - ml; q += d * d; }
    q = x32sum(x16sum(q));
    if (g == 0) { red[(2 * v + i) * 32 + t * 16 + c] = ml; red[320 + (2 * v + i) * 32 + t * 16 + c] = q; }
  }
  lds_inc<L_MISC + 80>(lds0);
  if (v == 0) {
    lds_wait(cnt, 10u);
    LOGF2(3);
    float mw[10], ml = 0.f, m2 = 0.f;
#pragma unroll
    for (int i = 0; i < 10; ++i) { mw[i] = red[i * 32 + (lane & 31)]; ml += mw[i]; m2 += red[320 + i * 32 + (lane & 31)]; }
    ml *= 0.1f;
#pragma unroll
    for (int i = 0; i < 10; ++i) m2 += 16.0f * (mw[i] - ml) * (mw[i] - ml);
    const unsigned xs = (unsigned)OFF_LNX + rb * 1024;
    if (lane < 32) SC1_STORE8S(((u32x2){f2u(ml), nz(f2u(m2))}), (cq * 32 + lane) * 8, xs);
    wait_cyc(cyc32() + FR_D);
    u32x2 x0, x1;
    for (unsigned spins = 0; spins < SPIN_CAP; ++spins) {
      x0 = __builtin_amdgcn_raw_buffer_load_b64(rs, lane * 8, xs, 16); x1 = __builtin_amdgcn_raw_buffer_load_b64(rs, 512 + lane * 8, xs, 16);
      if (wave_all(x0[1] != 0u && x1[1] != 0u)) break;
      asm volatile("" ::: "memory");
    }
    const float m0 = u2f(x0[0]), m1 = u2f(x1[0]);
    const float mean = x32sum(m0 + m1) * 0.25f;
    const float M2 = x32sum((u2f(x0[1]) + u2f(x1[1])) + 160.0f * ((m0 - mean) * (m0 - mean) + (m1 - mean) * (m1 - mean)));
    const float mb = rbf(mean), den = rbf(__builtin_sqrtf(rbf(rbf(M2 * (1.0f / DM)) + 1e-5f)));
    if (lane < 32) {
      fin[lane] = mb; fin[32 + lane] = 1.0f / den;
      if (cq == 0) __hip_atomic_store((GAS unsigned*)(a.ws + OFF_XNRM) + (rb * 32 + lane) * 4, f2u(1.02f * (M2 + DM * (mean - mb) * (mean - mb)) / (den * den)), RLX_AGENT);
    }
    lds_inc<L_MISC + 84>(lds0);
  }
  lds_wait(cnt + 1, 1u);
  LOGF2(4);
  const __amdgpu_buffer_rsrc_t rx = __builtin_amdgcn_make_buffer_rsrc(a.ws + OFF_XN, 0, (int)(OFF_QB - OFF_XN), 0x00020000);
  auto put = [&](const u32x2 p0, const u32x2 p1, unsigned so) {
    const auto r0 = __builtin_amdgcn_permlane16_swap(p0[0], p1[0], false, false), r1 = __builtin_amdgcn_permlane16_swap(p0[1], p1[1], false, false);
    __builtin_amdgcn_raw_buffer_store_b128((u32x4){r0[0], r1[0], r0[1], r1[1]}, rx, f16, so, 16);
  };
#pragma unroll
  for (int i = 0; i < 2; ++i) {
    u32x2 y[2];
#pragma unroll
    for (int t = 0; t < 2; ++t) {
      const float mb = fin[t * 16 + c], rden = fin[32 + t * 16 + c];
      y[t] = (u32x2){pack2(rbf(acc[i][t][0] - mb) * rden, rbf(acc[i][t][1] - mb) * rden), pack2(rbf(acc[i][t][2] - mb) * rden, rbf(acc[i][t][3] - mb) * rden)};
    }
    put(y[0], y[1], i * 512);
  }
  VM_DRAIN();
  lds_inc<L_MISC + 80>(lds0);
#pragma unroll
  for (int i = 0; i < 2; ++i) put(xp[i][0], xp[i][1], (unsigned)(OFF_XB - OFF_XN) + i * 512);
  if (v == 0) {
    lds_wait(cnt, 15u);
    if (rb < 4 && lane < 8) __hip_atomic_fetch_add((GAS unsigned*)(a.ws + OFF_RDY) + lane * 64, 1u, RLX_AGENT);
  }
  LOGF2(5);
}

__global__ __launch_bounds__(512, 2) void rec_kernel(RecArgs a) {
  __shared__ __attribute__((aligned(16))) char lds[L_BYTES];
  const int tid = __builtin_amdgcn_workitem_id_x(), wid = __builtin_amdgcn_readfirstlane(tid >> 6), w = wid & 3;
  const __amdgpu_buffer_rsrc_t rs = __builtin_amdgcn_make_buffer_rsrc(a.ws, 0, (int)COMM_BYTES, 0x00020000);
  if (tid < 32) ((float*)(lds + L_MISC))[tid] = (tid == 4 || tid == 5) ? 1.0f : 0.0f;
  BAR();
  if (a.layer && wid < 5) proj_front(a, lds, rs, wid);
  if (wid < 4) {
    unsigned tlast;
    if (w == 0) rec_waves<0>(a, lds, rs, tlast);
    else if (w == 1) rec_waves<1>(a, lds, rs, tlast);
    else if (w == 2) rec_waves<2>(a, lds, rs, tlast);
    else rec_waves<3>(a, lds, rs, tlast);
    drain(a, lds, rs, w, tlast);
  } else {
    unsigned vo[2][2];
    if (w == 0) token_waves<0>(a, lds, rs, vo);
    else if (w == 1) token_waves<1>(a, lds, rs, vo);
    else if (w == 2) token_waves<2>(a, lds, rs, vo);
    else token_waves<3>(a, lds, rs, vo);
    qkv_tail(a, lds, w, vo);
  }
}

#if defined(__HIP_DEVICE_COMPILE__)
#pragma clang attribute pop
#endif

constexpr int KQ = S_ / 4;
#define ATT_P1 6
#define ATT_P2 10
#define ATT_P3 14
#define ATT_THR 6.0f
#define ATT_NQ 4
#define ATT_LMAX 1.0e12f
#if defined(__HIP_DEVICE_COMPILE__)
#pragma clang attribute push(__attribute__((target("no-packed-fp32-ops"))), apply_to = function)
#endif
__global__ __launch_bounds__(256 * ATT_NQ, 4) void attb_kernel(const u16* __restrict__ Qp, const u16* __restrict__ KV, u16* __restrict__ O) {
  __shared__ f32x4 mo_[ATT_NQ][4][2][3][64];
  __shared__ float mm_[ATT_NQ][4][2][64];
  const int tid = __builtin_amdgcn_workitem_id_x(), bid = __builtin_amdgcn_workgroup_id_x();
  __shared__ unsigned sink[64];
  own_code<5000>(tid, sink);
  const int lane = tid & 63, wid = __builtin_amdgcn_readfirstlane(tid >> 6), kh = wid / ATT_NQ, r = lane & 31, hh = lane >> 5;
  const int hd = (bid & 7) * 4 + ((bid >> 3) & 3), qb = (bid >> 5) * ATT_NQ + wid % ATT_NQ;
  const int q0 = qb * 64;
  bf16x8 qa[2]; u32x2 qb2[2];
#pragma unroll
  for (int qt = 0; qt < 2; ++qt) {
    { const u32x2* qp = (const u32x2*)(Qp + ((size_t)hd * S_ + q0 + qt * 32 + r) * QST + 8 * hh); const u32x2 q0_ = qp[0], q1_ = qp[1]; qa[qt] = mk8(q0_[0], q0_[1], q1_[0], q1_[1]); }
    const u32x2 t = *(const u32x2*)(Qp + ((size_t)hd * S_ + q0 + qt * 32 + r) * QST + 16);
    qb2[qt] = t;
  }
  const __amdgpu_buffer_rsrc_t rk = __builtin_amdgcn_make_buffer_rsrc((void*)(KV + ((size_t)hd * 64 + kh * 16) * (KVT / 2)), 0, 16 * KVT, 0x00020000);
  unsigned ok1 = lane * 16, ok2 = hh ? 1600 : 1024 + r * 8, ov = r > 20 ? 1u << 30 : 1280 + (hh * 21 + r) * 16;
  f32x16 o[2];
  float m[2];
  const f32x16 Z16 = {0.f, 0.f, 0.f, 0.f, 0.f, 0.f, 0.f, 0.f, 0.f, 0.f, 0.f, 0.f, 0.f, 0.f, 0.f, 0.f};
  int odd = 0xDDDD;
  asm volatile("" : "+v"(odd));
  struct KvT { u32x4 k1, v0, v1; u32x2 k2; };
  auto fetch_k = [&](KvT& x, int t) {
    x.k1 = __builtin_amdgcn_raw_buffer_load_b128(rk, ok1, imin(t, KQ / 32 - 1) * KVT, 0); x.k2 = __builtin_amdgcn_raw_buffer_load_b64(rk, ok2, imin(t, KQ / 32 - 1) * KVT, 0);
    __builtin_amdgcn_sched_barrier(0);
  };
  auto fetch_v = [&](KvT& x, int t) {
    x.v0 = __builtin_amdgcn_raw_buffer_load_b128(rk, ov, imin(t, KQ / 32 - 1) * KVT, 0); x.v1 = __builtin_amdgcn_raw_buffer_load_b128(rk, ov + 672, imin(t, KQ / 32 - 1) * KVT, 0);
    __builtin_amdgcn_sched_barrier(0);
  };
  auto fetch = [&](int t) { KvT x; __builtin_amdgcn_sched_barrier(0); fetch_k(x, t); fetch_v(x, t); return x; };
  auto qk1 = [&](const KvT& x, int qt) { return __builtin_amdgcn_mfma_f32_32x32x16_bf16(as8(x.k1), qa[qt], Z16, 0, 0, 0); };
  auto qk2 = [&](const KvT& x, int qt, f32x16 c) { return __builtin_amdgcn_mfma_f32_32x32x8bf16_1k(__builtin_bit_cast(v4i16_t, x.k2), __builtin_bit_cast(v4i16_t, qb2[qt]), c, 0, 0, 0); };
  auto pv = [&](const u32x4 v, const f32x16 p, int hi, f32x16 c) {
    const f32x8 ph = hi ? __builtin_shufflevector(p, p, 8, 9, 10, 11, 12, 13, 14, 15) : __builtin_shufflevector(p, p, 0, 1, 2, 3, 4, 5, 6, 7);
    return __builtin_amdgcn_smfmac_f32_32x32x32_bf16(__builtin_bit_cast(hbf16x8, v), __builtin_bit_cast(hbf16x16, ph), c, odd, 0, 0);
  };
  auto tile = [&](const KvT& x, auto MODE) {
    f32x16 sa[2];
#pragma unroll
    for (int qt = 0; qt < 2; ++qt) sa[qt] = qk1(x, qt);
#pragma unroll
    for (int qt = 0; qt < 2; ++qt) sa[qt] = qk2(x, qt, sa[qt]);
#pragma unroll
    for (int qt = 0; qt < 2; ++qt) {
      if (MODE != 0) {
        float mx = fmx(fmx(sa[qt][0], sa[qt][1]), sa[qt][2]);
#pragma unroll
        for (int i = 3; i < 15; i += 2) mx = fmx(fmx(mx, sa[qt][i]), sa[qt][i + 1]);
        mx = fmx(mx, sa[qt][15]);
        if (MODE == 1 || !wave_all(mx <= ATT_THR)) {
          mx = x32mx(mx);
          if (MODE != 1) mx = fmx(mx, 0.f);
          const float mn = m[qt] + mx;
          const unsigned h1 = f2bf(mn); const float r1 = mn - bf2f(h1);
          const unsigned h2 = f2bf(r1); const float r2 = r1 - bf2f(h2);
          const unsigned h3 = f2bf(r2);
          const float me = (bf2f(h1) + bf2f(h2)) + bf2f(h3);
          const float dm = me - m[qt];
          m[qt] = me;
          if (hh) qb2[qt] = (u32x2){(h1 | (h2 << 16)) ^ 0x80008000u, h3 ^ 0x8000u};
          const float alpha = __builtin_amdgcn_exp2f(-dm);
#pragma unroll
          for (int i = 0; i < 16; ++i) { if (MODE != 1) o[qt][i] *= alpha; sa[qt][i] -= dm; }
        }
      }
#pragma unroll
      for (int i = 0; i < 16; ++i) sa[qt][i] = __builtin_amdgcn_exp2f(sa[qt][i]);
      asm volatile("" : "+v"(sa[qt]));
      if (MODE == 1) { o[qt] = Z16; asm volatile("" : "+v"(o[qt])); }
      o[qt] = pv(x.v1, sa[qt], 1, pv(x.v0, sa[qt], 0, o[qt]));
      __builtin_amdgcn_sched_barrier(0);
    }
  };
  typedef std::integral_constant<int, 1> I1; typedef std::integral_constant<int, 2> I2;
  auto reset = [&]() {
    asm volatile("" : "+v"(ok1), "+v"(ok2), "+v"(ov) :: "memory");
    m[0] = m[1] = 0.f;
    if (hh) qb2[0] = qb2[1] = (u32x2){0u, 0u};
  };
  auto slow = [&]() {
    reset();
    tile(fetch(0), I1{});
#pragma unroll 1
    for (int t = 1; t < KQ / 32; ++t) tile(fetch(t), I2{});
  };
  f32x16 sc[2];
  auto phase = [&](int qt, const u32x4 vp, const KvT& xk, const u32x4 v0, auto mid) {
    auto ex = [&](int i0, int i1) {
#pragma unroll
      for (int i = i0; i < i1; ++i) sc[qt][i] = __builtin_amdgcn_exp2f(sc[qt][i]);
      asm volatile("" : "+v"(sc[qt]));
      __builtin_amdgcn_sched_barrier(0);
    };
    o[1 - qt] = pv(vp, sc[1 - qt], 1, o[1 - qt]);
    __builtin_amdgcn_sched_barrier(0);
    ex(0, 3);
    sc[1 - qt] = qk1(xk, 1 - qt);
    __builtin_amdgcn_sched_barrier(0);
    ex(3, 6);
    sc[1 - qt] = qk2(xk, 1 - qt, sc[1 - qt]);
    __builtin_amdgcn_sched_barrier(0);
    ex(6, 10);
    o[qt] = pv(v0, sc[qt], 0, o[qt]);
    __builtin_amdgcn_sched_barrier(0);
    mid();
    ex(10, 16);
  };
  auto fast = [&]() {
    reset();
    KvT xa = fetch(0), xb = fetch(1);
    tile(xa, I1{});
    fetch_k(xa, 2);
    sc[0] = qk2(xb, 0, qk1(xb, 0)); sc[1] = Z16;
    asm volatile("" : "+v"(sc[1]));
    auto step = [&](KvT& x, KvT& y, int t) {
      phase(0, y.v1, x, x.v0, [&]() { fetch_v(y, t + 1); fetch_k(x, t + 2); });
      phase(1, x.v1, y, x.v0, []() {});
    };
    step(xb, xa, 1);
#pragma unroll 1
    for (int t = 2; t < KQ / 32; t += 2) {
      if (t == ATT_P1) __builtin_amdgcn_s_setprio(2);
      if (t == ATT_P2) __builtin_amdgcn_s_setprio(1);
      if (t == ATT_P3) __builtin_amdgcn_s_setprio(0);
      step(xa, xb, t);
      step(xb, xa, t + 1);
    }
    o[1] = pv(xb.v1, sc[1], 1, o[1]);
  };
  if (ATT_P1) __builtin_amdgcn_s_setprio(3);
  fast();
  {
    float big = 0.f;
#pragma unroll
    for (int qt = 0; qt < 2; ++qt) {
      big = fmx(big, hh ? o[qt][8] * (3.0e38f / ATT_LMAX) : __builtin_fabsf(o[qt][8]));
#pragma unroll
      for (int i = 0; i < 12; ++i) if (i != 8) big = fmx(big, (i > 8 && hh) ? 0.f : __builtin_fabsf(o[qt][i]));
    }
    if (__builtin_expect(!wave_all(big <= 3.0e38f), 0)) slow();
  }
  f32x4 (*mo)[2][3][64] = mo_[wid % ATT_NQ];
  float (*mm)[2][64] = mm_[wid % ATT_NQ];
#pragma unroll
  for (int qt = 0; qt < 2; ++qt) {
#pragma unroll
    for (int k = 0; k < 3; ++k) mo[kh][qt][k][lane] = (f32x4){o[qt][4 * k], o[qt][4 * k + 1], o[qt][4 * k + 2], o[qt][4 * k + 3]};
    mm[kh][qt][lane] = m[qt];
  }
  const int qt = kh & 1, grp = kh >> 1, row = q0 + qt * 32 + r, k0 = hd * BD + 4 * hh + 8 * grp;
  auto oaddr = [&](int k) { return O + (((size_t)(row >> 4) * 20 + (k >> 5)) * 64 + ((k & 31) >> 3) * 16 + (row & 15)) * 8 + (k & 7); };
  u16 *op0 = oaddr(k0), *op1 = oaddr(k0 + 16);
  asm volatile("" : "+v"(op0), "+v"(op1));
  asm volatile("s_waitcnt lgkmcnt(0)\n\ts_barrier" ::: "memory");
  {
    float a[4], l[4];
    f32x4 u[4], x[4];
#pragma unroll
    for (int i = 0; i < 4; ++i) { a[i] = mm[i][qt][lane]; l[i] = mo[i][qt][2][lane | 32][0]; u[i] = mo[i][qt][grp][lane]; x[i] = mo[i][qt][2][lane]; }
    const float mn = fmx(fmx(a[0], a[1]), fmx(a[2], a[3]));
#pragma unroll
    for (int i = 0; i < 4; ++i) a[i] = __builtin_amdgcn_exp2f(a[i] - mn);
    const float inv = __builtin_amdgcn_rcpf((l[0] * a[0] + l[1] * a[1]) + (l[2] * a[2] + l[3] * a[3]));
    const f32x4 v = ((u[0] * a[0] + u[1] * a[1]) + (u[2] * a[2] + u[3] * a[3])) * inv;
    *(GAS u32x2*)op0 = (u32x2){pack2(v[0], v[1]), pack2(v[2], v[3])};
    if (grp == 0) {
      const f32x4 y = ((x[0] * a[0] + x[1] * a[1]) + (x[2] * a[2] + x[3] * a[3])) * inv;
      if (hh == 0) *(GAS u32x2*)op1 = (u32x2){pack2(y[0], y[1]), pack2(y[2], y[3])};
    }
  }
}
#if defined(__HIP_DEVICE_COMPILE__)
#pragma clang attribute pop
#endif

__global__ __launch_bounds__(640, 3) void proj_kernel(const u16* __restrict__ Of, const u16* __restrict__ Wf, const u16* __restrict__ xold,
                                                      u16* __restrict__ xnew) {
  __shared__ __attribute__((aligned(16))) char ob[40960];
  const int tid = threadIdx.x, lane = tid & 63, w = __builtin_amdgcn_readfirstlane(tid >> 6), g = lane >> 4, c = lane & 15;
  __shared__ unsigned sink[64];
  own_code<1500>(tid, sink);
  const int rb = (blockIdx.x & 7) + 8 * (blockIdx.x >> 5), cb = (blockIdx.x >> 3) & 3;
  const int col = cb * 160 + w * 16 + 4 * g;
  const u16* wp = Wf + (size_t)(cb * 10 + w) * 20 * 512 + lane * 8;
  u32x4 st[4];
#pragma unroll
  for (int i = 0; i < 4; ++i) st[i] = *(const u32x4*)(Of + (size_t)rb * 40 * 512 + (i * 640 + tid) * 8);
  bf16x8 wf[20];
#pragma unroll
  for (int ks = 0; ks < 20; ++ks) wf[ks] = *(const bf16x8*)(wp + (size_t)ks * 512);
  u32x2 xo[2];
#pragma unroll
  for (int t = 0; t < 2; ++t) xo[t] = *(const u32x2*)(xold + (((size_t)(rb * 2 + t) * 20 + (col >> 5)) * 64 + ((col & 31) >> 3) * 16 + c) * 8 + (col & 7));
#pragma unroll
  for (int i = 0; i < 4; ++i) *(u32x4*)(ob + (i * 640 + tid) * 16) = st[i];
  BAR();
  f32x4 acc[2] = {{0.f, 0.f, 0.f, 0.f}, {0.f, 0.f, 0.f, 0.f}};
#pragma unroll
  for (int ks = 0; ks < 20; ++ks)
#pragma unroll
    for (int t = 0; t < 2; ++t) acc[t] = mfma32(wf[ks], *(const bf16x8*)(ob + (t * 20 + ks) * 1024 + lane * 16), acc[t]);
#pragma unroll
  for (int t = 0; t < 2; ++t) {
    const unsigned r01 = pack2(acc[t][0], acc[t][1]), r23 = pack2(acc[t][2], acc[t][3]);
    const unsigned p01 = pack2(u2f(xo[t][0] << 16) + u2f(r01 << 16), u2f(xo[t][0] & 0xffff0000u) + u2f(r01 & 0xffff0000u));
    const unsigned p23 = pack2(u2f(xo[t][1] << 16) + u2f(r23 << 16), u2f(xo[t][1] & 0xffff0000u) + u2f(r23 & 0xffff0000u));
    *(u32x2*)(xnew + (size_t)(rb * 32 + t * 16 + c) * DM + col) = (u32x2){p01, p23};
  }
}

extern "C" void kernel_launch(void* const* d_in, const int* in_sizes, int n_in, void* d_out, int out_size, void* d_ws,
                              size_t ws_size, hipStream_t stream) {
  const u16* A_x = (const u16*)d_in[0];
  const u16* A_mem0 = (const u16*)d_in[1];
  const u16* A_Wq = (const u16*)d_in[2];
  const u16* A_Wk = (const u16*)d_in[3];
  const u16* A_Wv = (const u16*)d_in[4];
  const u16* A_Wo = (const u16*)d_in[5];
  const u16* B_wqkv = (const u16*)d_in[8];
  const u16* B_wo = (const u16*)d_in[9];
  char* ws = (char*)(((uintptr_t)d_ws + ((size_t)WS_OFF << 20) + WS_AL - 1) & ~(uintptr_t)(WS_AL - 1));
  u16* xa = (u16*)(ws + OFF_XA); u16* Ob = (u16*)(ws + OFF_OB);

  prologue_kernel<<<256, 640, 0, stream>>>(ProArgs{A_x, A_Wq, A_Wk, A_Wv, A_Wo, B_wqkv, B_wo, ws});
  for (int layer = 0; layer < 2; ++layer) {
    rec_kernel<<<256, 512, 0, stream>>>(RecArgs{A_mem0, A_Wq, A_Wk, (const u16*)(ws + OFF_XB), xa, ws, (unsigned)layer, 0u});
    attb_kernel<<<BH * 32 / ATT_NQ, 256 * ATT_NQ, 0, stream>>>((const u16*)(ws + OFF_QB), (const u16*)(ws + OFF_KV), Ob);
  }
  proj_kernel<<<256, 640, 0, stream>>>(Ob, (const u16*)(ws + OFF_WOBT), xa, (u16*)d_out);
}
```

```cpp
#include <hip/hip_runtime.h>
#include <hip/hip_bf16.h>
#include <stdint.h>
#include <type_traits>

typedef unsigned short u16;
typedef unsigned long long u64;
typedef __attribute__((ext_vector_type(8))) short bf16x8;
typedef __attribute__((ext_vector_type(16))) short bf16x16;
typedef __attribute__((ext_vector_type(4))) float f32x4;
typedef __attribute__((ext_vector_type(2))) float f32x2;
typedef __attribute__((ext_vector_type(16))) float f32x16;
typedef __attribute__((ext_vector_type(8))) float f32x8;
typedef __attribute__((ext_vector_type(4))) unsigned u32x4;
typedef __attribute__((ext_vector_type(2))) unsigned u32x2;
typedef __attribute__((ext_vector_type(3))) unsigned u32x3;
typedef __attribute__((ext_vector_type(8))) unsigned u32x8;
typedef __attribute__((ext_vector_type(16))) unsigned u32x16;
typedef __attribute__((ext_vector_type(32))) unsigned u32x32;
#define GAS __attribute__((address_space(1)))
#define LAS __attribute__((address_space(3)))
typedef GAS unsigned long long gu64;
#define BAR() do { __builtin_amdgcn_s_waitcnt(0xC07F); asm volatile("s_waitcnt lgkmcnt(0)\n\ts_barrier" ::: "memory"); } while (0)

constexpr int S_ = 2048, DM = 640;
constexpr int NH = 32, DH = 192, HD = NH * DH;
constexpr int MEM = 8, SEG = 32, NSEG = S_ / SEG;
constexpr int BH = 32, BD = 20;

constexpr size_t al256(size_t x) { return (x + 255) & ~(size_t)255; }
constexpr int LAG3 = 2, LAG4 = 3, LAG5 = 6;
#define SR_ 8
constexpr int SR = SR_;
constexpr unsigned SCT_STEP = 131072, OFF_SCM = SR * SCT_STEP, SCM_STEP = 32768;
constexpr int ACC_NB = 192;
constexpr size_t ACC_STEP = 65536;
constexpr unsigned ACC_TS = ACC_NB * 256;
constexpr size_t ACC_LAYER = SR * ACC_STEP;
constexpr size_t OFF_ACC = 3 * 524288;
static_assert(SR == 8, "regions");
__device__ __forceinline__ unsigned ts_piece(int i) { return ACC_TS + (unsigned)(i < 128 ? (i >> 1) * 256 + (i & 1) * 16 : 128 + (i & 1) * 8192); }
constexpr size_t OFF_LNX = OFF_SCM + SR * SCM_STEP;
constexpr size_t OFF_RDY = OFF_LNX + 65536;
constexpr size_t PG = 2097152;
#define RING_ 4
constexpr int RING = RING_;
constexpr size_t TS_SLOT = (size_t)NH * 8 * 2560, PT_SLOT = (size_t)NH * 2560, OP_SLOT = (size_t)256 * 2560;
__device__ __forceinline__ unsigned ts_tok(int h, int j) { return (unsigned)((h & 15) * 32768 + (j >> 2) * 16384 + (j & 3) * 2048); }
__device__ __forceinline__ unsigned ts_mem(int h, int j) { return (unsigned)(524288 + ((h & 15) >> 1) * 16384 + (h & 1) * 4096 + j * 512); }
__device__ __forceinline__ unsigned pt_tok(int h) { return (unsigned)(((h & 15) >> 2) * 16384 + (h & 3) * 2048); }
__device__ __forceinline__ unsigned pt_mem(int h) { return (unsigned)(65536 + (h & 15) * 512); }
constexpr size_t OFF_X = 2 * PG + TS_SLOT + 3 * PT_SLOT, X_BYTES = 7 * OP_SLOT + 4 * PT_SLOT;
constexpr size_t OFF_OP = OFF_X + TS_SLOT + 4 * PT_SLOT;
constexpr size_t COMM_BYTES = OFF_X + X_BYTES + RING * OP_SLOT;
__device__ __forceinline__ unsigned near_half(unsigned ck, unsigned o) { return o + ((ck >> (o >= 2 * PG ? 8 : 4)) & 8192u); }
__device__ __forceinline__ unsigned ts_slot(unsigned ck, int sl) {
  return near_half(ck, sl == NSEG - 1 ? (unsigned)OFF_X : ((sl & 3) == 3 ? (unsigned)(2 * PG) : (unsigned)PG + (unsigned)(sl & 3) * (unsigned)TS_SLOT));
}
__device__ __forceinline__ unsigned pt_slot(unsigned ck, int sl) {
  return near_half(ck, sl >= NSEG - 4 ? (unsigned)(OFF_X + TS_SLOT) + (unsigned)(sl - (NSEG - 4)) * (unsigned)PT_SLOT
                                      : ((sl & 3) == 0 ? (unsigned)(PG + 3 * TS_SLOT) : (unsigned)(2 * PG + TS_SLOT - PT_SLOT) + (unsigned)(sl & 3) * (unsigned)PT_SLOT));
}
__device__ __forceinline__ unsigned op_slot(int sl) { return (unsigned)OFF_OP + (unsigned)(sl >= NSEG - 6 ? sl - (NSEG - 6) : 6 + sl % RING) * (unsigned)OP_SLOT; }
static_assert(RING == 4 && OFF_OP <= 3 * PG && OFF_RDY + 2048 <= OFF_ACC && OFF_ACC + ACC_LAYER == PG && TS_SLOT % 16384 == 0 && PT_SLOT % 16384 == 0, "pages");
static_assert(TS_SLOT == OP_SLOT && RING >= 4 && NSEG % SR == 0 && SR >= 5 && X_BYTES % 4096 == 0 && X_BYTES <= 256 * 20 * 1024, "slots");
constexpr size_t OFF_WB = (COMM_BYTES + 16383) / 16384 * 16384, WB_BYTES = (size_t)256 * 200 * 1024;
__device__ __forceinline__ unsigned wb_off(int F) { const unsigned o = (unsigned)((F >> 3) * 16384 + (F & 7) * 1024); return o | ((((unsigned)OFF_WB + o) >> 8) & 8192u); }
constexpr size_t OFF_PHI = OFF_WB + WB_BYTES;
constexpr size_t OFF_BNRM = OFF_PHI + 1024;
constexpr size_t OFF_LAT = OFF_BNRM + 1024;
constexpr size_t OFF_XNRM = OFF_LAT + 2048;
constexpr size_t OFF_WQKVBT = OFF_XNRM + al256(S_ * 16);
constexpr size_t OFF_WOBT = OFF_WQKVBT + al256((size_t)3 * DM * DM * 2);
constexpr size_t OFF_XN = OFF_WOBT + al256((size_t)DM * DM * 2);
constexpr int QST = BD;
constexpr size_t OFF_XA = OFF_XN + al256((size_t)S_ * DM * 2);
constexpr size_t OFF_XB = OFF_XA + al256((size_t)S_ * DM * 2);
constexpr size_t OFF_QB = OFF_XB + al256((size_t)S_ * DM * 2);
constexpr int KVT = 2624;
constexpr size_t OFF_KV = OFF_QB + al256((size_t)BH * S_ * QST * 2);
constexpr size_t OFF_OB = OFF_KV + al256((size_t)BH * 64 * KVT);
constexpr size_t OFF_DBG = OFF_OB + al256((size_t)S_ * DM * 2);
constexpr size_t WS_TOTAL = OFF_DBG + (size_t)2 * 64 * 8 * DM * 2;
#define WS_AL 2097152
#define WS_OFF 0
static_assert(WS_TOTAL + WS_AL + ((size_t)WS_OFF << 20) <= ((size_t)256 << 20), "workspace budget");

#if defined(__HIP_DEVICE_COMPILE__)
#pragma clang attribute push(__attribute__((target("no-packed-fp32-ops"))), apply_to = function)
#endif
__device__ __forceinline__ float u2f(unsigned v) { return __builtin_bit_cast(float, v); }
__device__ __forceinline__ unsigned f2u(float v) { return __builtin_bit_cast(unsigned, v); }
__device__ __forceinline__ float bf2f(unsigned v) { return __builtin_bit_cast(float, v << 16); }
__device__ __forceinline__ unsigned f2bf(float f) { __bf16 b = (__bf16)f; return (unsigned)__builtin_bit_cast(u16, b); }
__device__ __forceinline__ float rbf(float f) { return bf2f(f2bf(f)); }
typedef __attribute__((ext_vector_type(2))) __bf16 hbf16x2;
__device__ __forceinline__ unsigned pack2(float lo, float hi) { f32x2 v = {lo, hi}; hbf16x2 b = __builtin_convertvector(v, hbf16x2); return __builtin_bit_cast(unsigned, b); }
__device__ __forceinline__ f32x4 mfma32(bf16x8 a, bf16x8 b, f32x4 c) { return __builtin_amdgcn_mfma_f32_16x16x32_bf16(a, b, c, 0, 0, 0); }
typedef __attribute__((ext_vector_type(8))) __bf16 hbf16x8;
typedef __attribute__((ext_vector_type(16))) __bf16 hbf16x16;
__device__ __forceinline__ f32x4 smfma64(bf16x8 a, bf16x16 b, f32x4 c, int idx) {
  return __builtin_amdgcn_smfmac_f32_16x16x64_bf16(__builtin_bit_cast(hbf16x8, a), __builtin_bit_cast(hbf16x16, b), c, idx, 0, 0);
}
__device__ __forceinline__ bf16x16 cat16(bf16x8 a, bf16x8 b) { return __builtin_shufflevector(a, b, 0, 1, 2, 3, 4, 5, 6, 7, 8, 9, 10, 11, 12, 13, 14, 15); }
__device__ __forceinline__ bf16x8 half8(bf16x16 v, int hi) { return hi ? __builtin_shufflevector(v, v, 8, 9, 10, 11, 12, 13, 14, 15) : __builtin_shufflevector(v, v, 0, 1, 2, 3, 4, 5, 6, 7); }
__device__ __forceinline__ bf16x8 mk8(unsigned a, unsigned b, unsigned c, unsigned d) { u32x4 v = {a, b, c, d}; return __builtin_bit_cast(bf16x8, v); }
__device__ __forceinline__ unsigned dpp_ror8(unsigned v) { return (unsigned)__builtin_amdgcn_mov_dpp((int)v, 0x128, 0xf, 0xf, true); }
__device__ __forceinline__ float fmx(float a, float b) { return __builtin_elementwise_maximum(a, b); }

__device__ __forceinline__ int imax(int a, int b) { return a > b ? a : b; }
__device__ __forceinline__ int imin(int a, int b) { return a < b ? a : b; }
__device__ __forceinline__ unsigned umin(unsigned a, unsigned b) { return a < b ? a : b; }
__device__ __forceinline__ unsigned umax(unsigned a, unsigned b) { return a > b ? a : b; }
__device__ __forceinline__ bool wave_all(bool p) { return __builtin_amdgcn_ballot_w64(!p) == 0ull; }
__device__ __forceinline__ float x16mx(float x) { unsigned u = __builtin_bit_cast(unsigned, x); auto r = __builtin_amdgcn_permlane16_swap(u, u, false, false); return fmx(u2f(r[0]), u2f(r[1])); }
__device__ __forceinline__ float x32mx(float x) { unsigned u = __builtin_bit_cast(unsigned, x); auto r = __builtin_amdgcn_permlane32_swap(u, u, false, false); return fmx(u2f(r[0]), u2f(r[1])); }
__device__ __forceinline__ float x16sum(float x) { unsigned u = __builtin_bit_cast(unsigned, x); auto r = __builtin_amdgcn_permlane16_swap(u, u, false, false); return u2f(r[0]) + u2f(r[1]); }
__device__ __forceinline__ float x32sum(float x) { unsigned u = __builtin_bit_cast(unsigned, x); auto r = __builtin_amdgcn_permlane32_swap(u, u, false, false); return u2f(r[0]) + u2f(r[1]); }
template <int CTRL> __device__ __forceinline__ float dppf(float v) { return u2f((unsigned)__builtin_amdgcn_mov_dpp((int)f2u(v), CTRL, 0xf, 0xf, true)); }
#define OWN_PF 1
template <int BYTES> __device__ __forceinline__ void own_code(int tid, unsigned* sink) {
  if (!OWN_PF || __builtin_amdgcn_readfirstlane(tid >> 6) * 4096 >= BYTES) return;
  const char* pc; unsigned keep;
  asm volatile("s_getpc_b64 %0" : "=s"(pc));
  asm volatile("s_mov_b32 %0, m0\n\ts_mov_b32 m0, %3\n\ts_nop 0\n\tglobal_load_lds_dword %1, %2\n\ts_mov_b32 m0, %0"
               : "=&s"(keep) : "v"(tid * 64 < BYTES ? tid * 64 : 0), "s"(pc), "s"((unsigned)(uintptr_t)(LAS unsigned*)sink) : "memory");
}
constexpr int DPP_XOR1 = 0xB1, DPP_XOR2 = 0x4E, DPP_ROR4 = 0x124, DPP_ROR8 = 0x128;
#if defined(__HIP_DEVICE_COMPILE__)
#pragma clang attribute pop
#endif

typedef short v4i16_t __attribute__((ext_vector_type(4)));
__device__ __forceinline__ bf16x8 tr_frag(const char* lo_, const char* hi_) {
  const v4i16_t lo = __builtin_amdgcn_ds_read_tr16_b64_v4i16((LAS v4i16_t*)lo_), hi = __builtin_amdgcn_ds_read_tr16_b64_v4i16((LAS v4i16_t*)hi_);
  return (bf16x8){lo[0], lo[1], lo[2], lo[3], hi[0], hi[1], hi[2], hi[3]};
}
__device__ __forceinline__ int rec_wg(int gp, int m) { return (((gp & 1) * 16 + m) << 3) | (gp >> 1); }
__device__ __forceinline__ float wsum(float v) {
  v += dppf<DPP_XOR1>(v); v += dppf<DPP_XOR2>(v); v += dppf<DPP_ROR4>(v); v += dppf<DPP_ROR8>(v);
  return x32sum(x16sum(v));
}
__device__ __forceinline__ float ln_row(int lane, u32x4 (&xo)[2]) {
  const bool two = lane < 16;
  float v[16];
#pragma unroll
  for (int hf = 0; hf < 2; ++hf)
#pragma unroll
    for (int i = 0; i < 4; ++i) { const unsigned u = xo[hf][i]; v[hf * 8 + 2 * i] = bf2f(u & 0xffff); v[hf * 8 + 2 * i + 1] = bf2f(u >> 16); }
  float s0 = 0.f, s1 = 0.f;
#pragma unroll
  for (int i = 0; i < 8; ++i) { s0 += v[i]; s1 += v[8 + i]; }
  const float mean = wsum(two ? s0 + s1 : s0) * (1.0f / DM);
  s0 = s1 = 0.f;
#pragma unroll
  for (int i = 0; i < 8; ++i) { const float d0 = v[i] - mean, d1 = v[8 + i] - mean; s0 += d0 * d0; s1 += d1 * d1; }
  const float var = rbf(wsum(two ? s0 + s1 : s0) * (1.0f / DM));
  const float mb = rbf(mean);
  const float rden = 1.0f / rbf(sqrtf(rbf(var + 1e-5f)));
  float n2[2] = {0.f, 0.f};
#pragma unroll
  for (int hf = 0; hf < 2; ++hf) {
    float y[8];
#pragma unroll
    for (int i = 0; i < 8; ++i) { y[i] = rbf(rbf(v[hf * 8 + i] - mb) * rden); n2[hf] += y[i] * y[i]; }
    xo[hf] = (u32x4){pack2(y[0], y[1]), pack2(y[2], y[3]), pack2(y[4], y[5]), pack2(y[6], y[7])};
  }
  return wsum(two ? n2[0] + n2[1] : n2[0]);
}

#define PROT(k)
#define WARM_CODE 1
#define B_AUX 16
#define X_AUX 16
#define T_AUX 16
#define WB_NEAR 1
#define PF_POS 0
#define PRB_N0 3
#define PRB_N1 0
#define PRB_LOG(i, v)
#define PLD(p) __builtin_nontemporal_load((const u32x4*)(p))
struct ProArgs { const u16* x; const u16* wq; const u16* wk; const u16* wv; const u16* wo; const u16* wqkvb; const u16* wob; char* ws; };
__global__ __launch_bounds__(640, 3) void prologue_kernel(ProArgs a) {
  __shared__ __attribute__((aligned(16))) char img[192 * 320];
  __shared__ __attribute__((aligned(16))) char slots[10 * 6144];
  __shared__ float red[10][160];
  __shared__ unsigned latw[10];
  __shared__ u32x4 xst[2][640];
  const int b = blockIdx.x, tid = threadIdx.x, lane = tid & 63, v = __builtin_amdgcn_readfirstlane(tid >> 6), c = lane & 15, g = lane >> 4;
  const int h = (b & 7) * 4 + ((b >> 3) & 3), dh = (b >> 5) & 1, cq = b >> 6, ks = dh * 10 + v;
  char* const slot = slots + v * 6144;
  PROT(0);
  __shared__ unsigned sink[64];
  own_code<9600>(tid, sink);
  const unsigned zo = v < 8 ? (unsigned)(((b >> 6) * 512 + ((b & 7) * 2 + (v >> 2)) * 32 + ((b >> 3) & 7) * 4 + (v & 3)) * 1024) : (unsigned)(((v - 7) * 2048 + (b & 7) * 32 + ((b >> 3) & 7) * 4 + (b >> 6)) * 1024);
  __builtin_amdgcn_raw_buffer_store_b128((u32x4){0u, 0u, 0u, 0u}, __builtin_amdgcn_make_buffer_rsrc(a.ws, 0, (int)(3 * PG), 0x00020000), zo + lane * 16, 0, 16);
  __builtin_amdgcn_sched_barrier(0);
  u32x4 wa[2][6], wb[6], tr[4], xo[2];
  const int xrow = b * 8 + (v & 7);
  xo[0] = *(const u32x4*)(a.x + (size_t)xrow * DM + lane * 8);
  xo[1] = *(const u32x4*)(a.x + (size_t)xrow * DM + 512 + c * 8);
  const int job = imin(b + 256 * (9 - v), 800);
  const int jn = job < 600 ? job / 20 : (job % 200) / 20, jk = job % 20;
  {
    const u16* src = job < 600 ? a.wqkvb + (size_t)(32 * jk) * 3 * DM : a.wob + (size_t)(32 * jk) * DM;
#pragma unroll
    for (int i = 0; i < 4; ++i) tr[i] = *(const u32x4*)(src + (size_t)(i * 8 + (lane >> 3)) * (job < 600 ? 3 * DM : DM) + jn * 64 + (lane & 7) * 8);
  }
#pragma unroll
  for (int i = 0; i < 6; ++i) {
    const int idx = (v * 6 + i) * 64 + lane, row = idx / 20;
    wb[i] = PLD(a.wo + (size_t)(h * DH + row) * DM + cq * 160 + (idx - row * 20) * 8);
  }
#pragma unroll
  for (int i = 0; i < 6; ++i) {
    const int idx = i * 64 + lane, r = idx / 24;
#pragma unroll
    for (int u = 0; u < 2; ++u) wa[u][i] = PLD(a.wv + (size_t)(32 * ks + 8 * (r >> 2) + (r & 3) + 4 * u) * HD + h * DH + (idx - r * 24) * 8);
  }
  unsigned pf[2] = {0u, 0u};
  auto prefetch = [&]() {
#pragma unroll
    for (int i = 0; i < 2; ++i) pf[i] = *(const unsigned*)((i ? a.wq : a.wk) + ((size_t)(b * 10 + v) * 48 + imin(lane, 47)) * 32);
  };
  if (PF_POS == 0) prefetch();
  __builtin_amdgcn_sched_barrier(0);
  unsigned lat = ~0u;
  auto probe = [&](int n0, int n) {
    const char* p = a.ws + zo;
#pragma unroll 1
    for (int i = n0; i < n0 + n; ++i) {
      u64 t0, t1; unsigned d;
      asm volatile("s_memtime %0\n\ts_waitcnt lgkmcnt(0)\n\ts_load_dword %2, %3, %4\n\ts_waitcnt lgkmcnt(0)\n\ts_memtime %1\n\ts_waitcnt lgkmcnt(0)"
                   : "=&s"(t0), "=&s"(t1), "=&s"(d) : "s"(p), "s"((i & 3) * 256 + (i >> 2) * 128) : "memory");
      PRB_LOG(i, (unsigned)t1 - (unsigned)t0);
      lat = umin(lat, (unsigned)t1 - (unsigned)t0);
    }
  };
  asm volatile("s_waitcnt vmcnt(26)" ::: "memory");
  probe(0, PRB_N0);
  latw[v] = lat;
  __builtin_amdgcn_sched_barrier(0);
  auto xput = [&](int k) {
    if (v < 8) { xst[k][lane * 8 + (v ^ (lane & 7))] = xo[0]; if (lane < 16) xst[k][(64 + lane) * 8 + (v ^ (lane & 7))] = xo[1]; }
  };
  xput(0);
  const float xn2 = u2f(__builtin_amdgcn_readfirstlane(f2u(ln_row(lane, xo))));
  xput(1);
#pragma unroll
  for (int i = 0; i < 4; ++i) *(u32x4*)(slot + (i * 8 + (lane >> 3)) * 144 + (lane & 7) * 16) = tr[i];
  __builtin_amdgcn_fence(__ATOMIC_RELEASE, "wavefront"); __builtin_amdgcn_wave_barrier();
  bf16x8 tf[4];
#pragma unroll
  for (int t = 0; t < 4; ++t) {
    const char* bp = slot + (8 * g + (c >> 2)) * 144 + t * 32 + (c & 3) * 8;
    tf[t] = tr_frag(bp, bp + 4 * 144);
  }
  __builtin_amdgcn_fence(__ATOMIC_RELEASE, "wavefront"); __builtin_amdgcn_wave_barrier();
  __builtin_amdgcn_sched_barrier(0);
#pragma unroll
  for (int i = 0; i < 6; ++i) {
    const int idx = (v * 6 + i) * 64 + lane, row = idx / 20;
    *(u32x4*)(img + row * 320 + (((idx - row * 20) ^ ((row >> 2) & 2)) * 16)) = wb[i];
  }
  bf16x8 A[2][6];
#pragma unroll
  for (int u = 0; u < 2; ++u) {
#pragma unroll
    for (int i = 0; i < 6; ++i) {
      const int idx = i * 64 + lane, r = idx / 24;
      *(u32x4*)(slot + r * 384 + (((idx - r * 24) ^ (r >> 1)) * 16)) = wa[u][i];
    }
    __builtin_amdgcn_fence(__ATOMIC_RELEASE, "wavefront"); __builtin_amdgcn_wave_barrier();
#pragma unroll
    for (int e = 0; e < 6; ++e) A[u][e] = *(const bf16x8*)(slot + c * 384 + (((4 * e + g) ^ (c >> 1)) * 16));
    __builtin_amdgcn_fence(__ATOMIC_RELEASE, "wavefront"); __builtin_amdgcn_wave_barrier();
  }
  BAR();
  PROT(1);
  if (PF_POS == 0) asm volatile("" :: "v"(pf[0]), "v"(pf[1]));
  if (PF_POS == 1) prefetch();
  {
    const unsigned xf = (unsigned)((b >> 1) * 20480 + (tid >> 3) * 256 + (b & 1) * 128 + (tid & 7) * 16);
    const __amdgpu_buffer_rsrc_t rx = __builtin_amdgcn_make_buffer_rsrc(a.ws + OFF_XN, 0, (int)(OFF_QB - OFF_XN), 0x00020000);
    __builtin_amdgcn_raw_buffer_store_b128(xst[1][tid ^ ((tid >> 3) & 7)], rx, xf, 0, X_AUX);
    __builtin_amdgcn_raw_buffer_store_b128(xst[0][tid ^ ((tid >> 3) & 7)], rx, xf + (unsigned)(OFF_XB - OFF_XN), 0, X_AUX);
    if (v < 8 && lane < 4) ((float*)(a.ws + OFF_XNRM))[xrow * 4 + lane] = lane == 0 ? xn2 : 0.f;
  }
  if (job < 800) {
    const __amdgpu_buffer_rsrc_t rt = __builtin_amdgcn_make_buffer_rsrc(a.ws + (job < 600 ? OFF_WQKVBT : OFF_WOBT) + ((size_t)jn * 80 + jk) * 1024, 0, 81920, 0x00020000);
#pragma unroll
    for (int t = 0; t < 4; ++t) __builtin_amdgcn_raw_buffer_store_b128(__builtin_bit_cast(u32x4, tf[t]), rt, lane * 16 + t * 20480, 0, T_AUX);
  }
  const int gp = h >> 1, hh = h & 1;
  const __amdgpu_buffer_rsrc_t rwb = __builtin_amdgcn_make_buffer_rsrc(a.ws + OFF_WB, 0, (int)WB_BYTES, 0x00020000);
  const unsigned phi = WB_NEAR ? ((((unsigned)b >> 4) & 1u) ^ (unsigned)(latw[8] < latw[9]) ^ (WB_NEAR - 1)) << 13 : (unsigned)(b & 32) << 8;
  if (tid == 0) ((unsigned*)(a.ws + OFF_PHI))[b] = phi;
#pragma unroll
  for (int n = 0; n < 10; ++n) {
    const int off = n * 32 + 8 * (c & 3), T = cq * 10 + n;
    const char* bp = img + (8 * g + (c >> 2)) * 320 + (off ^ ((g & 1) << 5));
    f32x4 ac[2] = {{0.f, 0.f, 0.f, 0.f}, {0.f, 0.f, 0.f, 0.f}};
#pragma unroll
    for (int e = 0; e < 6; ++e) {
      const bf16x8 bf = tr_frag(bp + e * 32 * 320, bp + (e * 32 + 4) * 320);
      ac[0] = mfma32(A[0][e], bf, ac[0]); ac[1] = mfma32(A[1][e], bf, ac[1]);
    }
    const u32x4 o = {pack2(ac[0][0], ac[0][1]), pack2(ac[0][2], ac[0][3]), pack2(ac[1][0], ac[1][1]), pack2(ac[1][2], ac[1][3])};
    const unsigned oo = (T < 32 ? wb_off(rec_wg(gp, T & 15) * 100 + (hh * 3 + (T >> 4)) * 20 + ks) + (unsigned)(lane * 16)
                                : wb_off(rec_wg(gp, 2 * (T - 32) + (c >> 3)) * 100 + 40 + ks) + (unsigned)((g * 16 + hh * 8 + (c & 7)) * 16)) ^ phi;
    __builtin_amdgcn_raw_buffer_store_b128(o, rwb, oo, 0, B_AUX);
    float n2 = 0.f;
#pragma unroll
    for (int r = 0; r < 4; ++r) n2 += ac[0][r] * ac[0][r] + ac[1][r] * ac[1][r];
    n2 = x32sum(x16sum(n2));
    if (g == 0) red[v][n * 16 + c] = n2;
  }
  PROT(2);
  if (PF_POS == 1) asm volatile("" :: "v"(pf[0]), "v"(pf[1]));
  if (v >= 8 && b < 64 && lane == 0) ((unsigned*)(a.ws + OFF_LAT))[(v - 4) * 64 + b] = umin(lat, 60000u);
  if (WARM_CODE && v == 7) {
    const char* pc; int d0, d1;
    asm("s_getpc_b64 %0\n\ts_mov_b32 %1, _Z10rec_kernel7RecArgs@rel32@lo+4\n\ts_mov_b32 %2, _Z11proj_kernelPKtS0_S0_Pt@rel32@lo+12" : "=s"(pc), "=s"(d0), "=s"(d1));
    const char *c0 = pc + d0, *c1 = pc + d1;
    const long o = ((long)b * 64 + lane) * 64;
    if (o < c1 - c0) asm volatile("" :: "v"(*(const volatile GAS unsigned*)(c0 + o)));
  }
  if (v == 9 && lane < 32) {
    const int q = b * 32 + lane;
    *(u32x4*)(a.ws + OFF_KV + (size_t)(q >> 2) * KVT + 1280 + (q & 3) * 336 + 320) = (u32x4){0x3f803f80u, 0x3f803f80u, 0x3f803f80u, 0x3f803f80u};
  }
  BAR();
  if (v == 1 && lane == 0) ((unsigned*)(a.ws + OFF_LAT))[b] = umin(umin(umin(latw[0], latw[4]) + umin(latw[1], latw[5]), 30000u) + umin(umin(latw[2], latw[6]) + umin(latw[3], latw[7]), 30000u), 60000u);
  if (v == 0) {
    float mx = 0.f;
#pragma unroll
    for (int k = 0; k < 3; ++k) {
      const int col = k * 64 + lane;
      float sm = 0.f;
#pragma unroll
      for (int i = 0; i < 10; ++i) sm += red[i][col < 160 ? col : 0];
      mx = fmx(mx, sm);
    }
    mx = fmx(mx, dppf<DPP_XOR1>(mx)); mx = fmx(mx, dppf<DPP_XOR2>(mx)); mx = fmx(mx, dppf<DPP_ROR4>(mx)); mx = fmx(mx, dppf<DPP_ROR8>(mx));
    mx = x32mx(x16mx(mx));
    if (lane == 0) ((float*)(a.ws + OFF_BNRM))[(cq * 32 + h) * 2 + dh] = mx;
  }
  PROT(3);
}

struct RecArgs {
  const u16* mem0; const u16* wq; const u16* wk; const u16* xold; u16* xnew; char* ws;
  unsigned layer; unsigned pad;
};
#define RLX_AGENT __ATOMIC_RELAXED, __HIP_MEMORY_SCOPE_AGENT
constexpr unsigned SPIN_CAP = 1u << 20;
#define TSTAMP(k, v)
#define ARRIVE(k)
#define ARRIVE_OUT()
#define ARRIVE_DECL()
#define LOGV(k, v)
#define LOGF(k, v)
#define LOGP(k)
#define LOGQ(k)
#define LOGF2(k)
#define LOGD(k)
#define LOGT(k, v)
#define STAMP_DECL()
#define STAMP(k)
#define STAMPV(k, v)
#define STAMP_OUT()
#define REC_D1 520
#define SCH_V 18
#define SCH_VMIN 0
#define SCH_DMIN 4
#define SCH_PEN 150
#define SCH_VMIN2 (-4)
#define SCH_PEN2 40
#define SCH_GU 1
#define SCH_GD 2
#define SCH_UP 6
#define SCH_D0 196
#define SCH_FIRST 120
#define EXP_NODMA 0
#define BG_HOLD 220
#define QBATCH 0
#define WPF_N 5
#define WPF_W 0
#define WPF0 (63 - WPF_N)
#define BG_HOLD4 250
#define D1_W1 20
#define F3_D 1200
#define T2W 2
#define AT_ASM 1
#define PM_Z 1
#define P2_X 1
#define ST_KS 14
#define ST_KS0 ST_KS
#define ST_KS1 ST_KS
#define PADR 0x6666
#define PADT 0x000
#define CODE_PAD(n) asm volatile(".p2align 6\n.rept %0\ns_nop 0\n.endr" :: "n"(n))
#define ST_X 300
#define ST_ALL 4
#define CPF_S 61
#define FR_D 700
#define FR_PACE 6
#define END_E60 600
#define END_E1 1300
#define END_E2 3400
#define END_E62 2800
#define END_E3 6800
#define END_E4 11400
#define EARLY_N 6
#define EARLY_D1 0
#define EARLY_V 4
#define EARLY_V1 10
#define F3 0
#define F3A 0
#define F3A_D 400
#define T4_D 300
#define T4_P 1
#define P2_W 1
#define PS_SL 1
#define F2B 4
#define F0C 0
#define F2C 0
#define W2_P 1
#define SCH_W 1
#define SP_SL 0
#define D1_M 300
#define D1_MIN 260
#define D1_C 96
#define D1_G 2
#define BG_HOLD6 0
#define W2_D 0
#define END_E61 2600
#define D1_LATE 200
#define P2_G0 10
#define P2_G2 6
#define P2_G3 6
#define SCH_RL 0
#define SCH_P 3
#define MMA_P 0
#define SP_P 0
#define F6 8
#define F7 10
#define XPF_W 2
#define SCH_LATE 0
#define LATE_V 6
#define QW 3
#define FB_SL 0
#define P2_G1 1
#define P1_G 0
#define F4Z 0
#define F5Z 0
#define T5_Z 2
#define F0A 3
#define FB_E 0
#define EXACT 1
#define D1_P 3
#define VO_A0 9
#define VO_A2 10
#define VO_B0 3
#define VO_B1 0
#define VO_N2 7
#define VO_N3 10
#define VO_W -1
#define UN_P 2
#define VP_E 1
#define TS_B 0
#define ST_E 1
#define W1_SL 0
#define VO_A1 4
#define LAST_V 18
#define END_E59 3200
#define END_G2 600
#define END_G3 600
#define QR 4
#define WPF_S 128
#define END_SL 4
#define SCH_DR 184
#define SCH_DK 0
#define KP0 0
#define KP1 0
#define XPF 3
#define W1_D 0
#define F4 12
#define F5 0
#define F67 0
#define KP2 0
#define REC_RNG 70u
#define SCH_DRAIN 40
#define PL_X 0
#define FC(k)
#define PIN_V(x) asm volatile("" : "+v"(x))
#define PIN_S(x) asm volatile("" : "+s"(x))
#define VM_DRAIN() do { asm volatile("s_waitcnt vmcnt(0)" ::: "memory"); __builtin_amdgcn_s_waitcnt(0x0F70); } while (0)
#define SC1_LOAD(off) __builtin_amdgcn_raw_buffer_load_b128(rs, (off), 0, 16)
#define SC1_LOADS(off, soff) __builtin_amdgcn_raw_buffer_load_b128(rs, (off), (soff), 16)
#define SC1_STORE8S(v, off, soff) __builtin_amdgcn_raw_buffer_store_b64((v), rs, (off), (soff), 16)
#define SC1_LOAD8(off) __builtin_amdgcn_raw_buffer_load_b64(rs, (off), 0, 16)
#define SC1_STORE(v, off) __builtin_amdgcn_raw_buffer_store_b128((v), rs, (off), 0, 16)
#define SC1_STORE8(v, off) __builtin_amdgcn_raw_buffer_store_b64((v), rs, (off), 0, 16)
#if defined(__HIP_DEVICE_COMPILE__)
#pragma clang attribute push(__attribute__((target("no-packed-fp32-ops"))), apply_to = function)
#endif
__device__ __forceinline__ unsigned now32() { return (unsigned)__builtin_amdgcn_s_memrealtime(); }
__device__ __forceinline__ void wait_until(unsigned t) { for (;;) { const int d = (int)(t - now32()); if (d <= 0) break; if (d > 8) __builtin_amdgcn_s_sleep(1); } }
__device__ __forceinline__ unsigned cyc32() { return (unsigned)__builtin_amdgcn_s_memtime(); }
__device__ __forceinline__ void wait_cyc(unsigned t) { for (;;) { const int d = (int)(t - cyc32()); if (d <= 0) break; if (d > 200) __builtin_amdgcn_s_sleep(1); } }
#define WAIT_ASM(clk, far_) "1:\n\t" clk " vcc\n\ts_waitcnt lgkmcnt(0)\n\ts_sub_i32 %0, %1, vcc_lo\n\ts_cmp_lt_i32 %0, " far_ "\n\ts_cbranch_scc1 3f\n\ts_sleep 1\n\ts_branch 1b\n" \
                            "2:\n\t" clk " vcc\n\ts_waitcnt lgkmcnt(0)\n\ts_sub_i32 %0, %1, vcc_lo\n3:\n\ts_cmp_gt_i32 %0, 0\n\ts_cbranch_scc1 2b"
__device__ __forceinline__ int spin_until(unsigned t) { int d; asm volatile(WAIT_ASM("s_memrealtime", "9") : "=&s"(d) : "s"(t) : "vcc", "scc", "memory"); return d; }
__device__ __forceinline__ int spin_cyc(unsigned t) { int d; asm volatile(WAIT_ASM("s_memtime", "200") : "=&s"(d) : "s"(t) : "vcc", "scc", "memory"); return d; }
__device__ __forceinline__ void spin_exact(unsigned t) {
  if (!EXACT) { spin_cyc(t); return; }
  int d;
  asm volatile("1:\n\ts_memtime vcc\n\ts_waitcnt lgkmcnt(0)\n\ts_sub_i32 %0, %1, vcc_lo\n\ts_cmp_gt_i32 %0, 95\n\ts_cbranch_scc1 1b\n\t"
               "s_max_i32 %0, %0, 0\n\ts_andn2_b32 %0, %0, 3\n\ts_sub_i32 %0, 3f-2f, %0\n\ts_getpc_b64 vcc\n"
               "2:\n\ts_add_u32 vcc_lo, vcc_lo, %0\n\ts_addc_u32 vcc_hi, vcc_hi, 0\n\ts_setpc_b64 vcc\n\t.rept 24\n\ts_nop 0\n\t.endr\n3:"
               : "=&s"(d) : "s"(t) : "scc", "vcc", "memory");
}
template <int OFF, int SL> __device__ __forceinline__ void lds_spin(unsigned lds0, unsigned want) {
  unsigned v;
  asm volatile("1:\n\tds_read_b32 %0, %1 offset:%3\n\t.if %4\n\ts_sleep %4\n\t.endif\n\ts_waitcnt lgkmcnt(0)\n\tv_cmp_gt_u32 vcc, %2, %0\n\ts_cbranch_vccnz 1b"
               : "=&v"(v) : "v"(lds0), "s"(want), "n"(OFF), "n"(SL) : "vcc", "memory");
}
template <int CTRL> __device__ __forceinline__ int dppi(int v) { return __builtin_amdgcn_mov_dpp(v, CTRL, 0xf, 0xf, true); }
__device__ __forceinline__ int wave_imax(int l) {
  l = imax(l, dppi<DPP_XOR1>(l)); l = imax(l, dppi<DPP_XOR2>(l)); l = imax(l, dppi<DPP_ROR4>(l)); l = imax(l, dppi<DPP_ROR8>(l));
  int a = imax(__builtin_amdgcn_readlane(l, 0), __builtin_amdgcn_readlane(l, 16)), b = imax(__builtin_amdgcn_readlane(l, 32), __builtin_amdgcn_readlane(l, 48));
  PIN_S(a); PIN_S(b);
  return imax(a, b);
}
__device__ __forceinline__ void glds16(const void* gsrc, unsigned lds_dst) {
  unsigned keep;
  asm volatile("s_mov_b32 %0, m0\n\ts_mov_b32 m0, %2\n\ts_nop 0\n\tglobal_load_lds_dwordx4 %1, off\n\ts_mov_b32 m0, %0" : "=&s"(keep) : "v"(gsrc), "s"(lds_dst) : "memory");
}
__device__ __forceinline__ void glds16_sc1(const void* gsrc, unsigned lds_dst) {
  unsigned keep;
  asm volatile("s_mov_b32 %0, m0\n\ts_mov_b32 m0, %2\n\ts_nop 0\n\tglobal_load_lds_dwordx4 %1, off sc1\n\ts_mov_b32 m0, %0" : "=&s"(keep) : "v"(gsrc), "s"(lds_dst) : "memory");
}
__device__ __forceinline__ unsigned acc_off(int G, int row) {
  return (unsigned)(G < 128 ? ((G & 31) | ((G & 32) << 1) | ((G & 64) >> 1)) * 256 + row * 16 : (2 * G - 128 + (row >> 2)) * 256 + (row & 3) * 16);
}
__device__ __forceinline__ unsigned nz(unsigned v) { return v > 1u ? v : 1u; }
__device__ __forceinline__ unsigned pack2n(float lo, float hi) { return nz(pack2(lo, hi)); }
__device__ __forceinline__ unsigned min4(u32x4 v) { return umin(umin(v[0], v[1]), umin(v[2], v[3])); }
__device__ __forceinline__ bf16x8 as8(u32x4 v) { return __builtin_bit_cast(bf16x8, v); }

constexpr int NV = 7, NKQ = 2;
constexpr int L_RED1 = 0;
constexpr int L_BM = L_RED1 + 8 * 1536;
constexpr int L_STG = L_BM + 10240;
constexpr int L_ST = L_STG + 2048 + 2560;
constexpr int L_MISC = L_ST + 512;
constexpr int L_KX = L_MISC + 128;
constexpr int L_QX = L_KX + NKQ * 2048;
constexpr int L_XN = L_QX + NKQ * 2048;
constexpr int L_VOX = L_XN + 81920;
constexpr int L_VOM = L_VOX + NV * 5120;
constexpr int L_BYTES = L_VOM + NV * 1280;
constexpr int S_IMG = L_XN + 40960;
static_assert(S_IMG + 61440 <= L_VOM, "start-up image");
static_assert(L_BYTES <= 160 * 1024, "LDS budget");

__device__ __forceinline__ bf16x8 wqk_frag(const char* lds, int t, int ks, int lane) {
  const int c = lane & 15, g = lane >> 4, p = c & 3;
  const char* q = lds + S_IMG + (32 * ks + 8 * g + (c >> 2)) * 96 + 8 * p + (t == 0 ? 0 : (t == 1 ? 48 : (p < 2 ? 32 : 64)));
  const v4i16_t lo = __builtin_amdgcn_ds_read_tr16_b64_v4i16((LAS v4i16_t*)q), hi = __builtin_amdgcn_ds_read_tr16_b64_v4i16((LAS v4i16_t*)(q + 4 * 96));
  return (bf16x8){lo[0], lo[1], lo[2], lo[3], hi[0], hi[1], hi[2], hi[3]};
}
__device__ __forceinline__ bf16x8 sel_frag(int c, int base) {
  bf16x8 r;
#pragma unroll
  for (int i = 0; i < 8; ++i) r[i] = (c == base + i) ? (short)0x3f80 : (short)0;
  return r;
}
__device__ __forceinline__ bf16x8 qk_frag(const char* p4, const char* p2, const char* p2b) {
  f32x4 a0 = *(const f32x4*)(p4), a1 = *(const f32x4*)(p4 + 1792), a2 = *(const f32x4*)(p4 + 3584), a3 = *(const f32x4*)(p4 + 5376);
  f32x2 b0 = *(const f32x2*)(p2), b1 = *(const f32x2*)(p2 + 1792), b2 = *(const f32x2*)(p2b), b3 = *(const f32x2*)(p2b + 1792);
  f32x4 va = (a0 + a1) + (a2 + a3);
  f32x2 vb = (b0 + b1) + (b2 + b3);
  return mk8(pack2(va[0], va[1]), pack2(va[2], va[3]), pack2(vb[0], vb[1]), 0u);
}
__device__ __forceinline__ void x_fetch(const u16* src, unsigned lds0, int seg, int p, int lane) {
  if (EXP_NODMA) return;
  glds16_sc1(src + (size_t)p * 512 + lane * 8, lds0 + L_XN + (seg & 1) * 40960 + p * 1024);
}
template <int N> __device__ __forceinline__ void x_fetch_n(const u16* src, unsigned lds0, int seg, int p0, unsigned voff) {
  if (EXP_NODMA) return;
#pragma unroll
  for (int i = 0; i < N; i += 4) {
    unsigned keep;
    asm volatile("s_mov_b32 %0, m0\n\ts_mov_b32 m0, %3\n\ts_nop 0\n\tglobal_load_lds_dwordx4 %1, %2 sc1\n\t"
                 ".if %4 > 1\n\tglobal_load_lds_dwordx4 %1, %2 offset:1024 sc1\n\t.endif\n\t.if %4 > 2\n\tglobal_load_lds_dwordx4 %1, %2 offset:2048 sc1\n\t.endif\n\t"
                 ".if %4 > 3\n\tglobal_load_lds_dwordx4 %1, %2 offset:3072 sc1\n\t.endif\n\ts_mov_b32 m0, %0"
                 : "=&s"(keep) : "v"(voff), "s"(src + (size_t)(p0 + i) * 512), "s"(lds0 + L_XN + (seg & 1) * 40960 + (p0 + i) * 1024), "n"(N - i < 4 ? N - i : 4) : "memory");
  }
}
__device__ __forceinline__ int qkv_row(int b) { return (b & 7) / QR * (2 * QR) + (b >> 3) / (16 / QR); }
__device__ __forceinline__ int qkv_col(int b) { return (b & 7) % QR * (16 / QR) + (b >> 3) % (16 / QR); }
__device__ __forceinline__ const u16* x_src(const RecArgs& a, int seg, int b) {
  return seg < NSEG ? (const u16*)(a.ws + OFF_XN) + (size_t)seg * 32 * DM : a.xnew + (size_t)(qkv_row(b) + 16 * (seg - NSEG)) * 32 * DM;
}
template <int N>
__device__ __forceinline__ void img_fetch(const RecArgs& a, unsigned lds0, int h, int j, int part, int lane) {
#pragma unroll
  for (int i = 0; i < N; ++i) {
    const int n = part * N + i, idx = n * 64 + lane, row = idx / 6, pc = idx - row * 6;
    if (FR_PACE && N == 20) asm volatile("s_waitcnt vmcnt(%0)" :: "n"(FR_PACE) : "memory");
    glds16((pc < 3 ? a.wq : a.wk - 24) + (size_t)row * HD + h * DH + j * 24 + pc * 8, lds0 + S_IMG + n * 1024);
  }
}
constexpr int P2B = F4 + F5 + 2 * F67 + F6 + F7, PZ = P2B + F2B, PA = PZ, P0C = PA + 2 * F0A, F3C = 40 - F3A - F3 - P0C - 2 * F0C - F2C;
static_assert(F3C >= 0, "fetch plan");
template <int OFF> __device__ __forceinline__ void lds_inc(unsigned lds0) {
  asm volatile("s_mov_b64 exec, 1\n\tds_add_u32 %0, %1 offset:%2\n\ts_mov_b64 exec, -1" :: "v"(lds0), "v"(1u), "n"(OFF) : "memory");
}
__device__ __forceinline__ void lds_arrive(unsigned* p, int lane) {
  asm volatile("s_waitcnt lgkmcnt(0)" ::: "memory");
  if (lane == 0) __hip_atomic_fetch_add(p, 1u, __ATOMIC_RELAXED, __HIP_MEMORY_SCOPE_WORKGROUP);
}
__device__ __forceinline__ void lds_wait(unsigned* p, unsigned want) {
  while (__hip_atomic_load(p, __ATOMIC_RELAXED, __HIP_MEMORY_SCOPE_WORKGROUP) < want) __builtin_amdgcn_s_sleep(1);
  asm volatile("" ::: "memory");
}
constexpr float KEXP = 0.07216878364870322f * 1.4426950408889634f;
#define FRESH_LANE() int lane = lane0; asm volatile("" : "+v"(lane)); const int c = lane & 15, g = lane >> 4; (void)c; (void)g

struct OutTask {
  u32x4 y[4]; u32x2 xo, out;
  bf16x8 sel;
  unsigned t5f, t5o;
  __device__ __forceinline__ void init(int b, int lane) {
    const int um = b / 10, uq = (b / 5) & 1, up = b % 5, tok = lane & 15, k = up < 4 ? (up >> 1) * 256 + um * 16 + (up & 1) * 8 : 512 + um * 8, q4 = ((lane >> 4) & 1) * 4;
    t5f = (unsigned)(((uq * 20 + (k >> 5)) * 64 + ((k & 31) >> 3) * 16 + tok) * 8 + q4);
    t5o = (unsigned)(((um * 2 + uq) * 16 + (lane >> 4)) * 1280 + up * 256 + tok * 16);
    if (b >= 160) { t5f = 0u; t5o = 0x7ffffff0u; }
    sel = sel_frag(tok, 0);
    PIN_V(sel);
  }
  static __device__ __forceinline__ bool ok(int sl, int b) { return sl >= 0 && b < 160; }
  __device__ __forceinline__ void load(const RecArgs& a, const __amdgpu_buffer_rsrc_t rs, int sl, int b, int part = 3) {
    const unsigned o = t5o | (sl < 0 ? 0x7ff00000u : 0u);
    sl = imax(sl, 0);
#pragma unroll
    for (int k = 0; k < 4; ++k) if (part & 1) y[k] = SC1_LOADS(o, op_slot(sl) + k * 5120);
    if (part & 2) xo = __builtin_bit_cast(u32x2, __hip_atomic_load((const gu64*)(a.xold + (size_t)sl * 32 * DM + t5f), RLX_AGENT));
  }
  __device__ __forceinline__ bool ready(int b) { return b >= 160 || wave_all(umin(umin(min4(y[0]), min4(y[1])), umin(min4(y[2]), min4(y[3]))) != 0u); }
  __device__ __forceinline__ void run(int lane) {
    f32x4 d = {0.f, 0.f, 0.f, 0.f};
#pragma unroll
    for (int k = 0; k < 4; ++k) d = mfma32(sel, as8(y[k]), d);
#pragma unroll
    for (int i = 0; i < 2; ++i) {
      const unsigned r = pack2(d[2 * i], d[2 * i + 1]), x = xo[i];
      out[i] = pack2(u2f(x << 16) + u2f(r << 16), u2f(x & 0xffff0000u) + u2f(r & 0xffff0000u));
    }
    PIN_V(out);
  }
  __device__ __forceinline__ void store(const RecArgs& a, int sl, int b, int lane) {
    __builtin_amdgcn_raw_buffer_store_b64(out, __builtin_amdgcn_make_buffer_rsrc(a.xnew, 0, ok(sl, b) ? S_ * DM * 2 : 0, 0x00020000), (t5f + (unsigned)sl * 32 * DM) * 2u | (unsigned)(lane & 32) << 25, 0, 16);
  }
};

struct PvTask2 {
  u32x4 y[2][2], pa[2], pb[2];
  unsigned lo, lm, vr, ma, mz, so, sz;
  __device__ __forceinline__ void init(int gp, int lane) {
    const int hi = lane >> 5;
    lo = pt_tok(2 * gp) + (unsigned)(lane * 16); lm = pt_mem(2 * gp + hi) + (unsigned)((lane & 31) * 8);
    vr = (unsigned)(L_VOX + 2048 + (lane ^ 8) * 16);
    ma = (unsigned)(L_VOM + hi * 768 + (lane & 31) * 8); mz = (unsigned)(L_VOM + 512 + ((lane & 31) ^ (hi * 8)) * 8);
    so = (unsigned)(L_STG + 2048 + (hi * 16 + (lane & 15)) * 16 + ((lane >> 4) & 1) * 8);
    sz = hi ? (unsigned)(L_RED1 + 8704 + lane * 8) : so + 1024;
    PIN_V(lo); PIN_V(lm); PIN_V(vr); PIN_V(ma); PIN_V(mz); PIN_V(so); PIN_V(sz);
#pragma unroll
    for (int q = 0; q < 2; ++q) { pa[q] = pb[q] = y[q][0] = y[q][1] = (u32x4){0u, 0u, 0u, 0u}; PIN_V(pa[q]); PIN_V(pb[q]); }
  }
  __device__ __forceinline__ void load(const __amdgpu_buffer_rsrc_t rs, unsigned o) {
#pragma unroll
    for (int q = 0; q < 2; ++q) {
      y[q][0] = SC1_LOADS(lo + q * 1024, o); y[q][1] = SC1_LOADS(lo + 2048 + q * 1024, o);
      const u32x2 p = __builtin_amdgcn_raw_buffer_load_b64(rs, lm + q * 256, o, 16);
      pa[q][0] = p[0]; pa[q][1] = p[1];
    }
  }
  __device__ __forceinline__ bool ready() {
    return wave_all(umin(umin(umin(min4(y[0][0]), min4(y[0][1])), umin(min4(y[1][0]), min4(y[1][1]))), umin(umin(pa[0][0], pa[0][1]), umin(pa[1][0], pa[1][1]))) != 0u);
  }
  template <bool DIRECT> __device__ __forceinline__ void run(char* lds, int sv, int lane, const __amdgpu_buffer_rsrc_t rs, unsigned og) {
    const f32x4 Z4 = {0.f, 0.f, 0.f, 0.f};
    const char* vp = lds + L_VOX + sv * 5120 + lane * 16;
    bf16x8 vx[6];
#pragma unroll
    for (int t = 0; t < 5; ++t) vx[t] = *(const bf16x8*)(vp + t * 1024);
    vx[5] = *(const bf16x8*)(lds + vr + sv * 5120);
    const u32x2 a0 = *(const u32x2*)(lds + ma + sv * 1280), a1 = *(const u32x2*)(lds + ma + sv * 1280 + 256);
    const u32x2 z0 = *(const u32x2*)(lds + mz + sv * 1280), z1 = *(const u32x2*)(lds + mz + sv * 1280 + 256);
    const bf16x8 vm = mk8(a0[0], a0[1], a1[0], a1[1]), vz = mk8(z0[0], z0[1], z1[0], z1[1]);
#pragma unroll
    for (int q = 0; q < 2; ++q) { PIN_V(pa[q]); pb[q][2] = pa[q][0]; pb[q][3] = pa[q][1]; PIN_V(pb[q]); }
    __builtin_amdgcn_sched_barrier(0);
    f32x4 o[2][3];
#pragma unroll
    for (int q = 0; q < 2; ++q) {
#pragma unroll
      for (int t = 0; t < 3; ++t) o[q][t] = mfma32(vx[t], as8(y[q][0]), Z4);
#pragma unroll
      for (int t = 0; t < 3; ++t) o[q][t] = mfma32(vx[3 + t], as8(y[q][1]), o[q][t]);
      o[q][0] = mfma32(vm, as8(pa[q]), o[q][0]); o[q][1] = mfma32(vm, as8(pb[q]), o[q][1]); o[q][2] = mfma32(vz, as8(pa[q]), o[q][2]);
    }
#pragma unroll
    for (int q = 0; q < 2; ++q) {
#pragma unroll
      for (int t = 0; t < 3; ++t) {
        const u32x2 v = {pack2n(o[q][t][0], o[q][t][1]), pack2n(o[q][t][2], o[q][t][3])};
        if (DIRECT) SC1_STORE8S(v, (so - (L_STG + 2048) + t * 512) | (t == 2 ? (unsigned)(lane & 32) << 25 : 0u), og + q * 20480);
        else *(u32x2*)(lds + (t < 2 ? so + t * 512 : sz) + q * 1280) = v;
      }
    }
  }
};

struct TokScores {
  bf16x8 k[2], q[2]; u32x4 v[2];
  __device__ __forceinline__ void get(const char* lds, int seg, int lane) {
#pragma unroll
    for (int i = 0; i < 2; ++i) { k[i] = *(const bf16x8*)(lds + L_KX + (seg & 1) * 2048 + i * 1024 + lane * 16); q[i] = *(const bf16x8*)(lds + L_QX + (seg & 1) * 2048 + i * 1024 + lane * 16); }
  }
  __device__ __forceinline__ void run(int qt) {
    const f32x4 Z4 = {0.f, 0.f, 0.f, 0.f};
    const f32x4 d1 = mfma32(k[0], q[qt], Z4), d2 = mfma32(k[1], q[qt], Z4);
    v[qt] = (u32x4){pack2n(d1[0], d1[1]), pack2n(d1[2], d1[3]), pack2n(d2[0], d2[1]), pack2n(d2[2], d2[3])};
    PIN_V(v[qt]);
  }
  __device__ __forceinline__ void store(const __amdgpu_buffer_rsrc_t rs, unsigned off, unsigned o) {
    __builtin_amdgcn_raw_buffer_store_b128(v[0], rs, off, o, 16); __builtin_amdgcn_raw_buffer_store_b128(v[1], rs, off + 1024, o, 16);
    asm volatile("s_nop 1" ::: "memory");
  }
};

__device__ __forceinline__ void touch(const char* base, unsigned off, unsigned lds0) {
  unsigned keep;
  asm volatile("s_mov_b32 %0, m0\n\ts_mov_b32 m0, %3\n\ts_nop 0\n\tglobal_load_lds_dword %1, %2 sc1\n\ts_mov_b32 m0, %0" : "=&s"(keep) : "v"(off), "s"(base), "s"(lds0 + L_RED1 + 8192) : "memory");
}
template <int N> __device__ __forceinline__ void stouch(const char* p, unsigned& d) {
  asm volatile(".set pf_o, 0\n\t.rept %2\n\ts_load_dword %0, %1, pf_o\n\t.set pf_o, pf_o + 128\n\t.endr" : "+s"(d) : "s"(p), "n"(N) : "memory");
}
__device__ __forceinline__ void code_touch(int b, int lane, unsigned lds0) {
  const char* pc; int d0, d1;
  asm volatile("s_getpc_b64 %0\n\ts_mov_b32 %1, _Z10rec_kernel7RecArgs@rel32@lo+4\n\ts_mov_b32 %2, _Z11attb_kernelPKtS0_Pt@rel32@lo+12" : "=s"(pc), "=s"(d0), "=s"(d1));
  const int o = (lane * 32 + (b >> 3)) * 128;
  touch(pc + d0, (unsigned)(o < d1 - d0 ? o : 0), lds0);
}
__device__ __forceinline__ unsigned zero_base(int b, int h, int j, int ln) {
  return ln < 32 ? (unsigned)((h & 3) * 32768 + j * 512 + ln * 16) : ln < 40 ? (unsigned)((h & 2) * 1024 + j * 256 + (ln & 4) * 32 + (h & 1) * 64 + (ln & 3) * 16)
                 : (unsigned)OFF_ACC + (b < ACC_NB ? (unsigned)(b * 256 + (ln - 40) * 16) : ts_piece((b - ACC_NB) * 8 + ln - 40));
}
__device__ __forceinline__ unsigned sct_slot(unsigned ck, unsigned sl) { return sl * SCT_STEP + ((ck >> (sl & 4u)) & 7u) * 4096u; }
__device__ __forceinline__ unsigned scm_slot(unsigned ck, unsigned sl) { return OFF_SCM + sl * SCM_STEP + ((ck >> 8) & 7u) * 4096u; }
__device__ __forceinline__ int ck_rot(int x) { return ((x & 1) << 2) | (x >> 1); }
struct Place {
  unsigned t[8];
  __device__ __forceinline__ void load(const char* ws, int lane) {
#pragma unroll
    for (int x = 0; x < 8; ++x) t[x] = *(const unsigned*)(ws + OFF_LAT + ((imin(lane >> 3, 5) * 8 + (ck_rot(x) ^ (lane & 7))) * 8 + x) * 4);
  }
  __device__ __forceinline__ unsigned get(int b, int lane) {
    int c = (int)((((t[0] + t[1]) + (t[2] + t[3])) + ((t[4] + t[5]) + (t[6] + t[7]))) * 8u) + (lane & 7);
    c = imin(c, dppi<DPP_XOR1>(c)); c = imin(c, dppi<DPP_XOR2>(c)); c = imin(c, dppi<0x141>(c));
    unsigned k = 0u;
#pragma unroll
    for (int r = 0; r < 6; ++r) k |= (unsigned)(__builtin_amdgcn_readlane(c, 8 * r) & 7) << (4 * r);
    return (k ^ PL_X) ^ (unsigned)ck_rot(b & 7) * 0x111111u;
  }
};

struct Sched {
  u32x4 tsv, rec;
  unsigned side, o;
  __device__ __forceinline__ void pre(int s) { o = (unsigned)OFF_ACC + (unsigned)((s + SR - 1) % SR) * (unsigned)ACC_STEP + ACC_TS + side; PIN_S(o); }
  __device__ __forceinline__ void load(const __amdgpu_buffer_rsrc_t rs, int lane) {
    tsv = SC1_LOADS(ts_piece(lane & 63) - ACC_TS, o); rec = SC1_LOADS(128, o);
  }
  __device__ __forceinline__ unsigned last(unsigned ref) {
    const unsigned o = 0x40000000u - ref;
    int l = (int)umax(umax(tsv[0] ? tsv[0] + o : 0u, tsv[1] ? tsv[1] + o : 0u), umax(tsv[2] ? tsv[2] + o : 0u, tsv[3] ? tsv[3] + o : 0u));
    return (unsigned)wave_imax(l) - o;
  }
  __device__ __forceinline__ unsigned run(const RecArgs& a, const __amdgpu_buffer_rsrc_t rs, int s, int b, int lane) {
    constexpr int w = 3; (void)w;
    unsigned acur = __builtin_amdgcn_readfirstlane(rec[0]), aprev = __builtin_amdgcn_readfirstlane(rec[1]), hist = __builtin_amdgcn_readfirstlane(rec[3]);
    int D8 = (int)__builtin_amdgcn_readfirstlane(rec[2]);
    const int d = wave_imax((int)(umax(umax(tsv[0], tsv[1]), umax(tsv[2], tsv[3])) - aprev)), dd = 8 * d - D8;
    int Dn = D8 + (dd >> (dd > 0 ? SCH_GU : SCH_GD));
    unsigned anext = (acur + (unsigned)((Dn + 4 + imax(Dn - 8 * SCH_DR, 0) * SCH_DK / 8) >> 3) + SCH_V + (s < EARLY_N ? EARLY_V : (s >= WPF0 ? (s == NSEG - 2 ? LAST_V : LATE_V) : 0))) | 1u;
    if (__builtin_expect(hist < 2u || aprev == 0u || d < 1 || d + SCH_VMIN > (int)(acur - aprev), 0)) {
      if (s == 0) {
        for (int it = 0; it < 40 && !wave_all(min4(tsv) != 0u); ++it) {
          __builtin_amdgcn_s_sleep(ST_ALL);
          asm volatile("" ::: "memory");
          load(rs, lane);
        }
      }
      const unsigned now = now32(), L = last(now);
      if (acur == 0u) { D8 = 8 * SCH_D0; hist = 0u; }
      const bool late = acur == 0u || (int)(L + SCH_VMIN - acur) > 0;
      if (late) { if (b == 0) FC(3); if (acur) D8 += 8 * (hist ? SCH_UP : 3 * SCH_UP); }
      else if (aprev) { if (hist < 2u) D8 = 8 * imin(imax((int)(L - aprev), 1), 4000); hist = umin(hist + 1u, 2u); }
      const unsigned base = acur == 0u ? L + SCH_FIRST : (late ? L + SCH_V + ((int)(L + SCH_VMIN2 - acur) <= 0 ? SCH_PEN2 : SCH_PEN) : acur);
      anext = (base + (unsigned)((D8 + 4) >> 3) + SCH_V + (s == 1 ? EARLY_V1 : (s < EARLY_N ? EARLY_V : 0))) | 1u;
      if (s == 0) anext = (L + ST_X) | 1u;
      if ((int)(now - L) > 50000) anext = 0u;
      if (late) acur = 0u;
      Dn = D8;
    }
    if (__builtin_expect(b == 0, 0))
      __builtin_amdgcn_raw_buffer_store_b128((u32x4){anext, acur, (unsigned)Dn, hist}, rs, lane < 2 ? lane * 8192 : 0x7ffffff0, (unsigned)OFF_ACC + (unsigned)(s % SR) * (unsigned)ACC_STEP + ACC_TS + 128u, 16);
    LOGV(0, anext); LOGV(1, acur == 0u); LOGV(2, (Dn + 4) >> 3); LOGV(3, d); LOGV(4, acur - aprev - d); LOGV(5, aprev);
    return anext;
  }
};

struct TokTask {
  u32x4 y[2]; u32x2 ym; u32x4 res;
  unsigned l0, l1, lm, s0, s1;
  static __device__ __forceinline__ bool ok(int sl) { return sl >= 0 && sl < NSEG; }
  __device__ __forceinline__ void init(int h, int j, int lane) {
    const int c = lane & 15, g = lane >> 4;
    l0 = ts_tok(h, g) + (unsigned)(j * 256 + c * 16); l1 = l0 + 16384u;
    lm = ts_mem(h, g + 4 * (c >> 3)) + (unsigned)(j * 64 + (c & 7) * 8);
    res = (u32x4){0u, 0u, 0u, 0u};
    const int qi = 4 * j + (c & 3);
    s0 = g < 2 ? pt_tok(h) + (unsigned)((qi >> 4) * 1024 + ((c >> 2) * 16 + (qi & 15)) * 16 + g * 8) : 0x7ffffff0u;
    s1 = lane < 8 ? pt_mem(h) + (unsigned)((qi >> 4) * 256 + ((c >> 2) * 16 + (qi & 15)) * 8) : 0x7ffffff0u;
    PIN_V(l0); PIN_V(l1); PIN_V(lm); PIN_V(s0); PIN_V(s1);
  }
  __device__ __forceinline__ void load(const __amdgpu_buffer_rsrc_t rs, unsigned o) {
    y[0] = SC1_LOADS(l0, o); y[1] = SC1_LOADS(l1, o);
    ym = __builtin_amdgcn_raw_buffer_load_b64(rs, lm, o, 16);
  }
  __device__ __forceinline__ bool ready() { return wave_all(umin(umin(min4(y[0]), min4(y[1])), umin(ym[0], ym[1])) != 0u); }
  __device__ __forceinline__ void run(int j, int lane, const bf16x8 SelLo) {
    const int c = lane & 15, g = lane >> 4;
    const f32x4 Z4 = {0.f, 0.f, 0.f, 0.f};
    const f32x4 d1 = mfma32(SelLo, as8(y[1]), mfma32(SelLo, as8(y[0]), Z4));
    const f32x4 d0 = mfma32(SelLo, mk8(ym[0], ym[1], 0u, 0u), Z4);
    const int qi = 4 * j + (c & 3), k0 = 16 * g + 4 * (c >> 2);
    float t[4], mv[4];
    float mx = -INFINITY;
#pragma unroll
    for (int r = 0; r < 4; ++r) {
      t[r] = (g < 2 && k0 + r <= qi) ? d1[r] : -INFINITY;
      const float mt = d0[r] + dppf<DPP_ROR8>(d0[r]);
      mv[r] = lane < 8 ? mt : -INFINITY;
      mx = fmx(mx, fmx(t[r], mv[r]));
    }
    mx = fmx(mx, dppf<DPP_ROR8>(mx)); mx = fmx(mx, dppf<DPP_ROR4>(mx));
    mx = x16mx(mx);
    float sum = 0.f;
#pragma unroll
    for (int r = 0; r < 4; ++r) {
      t[r] = __builtin_amdgcn_exp2f(t[r] - mx); mv[r] = __builtin_amdgcn_exp2f(mv[r] - mx);
      sum += t[r] + mv[r];
    }
    sum += dppf<DPP_ROR8>(sum); sum += dppf<DPP_ROR4>(sum);
    sum = x16sum(sum);
    const float rs_ = __builtin_amdgcn_rcpf(sum);
    res = (u32x4){pack2n(t[0] * rs_, t[1] * rs_), pack2n(t[2] * rs_, t[3] * rs_), pack2n(mv[0] * rs_, mv[1] * rs_), pack2n(mv[2] * rs_, mv[3] * rs_)};
    PIN_V(res);
  }
  __device__ __forceinline__ void store(const __amdgpu_buffer_rsrc_t rs, unsigned o, unsigned no = 0u) {
    SC1_STORE8S(((u32x2){res[0], res[1]}), s0 | no, o);
    SC1_STORE8S(((u32x2){res[2], res[3]}), s1 | no, o);
  }
};

constexpr int L_DRN = L_VOX + (NSEG % NV) * 5120;
__device__ __forceinline__ void drain_pre(char* lds, int b, int lane) {
  const int gp = (b & 7) * 2 + (b >> 7), m = (b >> 3) & 15;
  TokTask tk; PvTask2 pv; OutTask ot;
  tk.init(2 * gp + (m >> 3), m & 7, lane); pv.init(gp, lane); ot.init(b, lane);
  u32x4* p = (u32x4*)(lds + L_DRN) + lane;
  p[0] = (u32x4){tk.l0, tk.lm, tk.s0, tk.s1}; p[64] = (u32x4){pv.lo, pv.lm, pv.vr, pv.ma}; p[128] = (u32x4){pv.mz, pv.so, ot.t5f, ot.t5o};
  p[192] = __builtin_bit_cast(u32x4, ot.sel);
}
__device__ __forceinline__ void qkv_pre(unsigned (&vo)[2][2], int b, int w, int lane) {
  const int cb = qkv_col(b), c = lane & 15, g = lane >> 4, t0 = w & 1, col0 = (cb % 5) * 128 + w * 32;
  const bool SW = cb < 10;
#pragma unroll
  for (int i = 0; i < 2; ++i)
#pragma unroll
    for (int tt = 0; tt < 2; ++tt) {
      const int tq = tt ^ t0, tk = tq * 16 + c, cc = col0 + i * 16 + (SW ? 4 * g : c), hd = cc / BD, e = cc - hd * BD;
      const int kv = (int)(OFF_KV - OFF_QB) + hd * 64 * KVT;
      vo[i][tt] = (unsigned)(!SW ? kv + 1280 + tq * 672 + ((g >> 1) * 21 + e) * 16 + (g & 1) * 8
                                 : (cb < 5 ? (hd * S_ + tk) * 2 * QST + e * 2 : kv + (e < 16 ? (tk + 32 * (e >> 3)) * 16 + (e & 7) * 2 : 1024 + tk * 8)));
      PIN_V(vo[i][tt]);
    }
}

template <int w>
__device__ __forceinline__ void token_waves(const RecArgs& a, char* lds, const __amdgpu_buffer_rsrc_t rs, unsigned (&vo)[2][2]) {
  const int lane0 = __builtin_amdgcn_workitem_id_x() & 63, b = __builtin_amdgcn_workgroup_id_x(), wid = w + 4; (void)wid;
  const int gp = (b & 7) * 2 + (b >> 7), m = (b >> 3) & 15, h0 = 2 * gp, h = h0 + (m >> 3), j = m & 7;
  float* const misc = (float*)(lds + L_MISC);
  const f32x4 Z4 = {0.f, 0.f, 0.f, 0.f};
  const unsigned lds0 = (unsigned)(uintptr_t)(LAS char*)lds;
  unsigned* const cnt = (unsigned*)(misc + 6);
  const float* const xnrm = (const float*)(a.ws + OFF_XNRM);
  bf16x16 W[2][10];
  auto btile = [&](int i) { return w == 1 ? 0 : (w == 2 ? 1 + i : 3 + i); };
  if (w == 1) code_touch(b, lane0, lds0);
  Place pl;
  unsigned phi[2][2], ph[2][2][2];
#pragma unroll
  for (int k = 0; k < 8; ++k) {
    const int bt = btile(k >> 2), hw = h0 + (bt == 2 ? k & 1 : bt / 3), T = bt == 2 ? 32 + (m >> 1) : (bt % 3) * 16 + m;
    ph[k >> 2][(k >> 1) & 1][k & 1] = ((const __attribute__((address_space(4))) unsigned*)(a.ws + OFF_PHI))[(hw >> 2) | (hw & 3) << 3 | (k & 2) << 4 | (T / 10) << 6];
  }
  __builtin_amdgcn_sched_barrier(0);
  if (w == 3) pl.load(a.ws, lane0);
  if (a.layer) {
    if (w != 0) img_fetch<20>(a, lds0, h, j, w - 1, lane0);
  } else {
    if (w == 0) img_fetch<12>(a, lds0, h, j, 4, lane0);
#pragma unroll
    for (int i = 0; i < 10; ++i) x_fetch(x_src(a, 0, b), lds0, 0, 10 * w + i, lane0);
  }
  const __amdgpu_buffer_rsrc_t rwb = __builtin_amdgcn_make_buffer_rsrc(a.ws + OFF_WB, 0, (int)WB_BYTES, 0x00020000);
#pragma unroll
  for (int k = 0; k < 4; ++k) phi[k >> 1][k & 1] = (((lane0 & 8) ? ph[k >> 1][k & 1][1] : ph[k >> 1][k & 1][0]) & 8192u) | (unsigned)(lane0 * 16);
#pragma unroll
  for (int i = 0; i < 2; ++i)
#pragma unroll
    for (int p = 0; p < 10; ++p)
      if (w >= 2 || (w == 1 && i == 1)) {
        const unsigned wo = wb_off((b * 5 + btile(i)) * 20 + 2 * p), wv = phi[i][p / 5] ^ (wo & 8192u);
        if (FR_PACE && a.layer) asm volatile("s_waitcnt vmcnt(%0)" :: "n"(FR_PACE) : "memory");
        W[i][p] = cat16(as8(__builtin_amdgcn_raw_buffer_load_b128(rwb, wv, wo & ~8192u, 0)), as8(__builtin_amdgcn_raw_buffer_load_b128(rwb, wv + 1024, wo & ~8192u, 0)));
      }
  f32x4 bn = Z4;
  if (w == 0) {
    bn = *(const f32x4*)(a.ws + OFF_BNRM + lane0 * 16);
    if (!a.layer) asm volatile("s_waitcnt vmcnt(0)" ::: "memory");
  } else asm volatile("s_waitcnt vmcnt(%0)" :: "n"(w == 1 ? 20 : 40) : "memory");
  unsigned ck = 0u;
  if (w == 3) { ck = pl.get(b, lane0); *(unsigned*)(misc + 11) = ck; }
  BAR();
#pragma unroll
  for (int p = 0; p < 10; ++p) {
    if (w < 2) W[0][p] = cat16(wqk_frag(lds, 2 * w, 2 * p, lane0), wqk_frag(lds, 2 * w, 2 * p + 1, lane0));
    if (w == 0) W[1][p] = cat16(wqk_frag(lds, 1, 2 * p, lane0), wqk_frag(lds, 1, 2 * p + 1, lane0));
  }
  float cb2 = 0.f;
  if (w == 0) {
    float n = __builtin_amdgcn_sqrtf(bn[0] + bn[1]) + __builtin_amdgcn_sqrtf(bn[2] + bn[3]);
    n += dppf<DPP_XOR1>(n); n += dppf<DPP_XOR2>(n); n += dppf<DPP_ROR4>(n); n += dppf<DPP_ROR8>(n);
    n = x32mx(x16mx(n));
    cb2 = u2f(__builtin_amdgcn_readfirstlane(f2u(1.15f * n * n)));
  }
  const float cm2 = u2f((f2u(cb2) & 0x7fffffu) | 0x40000000u);
  const int ek = imax((int)((f2u(cb2) >> 23) & 0xffu), 1) - 252;
  if (w >= 2) {
#pragma unroll
    for (int i = 0; i < 4; ++i) *(unsigned*)(lds + L_KX + (w - 2) * 4096 + i * 1024 + lane0 * 16 + 12) = 0u;
  }
  OutTask ot;
  const int zl = lane0 - 16;
  const bool zon = w == 3 && zl >= 0 && (zl < 40 || b < ACC_NB + 17);
  unsigned zo = zon ? zero_base(b, h, j, zl) + (zl < 32 ? sct_slot(ck, 0u) : (zl < 40 ? scm_slot(ck, 0u) : 0u)) : 0x7ffffff0u;
  unsigned zm = zon ? (zl < 32 ? SCT_STEP + 1u : (zl < 40 ? SCM_STEP : (unsigned)ACC_STEP)) : 0u;
  const unsigned zx = ((ck ^ (ck >> 4)) & 7u) * 4096u;
  if (w == 3) { PIN_V(zo); PIN_V(zm); }
  auto st_out = [&](int s, int lane, auto go) {
    if (w < 2) return;
    if (ST_E) lds_spin<L_MISC + 112, 1>(lds0, (unsigned)imax(s, 0)); else go();
    const int sl = s - 2 - LAG4;
    const char* sp = lds + L_STG + 2048 + (w & 1) * 1280;
    u32x4 v0 = *(const u32x4*)(sp + lane * 16), v1 = *(const u32x4*)(sp + 1024 + (lane & 15) * 16);
    const u32x4 z = {0u, 0u, 0u, 0u};
    unsigned oo = (sl < 0 ? 0x7ff00000u : op_slot(sl) + (unsigned)((m * 32 + (w & 1) * 16 + gp) * 1280)) + (unsigned)(lane * 16), za = 0x7ffffff0u;
    if (w == 3) {
      const unsigned zs = (unsigned)(s + 2) % SR;
      za = (zo + (s < 0 ? 1024u : zs) * (zm & ~1u)) ^ ((zm & 1u) * (zs & 4u ? zx : 0u));
#pragma unroll
      for (int i = 0; i < 4; ++i) { v1[i] = lane < 16 ? v1[i] : z[i]; PIN_V(v1[i]); }
    }
    za = lane < 16 ? oo + 1024 : za;
    PIN_V(za); PIN_V(oo); PIN_V(v0); PIN_V(v1);
    __builtin_amdgcn_sched_barrier(0);
    if (ST_E) go();
    SC1_STORE(v0, oo);
    SC1_STORE(v1, za);
    __builtin_amdgcn_sched_barrier(0);
    if (w == 2) ot.store(a, s - 2 - LAG5, b, lane);
    else {
      const unsigned xo_ = (unsigned)((s - 8) * 1024 + lane * 16);
      if (__builtin_expect((unsigned)(s - 8) < 20u, 0)) SC1_STORE(z, xo_ < X_BYTES / 256 ? (unsigned)(OFF_X + b * (X_BYTES / 256)) + xo_ : 0x7ffffff0u);
    }
  };
  STAMP_DECL(); ARRIVE_DECL();
  auto mma_seg = [&](int seg, int sv, int lane, auto pause) {
    const int c = lane & 15, g = lane >> 4;
    const char* xp = lds + L_XN + (seg & 1) * 40960 + lane * 16;
    f32x4 acc[2][2] = {{Z4, Z4}, {Z4, Z4}};
    bf16x8 x[4][2];
#pragma unroll
    for (int p = 0; p < 3; ++p) { x[p][0] = *(const bf16x8*)(xp + p * 1024); x[p][1] = *(const bf16x8*)(xp + (20 + p) * 1024); }
    unsigned eo[2];
#pragma unroll
    for (int ks = 0; ks < 20; ++ks) {
      if (ks == (w == 0 ? KP0 : (w == 1 ? KP1 : KP2))) { pause(); __builtin_amdgcn_sched_barrier(0); }
      if (ks == 17) {
#pragma unroll
        for (int i = 0; i < 2; ++i) {
          eo[i] = (unsigned)(w == 0 ? (i ? L_KX : L_QX) + (seg % NKQ) * 2048 + lane * 16
                                    : (w == 1 && i == 0 ? ((g >> 1) ? L_KX : L_QX) + (seg % NKQ) * 2048 + (2 * (g & 1) * 16 + c) * 16 + 8 : L_VOX + sv * 5120 + btile(i) * 1024 + lane * 16));
          if (i == 0 || w == 1) PIN_V(eo[i]);
        }
        if (w == 0) eo[1] = eo[0] + (L_KX - L_QX);
        if (w >= 2) eo[1] = eo[0] + 1024;
      }
      __builtin_amdgcn_sched_barrier(0);
#pragma unroll
      for (int q = 0; q < 4; ++q) {
        const int tt = 1 - (q >> 1), i = q & 1;
        const bool isqk = w == 0 || (w == 1 && i == 0);
        const bf16x8 wf = half8(W[i][ks >> 1], ks & 1);
        acc[i][tt] = isqk ? mfma32(wf, x[ks & 3][tt], acc[i][tt]) : mfma32(x[ks & 3][tt], wf, acc[i][tt]);
        __builtin_amdgcn_sched_barrier(0);
        if (q < 2 && ks + 3 < 20) x[(ks + 3) & 3][q] = *(const bf16x8*)(xp + (q * 20 + ks + 3) * 1024);
        __builtin_amdgcn_sched_barrier(0);
      }
    }
#pragma unroll
    for (int i = 0; i < 2; ++i) {
      if (w + i == 1) { const float ks_ = (w == 0 || (g >> 1)) ? KEXP : 1.0f; acc[i][0] *= ks_; acc[i][1] *= ks_; }
      const u32x4 v = {pack2(acc[i][0][0], acc[i][0][1]), pack2(acc[i][0][2], acc[i][0][3]), pack2(acc[i][1][0], acc[i][1][1]), pack2(acc[i][1][2], acc[i][1][3])};
      char* p = lds + eo[i];
      if (w == 0) { *(u32x2*)(p) = (u32x2){v[0], v[1]}; *(u32x2*)(p + 1024) = (u32x2){v[2], v[3]}; }
      else if (w == 1 && i == 0) { *(unsigned*)(p) = v[0]; *(unsigned*)(p + 256) = v[1]; *(unsigned*)(p + 1024) = v[2]; *(unsigned*)(p + 1280) = v[3]; }
      else *(u32x4*)p = v;
    }
  };
  unsigned vp, mp, ip;
  int sidx, ei = 127;
  float inv;
  f32x4 acc[2];
  auto vo_pre = [&](int s, int sv, int lane) {
    vp = (unsigned)(w == 0 ? L_MISC + (s & 3) * 4 : L_VOM + sv * 1280 + (btile(0) * 32 + (w == 1 ? lane & 31 : lane)) * 8);
    mp = (unsigned)(L_BM + lane * 16);
    PIN_V(vp); PIN_V(mp);
    if (w == 0) { ip = (unsigned)(L_MISC + 16 + ((s + 1) & 1) * 4); PIN_V(ip); }
    else {
      inv = misc[4 + (s & 1)];
      sidx = (lane & 8) ? 0xEEEE : 0x4444;
      acc[0] = acc[1] = Z4;
      PIN_V(sidx); PIN_V(acc[1]);
      if (w >= 2) { PIN_V(acc[0]); PIN_V(inv); }
    }
    __builtin_amdgcn_sched_barrier(0);
  };
  auto vo_mem = [&](int s, auto last) {
    __builtin_amdgcn_s_setprio(2);
    asm volatile(".rept %0\n\ts_nop 3\n\t.endr" :: "n"(w == 0 ? VO_A0 : (w == 1 ? VO_A1 : VO_A2)) : "memory");
    float xmax = 0.f;
    if (w == 0) xmax = *(const float*)(lds + vp);
    constexpr int NX = w == 2 ? VO_N2 : (w == 3 ? VO_N3 : 10);
    bf16x8 x[10];
#pragma unroll
    for (int p = 0; p < NX; ++p) x[p] = *(const bf16x8*)(lds + mp + p * 1024);
    __builtin_amdgcn_sched_barrier(0);
    if (w < 2) asm volatile(".rept %0\n\ts_nop 3\n\t.endr" :: "n"(w == 0 ? VO_B0 : VO_B1) : "memory");
    if (VO_W >= 0) asm volatile("s_waitcnt lgkmcnt(%0)" :: "n"(VO_W < 0 ? 0 : VO_W) : "memory");
    STAMP(5);
    __builtin_amdgcn_sched_barrier(0);
    int ex = 0;
#pragma unroll
    for (int p = 0; p < 10; ++p) {
      if (w == 0) acc[0] = mfma32(x[p], x[p], p ? acc[0] : Z4);
      if (w == 0 && p == 9) { ex = imax((int)((__builtin_amdgcn_readfirstlane(f2u(__builtin_amdgcn_sqrtf(cb2 * xmax))) >> 23) & 0xffu) + 1, 32); PIN_S(ex); }
#pragma unroll
      for (int i = (w == 1); i < 2 && w != 0; ++i) acc[i] = smfma64(x[p], W[i][p], acc[i], sidx);
      __builtin_amdgcn_sched_barrier(0);
      if (p + NX < 10) x[p + NX] = *(const bf16x8*)(lds + mp + (p + NX) * 1024);
      __builtin_amdgcn_sched_barrier(0);
    }
    STAMP(7);
    if (w != 0) {
      float v[4];
#pragma unroll
      for (int r = 0; r < 4; ++r) {
        const auto sw = __builtin_amdgcn_permlane32_swap(f2u(acc[w == 1][r]), f2u(acc[1][r]), false, false);
        v[r] = (u2f(sw[0]) + u2f(sw[1])) * inv;
      }
      *(u32x2*)(lds + vp) = (u32x2){pack2(v[0], v[1]), pack2(v[2], v[3])};
    } else {
      const int t = (int)f2u(fmx(fmx(acc[0][0], acc[0][1]), fmx(acc[0][2], acc[0][3])) * cm2);
      const int et = wave_imax(t) >> 23, em = ((et + ek) >> 1) + ei;
      int e = imax(et == 255 ? 256 : em, ex);
      PIN_S(e);
      e = imin(e, 256);
      ei = e - 31;
      *(unsigned*)(lds + ip) = (unsigned)ei << 23;
      const u32x3 sr = {(unsigned)(285 - e) << 23, (unsigned)(e - 87) < 79u ? REC_RNG << 23 : 0u, (unsigned)(s + 1)};
      asm volatile("ds_write_b96 %0, %1 offset:%2" :: "v"(lds0), "v"(sr), "n"(L_MISC + 32) : "memory");
    }
    if (last == 0 && w >= 2) asm volatile("s_waitcnt vmcnt(%0)" :: "n"(w == SCH_W ? 2 : 0) : "memory");
    if (w != 0) lds_inc<L_MISC + 28>(lds0);
    STAMP(8);
    if (last) __builtin_amdgcn_s_setprio(0);
    if (last && w == 2) { VM_DRAIN(); lds_inc<L_MISC + 28>(lds0); }
  };
  unsigned* const cntq = (unsigned*)(misc + 12);
  unsigned* const cntt = (unsigned*)(misc + 26 + (w & 1));
  unsigned* const cntp = (unsigned*)(misc + 23);
  unsigned* const cntx2 = (unsigned*)(misc + 28);
  float xn4 = 0.f;
  auto wpf = [&](int s, int lane) {
    constexpr int PER = 81920 / QR / WPF_N;
    static_assert(PER % WPF_S == 0 && PER / WPF_S <= 64, "prefetch");
    touch(a.ws + OFF_WQKVBT + (size_t)imin(qkv_col(b), 14) * 163840 + (qkv_row(b) % (2 * QR)) * (81920 / QR), (s - WPF0) * PER + imin(lane, PER / WPF_S - 1) * WPF_S, lds0);
  };
  auto t5_get = [&](int s, int lane) {
    ot.load(a, rs, s - 1 - LAG5, b);
    xn4 = u2f(__hip_atomic_load((const GAS unsigned*)xnrm + (imax(s + 1, 0) * 32 + (lane & 31)) * 4, RLX_AGENT));
    __builtin_amdgcn_sched_barrier(0);
  };
  Sched sch;
  auto late = [&](int s, int lane) {
    if (w == SCH_W && SCH_LATE && __builtin_expect(s >= 0, 1)) *(u32x2*)(misc + 24) = (u32x2){sch.run(a, rs, s, b, lane), (unsigned)(s + 1)};
    if ((w == 2 && F6) || (w == 0 && F4) || (w == 1 && F5) || (w == 3 && F7)) { lds_spin<L_MISC + 48, 1>(lds0, 2u * (unsigned)imax(s + 1, 0)); if (FB_SL && w != 2) __builtin_amdgcn_s_sleep(FB_SL); }
    if (w >= 2) { x_fetch_n<w == 2 ? F6 : F7>(x_src(a, s + 3, b), lds0, s + 3, F4 + F5 + 2 * F67 + (w == 3 ? F6 : 0), lane * 16); return; }
    unsigned sd = 0u;
    if ((w == 0 && XPF == 3) || XPF == 4 || (w == 1 && XPF == 5)) stouch<10>(a.ws + OFF_XN + (size_t)imin(s + 4, NSEG - 1) * (s < 1 ? 0 : 40960) + (b >> 3) * 1280 + w * 64, sd);
    x_fetch_n<w == 0 ? F4 : F5>(x_src(a, s + 3, b), lds0, s + 3, w == 0 ? 0 : F4, lane * 16);
    if (WPF_W == 0 && w == 0 && __builtin_expect(s >= WPF0 && s < WPF0 + WPF_N, 0)) wpf(s, lane);
    if (w == 1 && __builtin_expect(s == CPF_S, 0)) code_touch(b, lane, lds0);
    lds_spin<L_MISC + 104 + 4 * (w & 1), PS_SL>(lds0, (unsigned)imax(s + 1, 0));
    asm volatile("" :: "s"(sd));
  };
  auto xn_put = [&](int s) {
    float xn2 = xn4;
    xn2 = fmx(xn2, dppf<DPP_XOR1>(xn2)); xn2 = fmx(xn2, dppf<DPP_XOR2>(xn2));
    xn2 = fmx(xn2, dppf<DPP_ROR4>(xn2)); xn2 = fmx(xn2, dppf<DPP_ROR8>(xn2));
    misc[s & 3] = x16mx(xn2);
  };
  if (w == 2) { ot.init(b, lane0); ot.out = (u32x2){0u, 0u}; ot.xo = ot.out; ot.y[0] = ot.y[1] = ot.y[2] = ot.y[3] = (u32x4){0u, 0u, 0u, 0u}; }
  VM_DRAIN();
  lds_arrive(cnt, lane0);
  lds_wait(cnt, 8u);
  sch.tsv = sch.rec = (u32x4){0u, 0u, 0u, 0u};
  sch.side = __builtin_amdgcn_readfirstlane(*(const unsigned*)(misc + 11)) & 8192u;
  sch.o = 0u;
  int sv = NV - 1;
  CODE_PAD((PADT >> (4 * w)) & 15);
#pragma unroll 1
  for (int s = -1; s < NSEG - 1; ++s) {
    const int sv1 = sv == NV - 1 ? 0 : sv + 1;
    FRESH_LANE();
    STAMP(0);
    mma_seg(s + 1, sv1, lane, [&]() { STAMP(1); late(s - 1, lane); if (w == SCH_W) sch.pre(s); STAMP(9); });
    STAMP(2);
    __builtin_amdgcn_sched_barrier(0);
    VM_DRAIN();
    if (w == 2) { ot.run(lane); xn_put(s); }
    STAMP(10);
    st_out(s, lane, [&]() { lds_spin<L_MISC + 92, W1_SL>(lds0, 4u * (unsigned)imax(s, 0)); });
    if (w == 2) t5_get(s, lane);
    __builtin_amdgcn_sched_barrier(0);
    if (w >= 2) vo_pre(s, sv, lane);
    STAMP(3);
    BAR();
    STAMP(4);
    if (__builtin_expect(s >= 0, 1)) {
      if (w == SCH_W) { sch.load(rs, lane0); __builtin_amdgcn_sched_barrier(0); }
      if (w < 2) vo_pre(s, sv, lane);
      vo_mem(s, std::integral_constant<int, 0>{});
      STAMP(6);
      if (w == SCH_W && !SCH_LATE) {
        __builtin_amdgcn_s_setprio(SCH_P);
        *(u32x2*)(misc + 24) = (u32x2){sch.run(a, rs, s, b, lane0), (unsigned)(s + 1)};
      }
      STAMP(11);
    } else BAR();
    __builtin_amdgcn_s_setprio(MMA_P);
    __builtin_amdgcn_sched_barrier(0);
    sv = sv1;
  }
  __builtin_amdgcn_s_setprio(0);
  STAMP_OUT();
  late(NSEG - 2, lane0);
  if (w == 2) ot.run(lane0);
  { FRESH_LANE(); if (w == 0) drain_pre(lds, b, lane); qkv_pre(vo, b, w, lane); }
  st_out(NSEG - 1, lane0, [&]() { lds_spin<L_MISC + 92, 1>(lds0, 4u * (NSEG - 1)); });
  if (w != 0) vo_pre(NSEG - 1, (NSEG - 1) % NV, lane0);
  BAR();
  if (w != 0) vo_mem(NSEG - 1, std::integral_constant<int, 1>{});
}

__device__ __forceinline__ void qkv_tail(const RecArgs& a, char* lds, const int w, const unsigned (&vo)[2][2]) {
  const int lane0 = __builtin_amdgcn_workitem_id_x() & 63, b = __builtin_amdgcn_workgroup_id_x();
  const int r = qkv_row(b), cb = qkv_col(b);
  if (cb >= 15) return;
  const unsigned lds0 = (unsigned)(uintptr_t)(LAS char*)lds;
  unsigned* const stage = (unsigned*)(lds + L_MISC) + 17;
  unsigned* const cnty = stage + 1;
  const f32x4 Z4 = {0.f, 0.f, 0.f, 0.f};
  auto fetch = [&](int i) {
    x_fetch_n<10>(x_src(a, NSEG + i, b), lds0, i, 10 * w, lane0 * 16);
  };
  unsigned want = 0;
  auto sync = [&]() { VM_DRAIN(); want += 4u; lds_inc<L_MISC + 72>(lds0); while (__hip_atomic_load(cnty, __ATOMIC_RELAXED, __HIP_MEMORY_SCOPE_WORKGROUP) < want) { } asm volatile("" ::: "memory"); };
  LOGQ(0);
  bf16x8 W[2][20];
  const __amdgpu_buffer_rsrc_t rw = __builtin_amdgcn_make_buffer_rsrc(a.ws + OFF_WQKVBT + (size_t)(cb * 8 + 2 * w) * 20480, 0, 40960, 0x00020000);
  auto w_get = [&](int ks) {
#pragma unroll
    for (int i = 0; i < 2; ++i) W[i][ks] = as8(__builtin_amdgcn_raw_buffer_load_b128(rw, lane0 * 16 + (ks & 3) * 1024, (i * 20 + (ks & ~3)) * 1024, 0));
    __builtin_amdgcn_sched_barrier(0);
  };
#pragma unroll
  for (int ks = 0; ks < QW; ++ks) w_get(ks);
  bf16x8 ones = mk8(0x3f803f80u, 0x3f803f80u, 0x3f803f80u, 0x3f803f80u);
  PIN_V(ones);
  const __amdgpu_buffer_rsrc_t ro = __builtin_amdgcn_make_buffer_rsrc(a.ws + OFF_QB, 0, (int)(OFF_OB - OFF_QB), 0x00020000);
  const int t0 = w & 1;
  auto body = [&](auto SW, auto SQ) {
    const float qs = cb < 5 ? 0.22360679774997896f * 1.4426950408889634f : 1.0f;
    f32x4 cs[2] = {Z4, Z4};
    LOGQ(1);
    f32x4 acc[2][2], st;
    auto mma = [&](int it, auto FIRST) {
      const char* xp0 = lds + L_XN + (it & 1) * 40960 + t0 * 20480 + lane0 * 16;
      const char* xp1 = lds + L_XN + (it & 1) * 40960 + (1 - t0) * 20480 + lane0 * 16;
      acc[0][0] = acc[0][1] = acc[1][0] = acc[1][1] = st = Z4;
      bf16x8 x[4][2];
#pragma unroll
      for (int p = 0; p < 3; ++p) { x[p][0] = *(const bf16x8*)(xp0 + p * 1024); x[p][1] = *(const bf16x8*)(xp1 + p * 1024); }
      __builtin_amdgcn_sched_barrier(0);
#pragma unroll
      for (int ks = 0; ks < 20; ++ks) {
        if (FIRST && ks + QW < 20) w_get(ks + QW);
#pragma unroll
        for (int q = 0; q < 4; ++q) {
          const int tt = 1 - (q >> 1), i = 1 - (q & 1);
          acc[i][tt] = SW ? mfma32(W[i][ks], x[ks & 3][tt], acc[i][tt]) : mfma32(x[ks & 3][tt], W[i][ks], acc[i][tt]);
          __builtin_amdgcn_sched_barrier(0);
          if (q < 2 && ks + 3 < 20) x[(ks + 3) & 3][q] = *(const bf16x8*)((q ? xp1 : xp0) + (ks + 3) * 1024);
          __builtin_amdgcn_sched_barrier(0);
        }
        st = mfma32(SQ ? x[ks & 3][0] : ones, x[ks & 3][0], st);
        if (FIRST) {
#pragma unroll
          for (int i = 0; i < 2; ++i) cs[i] = SW ? mfma32(W[i][ks], ones, cs[i]) : mfma32(ones, W[i][ks], cs[i]);
        }
        __builtin_amdgcn_sched_barrier(0);
      }
    };
    mma(0, std::integral_constant<bool, true>{});
#pragma unroll 1
    for (int it = 0; it < 4; ++it) {
      if (it) mma(it, std::integral_constant<bool, false>{});
      LOGQ(2 + 3 * it);
      FRESH_LANE();
      float* const sp = (float*)(lds + L_ST) + (it & 1) * 64;
      if (SQ) { if ((c >> 2) == g) sp[32 + t0 * 16 + c] = (c & 3) == 0 ? st[0] : ((c & 3) == 1 ? st[1] : ((c & 3) == 2 ? st[2] : st[3])); }
      else if (g == 0) sp[t0 * 16 + c] = st[0];
      sync();
      LOGQ(3 + 3 * it);
      if (it == 0) fetch(2);
      const bool late = r + 48 >= 55;
      if (it == 1 && !late) fetch(3);
      const unsigned so = (unsigned)((r + 16 * it) * (cb < 5 ? 64 * QST : KVT));
      auto stat = [&](float sm, float sq, float& mean, float& rstd) {
        mean = sm * (1.0f / DM);
        rstd = __builtin_amdgcn_rsqf(__builtin_fmaxf(sq * (1.0f / DM) - mean * mean, 0.f) + 1e-5f);
      };
#pragma unroll
      for (int tt = 0; tt < 2; ++tt) {
        const int tq = tt ^ t0;
        float mean[4], rstd[4];
        if (SW) {
          stat(sp[tq * 16 + c], sp[32 + tq * 16 + c], mean[0], rstd[0]);
          rstd[0] *= qs;
        } else {
          const f32x4 sm = *(const f32x4*)(sp + tq * 16 + 4 * g), sq = *(const f32x4*)(sp + 32 + tq * 16 + 4 * g);
#pragma unroll
          for (int k = 0; k < 4; ++k) stat(sm[k], sq[k], mean[k], rstd[k]);
        }
#pragma unroll
        for (int i = 0; i < 2; ++i) {
          float v[4];
#pragma unroll
          for (int k = 0; k < 4; ++k) v[k] = (acc[i][tt][k] - mean[SW ? 0 : k] * cs[i][SW ? k : 0]) * rstd[SW ? 0 : k];
          __builtin_amdgcn_raw_buffer_store_b64((u32x2){pack2(v[0], v[1]), pack2(v[2], v[3])}, ro, vo[i][tt], so, 0);
        }
      }
      LOGQ(4 + 3 * it);
      if (it == 2 && late) lds_spin<L_MISC + 68, 0>(lds0, 5u);
    }
  };
  typedef std::integral_constant<bool, false> F_; typedef std::integral_constant<bool, true> T_;
  if (cb < 10) { if (w >> 1) body(T_{}, T_{}); else body(T_{}, F_{}); }
  else { if (w >> 1) body(F_{}, T_{}); else body(F_{}, F_{}); }
}

__device__ __forceinline__ void t2_mem(const bf16x8 fr, const bf16x8 q0, const bf16x8 q1, const __amdgpu_buffer_rsrc_t rs, unsigned o, int h, int j, float inv, int lane) {
  const f32x4 Z4 = {0.f, 0.f, 0.f, 0.f};
  const f32x4 d0 = mfma32(fr, q0, Z4) * inv, d1 = mfma32(fr, q1, Z4) * inv;
  const unsigned a0 = pack2n(d0[0], d0[1]), a1 = pack2n(d0[2], d0[3]), b0 = pack2n(d1[0], d1[1]), b1 = pack2n(d1[2], d1[3]);
  const auto r0 = __builtin_amdgcn_permlane32_swap(a0, b0, false, false), r1 = __builtin_amdgcn_permlane32_swap(a1, b1, false, false);
  SC1_STORE8S(((u32x2){r0[1], r1[1]}), ts_mem(h, j) + (lane >> 5) * 256 + (lane & 12) * 16 + (lane & 16) * 2 + (lane & 3) * 8, o);
}

template <int w>
__device__ __forceinline__ void rec_waves(const RecArgs& a, char* lds, __amdgpu_buffer_rsrc_t rs, unsigned& tlast) {
  PIN_S(rs);
  const int lane = __builtin_amdgcn_workitem_id_x() & 63, b = __builtin_amdgcn_workgroup_id_x(), c = lane & 15, g = lane >> 4, wid = w; (void)wid;
  const int gp = (b & 7) * 2 + (b >> 7), m = (b >> 3) & 15, h0 = 2 * gp, h = h0 + (m >> 3), j = m & 7;
  float* const misc = (float*)(lds + L_MISC);
  const f32x4 Z4 = {0.f, 0.f, 0.f, 0.f};
  LOGP(0);
  __builtin_amdgcn_s_setprio(3);
  const unsigned lds0 = (unsigned)(uintptr_t)(LAS char*)lds;
  if (!a.layer) img_fetch<12>(a, lds0, h, j, w, lane);
  constexpr int NC = w < 2 ? 2 : 3;
  const int far = ((b >> 2) & 1) ^ 1;
  auto chunk = [&](int f) { return f == 0 ? 4 * far + w : (w == 2 && f == 1 ? 8 + far : (w == 3 && f == 2 ? 9 - far : 4 - 4 * far + w)); };
  bf16x16 Wqk[3][NC];
  u32x4 bm0[NC][2];
#pragma unroll
  for (int f = 0; f < 2 * NC; ++f) bm0[f >> 1][f & 1] = *(const u32x4*)(a.mem0 + (size_t)(c & 7) * DM + 64 * chunk(f >> 1) + 16 * g + 8 * (f & 1));
  if (!a.layer) asm volatile("s_waitcnt vmcnt(0)" ::: "memory");
  BAR();
  LOGP(9);
  const unsigned ck = __builtin_amdgcn_readfirstlane(*(const unsigned*)(misc + 11));
  const unsigned flip = (ck ^ (unsigned)(b << 11)) & 8192u;
#pragma unroll
  for (int t = 0; t < 3; ++t)
#pragma unroll
    for (int f = 0; f < NC; ++f) Wqk[t][f] = cat16(wqk_frag(lds, t, 2 * chunk(f), lane), wqk_frag(lds, t, 2 * chunk(f) + 1, lane));
  if (!a.layer) lds_arrive((unsigned*)(misc + 6), lane);
  else {
    if (w == 0) {
      for (unsigned spins = 0; spins < SPIN_CAP; ++spins) {
        if (__builtin_amdgcn_raw_buffer_load_b32(rs, (b & 7) * 256, (unsigned)OFF_RDY, 16) >= 16u) break;
        __builtin_amdgcn_s_sleep(4);
        asm volatile("" ::: "memory");
      }
      lds_inc<L_MISC + 84>(lds0);
    }
    lds_wait((unsigned*)(misc + 21), 2u);
#pragma unroll
    for (int f = 0; f < 2 * NC; ++f) PIN_V(bm0[f >> 1][f & 1]);
#pragma unroll
    for (int i = 0; i < 10; ++i) x_fetch(x_src(a, 0, b), lds0, 0, 10 * w + i, lane);
  }
  const bf16x8 SelLo = sel_frag(c, 0), SelHi = sel_frag(c, 8);
  bf16x8 ONES = mk8(0x3f803f80u, 0x3f803f80u, 0x3f803f80u, 0x3f803f80u);
  if (w < 2) PIN_V(ONES);
  bool dead = false;
  float inv = 1.0f;
  unsigned* const cnt2 = (unsigned*)(misc + 7);
  unsigned l2off[2 * NC];
#pragma unroll
  for (int f = 0; f < 2 * NC; ++f) { l2off[f] = acc_off(16 * chunk(f >> 1) + 4 * g + 2 * (f & 1) + (c >> 3), c & 7) ^ flip; PIN_V(l2off[f]); }
  int sidx = (c >> 3) ? 0xEEEE : 0x4444;
  PIN_V(sidx);
  const unsigned p1off = (unsigned)((w == 0 ? (h & 2) * 1024 + j * 256 + (h & 1) * 64 + (g & 1) * 8 : (h & 3) * 32768 + j * 512 + (g >> 1) * 256 + (g & 1) * 64) + (c & 4) * 32 + (c & 3) * 16);
  unsigned p1v = (c < 8) && (w != 0 || g >= 2) ? p1off : 0x7ffffff0u, p1cur = 0x7ffffff0u;
  PIN_V(p1v); PIN_V(p1cur);
  const int hd = (c >> 2) & 1, mr = hd ? 3 - (c & 3) : (c & 3);
  const unsigned l1off = (unsigned)(((h0 + hd) & 3) * 32768 + g * 512 + (w & 1) * 128 + (c >> 3) * 64 + mr * 16);
  const unsigned l1offm = (unsigned)((h0 & 2) * 1024 + (g + 4 * (c >> 3)) * 256 + (w & 1) * 128 + hd * 64 + mr * 16);
  const int plane = (lane & ~6) | ((lane & 2) << 1) | ((lane & 4) >> 1);
  unsigned acco = c < 8 ? (acc_off(64 * far + m * 4 + g, 4 * (w & 1) + mr) + (unsigned)(hd * 8)) ^ flip : 0x7ffffff0u;
  unsigned acco2 = c >= 8 ? (acc_off(64 - 64 * far + m * 4 + g, 4 * (w & 1) + mr) + (unsigned)(hd * 8)) ^ flip
                          : (c < 4 ? (acc_off(128 + m * 2 + (g & 1), 4 * (w & 1) + mr) + (unsigned)((g >> 1) * 8)) ^ flip : 0x7ffffff0u);
  if (w < 2) { PIN_V(acco); PIN_V(acco2); }
  unsigned bmo[NC];
#pragma unroll
  for (int f = 0; f < NC; ++f) { bmo[f] = (unsigned)(chunk(f) * 1024 + lane * 16); PIN_V(bmo[f]); }
  bf16x8 bm[NC];
  auto bm_put = [&](int f) { *(bf16x8*)(lds + L_BM + bmo[f]) = bm[f]; __builtin_amdgcn_sched_barrier(0); };
  f32x4 acc[3] = {Z4, Z4, Z4};
  const int rt2 = ((c >> 3) * 8 + 4 * (g & 1)) * 28 + 16 + (c & 7);
  unsigned redo[3] = {(unsigned)(4 * g * 28 + c), (unsigned)(g < 2 ? (4 * g + 2) * 28 + c : rt2), (unsigned)(g < 2 ? (4 * g + 10) * 28 + c : rt2 + 56)};
#pragma unroll
  for (int k = 0; k < 3; ++k) { redo[k] = L_RED1 + w * 1792 + redo[k] * 4; PIN_V(redo[k]); }
  auto red_put = [&]() {
    auto hs = [&](float x, float y) { const auto sw = __builtin_amdgcn_permlane32_swap(f2u(x), f2u(y), false, false); return u2f(sw[0]) + u2f(sw[1]); };
#pragma unroll
    for (int k = 0; k < 3; ++k) {
#pragma unroll
      for (int i = 0; i < 2; ++i) *(float*)(lds + redo[k] + i * 112) = k == 0 ? hs(acc[0][i], acc[1][i]) : hs(acc[k - 1][2 + i], acc[2][2 * k - 2 + i]);
      __builtin_amdgcn_sched_barrier(0);
    }
  };
#pragma unroll
  for (int f = 0; f < NC; ++f) {
    const u32x4 u = bm0[f][0], v = bm0[f][1];
    bm[f] = c < 8 ? mk8(u[0], u[2], v[0], v[2]) : mk8(u[1], u[3], v[1], v[3]);
    bm_put(f);
#pragma unroll
    for (int t = 0; t < 3; ++t) acc[t] = smfma64(bm[f], Wqk[t][f], acc[t], sidx);
  }
  red_put();
  VM_DRAIN();
  if (a.layer) lds_arrive((unsigned*)(misc + 6), lane);
  lds_wait((unsigned*)(misc + 6), 8u);
#pragma unroll
  for (int i = 0; i < 10; ++i) x_fetch(x_src(a, 1, b), lds0, 1, 10 * w + i, lane);
  LOGP(1);
  BAR();
  STAMP_DECL(); ARRIVE_DECL();
  bf16x8 fr, qx[2];
  auto kq_get = [&](int seg) {
    const char* p = lds + (w == 1 ? L_KX : L_QX) + (seg % NKQ) * 2048 + lane * 16;
    if (w == 1 || w == 2) { qx[0] = *(const bf16x8*)p; qx[1] = *(const bf16x8*)(p + 1024); }
  };
  unsigned qo4 = (unsigned)(L_RED1 + (w == 1 ? (c & 7) * 112 + (c >> 3) * 3584 : c * 112) + g * 16), qo2 = qo4 + 64u - (unsigned)(g * 8), qo2b = qo2 + 3584u;
  if (w < 3) { PIN_V(qo4); PIN_V(qo2); if (w != 1) PIN_V(qo2b); }
  auto pub1 = [&](unsigned off, unsigned o1) {
    if (w == 0) fr = qk_frag(lds + qo4, lds + qo2, lds + qo2b);
    else {
      const f32x4 a0 = *(const f32x4*)(lds + qo4), a1 = *(const f32x4*)(lds + qo4 + 1792);
      const f32x2 b0 = *(const f32x2*)(lds + qo2), b1 = *(const f32x2*)(lds + qo2 + 1792);
      float v[6];
#pragma unroll
      for (int i = 0; i < 6; ++i) v[i] = i < 4 ? a0[i] + a1[i] : b0[i - 4] + b1[i - 4];
#pragma unroll
      for (int i = 0; i < 6; ++i) v[i] += dppf<DPP_ROR8>(v[i]);
      fr = mk8(pack2(v[0], v[1]), pack2(v[2], v[3]), pack2(v[4], v[5]), 0u);
    }
    const f32x4 sc = mfma32(w == 0 ? fr : qx[0], fr, Z4);
    if (w == 0) SC1_STORE8S(((u32x2){pack2n(sc[0], sc[1]), pack2n(sc[2], sc[3])}), off, o1);
    else {
      const f32x4 s2 = mfma32(qx[1], fr, Z4);
      __builtin_amdgcn_raw_buffer_store_b128((u32x4){pack2n(sc[0], sc[1]), pack2n(sc[2], sc[3]), pack2n(s2[0], s2[1]), pack2n(s2[2], s2[3])}, rs, off, o1, 16);
    }
  };
  VM_DRAIN();
  kq_get(0);
  unsigned o1 = sct_slot(ck, 0u), o1m = scm_slot(ck, 0u);
  unsigned t2o = ts_tok(h, j) + (unsigned)((c >> 2) * 256 + g * 64 + (c & 3) * 16);
  TokScores t2;
  if (w == 3) { PIN_V(t2o); t2.get(lds, 0, lane); t2.run(0); t2.run(1); t2.store(rs, t2o, ts_slot(ck, 0)); }
  if (w < 2) pub1(p1v, w == 0 ? o1m : o1);
  const unsigned tst = now32() | 1u;
  if (w == 3 && ST_X) __builtin_amdgcn_raw_buffer_store_b32(tst, rs, lane < 2 ? ts_piece(b >> 2) - ACC_TS + (b & 3) * 4 + lane * 8192 : 0x7ffffff0, (unsigned)(OFF_ACC + (SR - 1) * ACC_STEP) + ACC_TS, 16);
  LOGP(2);
  BAR();
  LOGP(3);
#pragma unroll
  for (int i = 0; i < 10; ++i) x_fetch(x_src(a, 2, b), lds0, 2, 10 * w + i, lane);
  VM_DRAIN();
  unsigned o2 = (unsigned)OFF_ACC, o2p = 0u, tw = 0u;
  asm volatile("" : "+s"(o2p));
  unsigned tc0 = 0u, tstar = 0u;
  int dmin = 0, dmax = 0;
  int e1 = 100000;
  unsigned offs = 100u;
  TokTask tk;
  PvTask2 pv2;
  if (w == 3) pv2.init(gp, lane);
  if (w == 2) tk.init(h, j, lane);
  unsigned o4 = 0u;
  unsigned o3, o3p;
  u32x16 P = {0u, 0u, 0u, 0u, 0u, 0u, 0u, 0u, 0u, 0u, 0u, 0u, 0u, 0u, 0u, 0u}, V = P;
  u32x8 V8 = {0u, 0u, 0u, 0u, 0u, 0u, 0u, 0u};
  if (w < 2) asm volatile("" : "+{v[208:223]}"(P), "+{v[184:191]}"(V8), "+{v[192:207]}"(V));
  unsigned tso = lane < 2 ? ts_piece(b >> 2) + (unsigned)((b & 3) * 4 + lane * 8192) : 0x7ffffff0u;
  if (w < 2) PIN_V(tso);
  float minf = g >= 2 ? -INFINITY : 0.f;
  if (w < 2) PIN_V(minf);
  int sv = NV - 1;
  bf16x8 vox[5];
  unsigned vma[3], need2;
  u32x4 slo, shi;
  float k1;
  const u16* xsrc;
  float invv = 1.0f;
  auto prep0 = [&](int s) {
    xsrc = x_src(a, s + 3, b);
    if (w == 2) { o3p = ts_slot(ck, (s + 1 - LAG3) & 3); o3 = ts_slot(ck, imax(s, 0)); o4 = pt_slot(ck, imax(s - LAG3, 0)); PIN_S(o3); }
    if (w == 3) { o3p = pt_slot(ck, (s - LAG4) & 3); o4 = ts_slot(ck, s + 1); }
    PIN_S(xsrc); PIN_S(o3p); PIN_S(o4);
    __builtin_amdgcn_sched_barrier(0);
  };
  auto prep = [&](int s) {
    o2 = (unsigned)OFF_ACC + (unsigned)(s % SR) * (unsigned)ACC_STEP;
    sv = sv == NV - 1 ? 0 : sv + 1;
    PIN_S(o2); PIN_S(sv);
    if (w < 2) {
      PIN_S(tc0);
      o1 = sct_slot(ck, s % SR); o1m = scm_slot(ck, s % SR);
      need2 = 4u * (unsigned)(s + 1) + 2u * ((unsigned)(s + 2) / NSEG);
      PIN_S(o1); PIN_S(o1m); PIN_S(need2);
      __builtin_amdgcn_sched_barrier(0);
      kq_get(s);
    }
    invv = misc[4 + (s & 1)];
  };
  auto prep2 = [&](int s) {
#pragma unroll
    for (int k = 0; k < 3; ++k) {
      const int t = k == 0 ? 2 : (k == 1 ? far : 1 - far);
      const char* p = lds + L_VOX + sv * 5120 + t * 1024 + (plane & 47) * 16 + (g & 1) * 8;
      auto rd = [&](const char* q) { const u32x2 a0 = *(const u32x2*)q, a1 = *(const u32x2*)(q + 256); return mk8(a0[0], a0[1], a1[0], a1[1]); };
      vox[k == 0 ? 0 : 2 * k - 1] = rd(p);
      if (k) vox[2 * k] = rd(p + 3072);
      vma[k] = lds0 + (unsigned)(L_VOM + sv * 1280 + t * 256 + (plane & 31) * 8);
      PIN_V(vma[k]);
    }
    __builtin_amdgcn_sched_barrier(0);
    inv = u2f(__builtin_amdgcn_readfirstlane(f2u(invv)));
    slo = __builtin_bit_cast(u32x4, SelLo); shi = __builtin_bit_cast(u32x4, SelHi);
#pragma unroll
    for (int i = 0; i < 4; ++i) { slo[i] = ((slo[i] >> 7) & 0x10001u) * (f2u(inv) >> 16); shi[i] = ((shi[i] >> 7) & 0x10001u) * (f2u(inv) >> 16); }
    if (F0A) { xsrc = x_src(a, s + 3, b); PIN_S(xsrc); }
    k1 = KEXP * inv;
    PIN_V(k1); PIN_V(slo); PIN_V(shi);
    asm volatile("" : "+v"(vox[0]), "+v"(vox[1]), "+v"(vox[2]), "+v"(vox[3]), "+v"(vox[4]));
    __builtin_amdgcn_sched_barrier(0);
  };
  auto rows = [&](int s) {
    const int dl = spin_until(tw);
    if (w < 2) tc0 = cyc32();
    __builtin_amdgcn_sched_barrier(0);
    STAMP(1);
    unsigned spins = 0;
    auto pass = [&](bool first) {
      u32x4 x[2 * NC];
#pragma unroll
      for (int f = 0; f < NC; ++f) {
        if (f && w < 2 && first) { prep(s); asm volatile(".rept %0\n\ts_nop 3\n\t.endr" :: "n"(P2_G1)); }
        else if (f) asm volatile(".rept %0\n\ts_nop 3\n\t.endr" :: "n"(w < 2 ? P2_G0 : (f == 1 ? P2_G2 : P2_G3)));
        __builtin_amdgcn_sched_barrier(0);
        x[2 * f] = SC1_LOADS(l2off[2 * f], o2p); x[2 * f + 1] = SC1_LOADS(l2off[2 * f + 1], o2p);
        __builtin_amdgcn_sched_barrier(0);
      }
      if (first) lds_inc<L_MISC + 92>(lds0);
      if (first) { if (w >= 2) prep(s); else prep2(s); }
      if (w >= 2 && first && P2_W) lds_spin<L_MISC + 92, 0>(lds0, 4u * (unsigned)s);
      if (w == 2 && first) tk.store(rs, o4, s > LAG3 ? 0u : 0x7ff00000u);
      if (w == 3 && first) t2.store(rs, t2o, o4);
      if (first && w < 2) {
        p1cur = p1v; offs = (offs - (unsigned)dl) & ~3u;
        PIN_V(p1cur); PIN_S(offs);
        tstar = tc0 + (unsigned)(e1 + D1_M + (w == 1 ? D1_W1 : 0)) - offs * 20u;
        dmin = offs < 40u ? D1_MIN : REC_D1;
        dmin = s < EARLY_N ? REC_D1 + EARLY_D1 : dmin;
        dmax = REC_D1;
        PIN_S(tstar); PIN_S(dmin); PIN_S(dmax);
      }
      __builtin_amdgcn_sched_barrier(0);
      unsigned ok = 32u;
#pragma unroll
      for (int t = 0; t < 3; ++t) { acc[t] = Z4; PIN_V(acc[t]); }
      __builtin_amdgcn_sched_barrier(0);
      bool r = true;
#pragma unroll
      for (int f = 0; f < NC; ++f) {
        unsigned p[4];
#pragma unroll
        for (int k = 0; k < 2; ++k) {
          const u32x4 v = x[2 * f + k];
          p[2 * k] = pack2((float)(int)v[0], (float)(int)v[1]); p[2 * k + 1] = pack2((float)(int)v[2], (float)(int)v[3]);
          asm volatile("" : "+v"(p[2 * k]), "+v"(p[2 * k + 1]));
          __builtin_amdgcn_sched_barrier(0);
        }
        ok = (ok & x[2 * f][0] & x[2 * f][2]) & (x[2 * f + 1][0] & x[2 * f + 1][2]);
        PIN_V(ok);
        __builtin_amdgcn_sched_barrier(0);
        bm[f] = mk8(p[0], p[1], p[2], p[3]);
        acc[0] = smfma64(bm[f], Wqk[0][f], acc[0], sidx);
        __builtin_amdgcn_sched_barrier(0);
        bm_put(f);
        acc[1] = smfma64(bm[f], Wqk[1][f], acc[1], sidx);
        __builtin_amdgcn_sched_barrier(0);
        if (f == NC - 1) { u64 bad = __builtin_amdgcn_ballot_w64(ok == 0u); PIN_S(bad); r = bad == 0ull; }
        __builtin_amdgcn_sched_barrier(0);
        acc[2] = smfma64(bm[f], Wqk[2][f], acc[2], sidx);
        __builtin_amdgcn_sched_barrier(0);
      }
      red_put();
      return r;
    };
    if (__builtin_expect(!pass(true), 0)) {
      FC(0);
      do {
        if (dead || ++spins > SPIN_CAP) { dead = true; break; }
        asm volatile("" ::: "memory");
      } while (!pass(false));
    }
    STAMPV(12, spins); LOGF(1, spins);
    TSTAMP(2, now32()); TSTAMP(3, spins);
  };
  if (w >= 2) prep0(-1);
  prep(0);
  if (w < 2) prep2(0);
  CODE_PAD((PADR >> (4 * w)) & 15);
#pragma unroll 1
  for (int s = 0;; ++s) {
    if (__builtin_expect(o2p != 0u, 1)) rows(s);
    if (w < 2 && __builtin_expect(s == NSEG - 1, 0)) break;
    STAMP(2);
    BAR();
    STAMP(3);
    if (w < 2) pub1(p1cur, w == 0 ? o1m : o1);
    if (w == 2) tk.load(rs, o3p);
    if (w == 3) pv2.load(rs, o3p);
    const unsigned tp1 = cyc32();
    __builtin_amdgcn_sched_barrier(0);
    if (w == 2) {
      __builtin_amdgcn_s_setprio(W2_P);
      prep0(s);
      if (W2_D) spin_cyc(tp1 + W2_D);
      fr = qk_frag(lds + qo4, lds + qo2, lds + qo2b);
      t2_mem(fr, qx[0], qx[1], rs, o3, h, j, invv * KEXP, lane);
      __builtin_amdgcn_sched_barrier(0);
      asm volatile("s_waitcnt vmcnt(1)" ::: "memory");
      lds_inc<L_MISC + 28>(lds0);
      kq_get(s + 1);
    }
    if (w == 3) {
      __builtin_amdgcn_s_setprio(T4_P);
      prep0(s);
      t2.get(lds, s + 1, lane); t2.run(0); t2.run(1);
      __builtin_amdgcn_sched_barrier(0);
    }
    if (w >= 2 && __builtin_expect(s == NSEG - 2, 0)) { VM_DRAIN(); lds_inc<L_MISC + 28>(lds0); }
    if (w >= 2) __builtin_amdgcn_s_setprio(UN_P);
    TSTAMP(0, now32());
    STAMP(4);
    __builtin_amdgcn_sched_barrier(0);
    unsigned tpub = 0;
    u32x2 sw;
    if (w == 2) {
      STAMP(5);
      __builtin_amdgcn_sched_barrier(0);
      STAMP(6);
      ARRIVE(0);
      tk.run(j, lane, SelLo);
      if (__builtin_expect(s == NSEG - 1, 0)) { tlast = tp1; break; }
      if (F2B) { lds_spin<L_MISC + 48, 1>(lds0, 2u * (unsigned)(s + 1)); x_fetch_n<F2B>(xsrc, lds0, s + 3, P2B, lane * 16); }
      __builtin_amdgcn_s_setprio(SP_P);
    }
    if (w == 3) {
      pv2.run<false>(lds, sv < 1 + LAG4 ? sv + NV - 1 - LAG4 : sv - 1 - LAG4, lane, rs, 0u);
      if (__builtin_expect(s == NSEG - 1, 0)) { tlast = tp1; break; }
      if (ST_E) lds_inc<L_MISC + 112>(lds0);
      STAMP(11);
      __builtin_amdgcn_s_setprio(SP_P);
      STAMP(5);
      ARRIVE(0);
      if (F3) { lds_spin<L_MISC + 48, 1>(lds0, 2u * (unsigned)(s + 1)); x_fetch_n<F3>(xsrc, lds0, s + 3, 40 - F3C - F3, lane * 16); }
      STAMP(6);
    }
    if (w < 2) {
      __builtin_amdgcn_s_setprio(D1_P);
      x_fetch_n<F0A>(xsrc, lds0, s + 3, PA + w * F0A, lane * 16);
      int dq = imin((int)(tstar - tp1), dmax);
      dq = dq < -D1_LATE ? dmax : dq;
      PIN_S(dq);
      spin_exact(tp1 + (unsigned)imax(dq, dmin));
      STAMP(5);
      u32x32 S;
      auto sc_load = [&]() {
        asm volatile("buffer_load_dwordx4 v[224:227], %2, %4, %5 offen sc1\n\t.rept %7\n\ts_nop 3\n\t.endr\n\t"
                     "buffer_load_dwordx4 v[228:231], %3, %4, %6 offen sc1\n\tbuffer_load_dwordx4 v[232:235], %3, %4, %6 offen offset:2048 sc1\n\t"
                     "buffer_load_dwordx4 v[236:239], %3, %4, %6 offen offset:256 sc1\n\tbuffer_load_dwordx4 v[240:243], %3, %4, %6 offen offset:2304 sc1\n\t"
                     "v_mov_b32 v220, 0x42000000\n\tv_mov_b32 v221, 0\n\tv_mov_b32 v222, 0x42000000\n\tv_mov_b32 v223, 0"
                     : "=&{v[224:255]}"(S), "+{v[208:223]}"(P) : "v"(l1offm), "v"(l1off), "s"(rs), "s"(o1m), "s"(o1), "n"(P1_G) : "memory");
      };
      sc_load();
      __builtin_amdgcn_sched_barrier(0);
      lds_inc<L_MISC + 48>(lds0);
      STAMP(6);
      ARRIVE(0);
      lds_spin<L_MISC + 28, 0>(lds0, need2);
      __builtin_amdgcn_s_setprio(3);
      asm volatile("ds_read_b64 v[192:193], %3\n\tds_read_b64 v[196:197], %3 offset:768\n\tds_read_b64 v[200:201], %4\n\tds_read_b64 v[204:205], %4 offset:768\n\tds_read_b64 v[184:185], %2"
                   : "+{v[184:191]}"(V8), "+{v[192:207]}"(V) : "v"(vma[0]), "v"(vma[1]), "v"(vma[2]) : "memory");
      __builtin_amdgcn_sched_barrier(0);
      {
        const int de = imin((int)(tp1 - tc0) - e1, D1_C);
        e1 += s == 0 ? 0 : de >> (s < EARLY_N && de < -D1_C ? 0 : D1_G);
        PIN_S(e1);
      }
      STAMP(11);
      __builtin_amdgcn_sched_barrier(0);
      {
        unsigned spins = 0;
        u64 miss, bad;
#define MM(d, a, b, c) "v_mfma_f32_16x16x32_bf16 " d ", " a ", " b ", " c "\n\t"
#define FM(d, x, r, ctrl) "v_fmac_f32_dpp " d ", " x ", " r " " ctrl "\n\t"
#define ID_ "quad_perm:[0,1,2,3] row_mask:0xf bank_mask:"
#define HM_ "row_half_mirror row_mask:0xf bank_mask:"
#define R8_ "row_ror:8 row_mask:0xf bank_mask:"
#define MI_ "row_mirror row_mask:0xf bank_mask:"
        auto soft = [&]() {
          asm volatile(
            "s_waitcnt vmcnt(4)\n\t" MM("v[244:247]", "%6", "v[224:227]", "0") "v_min3_u32 v214, v224, v225, v226\n\t"
            "s_waitcnt vmcnt(3)\n\t" MM("v[248:251]", "%6", "v[228:231]", "0") "v_min3_u32 v214, v214, v227, v228\n\tv_min3_u32 v214, v214, v229, v230\n\t"
            "s_waitcnt vmcnt(2)\n\t" MM("v[248:251]", "%6", "v[232:235]", "v[248:251]") "v_min3_u32 v214, v214, v231, v232\n\t"
            "v_add_f32_dpp v212, v244, v244 " R8_ "0x3\n\tv_add_f32_dpp v213, v245, v245 " R8_ "0x3\n\t"
            "s_waitcnt vmcnt(1)\n\t" MM("v[248:251]", "%7", "v[236:239]", "v[248:251]")
            "v_add_f32_dpp v212, v246, v246 " R8_ "0xc\n\tv_add_f32_dpp v213, v247, v247 " R8_ "0xc\n\t"
            "s_waitcnt vmcnt(0) lgkmcnt(0)\n\t" MM("v[248:251]", "%7", "v[240:243]", "v[248:251]")
            "v_fma_f32 v212, v212, %14, %15\n\tv_fma_f32 v213, v213, %14, %15\n\tv_exp_f32 v216, v212\n\tv_exp_f32 v217, v213\n\t"
            "v_min3_u32 v214, v214, v233, v234\n\tv_cvt_pk_bf16_f32 v208, v216, v217\n\tv_min3_u32 v214, v214, v235, v236\n\tds_read_b96 v[188:190], %16 offset:%18\n\t"
            "v_exp_f32 v216, v248\n\tv_mov_b32_dpp v209, v208 " R8_ "0xf\n\tv_exp_f32 v217, v249\n\tv_exp_f32 v218, v250\n\t"
            MM("v[252:255]", "%8", "v[208:211]", "0") "v_exp_f32 v219, v251\n\tv_cvt_pk_bf16_f32 v232, v216, v217\n\t"
            MM("v[224:227]", "v[192:195]", "v[208:211]", "0") "v_cvt_pk_bf16_f32 v233, v218, v219\n\tv_min3_u32 v214, v214, v237, v238\n\tv_mov_b32_dpp v234, v232 " R8_ "0xf\n\t"
            MM("v[228:231]", "v[196:199]", "v[208:211]", "0") "v_mov_b32_dpp v235, v233 " R8_ "0xf\n\t"
            MM("v[244:247]", "v[204:207]", "v[208:211]", "0") "v_min3_u32 v214, v214, v239, v240\n\t"
            MM("v[252:255]", "%8", "v[232:235]", "v[252:255]") MM("v[224:227]", "%10", "v[232:235]", "v[224:227]") "v_min3_u32 v214, v214, v241, v242\n\t"
            MM("v[228:231]", "%11", "v[232:235]", "v[228:231]") "s_waitcnt lgkmcnt(0)\n\tv_sub_u32 v215, v190, %17 clamp\n\t"
            MM("v[236:239]", "v[200:203]", "v[208:211]", "0") "v_min3_u32 v214, v214, v243, v215\n\tv_cmp_eq_u32 %4, 0, v214\n\tv_rcp_f32 v216, v252\n\t"
            MM("v[240:243]", "v[184:187]", "v[208:211]", "0") "v_mul_f32 v216, v216, v188\n\tv_subrev_u32 v215, 0x2f800000, v252\n\t"
            MM("v[236:239]", "%12", "v[232:235]", "v[236:239]") FM("v220", "v224", "v216", ID_ "0x1") FM("v221", "v225", "v216", ID_ "0x1")
            "v_mov_b32_dpp v217, v216 " HM_ "0xf\n\t"
            MM("v[244:247]", "%13", "v[232:235]", "v[244:247]") FM("v220", "v230", "v216", ID_ "0x2") FM("v221", "v231", "v216", ID_ "0x2")
            MM("v[240:243]", "%9", "v[232:235]", "v[240:243]") FM("v220", "v228", "v217", HM_ "0x1") FM("v221", "v229", "v217", HM_ "0x1")
            "v_cmp_ge_u32 vcc, v215, v189\n\t" FM("v220", "v226", "v217", HM_ "0x2") FM("v221", "v227", "v217", HM_ "0x2")
            "s_or_b64 %5, %4, vcc"
            : "+{v[224:255]}"(S), "+{v[208:223]}"(P), "+{v[184:191]}"(V8), "+{v[192:207]}"(V), "=&s"(miss), "=&s"(bad)
            : "v"(slo), "v"(shi), "v"(ONES), "v"(vox[0]), "v"(vox[1]), "v"(vox[2]), "v"(vox[3]), "v"(vox[4]), "v"(k1), "v"(minf), "v"(lds0), "s"(s), "n"(L_MISC + 32)
            : "vcc", "scc", "memory");
        };
        soft();
        if (__builtin_expect(bad != 0ull, 0)) {
          if (miss) FC(1); else FC(2);
          while (miss != 0ull && !dead) {
            if (++spins > SPIN_CAP) dead = true;
            asm volatile("s_nop 7\n\ts_nop 7" ::: "memory");
            sc_load(); soft();
          }
          if (bad != 0ull) {
            u32x4 y[5];
#pragma unroll
            for (int k = 0; k < 4; ++k) y[k] = SC1_LOADS(l1off + (k & 1) * 2048 + (k >> 1) * 256, o1);
            y[4] = SC1_LOADS(l1offm, o1m);
            const f32x4 d0 = mfma32(as8(slo), as8(y[4]), Z4);
            f32x4 d1 = mfma32(as8(shi), as8(y[3]), mfma32(as8(shi), as8(y[2]), mfma32(as8(slo), as8(y[1]), mfma32(as8(slo), as8(y[0]), Z4))));
            const bool lo = c < 8;
            const float m0 = __builtin_fmaf((lo ? d0[0] : d0[2]) + dppf<DPP_ROR8>(lo ? d0[2] : d0[0]), k1, minf);
            const float m1 = __builtin_fmaf((lo ? d0[1] : d0[3]) + dppf<DPP_ROR8>(lo ? d0[3] : d0[1]), k1, minf);
            float mx = fmx(fmx(fmx(d1[0], d1[1]), d1[2]), fmx(fmx(d1[3], m0), m1));
            mx = fmx(mx, dppf<DPP_ROR8>(mx));
            mx = x32mx(x16mx(mx));
            d1 -= mx;
            const unsigned mm = pack2(__builtin_amdgcn_exp2f(m0 - mx), __builtin_amdgcn_exp2f(m1 - mx));
            const unsigned t01 = pack2(__builtin_amdgcn_exp2f(d1[0]), __builtin_amdgcn_exp2f(d1[1])), t23 = pack2(__builtin_amdgcn_exp2f(d1[2]), __builtin_amdgcn_exp2f(d1[3]));
            const bf16x8 pm = mk8(mm, dpp_ror8(mm), 0u, 0u), pt = mk8(t01, t23, dpp_ror8(t01), dpp_ror8(t23));
            const float r = __builtin_amdgcn_rcpf(mfma32(ONES, pt, mfma32(ONES, pm, Z4))[0]) * u2f(V8[4]), rm = dppf<0x141>(r);
            f32x4 o[5];
#pragma unroll
            for (int k = 0; k < 5; ++k) o[k] = mfma32(vox[k], pt, mfma32(k ? mk8(V[4 * k - 4], V[4 * k - 3], 0u, 0u) : mk8(V8[0], V8[1], 0u, 0u), pm, Z4));
#pragma unroll
            for (int i = 0; i < 2; ++i) {
              const float oa = dppf<0x141>(o[2][i]), ob = dppf<0x141>(o[1][2 + i]);
              P[12 + i] = f2u(__builtin_fmaf(hd ? ob : oa, rm, __builtin_fmaf(hd ? o[2][2 + i] : o[1][i], r, i ? 0.f : 32.0f)));
            }
            P[8] = f2u(r); P[9] = f2u(rm);
#pragma unroll
            for (int i = 0; i < 4; ++i) { S[12 + i] = f2u(o[3][i]); S[16 + i] = f2u(o[0][i]); S[20 + i] = f2u(o[4][i]); }
            asm volatile("s_nop 7\n\ts_nop 7" : "+{v[224:255]}"(S), "+{v[208:223]}"(P));
          }
        }
        STAMP(7); STAMPV(13, spins); LOGF(0, spins);
        {
          asm volatile("v_cvt_i32_f32 v218, v220\n\tv_cvt_i32_f32 v219, v221\n\tv_and_or_b32 v218, v218, %7, 2\n\tbuffer_atomic_add_x2 v[218:219], %3, %5, %6 offen\n\t"
                       FM("v222", "v236", "v216", R8_ "0x4") FM("v223", "v237", "v216", R8_ "0x4") "ds_read_b64 %2, %8 offset:%9\n\t"
                       FM("v222", "v238", "v217", MI_ "0x8") FM("v223", "v239", "v217", MI_ "0x8") "v_permlane32_swap_b32 v240, v242\n\t"
                       FM("v222", "v244", "v217", MI_ "0x4") FM("v223", "v245", "v217", MI_ "0x4") "v_permlane32_swap_b32 v241, v243\n\t"
                       FM("v222", "v246", "v216", R8_ "0x8") FM("v223", "v247", "v216", R8_ "0x8")
                       FM("v222", "v240", "v216", ID_ "0x1") FM("v223", "v241", "v216", ID_ "0x1") FM("v222", "v242", "v217", HM_ "0x1") FM("v223", "v243", "v217", HM_ "0x1")
                       "v_cvt_i32_f32 v212, v222\n\tv_cvt_i32_f32 v213, v223\n\tv_and_or_b32 v212, v212, %7, 2\n\tbuffer_atomic_add_x2 v[212:213], %4, %5, %6 offen\n\t"
                       "s_waitcnt lgkmcnt(0)"
                       : "+{v[224:255]}"(S), "+{v[208:223]}"(P), "=&v"(sw) : "v"(acco), "v"(acco2), "s"(rs), "s"(o2), "s"(~63u), "v"(lds0), "n"(L_MISC + 96) : "memory");
          __builtin_amdgcn_sched_barrier(0);
          tpub = now32();
          lds_inc<L_MISC + 104 + 4 * (w & 1)>(lds0);
          asm volatile("buffer_atomic_umax %0, %1, %2, %3 offen" :: "v"(tpub | 1u), "v"(tso), "s"(rs), "s"(o2) : "memory");
          STAMP(10);
        }
      }
    }
    STAMP(8);
    ARRIVE(1);
    o2p = o2;
    PIN_S(o2p);
    if (w < 2) {
      if (__builtin_expect(__builtin_amdgcn_readfirstlane(sw[1]) <= (unsigned)s, 0)) {
        lds_spin<L_MISC + 100, 0>(lds0, (unsigned)s + 1u);
        sw[0] = __hip_atomic_load((unsigned*)(misc + 24), __ATOMIC_RELAXED, __HIP_MEMORY_SCOPE_WORKGROUP);
      }
    } else {
      u32x4 q;
      asm volatile("1:\n\tds_read_b128 %0, %1 offset:%3\n\t.if %4\n\ts_sleep 1\n\t.endif\n\ts_waitcnt lgkmcnt(0)\n\tv_min3_u32 v201, v201, v202, v203\n\tv_cmp_ge_u32 vcc, %2, v201\n\ts_cbranch_vccnz 1b"
                   : "=&{v[200:203]}"(q) : "v"(lds0), "s"(s), "n"(L_MISC + 96), "n"(SP_SL) : "vcc", "memory");
      __builtin_amdgcn_s_setprio(3);
      tpub = now32();
      sw[0] = q[0];
    }
    STAMP(9);
    static_assert(F3C == 0 && F2C == 0 && F0C == 0, "nothing behind the atomics");
    LOGT(0, tpub); LOGT(1, tb2_);
    TSTAMP(1, tpub);
    ARRIVE_OUT();
    const unsigned an = __builtin_amdgcn_readfirstlane(sw[0]);
    const int ahead = (int)(an - tpub);
    tw = tpub + (unsigned)(an == 0u || ahead > 1000 ? SCH_FIRST : imax(ahead, w < 2 ? SCH_DMIN : 0));
    offs = tw - an;
  }
  STAMP_OUT();
  LOGP(4);
  __builtin_amdgcn_s_setprio(3);
  if (w < 2) { BAR(); tlast = cyc32(); }
  if (w == 2) tk.store(rs, o4);
  if (w == 3) {
    const unsigned oo = op_slot(NSEG - 2 - LAG4) + (unsigned)((m * 32 + gp) * 1280);
    const char* sp = lds + L_STG + 2048;
    const u32x4 v0 = *(const u32x4*)(sp + lane * 16), v1 = *(const u32x4*)(sp + 1280 + lane * 16), v2 = *(const u32x4*)(sp + (g & 1) * 1280 + 1024 + c * 16);
    SC1_STORE(v0, oo + lane * 16); SC1_STORE(v1, oo + 20480 + lane * 16); SC1_STORE(v2, lane < 32 ? oo + g * 20480 + 1024 + c * 16 : 0x7ffffff0u);
  }
  if (dead) tlast |= 1u; else tlast &= ~1u;
}

__device__ __forceinline__ void drain(const RecArgs& a, char* lds, const __amdgpu_buffer_rsrc_t rs, const int w, const unsigned tp1) {
  static_assert(LAG3 == 2 && LAG4 == 3 && LAG5 == 6, "drain schedule");
  const int lane = __builtin_amdgcn_workitem_id_x() & 63, b = __builtin_amdgcn_workgroup_id_x(), c = lane & 15;
  const int gp = (b & 7) * 2 + (b >> 7), m = (b >> 3) & 15, j = m & 7;
  unsigned* const misc = (unsigned*)(lds + L_MISC);
  const unsigned lds0 = (unsigned)(uintptr_t)(LAS char*)lds, ck = __builtin_amdgcn_readfirstlane(misc[11]);
  bool dead = tp1 & 1u;
  auto poll = [&](auto load, auto ready) {
    for (unsigned spins = 0;;) {
      asm volatile("" ::: "memory");
      load();
      if (ready() || dead) break;
      if (++spins > SPIN_CAP) dead = true;
      if (END_SL) __builtin_amdgcn_s_sleep(END_SL);
    }
  };
  LOGD(0);
#define TASK(ty, sg, tm) ((u64)((ty) | ((sg) - 56) << 2 | ((tm) / 100) << 5))
  u64 todo = w == 0 ? TASK(1, 63, END_E1) | TASK(2, 63, END_E2) << 12 | TASK(3, 62, END_E3) << 24
           : w == 1 ? TASK(1, 62, 0) | TASK(2, 62, END_E62) << 12 | TASK(3, 60, END_E3) << 24
           : w == 2 ? TASK(2, 60, 0) | TASK(3, 56, 0) << 12 : TASK(2, 61, END_E61) | TASK(3, 58, END_E59) << 12;
  TokTask tk;
  PvTask2 pv;
  OutTask ot;
  {
    const u32x4* p = (const u32x4*)(lds + L_DRN) + lane;
    const u32x4 q0 = p[0], q1 = p[64], q2 = p[128];
    tk.l0 = q0[0]; tk.l1 = q0[0] + 16384u; tk.lm = q0[1]; tk.s0 = q0[2]; tk.s1 = q0[3];
    pv.lo = q1[0]; pv.lm = q1[1]; pv.vr = q1[2]; pv.ma = q1[3]; pv.mz = q2[0]; pv.so = q2[1];
    pv.pa[0] = pv.pa[1] = pv.pb[0] = pv.pb[1] = (u32x4){0u, 0u, 0u, 0u};
    ot.t5f = q2[2]; ot.t5o = q2[3]; ot.sel = as8(p[192]);
  }
#pragma unroll 1
  for (int ti = 0; todo; todo >>= 12, ++ti) {
    const int ty = (int)todo & 3, sl = 56 + (((int)todo >> 2) & 7);
    const unsigned tm = tp1 + (((unsigned)todo >> 5) & 0x7fu) * 100u;
    if (ty == 1) {
      wait_cyc(tm);
      LOGD(3 * ti + 1);
      poll([&]() { tk.load(rs, ts_slot(ck, sl)); }, [&]() { return tk.ready(); });
      LOGD(3 * ti + 2);
      tk.run(j, lane, ot.sel); tk.store(rs, pt_slot(ck, sl));
    } else if (ty == 2) {
      const unsigned t0 = cyc32();
      if (sl >= NSEG - 2) { lds_wait(misc + 7, 4u * NSEG + 3u); wait_cyc(t0 + END_G2); }
      wait_cyc(tm);
      LOGD(3 * ti + 1);
      poll([&]() { pv.load(rs, pt_slot(ck, sl)); }, [&]() { return pv.ready(); });
      LOGD(3 * ti + 2);
      pv.run<true>(lds, sl % NV, lane, rs, op_slot(sl) + (unsigned)((m * 32 + gp) * 1280));
      if (sl == NSEG - 1) misc[14] = cyc32() | 1u;
    } else {
      if (sl >= NSEG - 4) { lds_wait(misc + 14, 1u); wait_cyc(__builtin_amdgcn_readfirstlane(misc[14]) + END_G3); }
      wait_cyc(tm);
      LOGD(3 * ti + 1);
      OutTask o2 = ot;
      poll([&]() { o2.load(a, rs, sl + 1, b); ot.load(a, rs, sl, b); }, [&]() { return ot.ready(b) & o2.ready(b); });
      LOGD(3 * ti + 2);
      o2.run(lane); o2.store(a, sl + 1, b, lane);
      ot.run(lane); ot.store(a, sl, b, lane);
    }
    LOGD(3 * ti + 3);
  }
  VM_DRAIN();
  if (w != 0) lds_inc<L_MISC + 64>(lds0);
  else {
    constexpr unsigned o2 = (unsigned)OFF_ACC + (unsigned)((NSEG - 1) % SR) * (unsigned)ACC_STEP;
    lds_wait(misc + 16, 3u);
    LOGD(10);
    if (lane == 0) __hip_atomic_fetch_add((gu64*)((GAS char*)(a.ws + o2) + acc_off(m * 4, 0)), (u64)1, RLX_AGENT);
    wait_until(now32() + SCH_DRAIN);
    wait_cyc(tp1 + END_E4);
    unsigned x;
    if (qkv_row(b) + 48 >= 55 && qkv_col(b) < 15) poll([&]() { x = __builtin_amdgcn_raw_buffer_load_b32(rs, acc_off(c * 4, 0), o2, 16); }, [&]() { return wave_all((x & 16u) != 0u); });
    lds_inc<L_MISC + 68>(lds0);
    LOGD(11);
  }
  if (qkv_row(b) + 48 >= 55 && qkv_col(b) < 15) {
    lds_wait(misc + 18, 8u);
    lds_spin<L_MISC + 68, 1>(lds0, 1u);
    x_fetch_n<10>(x_src(a, NSEG + 3, b), lds0, 3, 10 * w, lane * 16);
    VM_DRAIN();
    lds_inc<L_MISC + 68>(lds0);
  }
  LOGP(8);
}

__device__ __forceinline__ void proj_front(const RecArgs& a, char* lds, const __amdgpu_buffer_rsrc_t rs, const int v) {
  const int lane = __builtin_amdgcn_workitem_id_x() & 63, b = __builtin_amdgcn_workgroup_id_x(), c = lane & 15, g = lane >> 4;
  const int rb = (b & 7) + 8 * (b >> 5), cq = (b >> 3) & 3;
  const unsigned lds0 = (unsigned)(uintptr_t)(LAS char*)lds;
  unsigned* const cnt = (unsigned*)(lds + L_MISC) + 20;
  LOGF2(0);
  float* const red = (float*)(lds + L_RED1);
  float* const fin = red + 640;
  const f32x4 Z4 = {0.f, 0.f, 0.f, 0.f};
#pragma unroll
  for (int i = 0; i < 8; ++i) x_fetch((const u16*)(a.ws + OFF_OB) + (size_t)rb * 32 * DM, lds0, 0, 8 * v + i, lane);
  bf16x8 W[2][20];
#pragma unroll
  for (int k = 0; k < 40; ++k) W[k & 1][k >> 1] = *(const bf16x8*)(a.ws + OFF_WOBT + ((size_t)(cq * 10 + 2 * v + (k & 1)) * 20 + (k >> 1)) * 1024 + lane * 16);
  const unsigned fo = (unsigned)(rb * 40960 + (cq * 5 + v) * 1024 + ((g >> 1) * 16 + c) * 16), f8 = fo + (g & 1) * 8, f16 = fo + (g & 1) * 20480;
  u32x2 xo[2][2];
#pragma unroll
  for (int k = 0; k < 4; ++k)
    xo[k >> 1][k & 1] = __builtin_bit_cast(u32x2, __hip_atomic_load((const gu64*)((const char*)a.xnew + f8 + (k >> 1) * 512 + (k & 1) * 20480), RLX_AGENT));
  asm volatile("s_waitcnt vmcnt(44)" ::: "memory");
  lds_inc<L_MISC + 80>(lds0);
  lds_wait(cnt, 5u);
  LOGF2(1);
  f32x4 acc[2][2] = {{Z4, Z4}, {Z4, Z4}};
#pragma unroll
  for (int ks = 0; ks < 20; ++ks)
#pragma unroll
    for (int t = 0; t < 2; ++t) {
      const bf16x8 x = *(const bf16x8*)(lds + L_XN + (t * 20 + ks) * 1024 + lane * 16);
#pragma unroll
      for (int i = 0; i < 2; ++i) acc[i][t] = mfma32(W[i][ks], x, acc[i][t]);
    }
  u32x2 xp[2][2];
  PIN_V(acc[1][1]);
  LOGF2(2);
#pragma unroll
  for (int k = 0; k < 4; ++k) {
    const int i = k >> 1, t = k & 1;
#pragma unroll
    for (int q = 0; q < 2; ++q) {
      const unsigned r = pack2(acc[i][t][2 * q], acc[i][t][2 * q + 1]), x = xo[i][t][q];
      xp[i][t][q] = pack2(u2f(x << 16) + u2f(r << 16), u2f(x & 0xffff0000u) + u2f(r & 0xffff0000u));
      acc[i][t][2 * q] = u2f(xp[i][t][q] << 16); acc[i][t][2 * q + 1] = u2f(xp[i][t][q] & 0xffff0000u);
    }
    const float ml = x32sum(x16sum((acc[i][t][0] + acc[i][t][1]) + (acc[i][t][2] + acc[i][t][3]))) * (1.0f / 16.0f);
    float q = 0.f;
#pragma unroll
    for (int r = 0; r < 4; ++r) { const float d = acc[i][t][r] - ml; q += d * d; }
    q = x32sum(x16sum(q));
    if (g == 0) { red[(2 * v + i) * 32 + t * 16 + c] = ml; red[320 + (2 * v + i) * 32 + t * 16 + c] = q; }
  }
  lds_inc<L_MISC + 80>(lds0);
  if (v == 0) {
    lds_wait(cnt, 10u);
    LOGF2(3);
    float mw[10], ml = 0.f, m2 = 0.f;
#pragma unroll
    for (int i = 0; i < 10; ++i) { mw[i] = red[i * 32 + (lane & 31)]; ml += mw[i]; m2 += red[320 + i * 32 + (lane & 31)]; }
    ml *= 0.1f;
#pragma unroll
    for (int i = 0; i < 10; ++i) m2 += 16.0f * (mw[i] - ml) * (mw[i] - ml);
    const unsigned xs = (unsigned)OFF_LNX + rb * 1024;
    if (lane < 32) SC1_STORE8S(((u32x2){f2u(ml), nz(f2u(m2))}), (cq * 32 + lane) * 8, xs);
    wait_cyc(cyc32() + FR_D);
    u32x2 x0, x1;
    for (unsigned spins = 0; spins < SPIN_CAP; ++spins) {
      x0 = __builtin_amdgcn_raw_buffer_load_b64(rs, lane * 8, xs, 16); x1 = __builtin_amdgcn_raw_buffer_load_b64(rs, 512 + lane * 8, xs, 16);
      if (wave_all(x0[1] != 0u && x1[1] != 0u)) break;
      asm volatile("" ::: "memory");
    }
    const float m0 = u2f(x0[0]), m1 = u2f(x1[0]);
    const float mean = x32sum(m0 + m1) * 0.25f;
    const float M2 = x32sum((u2f(x0[1]) + u2f(x1[1])) + 160.0f * ((m0 - mean) * (m0 - mean) + (m1 - mean) * (m1 - mean)));
    const float mb = rbf(mean), den = rbf(__builtin_sqrtf(rbf(rbf(M2 * (1.0f / DM)) + 1e-5f)));
    if (lane < 32) {
      fin[lane] = mb; fin[32 + lane] = 1.0f / den;
      if (cq == 0) __hip_atomic_store((GAS unsigned*)(a.ws + OFF_XNRM) + (rb * 32 + lane) * 4, f2u(1.02f * (M2 + DM * (mean - mb) * (mean - mb)) / (den * den)), RLX_AGENT);
    }
    lds_inc<L_MISC + 84>(lds0);
  }
  lds_wait(cnt + 1, 1u);
  LOGF2(4);
  const __amdgpu_buffer_rsrc_t rx = __builtin_amdgcn_make_buffer_rsrc(a.ws + OFF_XN, 0, (int)(OFF_QB - OFF_XN), 0x00020000);
  auto put = [&](const u32x2 p0, const u32x2 p1, unsigned so) {
    const auto r0 = __builtin_amdgcn_permlane16_swap(p0[0], p1[0], false, false), r1 = __builtin_amdgcn_permlane16_swap(p0[1], p1[1], false, false);
    __builtin_amdgcn_raw_buffer_store_b128((u32x4){r0[0], r1[0], r0[1], r1[1]}, rx, f16, so, 16);
  };
#pragma unroll
  for (int i = 0; i < 2; ++i) {
    u32x2 y[2];
#pragma unroll
    for (int t = 0; t < 2; ++t) {
      const float mb = fin[t * 16 + c], rden = fin[32 + t * 16 + c];
      y[t] = (u32x2){pack2(rbf(acc[i][t][0] - mb) * rden, rbf(acc[i][t][1] - mb) * rden), pack2(rbf(acc[i][t][2] - mb) * rden, rbf(acc[i][t][3] - mb) * rden)};
    }
    put(y[0], y[1], i * 512);
  }
  VM_DRAIN();
  lds_inc<L_MISC + 80>(lds0);
#pragma unroll
  for (int i = 0; i < 2; ++i) put(xp[i][0], xp[i][1], (unsigned)(OFF_XB - OFF_XN) + i * 512);
  if (v == 0) {
    lds_wait(cnt, 15u);
    if (rb < 4 && lane < 8) __hip_atomic_fetch_add((GAS unsigned*)(a.ws + OFF_RDY) + lane * 64, 1u, RLX_AGENT);
  }
  LOGF2(5);
}

__global__ __launch_bounds__(512, 2) void rec_kernel(RecArgs a) {
  __shared__ __attribute__((aligned(16))) char lds[L_BYTES];
  const int tid = __builtin_amdgcn_workitem_id_x(), wid = __builtin_amdgcn_readfirstlane(tid >> 6), w = wid & 3;
  const __amdgpu_buffer_rsrc_t rs = __builtin_amdgcn_make_buffer_rsrc(a.ws, 0, (int)COMM_BYTES, 0x00020000);
  if (tid < 32) ((float*)(lds + L_MISC))[tid] = (tid == 4 || tid == 5) ? 1.0f : 0.0f;
  BAR();
  if (a.layer && wid < 5) proj_front(a, lds, rs, wid);
  if (wid < 4) {
    unsigned tlast;
    if (w == 0) rec_waves<0>(a, lds, rs, tlast);
    else if (w == 1) rec_waves<1>(a, lds, rs, tlast);
    else if (w == 2) rec_waves<2>(a, lds, rs, tlast);
    else rec_waves<3>(a, lds, rs, tlast);
    drain(a, lds, rs, w, tlast);
  } else {
    unsigned vo[2][2];
    if (w == 0) token_waves<0>(a, lds, rs, vo);
    else if (w == 1) token_waves<1>(a, lds, rs, vo);
    else if (w == 2) token_waves<2>(a, lds, rs, vo);
    else token_waves<3>(a, lds, rs, vo);
    qkv_tail(a, lds, w, vo);
  }
}

#if defined(__HIP_DEVICE_COMPILE__)
#pragma clang attribute pop
#endif

constexpr int KQ = S_ / 4;
#define ATT_P1 6
#define ATT_P2 10
#define ATT_P3 14
#define ATT_THR 6.0f
#define ATT_NQ 4
#define ATT_LMAX 1.0e12f
#if defined(__HIP_DEVICE_COMPILE__)
#pragma clang attribute push(__attribute__((target("no-packed-fp32-ops"))), apply_to = function)
#endif
__global__ __launch_bounds__(256 * ATT_NQ, 4) void attb_kernel(const u16* __restrict__ Qp, const u16* __restrict__ KV, u16* __restrict__ O) {
  __shared__ f32x4 mo_[ATT_NQ][4][2][3][64];
  __shared__ float mm_[ATT_NQ][4][2][64];
  const int tid = __builtin_amdgcn_workitem_id_x(), bid = __builtin_amdgcn_workgroup_id_x();
  __shared__ unsigned sink[64];
  own_code<5000>(tid, sink);
  const int lane = tid & 63, wid = __builtin_amdgcn_readfirstlane(tid >> 6), kh = wid / ATT_NQ, r = lane & 31, hh = lane >> 5;
  const int hd = (bid & 7) * 4 + ((bid >> 3) & 3), qb = (bid >> 5) * ATT_NQ + wid % ATT_NQ;
  const int q0 = qb * 64;
  bf16x8 qa[2]; u32x2 qb2[2];
#pragma unroll
  for (int qt = 0; qt < 2; ++qt) {
    { const u32x2* qp = (const u32x2*)(Qp + ((size_t)hd * S_ + q0 + qt * 32 + r) * QST + 8 * hh); const u32x2 q0_ = qp[0], q1_ = qp[1]; qa[qt] = mk8(q0_[0], q0_[1], q1_[0], q1_[1]); }
    const u32x2 t = *(const u32x2*)(Qp + ((size_t)hd * S_ + q0 + qt * 32 + r) * QST + 16);
    qb2[qt] = t;
  }
  const __amdgpu_buffer_rsrc_t rk = __builtin_amdgcn_make_buffer_rsrc((void*)(KV + ((size_t)hd * 64 + kh * 16) * (KVT / 2)), 0, 16 * KVT, 0x00020000);
  unsigned ok1 = lane * 16, ok2 = hh ? 1600 : 1024 + r * 8, ov = r > 20 ? 1u << 30 : 1280 + (hh * 21 + r) * 16;
  f32x16 o[2];
  float m[2];
  const f32x16 Z16 = {0.f, 0.f, 0.f, 0.f, 0.f, 0.f, 0.f, 0.f, 0.f, 0.f, 0.f, 0.f, 0.f, 0.f, 0.f, 0.f};
  int odd = 0xDDDD;
  asm volatile("" : "+v"(odd));
  struct KvT { u32x4 k1, v0, v1; u32x2 k2; };
  auto fetch_k = [&](KvT& x, int t) {
    x.k1 = __builtin_amdgcn_raw_buffer_load_b128(rk, ok1, imin(t, KQ / 32 - 1) * KVT, 0); x.k2 = __builtin_amdgcn_raw_buffer_load_b64(rk, ok2, imin(t, KQ / 32 - 1) * KVT, 0);
    __builtin_amdgcn_sched_barrier(0);
  };
  auto fetch_v = [&](KvT& x, int t) {
    x.v0 = __builtin_amdgcn_raw_buffer_load_b128(rk, ov, imin(t, KQ / 32 - 1) * KVT, 0); x.v1 = __builtin_amdgcn_raw_buffer_load_b128(rk, ov + 672, imin(t, KQ / 32 - 1) * KVT, 0);
    __builtin_amdgcn_sched_barrier(0);
  };
  auto fetch = [&](int t) { KvT x; __builtin_amdgcn_sched_barrier(0); fetch_k(x, t); fetch_v(x, t); return x; };
  auto qk1 = [&](const KvT& x, int qt) { return __builtin_amdgcn_mfma_f32_32x32x16_bf16(as8(x.k1), qa[qt], Z16, 0, 0, 0); };
  auto qk2 = [&](const KvT& x, int qt, f32x16 c) { return __builtin_amdgcn_mfma_f32_32x32x8bf16_1k(__builtin_bit_cast(v4i16_t, x.k2), __builtin_bit_cast(v4i16_t, qb2[qt]), c, 0, 0, 0); };
  auto pv = [&](const u32x4 v, const f32x16 p, int hi, f32x16 c) {
    const f32x8 ph = hi ? __builtin_shufflevector(p, p, 8, 9, 10, 11, 12, 13, 14, 15) : __builtin_shufflevector(p, p, 0, 1, 2, 3, 4, 5, 6, 7);
    return __builtin_amdgcn_smfmac_f32_32x32x32_bf16(__builtin_bit_cast(hbf16x8, v), __builtin_bit_cast(hbf16x16, ph), c, odd, 0, 0);
  };
  auto tile = [&](const KvT& x, auto MODE) {
    f32x16 sa[2];
#pragma unroll
    for (int qt = 0; qt < 2; ++qt) sa[qt] = qk1(x, qt);
#pragma unroll
    for (int qt = 0; qt < 2; ++qt) sa[qt] = qk2(x, qt, sa[qt]);
#pragma unroll
    for (int qt = 0; qt < 2; ++qt) {
      if (MODE != 0) {
        float mx = fmx(fmx(sa[qt][0], sa[qt][1]), sa[qt][2]);
#pragma unroll
        for (int i = 3; i < 15; i += 2) mx = fmx(fmx(mx, sa[qt][i]), sa[qt][i + 1]);
        mx = fmx(mx, sa[qt][15]);
        if (MODE == 1 || !wave_all(mx <= ATT_THR)) {
          mx = x32mx(mx);
          if (MODE != 1) mx = fmx(mx, 0.f);
          const float mn = m[qt] + mx;
          const unsigned h1 = f2bf(mn); const float r1 = mn - bf2f(h1);
          const unsigned h2 = f2bf(r1); const float r2 = r1 - bf2f(h2);
          const unsigned h3 = f2bf(r2);
          const float me = (bf2f(h1) + bf2f(h2)) + bf2f(h3);
          const float dm = me - m[qt];
          m[qt] = me;
          if (hh) qb2[qt] = (u32x2){(h1 | (h2 << 16)) ^ 0x80008000u, h3 ^ 0x8000u};
          const float alpha = __builtin_amdgcn_exp2f(-dm);
#pragma unroll
          for (int i = 0; i < 16; ++i) { if (MODE != 1) o[qt][i] *= alpha; sa[qt][i] -= dm; }
        }
      }
#pragma unroll
      for (int i = 0; i < 16; ++i) sa[qt][i] = __builtin_amdgcn_exp2f(sa[qt][i]);
      asm volatile("" : "+v"(sa[qt]));
      if (MODE == 1) { o[qt] = Z16; asm volatile("" : "+v"(o[qt])); }
      o[qt] = pv(x.v1, sa[qt], 1, pv(x.v0, sa[qt], 0, o[qt]));
      __builtin_amdgcn_sched_barrier(0);
    }
  };
  typedef std::integral_constant<int, 1> I1; typedef std::integral_constant<int, 2> I2;
  auto reset = [&]() {
    asm volatile("" : "+v"(ok1), "+v"(ok2), "+v"(ov) :: "memory");
    m[0] = m[1] = 0.f;
    if (hh) qb2[0] = qb2[1] = (u32x2){0u, 0u};
  };
  auto slow = [&]() {
    reset();
    tile(fetch(0), I1{});
#pragma unroll 1
    for (int t = 1; t < KQ / 32; ++t) tile(fetch(t), I2{});
  };
  f32x16 sc[2];
  auto phase = [&](int qt, const u32x4 vp, const KvT& xk, const u32x4 v0, auto mid) {
    auto ex = [&](int i0, int i1) {
#pragma unroll
      for (int i = i0; i < i1; ++i) sc[qt][i] = __builtin_amdgcn_exp2f(sc[qt][i]);
      asm volatile("" : "+v"(sc[qt]));
      __builtin_amdgcn_sched_barrier(0);
    };
    o[1 - qt] = pv(vp, sc[1 - qt], 1, o[1 - qt]);
    __builtin_amdgcn_sched_barrier(0);
    ex(0, 3);
    sc[1 - qt] = qk1(xk, 1 - qt);
    __builtin_amdgcn_sched_barrier(0);
    ex(3, 6);
    sc[1 - qt] = qk2(xk, 1 - qt, sc[1 - qt]);
    __builtin_amdgcn_sched_barrier(0);
    ex(6, 10);
    o[qt] = pv(v0, sc[qt], 0, o[qt]);
    __builtin_amdgcn_sched_barrier(0);
    mid();
    ex(10, 16);
  };
  auto fast = [&]() {
    reset();
    KvT xa = fetch(0), xb = fetch(1);
    tile(xa, I1{});
    fetch_k(xa, 2);
    sc[0] = qk2(xb, 0, qk1(xb, 0)); sc[1] = Z16;
    asm volatile("" : "+v"(sc[1]));
    auto step = [&](KvT& x, KvT& y, int t) {
      phase(0, y.v1, x, x.v0, [&]() { fetch_v(y, t + 1); fetch_k(x, t + 2); });
      phase(1, x.v1, y, x.v0, []() {});
    };
    step(xb, xa, 1);
#pragma unroll 1
    for (int t = 2; t < KQ / 32; t += 2) {
      if (t == ATT_P1) __builtin_amdgcn_s_setprio(2);
      if (t == ATT_P2) __builtin_amdgcn_s_setprio(1);
      if (t == ATT_P3) __builtin_amdgcn_s_setprio(0);
      step(xa, xb, t);
      step(xb, xa, t + 1);
    }
    o[1] = pv(xb.v1, sc[1], 1, o[1]);
  };
  if (ATT_P1) __builtin_amdgcn_s_setprio(3);
  fast();
  {
    float big = 0.f;
#pragma unroll
    for (int qt = 0; qt < 2; ++qt) {
      big = fmx(big, hh ? o[qt][8] * (3.0e38f / ATT_LMAX) : __builtin_fabsf(o[qt][8]));
#pragma unroll
      for (int i = 0; i < 12; ++i) if (i != 8) big = fmx(big, (i > 8 && hh) ? 0.f : __builtin_fabsf(o[qt][i]));
    }
    if (__builtin_expect(!wave_all(big <= 3.0e38f), 0)) slow();
  }
  f32x4 (*mo)[2][3][64] = mo_[wid % ATT_NQ];
  float (*mm)[2][64] = mm_[wid % ATT_NQ];
#pragma unroll
  for (int qt = 0; qt < 2; ++qt) {
#pragma unroll
    for (int k = 0; k < 3; ++k) mo[kh][qt][k][lane] = (f32x4){o[qt][4 * k], o[qt][4 * k + 1], o[qt][4 * k + 2], o[qt][4 * k + 3]};
    mm[kh][qt][lane] = m[qt];
  }
  const int qt = kh & 1, grp = kh >> 1, row = q0 + qt * 32 + r, k0 = hd * BD + 4 * hh + 8 * grp;
  auto oaddr = [&](int k) { return O + (((size_t)(row >> 4) * 20 + (k >> 5)) * 64 + ((k & 31) >> 3) * 16 + (row & 15)) * 8 + (k & 7); };
  u16 *op0 = oaddr(k0), *op1 = oaddr(k0 + 16);
  asm volatile("" : "+v"(op0), "+v"(op1));
  asm volatile("s_waitcnt lgkmcnt(0)\n\ts_barrier" ::: "memory");
  {
    float a[4], l[4];
    f32x4 u[4], x[4];
#pragma unroll
    for (int i = 0; i < 4; ++i) { a[i] = mm[i][qt][lane]; l[i] = mo[i][qt][2][lane | 32][0]; u[i] = mo[i][qt][grp][lane]; x[i] = mo[i][qt][2][lane]; }
    const float mn = fmx(fmx(a[0], a[1]), fmx(a[2], a[3]));
#pragma unroll
    for (int i = 0; i < 4; ++i) a[i] = __builtin_amdgcn_exp2f(a[i] - mn);
    const float inv = __builtin_amdgcn_rcpf((l[0] * a[0] + l[1] * a[1]) + (l[2] * a[2] + l[3] * a[3]));
    const f32x4 v = ((u[0] * a[0] + u[1] * a[1]) + (u[2] * a[2] + u[3] * a[3])) * inv;
    *(GAS u32x2*)op0 = (u32x2){pack2(v[0], v[1]), pack2(v[2], v[3])};
    if (grp == 0) {
      const f32x4 y = ((x[0] * a[0] + x[1] * a[1]) + (x[2] * a[2] + x[3] * a[3])) * inv;
      if (hh == 0) *(GAS u32x2*)op1 = (u32x2){pack2(y[0], y[1]), pack2(y[2], y[3])};
    }
  }
}
#if defined(__HIP_DEVICE_COMPILE__)
#pragma clang attribute pop
#endif

__global__ __launch_bounds__(640, 3) void proj_kernel(const u16* __restrict__ Of, const u16* __restrict__ Wf, const u16* __restrict__ xold,
                                                      u16* __restrict__ xnew) {
  __shared__ __attribute__((aligned(16))) char ob[40960];
  const int tid = threadIdx.x, lane = tid & 63, w = __builtin_amdgcn_readfirstlane(tid >> 6), g = lane >> 4, c = lane & 15;
  __shared__ unsigned sink[64];
  own_code<1500>(tid, sink);
  const int rb = (blockIdx.x & 7) + 8 * (blockIdx.x >> 5), cb = (blockIdx.x >> 3) & 3;
  const int col = cb * 160 + w * 16 + 4 * g;
  const u16* wp = Wf + (size_t)(cb * 10 + w) * 20 * 512 + lane * 8;
  u32x4 st[4];
#pragma unroll
  for (int i = 0; i < 4; ++i) st[i] = *(const u32x4*)(Of + (size_t)rb * 40 * 512 + (i * 640 + tid) * 8);
  bf16x8 wf[20];
#pragma unroll
  for (int ks = 0; ks < 20; ++ks) wf[ks] = *(const bf16x8*)(wp + (size_t)ks * 512);
  u32x2 xo[2];
#pragma unroll
  for (int t = 0; t < 2; ++t) xo[t] = *(const u32x2*)(xold + (((size_t)(rb * 2 + t) * 20 + (col >> 5)) * 64 + ((col & 31) >> 3) * 16 + c) * 8 + (col & 7));
#pragma unroll
  for (int i = 0; i < 4; ++i) *(u32x4*)(ob + (i * 640 + tid) * 16) = st[i];
  BAR();
  f32x4 acc[2] = {{0.f, 0.f, 0.f, 0.f}, {0.f, 0.f, 0.f, 0.f}};
#pragma unroll
  for (int ks = 0; ks < 20; ++ks)
#pragma unroll
    for (int t = 0; t < 2; ++t) acc[t] = mfma32(wf[ks], *(const bf16x8*)(ob + (t * 20 + ks) * 1024 + lane * 16), acc[t]);
#pragma unroll
  for (int t = 0; t < 2; ++t) {
    const unsigned r01 = pack2(acc[t][0], acc[t][1]), r23 = pack2(acc[t][2], acc[t][3]);
    const unsigned p01 = pack2(u2f(xo[t][0] << 16) + u2f(r01 << 16), u2f(xo[t][0] & 0xffff0000u) + u2f(r01 & 0xffff0000u));
    const unsigned p23 = pack2(u2f(xo[t][1] << 16) + u2f(r23 << 16), u2f(xo[t][1] & 0xffff0000u) + u2f(r23 & 0xffff0000u));
    *(u32x2*)(xnew + (size_t)(rb * 32 + t * 16 + c) * DM + col) = (u32x2){p01, p23};
  }
}

extern "C" void kernel_launch(void* const* d_in, const int* in_sizes, int n_in, void* d_out, int out_size, void* d_ws,
                              size_t ws_size, hipStream_t stream) {
  const u16* A_x = (const u16*)d_in[0];
  const u16* A_mem0 = (const u16*)d_in[1];
  const u16* A_Wq = (const u16*)d_in[2];
  const u16* A_Wk = (const u16*)d_in[3];
  const u16* A_Wv = (const u16*)d_in[4];
  const u16* A_Wo = (const u16*)d_in[5];
  const u16* B_wqkv = (const u16*)d_in[8];
  const u16* B_wo = (const u16*)d_in[9];
  char* ws = (char*)(((uintptr_t)d_ws + ((size_t)WS_OFF << 20) + WS_AL - 1) & ~(uintptr_t)(WS_AL - 1));
  u16* xa = (u16*)(ws + OFF_XA); u16* Ob = (u16*)(ws + OFF_OB);

  prologue_kernel<<<256, 640, 0, stream>>>(ProArgs{A_x, A_Wq, A_Wk, A_Wv, A_Wo, B_wqkv, B_wo, ws});
  for (int layer = 0; layer < 2; ++layer) {
    rec_kernel<<<256, 512, 0, stream>>>(RecArgs{A_mem0, A_Wq, A_Wk, (const u16*)(ws + OFF_XB), xa, ws, (unsigned)layer, 0u});
    attb_kernel<<<BH * 32 / ATT_NQ, 256 * ATT_NQ, 0, stream>>>((const u16*)(ws + OFF_QB), (const u16*)(ws + OFF_KV), Ob);
  }
  proj_kernel<<<256, 640, 0, stream>>>(Ob, (const u16*)(ws + OFF_WOBT), xa, (u16*)d_out);
}
```

```cpp
#include <hip/hip_runtime.h>
#include <hip/hip_bf16.h>
#include <stdint.h>
#include <type_traits>

typedef unsigned short u16;
typedef unsigned long long u64;
typedef __attribute__((ext_vector_type(8))) short bf16x8;
typedef __attribute__((ext_vector_type(16))) short bf16x16;
typedef __attribute__((ext_vector_type(4))) float f32x4;
typedef __attribute__((ext_vector_type(2))) float f32x2;
typedef __attribute__((ext_vector_type(16))) float f32x16;
typedef __attribute__((ext_vector_type(8))) float f32x8;
typedef __attribute__((ext_vector_type(4))) unsigned u32x4;
typedef __attribute__((ext_vector_type(2))) unsigned u32x2;
typedef __attribute__((ext_vector_type(3))) unsigned u32x3;
typedef __attribute__((ext_vector_type(8))) unsigned u32x8;
typedef __attribute__((ext_vector_type(16))) unsigned u32x16;
typedef __attribute__((ext_vector_type(32))) unsigned u32x32;
#define GAS __attribute__((address_space(1)))
#define LAS __attribute__((address_space(3)))
typedef GAS unsigned long long gu64;
#define BAR() do { __builtin_amdgcn_s_waitcnt(0xC07F); asm volatile("s_waitcnt lgkmcnt(0)\n\ts_barrier" ::: "memory"); } while (0)

constexpr int S_ = 2048, DM = 640;
constexpr int NH = 32, DH = 192, HD = NH * DH;
constexpr int MEM = 8, SEG = 32, NSEG = S_ / SEG;
constexpr int BH = 32, BD = 20;

constexpr size_t al256(size_t x) { return (x + 255) & ~(size_t)255; }
constexpr int LAG3 = 2, LAG4 = 3, LAG5 = 6;
#define SR_ 8
constexpr int SR = SR_;
constexpr unsigned SCT_STEP = 131072, OFF_SCM = SR * SCT_STEP, SCM_STEP = 32768;
constexpr int ACC_NB = 192;
constexpr size_t ACC_STEP = 65536;
constexpr unsigned ACC_TS = ACC_NB * 256;
constexpr size_t ACC_LAYER = SR * ACC_STEP;
constexpr size_t OFF_ACC = 3 * 524288;
static_assert(SR == 8, "regions");
__device__ __forceinline__ unsigned ts_piece(int i) { return ACC_TS + (unsigned)(i < 128 ? (i >> 1) * 256 + (i & 1) * 16 : 128 + (i & 1) * 8192); }
constexpr size_t OFF_LNX = OFF_SCM + SR * SCM_STEP;
constexpr size_t OFF_RDY = OFF_LNX + 65536;
constexpr size_t PG = 2097152;
#define RING_ 4
constexpr int RING = RING_;
constexpr size_t TS_SLOT = (size_t)NH * 8 * 2560, PT_SLOT = (size_t)NH * 2560, OP_SLOT = (size_t)256 * 2560;
__device__ __forceinline__ unsigned ts_tok(int h, int j) { return (unsigned)((h & 15) * 32768 + (j >> 2) * 16384 + (j & 3) * 2048); }
__device__ __forceinline__ unsigned ts_mem(int h, int j) { return (unsigned)(524288 + ((h & 15) >> 1) * 16384 + (h & 1) * 4096 + j * 512); }
__device__ __forceinline__ unsigned pt_tok(int h) { return (unsigned)(((h & 15) >> 2) * 16384 + (h & 3) * 2048); }
__device__ __forceinline__ unsigned pt_mem(int h) { return (unsigned)(65536 + (h & 15) * 512); }
constexpr size_t OFF_X = 2 * PG + TS_SLOT + 3 * PT_SLOT, X_BYTES = 7 * OP_SLOT + 4 * PT_SLOT;
constexpr size_t OFF_OP = OFF_X + TS_SLOT + 4 * PT_SLOT;
constexpr size_t COMM_BYTES = OFF_X + X_BYTES + RING * OP_SLOT;
__device__ __forceinline__ unsigned near_half(unsigned ck, unsigned o) { return o + ((ck >> (o >= 2 * PG ? 8 : 4)) & 8192u); }
__device__ __forceinline__ unsigned ts_slot(unsigned ck, int sl) {
  return near_half(ck, sl == NSEG - 1 ? (unsigned)OFF_X : ((sl & 3) == 3 ? (unsigned)(2 * PG) : (unsigned)PG + (unsigned)(sl & 3) * (unsigned)TS_SLOT));
}
__device__ __forceinline__ unsigned pt_slot(unsigned ck, int sl) {
  return near_half(ck, sl >= NSEG - 4 ? (unsigned)(OFF_X + TS_SLOT) + (unsigned)(sl - (NSEG - 4)) * (unsigned)PT_SLOT
                                      : ((sl & 3) == 0 ? (unsigned)(PG + 3 * TS_SLOT) : (unsigned)(2 * PG + TS_SLOT - PT_SLOT) + (unsigned)(sl & 3) * (unsigned)PT_SLOT));
}
__device__ __forceinline__ unsigned op_slot(int sl) { return (unsigned)OFF_OP + (unsigned)(sl >= NSEG - 6 ? sl - (NSEG - 6) : 6 + sl % RING) * (unsigned)OP_SLOT; }
static_assert(RING == 4 && OFF_OP <= 3 * PG && OFF_RDY + 2048 <= OFF_ACC && OFF_ACC + ACC_LAYER == PG && TS_SLOT % 16384 == 0 && PT_SLOT % 16384 == 0, "pages");
static_assert(TS_SLOT == OP_SLOT && RING >= 4 && NSEG % SR == 0 && SR >= 5 && X_BYTES % 4096 == 0 && X_BYTES <= 256 * 20 * 1024, "slots");
constexpr size_t OFF_WB = (COMM_BYTES + 16383) / 16384 * 16384, WB_BYTES = (size_t)256 * 200 * 1024;
__device__ __forceinline__ unsigned wb_off(int F) { const unsigned o = (unsigned)((F >> 3) * 16384 + (F & 7) * 1024); return o | ((((unsigned)OFF_WB + o) >> 8) & 8192u); }
constexpr size_t OFF_PHI = OFF_WB + WB_BYTES;
constexpr size_t OFF_BNRM = OFF_PHI + 1024;
constexpr size_t OFF_LAT = OFF_BNRM + 1024;
constexpr size_t OFF_XNRM = OFF_LAT + 2048;
constexpr size_t OFF_WQKVBT = OFF_XNRM + al256(S_ * 16);
constexpr size_t OFF_WOBT = OFF_WQKVBT + al256((size_t)3 * DM * DM * 2);
constexpr size_t OFF_XN = OFF_WOBT + al256((size_t)DM * DM * 2);
constexpr int QST = BD;
constexpr size_t OFF_XA = OFF_XN + al256((size_t)S_ * DM * 2);
constexpr size_t OFF_XB = OFF_XA + al256((size_t)S_ * DM * 2);
constexpr size_t OFF_QB = OFF_XB + al256((size_t)S_ * DM * 2);
constexpr int KVT = 2624;
constexpr size_t OFF_KV = OFF_QB + al256((size_t)BH * S_ * QST * 2);
constexpr size_t OFF_OB = OFF_KV + al256((size_t)BH * 64 * KVT);
constexpr size_t OFF_DBG = OFF_OB + al256((size_t)S_ * DM * 2);
constexpr size_t WS_TOTAL = OFF_DBG + (size_t)2 * 64 * 8 * DM * 2;
#define WS_AL 2097152
#define WS_OFF 0
static_assert(WS_TOTAL + WS_AL + ((size_t)WS_OFF << 20) <= ((size_t)256 << 20), "workspace budget");

#if defined(__HIP_DEVICE_COMPILE__)
#pragma clang attribute push(__attribute__((target("no-packed-fp32-ops"))), apply_to = function)
#endif
__device__ __forceinline__ float u2f(unsigned v) { return __builtin_bit_cast(float, v); }
__device__ __forceinline__ unsigned f2u(float v) { return __builtin_bit_cast(unsigned, v); }
__device__ __forceinline__ float bf2f(unsigned v) { return __builtin_bit_cast(float, v << 16); }
__device__ __forceinline__ unsigned f2bf(float f) { __bf16 b = (__bf16)f; return (unsigned)__builtin_bit_cast(u16, b); }
__device__ __forceinline__ float rbf(float f) { return bf2f(f2bf(f)); }
typedef __attribute__((ext_vector_type(2))) __bf16 hbf16x2;
__device__ __forceinline__ unsigned pack2(float lo, float hi) { f32x2 v = {lo, hi}; hbf16x2 b = __builtin_convertvector(v, hbf16x2); return __builtin_bit_cast(unsigned, b); }
__device__ __forceinline__ f32x4 mfma32(bf16x8 a, bf16x8 b, f32x4 c) { return __builtin_amdgcn_mfma_f32_16x16x32_bf16(a, b, c, 0, 0, 0); }
typedef __attribute__((ext_vector_type(8))) __bf16 hbf16x8;
typedef __attribute__((ext_vector_type(16))) __bf16 hbf16x16;
__device__ __forceinline__ f32x4 smfma64(bf16x8 a, bf16x16 b, f32x4 c, int idx) {
  return __builtin_amdgcn_smfmac_f32_16x16x64_bf16(__builtin_bit_cast(hbf16x8, a), __builtin_bit_cast(hbf16x16, b), c, idx, 0, 0);
}
__device__ __forceinline__ bf16x16 cat16(bf16x8 a, bf16x8 b) { return __builtin_shufflevector(a, b, 0, 1, 2, 3, 4, 5, 6, 7, 8, 9, 10, 11, 12, 13, 14, 15); }
__device__ __forceinline__ bf16x8 half8(bf16x16 v, int hi) { return hi ? __builtin_shufflevector(v, v, 8, 9, 10, 11, 12, 13, 14, 15) : __builtin_shufflevector(v, v, 0, 1, 2, 3, 4, 5, 6, 7); }
__device__ __forceinline__ bf16x8 mk8(unsigned a, unsigned b, unsigned c, unsigned d) { u32x4 v = {a, b, c, d}; return __builtin_bit_cast(bf16x8, v); }
__device__ __forceinline__ unsigned dpp_ror8(unsigned v) { return (unsigned)__builtin_amdgcn_mov_dpp((int)v, 0x128, 0xf, 0xf, true); }
__device__ __forceinline__ float fmx(float a, float b) { return __builtin_elementwise_maximum(a, b); }

__device__ __forceinline__ int imax(int a, int b) { return a > b ? a : b; }
__device__ __forceinline__ int imin(int a, int b) { return a < b ? a : b; }
__device__ __forceinline__ unsigned umin(unsigned a, unsigned b) { return a < b ? a : b; }
__device__ __forceinline__ unsigned umax(unsigned a, unsigned b) { return a > b ? a : b; }
__device__ __forceinline__ bool wave_all(bool p) { return __builtin_amdgcn_ballot_w64(!p) == 0ull; }
__device__ __forceinline__ float x16mx(float x) { unsigned u = __builtin_bit_cast(unsigned, x); auto r = __builtin_amdgcn_permlane16_swap(u, u, false, false); return fmx(u2f(r[0]), u2f(r[1])); }
__device__ __forceinline__ float x32mx(float x) { unsigned u = __builtin_bit_cast(unsigned, x); auto r = __builtin_amdgcn_permlane32_swap(u, u, false, false); return fmx(u2f(r[0]), u2f(r[1])); }
__device__ __forceinline__ float x16sum(float x) { unsigned u = __builtin_bit_cast(unsigned, x); auto r = __builtin_amdgcn_permlane16_swap(u, u, false, false); return u2f(r[0]) + u2f(r[1]); }
__device__ __forceinline__ float x32sum(float x) { unsigned u = __builtin_bit_cast(unsigned, x); auto r = __builtin_amdgcn_permlane32_swap(u, u, false, false); return u2f(r[0]) + u2f(r[1]); }
template <int CTRL> __device__ __forceinline__ float dppf(float v) { return u2f((unsigned)__builtin_amdgcn_mov_dpp((int)f2u(v), CTRL, 0xf, 0xf, true)); }
#define OWN_PF 1
template <int BYTES> __device__ __forceinline__ void own_code(int tid, unsigned* sink) {
  if (!OWN_PF || __builtin_amdgcn_readfirstlane(tid >> 6) * 4096 >= BYTES) return;
  const char* pc; unsigned keep;
  asm volatile("s_getpc_b64 %0" : "=s"(pc));
  asm volatile("s_mov_b32 %0, m0\n\ts_mov_b32 m0, %3\n\ts_nop 0\n\tglobal_load_lds_dword %1, %2\n\ts_mov_b32 m0, %0"
               : "=&s"(keep) : "v"(tid * 64 < BYTES ? tid * 64 : 0), "s"(pc), "s"((unsigned)(uintptr_t)(LAS unsigned*)sink) : "memory");
}
constexpr int DPP_XOR1 = 0xB1, DPP_XOR2 = 0x4E, DPP_ROR4 = 0x124, DPP_ROR8 = 0x128;
#if defined(__HIP_DEVICE_COMPILE__)
#pragma clang attribute pop
#endif

typedef short v4i16_t __attribute__((ext_vector_type(4)));
__device__ __forceinline__ bf16x8 tr_frag(const char* lo_, const char* hi_) {
  const v4i16_t lo = __builtin_amdgcn_ds_read_tr16_b64_v4i16((LAS v4i16_t*)lo_), hi = __builtin_amdgcn_ds_read_tr16_b64_v4i16((LAS v4i16_t*)hi_);
  return (bf16x8){lo[0], lo[1], lo[2], lo[3], hi[0], hi[1], hi[2], hi[3]};
}
__device__ __forceinline__ int rec_wg(int gp, int m) { return (((gp & 1) * 16 + m) << 3) | (gp >> 1); }
__device__ __forceinline__ float wsum(float v) {
  v += dppf<DPP_XOR1>(v); v += dppf<DPP_XOR2>(v); v += dppf<DPP_ROR4>(v); v += dppf<DPP_ROR8>(v);
  return x32sum(x16sum(v));
}
__device__ __forceinline__ float ln_row(int lane, u32x4 (&xo)[2]) {
  const bool two = lane < 16;
  float v[16];
#pragma unroll
  for (int hf = 0; hf < 2; ++hf)
#pragma unroll
    for (int i = 0; i < 4; ++i) { const unsigned u = xo[hf][i]; v[hf * 8 + 2 * i] = bf2f(u & 0xffff); v[hf * 8 + 2 * i + 1] = bf2f(u >> 16); }
  float s0 = 0.f, s1 = 0.f;
#pragma unroll
  for (int i = 0; i < 8; ++i) { s0 += v[i]; s1 += v[8 + i]; }
  const float mean = wsum(two ? s0 + s1 : s0) * (1.0f / DM);
  s0 = s1 = 0.f;
#pragma unroll
  for (int i = 0; i < 8; ++i) { const float d0 = v[i] - mean, d1 = v[8 + i] - mean; s0 += d0 * d0; s1 += d1 * d1; }
  const float var = rbf(wsum(two ? s0 + s1 : s0) * (1.0f / DM));
  const float mb = rbf(mean);
  const float rden = 1.0f / rbf(sqrtf(rbf(var + 1e-5f)));
  float n2[2] = {0.f, 0.f};
#pragma unroll
  for (int hf = 0; hf < 2; ++hf) {
    float y[8];
#pragma unroll
    for (int i = 0; i < 8; ++i) { y[i] = rbf(rbf(v[hf * 8 + i] - mb) * rden); n2[hf] += y[i] * y[i]; }
    xo[hf] = (u32x4){pack2(y[0], y[1]), pack2(y[2], y[3]), pack2(y[4], y[5]), pack2(y[6], y[7])};
  }
  return wsum(two ? n2[0] + n2[1] : n2[0]);
}

#define PROT(k)
#define WARM_CODE 1
#define B_AUX 16
#define X_AUX 16
#define T_AUX 16
#define WB_NEAR 1
#define PF_POS 0
#define PRB_N0 3
#define PRB_N1 0
#define PRB_LOG(i, v)
#define PLD(p) __builtin_nontemporal_load((const u32x4*)(p))
struct ProArgs { const u16* x; const u16* wq; const u16* wk; const u16* wv; const u16* wo; const u16* wqkvb; const u16* wob; char* ws; };
__global__ __launch_bounds__(640, 3) void prologue_kernel(ProArgs a) {
  __shared__ __attribute__((aligned(16))) char img[192 * 320];
  __shared__ __attribute__((aligned(16))) char slots[10 * 6144];
  __shared__ float red[10][160];
  __shared__ unsigned latw[10];
  __shared__ u32x4 xst[2][640];
  const int b = blockIdx.x, tid = threadIdx.x, lane = tid & 63, v = __builtin_amdgcn_readfirstlane(tid >> 6), c = lane & 15, g = lane >> 4;
  const int h = (b & 7) * 4 + ((b >> 3) & 3), dh = (b >> 5) & 1, cq = b >> 6, ks = dh * 10 + v;
  char* const slot = slots + v * 6144;
  PROT(0);
  __shared__ unsigned sink[64];
  own_code<9600>(tid, sink);
  const unsigned zo = v < 8 ? (unsigned)(((b >> 6) * 512 + ((b & 7) * 2 + (v >> 2)) * 32 + ((b >> 3) & 7) * 4 + (v & 3)) * 1024) : (unsigned)(((v - 7) * 2048 + (b & 7) * 32 + ((b >> 3) & 7) * 4 + (b >> 6)) * 1024);
  __builtin_amdgcn_raw_buffer_store_b128((u32x4){0u, 0u, 0u, 0u}, __builtin_amdgcn_make_buffer_rsrc(a.ws, 0, (int)(3 * PG), 0x00020000), zo + lane * 16, 0, 16);
  __builtin_amdgcn_sched_barrier(0);
  u32x4 wa[2][6], wb[6], tr[4], xo[2];
  const int xrow = b * 8 + (v & 7);
  xo[0] = *(const u32x4*)(a.x + (size_t)xrow * DM + lane * 8);
  xo[1] = *(const u32x4*)(a.x + (size_t)xrow * DM + 512 + c * 8);
  const int job = imin(b + 256 * (9 - v), 800);
  const int jn = job < 600 ? job / 20 : (job % 200) / 20, jk = job % 20;
  {
    const u16* src = job < 600 ? a.wqkvb + (size_t)(32 * jk) * 3 * DM : a.wob + (size_t)(32 * jk) * DM;
#pragma unroll
    for (int i = 0; i < 4; ++i) tr[i] = *(const u32x4*)(src + (size_t)(i * 8 + (lane >> 3)) * (job < 600 ? 3 * DM : DM) + jn * 64 + (lane & 7) * 8);
  }
#pragma unroll
  for (int i = 0; i < 6; ++i) {
    const int idx = (v * 6 + i) * 64 + lane, row = idx / 20;
    wb[i] = PLD(a.wo + (size_t)(h * DH + row) * DM + cq * 160 + (idx - row * 20) * 8);
  }
#pragma unroll
  for (int i = 0; i < 6; ++i) {
    const int idx = i * 64 + lane, r = idx / 24;
#pragma unroll
    for (int u = 0; u < 2; ++u) wa[u][i] = PLD(a.wv + (size_t)(32 * ks + 8 * (r >> 2) + (r & 3) + 4 * u) * HD + h * DH + (idx - r * 24) * 8);
  }
  unsigned pf[2] = {0u, 0u};
  auto prefetch = [&]() {
#pragma unroll
    for (int i = 0; i < 2; ++i) pf[i] = *(const unsigned*)((i ? a.wq : a.wk) + ((size_t)(b * 10 + v) * 48 + imin(lane, 47)) * 32);
  };
  if (PF_POS == 0) prefetch();
  __builtin_amdgcn_sched_barrier(0);
  unsigned lat = ~0u;
  auto probe = [&](int n0, int n) {
    const char* p = a.ws + zo;
#pragma unroll 1
    for (int i = n0; i < n0 + n; ++i) {
      u64 t0, t1; unsigned d;
      asm volatile("s_memtime %0\n\ts_waitcnt lgkmcnt(0)\n\ts_load_dword %2, %3, %4\n\ts_waitcnt lgkmcnt(0)\n\ts_memtime %1\n\ts_waitcnt lgkmcnt(0)"
                   : "=&s"(t0), "=&s"(t1), "=&s"(d) : "s"(p), "s"((i & 3) * 256 + (i >> 2) * 128) : "memory");
      PRB_LOG(i, (unsigned)t1 - (unsigned)t0);
      lat = umin(lat, (unsigned)t1 - (unsigned)t0);
    }
  };
  asm volatile("s_waitcnt vmcnt(26)" ::: "memory");
  probe(0, PRB_N0);
  latw[v] = lat;
  __builtin_amdgcn_sched_barrier(0);
  auto xput = [&](int k) {
    if (v < 8) { xst[k][lane * 8 + (v ^ (lane & 7))] = xo[0]; if (lane < 16) xst[k][(64 + lane) * 8 + (v ^ (lane & 7))] = xo[1]; }
  };
  xput(0);
  const float xn2 = u2f(__builtin_amdgcn_readfirstlane(f2u(ln_row(lane, xo))));
  xput(1);
#pragma unroll
  for (int i = 0; i < 4; ++i) *(u32x4*)(slot + (i * 8 + (lane >> 3)) * 144 + (lane & 7) * 16) = tr[i];
  __builtin_amdgcn_fence(__ATOMIC_RELEASE, "wavefront"); __builtin_amdgcn_wave_barrier();
  bf16x8 tf[4];
#pragma unroll
  for (int t = 0; t < 4; ++t) {
    const char* bp = slot + (8 * g + (c >> 2)) * 144 + t * 32 + (c & 3) * 8;
    tf[t] = tr_frag(bp, bp + 4 * 144);
  }
  __builtin_amdgcn_fence(__ATOMIC_RELEASE, "wavefront"); __builtin_amdgcn_wave_barrier();
  __builtin_amdgcn_sched_barrier(0);
#pragma unroll
  for (int i = 0; i < 6; ++i) {
    const int idx = (v * 6 + i) * 64 + lane, row = idx / 20;
    *(u32x4*)(img + row * 320 + (((idx - row * 20) ^ ((row >> 2) & 2)) * 16)) = wb[i];
  }
  bf16x8 A[2][6];
#pragma unroll
  for (int u = 0; u < 2; ++u) {
#pragma unroll
    for (int i = 0; i < 6; ++i) {
      const int idx = i * 64 + lane, r = idx / 24;
      *(u32x4*)(slot + r * 384 + (((idx - r * 24) ^ (r >> 1)) * 16)) = wa[u][i];
    }
    __builtin_amdgcn_fence(__ATOMIC_RELEASE, "wavefront"); __builtin_amdgcn_wave_barrier();
#pragma unroll
    for (int e = 0; e < 6; ++e) A[u][e] = *(const bf16x8*)(slot + c * 384 + (((4 * e + g) ^ (c >> 1)) * 16));
    __builtin_amdgcn_fence(__ATOMIC_RELEASE, "wavefront"); __builtin_amdgcn_wave_barrier();
  }
  BAR();
  PROT(1);
  if (PF_POS == 0) asm volatile("" :: "v"(pf[0]), "v"(pf[1]));
  if (PF_POS == 1) prefetch();
  {
    const unsigned xf = (unsigned)((b >> 1) * 20480 + (tid >> 3) * 256 + (b & 1) * 128 + (tid & 7) * 16);
    const __amdgpu_buffer_rsrc_t rx = __builtin_amdgcn_make_buffer_rsrc(a.ws + OFF_XN, 0, (int)(OFF_QB - OFF_XN), 0x00020000);
    __builtin_amdgcn_raw_buffer_store_b128(xst[1][tid ^ ((tid >> 3) & 7)], rx, xf, 0, X_AUX);
    __builtin_amdgcn_raw_buffer_store_b128(xst[0][tid ^ ((tid >> 3) & 7)], rx, xf + (unsigned)(OFF_XB - OFF_XN), 0, X_AUX);
    if (v < 8 && lane < 4) ((float*)(a.ws + OFF_XNRM))[xrow * 4 + lane] = lane == 0 ? xn2 : 0.f;
  }
  if (job < 800) {
    const __amdgpu_buffer_rsrc_t rt = __builtin_amdgcn_make_buffer_rsrc(a.ws + (job < 600 ? OFF_WQKVBT : OFF_WOBT) + ((size_t)jn * 80 + jk) * 1024, 0, 81920, 0x00020000);
#pragma unroll
    for (int t = 0; t < 4; ++t) __builtin_amdgcn_raw_buffer_store_b128(__builtin_bit_cast(u32x4, tf[t]), rt, lane * 16 + t * 20480, 0, T_AUX);
  }
  const int gp = h >> 1, hh = h & 1;
  const __amdgpu_buffer_rsrc_t rwb = __builtin_amdgcn_make_buffer_rsrc(a.ws + OFF_WB, 0, (int)WB_BYTES, 0x00020000);
  const unsigned phi = WB_NEAR ? ((((unsigned)b >> 4) & 1u) ^ (unsigned)(latw[8] < latw[9]) ^ (WB_NEAR - 1)) << 13 : (unsigned)(b & 32) << 8;
  if (tid == 0) ((unsigned*)(a.ws + OFF_PHI))[b] = phi;
#pragma unroll
  for (int n = 0; n < 10; ++n) {
    const int off = n * 32 + 8 * (c & 3), T = cq * 10 + n;
    const char* bp = img + (8 * g + (c >> 2)) * 320 + (off ^ ((g & 1) << 5));
    f32x4 ac[2] = {{0.f, 0.f, 0.f, 0.f}, {0.f, 0.f, 0.f, 0.f}};
#pragma unroll
    for (int e = 0; e < 6; ++e) {
      const bf16x8 bf = tr_frag(bp + e * 32 * 320, bp + (e * 32 + 4) * 320);
      ac[0] = mfma32(A[0][e], bf, ac[0]); ac[1] = mfma32(A[1][e], bf, ac[1]);
    }
    const u32x4 o = {pack2(ac[0][0], ac[0][1]), pack2(ac[0][2], ac[0][3]), pack2(ac[1][0], ac[1][1]), pack2(ac[1][2], ac[1][3])};
    const unsigned oo = (T < 32 ? wb_off(rec_wg(gp, T & 15) * 100 + (hh * 3 + (T >> 4)) * 20 + ks) + (unsigned)(lane * 16)
                                : wb_off(rec_wg(gp, 2 * (T - 32) + (c >> 3)) * 100 + 40 + ks) + (unsigned)((g * 16 + hh * 8 + (c & 7)) * 16)) ^ phi;
    __builtin_amdgcn_raw_buffer_store_b128(o, rwb, oo, 0, B_AUX);
    float n2 = 0.f;
#pragma unroll
    for (int r = 0; r < 4; ++r) n2 += ac[0][r] * ac[0][r] + ac[1][r] * ac[1][r];
    n2 = x32sum(x16sum(n2));
    if (g == 0) red[v][n * 16 + c] = n2;
  }
  PROT(2);
  if (PF_POS == 1) asm volatile("" :: "v"(pf[0]), "v"(pf[1]));
  if (v >= 8 && b < 64 && lane == 0) ((unsigned*)(a.ws + OFF_LAT))[(v - 4) * 64 + b] = umin(lat, 60000u);
  if (WARM_CODE && v == 7) {
    const char* pc; int d0, d1;
    asm("s_getpc_b64 %0\n\ts_mov_b32 %1, _Z10rec_kernel7RecArgs@rel32@lo+4\n\ts_mov_b32 %2, _Z11proj_kernelPKtS0_S0_Pt@rel32@lo+12" : "=s"(pc), "=s"(d0), "=s"(d1));
    const char *c0 = pc + d0, *c1 = pc + d1;
    const long o = ((long)b * 64 + lane) * 64;
    if (o < c1 - c0) asm volatile("" :: "v"(*(const volatile GAS unsigned*)(c0 + o)));
  }
  if (v == 9 && lane < 32) {
    const int q = b * 32 + lane;
    *(u32x4*)(a.ws + OFF_KV + (size_t)(q >> 2) * KVT + 1280 + (q & 3) * 336 + 320) = (u32x4){0x3f803f80u, 0x3f803f80u, 0x3f803f80u, 0x3f803f80u};
  }
  BAR();
  if (v == 1 && lane == 0) ((unsigned*)(a.ws + OFF_LAT))[b] = umin(umin(umin(latw[0], latw[4]) + umin(latw[1], latw[5]), 30000u) + umin(umin(latw[2], latw[6]) + umin(latw[3], latw[7]), 30000u), 60000u);
  if (v == 0) {
    float mx = 0.f;
#pragma unroll
    for (int k = 0; k < 3; ++k) {
      const int col = k * 64 + lane;
      float sm = 0.f;
#pragma unroll
      for (int i = 0; i < 10; ++i) sm += red[i][col < 160 ? col : 0];
      mx = fmx(mx, sm);
    }
    mx = fmx(mx, dppf<DPP_XOR1>(mx)); mx = fmx(mx, dppf<DPP_XOR2>(mx)); mx = fmx(mx, dppf<DPP_ROR4>(mx)); mx = fmx(mx, dppf<DPP_ROR8>(mx));
    mx = x32mx(x16mx(mx));
    if (lane == 0) ((float*)(a.ws + OFF_BNRM))[(cq * 32 + h) * 2 + dh] = mx;
  }
  PROT(3);
}

struct RecArgs {
  const u16* mem0; const u16* wq; const u16* wk; const u16* xold; u16* xnew; char* ws;
  unsigned layer; unsigned pad;
};
#define RLX_AGENT __ATOMIC_RELAXED, __HIP_MEMORY_SCOPE_AGENT
constexpr unsigned SPIN_CAP = 1u << 20;
#define TSTAMP(k, v)
#define ARRIVE(k)
#define ARRIVE_OUT()
#define ARRIVE_DECL()
#define LOGV(k, v)
#define LOGF(k, v)
#define LOGP(k)
#define LOGQ(k)
#define LOGF2(k)
#define LOGD(k)
#define LOGT(k, v)
#define STAMP_DECL()
#define STAMP(k)
#define STAMPV(k, v)
#define STAMP_OUT()
#define REC_D1 520
#define SCH_V 18
#define SCH_VMIN 0
#define SCH_DMIN 4
#define SCH_PEN 150
#define SCH_VMIN2 (-4)
#define SCH_PEN2 40
#define SCH_GU 1
#define SCH_GD 2
#define SCH_UP 6
#define SCH_D0 196
#define SCH_FIRST 120
#define EXP_NODMA 0
#define BG_HOLD 220
#define QBATCH 0
#define WPF_N 5
#define WPF_W 0
#define WPF0 (63 - WPF_N)
#define BG_HOLD4 250
#define D1_W1 20
#define F3_D 1200
#define T2W 2
#define AT_ASM 1
#define PM_Z 1
#define P2_X 1
#define ST_KS 14
#define ST_KS0 ST_KS
#define ST_KS1 ST_KS
#define PADR 0x6666
#define PADT 0x000
#define CODE_PAD(n) asm volatile(".p2align 6\n.rept %0\ns_nop 0\n.endr" :: "n"(n))
#define ST_X 250
#define ST_ALL 4
#define CPF_S 61
#define FR_D 700
#define FR_PACE 6
#define END_E60 600
#define END_E1 1300
#define END_E2 3400
#define END_E62 2800
#define END_E3 6800
#define END_E4 11400
#define EARLY_N 6
#define EARLY_D1 0
#define EARLY_V 4
#define EARLY_V1 10
#define F3 0
#define F3A 0
#define F3A_D 400
#define T4_D 300
#define T4_P 1
#define P2_W 1
#define PS_SL 1
#define F2B 4
#define F0C 0
#define F2C 0
#define W2_P 1
#define SCH_W 1
#define SP_SL 0
#define D1_M 300
#define D1_MIN 260
#define D1_C 96
#define D1_G 2
#define BG_HOLD6 0
#define W2_D 0
#define END_E61 2600
#define D1_LATE 200
#define P2_G0 10
#define P2_G2 6
#define P2_G3 6
#define SCH_RL 0
#define SCH_P 3
#define MMA_P 0
#define SP_P 0
#define F6 8
#define F7 10
#define XPF_W 2
#define SCH_LATE 0
#define LATE_V 6
#define QW 3
#define FB_SL 0
#define P2_G1 1
#define P1_G 0
#define F4Z 0
#define F5Z 0
#define T5_Z 2
#define F0A 3
#define FB_E 0
#define EXACT 1
#define D1_P 3
#define VO_A0 9
#define VO_A2 10
#define VO_B0 3
#define VO_B1 0
#define VO_N2 7
#define VO_N3 10
#define VO_W -1
#define UN_P 2
#define VP_E 1
#define TS_B 0
#define ST_E 1
#define W1_SL 0
#define VO_A1 4
#define LAST_V 18
#define END_E59 3200
#define END_G2 600
#define END_G3 600
#define QR 4
#define WPF_S 128
#define END_SL 4
#define SCH_DR 184
#define SCH_DK 0
#define FR_HOLD 0
#define FW 8
#define KP0 0
#define KP1 0
#define XPF 3
#define W1_D 0
#define F4 12
#define F5 0
#define F67 0
#define KP2 0
#define REC_RNG 70u
#define SCH_DRAIN 40
#define PL_X 0
#define FC(k)
#define PIN_V(x) asm volatile("" : "+v"(x))
#define PIN_S(x) asm volatile("" : "+s"(x))
#define VM_DRAIN() do { asm volatile("s_waitcnt vmcnt(0)" ::: "memory"); __builtin_amdgcn_s_waitcnt(0x0F70); } while (0)
#define SC1_LOAD(off) __builtin_amdgcn_raw_buffer_load_b128(rs, (off), 0, 16)
#define SC1_LOADS(off, soff) __builtin_amdgcn_raw_buffer_load_b128(rs, (off), (soff), 16)
#define SC1_STORE8S(v, off, soff) __builtin_amdgcn_raw_buffer_store_b64((v), rs, (off), (soff), 16)
#define SC1_LOAD8(off) __builtin_amdgcn_raw_buffer_load_b64(rs, (off), 0, 16)
#define SC1_STORE(v, off) __builtin_amdgcn_raw_buffer_store_b128((v), rs, (off), 0, 16)
#define SC1_STORE8(v, off) __builtin_amdgcn_raw_buffer_store_b64((v), rs, (off), 0, 16)
#if defined(__HIP_DEVICE_COMPILE__)
#pragma clang attribute push(__attribute__((target("no-packed-fp32-ops"))), apply_to = function)
#endif
__device__ __forceinline__ unsigned now32() { return (unsigned)__builtin_amdgcn_s_memrealtime(); }
__device__ __forceinline__ void wait_until(unsigned t) { for (;;) { const int d = (int)(t - now32()); if (d <= 0) break; if (d > 8) __builtin_amdgcn_s_sleep(1); } }
__device__ __forceinline__ unsigned cyc32() { return (unsigned)__builtin_amdgcn_s_memtime(); }
__device__ __forceinline__ void wait_cyc(unsigned t) { for (;;) { const int d = (int)(t - cyc32()); if (d <= 0) break; if (d > 200) __builtin_amdgcn_s_sleep(1); } }
#define WAIT_ASM(clk, far_) "1:\n\t" clk " vcc\n\ts_waitcnt lgkmcnt(0)\n\ts_sub_i32 %0, %1, vcc_lo\n\ts_cmp_lt_i32 %0, " far_ "\n\ts_cbranch_scc1 3f\n\ts_sleep 1\n\ts_branch 1b\n" \
                            "2:\n\t" clk " vcc\n\ts_waitcnt lgkmcnt(0)\n\ts_sub_i32 %0, %1, vcc_lo\n3:\n\ts_cmp_gt_i32 %0, 0\n\ts_cbranch_scc1 2b"
__device__ __forceinline__ int spin_until(unsigned t) { int d; asm volatile(WAIT_ASM("s_memrealtime", "9") : "=&s"(d) : "s"(t) : "vcc", "scc", "memory"); return d; }
__device__ __forceinline__ int spin_cyc(unsigned t) { int d; asm volatile(WAIT_ASM("s_memtime", "200") : "=&s"(d) : "s"(t) : "vcc", "scc", "memory"); return d; }
__device__ __forceinline__ void spin_exact(unsigned t) {
  if (!EXACT) { spin_cyc(t); return; }
  int d;
  asm volatile("1:\n\ts_memtime vcc\n\ts_waitcnt lgkmcnt(0)\n\ts_sub_i32 %0, %1, vcc_lo\n\ts_cmp_gt_i32 %0, 95\n\ts_cbranch_scc1 1b\n\t"
               "s_max_i32 %0, %0, 0\n\ts_andn2_b32 %0, %0, 3\n\ts_sub_i32 %0, 3f-2f, %0\n\ts_getpc_b64 vcc\n"
               "2:\n\ts_add_u32 vcc_lo, vcc_lo, %0\n\ts_addc_u32 vcc_hi, vcc_hi, 0\n\ts_setpc_b64 vcc\n\t.rept 24\n\ts_nop 0\n\t.endr\n3:"
               : "=&s"(d) : "s"(t) : "scc", "vcc", "memory");
}
template <int OFF, int SL> __device__ __forceinline__ void lds_spin(unsigned lds0, unsigned want) {
  unsigned v;
  asm volatile("1:\n\tds_read_b32 %0, %1 offset:%3\n\t.if %4\n\ts_sleep %4\n\t.endif\n\ts_waitcnt lgkmcnt(0)\n\tv_cmp_gt_u32 vcc, %2, %0\n\ts_cbranch_vccnz 1b"
               : "=&v"(v) : "v"(lds0), "s"(want), "n"(OFF), "n"(SL) : "vcc", "memory");
}
template <int CTRL> __device__ __forceinline__ int dppi(int v) { return __builtin_amdgcn_mov_dpp(v, CTRL, 0xf, 0xf, true); }
__device__ __forceinline__ int wave_imax(int l) {
  l = imax(l, dppi<DPP_XOR1>(l)); l = imax(l, dppi<DPP_XOR2>(l)); l = imax(l, dppi<DPP_ROR4>(l)); l = imax(l, dppi<DPP_ROR8>(l));
  int a = imax(__builtin_amdgcn_readlane(l, 0), __builtin_amdgcn_readlane(l, 16)), b = imax(__builtin_amdgcn_readlane(l, 32), __builtin_amdgcn_readlane(l, 48));
  PIN_S(a); PIN_S(b);
  return imax(a, b);
}
__device__ __forceinline__ void glds16(const void* gsrc, unsigned lds_dst) {
  unsigned keep;
  asm volatile("s_mov_b32 %0, m0\n\ts_mov_b32 m0, %2\n\ts_nop 0\n\tglobal_load_lds_dwordx4 %1, off\n\ts_mov_b32 m0, %0" : "=&s"(keep) : "v"(gsrc), "s"(lds_dst) : "memory");
}
__device__ __forceinline__ void glds16_sc1(const void* gsrc, unsigned lds_dst) {
  unsigned keep;
  asm volatile("s_mov_b32 %0, m0\n\ts_mov_b32 m0, %2\n\ts_nop 0\n\tglobal_load_lds_dwordx4 %1, off sc1\n\ts_mov_b32 m0, %0" : "=&s"(keep) : "v"(gsrc), "s"(lds_dst) : "memory");
}
__device__ __forceinline__ unsigned acc_off(int G, int row) {
  return (unsigned)(G < 128 ? ((G & 31) | ((G & 32) << 1) | ((G & 64) >> 1)) * 256 + row * 16 : (2 * G - 128 + (row >> 2)) * 256 + (row & 3) * 16);
}
__device__ __forceinline__ unsigned nz(unsigned v) { return v > 1u ? v : 1u; }
__device__ __forceinline__ unsigned pack2n(float lo, float hi) { return nz(pack2(lo, hi)); }
__device__ __forceinline__ unsigned min4(u32x4 v) { return umin(umin(v[0], v[1]), umin(v[2], v[3])); }
__device__ __forceinline__ bf16x8 as8(u32x4 v) { return __builtin_bit_cast(bf16x8, v); }

constexpr int NV = 7, NKQ = 2;
constexpr int L_RED1 = 0;
constexpr int L_BM = L_RED1 + 8 * 1536;
constexpr int L_STG = L_BM + 10240;
constexpr int L_ST = L_STG + 2048 + 2560;
constexpr int L_MISC = L_ST + 512;
constexpr int L_KX = L_MISC + 128;
constexpr int L_QX = L_KX + NKQ * 2048;
constexpr int L_XN = L_QX + NKQ * 2048;
constexpr int L_VOX = L_XN + 81920;
constexpr int L_VOM = L_VOX + NV * 5120;
constexpr int L_BYTES = L_VOM + NV * 1280;
constexpr int S_IMG = L_XN + 40960;
static_assert(S_IMG + 61440 <= L_VOM, "start-up image");
static_assert(L_BYTES <= 160 * 1024, "LDS budget");

__device__ __forceinline__ bf16x8 wqk_frag(const char* lds, int t, int ks, int lane) {
  const int c = lane & 15, g = lane >> 4, p = c & 3;
  const char* q = lds + S_IMG + (32 * ks + 8 * g + (c >> 2)) * 96 + 8 * p + (t == 0 ? 0 : (t == 1 ? 48 : (p < 2 ? 32 : 64)));
  const v4i16_t lo = __builtin_amdgcn_ds_read_tr16_b64_v4i16((LAS v4i16_t*)q), hi = __builtin_amdgcn_ds_read_tr16_b64_v4i16((LAS v4i16_t*)(q + 4 * 96));
  return (bf16x8){lo[0], lo[1], lo[2], lo[3], hi[0], hi[1], hi[2], hi[3]};
}
__device__ __forceinline__ bf16x8 sel_frag(int c, int base) {
  bf16x8 r;
#pragma unroll
  for (int i = 0; i < 8; ++i) r[i] = (c == base + i) ? (short)0x3f80 : (short)0;
  return r;
}
__device__ __forceinline__ bf16x8 qk_frag(const char* p4, const char* p2, const char* p2b) {
  f32x4 a0 = *(const f32x4*)(p4), a1 = *(const f32x4*)(p4 + 1792), a2 = *(const f32x4*)(p4 + 3584), a3 = *(const f32x4*)(p4 + 5376);
  f32x2 b0 = *(const f32x2*)(p2), b1 = *(const f32x2*)(p2 + 1792), b2 = *(const f32x2*)(p2b), b3 = *(const f32x2*)(p2b + 1792);
  f32x4 va = (a0 + a1) + (a2 + a3);
  f32x2 vb = (b0 + b1) + (b2 + b3);
  return mk8(pack2(va[0], va[1]), pack2(va[2], va[3]), pack2(vb[0], vb[1]), 0u);
}
__device__ __forceinline__ void x_fetch(const u16* src, unsigned lds0, int seg, int p, int lane) {
  if (EXP_NODMA) return;
  glds16_sc1(src + (size_t)p * 512 + lane * 8, lds0 + L_XN + (seg & 1) * 40960 + p * 1024);
}
template <int N> __device__ __forceinline__ void x_fetch_n(const u16* src, unsigned lds0, int seg, int p0, unsigned voff) {
  if (EXP_NODMA) return;
#pragma unroll
  for (int i = 0; i < N; i += 4) {
    unsigned keep;
    asm volatile("s_mov_b32 %0, m0\n\ts_mov_b32 m0, %3\n\ts_nop 0\n\tglobal_load_lds_dwordx4 %1, %2 sc1\n\t"
                 ".if %4 > 1\n\tglobal_load_lds_dwordx4 %1, %2 offset:1024 sc1\n\t.endif\n\t.if %4 > 2\n\tglobal_load_lds_dwordx4 %1, %2 offset:2048 sc1\n\t.endif\n\t"
                 ".if %4 > 3\n\tglobal_load_lds_dwordx4 %1, %2 offset:3072 sc1\n\t.endif\n\ts_mov_b32 m0, %0"
                 : "=&s"(keep) : "v"(voff), "s"(src + (size_t)(p0 + i) * 512), "s"(lds0 + L_XN + (seg & 1) * 40960 + (p0 + i) * 1024), "n"(N - i < 4 ? N - i : 4) : "memory");
  }
}
__device__ __forceinline__ int qkv_row(int b) { return (b & 7) / QR * (2 * QR) + (b >> 3) / (16 / QR); }
__device__ __forceinline__ int qkv_col(int b) { return (b & 7) % QR * (16 / QR) + (b >> 3) % (16 / QR); }
__device__ __forceinline__ const u16* x_src(const RecArgs& a, int seg, int b) {
  return seg < NSEG ? (const u16*)(a.ws + OFF_XN) + (size_t)seg * 32 * DM : a.xnew + (size_t)(qkv_row(b) + 16 * (seg - NSEG)) * 32 * DM;
}
template <int N>
__device__ __forceinline__ void img_fetch(const RecArgs& a, unsigned lds0, int h, int j, int part, int lane) {
#pragma unroll
  for (int i = 0; i < N; ++i) {
    const int n = part * N + i, idx = n * 64 + lane, row = idx / 6, pc = idx - row * 6;
    if (FR_PACE && N == 20) asm volatile("s_waitcnt vmcnt(%0)" :: "n"(FR_PACE) : "memory");
    glds16((pc < 3 ? a.wq : a.wk - 24) + (size_t)row * HD + h * DH + j * 24 + pc * 8, lds0 + S_IMG + n * 1024);
  }
}
constexpr int P2B = F4 + F5 + 2 * F67 + F6 + F7, PZ = P2B + F2B, PA = PZ, P0C = PA + 2 * F0A, F3C = 40 - F3A - F3 - P0C - 2 * F0C - F2C;
static_assert(F3C >= 0, "fetch plan");
template <int OFF> __device__ __forceinline__ void lds_inc(unsigned lds0) {
  asm volatile("s_mov_b64 exec, 1\n\tds_add_u32 %0, %1 offset:%2\n\ts_mov_b64 exec, -1" :: "v"(lds0), "v"(1u), "n"(OFF) : "memory");
}
__device__ __forceinline__ void lds_arrive(unsigned* p, int lane) {
  asm volatile("s_waitcnt lgkmcnt(0)" ::: "memory");
  if (lane == 0) __hip_atomic_fetch_add(p, 1u, __ATOMIC_RELAXED, __HIP_MEMORY_SCOPE_WORKGROUP);
}
__device__ __forceinline__ void lds_wait(unsigned* p, unsigned want) {
  while (__hip_atomic_load(p, __ATOMIC_RELAXED, __HIP_MEMORY_SCOPE_WORKGROUP) < want) __builtin_amdgcn_s_sleep(1);
  asm volatile("" ::: "memory");
}
constexpr float KEXP = 0.07216878364870322f * 1.4426950408889634f;
#define FRESH_LANE() int lane = lane0; asm volatile("" : "+v"(lane)); const int c = lane & 15, g = lane >> 4; (void)c; (void)g

struct OutTask {
  u32x4 y[4]; u32x2 xo, out;
  bf16x8 sel;
  unsigned t5f, t5o;
  __device__ __forceinline__ void init(int b, int lane) {
    const int um = b / 10, uq = (b / 5) & 1, up = b % 5, tok = lane & 15, k = up < 4 ? (up >> 1) * 256 + um * 16 + (up & 1) * 8 : 512 + um * 8, q4 = ((lane >> 4) & 1) * 4;
    t5f = (unsigned)(((uq * 20 + (k >> 5)) * 64 + ((k & 31) >> 3) * 16 + tok) * 8 + q4);
    t5o = (unsigned)(((um * 2 + uq) * 16 + (lane >> 4)) * 1280 + up * 256 + tok * 16);
    if (b >= 160) { t5f = 0u; t5o = 0x7ffffff0u; }
    sel = sel_frag(tok, 0);
    PIN_V(sel);
  }
  static __device__ __forceinline__ bool ok(int sl, int b) { return sl >= 0 && b < 160; }
  __device__ __forceinline__ void load(const RecArgs& a, const __amdgpu_buffer_rsrc_t rs, int sl, int b, int part = 3) {
    const unsigned o = t5o | (sl < 0 ? 0x7ff00000u : 0u);
    sl = imax(sl, 0);
#pragma unroll
    for (int k = 0; k < 4; ++k) if (part & 1) y[k] = SC1_LOADS(o, op_slot(sl) + k * 5120);
    if (part & 2) xo = __builtin_bit_cast(u32x2, __hip_atomic_load((const gu64*)(a.xold + (size_t)sl * 32 * DM + t5f), RLX_AGENT));
  }
  __device__ __forceinline__ bool ready(int b) { return b >= 160 || wave_all(umin(umin(min4(y[0]), min4(y[1])), umin(min4(y[2]), min4(y[3]))) != 0u); }
  __device__ __forceinline__ void run(int lane) {
    f32x4 d = {0.f, 0.f, 0.f, 0.f};
#pragma unroll
    for (int k = 0; k < 4; ++k) d = mfma32(sel, as8(y[k]), d);
#pragma unroll
    for (int i = 0; i < 2; ++i) {
      const unsigned r = pack2(d[2 * i], d[2 * i + 1]), x = xo[i];
      out[i] = pack2(u2f(x << 16) + u2f(r << 16), u2f(x & 0xffff0000u) + u2f(r & 0xffff0000u));
    }
    PIN_V(out);
  }
  __device__ __forceinline__ void store(const RecArgs& a, int sl, int b, int lane) {
    __builtin_amdgcn_raw_buffer_store_b64(out, __builtin_amdgcn_make_buffer_rsrc(a.xnew, 0, ok(sl, b) ? S_ * DM * 2 : 0, 0x00020000), (t5f + (unsigned)sl * 32 * DM) * 2u | (unsigned)(lane & 32) << 25, 0, 16);
  }
};

struct PvTask2 {
  u32x4 y[2][2], pa[2], pb[2];
  unsigned lo, lm, vr, ma, mz, so, sz;
  __device__ __forceinline__ void init(int gp, int lane) {
    const int hi = lane >> 5;
    lo = pt_tok(2 * gp) + (unsigned)(lane * 16); lm = pt_mem(2 * gp + hi) + (unsigned)((lane & 31) * 8);
    vr = (unsigned)(L_VOX + 2048 + (lane ^ 8) * 16);
    ma = (unsigned)(L_VOM + hi * 768 + (lane & 31) * 8); mz = (unsigned)(L_VOM + 512 + ((lane & 31) ^ (hi * 8)) * 8);
    so = (unsigned)(L_STG + 2048 + (hi * 16 + (lane & 15)) * 16 + ((lane >> 4) & 1) * 8);
    sz = hi ? (unsigned)(L_RED1 + 8704 + lane * 8) : so + 1024;
    PIN_V(lo); PIN_V(lm); PIN_V(vr); PIN_V(ma); PIN_V(mz); PIN_V(so); PIN_V(sz);
#pragma unroll
    for (int q = 0; q < 2; ++q) { pa[q] = pb[q] = y[q][0] = y[q][1] = (u32x4){0u, 0u, 0u, 0u}; PIN_V(pa[q]); PIN_V(pb[q]); }
  }
  __device__ __forceinline__ void load(const __amdgpu_buffer_rsrc_t rs, unsigned o) {
#pragma unroll
    for (int q = 0; q < 2; ++q) {
      y[q][0] = SC1_LOADS(lo + q * 1024, o); y[q][1] = SC1_LOADS(lo + 2048 + q * 1024, o);
      const u32x2 p = __builtin_amdgcn_raw_buffer_load_b64(rs, lm + q * 256, o, 16);
      pa[q][0] = p[0]; pa[q][1] = p[1];
    }
  }
  __device__ __forceinline__ bool ready() {
    return wave_all(umin(umin(umin(min4(y[0][0]), min4(y[0][1])), umin(min4(y[1][0]), min4(y[1][1]))), umin(umin(pa[0][0], pa[0][1]), umin(pa[1][0], pa[1][1]))) != 0u);
  }
  template <bool DIRECT> __device__ __forceinline__ void run(char* lds, int sv, int lane, const __amdgpu_buffer_rsrc_t rs, unsigned og) {
    const f32x4 Z4 = {0.f, 0.f, 0.f, 0.f};
    const char* vp = lds + L_VOX + sv * 5120 + lane * 16;
    bf16x8 vx[6];
#pragma unroll
    for (int t = 0; t < 5; ++t) vx[t] = *(const bf16x8*)(vp + t * 1024);
    vx[5] = *(const bf16x8*)(lds + vr + sv * 5120);
    const u32x2 a0 = *(const u32x2*)(lds + ma + sv * 1280), a1 = *(const u32x2*)(lds + ma + sv * 1280 + 256);
    const u32x2 z0 = *(const u32x2*)(lds + mz + sv * 1280), z1 = *(const u32x2*)(lds + mz + sv * 1280 + 256);
    const bf16x8 vm = mk8(a0[0], a0[1], a1[0], a1[1]), vz = mk8(z0[0], z0[1], z1[0], z1[1]);
#pragma unroll
    for (int q = 0; q < 2; ++q) { PIN_V(pa[q]); pb[q][2] = pa[q][0]; pb[q][3] = pa[q][1]; PIN_V(pb[q]); }
    __builtin_amdgcn_sched_barrier(0);
    f32x4 o[2][3];
#pragma unroll
    for (int q = 0; q < 2; ++q) {
#pragma unroll
      for (int t = 0; t < 3; ++t) o[q][t] = mfma32(vx[t], as8(y[q][0]), Z4);
#pragma unroll
      for (int t = 0; t < 3; ++t) o[q][t] = mfma32(vx[3 + t], as8(y[q][1]), o[q][t]);
      o[q][0] = mfma32(vm, as8(pa[q]), o[q][0]); o[q][1] = mfma32(vm, as8(pb[q]), o[q][1]); o[q][2] = mfma32(vz, as8(pa[q]), o[q][2]);
    }
#pragma unroll
    for (int q = 0; q < 2; ++q) {
#pragma unroll
      for (int t = 0; t < 3; ++t) {
        const u32x2 v = {pack2n(o[q][t][0], o[q][t][1]), pack2n(o[q][t][2], o[q][t][3])};
        if (DIRECT) SC1_STORE8S(v, (so - (L_STG + 2048) + t * 512) | (t == 2 ? (unsigned)(lane & 32) << 25 : 0u), og + q * 20480);
        else *(u32x2*)(lds + (t < 2 ? so + t * 512 : sz) + q * 1280) = v;
      }
    }
  }
};

struct TokScores {
  bf16x8 k[2], q[2]; u32x4 v[2];
  __device__ __forceinline__ void get(const char* lds, int seg, int lane) {
#pragma unroll
    for (int i = 0; i < 2; ++i) { k[i] = *(const bf16x8*)(lds + L_KX + (seg & 1) * 2048 + i * 1024 + lane * 16); q[i] = *(const bf16x8*)(lds + L_QX + (seg & 1) * 2048 + i * 1024 + lane * 16); }
  }
  __device__ __forceinline__ void run(int qt) {
    const f32x4 Z4 = {0.f, 0.f, 0.f, 0.f};
    const f32x4 d1 = mfma32(k[0], q[qt], Z4), d2 = mfma32(k[1], q[qt], Z4);
    v[qt] = (u32x4){pack2n(d1[0], d1[1]), pack2n(d1[2], d1[3]), pack2n(d2[0], d2[1]), pack2n(d2[2], d2[3])};
    PIN_V(v[qt]);
  }
  __device__ __forceinline__ void store(const __amdgpu_buffer_rsrc_t rs, unsigned off, unsigned o) {
    __builtin_amdgcn_raw_buffer_store_b128(v[0], rs, off, o, 16); __builtin_amdgcn_raw_buffer_store_b128(v[1], rs, off + 1024, o, 16);
    asm volatile("s_nop 1" ::: "memory");
  }
};

__device__ __forceinline__ void touch(const char* base, unsigned off, unsigned lds0) {
  unsigned keep;
  asm volatile("s_mov_b32 %0, m0\n\ts_mov_b32 m0, %3\n\ts_nop 0\n\tglobal_load_lds_dword %1, %2 sc1\n\ts_mov_b32 m0, %0" : "=&s"(keep) : "v"(off), "s"(base), "s"(lds0 + L_RED1 + 8192) : "memory");
}
template <int N> __device__ __forceinline__ void stouch(const char* p, unsigned& d) {
  asm volatile(".set pf_o, 0\n\t.rept %2\n\ts_load_dword %0, %1, pf_o\n\t.set pf_o, pf_o + 128\n\t.endr" : "+s"(d) : "s"(p), "n"(N) : "memory");
}
__device__ __forceinline__ void code_touch(int b, int lane, unsigned lds0) {
  const char* pc; int d0, d1;
  asm volatile("s_getpc_b64 %0\n\ts_mov_b32 %1, _Z10rec_kernel7RecArgs@rel32@lo+4\n\ts_mov_b32 %2, _Z11attb_kernelPKtS0_Pt@rel32@lo+12" : "=s"(pc), "=s"(d0), "=s"(d1));
  const int o = (lane * 32 + (b >> 3)) * 128;
  touch(pc + d0, (unsigned)(o < d1 - d0 ? o : 0), lds0);
}
__device__ __forceinline__ unsigned zero_base(int b, int h, int j, int ln) {
  return ln < 32 ? (unsigned)((h & 3) * 32768 + j * 512 + ln * 16) : ln < 40 ? (unsigned)((h & 2) * 1024 + j * 256 + (ln & 4) * 32 + (h & 1) * 64 + (ln & 3) * 16)
                 : (unsigned)OFF_ACC + (b < ACC_NB ? (unsigned)(b * 256 + (ln - 40) * 16) : ts_piece((b - ACC_NB) * 8 + ln - 40));
}
__device__ __forceinline__ unsigned sct_slot(unsigned ck, unsigned sl) { return sl * SCT_STEP + ((ck >> (sl & 4u)) & 7u) * 4096u; }
__device__ __forceinline__ unsigned scm_slot(unsigned ck, unsigned sl) { return OFF_SCM + sl * SCM_STEP + ((ck >> 8) & 7u) * 4096u; }
__device__ __forceinline__ int ck_rot(int x) { return ((x & 1) << 2) | (x >> 1); }
struct Place {
  unsigned t[8];
  __device__ __forceinline__ void load(const char* ws, int lane) {
#pragma unroll
    for (int x = 0; x < 8; ++x) t[x] = *(const unsigned*)(ws + OFF_LAT + ((imin(lane >> 3, 5) * 8 + (ck_rot(x) ^ (lane & 7))) * 8 + x) * 4);
  }
  __device__ __forceinline__ unsigned get(int b, int lane) {
    int c = (int)((((t[0] + t[1]) + (t[2] + t[3])) + ((t[4] + t[5]) + (t[6] + t[7]))) * 8u) + (lane & 7);
    c = imin(c, dppi<DPP_XOR1>(c)); c = imin(c, dppi<DPP_XOR2>(c)); c = imin(c, dppi<0x141>(c));
    unsigned k = 0u;
#pragma unroll
    for (int r = 0; r < 6; ++r) k |= (unsigned)(__builtin_amdgcn_readlane(c, 8 * r) & 7) << (4 * r);
    return (k ^ PL_X) ^ (unsigned)ck_rot(b & 7) * 0x111111u;
  }
};

struct Sched {
  u32x4 tsv, rec;
  unsigned side, o;
  __device__ __forceinline__ void pre(int s) { o = (unsigned)OFF_ACC + (unsigned)((s + SR - 1) % SR) * (unsigned)ACC_STEP + ACC_TS + side; PIN_S(o); }
  __device__ __forceinline__ void load(const __amdgpu_buffer_rsrc_t rs, int lane) {
    tsv = SC1_LOADS(ts_piece(lane & 63) - ACC_TS, o); rec = SC1_LOADS(128, o);
  }
  __device__ __forceinline__ unsigned last(unsigned ref) {
    const unsigned o = 0x40000000u - ref;
    int l = (int)umax(umax(tsv[0] ? tsv[0] + o : 0u, tsv[1] ? tsv[1] + o : 0u), umax(tsv[2] ? tsv[2] + o : 0u, tsv[3] ? tsv[3] + o : 0u));
    return (unsigned)wave_imax(l) - o;
  }
  __device__ __forceinline__ unsigned run(const RecArgs& a, const __amdgpu_buffer_rsrc_t rs, int s, int b, int lane) {
    constexpr int w = 3; (void)w;
    unsigned acur = __builtin_amdgcn_readfirstlane(rec[0]), aprev = __builtin_amdgcn_readfirstlane(rec[1]), hist = __builtin_amdgcn_readfirstlane(rec[3]);
    int D8 = (int)__builtin_amdgcn_readfirstlane(rec[2]);
    const int d = wave_imax((int)(umax(umax(tsv[0], tsv[1]), umax(tsv[2], tsv[3])) - aprev)), dd = 8 * d - D8;
    int Dn = D8 + (dd >> (dd > 0 ? SCH_GU : SCH_GD));
    unsigned anext = (acur + (unsigned)((Dn + 4 + imax(Dn - 8 * SCH_DR, 0) * SCH_DK / 8) >> 3) + SCH_V + (s < EARLY_N ? EARLY_V : (s >= WPF0 ? (s == NSEG - 2 ? LAST_V : LATE_V) : 0))) | 1u;
    if (__builtin_expect(hist < 2u || aprev == 0u || d < 1 || d + SCH_VMIN > (int)(acur - aprev), 0)) {
      if (s == 0) {
        for (int it = 0; it < 40 && !wave_all(min4(tsv) != 0u); ++it) {
          __builtin_amdgcn_s_sleep(ST_ALL);
          asm volatile("" ::: "memory");
          load(rs, lane);
        }
      }
      const unsigned now = now32(), L = last(now);
      if (acur == 0u) { D8 = 8 * SCH_D0; hist = 0u; }
      const bool late = acur == 0u || (int)(L + SCH_VMIN - acur) > 0;
      if (late) { if (b == 0) FC(3); if (acur) D8 += 8 * (hist ? SCH_UP : 3 * SCH_UP); }
      else if (aprev) { if (hist < 2u) D8 = 8 * imin(imax((int)(L - aprev), 1), 4000); hist = umin(hist + 1u, 2u); }
      const unsigned base = acur == 0u ? L + SCH_FIRST : (late ? L + SCH_V + ((int)(L + SCH_VMIN2 - acur) <= 0 ? SCH_PEN2 : SCH_PEN) : acur);
      anext = (base + (unsigned)((D8 + 4) >> 3) + SCH_V + (s == 1 ? EARLY_V1 : (s < EARLY_N ? EARLY_V : 0))) | 1u;
      if (s == 0) anext = (L + ST_X) | 1u;
      if ((int)(now - L) > 50000) anext = 0u;
      if (late) acur = 0u;
      Dn = D8;
    }
    if (__builtin_expect(b == 0, 0))
      __builtin_amdgcn_raw_buffer_store_b128((u32x4){anext, acur, (unsigned)Dn, hist}, rs, lane < 2 ? lane * 8192 : 0x7ffffff0, (unsigned)OFF_ACC + (unsigned)(s % SR) * (unsigned)ACC_STEP + ACC_TS + 128u, 16);
    LOGV(0, anext); LOGV(1, acur == 0u); LOGV(2, (Dn + 4) >> 3); LOGV(3, d); LOGV(4, acur - aprev - d); LOGV(5, aprev);
    return anext;
  }
};

struct TokTask {
  u32x4 y[2]; u32x2 ym; u32x4 res;
  unsigned l0, l1, lm, s0, s1;
  static __device__ __forceinline__ bool ok(int sl) { return sl >= 0 && sl < NSEG; }
  __device__ __forceinline__ void init(int h, int j, int lane) {
    const int c = lane & 15, g = lane >> 4;
    l0 = ts_tok(h, g) + (unsigned)(j * 256 + c * 16); l1 = l0 + 16384u;
    lm = ts_mem(h, g + 4 * (c >> 3)) + (unsigned)(j * 64 + (c & 7) * 8);
    res = (u32x4){0u, 0u, 0u, 0u};
    const int qi = 4 * j + (c & 3);
    s0 = g < 2 ? pt_tok(h) + (unsigned)((qi >> 4) * 1024 + ((c >> 2) * 16 + (qi & 15)) * 16 + g * 8) : 0x7ffffff0u;
    s1 = lane < 8 ? pt_mem(h) + (unsigned)((qi >> 4) * 256 + ((c >> 2) * 16 + (qi & 15)) * 8) : 0x7ffffff0u;
    PIN_V(l0); PIN_V(l1); PIN_V(lm); PIN_V(s0); PIN_V(s1);
  }
  __device__ __forceinline__ void load(const __amdgpu_buffer_rsrc_t rs, unsigned o) {
    y[0] = SC1_LOADS(l0, o); y[1] = SC1_LOADS(l1, o);
    ym = __builtin_amdgcn_raw_buffer_load_b64(rs, lm, o, 16);
  }
  __device__ __forceinline__ bool ready() { return wave_all(umin(umin(min4(y[0]), min4(y[1])), umin(ym[0], ym[1])) != 0u); }
  __device__ __forceinline__ void run(int j, int lane, const bf16x8 SelLo) {
    const int c = lane & 15, g = lane >> 4;
    const f32x4 Z4 = {0.f, 0.f, 0.f, 0.f};
    const f32x4 d1 = mfma32(SelLo, as8(y[1]), mfma32(SelLo, as8(y[0]), Z4));
    const f32x4 d0 = mfma32(SelLo, mk8(ym[0], ym[1], 0u, 0u), Z4);
    const int qi = 4 * j + (c & 3), k0 = 16 * g + 4 * (c >> 2);
    float t[4], mv[4];
    float mx = -INFINITY;
#pragma unroll
    for (int r = 0; r < 4; ++r) {
      t[r] = (g < 2 && k0 + r <= qi) ? d1[r] : -INFINITY;
      const float mt = d0[r] + dppf<DPP_ROR8>(d0[r]);
      mv[r] = lane < 8 ? mt : -INFINITY;
      mx = fmx(mx, fmx(t[r], mv[r]));
    }
    mx = fmx(mx, dppf<DPP_ROR8>(mx)); mx = fmx(mx, dppf<DPP_ROR4>(mx));
    mx = x16mx(mx);
    float sum = 0.f;
#pragma unroll
    for (int r = 0; r < 4; ++r) {
      t[r] = __builtin_amdgcn_exp2f(t[r] - mx); mv[r] = __builtin_amdgcn_exp2f(mv[r] - mx);
      sum += t[r] + mv[r];
    }
    sum += dppf<DPP_ROR8>(sum); sum += dppf<DPP_ROR4>(sum);
    sum = x16sum(sum);
    const float rs_ = __builtin_amdgcn_rcpf(sum);
    res = (u32x4){pack2n(t[0] * rs_, t[1] * rs_), pack2n(t[2] * rs_, t[3] * rs_), pack2n(mv[0] * rs_, mv[1] * rs_), pack2n(mv[2] * rs_, mv[3] * rs_)};
    PIN_V(res);
  }
  __device__ __forceinline__ void store(const __amdgpu_buffer_rsrc_t rs, unsigned o, unsigned no = 0u) {
    SC1_STORE8S(((u32x2){res[0], res[1]}), s0 | no, o);
    SC1_STORE8S(((u32x2){res[2], res[3]}), s1 | no, o);
  }
};

constexpr int L_DRN = L_VOX + (NSEG % NV) * 5120;
__device__ __forceinline__ void drain_pre(char* lds, int b, int lane) {
  const int gp = (b & 7) * 2 + (b >> 7), m = (b >> 3) & 15;
  TokTask tk; PvTask2 pv; OutTask ot;
  tk.init(2 * gp + (m >> 3), m & 7, lane); pv.init(gp, lane); ot.init(b, lane);
  u32x4* p = (u32x4*)(lds + L_DRN) + lane;
  p[0] = (u32x4){tk.l0, tk.lm, tk.s0, tk.s1}; p[64] = (u32x4){pv.lo, pv.lm, pv.vr, pv.ma}; p[128] = (u32x4){pv.mz, pv.so, ot.t5f, ot.t5o};
  p[192] = __builtin_bit_cast(u32x4, ot.sel);
}
__device__ __forceinline__ void qkv_pre(unsigned (&vo)[2][2], int b, int w, int lane) {
  const int cb = qkv_col(b), c = lane & 15, g = lane >> 4, t0 = w & 1, col0 = (cb % 5) * 128 + w * 32;
  const bool SW = cb < 10;
#pragma unroll
  for (int i = 0; i < 2; ++i)
#pragma unroll
    for (int tt = 0; tt < 2; ++tt) {
      const int tq = tt ^ t0, tk = tq * 16 + c, cc = col0 + i * 16 + (SW ? 4 * g : c), hd = cc / BD, e = cc - hd * BD;
      const int kv = (int)(OFF_KV - OFF_QB) + hd * 64 * KVT;
      vo[i][tt] = (unsigned)(!SW ? kv + 1280 + tq * 672 + ((g >> 1) * 21 + e) * 16 + (g & 1) * 8
                                 : (cb < 5 ? (hd * S_ + tk) * 2 * QST + e * 2 : kv + (e < 16 ? (tk + 32 * (e >> 3)) * 16 + (e & 7) * 2 : 1024 + tk * 8)));
      PIN_V(vo[i][tt]);
    }
}

template <int w>
__device__ __forceinline__ void token_waves(const RecArgs& a, char* lds, const __amdgpu_buffer_rsrc_t rs, unsigned (&vo)[2][2]) {
  const int lane0 = __builtin_amdgcn_workitem_id_x() & 63, b = __builtin_amdgcn_workgroup_id_x(), wid = w + 4; (void)wid;
  const int gp = (b & 7) * 2 + (b >> 7), m = (b >> 3) & 15, h0 = 2 * gp, h = h0 + (m >> 3), j = m & 7;
  float* const misc = (float*)(lds + L_MISC);
  const f32x4 Z4 = {0.f, 0.f, 0.f, 0.f};
  const unsigned lds0 = (unsigned)(uintptr_t)(LAS char*)lds;
  unsigned* const cnt = (unsigned*)(misc + 6);
  const float* const xnrm = (const float*)(a.ws + OFF_XNRM);
  bf16x16 W[2][10];
  auto btile = [&](int i) { return w == 1 ? 0 : (w == 2 ? 1 + i : 3 + i); };
  if (w == 1) code_touch(b, lane0, lds0);
  Place pl;
  unsigned phi[2][2], ph[2][2][2];
#pragma unroll
  for (int k = 0; k < 8; ++k) {
    const int bt = btile(k >> 2), hw = h0 + (bt == 2 ? k & 1 : bt / 3), T = bt == 2 ? 32 + (m >> 1) : (bt % 3) * 16 + m;
    ph[k >> 2][(k >> 1) & 1][k & 1] = ((const __attribute__((address_space(4))) unsigned*)(a.ws + OFF_PHI))[(hw >> 2) | (hw & 3) << 3 | (k & 2) << 4 | (T / 10) << 6];
  }
  __builtin_amdgcn_sched_barrier(0);
  if (w == 3) pl.load(a.ws, lane0);
  if (a.layer) {
    if (w != 0 && FR_HOLD) wait_until(now32() + FR_HOLD);
    if (w != 0) img_fetch<20>(a, lds0, h, j, w - 1, lane0);
  } else {
    if (w == 0) img_fetch<12>(a, lds0, h, j, 4, lane0);
#pragma unroll
    for (int i = 0; i < 10; ++i) x_fetch(x_src(a, 0, b), lds0, 0, 10 * w + i, lane0);
  }
  const __amdgpu_buffer_rsrc_t rwb = __builtin_amdgcn_make_buffer_rsrc(a.ws + OFF_WB, 0, (int)WB_BYTES, 0x00020000);
#pragma unroll
  for (int k = 0; k < 4; ++k) phi[k >> 1][k & 1] = (((lane0 & 8) ? ph[k >> 1][k & 1][1] : ph[k >> 1][k & 1][0]) & 8192u) | (unsigned)(lane0 * 16);
#pragma unroll
  for (int i = 0; i < 2; ++i)
#pragma unroll
    for (int p = 0; p < 10; ++p)
      if (w >= 2 || (w == 1 && i == 1)) {
        const unsigned wo = wb_off((b * 5 + btile(i)) * 20 + 2 * p), wv = phi[i][p / 5] ^ (wo & 8192u);
        if (FR_PACE && a.layer) asm volatile("s_waitcnt vmcnt(%0)" :: "n"(FR_PACE) : "memory");
        W[i][p] = cat16(as8(__builtin_amdgcn_raw_buffer_load_b128(rwb, wv, wo & ~8192u, 0)), as8(__builtin_amdgcn_raw_buffer_load_b128(rwb, wv + 1024, wo & ~8192u, 0)));
      }
  f32x4 bn = Z4;
  if (w == 0) {
    bn = *(const f32x4*)(a.ws + OFF_BNRM + lane0 * 16);
    if (!a.layer) asm volatile("s_waitcnt vmcnt(0)" ::: "memory");
  } else asm volatile("s_waitcnt vmcnt(%0)" :: "n"(w == 1 ? 20 : 40) : "memory");
  unsigned ck = 0u;
  if (w == 3) { ck = pl.get(b, lane0); *(unsigned*)(misc + 11) = ck; }
  BAR();
#pragma unroll
  for (int p = 0; p < 10; ++p) {
    if (w < 2) W[0][p] = cat16(wqk_frag(lds, 2 * w, 2 * p, lane0), wqk_frag(lds, 2 * w, 2 * p + 1, lane0));
    if (w == 0) W[1][p] = cat16(wqk_frag(lds, 1, 2 * p, lane0), wqk_frag(lds, 1, 2 * p + 1, lane0));
  }
  float cb2 = 0.f;
  if (w == 0) {
    float n = __builtin_amdgcn_sqrtf(bn[0] + bn[1]) + __builtin_amdgcn_sqrtf(bn[2] + bn[3]);
    n += dppf<DPP_XOR1>(n); n += dppf<DPP_XOR2>(n); n += dppf<DPP_ROR4>(n); n += dppf<DPP_ROR8>(n);
    n = x32mx(x16mx(n));
    cb2 = u2f(__builtin_amdgcn_readfirstlane(f2u(1.15f * n * n)));
  }
  const float cm2 = u2f((f2u(cb2) & 0x7fffffu) | 0x40000000u);
  const int ek = imax((int)((f2u(cb2) >> 23) & 0xffu), 1) - 252;
  if (w >= 2) {
#pragma unroll
    for (int i = 0; i < 4; ++i) *(unsigned*)(lds + L_KX + (w - 2) * 4096 + i * 1024 + lane0 * 16 + 12) = 0u;
  }
  OutTask ot;
  const int zl = lane0 - 16;
  const bool zon = w == 3 && zl >= 0 && (zl < 40 || b < ACC_NB + 17);
  unsigned zo = zon ? zero_base(b, h, j, zl) + (zl < 32 ? sct_slot(ck, 0u) : (zl < 40 ? scm_slot(ck, 0u) : 0u)) : 0x7ffffff0u;
  unsigned zm = zon ? (zl < 32 ? SCT_STEP + 1u : (zl < 40 ? SCM_STEP : (unsigned)ACC_STEP)) : 0u;
  const unsigned zx = ((ck ^ (ck >> 4)) & 7u) * 4096u;
  if (w == 3) { PIN_V(zo); PIN_V(zm); }
  auto st_out = [&](int s, int lane, auto go) {
    if (w < 2) return;
    if (ST_E) lds_spin<L_MISC + 112, 1>(lds0, (unsigned)imax(s, 0)); else go();
    const int sl = s - 2 - LAG4;
    const char* sp = lds + L_STG + 2048 + (w & 1) * 1280;
    u32x4 v0 = *(const u32x4*)(sp + lane * 16), v1 = *(const u32x4*)(sp + 1024 + (lane & 15) * 16);
    const u32x4 z = {0u, 0u, 0u, 0u};
    unsigned oo = (sl < 0 ? 0x7ff00000u : op_slot(sl) + (unsigned)((m * 32 + (w & 1) * 16 + gp) * 1280)) + (unsigned)(lane * 16), za = 0x7ffffff0u;
    if (w == 3) {
      const unsigned zs = (unsigned)(s + 2) % SR;
      za = (zo + (s < 0 ? 1024u : zs) * (zm & ~1u)) ^ ((zm & 1u) * (zs & 4u ? zx : 0u));
#pragma unroll
      for (int i = 0; i < 4; ++i) { v1[i] = lane < 16 ? v1[i] : z[i]; PIN_V(v1[i]); }
    }
    za = lane < 16 ? oo + 1024 : za;
    PIN_V(za); PIN_V(oo); PIN_V(v0); PIN_V(v1);
    __builtin_amdgcn_sched_barrier(0);
    if (ST_E) go();
    SC1_STORE(v0, oo);
    SC1_STORE(v1, za);
    __builtin_amdgcn_sched_barrier(0);
    if (w == 2) ot.store(a, s - 2 - LAG5, b, lane);
    else {
      const unsigned xo_ = (unsigned)((s - 8) * 1024 + lane * 16);
      if (__builtin_expect((unsigned)(s - 8) < 20u, 0)) SC1_STORE(z, xo_ < X_BYTES / 256 ? (unsigned)(OFF_X + b * (X_BYTES / 256)) + xo_ : 0x7ffffff0u);
    }
  };
  STAMP_DECL(); ARRIVE_DECL();
  auto mma_seg = [&](int seg, int sv, int lane, auto pause) {
    const int c = lane & 15, g = lane >> 4;
    const char* xp = lds + L_XN + (seg & 1) * 40960 + lane * 16;
    f32x4 acc[2][2] = {{Z4, Z4}, {Z4, Z4}};
    bf16x8 x[4][2];
#pragma unroll
    for (int p = 0; p < 3; ++p) { x[p][0] = *(const bf16x8*)(xp + p * 1024); x[p][1] = *(const bf16x8*)(xp + (20 + p) * 1024); }
    unsigned eo[2];
#pragma unroll
    for (int ks = 0; ks < 20; ++ks) {
      if (ks == (w == 0 ? KP0 : (w == 1 ? KP1 : KP2))) { pause(); __builtin_amdgcn_sched_barrier(0); }
      if (ks == 17) {
#pragma unroll
        for (int i = 0; i < 2; ++i) {
          eo[i] = (unsigned)(w == 0 ? (i ? L_KX : L_QX) + (seg % NKQ) * 2048 + lane * 16
                                    : (w == 1 && i == 0 ? ((g >> 1) ? L_KX : L_QX) + (seg % NKQ) * 2048 + (2 * (g & 1) * 16 + c) * 16 + 8 : L_VOX + sv * 5120 + btile(i) * 1024 + lane * 16));
          if (i == 0 || w == 1) PIN_V(eo[i]);
        }
        if (w == 0) eo[1] = eo[0] + (L_KX - L_QX);
        if (w >= 2) eo[1] = eo[0] + 1024;
      }
      __builtin_amdgcn_sched_barrier(0);
#pragma unroll
      for (int q = 0; q < 4; ++q) {
        const int tt = 1 - (q >> 1), i = q & 1;
        const bool isqk = w == 0 || (w == 1 && i == 0);
        const bf16x8 wf = half8(W[i][ks >> 1], ks & 1);
        acc[i][tt] = isqk ? mfma32(wf, x[ks & 3][tt], acc[i][tt]) : mfma32(x[ks & 3][tt], wf, acc[i][tt]);
        __builtin_amdgcn_sched_barrier(0);
        if (q < 2 && ks + 3 < 20) x[(ks + 3) & 3][q] = *(const bf16x8*)(xp + (q * 20 + ks + 3) * 1024);
        __builtin_amdgcn_sched_barrier(0);
      }
    }
#pragma unroll
    for (int i = 0; i < 2; ++i) {
      if (w + i == 1) { const float ks_ = (w == 0 || (g >> 1)) ? KEXP : 1.0f; acc[i][0] *= ks_; acc[i][1] *= ks_; }
      const u32x4 v = {pack2(acc[i][0][0], acc[i][0][1]), pack2(acc[i][0][2], acc[i][0][3]), pack2(acc[i][1][0], acc[i][1][1]), pack2(acc[i][1][2], acc[i][1][3])};
      char* p = lds + eo[i];
      if (w == 0) { *(u32x2*)(p) = (u32x2){v[0], v[1]}; *(u32x2*)(p + 1024) = (u32x2){v[2], v[3]}; }
      else if (w == 1 && i == 0) { *(unsigned*)(p) = v[0]; *(unsigned*)(p + 256) = v[1]; *(unsigned*)(p + 1024) = v[2]; *(unsigned*)(p + 1280) = v[3]; }
      else *(u32x4*)p = v;
    }
  };
  unsigned vp, mp, ip;
  int sidx, ei = 127;
  float inv;
  f32x4 acc[2];
  auto vo_pre = [&](int s, int sv, int lane) {
    vp = (unsigned)(w == 0 ? L_MISC + (s & 3) * 4 : L_VOM + sv * 1280 + (btile(0) * 32 + (w == 1 ? lane & 31 : lane)) * 8);
    mp = (unsigned)(L_BM + lane * 16);
    PIN_V(vp); PIN_V(mp);
    if (w == 0) { ip = (unsigned)(L_MISC + 16 + ((s + 1) & 1) * 4); PIN_V(ip); }
    else {
      inv = misc[4 + (s & 1)];
      sidx = (lane & 8) ? 0xEEEE : 0x4444;
      acc[0] = acc[1] = Z4;
      PIN_V(sidx); PIN_V(acc[1]);
      if (w >= 2) { PIN_V(acc[0]); PIN_V(inv); }
    }
    __builtin_amdgcn_sched_barrier(0);
  };
  auto vo_mem = [&](int s, auto last) {
    __builtin_amdgcn_s_setprio(2);
    asm volatile(".rept %0\n\ts_nop 3\n\t.endr" :: "n"(w == 0 ? VO_A0 : (w == 1 ? VO_A1 : VO_A2)) : "memory");
    float xmax = 0.f;
    if (w == 0) xmax = *(const float*)(lds + vp);
    constexpr int NX = w == 2 ? VO_N2 : (w == 3 ? VO_N3 : 10);
    bf16x8 x[10];
#pragma unroll
    for (int p = 0; p < NX; ++p) x[p] = *(const bf16x8*)(lds + mp + p * 1024);
    __builtin_amdgcn_sched_barrier(0);
    if (w < 2) asm volatile(".rept %0\n\ts_nop 3\n\t.endr" :: "n"(w == 0 ? VO_B0 : VO_B1) : "memory");
    if (VO_W >= 0) asm volatile("s_waitcnt lgkmcnt(%0)" :: "n"(VO_W < 0 ? 0 : VO_W) : "memory");
    STAMP(5);
    __builtin_amdgcn_sched_barrier(0);
    int ex = 0;
#pragma unroll
    for (int p = 0; p < 10; ++p) {
      if (w == 0) acc[0] = mfma32(x[p], x[p], p ? acc[0] : Z4);
      if (w == 0 && p == 9) { ex = imax((int)((__builtin_amdgcn_readfirstlane(f2u(__builtin_amdgcn_sqrtf(cb2 * xmax))) >> 23) & 0xffu) + 1, 32); PIN_S(ex); }
#pragma unroll
      for (int i = (w == 1); i < 2 && w != 0; ++i) acc[i] = smfma64(x[p], W[i][p], acc[i], sidx);
      __builtin_amdgcn_sched_barrier(0);
      if (p + NX < 10) x[p + NX] = *(const bf16x8*)(lds + mp + (p + NX) * 1024);
      __builtin_amdgcn_sched_barrier(0);
    }
    STAMP(7);
    if (w != 0) {
      float v[4];
#pragma unroll
      for (int r = 0; r < 4; ++r) {
        const auto sw = __builtin_amdgcn_permlane32_swap(f2u(acc[w == 1][r]), f2u(acc[1][r]), false, false);
        v[r] = (u2f(sw[0]) + u2f(sw[1])) * inv;
      }
      *(u32x2*)(lds + vp) = (u32x2){pack2(v[0], v[1]), pack2(v[2], v[3])};
    } else {
      const int t = (int)f2u(fmx(fmx(acc[0][0], acc[0][1]), fmx(acc[0][2], acc[0][3])) * cm2);
      const int et = wave_imax(t) >> 23, em = ((et + ek) >> 1) + ei;
      int e = imax(et == 255 ? 256 : em, ex);
      PIN_S(e);
      e = imin(e, 256);
      ei = e - 31;
      *(unsigned*)(lds + ip) = (unsigned)ei << 23;
      const u32x3 sr = {(unsigned)(285 - e) << 23, (unsigned)(e - 87) < 79u ? REC_RNG << 23 : 0u, (unsigned)(s + 1)};
      asm volatile("ds_write_b96 %0, %1 offset:%2" :: "v"(lds0), "v"(sr), "n"(L_MISC + 32) : "memory");
    }
    if (last == 0 && w >= 2) asm volatile("s_waitcnt vmcnt(%0)" :: "n"(w == SCH_W ? 2 : 0) : "memory");
    if (w != 0) lds_inc<L_MISC + 28>(lds0);
    STAMP(8);
    if (last) __builtin_amdgcn_s_setprio(0);
    if (last && w == 2) { VM_DRAIN(); lds_inc<L_MISC + 28>(lds0); }
  };
  unsigned* const cntq = (unsigned*)(misc + 12);
  unsigned* const cntt = (unsigned*)(misc + 26 + (w & 1));
  unsigned* const cntp = (unsigned*)(misc + 23);
  unsigned* const cntx2 = (unsigned*)(misc + 28);
  float xn4 = 0.f;
  auto wpf = [&](int s, int lane) {
    constexpr int PER = 81920 / QR / WPF_N;
    static_assert(PER % WPF_S == 0 && PER / WPF_S <= 64, "prefetch");
    touch(a.ws + OFF_WQKVBT + (size_t)imin(qkv_col(b), 14) * 163840 + (qkv_row(b) % (2 * QR)) * (81920 / QR), (s - WPF0) * PER + imin(lane, PER / WPF_S - 1) * WPF_S, lds0);
  };
  auto t5_get = [&](int s, int lane) {
    ot.load(a, rs, s - 1 - LAG5, b);
    xn4 = u2f(__hip_atomic_load((const GAS unsigned*)xnrm + (imax(s + 1, 0) * 32 + (lane & 31)) * 4, RLX_AGENT));
    __builtin_amdgcn_sched_barrier(0);
  };
  Sched sch;
  auto late = [&](int s, int lane) {
    if (__builtin_expect(s < 0, 0)) return;
    if (w == SCH_W && SCH_LATE && __builtin_expect(s >= 0, 1)) *(u32x2*)(misc + 24) = (u32x2){sch.run(a, rs, s, b, lane), (unsigned)(s + 1)};
    if ((w == 2 && F6) || (w == 0 && F4) || (w == 1 && F5) || (w == 3 && F7)) { lds_spin<L_MISC + 48, 1>(lds0, 2u * (unsigned)imax(s + 1, 0)); if (FB_SL && w != 2) __builtin_amdgcn_s_sleep(FB_SL); }
    if (w >= 2) { x_fetch_n<w == 2 ? F6 : F7>(x_src(a, s + 3, b), lds0, s + 3, F4 + F5 + 2 * F67 + (w == 3 ? F6 : 0), lane * 16); return; }
    unsigned sd = 0u;
    if ((w == 0 && XPF == 3) || XPF == 4 || (w == 1 && XPF == 5)) stouch<10>(a.ws + OFF_XN + (size_t)imin(s + 4, NSEG - 1) * (s < 1 ? 0 : 40960) + (b >> 3) * 1280 + w * 64, sd);
    x_fetch_n<w == 0 ? F4 : F5>(x_src(a, s + 3, b), lds0, s + 3, w == 0 ? 0 : F4, lane * 16);
    if (WPF_W == 0 && w == 0 && __builtin_expect(s >= WPF0 && s < WPF0 + WPF_N, 0)) wpf(s, lane);
    if (w == 1 && __builtin_expect(s == CPF_S, 0)) code_touch(b, lane, lds0);
    lds_spin<L_MISC + 104 + 4 * (w & 1), PS_SL>(lds0, (unsigned)imax(s + 1, 0));
    asm volatile("" :: "s"(sd));
  };
  auto xn_put = [&](int s) {
    float xn2 = xn4;
    xn2 = fmx(xn2, dppf<DPP_XOR1>(xn2)); xn2 = fmx(xn2, dppf<DPP_XOR2>(xn2));
    xn2 = fmx(xn2, dppf<DPP_ROR4>(xn2)); xn2 = fmx(xn2, dppf<DPP_ROR8>(xn2));
    misc[s & 3] = x16mx(xn2);
  };
  if (w == 2) { ot.init(b, lane0); ot.out = (u32x2){0u, 0u}; ot.xo = ot.out; ot.y[0] = ot.y[1] = ot.y[2] = ot.y[3] = (u32x4){0u, 0u, 0u, 0u}; }
  VM_DRAIN();
  lds_arrive(cnt, lane0);
  lds_wait(cnt, 8u);
  sch.tsv = sch.rec = (u32x4){0u, 0u, 0u, 0u};
  sch.side = __builtin_amdgcn_readfirstlane(*(const unsigned*)(misc + 11)) & 8192u;
  sch.o = 0u;
  int sv = NV - 1;
  CODE_PAD((PADT >> (4 * w)) & 15);
#pragma unroll 1
  for (int s = -1; s < NSEG - 1; ++s) {
    const int sv1 = sv == NV - 1 ? 0 : sv + 1;
    FRESH_LANE();
    STAMP(0);
    mma_seg(s + 1, sv1, lane, [&]() { STAMP(1); late(s - 1, lane); if (w == SCH_W) sch.pre(s); STAMP(9); });
    STAMP(2);
    __builtin_amdgcn_sched_barrier(0);
    VM_DRAIN();
    if (w == 2) { ot.run(lane); xn_put(s); }
    STAMP(10);
    st_out(s, lane, [&]() { lds_spin<L_MISC + 92, W1_SL>(lds0, 4u * (unsigned)imax(s, 0)); });
    if (w == 2) t5_get(s, lane);
    __builtin_amdgcn_sched_barrier(0);
    if (w >= 2) vo_pre(s, sv, lane);
    STAMP(3);
    BAR();
    STAMP(4);
    if (__builtin_expect(s >= 0, 1)) {
      if (w == SCH_W) { sch.load(rs, lane0); __builtin_amdgcn_sched_barrier(0); }
      if (w < 2) vo_pre(s, sv, lane);
      vo_mem(s, std::integral_constant<int, 0>{});
      STAMP(6);
      if (w == SCH_W && !SCH_LATE) {
        __builtin_amdgcn_s_setprio(SCH_P);
        *(u32x2*)(misc + 24) = (u32x2){sch.run(a, rs, s, b, lane0), (unsigned)(s + 1)};
      }
      STAMP(11);
    }
    __builtin_amdgcn_s_setprio(MMA_P);
    __builtin_amdgcn_sched_barrier(0);
    sv = sv1;
  }
  __builtin_amdgcn_s_setprio(0);
  STAMP_OUT();
  late(NSEG - 2, lane0);
  if (w == 2) ot.run(lane0);
  { FRESH_LANE(); if (w == 0) drain_pre(lds, b, lane); qkv_pre(vo, b, w, lane); }
  st_out(NSEG - 1, lane0, [&]() { lds_spin<L_MISC + 92, 1>(lds0, 4u * (NSEG - 1)); });
  if (w != 0) vo_pre(NSEG - 1, (NSEG - 1) % NV, lane0);
  BAR();
  if (w != 0) vo_mem(NSEG - 1, std::integral_constant<int, 1>{});
}

__device__ __forceinline__ void qkv_tail(const RecArgs& a, char* lds, const int w, const unsigned (&vo)[2][2]) {
  const int lane0 = __builtin_amdgcn_workitem_id_x() & 63, b = __builtin_amdgcn_workgroup_id_x();
  const int r = qkv_row(b), cb = qkv_col(b);
  if (cb >= 15) return;
  const unsigned lds0 = (unsigned)(uintptr_t)(LAS char*)lds;
  unsigned* const stage = (unsigned*)(lds + L_MISC) + 17;
  unsigned* const cnty = stage + 1;
  const f32x4 Z4 = {0.f, 0.f, 0.f, 0.f};
  auto fetch = [&](int i) {
    x_fetch_n<10>(x_src(a, NSEG + i, b), lds0, i, 10 * w, lane0 * 16);
  };
  unsigned want = 0;
  auto sync = [&]() { VM_DRAIN(); want += 4u; lds_inc<L_MISC + 72>(lds0); while (__hip_atomic_load(cnty, __ATOMIC_RELAXED, __HIP_MEMORY_SCOPE_WORKGROUP) < want) { } asm volatile("" ::: "memory"); };
  LOGQ(0);
  bf16x8 W[2][20];
  const __amdgpu_buffer_rsrc_t rw = __builtin_amdgcn_make_buffer_rsrc(a.ws + OFF_WQKVBT + (size_t)(cb * 8 + 2 * w) * 20480, 0, 40960, 0x00020000);
  auto w_get = [&](int ks) {
#pragma unroll
    for (int i = 0; i < 2; ++i) W[i][ks] = as8(__builtin_amdgcn_raw_buffer_load_b128(rw, lane0 * 16 + (ks & 3) * 1024, (i * 20 + (ks & ~3)) * 1024, 0));
    __builtin_amdgcn_sched_barrier(0);
  };
#pragma unroll
  for (int ks = 0; ks < QW; ++ks) w_get(ks);
  bf16x8 ones = mk8(0x3f803f80u, 0x3f803f80u, 0x3f803f80u, 0x3f803f80u);
  PIN_V(ones);
  const __amdgpu_buffer_rsrc_t ro = __builtin_amdgcn_make_buffer_rsrc(a.ws + OFF_QB, 0, (int)(OFF_OB - OFF_QB), 0x00020000);
  const int t0 = w & 1;
  auto body = [&](auto SW, auto SQ) {
    const float qs = cb < 5 ? 0.22360679774997896f * 1.4426950408889634f : 1.0f;
    f32x4 cs[2] = {Z4, Z4};
    LOGQ(1);
    f32x4 acc[2][2], st;
    auto mma = [&](int it, auto FIRST) {
      const char* xp0 = lds + L_XN + (it & 1) * 40960 + t0 * 20480 + lane0 * 16;
      const char* xp1 = lds + L_XN + (it & 1) * 40960 + (1 - t0) * 20480 + lane0 * 16;
      acc[0][0] = acc[0][1] = acc[1][0] = acc[1][1] = st = Z4;
      bf16x8 x[4][2];
#pragma unroll
      for (int p = 0; p < 3; ++p) { x[p][0] = *(const bf16x8*)(xp0 + p * 1024); x[p][1] = *(const bf16x8*)(xp1 + p * 1024); }
      __builtin_amdgcn_sched_barrier(0);
#pragma unroll
      for (int ks = 0; ks < 20; ++ks) {
        if (FIRST && ks + QW < 20) w_get(ks + QW);
#pragma unroll
        for (int q = 0; q < 4; ++q) {
          const int tt = 1 - (q >> 1), i = 1 - (q & 1);
          acc[i][tt] = SW ? mfma32(W[i][ks], x[ks & 3][tt], acc[i][tt]) : mfma32(x[ks & 3][tt], W[i][ks], acc[i][tt]);
          __builtin_amdgcn_sched_barrier(0);
          if (q < 2 && ks + 3 < 20) x[(ks + 3) & 3][q] = *(const bf16x8*)((q ? xp1 : xp0) + (ks + 3) * 1024);
          __builtin_amdgcn_sched_barrier(0);
        }
        st = mfma32(SQ ? x[ks & 3][0] : ones, x[ks & 3][0], st);
        if (FIRST) {
#pragma unroll
          for (int i = 0; i < 2; ++i) cs[i] = SW ? mfma32(W[i][ks], ones, cs[i]) : mfma32(ones, W[i][ks], cs[i]);
        }
        __builtin_amdgcn_sched_barrier(0);
      }
    };
    mma(0, std::integral_constant<bool, true>{});
#pragma unroll 1
    for (int it = 0; it < 4; ++it) {
      if (it) mma(it, std::integral_constant<bool, false>{});
      LOGQ(2 + 3 * it);
      FRESH_LANE();
      float* const sp = (float*)(lds + L_ST) + (it & 1) * 64;
      if (SQ) { if ((c >> 2) == g) sp[32 + t0 * 16 + c] = (c & 3) == 0 ? st[0] : ((c & 3) == 1 ? st[1] : ((c & 3) == 2 ? st[2] : st[3])); }
      else if (g == 0) sp[t0 * 16 + c] = st[0];
      sync();
      LOGQ(3 + 3 * it);
      if (it == 0) fetch(2);
      const bool late = r + 48 >= 55;
      if (it == 1 && !late) fetch(3);
      const unsigned so = (unsigned)((r + 16 * it) * (cb < 5 ? 64 * QST : KVT));
      auto stat = [&](float sm, float sq, float& mean, float& rstd) {
        mean = sm * (1.0f / DM);
        rstd = __builtin_amdgcn_rsqf(__builtin_fmaxf(sq * (1.0f / DM) - mean * mean, 0.f) + 1e-5f);
      };
#pragma unroll
      for (int tt = 0; tt < 2; ++tt) {
        const int tq = tt ^ t0;
        float mean[4], rstd[4];
        if (SW) {
          stat(sp[tq * 16 + c], sp[32 + tq * 16 + c], mean[0], rstd[0]);
          rstd[0] *= qs;
        } else {
          const f32x4 sm = *(const f32x4*)(sp + tq * 16 + 4 * g), sq = *(const f32x4*)(sp + 32 + tq * 16 + 4 * g);
#pragma unroll
          for (int k = 0; k < 4; ++k) stat(sm[k], sq[k], mean[k], rstd[k]);
        }
#pragma unroll
        for (int i = 0; i < 2; ++i) {
          float v[4];
#pragma unroll
          for (int k = 0; k < 4; ++k) v[k] = (acc[i][tt][k] - mean[SW ? 0 : k] * cs[i][SW ? k : 0]) * rstd[SW ? 0 : k];
          __builtin_amdgcn_raw_buffer_store_b64((u32x2){pack2(v[0], v[1]), pack2(v[2], v[3])}, ro, vo[i][tt], so, 0);
        }
      }
      LOGQ(4 + 3 * it);
      if (it == 2 && late) lds_spin<L_MISC + 68, 0>(lds0, 5u);
    }
  };
  typedef std::integral_constant<bool, false> F_; typedef std::integral_constant<bool, true> T_;
  if (cb < 10) { if (w >> 1) body(T_{}, T_{}); else body(T_{}, F_{}); }
  else { if (w >> 1) body(F_{}, T_{}); else body(F_{}, F_{}); }
}

__device__ __forceinline__ void t2_mem(const bf16x8 fr, const bf16x8 q0, const bf16x8 q1, const __amdgpu_buffer_rsrc_t rs, unsigned o, int h, int j, float inv, int lane) {
  const f32x4 Z4 = {0.f, 0.f, 0.f, 0.f};
  const f32x4 d0 = mfma32(fr, q0, Z4) * inv, d1 = mfma32(fr, q1, Z4) * inv;
  const unsigned a0 = pack2n(d0[0], d0[1]), a1 = pack2n(d0[2], d0[3]), b0 = pack2n(d1[0], d1[1]), b1 = pack2n(d1[2], d1[3]);
  const auto r0 = __builtin_amdgcn_permlane32_swap(a0, b0, false, false), r1 = __builtin_amdgcn_permlane32_swap(a1, b1, false, false);
  SC1_STORE8S(((u32x2){r0[1], r1[1]}), ts_mem(h, j) + (lane >> 5) * 256 + (lane & 12) * 16 + (lane & 16) * 2 + (lane & 3) * 8, o);
}

template <int w>
__device__ __forceinline__ void rec_waves(const RecArgs& a, char* lds, __amdgpu_buffer_rsrc_t rs, unsigned& tlast) {
  PIN_S(rs);
  const int lane = __builtin_amdgcn_workitem_id_x() & 63, b = __builtin_amdgcn_workgroup_id_x(), c = lane & 15, g = lane >> 4, wid = w; (void)wid;
  const int gp = (b & 7) * 2 + (b >> 7), m = (b >> 3) & 15, h0 = 2 * gp, h = h0 + (m >> 3), j = m & 7;
  float* const misc = (float*)(lds + L_MISC);
  const f32x4 Z4 = {0.f, 0.f, 0.f, 0.f};
  LOGP(0);
  __builtin_amdgcn_s_setprio(3);
  const unsigned lds0 = (unsigned)(uintptr_t)(LAS char*)lds;
  if (!a.layer) img_fetch<12>(a, lds0, h, j, w, lane);
  constexpr int NC = w < 2 ? 2 : 3;
  const int far = ((b >> 2) & 1) ^ 1;
  auto chunk = [&](int f) { return f == 0 ? 4 * far + w : (w == 2 && f == 1 ? 8 + far : (w == 3 && f == 2 ? 9 - far : 4 - 4 * far + w)); };
  bf16x16 Wqk[3][NC];
  u32x4 bm0[NC][2];
#pragma unroll
  for (int f = 0; f < 2 * NC; ++f) bm0[f >> 1][f & 1] = *(const u32x4*)(a.mem0 + (size_t)(c & 7) * DM + 64 * chunk(f >> 1) + 16 * g + 8 * (f & 1));
  if (!a.layer) asm volatile("s_waitcnt vmcnt(0)" ::: "memory");
  BAR();
  LOGP(9);
  const unsigned ck = __builtin_amdgcn_readfirstlane(*(const unsigned*)(misc + 11));
  const unsigned flip = (ck ^ (unsigned)(b << 11)) & 8192u;
#pragma unroll
  for (int t = 0; t < 3; ++t)
#pragma unroll
    for (int f = 0; f < NC; ++f) Wqk[t][f] = cat16(wqk_frag(lds, t, 2 * chunk(f), lane), wqk_frag(lds, t, 2 * chunk(f) + 1, lane));
  if (a.layer) {
    if (w == 0) {
      for (unsigned spins = 0; spins < SPIN_CAP; ++spins) {
        if (__builtin_amdgcn_raw_buffer_load_b32(rs, (b & 7) * 256, (unsigned)OFF_RDY, 16) >= 16u) break;
        __builtin_amdgcn_s_sleep(4);
        asm volatile("" ::: "memory");
      }
      lds_inc<L_MISC + 84>(lds0);
    }
    lds_wait((unsigned*)(misc + 21), 2u);
#pragma unroll
    for (int f = 0; f < 2 * NC; ++f) PIN_V(bm0[f >> 1][f & 1]);
#pragma unroll
    for (int i = 0; i < 10; ++i) x_fetch(x_src(a, 0, b), lds0, 0, 10 * w + i, lane);
  }
  const bf16x8 SelLo = sel_frag(c, 0), SelHi = sel_frag(c, 8);
  bf16x8 ONES = mk8(0x3f803f80u, 0x3f803f80u, 0x3f803f80u, 0x3f803f80u);
  if (w < 2) PIN_V(ONES);
  bool dead = false;
  float inv = 1.0f;
  unsigned* const cnt2 = (unsigned*)(misc + 7);
  unsigned l2off[2 * NC];
#pragma unroll
  for (int f = 0; f < 2 * NC; ++f) { l2off[f] = acc_off(16 * chunk(f >> 1) + 4 * g + 2 * (f & 1) + (c >> 3), c & 7) ^ flip; PIN_V(l2off[f]); }
  int sidx = (c >> 3) ? 0xEEEE : 0x4444;
  PIN_V(sidx);
  const unsigned p1off = (unsigned)((w == 0 ? (h & 2) * 1024 + j * 256 + (h & 1) * 64 + (g & 1) * 8 : (h & 3) * 32768 + j * 512 + (g >> 1) * 256 + (g & 1) * 64) + (c & 4) * 32 + (c & 3) * 16);
  unsigned p1v = (c < 8) && (w != 0 || g >= 2) ? p1off : 0x7ffffff0u, p1cur = 0x7ffffff0u;
  PIN_V(p1v); PIN_V(p1cur);
  const int hd = (c >> 2) & 1, mr = hd ? 3 - (c & 3) : (c & 3);
  const unsigned l1off = (unsigned)(((h0 + hd) & 3) * 32768 + g * 512 + (w & 1) * 128 + (c >> 3) * 64 + mr * 16);
  const unsigned l1offm = (unsigned)((h0 & 2) * 1024 + (g + 4 * (c >> 3)) * 256 + (w & 1) * 128 + hd * 64 + mr * 16);
  const int plane = (lane & ~6) | ((lane & 2) << 1) | ((lane & 4) >> 1);
  unsigned acco = c < 8 ? (acc_off(64 * far + m * 4 + g, 4 * (w & 1) + mr) + (unsigned)(hd * 8)) ^ flip : 0x7ffffff0u;
  unsigned acco2 = c >= 8 ? (acc_off(64 - 64 * far + m * 4 + g, 4 * (w & 1) + mr) + (unsigned)(hd * 8)) ^ flip
                          : (c < 4 ? (acc_off(128 + m * 2 + (g & 1), 4 * (w & 1) + mr) + (unsigned)((g >> 1) * 8)) ^ flip : 0x7ffffff0u);
  if (w < 2) { PIN_V(acco); PIN_V(acco2); }
  unsigned bmo[NC];
#pragma unroll
  for (int f = 0; f < NC; ++f) { bmo[f] = (unsigned)(chunk(f) * 1024 + lane * 16); PIN_V(bmo[f]); }
  bf16x8 bm[NC];
  auto bm_put = [&](int f) { *(bf16x8*)(lds + L_BM + bmo[f]) = bm[f]; __builtin_amdgcn_sched_barrier(0); };
  f32x4 acc[3] = {Z4, Z4, Z4};
  const int rt2 = ((c >> 3) * 8 + 4 * (g & 1)) * 28 + 16 + (c & 7);
  unsigned redo[3] = {(unsigned)(4 * g * 28 + c), (unsigned)(g < 2 ? (4 * g + 2) * 28 + c : rt2), (unsigned)(g < 2 ? (4 * g + 10) * 28 + c : rt2 + 56)};
#pragma unroll
  for (int k = 0; k < 3; ++k) { redo[k] = L_RED1 + w * 1792 + redo[k] * 4; PIN_V(redo[k]); }
  auto red_put = [&]() {
    auto hs = [&](float x, float y) { const auto sw = __builtin_amdgcn_permlane32_swap(f2u(x), f2u(y), false, false); return u2f(sw[0]) + u2f(sw[1]); };
#pragma unroll
    for (int k = 0; k < 3; ++k) {
#pragma unroll
      for (int i = 0; i < 2; ++i) *(float*)(lds + redo[k] + i * 112) = k == 0 ? hs(acc[0][i], acc[1][i]) : hs(acc[k - 1][2 + i], acc[2][2 * k - 2 + i]);
      __builtin_amdgcn_sched_barrier(0);
    }
  };
#pragma unroll
  for (int f = 0; f < NC; ++f) {
    const u32x4 u = bm0[f][0], v = bm0[f][1];
    bm[f] = c < 8 ? mk8(u[0], u[2], v[0], v[2]) : mk8(u[1], u[3], v[1], v[3]);
    bm_put(f);
#pragma unroll
    for (int t = 0; t < 3; ++t) acc[t] = smfma64(bm[f], Wqk[t][f], acc[t], sidx);
  }
  red_put();
  VM_DRAIN();
  lds_arrive((unsigned*)(misc + 6), lane);
  lds_wait((unsigned*)(misc + 6), 8u);
  x_fetch_n<10>(x_src(a, 1, b), lds0, 1, 10 * w, lane * 16);
  LOGP(1);
  STAMP_DECL(); ARRIVE_DECL();
  bf16x8 fr, qx[2];
  auto kq_get = [&](int seg) {
    const char* p = lds + (w == 1 ? L_KX : L_QX) + (seg % NKQ) * 2048 + lane * 16;
    if (w == 1 || w == 2) { qx[0] = *(const bf16x8*)p; qx[1] = *(const bf16x8*)(p + 1024); }
  };
  unsigned qo4 = (unsigned)(L_RED1 + (w == 1 ? (c & 7) * 112 + (c >> 3) * 3584 : c * 112) + g * 16), qo2 = qo4 + 64u - (unsigned)(g * 8), qo2b = qo2 + 3584u;
  if (w < 3) { PIN_V(qo4); PIN_V(qo2); if (w != 1) PIN_V(qo2b); }
  auto pub1 = [&](unsigned off, unsigned o1) {
    if (w == 0) fr = qk_frag(lds + qo4, lds + qo2, lds + qo2b);
    else {
      const f32x4 a0 = *(const f32x4*)(lds + qo4), a1 = *(const f32x4*)(lds + qo4 + 1792);
      const f32x2 b0 = *(const f32x2*)(lds + qo2), b1 = *(const f32x2*)(lds + qo2 + 1792);
      float v[6];
#pragma unroll
      for (int i = 0; i < 6; ++i) v[i] = i < 4 ? a0[i] + a1[i] : b0[i - 4] + b1[i - 4];
#pragma unroll
      for (int i = 0; i < 6; ++i) v[i] += dppf<DPP_ROR8>(v[i]);
      fr = mk8(pack2(v[0], v[1]), pack2(v[2], v[3]), pack2(v[4], v[5]), 0u);
    }
    const f32x4 sc = mfma32(w == 0 ? fr : qx[0], fr, Z4);
    if (w == 0) SC1_STORE8S(((u32x2){pack2n(sc[0], sc[1]), pack2n(sc[2], sc[3])}), off, o1);
    else {
      const f32x4 s2 = mfma32(qx[1], fr, Z4);
      __builtin_amdgcn_raw_buffer_store_b128((u32x4){pack2n(sc[0], sc[1]), pack2n(sc[2], sc[3]), pack2n(s2[0], s2[1]), pack2n(s2[2], s2[3])}, rs, off, o1, 16);
    }
  };
  unsigned o1 = sct_slot(ck, 0u), o1m = scm_slot(ck, 0u);
  unsigned t2o = ts_tok(h, j) + (unsigned)((c >> 2) * 256 + g * 64 + (c & 3) * 16);
  TokScores t2;
  TokTask tk;
  PvTask2 pv2;
  if (w == 2) tk.init(h, j, lane);
  if (w == 3) { PIN_V(t2o); pv2.init(gp, lane); }
  if (w == 0) pub1(p1v, o1m);
  VM_DRAIN();
  BAR();
  const unsigned tst = now32() | 1u;
  if (w == 3 && ST_X) __builtin_amdgcn_raw_buffer_store_b32(tst, rs, lane < 2 ? ts_piece(b >> 2) - ACC_TS + (b & 3) * 4 + lane * 8192 : 0x7ffffff0, (unsigned)(OFF_ACC + (SR - 1) * ACC_STEP) + ACC_TS, 16);
  LOGP(2);
  kq_get(0);
  if (w == 1) pub1(p1v, o1);
  x_fetch_n<10>(x_src(a, 2, b), lds0, 2, 10 * w, lane * 16);
  if (w == 3) { t2.get(lds, 0, lane); t2.run(0); t2.run(1); t2.store(rs, t2o, ts_slot(ck, 0)); }
  LOGP(3);
  unsigned o2 = (unsigned)OFF_ACC, o2p = 0u, tw = 0u;
  asm volatile("" : "+s"(o2p));
  unsigned tc0 = 0u, tstar = 0u;
  int dmin = 0, dmax = 0;
  int e1 = 100000;
  unsigned offs = 100u;
  unsigned o4 = 0u;
  unsigned o3, o3p;
  u32x16 P = {0u, 0u, 0u, 0u, 0u, 0u, 0u, 0u, 0u, 0u, 0u, 0u, 0u, 0u, 0u, 0u}, V = P;
  u32x8 V8 = {0u, 0u, 0u, 0u, 0u, 0u, 0u, 0u};
  if (w < 2) asm volatile("" : "+{v[208:223]}"(P), "+{v[184:191]}"(V8), "+{v[192:207]}"(V));
  unsigned tso = lane < 2 ? ts_piece(b >> 2) + (unsigned)((b & 3) * 4 + lane * 8192) : 0x7ffffff0u;
  if (w < 2) PIN_V(tso);
  float minf = g >= 2 ? -INFINITY : 0.f;
  if (w < 2) PIN_V(minf);
  int sv = NV - 1;
  bf16x8 vox[5];
  unsigned vma[3], need2;
  u32x4 slo, shi;
  float k1;
  const u16* xsrc;
  float invv = 1.0f;
  auto prep0 = [&](int s) {
    xsrc = x_src(a, s + 3, b);
    if (w == 2) { o3p = ts_slot(ck, (s + 1 - LAG3) & 3); o3 = ts_slot(ck, imax(s, 0)); o4 = pt_slot(ck, imax(s - LAG3, 0)); PIN_S(o3); }
    if (w == 3) { o3p = pt_slot(ck, (s - LAG4) & 3); o4 = ts_slot(ck, s + 1); }
    PIN_S(xsrc); PIN_S(o3p); PIN_S(o4);
    __builtin_amdgcn_sched_barrier(0);
  };
  auto prep = [&](int s) {
    o2 = (unsigned)OFF_ACC + (unsigned)(s % SR) * (unsigned)ACC_STEP;
    sv = sv == NV - 1 ? 0 : sv + 1;
    PIN_S(o2); PIN_S(sv);
    if (w < 2) {
      PIN_S(tc0);
      o1 = sct_slot(ck, s % SR); o1m = scm_slot(ck, s % SR);
      need2 = 4u * (unsigned)(s + 1) + 2u * ((unsigned)(s + 2) / NSEG);
      PIN_S(o1); PIN_S(o1m); PIN_S(need2);
      __builtin_amdgcn_sched_barrier(0);
      kq_get(s);
    }
    invv = misc[4 + (s & 1)];
  };
  auto prep2 = [&](int s) {
#pragma unroll
    for (int k = 0; k < 3; ++k) {
      const int t = k == 0 ? 2 : (k == 1 ? far : 1 - far);
      const char* p = lds + L_VOX + sv * 5120 + t * 1024 + (plane & 47) * 16 + (g & 1) * 8;
      auto rd = [&](const char* q) { const u32x2 a0 = *(const u32x2*)q, a1 = *(const u32x2*)(q + 256); return mk8(a0[0], a0[1], a1[0], a1[1]); };
      vox[k == 0 ? 0 : 2 * k - 1] = rd(p);
      if (k) vox[2 * k] = rd(p + 3072);
      vma[k] = lds0 + (unsigned)(L_VOM + sv * 1280 + t * 256 + (plane & 31) * 8);
      PIN_V(vma[k]);
    }
    __builtin_amdgcn_sched_barrier(0);
    inv = u2f(__builtin_amdgcn_readfirstlane(f2u(invv)));
    slo = __builtin_bit_cast(u32x4, SelLo); shi = __builtin_bit_cast(u32x4, SelHi);
#pragma unroll
    for (int i = 0; i < 4; ++i) { slo[i] = ((slo[i] >> 7) & 0x10001u) * (f2u(inv) >> 16); shi[i] = ((shi[i] >> 7) & 0x10001u) * (f2u(inv) >> 16); }
    if (F0A) { xsrc = x_src(a, s + 3, b); PIN_S(xsrc); }
    k1 = KEXP * inv;
    PIN_V(k1); PIN_V(slo); PIN_V(shi);
    asm volatile("" : "+v"(vox[0]), "+v"(vox[1]), "+v"(vox[2]), "+v"(vox[3]), "+v"(vox[4]));
    __builtin_amdgcn_sched_barrier(0);
  };
  auto rows = [&](int s) {
    const int dl = spin_until(tw);
    if (w < 2) tc0 = cyc32();
    __builtin_amdgcn_sched_barrier(0);
    STAMP(1);
    unsigned spins = 0;
    auto pass = [&](bool first) {
      u32x4 x[2 * NC];
#pragma unroll
      for (int f = 0; f < NC; ++f) {
        if (f && w < 2 && first) { prep(s); asm volatile(".rept %0\n\ts_nop 3\n\t.endr" :: "n"(P2_G1)); }
        else if (f) asm volatile(".rept %0\n\ts_nop 3\n\t.endr" :: "n"(w < 2 ? P2_G0 : (f == 1 ? P2_G2 : P2_G3)));
        __builtin_amdgcn_sched_barrier(0);
        x[2 * f] = SC1_LOADS(l2off[2 * f], o2p); x[2 * f + 1] = SC1_LOADS(l2off[2 * f + 1], o2p);
        __builtin_amdgcn_sched_barrier(0);
      }
      if (first) lds_inc<L_MISC + 92>(lds0);
      if (first) { if (w >= 2) prep(s); else prep2(s); }
      if (w >= 2 && first && P2_W) lds_spin<L_MISC + 92, 0>(lds0, 4u * (unsigned)s);
      if (w == 2 && first) tk.store(rs, o4, s > LAG3 ? 0u : 0x7ff00000u);
      if (w == 3 && first) t2.store(rs, t2o, o4);
      if (first && w < 2) {
        p1cur = p1v; offs = (offs - (unsigned)dl) & ~3u;
        PIN_V(p1cur); PIN_S(offs);
        tstar = tc0 + (unsigned)(e1 + D1_M + (w == 1 ? D1_W1 : 0)) - offs * 20u;
        dmin = offs < 40u ? D1_MIN : REC_D1;
        dmin = s < EARLY_N ? REC_D1 + EARLY_D1 : dmin;
        dmax = REC_D1;
        PIN_S(tstar); PIN_S(dmin); PIN_S(dmax);
      }
      __builtin_amdgcn_sched_barrier(0);
      unsigned ok = 32u;
#pragma unroll
      for (int t = 0; t < 3; ++t) { acc[t] = Z4; PIN_V(acc[t]); }
      __builtin_amdgcn_sched_barrier(0);
      bool r = true;
#pragma unroll
      for (int f = 0; f < NC; ++f) {
        unsigned p[4];
#pragma unroll
        for (int k = 0; k < 2; ++k) {
          const u32x4 v = x[2 * f + k];
          p[2 * k] = pack2((float)(int)v[0], (float)(int)v[1]); p[2 * k + 1] = pack2((float)(int)v[2], (float)(int)v[3]);
          asm volatile("" : "+v"(p[2 * k]), "+v"(p[2 * k + 1]));
          __builtin_amdgcn_sched_barrier(0);
        }
        ok = (ok & x[2 * f][0] & x[2 * f][2]) & (x[2 * f + 1][0] & x[2 * f + 1][2]);
        PIN_V(ok);
        __builtin_amdgcn_sched_barrier(0);
        bm[f] = mk8(p[0], p[1], p[2], p[3]);
        acc[0] = smfma64(bm[f], Wqk[0][f], acc[0], sidx);
        __builtin_amdgcn_sched_barrier(0);
        bm_put(f);
        acc[1] = smfma64(bm[f], Wqk[1][f], acc[1], sidx);
        __builtin_amdgcn_sched_barrier(0);
        if (f == NC - 1) { u64 bad = __builtin_amdgcn_ballot_w64(ok == 0u); PIN_S(bad); r = bad == 0ull; }
        __builtin_amdgcn_sched_barrier(0);
        acc[2] = smfma64(bm[f], Wqk[2][f], acc[2], sidx);
        __builtin_amdgcn_sched_barrier(0);
      }
      red_put();
      return r;
    };
    if (__builtin_expect(!pass(true), 0)) {
      FC(0);
      do {
        if (dead || ++spins > SPIN_CAP) { dead = true; break; }
        asm volatile("" ::: "memory");
      } while (!pass(false));
    }
    STAMPV(12, spins); LOGF(1, spins);
    TSTAMP(2, now32()); TSTAMP(3, spins);
  };
  if (w >= 2) prep0(-1);
  prep(0);
  if (w < 2) prep2(0);
  VM_DRAIN();
  CODE_PAD((PADR >> (4 * w)) & 15);
#pragma unroll 1
  for (int s = 0;; ++s) {
    if (__builtin_expect(o2p != 0u, 1)) rows(s);
    if (w < 2 && __builtin_expect(s == NSEG - 1, 0)) break;
    STAMP(2);
    BAR();
    STAMP(3);
    if (w < 2) pub1(p1cur, w == 0 ? o1m : o1);
    if (w == 2) tk.load(rs, o3p);
    if (w == 3) pv2.load(rs, o3p);
    const unsigned tp1 = cyc32();
    __builtin_amdgcn_sched_barrier(0);
    if (w == 2) {
      __builtin_amdgcn_s_setprio(W2_P);
      prep0(s);
      if (W2_D) spin_cyc(tp1 + W2_D);
      fr = qk_frag(lds + qo4, lds + qo2, lds + qo2b);
      t2_mem(fr, qx[0], qx[1], rs, o3, h, j, invv * KEXP, lane);
      __builtin_amdgcn_sched_barrier(0);
      asm volatile("s_waitcnt vmcnt(1)" ::: "memory");
      lds_inc<L_MISC + 28>(lds0);
      kq_get(s + 1);
    }
    if (w == 3) {
      __builtin_amdgcn_s_setprio(T4_P);
      prep0(s);
      t2.get(lds, s + 1, lane); t2.run(0); t2.run(1);
      __builtin_amdgcn_sched_barrier(0);
    }
    if (w >= 2 && __builtin_expect(s == NSEG - 2, 0)) { VM_DRAIN(); lds_inc<L_MISC + 28>(lds0); }
    if (w >= 2) __builtin_amdgcn_s_setprio(UN_P);
    TSTAMP(0, now32());
    STAMP(4);
    __builtin_amdgcn_sched_barrier(0);
    unsigned tpub = 0;
    u32x2 sw;
    if (w == 2) {
      STAMP(5);
      __builtin_amdgcn_sched_barrier(0);
      STAMP(6);
      ARRIVE(0);
      tk.run(j, lane, SelLo);
      if (__builtin_expect(s == NSEG - 1, 0)) { tlast = tp1; break; }
      if (F2B) { lds_spin<L_MISC + 48, 1>(lds0, 2u * (unsigned)(s + 1)); x_fetch_n<F2B>(xsrc, lds0, s + 3, P2B, lane * 16); }
      __builtin_amdgcn_s_setprio(SP_P);
    }
    if (w == 3) {
      pv2.run<false>(lds, sv < 1 + LAG4 ? sv + NV - 1 - LAG4 : sv - 1 - LAG4, lane, rs, 0u);
      if (__builtin_expect(s == NSEG - 1, 0)) { tlast = tp1; break; }
      if (ST_E) lds_inc<L_MISC + 112>(lds0);
      STAMP(11);
      __builtin_amdgcn_s_setprio(SP_P);
      STAMP(5);
      ARRIVE(0);
      if (F3) { lds_spin<L_MISC + 48, 1>(lds0, 2u * (unsigned)(s + 1)); x_fetch_n<F3>(xsrc, lds0, s + 3, 40 - F3C - F3, lane * 16); }
      STAMP(6);
    }
    if (w < 2) {
      __builtin_amdgcn_s_setprio(D1_P);
      x_fetch_n<F0A>(xsrc, lds0, s + 3, PA + w * F0A, lane * 16);
      int dq = imin((int)(tstar - tp1), dmax);
      dq = dq < -D1_LATE ? dmax : dq;
      PIN_S(dq);
      spin_exact(tp1 + (unsigned)imax(dq, dmin));
      STAMP(5);
      u32x32 S;
      auto sc_load = [&]() {
        asm volatile("buffer_load_dwordx4 v[224:227], %2, %4, %5 offen sc1\n\t.rept %7\n\ts_nop 3\n\t.endr\n\t"
                     "buffer_load_dwordx4 v[228:231], %3, %4, %6 offen sc1\n\tbuffer_load_dwordx4 v[232:235], %3, %4, %6 offen offset:2048 sc1\n\t"
                     "buffer_load_dwordx4 v[236:239], %3, %4, %6 offen offset:256 sc1\n\tbuffer_load_dwordx4 v[240:243], %3, %4, %6 offen offset:2304 sc1\n\t"
                     "v_mov_b32 v220, 0x42000000\n\tv_mov_b32 v221, 0\n\tv_mov_b32 v222, 0x42000000\n\tv_mov_b32 v223, 0"
                     : "=&{v[224:255]}"(S), "+{v[208:223]}"(P) : "v"(l1offm), "v"(l1off), "s"(rs), "s"(o1m), "s"(o1), "n"(P1_G) : "memory");
      };
      sc_load();
      __builtin_amdgcn_sched_barrier(0);
      lds_inc<L_MISC + 48>(lds0);
      STAMP(6);
      ARRIVE(0);
      lds_spin<L_MISC + 28, 0>(lds0, need2);
      __builtin_amdgcn_s_setprio(3);
      asm volatile("ds_read_b64 v[192:193], %3\n\tds_read_b64 v[196:197], %3 offset:768\n\tds_read_b64 v[200:201], %4\n\tds_read_b64 v[204:205], %4 offset:768\n\tds_read_b64 v[184:185], %2"
                   : "+{v[184:191]}"(V8), "+{v[192:207]}"(V) : "v"(vma[0]), "v"(vma[1]), "v"(vma[2]) : "memory");
      __builtin_amdgcn_sched_barrier(0);
      {
        const int de = imin((int)(tp1 - tc0) - e1, D1_C);
        e1 += s == 0 ? 0 : de >> (s < EARLY_N && de < -D1_C ? 0 : D1_G);
        PIN_S(e1);
      }
      STAMP(11);
      __builtin_amdgcn_sched_barrier(0);
      {
        unsigned spins = 0;
        u64 miss, bad;
#define MM(d, a, b, c) "v_mfma_f32_16x16x32_bf16 " d ", " a ", " b ", " c "\n\t"
#define FM(d, x, r, ctrl) "v_fmac_f32_dpp " d ", " x ", " r " " ctrl "\n\t"
#define ID_ "quad_perm:[0,1,2,3] row_mask:0xf bank_mask:"
#define HM_ "row_half_mirror row_mask:0xf bank_mask:"
#define R8_ "row_ror:8 row_mask:0xf bank_mask:"
#define MI_ "row_mirror row_mask:0xf bank_mask:"
        auto soft = [&]() {
          asm volatile(
            "s_waitcnt vmcnt(4)\n\t" MM("v[244:247]", "%6", "v[224:227]", "0") "v_min3_u32 v214, v224, v225, v226\n\t"
            "s_waitcnt vmcnt(3)\n\t" MM("v[248:251]", "%6", "v[228:231]", "0") "v_min3_u32 v214, v214, v227, v228\n\tv_min3_u32 v214, v214, v229, v230\n\t"
            "s_waitcnt vmcnt(2)\n\t" MM("v[248:251]", "%6", "v[232:235]", "v[248:251]") "v_min3_u32 v214, v214, v231, v232\n\t"
            "v_add_f32_dpp v212, v244, v244 " R8_ "0x3\n\tv_add_f32_dpp v213, v245, v245 " R8_ "0x3\n\t"
            "s_waitcnt vmcnt(1)\n\t" MM("v[248:251]", "%7", "v[236:239]", "v[248:251]")
            "v_add_f32_dpp v212, v246, v246 " R8_ "0xc\n\tv_add_f32_dpp v213, v247, v247 " R8_ "0xc\n\t"
            "s_waitcnt vmcnt(0) lgkmcnt(0)\n\t" MM("v[248:251]", "%7", "v[240:243]", "v[248:251]")
            "v_fma_f32 v212, v212, %14, %15\n\tv_fma_f32 v213, v213, %14, %15\n\tv_exp_f32 v216, v212\n\tv_exp_f32 v217, v213\n\t"
            "v_min3_u32 v214, v214, v233, v234\n\tv_cvt_pk_bf16_f32 v208, v216, v217\n\tv_min3_u32 v214, v214, v235, v236\n\tds_read_b96 v[188:190], %16 offset:%18\n\t"
            "v_exp_f32 v216, v248\n\tv_mov_b32_dpp v209, v208 " R8_ "0xf\n\tv_exp_f32 v217, v249\n\tv_exp_f32 v218, v250\n\t"
            MM("v[252:255]", "%8", "v[208:211]", "0") "v_exp_f32 v219, v251\n\tv_cvt_pk_bf16_f32 v232, v216, v217\n\t"
            MM("v[224:227]", "v[192:195]", "v[208:211]", "0") "v_cvt_pk_bf16_f32 v233, v218, v219\n\tv_min3_u32 v214, v214, v237, v238\n\tv_mov_b32_dpp v234, v232 " R8_ "0xf\n\t"
            MM("v[228:231]", "v[196:199]", "v[208:211]", "0") "v_mov_b32_dpp v235, v233 " R8_ "0xf\n\t"
            MM("v[244:247]", "v[204:207]", "v[208:211]", "0") "v_min3_u32 v214, v214, v239, v240\n\t"
            MM("v[252:255]", "%8", "v[232:235]", "v[252:255]") MM("v[224:227]", "%10", "v[232:235]", "v[224:227]") "v_min3_u32 v214, v214, v241, v242\n\t"
            MM("v[228:231]", "%11", "v[232:235]", "v[228:231]") "s_waitcnt lgkmcnt(0)\n\tv_sub_u32 v215, v190, %17 clamp\n\t"
            MM("v[236:239]", "v[200:203]", "v[208:211]", "0") "v_min3_u32 v214, v214, v243, v215\n\tv_cmp_eq_u32 %4, 0, v214\n\tv_rcp_f32 v216, v252\n\t"
            MM("v[240:243]", "v[184:187]", "v[208:211]", "0") "v_mul_f32 v216, v216, v188\n\tv_subrev_u32 v215, 0x2f800000, v252\n\t"
            MM("v[236:239]", "%12", "v[232:235]", "v[236:239]") FM("v220", "v224", "v216", ID_ "0x1") FM("v221", "v225", "v216", ID_ "0x1")
            "v_mov_b32_dpp v217, v216 " HM_ "0xf\n\t"
            MM("v[244:247]", "%13", "v[232:235]", "v[244:247]") FM("v220", "v230", "v216", ID_ "0x2") FM("v221", "v231", "v216", ID_ "0x2")
            MM("v[240:243]", "%9", "v[232:235]", "v[240:243]") FM("v220", "v228", "v217", HM_ "0x1") FM("v221", "v229", "v217", HM_ "0x1")
            "v_cmp_ge_u32 vcc, v215, v189\n\t" FM("v220", "v226", "v217", HM_ "0x2") FM("v221", "v227", "v217", HM_ "0x2")
            "s_or_b64 %5, %4, vcc"
            : "+{v[224:255]}"(S), "+{v[208:223]}"(P), "+{v[184:191]}"(V8), "+{v[192:207]}"(V), "=&s"(miss), "=&s"(bad)
            : "v"(slo), "v"(shi), "v"(ONES), "v"(vox[0]), "v"(vox[1]), "v"(vox[2]), "v"(vox[3]), "v"(vox[4]), "v"(k1), "v"(minf), "v"(lds0), "s"(s), "n"(L_MISC + 32)
            : "vcc", "scc", "memory");
        };
        soft();
        if (__builtin_expect(bad != 0ull, 0)) {
          if (miss) FC(1); else FC(2);
          while (miss != 0ull && !dead) {
            if (++spins > SPIN_CAP) dead = true;
            asm volatile("s_nop 7\n\ts_nop 7" ::: "memory");
            sc_load(); soft();
          }
          if (bad != 0ull) {
            u32x4 y[5];
#pragma unroll
            for (int k = 0; k < 4; ++k) y[k] = SC1_LOADS(l1off + (k & 1) * 2048 + (k >> 1) * 256, o1);
            y[4] = SC1_LOADS(l1offm, o1m);
            const f32x4 d0 = mfma32(as8(slo), as8(y[4]), Z4);
            f32x4 d1 = mfma32(as8(shi), as8(y[3]), mfma32(as8(shi), as8(y[2]), mfma32(as8(slo), as8(y[1]), mfma32(as8(slo), as8(y[0]), Z4))));
            const bool lo = c < 8;
            const float m0 = __builtin_fmaf((lo ? d0[0] : d0[2]) + dppf<DPP_ROR8>(lo ? d0[2] : d0[0]), k1, minf);
            const float m1 = __builtin_fmaf((lo ? d0[1] : d0[3]) + dppf<DPP_ROR8>(lo ? d0[3] : d0[1]), k1, minf);
            float mx = fmx(fmx(fmx(d1[0], d1[1]), d1[2]), fmx(fmx(d1[3], m0), m1));
            mx = fmx(mx, dppf<DPP_ROR8>(mx));
            mx = x32mx(x16mx(mx));
            d1 -= mx;
            const unsigned mm = pack2(__builtin_amdgcn_exp2f(m0 - mx), __builtin_amdgcn_exp2f(m1 - mx));
            const unsigned t01 = pack2(__builtin_amdgcn_exp2f(d1[0]), __builtin_amdgcn_exp2f(d1[1])), t23 = pack2(__builtin_amdgcn_exp2f(d1[2]), __builtin_amdgcn_exp2f(d1[3]));
            const bf16x8 pm = mk8(mm, dpp_ror8(mm), 0u, 0u), pt = mk8(t01, t23, dpp_ror8(t01), dpp_ror8(t23));
            const float r = __builtin_amdgcn_rcpf(mfma32(ONES, pt, mfma32(ONES, pm, Z4))[0]) * u2f(V8[4]), rm = dppf<0x141>(r);
            f32x4 o[5];
#pragma unroll
            for (int k = 0; k < 5; ++k) o[k] = mfma32(vox[k], pt, mfma32(k ? mk8(V[4 * k - 4], V[4 * k - 3], 0u, 0u) : mk8(V8[0], V8[1], 0u, 0u), pm, Z4));
#pragma unroll
            for (int i = 0; i < 2; ++i) {
              const float oa = dppf<0x141>(o[2][i]), ob = dppf<0x141>(o[1][2 + i]);
              P[12 + i] = f2u(__builtin_fmaf(hd ? ob : oa, rm, __builtin_fmaf(hd ? o[2][2 + i] : o[1][i], r, i ? 0.f : 32.0f)));
            }
            P[8] = f2u(r); P[9] = f2u(rm);
#pragma unroll
            for (int i = 0; i < 4; ++i) { S[12 + i] = f2u(o[3][i]); S[16 + i] = f2u(o[0][i]); S[20 + i] = f2u(o[4][i]); }
            asm volatile("s_nop 7\n\ts_nop 7" : "+{v[224:255]}"(S), "+{v[208:223]}"(P));
          }
        }
        STAMP(7); STAMPV(13, spins); LOGF(0, spins);
        {
          asm volatile("v_cvt_i32_f32 v218, v220\n\tv_cvt_i32_f32 v219, v221\n\tv_and_or_b32 v218, v218, %7, 2\n\tbuffer_atomic_add_x2 v[218:219], %3, %5, %6 offen\n\t"
                       FM("v222", "v236", "v216", R8_ "0x4") FM("v223", "v237", "v216", R8_ "0x4") "ds_read_b64 %2, %8 offset:%9\n\t"
                       FM("v222", "v238", "v217", MI_ "0x8") FM("v223", "v239", "v217", MI_ "0x8") "v_permlane32_swap_b32 v240, v242\n\t"
                       FM("v222", "v244", "v217", MI_ "0x4") FM("v223", "v245", "v217", MI_ "0x4") "v_permlane32_swap_b32 v241, v243\n\t"
                       FM("v222", "v246", "v216", R8_ "0x8") FM("v223", "v247", "v216", R8_ "0x8")
                       FM("v222", "v240", "v216", ID_ "0x1") FM("v223", "v241", "v216", ID_ "0x1") FM("v222", "v242", "v217", HM_ "0x1") FM("v223", "v243", "v217", HM_ "0x1")
                       "v_cvt_i32_f32 v212, v222\n\tv_cvt_i32_f32 v213, v223\n\tv_and_or_b32 v212, v212, %7, 2\n\tbuffer_atomic_add_x2 v[212:213], %4, %5, %6 offen\n\t"
                       "s_waitcnt lgkmcnt(0)"
                       : "+{v[224:255]}"(S), "+{v[208:223]}"(P), "=&v"(sw) : "v"(acco), "v"(acco2), "s"(rs), "s"(o2), "s"(~63u), "v"(lds0), "n"(L_MISC + 96) : "memory");
          __builtin_amdgcn_sched_barrier(0);
          tpub = now32();
          lds_inc<L_MISC + 104 + 4 * (w & 1)>(lds0);
          asm volatile("buffer_atomic_umax %0, %1, %2, %3 offen" :: "v"(tpub | 1u), "v"(tso), "s"(rs), "s"(o2) : "memory");
          STAMP(10);
        }
      }
    }
    STAMP(8);
    ARRIVE(1);
    o2p = o2;
    PIN_S(o2p);
    if (w < 2) {
      if (__builtin_expect(__builtin_amdgcn_readfirstlane(sw[1]) <= (unsigned)s, 0)) {
        lds_spin<L_MISC + 100, 0>(lds0, (unsigned)s + 1u);
        sw[0] = __hip_atomic_load((unsigned*)(misc + 24), __ATOMIC_RELAXED, __HIP_MEMORY_SCOPE_WORKGROUP);
      }
    } else {
      u32x4 q;
      asm volatile("1:\n\tds_read_b128 %0, %1 offset:%3\n\t.if %4\n\ts_sleep 1\n\t.endif\n\ts_waitcnt lgkmcnt(0)\n\tv_min3_u32 v201, v201, v202, v203\n\tv_cmp_ge_u32 vcc, %2, v201\n\ts_cbranch_vccnz 1b"
                   : "=&{v[200:203]}"(q) : "v"(lds0), "s"(s), "n"(L_MISC + 96), "n"(SP_SL) : "vcc", "memory");
      __builtin_amdgcn_s_setprio(3);
      tpub = now32();
      sw[0] = q[0];
    }
    STAMP(9);
    static_assert(F3C == 0 && F2C == 0 && F0C == 0, "nothing behind the atomics");
    LOGT(0, tpub); LOGT(1, tb2_);
    TSTAMP(1, tpub);
    ARRIVE_OUT();
    const unsigned an = __builtin_amdgcn_readfirstlane(sw[0]);
    const int ahead = (int)(an - tpub);
    tw = tpub + (unsigned)(an == 0u || ahead > 1000 ? SCH_FIRST : imax(ahead, w < 2 ? SCH_DMIN : 0));
    offs = tw - an;
  }
  STAMP_OUT();
  LOGP(4);
  __builtin_amdgcn_s_setprio(3);
  if (w < 2) { BAR(); tlast = cyc32(); }
  if (w == 2) tk.store(rs, o4);
  if (w == 3) {
    const unsigned oo = op_slot(NSEG - 2 - LAG4) + (unsigned)((m * 32 + gp) * 1280);
    const char* sp = lds + L_STG + 2048;
    const u32x4 v0 = *(const u32x4*)(sp + lane * 16), v1 = *(const u32x4*)(sp + 1280 + lane * 16), v2 = *(const u32x4*)(sp + (g & 1) * 1280 + 1024 + c * 16);
    SC1_STORE(v0, oo + lane * 16); SC1_STORE(v1, oo + 20480 + lane * 16); SC1_STORE(v2, lane < 32 ? oo + g * 20480 + 1024 + c * 16 : 0x7ffffff0u);
  }
  if (dead) tlast |= 1u; else tlast &= ~1u;
}

__device__ __forceinline__ void drain(const RecArgs& a, char* lds, const __amdgpu_buffer_rsrc_t rs, const int w, const unsigned tp1) {
  static_assert(LAG3 == 2 && LAG4 == 3 && LAG5 == 6, "drain schedule");
  const int lane = __builtin_amdgcn_workitem_id_x() & 63, b = __builtin_amdgcn_workgroup_id_x(), c = lane & 15;
  const int gp = (b & 7) * 2 + (b >> 7), m = (b >> 3) & 15, j = m & 7;
  unsigned* const misc = (unsigned*)(lds + L_MISC);
  const unsigned lds0 = (unsigned)(uintptr_t)(LAS char*)lds, ck = __builtin_amdgcn_readfirstlane(misc[11]);
  bool dead = tp1 & 1u;
  auto poll = [&](auto load, auto ready) {
    for (unsigned spins = 0;;) {
      asm volatile("" ::: "memory");
      load();
      if (ready() || dead) break;
      if (++spins > SPIN_CAP) dead = true;
      if (END_SL) __builtin_amdgcn_s_sleep(END_SL);
    }
  };
  LOGD(0);
#define TASK(ty, sg, tm) ((u64)((ty) | ((sg) - 56) << 2 | ((tm) / 100) << 5))
  u64 todo = w == 0 ? TASK(1, 63, END_E1) | TASK(2, 63, END_E2) << 12 | TASK(3, 62, END_E3) << 24
           : w == 1 ? TASK(1, 62, 0) | TASK(2, 62, END_E62) << 12 | TASK(3, 60, END_E3) << 24
           : w == 2 ? TASK(2, 60, 0) | TASK(3, 56, 0) << 12 : TASK(2, 61, END_E61) | TASK(3, 58, END_E59) << 12;
  TokTask tk;
  PvTask2 pv;
  OutTask ot;
  {
    const u32x4* p = (const u32x4*)(lds + L_DRN) + lane;
    const u32x4 q0 = p[0], q1 = p[64], q2 = p[128];
    tk.l0 = q0[0]; tk.l1 = q0[0] + 16384u; tk.lm = q0[1]; tk.s0 = q0[2]; tk.s1 = q0[3];
    pv.lo = q1[0]; pv.lm = q1[1]; pv.vr = q1[2]; pv.ma = q1[3]; pv.mz = q2[0]; pv.so = q2[1];
    pv.pa[0] = pv.pa[1] = pv.pb[0] = pv.pb[1] = (u32x4){0u, 0u, 0u, 0u};
    ot.t5f = q2[2]; ot.t5o = q2[3]; ot.sel = as8(p[192]);
  }
#pragma unroll 1
  for (int ti = 0; todo; todo >>= 12, ++ti) {
    const int ty = (int)todo & 3, sl = 56 + (((int)todo >> 2) & 7);
    const unsigned tm = tp1 + (((unsigned)todo >> 5) & 0x7fu) * 100u;
    if (ty == 1) {
      wait_cyc(tm);
      LOGD(3 * ti + 1);
      poll([&]() { tk.load(rs, ts_slot(ck, sl)); }, [&]() { return tk.ready(); });
      LOGD(3 * ti + 2);
      tk.run(j, lane, ot.sel); tk.store(rs, pt_slot(ck, sl));
    } else if (ty == 2) {
      const unsigned t0 = cyc32();
      if (sl >= NSEG - 2) { lds_wait(misc + 7, 4u * NSEG + 3u); wait_cyc(t0 + END_G2); }
      wait_cyc(tm);
      LOGD(3 * ti + 1);
      poll([&]() { pv.load(rs, pt_slot(ck, sl)); }, [&]() { return pv.ready(); });
      LOGD(3 * ti + 2);
      pv.run<true>(lds, sl % NV, lane, rs, op_slot(sl) + (unsigned)((m * 32 + gp) * 1280));
      if (sl == NSEG - 1) misc[14] = cyc32() | 1u;
    } else {
      if (sl >= NSEG - 4) { lds_wait(misc + 14, 1u); wait_cyc(__builtin_amdgcn_readfirstlane(misc[14]) + END_G3); }
      wait_cyc(tm);
      LOGD(3 * ti + 1);
      OutTask o2 = ot;
      poll([&]() { o2.load(a, rs, sl + 1, b); ot.load(a, rs, sl, b); }, [&]() { return ot.ready(b) & o2.ready(b); });
      LOGD(3 * ti + 2);
      o2.run(lane); o2.store(a, sl + 1, b, lane);
      ot.run(lane); ot.store(a, sl, b, lane);
    }
    LOGD(3 * ti + 3);
  }
  VM_DRAIN();
  if (w != 0) lds_inc<L_MISC + 64>(lds0);
  else {
    constexpr unsigned o2 = (unsigned)OFF_ACC + (unsigned)((NSEG - 1) % SR) * (unsigned)ACC_STEP;
    lds_wait(misc + 16, 3u);
    LOGD(10);
    if (lane == 0) __hip_atomic_fetch_add((gu64*)((GAS char*)(a.ws + o2) + acc_off(m * 4, 0)), (u64)1, RLX_AGENT);
    wait_until(now32() + SCH_DRAIN);
    wait_cyc(tp1 + END_E4);
    unsigned x;
    if (qkv_row(b) + 48 >= 55 && qkv_col(b) < 15) poll([&]() { x = __builtin_amdgcn_raw_buffer_load_b32(rs, acc_off(c * 4, 0), o2, 16); }, [&]() { return wave_all((x & 16u) != 0u); });
    lds_inc<L_MISC + 68>(lds0);
    LOGD(11);
  }
  if (qkv_row(b) + 48 >= 55 && qkv_col(b) < 15) {
    lds_wait(misc + 18, 8u);
    lds_spin<L_MISC + 68, 1>(lds0, 1u);
    x_fetch_n<10>(x_src(a, NSEG + 3, b), lds0, 3, 10 * w, lane * 16);
    VM_DRAIN();
    lds_inc<L_MISC + 68>(lds0);
  }
  LOGP(8);
}

__device__ __forceinline__ void proj_front(const RecArgs& a, char* lds, const __amdgpu_buffer_rsrc_t rs, const int v) {
  const int lane = __builtin_amdgcn_workitem_id_x() & 63, b = __builtin_amdgcn_workgroup_id_x(), c = lane & 15, g = lane >> 4;
  const int rb = (b & 7) + 8 * (b >> 5), cq = (b >> 3) & 3;
  const unsigned lds0 = (unsigned)(uintptr_t)(LAS char*)lds;
  unsigned* const cnt = (unsigned*)(lds + L_MISC) + 20;
  LOGF2(0);
  float* const red = (float*)(lds + L_RED1);
  float* const fin = red + 640;
  const f32x4 Z4 = {0.f, 0.f, 0.f, 0.f};
  x_fetch_n<8>((const u16*)(a.ws + OFF_OB) + (size_t)rb * 32 * DM, lds0, 0, 8 * v, lane * 16);
  bf16x8 W[2][20];
  const __amdgpu_buffer_rsrc_t rw = __builtin_amdgcn_make_buffer_rsrc(a.ws + OFF_WOBT + (size_t)(cq * 10 + 2 * v) * 20480, 0, 40960, 0x00020000);
  auto w_get = [&](int ks) {
#pragma unroll
    for (int i = 0; i < 2; ++i) W[i][ks] = as8(__builtin_amdgcn_raw_buffer_load_b128(rw, lane * 16 + (ks & 3) * 1024, (i * 20 + (ks & ~3)) * 1024, 0));
    __builtin_amdgcn_sched_barrier(0);
  };
#pragma unroll
  for (int ks = 0; ks < FW; ++ks) w_get(ks);
  const unsigned fo = (unsigned)(rb * 40960 + (cq * 5 + v) * 1024 + ((g >> 1) * 16 + c) * 16), f8 = fo + (g & 1) * 8, f16 = fo + (g & 1) * 20480;
  u32x2 xo[2][2];
#pragma unroll
  for (int k = 0; k < 4; ++k)
    xo[k >> 1][k & 1] = __builtin_bit_cast(u32x2, __hip_atomic_load((const gu64*)((const char*)a.xnew + f8 + (k >> 1) * 512 + (k & 1) * 20480), RLX_AGENT));
  asm volatile("s_waitcnt vmcnt(%0)" :: "n"(2 * FW + 4) : "memory");
  lds_inc<L_MISC + 80>(lds0);
  lds_wait(cnt, 5u);
  LOGF2(1);
  f32x4 acc[2][2] = {{Z4, Z4}, {Z4, Z4}};
  bf16x8 x[20][2];
#pragma unroll
  for (int ks = -3; ks < 20; ++ks) {
    if (ks >= 0 && ks + FW < 20) w_get(ks + FW);
#pragma unroll
    for (int t = 0; t < 2; ++t) {
      if (ks + 3 < 20) x[ks + 3][t] = *(const bf16x8*)(lds + L_XN + (t * 20 + ks + 3) * 1024 + lane * 16);
#pragma unroll
      for (int i = 0; i < 2 && ks >= 0; ++i) acc[i][t] = mfma32(W[i][ks < 0 ? 0 : ks], x[ks < 0 ? 0 : ks][t], acc[i][t]);
    }
    __builtin_amdgcn_sched_barrier(0);
  }
  u32x2 xp[2][2];
  PIN_V(acc[1][1]);
  LOGF2(2);
#pragma unroll
  for (int t = 0; t < 2; ++t) {
#pragma unroll
    for (int k = 0; k < 4; ++k) {
      const int i = k >> 1, q = k & 1;
      const unsigned r = pack2(acc[i][t][2 * q], acc[i][t][2 * q + 1]), x = xo[i][t][q];
      xp[i][t][q] = pack2(u2f(x << 16) + u2f(r << 16), u2f(x & 0xffff0000u) + u2f(r & 0xffff0000u));
      acc[i][t][2 * q] = u2f(xp[i][t][q] << 16); acc[i][t][2 * q + 1] = u2f(xp[i][t][q] & 0xffff0000u);
    }
    const f32x4 sm = acc[0][t] + acc[1][t];
    const float ml = x32sum(x16sum((sm[0] + sm[1]) + (sm[2] + sm[3]))) * (1.0f / 32.0f);
    float q = 0.f;
#pragma unroll
    for (int r = 0; r < 8; ++r) { const float d = acc[r >> 2][t][r & 3] - ml; q += d * d; }
    q = x32sum(x16sum(q));
    *(f32x2*)(red + (v * 32 + t * 16 + c) * 2) = (f32x2){ml, q};
  }
  lds_inc<L_MISC + 80>(lds0);
  if (v == 0) {
    lds_wait(cnt, 10u);
    LOGF2(3);
    float mw[5], ml = 0.f, m2 = 0.f;
#pragma unroll
    for (int i = 0; i < 5; ++i) { const f32x2 u = *(const f32x2*)(red + (i * 32 + (lane & 31)) * 2); mw[i] = u[0]; ml += u[0]; m2 += u[1]; }
    ml *= 0.2f;
#pragma unroll
    for (int i = 0; i < 5; ++i) m2 += 32.0f * (mw[i] - ml) * (mw[i] - ml);
    const unsigned xs = (unsigned)OFF_LNX + rb * 1024;
    if (lane < 32) SC1_STORE8S(((u32x2){f2u(ml), nz(f2u(m2))}), (cq * 32 + lane) * 8, xs);
    wait_cyc(cyc32() + FR_D);
    u32x2 x0, x1;
    for (unsigned spins = 0; spins < SPIN_CAP; ++spins) {
      x0 = __builtin_amdgcn_raw_buffer_load_b64(rs, lane * 8, xs, 16); x1 = __builtin_amdgcn_raw_buffer_load_b64(rs, 512 + lane * 8, xs, 16);
      if (wave_all(x0[1] != 0u && x1[1] != 0u)) break;
      asm volatile("" ::: "memory");
    }
    const float m0 = u2f(x0[0]), m1 = u2f(x1[0]);
    const float mean = x32sum(m0 + m1) * 0.25f;
    const float M2 = x32sum((u2f(x0[1]) + u2f(x1[1])) + 160.0f * ((m0 - mean) * (m0 - mean) + (m1 - mean) * (m1 - mean)));
    const float mb = rbf(mean), den = rbf(__builtin_sqrtf(rbf(rbf(M2 * (1.0f / DM)) + 1e-5f)));
    if (lane < 32) {
      fin[lane] = mb; fin[32 + lane] = 1.0f / den;
      if (cq == 0) __hip_atomic_store((GAS unsigned*)(a.ws + OFF_XNRM) + (rb * 32 + lane) * 4, f2u(1.02f * (M2 + DM * (mean - mb) * (mean - mb)) / (den * den)), RLX_AGENT);
    }
    lds_inc<L_MISC + 84>(lds0);
  }
  lds_wait(cnt + 1, 1u);
  LOGF2(4);
  const __amdgpu_buffer_rsrc_t rx = __builtin_amdgcn_make_buffer_rsrc(a.ws + OFF_XN, 0, (int)(OFF_QB - OFF_XN), 0x00020000);
  auto put = [&](const u32x2 p0, const u32x2 p1, unsigned so) {
    const auto r0 = __builtin_amdgcn_permlane16_swap(p0[0], p1[0], false, false), r1 = __builtin_amdgcn_permlane16_swap(p0[1], p1[1], false, false);
    __builtin_amdgcn_raw_buffer_store_b128((u32x4){r0[0], r1[0], r0[1], r1[1]}, rx, f16, so, 16);
  };
#pragma unroll
  for (int i = 0; i < 2; ++i) {
    u32x2 y[2];
#pragma unroll
    for (int t = 0; t < 2; ++t) {
      const float mb = fin[t * 16 + c], rden = fin[32 + t * 16 + c];
      y[t] = (u32x2){pack2(rbf(acc[i][t][0] - mb) * rden, rbf(acc[i][t][1] - mb) * rden), pack2(rbf(acc[i][t][2] - mb) * rden, rbf(acc[i][t][3] - mb) * rden)};
    }
    put(y[0], y[1], i * 512);
  }
  VM_DRAIN();
  lds_inc<L_MISC + 80>(lds0);
#pragma unroll
  for (int i = 0; i < 2; ++i) put(xp[i][0], xp[i][1], (unsigned)(OFF_XB - OFF_XN) + i * 512);
  if (v == 0) {
    lds_wait(cnt, 15u);
    if (rb < 4 && lane < 8) __hip_atomic_fetch_add((GAS unsigned*)(a.ws + OFF_RDY) + lane * 64, 1u, RLX_AGENT);
  }
  LOGF2(5);
}

__global__ __launch_bounds__(512, 2) void rec_kernel(RecArgs a) {
  __shared__ __attribute__((aligned(16))) char lds[L_BYTES];
  const int tid = __builtin_amdgcn_workitem_id_x(), wid = __builtin_amdgcn_readfirstlane(tid >> 6), w = wid & 3;
  const __amdgpu_buffer_rsrc_t rs = __builtin_amdgcn_make_buffer_rsrc(a.ws, 0, (int)COMM_BYTES, 0x00020000);
  if (tid < 32) ((float*)(lds + L_MISC))[tid] = (tid == 4 || tid == 5) ? 1.0f : 0.0f;
  BAR();
  if (a.layer && wid < 5) proj_front(a, lds, rs, wid);
  if (wid < 4) {
    unsigned tlast;
    if (w == 0) rec_waves<0>(a, lds, rs, tlast);
    else if (w == 1) rec_waves<1>(a, lds, rs, tlast);
    else if (w == 2) rec_waves<2>(a, lds, rs, tlast);
    else rec_waves<3>(a, lds, rs, tlast);
    drain(a, lds, rs, w, tlast);
  } else {
    unsigned vo[2][2];
    if (w == 0) token_waves<0>(a, lds, rs, vo);
    else if (w == 1) token_waves<1>(a, lds, rs, vo);
    else if (w == 2) token_waves<2>(a, lds, rs, vo);
    else token_waves<3>(a, lds, rs, vo);
    qkv_tail(a, lds, w, vo);
  }
}

#if defined(__HIP_DEVICE_COMPILE__)
#pragma clang attribute pop
#endif

constexpr int KQ = S_ / 4;
#define ATT_P1 6
#define ATT_P2 10
#define ATT_P3 14
#define ATT_THR 6.0f
#define ATT_NQ 4
#define ATT_LMAX 1.0e12f
#if defined(__HIP_DEVICE_COMPILE__)
#pragma clang attribute push(__attribute__((target("no-packed-fp32-ops"))), apply_to = function)
#endif
__global__ __launch_bounds__(256 * ATT_NQ, 4) void attb_kernel(const u16* __restrict__ Qp, const u16* __restrict__ KV, u16* __restrict__ O) {
  __shared__ f32x4 mo_[ATT_NQ][4][2][3][64];
  __shared__ float mm_[ATT_NQ][4][2][64];
  const int tid = __builtin_amdgcn_workitem_id_x(), bid = __builtin_amdgcn_workgroup_id_x();
  __shared__ unsigned sink[64];
  own_code<5000>(tid, sink);
  const int lane = tid & 63, wid = __builtin_amdgcn_readfirstlane(tid >> 6), kh = wid / ATT_NQ, r = lane & 31, hh = lane >> 5;
  const int hd = (bid & 7) * 4 + ((bid >> 3) & 3), qb = (bid >> 5) * ATT_NQ + wid % ATT_NQ;
  const int q0 = qb * 64;
  bf16x8 qa[2]; u32x2 qb2[2];
#pragma unroll
  for (int qt = 0; qt < 2; ++qt) {
    { const u32x2* qp = (const u32x2*)(Qp + ((size_t)hd * S_ + q0 + qt * 32 + r) * QST + 8 * hh); const u32x2 q0_ = qp[0], q1_ = qp[1]; qa[qt] = mk8(q0_[0], q0_[1], q1_[0], q1_[1]); }
    const u32x2 t = *(const u32x2*)(Qp + ((size_t)hd * S_ + q0 + qt * 32 + r) * QST + 16);
    qb2[qt] = t;
  }
  const __amdgpu_buffer_rsrc_t rk = __builtin_amdgcn_make_buffer_rsrc((void*)(KV + ((size_t)hd * 64 + kh * 16) * (KVT / 2)), 0, 16 * KVT, 0x00020000);
  unsigned ok1 = lane * 16, ok2 = hh ? 1600 : 1024 + r * 8, ov = r > 20 ? 1u << 30 : 1280 + (hh * 21 + r) * 16;
  f32x16 o[2];
  float m[2];
  const f32x16 Z16 = {0.f, 0.f, 0.f, 0.f, 0.f, 0.f, 0.f, 0.f, 0.f, 0.f, 0.f, 0.f, 0.f, 0.f, 0.f, 0.f};
  int odd = 0xDDDD;
  asm volatile("" : "+v"(odd));
  struct KvT { u32x4 k1, v0, v1; u32x2 k2; };
  auto fetch_k = [&](KvT& x, int t) {
    x.k1 = __builtin_amdgcn_raw_buffer_load_b128(rk, ok1, imin(t, KQ / 32 - 1) * KVT, 0); x.k2 = __builtin_amdgcn_raw_buffer_load_b64(rk, ok2, imin(t, KQ / 32 - 1) * KVT, 0);
    __builtin_amdgcn_sched_barrier(0);
  };
  auto fetch_v = [&](KvT& x, int t) {
    x.v0 = __builtin_amdgcn_raw_buffer_load_b128(rk, ov, imin(t, KQ / 32 - 1) * KVT, 0); x.v1 = __builtin_amdgcn_raw_buffer_load_b128(rk, ov + 672, imin(t, KQ / 32 - 1) * KVT, 0);
    __builtin_amdgcn_sched_barrier(0);
  };
  auto fetch = [&](int t) { KvT x; __builtin_amdgcn_sched_barrier(0); fetch_k(x, t); fetch_v(x, t); return x; };
  auto qk1 = [&](const KvT& x, int qt) { return __builtin_amdgcn_mfma_f32_32x32x16_bf16(as8(x.k1), qa[qt], Z16, 0, 0, 0); };
  auto qk2 = [&](const KvT& x, int qt, f32x16 c) { return __builtin_amdgcn_mfma_f32_32x32x8bf16_1k(__builtin_bit_cast(v4i16_t, x.k2), __builtin_bit_cast(v4i16_t, qb2[qt]), c, 0, 0, 0); };
  auto pv = [&](const u32x4 v, const f32x16 p, int hi, f32x16 c) {
    const f32x8 ph = hi ? __builtin_shufflevector(p, p, 8, 9, 10, 11, 12, 13, 14, 15) : __builtin_shufflevector(p, p, 0, 1, 2, 3, 4, 5, 6, 7);
    return __builtin_amdgcn_smfmac_f32_32x32x32_bf16(__builtin_bit_cast(hbf16x8, v), __builtin_bit_cast(hbf16x16, ph), c, odd, 0, 0);
  };
  auto tile = [&](const KvT& x, auto MODE) {
    f32x16 sa[2];
#pragma unroll
    for (int qt = 0; qt < 2; ++qt) sa[qt] = qk1(x, qt);
#pragma unroll
    for (int qt = 0; qt < 2; ++qt) sa[qt] = qk2(x, qt, sa[qt]);
#pragma unroll
    for (int qt = 0; qt < 2; ++qt) {
      if (MODE != 0) {
        float mx = fmx(fmx(sa[qt][0], sa[qt][1]), sa[qt][2]);
#pragma unroll
        for (int i = 3; i < 15; i += 2) mx = fmx(fmx(mx, sa[qt][i]), sa[qt][i + 1]);
        mx = fmx(mx, sa[qt][15]);
        if (MODE == 1 || !wave_all(mx <= ATT_THR)) {
          mx = x32mx(mx);
          if (MODE != 1) mx = fmx(mx, 0.f);
          const float mn = m[qt] + mx;
          const unsigned h1 = f2bf(mn); const float r1 = mn - bf2f(h1);
          const unsigned h2 = f2bf(r1); const float r2 = r1 - bf2f(h2);
          const unsigned h3 = f2bf(r2);
          const float me = (bf2f(h1) + bf2f(h2)) + bf2f(h3);
          const float dm = me - m[qt];
          m[qt] = me;
          if (hh) qb2[qt] = (u32x2){(h1 | (h2 << 16)) ^ 0x80008000u, h3 ^ 0x8000u};
          const float alpha = __builtin_amdgcn_exp2f(-dm);
#pragma unroll
          for (int i = 0; i < 16; ++i) { if (MODE != 1) o[qt][i] *= alpha; sa[qt][i] -= dm; }
        }
      }
#pragma unroll
      for (int i = 0; i < 16; ++i) sa[qt][i] = __builtin_amdgcn_exp2f(sa[qt][i]);
      asm volatile("" : "+v"(sa[qt]));
      if (MODE == 1) { o[qt] = Z16; asm volatile("" : "+v"(o[qt])); }
      o[qt] = pv(x.v1, sa[qt], 1, pv(x.v0, sa[qt], 0, o[qt]));
      __builtin_amdgcn_sched_barrier(0);
    }
  };
  typedef std::integral_constant<int, 1> I1; typedef std::integral_constant<int, 2> I2;
  auto reset = [&]() {
    asm volatile("" : "+v"(ok1), "+v"(ok2), "+v"(ov) :: "memory");
    m[0] = m[1] = 0.f;
    if (hh) qb2[0] = qb2[1] = (u32x2){0u, 0u};
  };
  auto slow = [&]() {
    reset();
    tile(fetch(0), I1{});
#pragma unroll 1
    for (int t = 1; t < KQ / 32; ++t) tile(fetch(t), I2{});
  };
  f32x16 sc[2];
  auto phase = [&](int qt, const u32x4 vp, const KvT& xk, const u32x4 v0, auto mid) {
    auto ex = [&](int i0, int i1) {
#pragma unroll
      for (int i = i0; i < i1; ++i) sc[qt][i] = __builtin_amdgcn_exp2f(sc[qt][i]);
      asm volatile("" : "+v"(sc[qt]));
      __builtin_amdgcn_sched_barrier(0);
    };
    o[1 - qt] = pv(vp, sc[1 - qt], 1, o[1 - qt]);
    __builtin_amdgcn_sched_barrier(0);
    ex(0, 3);
    sc[1 - qt] = qk1(xk, 1 - qt);
    __builtin_amdgcn_sched_barrier(0);
    ex(3, 6);
    sc[1 - qt] = qk2(xk, 1 - qt, sc[1 - qt]);
    __builtin_amdgcn_sched_barrier(0);
    ex(6, 10);
    o[qt] = pv(v0, sc[qt], 0, o[qt]);
    __builtin_amdgcn_sched_barrier(0);
    mid();
    ex(10, 16);
  };
  auto fast = [&]() {
    reset();
    KvT xa = fetch(0), xb = fetch(1);
    tile(xa, I1{});
    fetch_k(xa, 2);
    sc[0] = qk2(xb, 0, qk1(xb, 0)); sc[1] = Z16;
    asm volatile("" : "+v"(sc[1]));
    auto step = [&](KvT& x, KvT& y, int t) {
      phase(0, y.v1, x, x.v0, [&]() { fetch_v(y, t + 1); fetch_k(x, t + 2); });
      phase(1, x.v1, y, x.v0, []() {});
    };
    step(xb, xa, 1);
#pragma unroll 1
    for (int t = 2; t < KQ / 32; t += 2) {
      if (t == ATT_P1) __builtin_amdgcn_s_setprio(2);
      if (t == ATT_P2) __builtin_amdgcn_s_setprio(1);
      if (t == ATT_P3) __builtin_amdgcn_s_setprio(0);
      step(xa, xb, t);
      step(xb, xa, t + 1);
    }
    o[1] = pv(xb.v1, sc[1], 1, o[1]);
  };
  if (ATT_P1) __builtin_amdgcn_s_setprio(3);
  fast();
  {
    float big = 0.f;
#pragma unroll
    for (int qt = 0; qt < 2; ++qt) {
      big = fmx(big, hh ? o[qt][8] * (3.0e38f / ATT_LMAX) : __builtin_fabsf(o[qt][8]));
#pragma unroll
      for (int i = 0; i < 12; ++i) if (i != 8) big = fmx(big, (i > 8 && hh) ? 0.f : __builtin_fabsf(o[qt][i]));
    }
    if (__builtin_expect(!wave_all(big <= 3.0e38f), 0)) slow();
  }
  f32x4 (*mo)[2][3][64] = mo_[wid % ATT_NQ];
  float (*mm)[2][64] = mm_[wid % ATT_NQ];
#pragma unroll
  for (int qt = 0; qt < 2; ++qt) {
#pragma unroll
    for (int k = 0; k < 3; ++k) mo[kh][qt][k][lane] = (f32x4){o[qt][4 * k], o[qt][4 * k + 1], o[qt][4 * k + 2], o[qt][4 * k + 3]};
    mm[kh][qt][lane] = m[qt];
  }
  const int qt = kh & 1, grp = kh >> 1, row = q0 + qt * 32 + r, k0 = hd * BD + 4 * hh + 8 * grp;
  auto oaddr = [&](int k) { return O + (((size_t)(row >> 4) * 20 + (k >> 5)) * 64 + ((k & 31) >> 3) * 16 + (row & 15)) * 8 + (k & 7); };
  u16 *op0 = oaddr(k0), *op1 = oaddr(k0 + 16);
  asm volatile("" : "+v"(op0), "+v"(op1));
  asm volatile("s_waitcnt lgkmcnt(0)\n\ts_barrier" ::: "memory");
  {
    float a[4], l[4];
    f32x4 u[4], x[4];
#pragma unroll
    for (int i = 0; i < 4; ++i) { a[i] = mm[i][qt][lane]; l[i] = mo[i][qt][2][lane | 32][0]; u[i] = mo[i][qt][grp][lane]; x[i] = mo[i][qt][2][lane]; }
    const float mn = fmx(fmx(a[0], a[1]), fmx(a[2], a[3]));
#pragma unroll
    for (int i = 0; i < 4; ++i) a[i] = __builtin_amdgcn_exp2f(a[i] - mn);
    const float inv = __builtin_amdgcn_rcpf((l[0] * a[0] + l[1] * a[1]) + (l[2] * a[2] + l[3] * a[3]));
    const f32x4 v = ((u[0] * a[0] + u[1] * a[1]) + (u[2] * a[2] + u[3] * a[3])) * inv;
    *(GAS u32x2*)op0 = (u32x2){pack2(v[0], v[1]), pack2(v[2], v[3])};
    if (grp == 0) {
      const f32x4 y = ((x[0] * a[0] + x[1] * a[1]) + (x[2] * a[2] + x[3] * a[3])) * inv;
      if (hh == 0) *(GAS u32x2*)op1 = (u32x2){pack2(y[0], y[1]), pack2(y[2], y[3])};
    }
  }
}
#if defined(__HIP_DEVICE_COMPILE__)
#pragma clang attribute pop
#endif

__global__ __launch_bounds__(640, 3) void proj_kernel(const u16* __restrict__ Of, const u16* __restrict__ Wf, const u16* __restrict__ xold,
                                                      u16* __restrict__ xnew) {
  __shared__ __attribute__((aligned(16))) char ob[40960];
  const int tid = threadIdx.x, lane = tid & 63, w = __builtin_amdgcn_readfirstlane(tid >> 6), g = lane >> 4, c = lane & 15;
  __shared__ unsigned sink[64];
  own_code<1500>(tid, sink);
  const int rb = (blockIdx.x & 7) + 8 * (blockIdx.x >> 5), cb = (blockIdx.x >> 3) & 3;
  const int col = cb * 160 + w * 16 + 4 * g;
  const u16* wp = Wf + (size_t)(cb * 10 + w) * 20 * 512 + lane * 8;
  u32x4 st[4];
#pragma unroll
  for (int i = 0; i < 4; ++i) st[i] = *(const u32x4*)(Of + (size_t)rb * 40 * 512 + (i * 640 + tid) * 8);
  bf16x8 wf[20];
#pragma unroll
  for (int ks = 0; ks < 20; ++ks) wf[ks] = *(const bf16x8*)(wp + (size_t)ks * 512);
  u32x2 xo[2];
#pragma unroll
  for (int t = 0; t < 2; ++t) xo[t] = *(const u32x2*)(xold + (((size_t)(rb * 2 + t) * 20 + (col >> 5)) * 64 + ((col & 31) >> 3) * 16 + c) * 8 + (col & 7));
#pragma unroll
  for (int i = 0; i < 4; ++i) *(u32x4*)(ob + (i * 640 + tid) * 16) = st[i];
  BAR();
  f32x4 acc[2] = {{0.f, 0.f, 0.f, 0.f}, {0.f, 0.f, 0.f, 0.f}};
#pragma unroll
  for (int ks = 0; ks < 20; ++ks)
#pragma unroll
    for (int t = 0; t < 2; ++t) acc[t] = mfma32(wf[ks], *(const bf16x8*)(ob + (t * 20 + ks) * 1024 + lane * 16), acc[t]);
#pragma unroll
  for (int t = 0; t < 2; ++t) {
    const unsigned r01 = pack2(acc[t][0], acc[t][1]), r23 = pack2(acc[t][2], acc[t][3]);
    const unsigned p01 = pack2(u2f(xo[t][0] << 16) + u2f(r01 << 16), u2f(xo[t][0] & 0xffff0000u) + u2f(r01 & 0xffff0000u));
    const unsigned p23 = pack2(u2f(xo[t][1] << 16) + u2f(r23 << 16), u2f(xo[t][1] & 0xffff0000u) + u2f(r23 & 0xffff0000u));
    *(u32x2*)(xnew + (size_t)(rb * 32 + t * 16 + c) * DM + col) = (u32x2){p01, p23};
  }
}

extern "C" void kernel_launch(void* const* d_in, const int* in_sizes, int n_in, void* d_out, int out_size, void* d_ws,
                              size_t ws_size, hipStream_t stream) {
  const u16* A_x = (const u16*)d_in[0];
  const u16* A_mem0 = (const u16*)d_in[1];
  const u16* A_Wq = (const u16*)d_in[2];
  const u16* A_Wk = (const u16*)d_in[3];
  const u16* A_Wv = (const u16*)d_in[4];
  const u16* A_Wo = (const u16*)d_in[5];
  const u16* B_wqkv = (const u16*)d_in[8];
  const u16* B_wo = (const u16*)d_in[9];
  char* ws = (char*)(((uintptr_t)d_ws + ((size_t)WS_OFF << 20) + WS_AL - 1) & ~(uintptr_t)(WS_AL - 1));
  u16* xa = (u16*)(ws + OFF_XA); u16* Ob = (u16*)(ws + OFF_OB);

  prologue_kernel<<<256, 640, 0, stream>>>(ProArgs{A_x, A_Wq, A_Wk, A_Wv, A_Wo, B_wqkv, B_wo, ws});
  for (int layer = 0; layer < 2; ++layer) {
    rec_kernel<<<256, 512, 0, stream>>>(RecArgs{A_mem0, A_Wq, A_Wk, (const u16*)(ws + OFF_XB), xa, ws, (unsigned)layer, 0u});
    attb_kernel<<<BH * 32 / ATT_NQ, 256 * ATT_NQ, 0, stream>>>((const u16*)(ws + OFF_QB), (const u16*)(ws + OFF_KV), Ob);
  }
  proj_kernel<<<256, 640, 0, stream>>>(Ob, (const u16*)(ws + OFF_WOBT), xa, (u16*)d_out);
}
```

```cpp
#include <hip/hip_runtime.h>
#include <hip/hip_bf16.h>
#include <stdint.h>
#include <type_traits>

typedef unsigned short u16;
typedef unsigned long long u64;
typedef __attribute__((ext_vector_type(8))) short bf16x8;
typedef __attribute__((ext_vector_type(16))) short bf16x16;
typedef __attribute__((ext_vector_type(4))) float f32x4;
typedef __attribute__((ext_vector_type(2))) float f32x2;
typedef __attribute__((ext_vector_type(16))) float f32x16;
typedef __attribute__((ext_vector_type(8))) float f32x8;
typedef __attribute__((ext_vector_type(4))) unsigned u32x4;
typedef __attribute__((ext_vector_type(2))) unsigned u32x2;
typedef __attribute__((ext_vector_type(3))) unsigned u32x3;
typedef __attribute__((ext_vector_type(8))) unsigned u32x8;
typedef __attribute__((ext_vector_type(16))) unsigned u32x16;
typedef __attribute__((ext_vector_type(32))) unsigned u32x32;
#define GAS __attribute__((address_space(1)))
#define LAS __attribute__((address_space(3)))
typedef GAS unsigned long long gu64;
#define BAR() do { __builtin_amdgcn_s_waitcnt(0xC07F); asm volatile("s_waitcnt lgkmcnt(0)\n\ts_barrier" ::: "memory"); } while (0)

constexpr int S_ = 2048, DM = 640;
constexpr int NH = 32, DH = 192, HD = NH * DH;
constexpr int MEM = 8, SEG = 32, NSEG = S_ / SEG;
constexpr int BH = 32, BD = 20;

constexpr size_t al256(size_t x) { return (x + 255) & ~(size_t)255; }
constexpr int LAG3 = 2, LAG4 = 3, LAG5 = 6;
#define SR_ 8
constexpr int SR = SR_;
constexpr unsigned SCT_STEP = 131072, OFF_SCM = SR * SCT_STEP, SCM_STEP = 32768;
constexpr int ACC_NB = 192;
constexpr size_t ACC_STEP = 65536;
constexpr unsigned ACC_TS = ACC_NB * 256;
constexpr size_t ACC_LAYER = SR * ACC_STEP;
constexpr size_t OFF_ACC = 3 * 524288;
static_assert(SR == 8, "regions");
__device__ __forceinline__ unsigned ts_piece(int i) { return ACC_TS + (unsigned)(i < 128 ? (i >> 1) * 256 + (i & 1) * 16 : 128 + (i & 1) * 8192); }
constexpr size_t OFF_LNX = OFF_SCM + SR * SCM_STEP;
constexpr size_t OFF_RDY = OFF_LNX + 65536;
constexpr size_t PG = 2097152;
#define RING_ 4
constexpr int RING = RING_;
constexpr size_t TS_SLOT = (size_t)NH * 8 * 2560, PT_SLOT = (size_t)NH * 2560, OP_SLOT = (size_t)256 * 2560;
__device__ __forceinline__ unsigned ts_tok(int h, int j) { return (unsigned)((h & 15) * 32768 + (j >> 2) * 16384 + (j & 3) * 2048); }
__device__ __forceinline__ unsigned ts_mem(int h, int j) { return (unsigned)(524288 + ((h & 15) >> 1) * 16384 + (h & 1) * 4096 + j * 512); }
__device__ __forceinline__ unsigned pt_tok(int h) { return (unsigned)(((h & 15) >> 2) * 16384 + (h & 3) * 2048); }
__device__ __forceinline__ unsigned pt_mem(int h) { return (unsigned)(65536 + (h & 15) * 512); }
constexpr size_t OFF_X = 2 * PG + TS_SLOT + 3 * PT_SLOT, X_BYTES = 7 * OP_SLOT + 4 * PT_SLOT;
constexpr size_t OFF_OP = OFF_X + TS_SLOT + 4 * PT_SLOT;
constexpr size_t COMM_BYTES = OFF_X + X_BYTES + RING * OP_SLOT;
__device__ __forceinline__ unsigned near_half(unsigned ck, unsigned o) { return o + ((ck >> (o >= 2 * PG ? 8 : 4)) & 8192u); }
__device__ __forceinline__ unsigned ts_slot(unsigned ck, int sl) {
  return near_half(ck, sl == NSEG - 1 ? (unsigned)OFF_X : ((sl & 3) == 3 ? (unsigned)(2 * PG) : (unsigned)PG + (unsigned)(sl & 3) * (unsigned)TS_SLOT));
}
__device__ __forceinline__ unsigned pt_slot(unsigned ck, int sl) {
  return near_half(ck, sl >= NSEG - 4 ? (unsigned)(OFF_X + TS_SLOT) + (unsigned)(sl - (NSEG - 4)) * (unsigned)PT_SLOT
                                      : ((sl & 3) == 0 ? (unsigned)(PG + 3 * TS_SLOT) : (unsigned)(2 * PG + TS_SLOT - PT_SLOT) + (unsigned)(sl & 3) * (unsigned)PT_SLOT));
}
__device__ __forceinline__ unsigned op_slot(int sl) { return (unsigned)OFF_OP + (unsigned)(sl >= NSEG - 6 ? sl - (NSEG - 6) : 6 + sl % RING) * (unsigned)OP_SLOT; }
static_assert(RING == 4 && OFF_OP <= 3 * PG && OFF_RDY + 2048 <= OFF_ACC && OFF_ACC + ACC_LAYER == PG && TS_SLOT % 16384 == 0 && PT_SLOT % 16384 == 0, "pages");
static_assert(TS_SLOT == OP_SLOT && RING >= 4 && NSEG % SR == 0 && SR >= 5 && X_BYTES % 4096 == 0 && X_BYTES <= 256 * 20 * 1024, "slots");
constexpr size_t OFF_WB = (COMM_BYTES + 16383) / 16384 * 16384, WB_BYTES = (size_t)256 * 200 * 1024;
__device__ __forceinline__ unsigned wb_off(int F) { const unsigned o = (unsigned)((F >> 3) * 16384 + (F & 7) * 1024); return o | ((((unsigned)OFF_WB + o) >> 8) & 8192u); }
constexpr size_t OFF_PHI = OFF_WB + WB_BYTES;
constexpr size_t OFF_BNRM = OFF_PHI + 1024;
constexpr size_t OFF_LAT = OFF_BNRM + 1024;
constexpr size_t OFF_XNRM = OFF_LAT + 2048;
constexpr size_t OFF_WQKVBT = OFF_XNRM + al256(S_ * 16);
constexpr size_t OFF_WOBT = OFF_WQKVBT + al256((size_t)3 * DM * DM * 2);
constexpr size_t OFF_XN = OFF_WOBT + al256((size_t)DM * DM * 2);
constexpr int QST = BD;
constexpr size_t OFF_XA = OFF_XN + al256((size_t)S_ * DM * 2);
constexpr size_t OFF_XB = OFF_XA + al256((size_t)S_ * DM * 2);
constexpr size_t OFF_QB = OFF_XB + al256((size_t)S_ * DM * 2);
constexpr int KVT = 2624;
constexpr size_t OFF_KV = OFF_QB + al256((size_t)BH * S_ * QST * 2);
constexpr size_t OFF_OB = OFF_KV + al256((size_t)BH * 64 * KVT);
constexpr size_t OFF_DBG = OFF_OB + al256((size_t)S_ * DM * 2);
constexpr size_t WS_TOTAL = OFF_DBG + (size_t)2 * 64 * 8 * DM * 2;
#define WS_AL 2097152
#define WS_OFF 0
static_assert(WS_TOTAL + WS_AL + ((size_t)WS_OFF << 20) <= ((size_t)256 << 20), "workspace budget");

#if defined(__HIP_DEVICE_COMPILE__)
#pragma clang attribute push(__attribute__((target("no-packed-fp32-ops"))), apply_to = function)
#endif
__device__ __forceinline__ float u2f(unsigned v) { return __builtin_bit_cast(float, v); }
__device__ __forceinline__ unsigned f2u(float v) { return __builtin_bit_cast(unsigned, v); }
__device__ __forceinline__ float bf2f(unsigned v) { return __builtin_bit_cast(float, v << 16); }
__device__ __forceinline__ unsigned f2bf(float f) { __bf16 b = (__bf16)f; return (unsigned)__builtin_bit_cast(u16, b); }
__device__ __forceinline__ float rbf(float f) { return bf2f(f2bf(f)); }
typedef __attribute__((ext_vector_type(2))) __bf16 hbf16x2;
__device__ __forceinline__ unsigned pack2(float lo, float hi) { f32x2 v = {lo, hi}; hbf16x2 b = __builtin_convertvector(v, hbf16x2); return __builtin_bit_cast(unsigned, b); }
__device__ __forceinline__ f32x4 mfma32(bf16x8 a, bf16x8 b, f32x4 c) { return __builtin_amdgcn_mfma_f32_16x16x32_bf16(a, b, c, 0, 0, 0); }
typedef __attribute__((ext_vector_type(8))) __bf16 hbf16x8;
typedef __attribute__((ext_vector_type(16))) __bf16 hbf16x16;
__device__ __forceinline__ f32x4 smfma64(bf16x8 a, bf16x16 b, f32x4 c, int idx) {
  return __builtin_amdgcn_smfmac_f32_16x16x64_bf16(__builtin_bit_cast(hbf16x8, a), __builtin_bit_cast(hbf16x16, b), c, idx, 0, 0);
}
__device__ __forceinline__ bf16x16 cat16(bf16x8 a, bf16x8 b) { return __builtin_shufflevector(a, b, 0, 1, 2, 3, 4, 5, 6, 7, 8, 9, 10, 11, 12, 13, 14, 15); }
__device__ __forceinline__ bf16x8 half8(bf16x16 v, int hi) { return hi ? __builtin_shufflevector(v, v, 8, 9, 10, 11, 12, 13, 14, 15) : __builtin_shufflevector(v, v, 0, 1, 2, 3, 4, 5, 6, 7); }
__device__ __forceinline__ bf16x8 mk8(unsigned a, unsigned b, unsigned c, unsigned d) { u32x4 v = {a, b, c, d}; return __builtin_bit_cast(bf16x8, v); }
__device__ __forceinline__ unsigned dpp_ror8(unsigned v) { return (unsigned)__builtin_amdgcn_mov_dpp((int)v, 0x128, 0xf, 0xf, true); }
__device__ __forceinline__ float fmx(float a, float b) { return __builtin_elementwise_maximum(a, b); }

__device__ __forceinline__ int imax(int a, int b) { return a > b ? a : b; }
__device__ __forceinline__ int imin(int a, int b) { return a < b ? a : b; }
__device__ __forceinline__ unsigned umin(unsigned a, unsigned b) { return a < b ? a : b; }
__device__ __forceinline__ unsigned umax(unsigned a, unsigned b) { return a > b ? a : b; }
__device__ __forceinline__ bool wave_all(bool p) { return __builtin_amdgcn_ballot_w64(!p) == 0ull; }
__device__ __forceinline__ float x16mx(float x) { unsigned u = __builtin_bit_cast(unsigned, x); auto r = __builtin_amdgcn_permlane16_swap(u, u, false, false); return fmx(u2f(r[0]), u2f(r[1])); }
__device__ __forceinline__ float x32mx(float x) { unsigned u = __builtin_bit_cast(unsigned, x); auto r = __builtin_amdgcn_permlane32_swap(u, u, false, false); return fmx(u2f(r[0]), u2f(r[1])); }
__device__ __forceinline__ float x16sum(float x) { unsigned u = __builtin_bit_cast(unsigned, x); auto r = __builtin_amdgcn_permlane16_swap(u, u, false, false); return u2f(r[0]) + u2f(r[1]); }
__device__ __forceinline__ float x32sum(float x) { unsigned u = __builtin_bit_cast(unsigned, x); auto r = __builtin_amdgcn_permlane32_swap(u, u, false, false); return u2f(r[0]) + u2f(r[1]); }
template <int CTRL> __device__ __forceinline__ float dppf(float v) { return u2f((unsigned)__builtin_amdgcn_mov_dpp((int)f2u(v), CTRL, 0xf, 0xf, true)); }
#define OWN_PF 1
template <int BYTES> __device__ __forceinline__ void own_code(int tid, unsigned* sink) {
  if (!OWN_PF || __builtin_amdgcn_readfirstlane(tid >> 6) * 4096 >= BYTES) return;
  const char* pc; unsigned keep;
  asm volatile("s_getpc_b64 %0" : "=s"(pc));
  asm volatile("s_mov_b32 %0, m0\n\ts_mov_b32 m0, %3\n\ts_nop 0\n\tglobal_load_lds_dword %1, %2\n\ts_mov_b32 m0, %0"
               : "=&s"(keep) : "v"(tid * 64 < BYTES ? tid * 64 : 0), "s"(pc), "s"((unsigned)(uintptr_t)(LAS unsigned*)sink) : "memory");
}
constexpr int DPP_XOR1 = 0xB1, DPP_XOR2 = 0x4E, DPP_ROR4 = 0x124, DPP_ROR8 = 0x128;
#if defined(__HIP_DEVICE_COMPILE__)
#pragma clang attribute pop
#endif

typedef short v4i16_t __attribute__((ext_vector_type(4)));
__device__ __forceinline__ bf16x8 tr_frag(const char* lo_, const char* hi_) {
  const v4i16_t lo = __builtin_amdgcn_ds_read_tr16_b64_v4i16((LAS v4i16_t*)lo_), hi = __builtin_amdgcn_ds_read_tr16_b64_v4i16((LAS v4i16_t*)hi_);
  return (bf16x8){lo[0], lo[1], lo[2], lo[3], hi[0], hi[1], hi[2], hi[3]};
}
__device__ __forceinline__ int rec_wg(int gp, int m) { return (((gp & 1) * 16 + m) << 3) | (gp >> 1); }
__device__ __forceinline__ float wsum(float v) {
  v += dppf<DPP_XOR1>(v); v += dppf<DPP_XOR2>(v); v += dppf<DPP_ROR4>(v); v += dppf<DPP_ROR8>(v);
  return x32sum(x16sum(v));
}
__device__ __forceinline__ float ln_row(int lane, u32x4 (&xo)[2]) {
  const bool two = lane < 16;
  float v[16];
#pragma unroll
  for (int hf = 0; hf < 2; ++hf)
#pragma unroll
    for (int i = 0; i < 4; ++i) { const unsigned u = xo[hf][i]; v[hf * 8 + 2 * i] = bf2f(u & 0xffff); v[hf * 8 + 2 * i + 1] = bf2f(u >> 16); }
  float s0 = 0.f, s1 = 0.f;
#pragma unroll
  for (int i = 0; i < 8; ++i) { s0 += v[i]; s1 += v[8 + i]; }
  const float mean = wsum(two ? s0 + s1 : s0) * (1.0f / DM);
  s0 = s1 = 0.f;
#pragma unroll
  for (int i = 0; i < 8; ++i) { const float d0 = v[i] - mean, d1 = v[8 + i] - mean; s0 += d0 * d0; s1 += d1 * d1; }
  const float var = rbf(wsum(two ? s0 + s1 : s0) * (1.0f / DM));
  const float mb = rbf(mean);
  const float rden = 1.0f / rbf(sqrtf(rbf(var + 1e-5f)));
  float n2[2] = {0.f, 0.f};
#pragma unroll
  for (int hf = 0; hf < 2; ++hf) {
    float y[8];
#pragma unroll
    for (int i = 0; i < 8; ++i) { y[i] = rbf(rbf(v[hf * 8 + i] - mb) * rden); n2[hf] += y[i] * y[i]; }
    xo[hf] = (u32x4){pack2(y[0], y[1]), pack2(y[2], y[3]), pack2(y[4], y[5]), pack2(y[6], y[7])};
  }
  return wsum(two ? n2[0] + n2[1] : n2[0]);
}

#define PROT(k)
#define WARM_CODE 1
#define B_AUX 16
#define X_AUX 16
#define T_AUX 16
#define WB_NEAR 1
#define PF_POS 0
#define PRB_N0 3
#define PRB_N1 0
#define PRB_LOG(i, v)
#define PLD(p) __builtin_nontemporal_load((const u32x4*)(p))
struct ProArgs { const u16* x; const u16* wq; const u16* wk; const u16* wv; const u16* wo; const u16* wqkvb; const u16* wob; char* ws; };
__global__ __launch_bounds__(640, 3) void prologue_kernel(ProArgs a) {
  __shared__ __attribute__((aligned(16))) char img[192 * 320];
  __shared__ __attribute__((aligned(16))) char slots[10 * 6144];
  __shared__ float red[10][160];
  __shared__ unsigned latw[10];
  __shared__ u32x4 xst[2][640];
  const int b = blockIdx.x, tid = threadIdx.x, lane = tid & 63, v = __builtin_amdgcn_readfirstlane(tid >> 6), c = lane & 15, g = lane >> 4;
  const int h = (b & 7) * 4 + ((b >> 3) & 3), dh = (b >> 5) & 1, cq = b >> 6, ks = dh * 10 + v;
  char* const slot = slots + v * 6144;
  PROT(0);
  __shared__ unsigned sink[64];
  own_code<9600>(tid, sink);
  const unsigned zo = v < 8 ? (unsigned)(((b >> 6) * 512 + ((b & 7) * 2 + (v >> 2)) * 32 + ((b >> 3) & 7) * 4 + (v & 3)) * 1024) : (unsigned)(((v - 7) * 2048 + (b & 7) * 32 + ((b >> 3) & 7) * 4 + (b >> 6)) * 1024);
  __builtin_amdgcn_raw_buffer_store_b128((u32x4){0u, 0u, 0u, 0u}, __builtin_amdgcn_make_buffer_rsrc(a.ws, 0, (int)(3 * PG), 0x00020000), zo + lane * 16, 0, 16);
  __builtin_amdgcn_sched_barrier(0);
  u32x4 wa[2][6], wb[6], tr[4], xo[2];
  const int xrow = b * 8 + (v & 7);
  xo[0] = *(const u32x4*)(a.x + (size_t)xrow * DM + lane * 8);
  xo[1] = *(const u32x4*)(a.x + (size_t)xrow * DM + 512 + c * 8);
  const int job = imin(b + 256 * (9 - v), 800);
  const int jn = job < 600 ? job / 20 : (job % 200) / 20, jk = job % 20;
  {
    const u16* src = job < 600 ? a.wqkvb + (size_t)(32 * jk) * 3 * DM : a.wob + (size_t)(32 * jk) * DM;
#pragma unroll
    for (int i = 0; i < 4; ++i) tr[i] = *(const u32x4*)(src + (size_t)(i * 8 + (lane >> 3)) * (job < 600 ? 3 * DM : DM) + jn * 64 + (lane & 7) * 8);
  }
#pragma unroll
  for (int i = 0; i < 6; ++i) {
    const int idx = (v * 6 + i) * 64 + lane, row = idx / 20;
    wb[i] = PLD(a.wo + (size_t)(h * DH + row) * DM + cq * 160 + (idx - row * 20) * 8);
  }
#pragma unroll
  for (int i = 0; i < 6; ++i) {
    const int idx = i * 64 + lane, r = idx / 24;
#pragma unroll
    for (int u = 0; u < 2; ++u) wa[u][i] = PLD(a.wv + (size_t)(32 * ks + 8 * (r >> 2) + (r & 3) + 4 * u) * HD + h * DH + (idx - r * 24) * 8);
  }
  unsigned pf[2] = {0u, 0u};
  auto prefetch = [&]() {
#pragma unroll
    for (int i = 0; i < 2; ++i) pf[i] = *(const unsigned*)((i ? a.wq : a.wk) + ((size_t)(b * 10 + v) * 48 + imin(lane, 47)) * 32);
  };
  if (PF_POS == 0) prefetch();
  __builtin_amdgcn_sched_barrier(0);
  unsigned lat = ~0u;
  auto probe = [&](int n0, int n) {
    const char* p = a.ws + zo;
#pragma unroll 1
    for (int i = n0; i < n0 + n; ++i) {
      u64 t0, t1; unsigned d;
      asm volatile("s_memtime %0\n\ts_waitcnt lgkmcnt(0)\n\ts_load_dword %2, %3, %4\n\ts_waitcnt lgkmcnt(0)\n\ts_memtime %1\n\ts_waitcnt lgkmcnt(0)"
                   : "=&s"(t0), "=&s"(t1), "=&s"(d) : "s"(p), "s"((i & 3) * 256 + (i >> 2) * 128) : "memory");
      PRB_LOG(i, (unsigned)t1 - (unsigned)t0);
      lat = umin(lat, (unsigned)t1 - (unsigned)t0);
    }
  };
  asm volatile("s_waitcnt vmcnt(26)" ::: "memory");
  probe(0, PRB_N0);
  latw[v] = lat;
  __builtin_amdgcn_sched_barrier(0);
  auto xput = [&](int k) {
    if (v < 8) { xst[k][lane * 8 + (v ^ (lane & 7))] = xo[0]; if (lane < 16) xst[k][(64 + lane) * 8 + (v ^ (lane & 7))] = xo[1]; }
  };
  xput(0);
  const float xn2 = u2f(__builtin_amdgcn_readfirstlane(f2u(ln_row(lane, xo))));
  xput(1);
#pragma unroll
  for (int i = 0; i < 4; ++i) *(u32x4*)(slot + (i * 8 + (lane >> 3)) * 144 + (lane & 7) * 16) = tr[i];
  __builtin_amdgcn_fence(__ATOMIC_RELEASE, "wavefront"); __builtin_amdgcn_wave_barrier();
  bf16x8 tf[4];
#pragma unroll
  for (int t = 0; t < 4; ++t) {
    const char* bp = slot + (8 * g + (c >> 2)) * 144 + t * 32 + (c & 3) * 8;
    tf[t] = tr_frag(bp, bp + 4 * 144);
  }
  __builtin_amdgcn_fence(__ATOMIC_RELEASE, "wavefront"); __builtin_amdgcn_wave_barrier();
  __builtin_amdgcn_sched_barrier(0);
#pragma unroll
  for (int i = 0; i < 6; ++i) {
    const int idx = (v * 6 + i) * 64 + lane, row = idx / 20;
    *(u32x4*)(img + row * 320 + (((idx - row * 20) ^ ((row >> 2) & 2)) * 16)) = wb[i];
  }
  bf16x8 A[2][6];
#pragma unroll
  for (int u = 0; u < 2; ++u) {
#pragma unroll
    for (int i = 0; i < 6; ++i) {
      const int idx = i * 64 + lane, r = idx / 24;
      *(u32x4*)(slot + r * 384 + (((idx - r * 24) ^ (r >> 1)) * 16)) = wa[u][i];
    }
    __builtin_amdgcn_fence(__ATOMIC_RELEASE, "wavefront"); __builtin_amdgcn_wave_barrier();
#pragma unroll
    for (int e = 0; e < 6; ++e) A[u][e] = *(const bf16x8*)(slot + c * 384 + (((4 * e + g) ^ (c >> 1)) * 16));
    __builtin_amdgcn_fence(__ATOMIC_RELEASE, "wavefront"); __builtin_amdgcn_wave_barrier();
  }
  BAR();
  PROT(1);
  if (PF_POS == 0) asm volatile("" :: "v"(pf[0]), "v"(pf[1]));
  if (PF_POS == 1) prefetch();
  {
    const unsigned xf = (unsigned)((b >> 1) * 20480 + (tid >> 3) * 256 + (b & 1) * 128 + (tid & 7) * 16);
    const __amdgpu_buffer_rsrc_t rx = __builtin_amdgcn_make_buffer_rsrc(a.ws + OFF_XN, 0, (int)(OFF_QB - OFF_XN), 0x00020000);
    __builtin_amdgcn_raw_buffer_store_b128(xst[1][tid ^ ((tid >> 3) & 7)], rx, xf, 0, X_AUX);
    __builtin_amdgcn_raw_buffer_store_b128(xst[0][tid ^ ((tid >> 3) & 7)], rx, xf + (unsigned)(OFF_XB - OFF_XN), 0, X_AUX);
    if (v < 8 && lane < 4) ((float*)(a.ws + OFF_XNRM))[xrow * 4 + lane] = lane == 0 ? xn2 : 0.f;
  }
  if (job < 800) {
    const __amdgpu_buffer_rsrc_t rt = __builtin_amdgcn_make_buffer_rsrc(a.ws + (job < 600 ? OFF_WQKVBT : OFF_WOBT) + ((size_t)jn * 80 + jk) * 1024, 0, 81920, 0x00020000);
#pragma unroll
    for (int t = 0; t < 4; ++t) __builtin_amdgcn_raw_buffer_store_b128(__builtin_bit_cast(u32x4, tf[t]), rt, lane * 16 + t * 20480, 0, T_AUX);
  }
  const int gp = h >> 1, hh = h & 1;
  const __amdgpu_buffer_rsrc_t rwb = __builtin_amdgcn_make_buffer_rsrc(a.ws + OFF_WB, 0, (int)WB_BYTES, 0x00020000);
  const unsigned phi = WB_NEAR ? ((((unsigned)b >> 4) & 1u) ^ (unsigned)(latw[8] < latw[9]) ^ (WB_NEAR - 1)) << 13 : (unsigned)(b & 32) << 8;
  if (tid == 0) ((unsigned*)(a.ws + OFF_PHI))[b] = phi;
#pragma unroll
  for (int n = 0; n < 10; ++n) {
    const int off = n * 32 + 8 * (c & 3), T = cq * 10 + n;
    const char* bp = img + (8 * g + (c >> 2)) * 320 + (off ^ ((g & 1) << 5));
    f32x4 ac[2] = {{0.f, 0.f, 0.f, 0.f}, {0.f, 0.f, 0.f, 0.f}};
#pragma unroll
    for (int e = 0; e < 6; ++e) {
      const bf16x8 bf = tr_frag(bp + e * 32 * 320, bp + (e * 32 + 4) * 320);
      ac[0] = mfma32(A[0][e], bf, ac[0]); ac[1] = mfma32(A[1][e], bf, ac[1]);
    }
    const u32x4 o = {pack2(ac[0][0], ac[0][1]), pack2(ac[0][2], ac[0][3]), pack2(ac[1][0], ac[1][1]), pack2(ac[1][2], ac[1][3])};
    const unsigned oo = (T < 32 ? wb_off(rec_wg(gp, T & 15) * 100 + (hh * 3 + (T >> 4)) * 20 + ks) + (unsigned)(lane * 16)
                                : wb_off(rec_wg(gp, 2 * (T - 32) + (c >> 3)) * 100 + 40 + ks) + (unsigned)((g * 16 + hh * 8 + (c & 7)) * 16)) ^ phi;
    __builtin_amdgcn_raw_buffer_store_b128(o, rwb, oo, 0, B_AUX);
    float n2 = 0.f;
#pragma unroll
    for (int r = 0; r < 4; ++r) n2 += ac[0][r] * ac[0][r] + ac[1][r] * ac[1][r];
    n2 = x32sum(x16sum(n2));
    if (g == 0) red[v][n * 16 + c] = n2;
  }
  PROT(2);
  if (PF_POS == 1) asm volatile("" :: "v"(pf[0]), "v"(pf[1]));
  if (v >= 8 && b < 64 && lane == 0) ((unsigned*)(a.ws + OFF_LAT))[(v - 4) * 64 + b] = umin(lat, 60000u);
  if (WARM_CODE && v == 7) {
    const char* pc; int d0, d1;
    asm("s_getpc_b64 %0\n\ts_mov_b32 %1, _Z10rec_kernel7RecArgs@rel32@lo+4\n\ts_mov_b32 %2, _Z11proj_kernelPKtS0_S0_Pt@rel32@lo+12" : "=s"(pc), "=s"(d0), "=s"(d1));
    const char *c0 = pc + d0, *c1 = pc + d1;
    const long o = ((long)b * 64 + lane) * 64;
    if (o < c1 - c0) asm volatile("" :: "v"(*(const volatile GAS unsigned*)(c0 + o)));
  }
  if (v == 9 && lane < 32) {
    const int q = b * 32 + lane;
    *(u32x4*)(a.ws + OFF_KV + (size_t)(q >> 2) * KVT + 1280 + (q & 3) * 336 + 320) = (u32x4){0x3f803f80u, 0x3f803f80u, 0x3f803f80u, 0x3f803f80u};
  }
  BAR();
  if (v == 1 && lane == 0) ((unsigned*)(a.ws + OFF_LAT))[b] = umin(umin(umin(latw[0], latw[4]) + umin(latw[1], latw[5]), 30000u) + umin(umin(latw[2], latw[6]) + umin(latw[3], latw[7]), 30000u), 60000u);
  if (v == 0) {
    float mx = 0.f;
#pragma unroll
    for (int k = 0; k < 3; ++k) {
      const int col = k * 64 + lane;
      float sm = 0.f;
#pragma unroll
      for (int i = 0; i < 10; ++i) sm += red[i][col < 160 ? col : 0];
      mx = fmx(mx, sm);
    }
    mx = fmx(mx, dppf<DPP_XOR1>(mx)); mx = fmx(mx, dppf<DPP_XOR2>(mx)); mx = fmx(mx, dppf<DPP_ROR4>(mx)); mx = fmx(mx, dppf<DPP_ROR8>(mx));
    mx = x32mx(x16mx(mx));
    if (lane == 0) ((float*)(a.ws + OFF_BNRM))[(cq * 32 + h) * 2 + dh] = mx;
  }
  PROT(3);
}

struct RecArgs {
  const u16* mem0; const u16* wq; const u16* wk; const u16* xold; u16* xnew; char* ws;
  unsigned layer; unsigned pad;
};
#define RLX_AGENT __ATOMIC_RELAXED, __HIP_MEMORY_SCOPE_AGENT
constexpr unsigned SPIN_CAP = 1u << 20;
#define TSTAMP(k, v)
#define ARRIVE(k)
#define ARRIVE_OUT()
#define ARRIVE_DECL()
#define LOGV(k, v)
#define LOGF(k, v)
#define LOGP(k)
#define LOGQ(k)
#define LOGF2(k)
#define LOGD(k)
#define LOGT(k, v)
#define STAMP_DECL()
#define STAMP(k)
#define STAMPV(k, v)
#define STAMP_OUT()
#define REC_D1 520
#define SCH_V 14
#define SCH_VMIN 0
#define SCH_DMIN 4
#define SCH_PEN 150
#define SCH_VMIN2 (-4)
#define SCH_PEN2 40
#define SCH_GU 1
#define SCH_GD 2
#define SCH_UP 6
#define SCH_D0 196
#define SCH_FIRST 120
#define EXP_NODMA 0
#define BG_HOLD 220
#define QBATCH 0
#define WPF_N 5
#define WPF_W 0
#define WPF0 (63 - WPF_N)
#define BG_HOLD4 250
#define D1_W1 20
#define F3_D 1200
#define T2W 2
#define AT_ASM 1
#define PM_Z 1
#define P2_X 1
#define ST_KS 14
#define ST_KS0 ST_KS
#define ST_KS1 ST_KS
#define PADR 0x6666
#define PADT 0x000
#define CODE_PAD(n) asm volatile(".p2align 6\n.rept %0\ns_nop 0\n.endr" :: "n"(n))
#define ST_X 250
#define ST_ALL 4
#define CPF_S 61
#define FR_D 700
#define FR_PACE 6
#define END_E60 600
#define END_E1 1300
#define END_E2 3400
#define END_E62 2800
#define END_E3 6800
#define END_E4 11400
#define EARLY_N 6
#define EARLY_D1 0
#define EARLY_V 4
#define EARLY_V1 10
#define F3 0
#define F3A 0
#define F3A_D 400
#define T4_D 300
#define T4_P 1
#define P2_W 1
#define PS_SL 1
#define F2B 4
#define F0C 0
#define F2C 0
#define W2_P 1
#define SCH_W 1
#define SP_SL 0
#define D1_M 300
#define D1_MIN 260
#define D1_C 96
#define D1_G 2
#define BG_HOLD6 0
#define W2_D 0
#define END_E61 2600
#define D1_LATE 200
#define P2_G0 10
#define P2_G2 6
#define P2_G3 6
#define SCH_RL 0
#define SCH_P 3
#define MMA_P 1
#define SP_P 0
#define F6 8
#define F7 10
#define XPF_W 2
#define SCH_LATE 0
#define LATE_V 6
#define QW 3
#define FB_SL 0
#define P2_G1 1
#define P1_G 0
#define F4Z 0
#define F5Z 0
#define T5_Z 2
#define F0A 3
#define FB_E 0
#define EXACT 1
#define D1_P 3
#define VO_A0 9
#define VO_A2 10
#define VO_B0 3
#define VO_B1 0
#define VO_N2 7
#define VO_N3 10
#define VO_W -1
#define UN_P 2
#define VP_E 1
#define TS_B 0
#define ST_E 1
#define W1_SL 0
#define VO_A1 4
#define LAST_V 18
#define END_E59 3200
#define END_G2 600
#define END_G3 600
#define QR 4
#define WPF_S 128
#define END_SL 4
#define SCH_DR 182
#define SCH_DK 3
#define FR_HOLD 0
#define FW 8
#define PR_P 0
#define REL_AT 1
#define KP0 0
#define KP1 0
#define XPF 3
#define W1_D 0
#define F4 12
#define F5 0
#define F67 0
#define KP2 0
#define REC_RNG 70u
#define SCH_DRAIN 40
#define PL_X 0
#define FC(k)
#define PIN_V(x) asm volatile("" : "+v"(x))
#define PIN_S(x) asm volatile("" : "+s"(x))
#define VM_DRAIN() do { asm volatile("s_waitcnt vmcnt(0)" ::: "memory"); __builtin_amdgcn_s_waitcnt(0x0F70); } while (0)
#define SC1_LOAD(off) __builtin_amdgcn_raw_buffer_load_b128(rs, (off), 0, 16)
#define SC1_LOADS(off, soff) __builtin_amdgcn_raw_buffer_load_b128(rs, (off), (soff), 16)
#define SC1_STORE8S(v, off, soff) __builtin_amdgcn_raw_buffer_store_b64((v), rs, (off), (soff), 16)
#define SC1_LOAD8(off) __builtin_amdgcn_raw_buffer_load_b64(rs, (off), 0, 16)
#define SC1_STORE(v, off) __builtin_amdgcn_raw_buffer_store_b128((v), rs, (off), 0, 16)
#define SC1_STORE8(v, off) __builtin_amdgcn_raw_buffer_store_b64((v), rs, (off), 0, 16)
#if defined(__HIP_DEVICE_COMPILE__)
#pragma clang attribute push(__attribute__((target("no-packed-fp32-ops"))), apply_to = function)
#endif
__device__ __forceinline__ unsigned now32() { return (unsigned)__builtin_amdgcn_s_memrealtime(); }
__device__ __forceinline__ void wait_until(unsigned t) { for (;;) { const int d = (int)(t - now32()); if (d <= 0) break; if (d > 8) __builtin_amdgcn_s_sleep(1); } }
__device__ __forceinline__ unsigned cyc32() { return (unsigned)__builtin_amdgcn_s_memtime(); }
__device__ __forceinline__ void wait_cyc(unsigned t) { for (;;) { const int d = (int)(t - cyc32()); if (d <= 0) break; if (d > 200) __builtin_amdgcn_s_sleep(1); } }
#define WAIT_ASM(clk, far_) "1:\n\t" clk " vcc\n\ts_waitcnt lgkmcnt(0)\n\ts_sub_i32 %0, %1, vcc_lo\n\ts_cmp_lt_i32 %0, " far_ "\n\ts_cbranch_scc1 3f\n\ts_sleep 1\n\ts_branch 1b\n" \
                            "2:\n\t" clk " vcc\n\ts_waitcnt lgkmcnt(0)\n\ts_sub_i32 %0, %1, vcc_lo\n3:\n\ts_cmp_gt_i32 %0, 0\n\ts_cbranch_scc1 2b"
__device__ __forceinline__ int spin_until(unsigned t) { int d; asm volatile(WAIT_ASM("s_memrealtime", "9") : "=&s"(d) : "s"(t) : "vcc", "scc", "memory"); return d; }
__device__ __forceinline__ int spin_cyc(unsigned t) { int d; asm volatile(WAIT_ASM("s_memtime", "200") : "=&s"(d) : "s"(t) : "vcc", "scc", "memory"); return d; }
__device__ __forceinline__ void spin_exact(unsigned t) {
  if (!EXACT) { spin_cyc(t); return; }
  int d;
  asm volatile("1:\n\ts_memtime vcc\n\ts_waitcnt lgkmcnt(0)\n\ts_sub_i32 %0, %1, vcc_lo\n\ts_cmp_gt_i32 %0, 95\n\ts_cbranch_scc1 1b\n\t"
               "s_max_i32 %0, %0, 0\n\ts_andn2_b32 %0, %0, 3\n\ts_sub_i32 %0, 3f-2f, %0\n\ts_getpc_b64 vcc\n"
               "2:\n\ts_add_u32 vcc_lo, vcc_lo, %0\n\ts_addc_u32 vcc_hi, vcc_hi, 0\n\ts_setpc_b64 vcc\n\t.rept 24\n\ts_nop 0\n\t.endr\n3:"
               : "=&s"(d) : "s"(t) : "scc", "vcc", "memory");
}
template <int OFF, int SL> __device__ __forceinline__ void lds_spin(unsigned lds0, unsigned want) {
  unsigned v;
  asm volatile("1:\n\tds_read_b32 %0, %1 offset:%3\n\t.if %4\n\ts_sleep %4\n\t.endif\n\ts_waitcnt lgkmcnt(0)\n\tv_cmp_gt_u32 vcc, %2, %0\n\ts_cbranch_vccnz 1b"
               : "=&v"(v) : "v"(lds0), "s"(want), "n"(OFF), "n"(SL) : "vcc", "memory");
}
template <int CTRL> __device__ __forceinline__ int dppi(int v) { return __builtin_amdgcn_mov_dpp(v, CTRL, 0xf, 0xf, true); }
__device__ __forceinline__ int wave_imax(int l) {
  l = imax(l, dppi<DPP_XOR1>(l)); l = imax(l, dppi<DPP_XOR2>(l)); l = imax(l, dppi<DPP_ROR4>(l)); l = imax(l, dppi<DPP_ROR8>(l));
  int a = imax(__builtin_amdgcn_readlane(l, 0), __builtin_amdgcn_readlane(l, 16)), b = imax(__builtin_amdgcn_readlane(l, 32), __builtin_amdgcn_readlane(l, 48));
  PIN_S(a); PIN_S(b);
  return imax(a, b);
}
__device__ __forceinline__ void glds16(const void* gsrc, unsigned lds_dst) {
  unsigned keep;
  asm volatile("s_mov_b32 %0, m0\n\ts_mov_b32 m0, %2\n\ts_nop 0\n\tglobal_load_lds_dwordx4 %1, off\n\ts_mov_b32 m0, %0" : "=&s"(keep) : "v"(gsrc), "s"(lds_dst) : "memory");
}
__device__ __forceinline__ void glds16_sc1(const void* gsrc, unsigned lds_dst) {
  unsigned keep;
  asm volatile("s_mov_b32 %0, m0\n\ts_mov_b32 m0, %2\n\ts_nop 0\n\tglobal_load_lds_dwordx4 %1, off sc1\n\ts_mov_b32 m0, %0" : "=&s"(keep) : "v"(gsrc), "s"(lds_dst) : "memory");
}
__device__ __forceinline__ unsigned acc_off(int G, int row) {
  return (unsigned)(G < 128 ? ((G & 31) | ((G & 32) << 1) | ((G & 64) >> 1)) * 256 + row * 16 : (2 * G - 128 + (row >> 2)) * 256 + (row & 3) * 16);
}
__device__ __forceinline__ unsigned nz(unsigned v) { return v > 1u ? v : 1u; }
__device__ __forceinline__ unsigned pack2n(float lo, float hi) { return nz(pack2(lo, hi)); }
__device__ __forceinline__ unsigned min4(u32x4 v) { return umin(umin(v[0], v[1]), umin(v[2], v[3])); }
__device__ __forceinline__ bf16x8 as8(u32x4 v) { return __builtin_bit_cast(bf16x8, v); }

constexpr int NV = 7, NKQ = 2;
constexpr int L_RED1 = 0;
constexpr int L_BM = L_RED1 + 8 * 1536;
constexpr int L_STG = L_BM + 10240;
constexpr int L_ST = L_STG + 2048 + 2560;
constexpr int L_MISC = L_ST + 512;
constexpr int L_KX = L_MISC + 128;
constexpr int L_QX = L_KX + NKQ * 2048;
constexpr int L_XN = L_QX + NKQ * 2048;
constexpr int L_VOX = L_XN + 81920;
constexpr int L_VOM = L_VOX + NV * 5120;
constexpr int L_BYTES = L_VOM + NV * 1280;
constexpr int S_IMG = L_XN + 40960;
static_assert(S_IMG + 61440 <= L_VOM, "start-up image");
static_assert(L_BYTES <= 160 * 1024, "LDS budget");

__device__ __forceinline__ bf16x8 wqk_frag(const char* lds, int t, int ks, int lane) {
  const int c = lane & 15, g = lane >> 4, p = c & 3;
  const char* q = lds + S_IMG + (32 * ks + 8 * g + (c >> 2)) * 96 + 8 * p + (t == 0 ? 0 : (t == 1 ? 48 : (p < 2 ? 32 : 64)));
  const v4i16_t lo = __builtin_amdgcn_ds_read_tr16_b64_v4i16((LAS v4i16_t*)q), hi = __builtin_amdgcn_ds_read_tr16_b64_v4i16((LAS v4i16_t*)(q + 4 * 96));
  return (bf16x8){lo[0], lo[1], lo[2], lo[3], hi[0], hi[1], hi[2], hi[3]};
}
__device__ __forceinline__ bf16x8 sel_frag(int c, int base) {
  bf16x8 r;
#pragma unroll
  for (int i = 0; i < 8; ++i) r[i] = (c == base + i) ? (short)0x3f80 : (short)0;
  return r;
}
__device__ __forceinline__ bf16x8 qk_frag(const char* p4, const char* p2, const char* p2b) {
  f32x4 a0 = *(const f32x4*)(p4), a1 = *(const f32x4*)(p4 + 1792), a2 = *(const f32x4*)(p4 + 3584), a3 = *(const f32x4*)(p4 + 5376);
  f32x2 b0 = *(const f32x2*)(p2), b1 = *(const f32x2*)(p2 + 1792), b2 = *(const f32x2*)(p2b), b3 = *(const f32x2*)(p2b + 1792);
  f32x4 va = (a0 + a1) + (a2 + a3);
  f32x2 vb = (b0 + b1) + (b2 + b3);
  return mk8(pack2(va[0], va[1]), pack2(va[2], va[3]), pack2(vb[0], vb[1]), 0u);
}
__device__ __forceinline__ void x_fetch(const u16* src, unsigned lds0, int seg, int p, int lane) {
  if (EXP_NODMA) return;
  glds16_sc1(src + (size_t)p * 512 + lane * 8, lds0 + L_XN + (seg & 1) * 40960 + p * 1024);
}
template <int N> __device__ __forceinline__ void x_fetch_n(const u16* src, unsigned lds0, int seg, int p0, unsigned voff) {
  if (EXP_NODMA) return;
#pragma unroll
  for (int i = 0; i < N; i += 4) {
    unsigned keep;
    asm volatile("s_mov_b32 %0, m0\n\ts_mov_b32 m0, %3\n\ts_nop 0\n\tglobal_load_lds_dwordx4 %1, %2 sc1\n\t"
                 ".if %4 > 1\n\tglobal_load_lds_dwordx4 %1, %2 offset:1024 sc1\n\t.endif\n\t.if %4 > 2\n\tglobal_load_lds_dwordx4 %1, %2 offset:2048 sc1\n\t.endif\n\t"
                 ".if %4 > 3\n\tglobal_load_lds_dwordx4 %1, %2 offset:3072 sc1\n\t.endif\n\ts_mov_b32 m0, %0"
                 : "=&s"(keep) : "v"(voff), "s"(src + (size_t)(p0 + i) * 512), "s"(lds0 + L_XN + (seg & 1) * 40960 + (p0 + i) * 1024), "n"(N - i < 4 ? N - i : 4) : "memory");
  }
}
__device__ __forceinline__ int qkv_row(int b) { return (b & 7) / QR * (2 * QR) + (b >> 3) / (16 / QR); }
__device__ __forceinline__ int qkv_col(int b) { return (b & 7) % QR * (16 / QR) + (b >> 3) % (16 / QR); }
__device__ __forceinline__ const u16* x_src(const RecArgs& a, int seg, int b) {
  return seg < NSEG ? (const u16*)(a.ws + OFF_XN) + (size_t)seg * 32 * DM : a.xnew + (size_t)(qkv_row(b) + 16 * (seg - NSEG)) * 32 * DM;
}
template <int N>
__device__ __forceinline__ void img_fetch(const RecArgs& a, unsigned lds0, int h, int j, int part, int lane) {
#pragma unroll
  for (int i = 0; i < N; ++i) {
    const int n = part * N + i, idx = n * 64 + lane, row = idx / 6, pc = idx - row * 6;
    if (FR_PACE && N == 20) asm volatile("s_waitcnt vmcnt(%0)" :: "n"(FR_PACE) : "memory");
    glds16((pc < 3 ? a.wq : a.wk - 24) + (size_t)row * HD + h * DH + j * 24 + pc * 8, lds0 + S_IMG + n * 1024);
  }
}
constexpr int P2B = F4 + F5 + 2 * F67 + F6 + F7, PZ = P2B + F2B, PA = PZ, P0C = PA + 2 * F0A, F3C = 40 - F3A - F3 - P0C - 2 * F0C - F2C;
static_assert(F3C >= 0, "fetch plan");
template <int OFF> __device__ __forceinline__ void lds_inc(unsigned lds0) {
  asm volatile("s_mov_b64 exec, 1\n\tds_add_u32 %0, %1 offset:%2\n\ts_mov_b64 exec, -1" :: "v"(lds0), "v"(1u), "n"(OFF) : "memory");
}
__device__ __forceinline__ void lds_arrive(unsigned* p, int lane) {
  asm volatile("s_waitcnt lgkmcnt(0)" ::: "memory");
  if (lane == 0) __hip_atomic_fetch_add(p, 1u, __ATOMIC_RELAXED, __HIP_MEMORY_SCOPE_WORKGROUP);
}
__device__ __forceinline__ void lds_wait(unsigned* p, unsigned want) {
  while (__hip_atomic_load(p, __ATOMIC_RELAXED, __HIP_MEMORY_SCOPE_WORKGROUP) < want) __builtin_amdgcn_s_sleep(1);
  asm volatile("" ::: "memory");
}
constexpr float KEXP = 0.07216878364870322f * 1.4426950408889634f;
#define FRESH_LANE() int lane = lane0; asm volatile("" : "+v"(lane)); const int c = lane & 15, g = lane >> 4; (void)c; (void)g

struct OutTask {
  u32x4 y[4]; u32x2 xo, out;
  bf16x8 sel;
  unsigned t5f, t5o;
  __device__ __forceinline__ void init(int b, int lane) {
    const int um = b / 10, uq = (b / 5) & 1, up = b % 5, tok = lane & 15, k = up < 4 ? (up >> 1) * 256 + um * 16 + (up & 1) * 8 : 512 + um * 8, q4 = ((lane >> 4) & 1) * 4;
    t5f = (unsigned)(((uq * 20 + (k >> 5)) * 64 + ((k & 31) >> 3) * 16 + tok) * 8 + q4);
    t5o = (unsigned)(((um * 2 + uq) * 16 + (lane >> 4)) * 1280 + up * 256 + tok * 16);
    if (b >= 160) { t5f = 0u; t5o = 0x7ffffff0u; }
    sel = sel_frag(tok, 0);
    PIN_V(sel);
  }
  static __device__ __forceinline__ bool ok(int sl, int b) { return sl >= 0 && b < 160; }
  __device__ __forceinline__ void load(const RecArgs& a, const __amdgpu_buffer_rsrc_t rs, int sl, int b, int part = 3) {
    const unsigned o = t5o | (sl < 0 ? 0x7ff00000u : 0u);
    sl = imax(sl, 0);
#pragma unroll
    for (int k = 0; k < 4; ++k) if (part & 1) y[k] = SC1_LOADS(o, op_slot(sl) + k * 5120);
    if (part & 2) xo = __builtin_bit_cast(u32x2, __hip_atomic_load((const gu64*)(a.xold + (size_t)sl * 32 * DM + t5f), RLX_AGENT));
  }
  __device__ __forceinline__ bool ready(int b) { return b >= 160 || wave_all(umin(umin(min4(y[0]), min4(y[1])), umin(min4(y[2]), min4(y[3]))) != 0u); }
  __device__ __forceinline__ void run(int lane) {
    f32x4 d = {0.f, 0.f, 0.f, 0.f};
#pragma unroll
    for (int k = 0; k < 4; ++k) d = mfma32(sel, as8(y[k]), d);
#pragma unroll
    for (int i = 0; i < 2; ++i) {
      const unsigned r = pack2(d[2 * i], d[2 * i + 1]), x = xo[i];
      out[i] = pack2(u2f(x << 16) + u2f(r << 16), u2f(x & 0xffff0000u) + u2f(r & 0xffff0000u));
    }
    PIN_V(out);
  }
  __device__ __forceinline__ void store(const RecArgs& a, int sl, int b, int lane) {
    __builtin_amdgcn_raw_buffer_store_b64(out, __builtin_amdgcn_make_buffer_rsrc(a.xnew, 0, ok(sl, b) ? S_ * DM * 2 : 0, 0x00020000), (t5f + (unsigned)sl * 32 * DM) * 2u | (unsigned)(lane & 32) << 25, 0, 16);
  }
};

struct PvTask2 {
  u32x4 y[2][2], pa[2], pb[2];
  unsigned lo, lm, vr, ma, mz, so, sz;
  __device__ __forceinline__ void init(int gp, int lane) {
    const int hi = lane >> 5;
    lo = pt_tok(2 * gp) + (unsigned)(lane * 16); lm = pt_mem(2 * gp + hi) + (unsigned)((lane & 31) * 8);
    vr = (unsigned)(L_VOX + 2048 + (lane ^ 8) * 16);
    ma = (unsigned)(L_VOM + hi * 768 + (lane & 31) * 8); mz = (unsigned)(L_VOM + 512 + ((lane & 31) ^ (hi * 8)) * 8);
    so = (unsigned)(L_STG + 2048 + (hi * 16 + (lane & 15)) * 16 + ((lane >> 4) & 1) * 8);
    sz = hi ? (unsigned)(L_RED1 + 8704 + lane * 8) : so + 1024;
    PIN_V(lo); PIN_V(lm); PIN_V(vr); PIN_V(ma); PIN_V(mz); PIN_V(so); PIN_V(sz);
#pragma unroll
    for (int q = 0; q < 2; ++q) { pa[q] = pb[q] = y[q][0] = y[q][1] = (u32x4){0u, 0u, 0u, 0u}; PIN_V(pa[q]); PIN_V(pb[q]); }
  }
  __device__ __forceinline__ void load(const __amdgpu_buffer_rsrc_t rs, unsigned o) {
#pragma unroll
    for (int q = 0; q < 2; ++q) {
      y[q][0] = SC1_LOADS(lo + q * 1024, o); y[q][1] = SC1_LOADS(lo + 2048 + q * 1024, o);
      const u32x2 p = __builtin_amdgcn_raw_buffer_load_b64(rs, lm + q * 256, o, 16);
      pa[q][0] = p[0]; pa[q][1] = p[1];
    }
  }
  __device__ __forceinline__ bool ready() {
    return wave_all(umin(umin(umin(min4(y[0][0]), min4(y[0][1])), umin(min4(y[1][0]), min4(y[1][1]))), umin(umin(pa[0][0], pa[0][1]), umin(pa[1][0], pa[1][1]))) != 0u);
  }
  template <bool DIRECT> __device__ __forceinline__ void run(char* lds, int sv, int lane, const __amdgpu_buffer_rsrc_t rs, unsigned og) {
    const f32x4 Z4 = {0.f, 0.f, 0.f, 0.f};
    const char* vp = lds + L_VOX + sv * 5120 + lane * 16;
    bf16x8 vx[6];
#pragma unroll
    for (int t = 0; t < 5; ++t) vx[t] = *(const bf16x8*)(vp + t * 1024);
    vx[5] = *(const bf16x8*)(lds + vr + sv * 5120);
    const u32x2 a0 = *(const u32x2*)(lds + ma + sv * 1280), a1 = *(const u32x2*)(lds + ma + sv * 1280 + 256);
    const u32x2 z0 = *(const u32x2*)(lds + mz + sv * 1280), z1 = *(const u32x2*)(lds + mz + sv * 1280 + 256);
    const bf16x8 vm = mk8(a0[0], a0[1], a1[0], a1[1]), vz = mk8(z0[0], z0[1], z1[0], z1[1]);
#pragma unroll
    for (int q = 0; q < 2; ++q) { PIN_V(pa[q]); pb[q][2] = pa[q][0]; pb[q][3] = pa[q][1]; PIN_V(pb[q]); }
    __builtin_amdgcn_sched_barrier(0);
    f32x4 o[2][3];
#pragma unroll
    for (int q = 0; q < 2; ++q) {
#pragma unroll
      for (int t = 0; t < 3; ++t) o[q][t] = mfma32(vx[t], as8(y[q][0]), Z4);
#pragma unroll
      for (int t = 0; t < 3; ++t) o[q][t] = mfma32(vx[3 + t], as8(y[q][1]), o[q][t]);
      o[q][0] = mfma32(vm, as8(pa[q]), o[q][0]); o[q][1] = mfma32(vm, as8(pb[q]), o[q][1]); o[q][2] = mfma32(vz, as8(pa[q]), o[q][2]);
    }
#pragma unroll
    for (int q = 0; q < 2; ++q) {
#pragma unroll
      for (int t = 0; t < 3; ++t) {
        const u32x2 v = {pack2n(o[q][t][0], o[q][t][1]), pack2n(o[q][t][2], o[q][t][3])};
        if (DIRECT) SC1_STORE8S(v, (so - (L_STG + 2048) + t * 512) | (t == 2 ? (unsigned)(lane & 32) << 25 : 0u), og + q * 20480);
        else *(u32x2*)(lds + (t < 2 ? so + t * 512 : sz) + q * 1280) = v;
      }
    }
  }
};

struct TokScores {
  bf16x8 k[2], q[2]; u32x4 v[2];
  __device__ __forceinline__ void get(const char* lds, int seg, int lane) {
#pragma unroll
    for (int i = 0; i < 2; ++i) { k[i] = *(const bf16x8*)(lds + L_KX + (seg & 1) * 2048 + i * 1024 + lane * 16); q[i] = *(const bf16x8*)(lds + L_QX + (seg & 1) * 2048 + i * 1024 + lane * 16); }
  }
  __device__ __forceinline__ void run(int qt) {
    const f32x4 Z4 = {0.f, 0.f, 0.f, 0.f};
    const f32x4 d1 = mfma32(k[0], q[qt], Z4), d2 = mfma32(k[1], q[qt], Z4);
    v[qt] = (u32x4){pack2n(d1[0], d1[1]), pack2n(d1[2], d1[3]), pack2n(d2[0], d2[1]), pack2n(d2[2], d2[3])};
    PIN_V(v[qt]);
  }
  __device__ __forceinline__ void store(const __amdgpu_buffer_rsrc_t rs, unsigned off, unsigned o) {
    __builtin_amdgcn_raw_buffer_store_b128(v[0], rs, off, o, 16); __builtin_amdgcn_raw_buffer_store_b128(v[1], rs, off + 1024, o, 16);
    asm volatile("s_nop 1" ::: "memory");
  }
};

__device__ __forceinline__ void touch(const char* base, unsigned off, unsigned lds0) {
  unsigned keep;
  asm volatile("s_mov_b32 %0, m0\n\ts_mov_b32 m0, %3\n\ts_nop 0\n\tglobal_load_lds_dword %1, %2 sc1\n\ts_mov_b32 m0, %0" : "=&s"(keep) : "v"(off), "s"(base), "s"(lds0 + L_RED1 + 8192) : "memory");
}
template <int N> __device__ __forceinline__ void stouch(const char* p, unsigned& d) {
  asm volatile(".set pf_o, 0\n\t.rept %2\n\ts_load_dword %0, %1, pf_o\n\t.set pf_o, pf_o + 128\n\t.endr" : "+s"(d) : "s"(p), "n"(N) : "memory");
}
__device__ __forceinline__ void code_touch(int b, int lane, unsigned lds0) {
  const char* pc; int d0, d1;
  asm volatile("s_getpc_b64 %0\n\ts_mov_b32 %1, _Z10rec_kernel7RecArgs@rel32@lo+4\n\ts_mov_b32 %2, _Z11attb_kernelPKtS0_Pt@rel32@lo+12" : "=s"(pc), "=s"(d0), "=s"(d1));
  const int o = (lane * 32 + (b >> 3)) * 128;
  touch(pc + d0, (unsigned)(o < d1 - d0 ? o : 0), lds0);
}
__device__ __forceinline__ unsigned zero_base(int b, int h, int j, int ln) {
  return ln < 32 ? (unsigned)((h & 3) * 32768 + j * 512 + ln * 16) : ln < 40 ? (unsigned)((h & 2) * 1024 + j * 256 + (ln & 4) * 32 + (h & 1) * 64 + (ln & 3) * 16)
                 : (unsigned)OFF_ACC + (b < ACC_NB ? (unsigned)(b * 256 + (ln - 40) * 16) : ts_piece((b - ACC_NB) * 8 + ln - 40));
}
__device__ __forceinline__ unsigned sct_slot(unsigned ck, unsigned sl) { return sl * SCT_STEP + ((ck >> (sl & 4u)) & 7u) * 4096u; }
__device__ __forceinline__ unsigned scm_slot(unsigned ck, unsigned sl) { return OFF_SCM + sl * SCM_STEP + ((ck >> 8) & 7u) * 4096u; }
__device__ __forceinline__ int ck_rot(int x) { return ((x & 1) << 2) | (x >> 1); }
struct Place {
  unsigned t[8];
  __device__ __forceinline__ void load(const char* ws, int lane) {
#pragma unroll
    for (int x = 0; x < 8; ++x) t[x] = *(const unsigned*)(ws + OFF_LAT + ((imin(lane >> 3, 5) * 8 + (ck_rot(x) ^ (lane & 7))) * 8 + x) * 4);
  }
  __device__ __forceinline__ unsigned get(int b, int lane) {
    int c = (int)((((t[0] + t[1]) + (t[2] + t[3])) + ((t[4] + t[5]) + (t[6] + t[7]))) * 8u) + (lane & 7);
    c = imin(c, dppi<DPP_XOR1>(c)); c = imin(c, dppi<DPP_XOR2>(c)); c = imin(c, dppi<0x141>(c));
    unsigned k = 0u;
#pragma unroll
    for (int r = 0; r < 6; ++r) k |= (unsigned)(__builtin_amdgcn_readlane(c, 8 * r) & 7) << (4 * r);
    return (k ^ PL_X) ^ (unsigned)ck_rot(b & 7) * 0x111111u;
  }
};

struct Sched {
  u32x4 tsv, rec;
  unsigned side, o;
  __device__ __forceinline__ void pre(int s) { o = (unsigned)OFF_ACC + (unsigned)((s + SR - 1) % SR) * (unsigned)ACC_STEP + ACC_TS + side; PIN_S(o); }
  __device__ __forceinline__ void load(const __amdgpu_buffer_rsrc_t rs, int lane) {
    tsv = SC1_LOADS(ts_piece(lane & 63) - ACC_TS, o); rec = SC1_LOADS(128, o);
  }
  __device__ __forceinline__ unsigned last(unsigned ref) {
    const unsigned o = 0x40000000u - ref;
    int l = (int)umax(umax(tsv[0] ? tsv[0] + o : 0u, tsv[1] ? tsv[1] + o : 0u), umax(tsv[2] ? tsv[2] + o : 0u, tsv[3] ? tsv[3] + o : 0u));
    return (unsigned)wave_imax(l) - o;
  }
  __device__ __forceinline__ unsigned run(const RecArgs& a, const __amdgpu_buffer_rsrc_t rs, int s, int b, int lane) {
    constexpr int w = 3; (void)w;
    unsigned acur = __builtin_amdgcn_readfirstlane(rec[0]), aprev = __builtin_amdgcn_readfirstlane(rec[1]), hist = __builtin_amdgcn_readfirstlane(rec[3]);
    int D8 = (int)__builtin_amdgcn_readfirstlane(rec[2]);
    const int d = wave_imax((int)(umax(umax(tsv[0], tsv[1]), umax(tsv[2], tsv[3])) - aprev)), dd = 8 * d - D8;
    int Dn = D8 + (dd >> (dd > 0 ? SCH_GU : SCH_GD));
    unsigned anext = (acur + (unsigned)((Dn + 4 + imax(Dn - 8 * SCH_DR, 0) * SCH_DK / 8) >> 3) + SCH_V + (s < EARLY_N ? EARLY_V : (s >= WPF0 ? (s == NSEG - 2 ? LAST_V : LATE_V) : 0))) | 1u;
    if (__builtin_expect(hist < 2u || aprev == 0u || d < 1 || d + SCH_VMIN > (int)(acur - aprev), 0)) {
      if (s == 0) {
        for (int it = 0; it < 40 && !wave_all(min4(tsv) != 0u); ++it) {
          __builtin_amdgcn_s_sleep(ST_ALL);
          asm volatile("" ::: "memory");
          load(rs, lane);
        }
      }
      const unsigned now = now32(), L = last(now);
      if (acur == 0u) { D8 = 8 * SCH_D0; hist = 0u; }
      const bool late = acur == 0u || (int)(L + SCH_VMIN - acur) > 0;
      if (late) { if (b == 0) FC(3); if (acur) D8 += 8 * (hist ? SCH_UP : 3 * SCH_UP); }
      else if (aprev) { if (hist < 2u) D8 = 8 * imin(imax((int)(L - aprev), 1), 4000); hist = umin(hist + 1u, 2u); }
      const unsigned base = acur == 0u ? L + SCH_FIRST : (late ? L + SCH_V + ((int)(L + SCH_VMIN2 - acur) <= 0 ? SCH_PEN2 : SCH_PEN) : acur);
      anext = (base + (unsigned)((D8 + 4) >> 3) + SCH_V + (s == 1 ? EARLY_V1 : (s < EARLY_N ? EARLY_V : 0))) | 1u;
      if (s == 0) anext = (L + ST_X) | 1u;
      if ((int)(now - L) > 50000) anext = 0u;
      if (late) acur = 0u;
      Dn = D8;
    }
    if (__builtin_expect(b == 0, 0))
      __builtin_amdgcn_raw_buffer_store_b128((u32x4){anext, acur, (unsigned)Dn, hist}, rs, lane < 2 ? lane * 8192 : 0x7ffffff0, (unsigned)OFF_ACC + (unsigned)(s % SR) * (unsigned)ACC_STEP + ACC_TS + 128u, 16);
    LOGV(0, anext); LOGV(1, acur == 0u); LOGV(2, (Dn + 4) >> 3); LOGV(3, d); LOGV(4, acur - aprev - d); LOGV(5, aprev);
    return anext;
  }
};

struct TokTask {
  u32x4 y[2]; u32x2 ym; u32x4 res;
  unsigned l0, l1, lm, s0, s1;
  static __device__ __forceinline__ bool ok(int sl) { return sl >= 0 && sl < NSEG; }
  __device__ __forceinline__ void init(int h, int j, int lane) {
    const int c = lane & 15, g = lane >> 4;
    l0 = ts_tok(h, g) + (unsigned)(j * 256 + c * 16); l1 = l0 + 16384u;
    lm = ts_mem(h, g + 4 * (c >> 3)) + (unsigned)(j * 64 + (c & 7) * 8);
    res = (u32x4){0u, 0u, 0u, 0u};
    const int qi = 4 * j + (c & 3);
    s0 = g < 2 ? pt_tok(h) + (unsigned)((qi >> 4) * 1024 + ((c >> 2) * 16 + (qi & 15)) * 16 + g * 8) : 0x7ffffff0u;
    s1 = lane < 8 ? pt_mem(h) + (unsigned)((qi >> 4) * 256 + ((c >> 2) * 16 + (qi & 15)) * 8) : 0x7ffffff0u;
    PIN_V(l0); PIN_V(l1); PIN_V(lm); PIN_V(s0); PIN_V(s1);
  }
  __device__ __forceinline__ void load(const __amdgpu_buffer_rsrc_t rs, unsigned o) {
    y[0] = SC1_LOADS(l0, o); y[1] = SC1_LOADS(l1, o);
    ym = __builtin_amdgcn_raw_buffer_load_b64(rs, lm, o, 16);
  }
  __device__ __forceinline__ bool ready() { return wave_all(umin(umin(min4(y[0]), min4(y[1])), umin(ym[0], ym[1])) != 0u); }
  __device__ __forceinline__ void run(int j, int lane, const bf16x8 SelLo) {
    const int c = lane & 15, g = lane >> 4;
    const f32x4 Z4 = {0.f, 0.f, 0.f, 0.f};
    const f32x4 d1 = mfma32(SelLo, as8(y[1]), mfma32(SelLo, as8(y[0]), Z4));
    const f32x4 d0 = mfma32(SelLo, mk8(ym[0], ym[1], 0u, 0u), Z4);
    const int qi = 4 * j + (c & 3), k0 = 16 * g + 4 * (c >> 2);
    float t[4], mv[4];
    float mx = -INFINITY;
#pragma unroll
    for (int r = 0; r < 4; ++r) {
      t[r] = (g < 2 && k0 + r <= qi) ? d1[r] : -INFINITY;
      const float mt = d0[r] + dppf<DPP_ROR8>(d0[r]);
      mv[r] = lane < 8 ? mt : -INFINITY;
      mx = fmx(mx, fmx(t[r], mv[r]));
    }
    mx = fmx(mx, dppf<DPP_ROR8>(mx)); mx = fmx(mx, dppf<DPP_ROR4>(mx));
    mx = x16mx(mx);
    float sum = 0.f;
#pragma unroll
    for (int r = 0; r < 4; ++r) {
      t[r] = __builtin_amdgcn_exp2f(t[r] - mx); mv[r] = __builtin_amdgcn_exp2f(mv[r] - mx);
      sum += t[r] + mv[r];
    }
    sum += dppf<DPP_ROR8>(sum); sum += dppf<DPP_ROR4>(sum);
    sum = x16sum(sum);
    const float rs_ = __builtin_amdgcn_rcpf(sum);
    res = (u32x4){pack2n(t[0] * rs_, t[1] * rs_), pack2n(t[2] * rs_, t[3] * rs_), pack2n(mv[0] * rs_, mv[1] * rs_), pack2n(mv[2] * rs_, mv[3] * rs_)};
    PIN_V(res);
  }
  __device__ __forceinline__ void store(const __amdgpu_buffer_rsrc_t rs, unsigned o, unsigned no = 0u) {
    SC1_STORE8S(((u32x2){res[0], res[1]}), s0 | no, o);
    SC1_STORE8S(((u32x2){res[2], res[3]}), s1 | no, o);
  }
};

constexpr int L_DRN = L_VOX + (NSEG % NV) * 5120;
__device__ __forceinline__ void drain_pre(char* lds, int b, int lane) {
  const int gp = (b & 7) * 2 + (b >> 7), m = (b >> 3) & 15;
  TokTask tk; PvTask2 pv; OutTask ot;
  tk.init(2 * gp + (m >> 3), m & 7, lane); pv.init(gp, lane); ot.init(b, lane);
  u32x4* p = (u32x4*)(lds + L_DRN) + lane;
  p[0] = (u32x4){tk.l0, tk.lm, tk.s0, tk.s1}; p[64] = (u32x4){pv.lo, pv.lm, pv.vr, pv.ma}; p[128] = (u32x4){pv.mz, pv.so, ot.t5f, ot.t5o};
  p[192] = __builtin_bit_cast(u32x4, ot.sel);
}
__device__ __forceinline__ void qkv_pre(unsigned (&vo)[2][2], int b, int w, int lane) {
  const int cb = qkv_col(b), c = lane & 15, g = lane >> 4, t0 = w & 1, col0 = (cb % 5) * 128 + w * 32;
  const bool SW = cb < 10;
#pragma unroll
  for (int i = 0; i < 2; ++i)
#pragma unroll
    for (int tt = 0; tt < 2; ++tt) {
      const int tq = tt ^ t0, tk = tq * 16 + c, cc = col0 + i * 16 + (SW ? 4 * g : c), hd = cc / BD, e = cc - hd * BD;
      const int kv = (int)(OFF_KV - OFF_QB) + hd * 64 * KVT;
      vo[i][tt] = (unsigned)(!SW ? kv + 1280 + tq * 672 + ((g >> 1) * 21 + e) * 16 + (g & 1) * 8
                                 : (cb < 5 ? (hd * S_ + tk) * 2 * QST + e * 2 : kv + (e < 16 ? (tk + 32 * (e >> 3)) * 16 + (e & 7) * 2 : 1024 + tk * 8)));
      PIN_V(vo[i][tt]);
    }
}

template <int w>
__device__ __forceinline__ void token_waves(const RecArgs& a, char* lds, const __amdgpu_buffer_rsrc_t rs, unsigned (&vo)[2][2]) {
  const int lane0 = __builtin_amdgcn_workitem_id_x() & 63, b = __builtin_amdgcn_workgroup_id_x(), wid = w + 4; (void)wid;
  const int gp = (b & 7) * 2 + (b >> 7), m = (b >> 3) & 15, h0 = 2 * gp, h = h0 + (m >> 3), j = m & 7;
  float* const misc = (float*)(lds + L_MISC);
  const f32x4 Z4 = {0.f, 0.f, 0.f, 0.f};
  const unsigned lds0 = (unsigned)(uintptr_t)(LAS char*)lds;
  unsigned* const cnt = (unsigned*)(misc + 6);
  const float* const xnrm = (const float*)(a.ws + OFF_XNRM);
  bf16x16 W[2][10];
  auto btile = [&](int i) { return w == 1 ? 0 : (w == 2 ? 1 + i : 3 + i); };
  if (w == 1) code_touch(b, lane0, lds0);
  Place pl;
  unsigned phi[2][2], ph[2][2][2];
#pragma unroll
  for (int k = 0; k < 8; ++k) {
    const int bt = btile(k >> 2), hw = h0 + (bt == 2 ? k & 1 : bt / 3), T = bt == 2 ? 32 + (m >> 1) : (bt % 3) * 16 + m;
    ph[k >> 2][(k >> 1) & 1][k & 1] = ((const __attribute__((address_space(4))) unsigned*)(a.ws + OFF_PHI))[(hw >> 2) | (hw & 3) << 3 | (k & 2) << 4 | (T / 10) << 6];
  }
  __builtin_amdgcn_sched_barrier(0);
  if (w == 3) pl.load(a.ws, lane0);
  if (a.layer) {
    if (w != 0 && FR_HOLD) wait_until(now32() + FR_HOLD);
    if (w != 0) img_fetch<20>(a, lds0, h, j, w - 1, lane0);
  } else {
    if (w == 0) img_fetch<12>(a, lds0, h, j, 4, lane0);
#pragma unroll
    for (int i = 0; i < 10; ++i) x_fetch(x_src(a, 0, b), lds0, 0, 10 * w + i, lane0);
  }
  const __amdgpu_buffer_rsrc_t rwb = __builtin_amdgcn_make_buffer_rsrc(a.ws + OFF_WB, 0, (int)WB_BYTES, 0x00020000);
#pragma unroll
  for (int k = 0; k < 4; ++k) phi[k >> 1][k & 1] = (((lane0 & 8) ? ph[k >> 1][k & 1][1] : ph[k >> 1][k & 1][0]) & 8192u) | (unsigned)(lane0 * 16);
#pragma unroll
  for (int i = 0; i < 2; ++i)
#pragma unroll
    for (int p = 0; p < 10; ++p)
      if (w >= 2 || (w == 1 && i == 1)) {
        const unsigned wo = wb_off((b * 5 + btile(i)) * 20 + 2 * p), wv = phi[i][p / 5] ^ (wo & 8192u);
        if (FR_PACE && a.layer) asm volatile("s_waitcnt vmcnt(%0)" :: "n"(FR_PACE) : "memory");
        W[i][p] = cat16(as8(__builtin_amdgcn_raw_buffer_load_b128(rwb, wv, wo & ~8192u, 0)), as8(__builtin_amdgcn_raw_buffer_load_b128(rwb, wv + 1024, wo & ~8192u, 0)));
      }
  f32x4 bn = Z4;
  if (w == 0) {
    bn = *(const f32x4*)(a.ws + OFF_BNRM + lane0 * 16);
    if (!a.layer) asm volatile("s_waitcnt vmcnt(0)" ::: "memory");
  } else asm volatile("s_waitcnt vmcnt(%0)" :: "n"(w == 1 ? 20 : 40) : "memory");
  unsigned ck = 0u;
  if (w == 3) { ck = pl.get(b, lane0); *(unsigned*)(misc + 11) = ck; }
  BAR();
#pragma unroll
  for (int p = 0; p < 10; ++p) {
    if (w < 2) W[0][p] = cat16(wqk_frag(lds, 2 * w, 2 * p, lane0), wqk_frag(lds, 2 * w, 2 * p + 1, lane0));
    if (w == 0) W[1][p] = cat16(wqk_frag(lds, 1, 2 * p, lane0), wqk_frag(lds, 1, 2 * p + 1, lane0));
  }
  float cb2 = 0.f;
  if (w == 0) {
    float n = __builtin_amdgcn_sqrtf(bn[0] + bn[1]) + __builtin_amdgcn_sqrtf(bn[2] + bn[3]);
    n += dppf<DPP_XOR1>(n); n += dppf<DPP_XOR2>(n); n += dppf<DPP_ROR4>(n); n += dppf<DPP_ROR8>(n);
    n = x32mx(x16mx(n));
    cb2 = u2f(__builtin_amdgcn_readfirstlane(f2u(1.15f * n * n)));
  }
  const float cm2 = u2f((f2u(cb2) & 0x7fffffu) | 0x40000000u);
  const int ek = imax((int)((f2u(cb2) >> 23) & 0xffu), 1) - 252;
  if (w >= 2) {
#pragma unroll
    for (int i = 0; i < 4; ++i) *(unsigned*)(lds + L_KX + (w - 2) * 4096 + i * 1024 + lane0 * 16 + 12) = 0u;
  }
  OutTask ot;
  const int zl = lane0 - 16;
  const bool zon = w == 3 && zl >= 0 && (zl < 40 || b < ACC_NB + 17);
  unsigned zo = zon ? zero_base(b, h, j, zl) + (zl < 32 ? sct_slot(ck, 0u) : (zl < 40 ? scm_slot(ck, 0u) : 0u)) : 0x7ffffff0u;
  unsigned zm = zon ? (zl < 32 ? SCT_STEP + 1u : (zl < 40 ? SCM_STEP : (unsigned)ACC_STEP)) : 0u;
  const unsigned zx = ((ck ^ (ck >> 4)) & 7u) * 4096u;
  if (w == 3) { PIN_V(zo); PIN_V(zm); }
  const __amdgpu_buffer_rsrc_t rx = __builtin_amdgcn_make_buffer_rsrc(a.ws + OFF_XNRM, 0, (int)(OFF_QB - OFF_XNRM), 0x00020000);
  u32x4 v0, v1;
  f32x4 d5;
  unsigned oo, za, xa, ya, n4, so_op, so_y, so_x;
  float xn4 = 0.f;
  auto w1_part = [&](int k, int s, int lane) {
    if (w < 2) return;
    if (w == 2) {
      if (k >= 1 && k <= 4) d5 = mfma32(ot.sel, as8(ot.y[k < 1 || k > 4 ? 0 : k - 1]), k == 1 ? Z4 : d5);
      if (k == 6 || k == 7) {
        const unsigned r = pack2(d5[2 * (k & 1)], d5[2 * (k & 1) + 1]), x = ot.xo[k & 1];
        ot.out[k & 1] = pack2(u2f(x << 16) + u2f(r << 16), u2f(x & 0xffff0000u) + u2f(r & 0xffff0000u));
        PIN_V(ot.out);
      }
      if (k == 8) xn4 = fmx(xn4, dppf<DPP_XOR1>(xn4));
      if (k == 9) xn4 = fmx(xn4, dppf<DPP_XOR2>(xn4));
      if (k == 10) xn4 = fmx(xn4, dppf<DPP_ROR4>(xn4));
      if (k == 11) xn4 = fmx(xn4, dppf<DPP_ROR8>(xn4));
      if (k >= 8 && k <= 11) PIN_V(xn4);
      if (k == 12) misc[s & 3] = x16mx(xn4);
      if (k == 5) { so_y = op_slot(imax(s - 1 - LAG5, 0)); so_x = (unsigned)imax(s - 1 - LAG5, 0) * 40960u; PIN_S(so_y); PIN_S(so_x); }
      if (k == 14) {
        ya = ot.t5o | (s <= LAG5 ? 0x7ff00000u : 0u);
        xa = (ot.t5f * 2u | (unsigned)(lane & 32) << 25) | (OutTask::ok(s - 2 - LAG5, b) ? 0u : 0x7ff00000u);
        PIN_V(ya); PIN_V(xa);
      }
    } else if (k == 8) {
      const unsigned zs = (unsigned)(s + 2) % SR;
      za = (zo + (s < 0 ? 1024u : zs) * (zm & ~1u)) ^ ((zm & 1u) * (zs & 4u ? zx : 0u));
      PIN_V(za);
    }
    if (k == 13) {
      n4 = __hip_atomic_load((unsigned*)(misc + 28), __ATOMIC_RELAXED, __HIP_MEMORY_SCOPE_WORKGROUP);
      so_op = s < 2 + LAG4 ? 0x7ff00000u : op_slot(s - 2 - LAG4) + (unsigned)((m * 32 + (w & 1) * 16 + gp) * 1280);
      PIN_S(so_op);
    }
    if (k == 15 && __builtin_expect((int)__builtin_amdgcn_readfirstlane(n4) < s, 0)) lds_spin<L_MISC + 112, 1>(lds0, (unsigned)s);
    if (k == 16) {
      asm volatile("" ::: "memory");
      const char* sp = lds + L_STG + 2048 + (w & 1) * 1280;
      v0 = *(const u32x4*)(sp + lane * 16); v1 = *(const u32x4*)(sp + 1024 + (lane & 15) * 16);
      oo = so_op + (unsigned)(lane * 16);
      za = lane < 16 ? oo + 1024 : (w == 3 ? za : 0x7ffffff0u);
      PIN_V(oo); PIN_V(za);
    }
    if (k == 19 && w == 3) {
#pragma unroll
      for (int i = 0; i < 4; ++i) { v1[i] = lane < 16 ? v1[i] : 0u; PIN_V(v1[i]); }
    }
    __builtin_amdgcn_sched_barrier(0);
  };
  auto w1_fire = [&](int s, int lane, auto last) {
    if (w < 2) return;
    lds_spin<L_MISC + 92, W1_SL>(lds0, 4u * (unsigned)imax(s, 0));
    SC1_STORE(v0, oo);
    SC1_STORE(v1, za);
    __builtin_amdgcn_sched_barrier(0);
    if (w == 2) {
      __builtin_amdgcn_raw_buffer_store_b64(ot.out, rx, xa, so_x + (unsigned)(OFF_XA - OFF_XNRM - 40960), 16);
      if (last == 0) {
#pragma unroll
        for (int k = 0; k < 4; ++k) ot.y[k] = SC1_LOADS(ya, so_y + k * 5120);
        ot.xo = __builtin_amdgcn_raw_buffer_load_b64(rx, ot.t5f * 2u, so_x + (unsigned)(OFF_XB - OFF_XNRM), 16);
        xn4 = u2f(__builtin_amdgcn_raw_buffer_load_b32(rx, (lane & 31) * 16, (s + 1) * 512, 16));
      }
    } else {
      const unsigned xo_ = (unsigned)((s - 8) * 1024 + lane * 16);
      if (__builtin_expect((unsigned)(s - 8) < 20u, 0)) SC1_STORE(((u32x4){0u, 0u, 0u, 0u}), xo_ < X_BYTES / 256 ? (unsigned)(OFF_X + b * (X_BYTES / 256)) + xo_ : 0x7ffffff0u);
    }
    __builtin_amdgcn_sched_barrier(0);
  };
  STAMP_DECL(); ARRIVE_DECL();
  auto mma_seg = [&](int seg, int sv, int lane, auto pause) {
    const int c = lane & 15, g = lane >> 4;
    const char* xp = lds + L_XN + (seg & 1) * 40960 + lane * 16;
    f32x4 acc[2][2] = {{Z4, Z4}, {Z4, Z4}};
    bf16x8 x[4][2];
#pragma unroll
    for (int p = 0; p < 3; ++p) { x[p][0] = *(const bf16x8*)(xp + p * 1024); x[p][1] = *(const bf16x8*)(xp + (20 + p) * 1024); }
    unsigned eo[2];
#pragma unroll
    for (int ks = 0; ks < 20; ++ks) {
      if (ks == (w == 0 ? KP0 : (w == 1 ? KP1 : KP2))) { pause(); __builtin_amdgcn_sched_barrier(0); }
      if (ks == 17) {
#pragma unroll
        for (int i = 0; i < 2; ++i) {
          eo[i] = (unsigned)(w == 0 ? (i ? L_KX : L_QX) + (seg % NKQ) * 2048 + lane * 16
                                    : (w == 1 && i == 0 ? ((g >> 1) ? L_KX : L_QX) + (seg % NKQ) * 2048 + (2 * (g & 1) * 16 + c) * 16 + 8 : L_VOX + sv * 5120 + btile(i) * 1024 + lane * 16));
          if (i == 0 || w == 1) PIN_V(eo[i]);
        }
        if (w == 0) eo[1] = eo[0] + (L_KX - L_QX);
        if (w >= 2) eo[1] = eo[0] + 1024;
      }
      __builtin_amdgcn_sched_barrier(0);
#pragma unroll
      for (int q = 0; q < 4; ++q) {
        const int tt = 1 - (q >> 1), i = q & 1;
        const bool isqk = w == 0 || (w == 1 && i == 0);
        const bf16x8 wf = half8(W[i][ks >> 1], ks & 1);
        acc[i][tt] = isqk ? mfma32(wf, x[ks & 3][tt], acc[i][tt]) : mfma32(x[ks & 3][tt], wf, acc[i][tt]);
        __builtin_amdgcn_sched_barrier(0);
        if (q < 2 && ks + 3 < 20) x[(ks + 3) & 3][q] = *(const bf16x8*)(xp + (q * 20 + ks + 3) * 1024);
        if (q == 2) w1_part(ks, seg - 1, lane);
        __builtin_amdgcn_sched_barrier(0);
      }
    }
#pragma unroll
    for (int i = 0; i < 2; ++i) {
      if (w + i == 1) { const float ks_ = (w == 0 || (g >> 1)) ? KEXP : 1.0f; acc[i][0] *= ks_; acc[i][1] *= ks_; }
      const u32x4 v = {pack2(acc[i][0][0], acc[i][0][1]), pack2(acc[i][0][2], acc[i][0][3]), pack2(acc[i][1][0], acc[i][1][1]), pack2(acc[i][1][2], acc[i][1][3])};
      char* p = lds + eo[i];
      if (w == 0) { *(u32x2*)(p) = (u32x2){v[0], v[1]}; *(u32x2*)(p + 1024) = (u32x2){v[2], v[3]}; }
      else if (w == 1 && i == 0) { *(unsigned*)(p) = v[0]; *(unsigned*)(p + 256) = v[1]; *(unsigned*)(p + 1024) = v[2]; *(unsigned*)(p + 1280) = v[3]; }
      else *(u32x4*)p = v;
    }
  };
  unsigned vp, mp, ip;
  int sidx, ei = 127;
  float inv;
  f32x4 acc[2];
  auto vo_pre = [&](int s, int sv, int lane) {
    vp = (unsigned)(w == 0 ? L_MISC + (s & 3) * 4 : L_VOM + sv * 1280 + (btile(0) * 32 + (w == 1 ? lane & 31 : lane)) * 8);
    mp = (unsigned)(L_BM + lane * 16);
    PIN_V(vp); PIN_V(mp);
    if (w == 0) { ip = (unsigned)(L_MISC + 16 + ((s + 1) & 1) * 4); PIN_V(ip); }
    else {
      inv = misc[4 + (s & 1)];
      sidx = (lane & 8) ? 0xEEEE : 0x4444;
      acc[0] = acc[1] = Z4;
      PIN_V(sidx); PIN_V(acc[1]);
      if (w >= 2) { PIN_V(acc[0]); PIN_V(inv); }
    }
    __builtin_amdgcn_sched_barrier(0);
  };
  auto vo_mem = [&](int s, auto last) {
    __builtin_amdgcn_s_setprio(2);
    asm volatile(".rept %0\n\ts_nop 3\n\t.endr" :: "n"(w == 0 ? VO_A0 : (w == 1 ? VO_A1 : VO_A2)) : "memory");
    float xmax = 0.f;
    if (w == 0) xmax = *(const float*)(lds + vp);
    constexpr int NX = w == 2 ? VO_N2 : (w == 3 ? VO_N3 : 10);
    bf16x8 x[10];
#pragma unroll
    for (int p = 0; p < NX; ++p) x[p] = *(const bf16x8*)(lds + mp + p * 1024);
    __builtin_amdgcn_sched_barrier(0);
    if (w < 2) asm volatile(".rept %0\n\ts_nop 3\n\t.endr" :: "n"(w == 0 ? VO_B0 : VO_B1) : "memory");
    if (VO_W >= 0) asm volatile("s_waitcnt lgkmcnt(%0)" :: "n"(VO_W < 0 ? 0 : VO_W) : "memory");
    STAMP(5);
    __builtin_amdgcn_sched_barrier(0);
    int ex = 0;
#pragma unroll
    for (int p = 0; p < 10; ++p) {
      if (w == 0) acc[0] = mfma32(x[p], x[p], p ? acc[0] : Z4);
      if (w == 0 && p == 9) { ex = imax((int)((__builtin_amdgcn_readfirstlane(f2u(__builtin_amdgcn_sqrtf(cb2 * xmax))) >> 23) & 0xffu) + 1, 32); PIN_S(ex); }
#pragma unroll
      for (int i = (w == 1); i < 2 && w != 0; ++i) acc[i] = smfma64(x[p], W[i][p], acc[i], sidx);
      __builtin_amdgcn_sched_barrier(0);
      if (p + NX < 10) x[p + NX] = *(const bf16x8*)(lds + mp + (p + NX) * 1024);
      __builtin_amdgcn_sched_barrier(0);
    }
    STAMP(7);
    if (w != 0) {
      float v[4];
#pragma unroll
      for (int r = 0; r < 4; ++r) {
        const auto sw = __builtin_amdgcn_permlane32_swap(f2u(acc[w == 1][r]), f2u(acc[1][r]), false, false);
        v[r] = (u2f(sw[0]) + u2f(sw[1])) * inv;
      }
      *(u32x2*)(lds + vp) = (u32x2){pack2(v[0], v[1]), pack2(v[2], v[3])};
    } else {
      const int t = (int)f2u(fmx(fmx(acc[0][0], acc[0][1]), fmx(acc[0][2], acc[0][3])) * cm2);
      const int et = wave_imax(t) >> 23, em = ((et + ek) >> 1) + ei;
      int e = imax(et == 255 ? 256 : em, ex);
      PIN_S(e);
      e = imin(e, 256);
      ei = e - 31;
      *(unsigned*)(lds + ip) = (unsigned)ei << 23;
      const u32x3 sr = {(unsigned)(285 - e) << 23, (unsigned)(e - 87) < 79u ? REC_RNG << 23 : 0u, (unsigned)(s + 1)};
      asm volatile("ds_write_b96 %0, %1 offset:%2" :: "v"(lds0), "v"(sr), "n"(L_MISC + 32) : "memory");
    }
    if (last == 0 && w >= 2) asm volatile("s_waitcnt vmcnt(%0)" :: "n"(w == SCH_W ? 2 : 0) : "memory");
    if (w != 0) lds_inc<L_MISC + 28>(lds0);
    STAMP(8);
    if (last) __builtin_amdgcn_s_setprio(0);
    if (last && w == 2) { VM_DRAIN(); lds_inc<L_MISC + 28>(lds0); }
  };
  unsigned* const cntq = (unsigned*)(misc + 12);
  unsigned* const cntt = (unsigned*)(misc + 26 + (w & 1));
  unsigned* const cntp = (unsigned*)(misc + 23);
  unsigned* const cntx2 = (unsigned*)(misc + 28);
  auto wpf = [&](int s, int lane) {
    constexpr int PER = 81920 / QR / WPF_N;
    static_assert(PER % WPF_S == 0 && PER / WPF_S <= 64, "prefetch");
    touch(a.ws + OFF_WQKVBT + (size_t)imin(qkv_col(b), 14) * 163840 + (qkv_row(b) % (2 * QR)) * (81920 / QR), (s - WPF0) * PER + imin(lane, PER / WPF_S - 1) * WPF_S, lds0);
  };
  Sched sch;
  auto late = [&](int s, int lane) {
    if (__builtin_expect(s < 0, 0)) return;
    if (w == SCH_W && SCH_LATE && __builtin_expect(s >= 0, 1)) *(u32x2*)(misc + 24) = (u32x2){sch.run(a, rs, s, b, lane), (unsigned)(s + 1)};
    if ((w == 2 && F6) || (w == 0 && F4) || (w == 1 && F5) || (w == 3 && F7)) { lds_spin<L_MISC + 48, 1>(lds0, 2u * (unsigned)imax(s + 1, 0)); if (FB_SL && w != 2) __builtin_amdgcn_s_sleep(FB_SL); }
    if (w >= 2) { x_fetch_n<w == 2 ? F6 : F7>(x_src(a, s + 3, b), lds0, s + 3, F4 + F5 + 2 * F67 + (w == 3 ? F6 : 0), lane * 16); return; }
    unsigned sd = 0u;
    if ((w == 0 && XPF == 3) || XPF == 4 || (w == 1 && XPF == 5)) stouch<10>(a.ws + OFF_XN + (size_t)imin(s + 4, NSEG - 1) * (s < 1 ? 0 : 40960) + (b >> 3) * 1280 + w * 64, sd);
    x_fetch_n<w == 0 ? F4 : F5>(x_src(a, s + 3, b), lds0, s + 3, w == 0 ? 0 : F4, lane * 16);
    if (WPF_W == 0 && w == 0 && __builtin_expect(s >= WPF0 && s < WPF0 + WPF_N, 0)) wpf(s, lane);
    if (w == 1 && __builtin_expect(s == CPF_S, 0)) code_touch(b, lane, lds0);
    lds_spin<L_MISC + 104 + 4 * (w & 1), PS_SL>(lds0, (unsigned)imax(s + 1, 0));
    asm volatile("" :: "s"(sd));
  };
  if (w == 2) { ot.init(b, lane0); ot.out = (u32x2){0u, 0u}; ot.xo = ot.out; ot.y[0] = ot.y[1] = ot.y[2] = ot.y[3] = (u32x4){0u, 0u, 0u, 0u}; }
  VM_DRAIN();
  lds_arrive(cnt, lane0);
  lds_wait(cnt, 8u);
  sch.tsv = sch.rec = (u32x4){0u, 0u, 0u, 0u};
  sch.side = __builtin_amdgcn_readfirstlane(*(const unsigned*)(misc + 11)) & 8192u;
  sch.o = 0u;
  int sv = NV - 1;
  CODE_PAD((PADT >> (4 * w)) & 15);
#pragma unroll 1
  for (int s = -1; s < NSEG - 1; ++s) {
    const int sv1 = sv == NV - 1 ? 0 : sv + 1;
    FRESH_LANE();
    STAMP(0);
    mma_seg(s + 1, sv1, lane, [&]() { STAMP(1); late(s - 1, lane); if (w == SCH_W) sch.pre(s); STAMP(9); });
    STAMP(2);
    __builtin_amdgcn_sched_barrier(0);
    VM_DRAIN();
    STAMP(10);
    w1_fire(s, lane, std::integral_constant<int, 0>{});
    if (w >= 2) vo_pre(s, sv, lane);
    STAMP(3);
    BAR();
    STAMP(4);
    if (__builtin_expect(s >= 0, 1)) {
      if (w == SCH_W) { sch.load(rs, lane0); __builtin_amdgcn_sched_barrier(0); }
      if (w < 2) vo_pre(s, sv, lane);
      vo_mem(s, std::integral_constant<int, 0>{});
      STAMP(6);
      if (w == SCH_W && !SCH_LATE) {
        __builtin_amdgcn_s_setprio(SCH_P);
        *(u32x2*)(misc + 24) = (u32x2){sch.run(a, rs, s, b, lane0), (unsigned)(s + 1)};
      }
      STAMP(11);
    }
    __builtin_amdgcn_s_setprio(MMA_P);
    if (w == 2) VM_DRAIN();
    __builtin_amdgcn_sched_barrier(0);
    sv = sv1;
  }
  __builtin_amdgcn_s_setprio(0);
  STAMP_OUT();
  late(NSEG - 2, lane0);
  { FRESH_LANE(); if (w == 0) drain_pre(lds, b, lane); qkv_pre(vo, b, w, lane); }
#pragma unroll
  for (int k = 0; k < 20; ++k) w1_part(k, NSEG - 1, lane0);
  w1_fire(NSEG - 1, lane0, std::integral_constant<int, 1>{});
  if (w != 0) vo_pre(NSEG - 1, (NSEG - 1) % NV, lane0);
  BAR();
  if (w != 0) vo_mem(NSEG - 1, std::integral_constant<int, 1>{});
}

__device__ __forceinline__ void qkv_tail(const RecArgs& a, char* lds, const int w, const unsigned (&vo)[2][2]) {
  const int lane0 = __builtin_amdgcn_workitem_id_x() & 63, b = __builtin_amdgcn_workgroup_id_x();
  const int r = qkv_row(b), cb = qkv_col(b);
  if (cb >= 15) return;
  const unsigned lds0 = (unsigned)(uintptr_t)(LAS char*)lds;
  unsigned* const stage = (unsigned*)(lds + L_MISC) + 17;
  unsigned* const cnty = stage + 1;
  const f32x4 Z4 = {0.f, 0.f, 0.f, 0.f};
  auto fetch = [&](int i) {
    x_fetch_n<10>(x_src(a, NSEG + i, b), lds0, i, 10 * w, lane0 * 16);
  };
  unsigned want = 0;
  auto sync = [&]() { VM_DRAIN(); want += 4u; lds_inc<L_MISC + 72>(lds0); while (__hip_atomic_load(cnty, __ATOMIC_RELAXED, __HIP_MEMORY_SCOPE_WORKGROUP) < want) { } asm volatile("" ::: "memory"); };
  LOGQ(0);
  bf16x8 W[2][20];
  const __amdgpu_buffer_rsrc_t rw = __builtin_amdgcn_make_buffer_rsrc(a.ws + OFF_WQKVBT + (size_t)(cb * 8 + 2 * w) * 20480, 0, 40960, 0x00020000);
  auto w_get = [&](int ks) {
#pragma unroll
    for (int i = 0; i < 2; ++i) W[i][ks] = as8(__builtin_amdgcn_raw_buffer_load_b128(rw, lane0 * 16 + (ks & 3) * 1024, (i * 20 + (ks & ~3)) * 1024, 0));
    __builtin_amdgcn_sched_barrier(0);
  };
#pragma unroll
  for (int ks = 0; ks < QW; ++ks) w_get(ks);
  bf16x8 ones = mk8(0x3f803f80u, 0x3f803f80u, 0x3f803f80u, 0x3f803f80u);
  PIN_V(ones);
  const __amdgpu_buffer_rsrc_t ro = __builtin_amdgcn_make_buffer_rsrc(a.ws + OFF_QB, 0, (int)(OFF_OB - OFF_QB), 0x00020000);
  const int t0 = w & 1;
  auto body = [&](auto SW, auto SQ) {
    const float qs = cb < 5 ? 0.22360679774997896f * 1.4426950408889634f : 1.0f;
    f32x4 cs[2] = {Z4, Z4};
    LOGQ(1);
    f32x4 acc[2][2], st;
    auto mma = [&](int it, auto FIRST) {
      const char* xp0 = lds + L_XN + (it & 1) * 40960 + t0 * 20480 + lane0 * 16;
      const char* xp1 = lds + L_XN + (it & 1) * 40960 + (1 - t0) * 20480 + lane0 * 16;
      acc[0][0] = acc[0][1] = acc[1][0] = acc[1][1] = st = Z4;
      bf16x8 x[4][2];
#pragma unroll
      for (int p = 0; p < 3; ++p) { x[p][0] = *(const bf16x8*)(xp0 + p * 1024); x[p][1] = *(const bf16x8*)(xp1 + p * 1024); }
      __builtin_amdgcn_sched_barrier(0);
#pragma unroll
      for (int ks = 0; ks < 20; ++ks) {
        if (FIRST && ks + QW < 20) w_get(ks + QW);
#pragma unroll
        for (int q = 0; q < 4; ++q) {
          const int tt = 1 - (q >> 1), i = 1 - (q & 1);
          acc[i][tt] = SW ? mfma32(W[i][ks], x[ks & 3][tt], acc[i][tt]) : mfma32(x[ks & 3][tt], W[i][ks], acc[i][tt]);
          __builtin_amdgcn_sched_barrier(0);
          if (q < 2 && ks + 3 < 20) x[(ks + 3) & 3][q] = *(const bf16x8*)((q ? xp1 : xp0) + (ks + 3) * 1024);
          __builtin_amdgcn_sched_barrier(0);
        }
        st = mfma32(SQ ? x[ks & 3][0] : ones, x[ks & 3][0], st);
        if (FIRST) {
#pragma unroll
          for (int i = 0; i < 2; ++i) cs[i] = SW ? mfma32(W[i][ks], ones, cs[i]) : mfma32(ones, W[i][ks], cs[i]);
        }
        __builtin_amdgcn_sched_barrier(0);
      }
    };
    mma(0, std::integral_constant<bool, true>{});
#pragma unroll 1
    for (int it = 0; it < 4; ++it) {
      if (it) mma(it, std::integral_constant<bool, false>{});
      LOGQ(2 + 3 * it);
      FRESH_LANE();
      float* const sp = (float*)(lds + L_ST) + (it & 1) * 64;
      if (SQ) { if ((c >> 2) == g) sp[32 + t0 * 16 + c] = (c & 3) == 0 ? st[0] : ((c & 3) == 1 ? st[1] : ((c & 3) == 2 ? st[2] : st[3])); }
      else if (g == 0) sp[t0 * 16 + c] = st[0];
      sync();
      LOGQ(3 + 3 * it);
      if (it == 0) fetch(2);
      const bool late = r + 48 >= 55;
      if (it == 1 && !late) fetch(3);
      const unsigned so = (unsigned)((r + 16 * it) * (cb < 5 ? 64 * QST : KVT));
      auto stat = [&](float sm, float sq, float& mean, float& rstd) {
        mean = sm * (1.0f / DM);
        rstd = __builtin_amdgcn_rsqf(__builtin_fmaxf(sq * (1.0f / DM) - mean * mean, 0.f) + 1e-5f);
      };
#pragma unroll
      for (int tt = 0; tt < 2; ++tt) {
        const int tq = tt ^ t0;
        float mean[4], rstd[4];
        if (SW) {
          stat(sp[tq * 16 + c], sp[32 + tq * 16 + c], mean[0], rstd[0]);
          rstd[0] *= qs;
        } else {
          const f32x4 sm = *(const f32x4*)(sp + tq * 16 + 4 * g), sq = *(const f32x4*)(sp + 32 + tq * 16 + 4 * g);
#pragma unroll
          for (int k = 0; k < 4; ++k) stat(sm[k], sq[k], mean[k], rstd[k]);
        }
#pragma unroll
        for (int i = 0; i < 2; ++i) {
          float v[4];
#pragma unroll
          for (int k = 0; k < 4; ++k) v[k] = (acc[i][tt][k] - mean[SW ? 0 : k] * cs[i][SW ? k : 0]) * rstd[SW ? 0 : k];
          __builtin_amdgcn_raw_buffer_store_b64((u32x2){pack2(v[0], v[1]), pack2(v[2], v[3])}, ro, vo[i][tt], so, 0);
        }
      }
      LOGQ(4 + 3 * it);
      if (it == 2 && late) lds_spin<L_MISC + 68, 0>(lds0, 5u);
    }
  };
  typedef std::integral_constant<bool, false> F_; typedef std::integral_constant<bool, true> T_;
  if (cb < 10) { if (w >> 1) body(T_{}, T_{}); else body(T_{}, F_{}); }
  else { if (w >> 1) body(F_{}, T_{}); else body(F_{}, F_{}); }
}

__device__ __forceinline__ void t2_mem(const bf16x8 fr, const bf16x8 q0, const bf16x8 q1, const __amdgpu_buffer_rsrc_t rs, unsigned o, int h, int j, float inv, int lane) {
  const f32x4 Z4 = {0.f, 0.f, 0.f, 0.f};
  const f32x4 d0 = mfma32(fr, q0, Z4) * inv, d1 = mfma32(fr, q1, Z4) * inv;
  const unsigned a0 = pack2n(d0[0], d0[1]), a1 = pack2n(d0[2], d0[3]), b0 = pack2n(d1[0], d1[1]), b1 = pack2n(d1[2], d1[3]);
  const auto r0 = __builtin_amdgcn_permlane32_swap(a0, b0, false, false), r1 = __builtin_amdgcn_permlane32_swap(a1, b1, false, false);
  SC1_STORE8S(((u32x2){r0[1], r1[1]}), ts_mem(h, j) + (lane >> 5) * 256 + (lane & 12) * 16 + (lane & 16) * 2 + (lane & 3) * 8, o);
}

template <int w>
__device__ __forceinline__ void rec_waves(const RecArgs& a, char* lds, __amdgpu_buffer_rsrc_t rs, unsigned& tlast) {
  PIN_S(rs);
  const int lane = __builtin_amdgcn_workitem_id_x() & 63, b = __builtin_amdgcn_workgroup_id_x(), c = lane & 15, g = lane >> 4, wid = w; (void)wid;
  const int gp = (b & 7) * 2 + (b >> 7), m = (b >> 3) & 15, h0 = 2 * gp, h = h0 + (m >> 3), j = m & 7;
  float* const misc = (float*)(lds + L_MISC);
  const f32x4 Z4 = {0.f, 0.f, 0.f, 0.f};
  LOGP(0);
  __builtin_amdgcn_s_setprio(3);
  const unsigned lds0 = (unsigned)(uintptr_t)(LAS char*)lds;
  if (!a.layer) img_fetch<12>(a, lds0, h, j, w, lane);
  constexpr int NC = w < 2 ? 2 : 3;
  const int far = ((b >> 2) & 1) ^ 1;
  auto chunk = [&](int f) { return f == 0 ? 4 * far + w : (w == 2 && f == 1 ? 8 + far : (w == 3 && f == 2 ? 9 - far : 4 - 4 * far + w)); };
  bf16x16 Wqk[3][NC];
  u32x4 bm0[NC][2];
#pragma unroll
  for (int f = 0; f < 2 * NC; ++f) bm0[f >> 1][f & 1] = *(const u32x4*)(a.mem0 + (size_t)(c & 7) * DM + 64 * chunk(f >> 1) + 16 * g + 8 * (f & 1));
  if (!a.layer) asm volatile("s_waitcnt vmcnt(0)" ::: "memory");
  BAR();
  LOGP(9);
  const unsigned ck = __builtin_amdgcn_readfirstlane(*(const unsigned*)(misc + 11));
  const unsigned flip = (ck ^ (unsigned)(b << 11)) & 8192u;
#pragma unroll
  for (int t = 0; t < 3; ++t)
#pragma unroll
    for (int f = 0; f < NC; ++f) Wqk[t][f] = cat16(wqk_frag(lds, t, 2 * chunk(f), lane), wqk_frag(lds, t, 2 * chunk(f) + 1, lane));
  if (a.layer) {
    if (w == 0) {
      for (unsigned spins = 0; spins < SPIN_CAP; ++spins) {
        if (__builtin_amdgcn_raw_buffer_load_b32(rs, (b & 7) * 256, (unsigned)OFF_RDY, 16) >= 16u) break;
        __builtin_amdgcn_s_sleep(4);
        asm volatile("" ::: "memory");
      }
      lds_inc<L_MISC + 84>(lds0);
    }
    lds_wait((unsigned*)(misc + 21), 2u);
#pragma unroll
    for (int f = 0; f < 2 * NC; ++f) PIN_V(bm0[f >> 1][f & 1]);
#pragma unroll
    for (int i = 0; i < 10; ++i) x_fetch(x_src(a, 0, b), lds0, 0, 10 * w + i, lane);
  }
  const bf16x8 SelLo = sel_frag(c, 0);
  bf16x8 ONES = mk8(0x3f803f80u, 0x3f803f80u, 0x3f803f80u, 0x3f803f80u);
  if (w < 2) PIN_V(ONES);
  bool dead = false;
  float inv = 0.f;
  unsigned* const cnt2 = (unsigned*)(misc + 7);
  unsigned l2off[2 * NC];
#pragma unroll
  for (int f = 0; f < 2 * NC; ++f) { l2off[f] = acc_off(16 * chunk(f >> 1) + 4 * g + 2 * (f & 1) + (c >> 3), c & 7) ^ flip; PIN_V(l2off[f]); }
  int sidx = (c >> 3) ? 0xEEEE : 0x4444;
  PIN_V(sidx);
  const unsigned p1off = (unsigned)((w == 0 ? (h & 2) * 1024 + j * 256 + (h & 1) * 64 + (g & 1) * 8 : (h & 3) * 32768 + j * 512 + (g >> 1) * 256 + (g & 1) * 64) + (c & 4) * 32 + (c & 3) * 16);
  unsigned p1v = (c < 8) && (w != 0 || g >= 2) ? p1off : 0x7ffffff0u, p1cur = 0x7ffffff0u;
  PIN_V(p1v); PIN_V(p1cur);
  const int hd = (c >> 2) & 1, mr = hd ? 3 - (c & 3) : (c & 3);
  const unsigned l1off = (unsigned)(((h0 + hd) & 3) * 32768 + g * 512 + (w & 1) * 128 + (c >> 3) * 64 + mr * 16);
  const unsigned l1offm = (unsigned)((h0 & 2) * 1024 + (g + 4 * (c >> 3)) * 256 + (w & 1) * 128 + hd * 64 + mr * 16);
  const int plane = (lane & ~6) | ((lane & 2) << 1) | ((lane & 4) >> 1);
  unsigned acco = c < 8 ? (acc_off(64 * far + m * 4 + g, 4 * (w & 1) + mr) + (unsigned)(hd * 8)) ^ flip : 0x7ffffff0u;
  unsigned acco2 = c >= 8 ? (acc_off(64 - 64 * far + m * 4 + g, 4 * (w & 1) + mr) + (unsigned)(hd * 8)) ^ flip
                          : (c < 4 ? (acc_off(128 + m * 2 + (g & 1), 4 * (w & 1) + mr) + (unsigned)((g >> 1) * 8)) ^ flip : 0x7ffffff0u);
  if (w < 2) { PIN_V(acco); PIN_V(acco2); }
  unsigned bmo[NC];
#pragma unroll
  for (int f = 0; f < NC; ++f) { bmo[f] = (unsigned)(chunk(f) * 1024 + lane * 16); PIN_V(bmo[f]); }
  bf16x8 bm[NC];
  auto bm_put = [&](int f) { *(bf16x8*)(lds + L_BM + bmo[f]) = bm[f]; __builtin_amdgcn_sched_barrier(0); };
  f32x4 acc[3] = {Z4, Z4, Z4};
  const int rt2 = ((c >> 3) * 8 + 4 * (g & 1)) * 28 + 16 + (c & 7);
  unsigned redo[3] = {(unsigned)(4 * g * 28 + c), (unsigned)(g < 2 ? (4 * g + 2) * 28 + c : rt2), (unsigned)(g < 2 ? (4 * g + 10) * 28 + c : rt2 + 56)};
#pragma unroll
  for (int k = 0; k < 3; ++k) { redo[k] = L_RED1 + w * 1792 + redo[k] * 4; PIN_V(redo[k]); }
  auto red_put = [&]() {
    auto hs = [&](float x, float y) { const auto sw = __builtin_amdgcn_permlane32_swap(f2u(x), f2u(y), false, false); return u2f(sw[0]) + u2f(sw[1]); };
#pragma unroll
    for (int k = 0; k < 3; ++k) {
#pragma unroll
      for (int i = 0; i < 2; ++i) *(float*)(lds + redo[k] + i * 112) = k == 0 ? hs(acc[0][i], acc[1][i]) : hs(acc[k - 1][2 + i], acc[2][2 * k - 2 + i]);
      __builtin_amdgcn_sched_barrier(0);
    }
  };
#pragma unroll
  for (int f = 0; f < NC; ++f) {
    const u32x4 u = bm0[f][0], v = bm0[f][1];
    bm[f] = c < 8 ? mk8(u[0], u[2], v[0], v[2]) : mk8(u[1], u[3], v[1], v[3]);
    bm_put(f);
#pragma unroll
    for (int t = 0; t < 3; ++t) acc[t] = smfma64(bm[f], Wqk[t][f], acc[t], sidx);
  }
  red_put();
  VM_DRAIN();
  lds_arrive((unsigned*)(misc + 6), lane);
  lds_wait((unsigned*)(misc + 6), 8u);
  x_fetch_n<10>(x_src(a, 1, b), lds0, 1, 10 * w, lane * 16);
  LOGP(1);
  STAMP_DECL(); ARRIVE_DECL();
  bf16x8 fr, qx[2];
  auto kq_get = [&](int seg) {
    const char* p = lds + (w == 1 ? L_KX : L_QX) + (seg % NKQ) * 2048 + lane * 16;
    if (w == 1 || w == 2) { qx[0] = *(const bf16x8*)p; qx[1] = *(const bf16x8*)(p + 1024); }
  };
  unsigned qo4 = (unsigned)(L_RED1 + (w == 1 ? (c & 7) * 112 + (c >> 3) * 3584 : c * 112) + g * 16), qo2 = qo4 + 64u - (unsigned)(g * 8), qo2b = qo2 + 3584u;
  if (w < 3) { PIN_V(qo4); PIN_V(qo2); if (w != 1) PIN_V(qo2b); }
  auto pub1 = [&](unsigned off, unsigned o1) {
    if (w == 0) fr = qk_frag(lds + qo4, lds + qo2, lds + qo2b);
    else {
      const f32x4 a0 = *(const f32x4*)(lds + qo4), a1 = *(const f32x4*)(lds + qo4 + 1792);
      const f32x2 b0 = *(const f32x2*)(lds + qo2), b1 = *(const f32x2*)(lds + qo2 + 1792);
      float v[6];
#pragma unroll
      for (int i = 0; i < 6; ++i) v[i] = i < 4 ? a0[i] + a1[i] : b0[i - 4] + b1[i - 4];
#pragma unroll
      for (int i = 0; i < 6; ++i) v[i] += dppf<DPP_ROR8>(v[i]);
      fr = mk8(pack2(v[0], v[1]), pack2(v[2], v[3]), pack2(v[4], v[5]), 0u);
    }
    const f32x4 sc = mfma32(w == 0 ? fr : qx[0], fr, Z4);
    if (w == 0) SC1_STORE8S(((u32x2){pack2n(sc[0], sc[1]), pack2n(sc[2], sc[3])}), off, o1);
    else {
      const f32x4 s2 = mfma32(qx[1], fr, Z4);
      __builtin_amdgcn_raw_buffer_store_b128((u32x4){pack2n(sc[0], sc[1]), pack2n(sc[2], sc[3]), pack2n(s2[0], s2[1]), pack2n(s2[2], s2[3])}, rs, off, o1, 16);
    }
  };
  unsigned o1 = sct_slot(ck, 0u), o1m = scm_slot(ck, 0u);
  unsigned t2o = ts_tok(h, j) + (unsigned)((c >> 2) * 256 + g * 64 + (c & 3) * 16);
  TokScores t2;
  TokTask tk;
  PvTask2 pv2;
  if (w == 2) tk.init(h, j, lane);
  if (w == 3) { PIN_V(t2o); pv2.init(gp, lane); }
  if (w == 0) pub1(p1v, o1m);
  VM_DRAIN();
  BAR();
  const unsigned tst = now32() | 1u;
  if (w == 3 && ST_X) __builtin_amdgcn_raw_buffer_store_b32(tst, rs, lane < 2 ? ts_piece(b >> 2) - ACC_TS + (b & 3) * 4 + lane * 8192 : 0x7ffffff0, (unsigned)(OFF_ACC + (SR - 1) * ACC_STEP) + ACC_TS, 16);
  LOGP(2);
  kq_get(0);
  if (w == 1) pub1(p1v, o1);
  x_fetch_n<10>(x_src(a, 2, b), lds0, 2, 10 * w, lane * 16);
  if (w == 3) { t2.get(lds, 0, lane); t2.run(0); t2.run(1); t2.store(rs, t2o, ts_slot(ck, 0)); }
  LOGP(3);
  unsigned o2 = (unsigned)OFF_ACC, o2p = 0u, tw = 0u;
  asm volatile("" : "+s"(o2p));
  unsigned tc0 = 0u, tstar = 0u;
  int dmin = 0, dmax = 0;
  int e1 = 100000;
  unsigned offs = 100u;
  unsigned o4 = 0u;
  unsigned o3, o3p;
  u32x16 P = {0u, 0u, 0u, 0u, 0u, 0u, 0u, 0u, 0u, 0u, 0u, 0u, 0u, 0u, 0u, 0u}, V = P;
  u32x8 V8 = {0u, 0u, 0u, 0u, 0u, 0u, 0u, 0u};
  if (w < 2) asm volatile("" : "+{v[208:223]}"(P), "+{v[184:191]}"(V8), "+{v[192:207]}"(V));
  unsigned tso = lane < 2 ? ts_piece(b >> 2) + (unsigned)((b & 3) * 4 + lane * 8192) : 0x7ffffff0u;
  if (w < 2) PIN_V(tso);
  float minf = g >= 2 ? -INFINITY : 0.f;
  if (w < 2) PIN_V(minf);
  int sv = NV - 1;
  bf16x8 vox[5];
  unsigned vma[3], need2;
  u32x4 slo = {0u, 0u, 0u, 0u}, shi = slo;
  float k1 = 0.f;
  const u16* xsrc;
  float invv = 1.0f;
  auto prep0 = [&](int s) {
    xsrc = x_src(a, s + 3, b);
    if (w == 2) { o3p = ts_slot(ck, (s + 1 - LAG3) & 3); o3 = ts_slot(ck, imax(s, 0)); o4 = pt_slot(ck, imax(s - LAG3, 0)); PIN_S(o3); }
    if (w == 3) { o3p = pt_slot(ck, (s - LAG4) & 3); o4 = ts_slot(ck, s + 1); }
    PIN_S(xsrc); PIN_S(o3p); PIN_S(o4);
    __builtin_amdgcn_sched_barrier(0);
  };
  auto prep = [&](int s) {
    o2 = (unsigned)OFF_ACC + (unsigned)(s % SR) * (unsigned)ACC_STEP;
    sv = sv == NV - 1 ? 0 : sv + 1;
    PIN_S(o2); PIN_S(sv);
    if (w < 2) {
      PIN_S(tc0);
      o1 = sct_slot(ck, s % SR); o1m = scm_slot(ck, s % SR);
      need2 = 4u * (unsigned)(s + 1) + 2u * ((unsigned)(s + 2) / NSEG);
      PIN_S(o1); PIN_S(o1m); PIN_S(need2);
      __builtin_amdgcn_sched_barrier(0);
      kq_get(s);
    }
    invv = misc[4 + (s & 1)];
  };
  auto prep2 = [&](int s) {
#pragma unroll
    for (int k = 0; k < 3; ++k) {
      const int t = k == 0 ? 2 : (k == 1 ? far : 1 - far);
      const char* p = lds + L_VOX + sv * 5120 + t * 1024 + (plane & 47) * 16 + (g & 1) * 8;
      auto rd = [&](const char* q) { const u32x2 a0 = *(const u32x2*)q, a1 = *(const u32x2*)(q + 256); return mk8(a0[0], a0[1], a1[0], a1[1]); };
      vox[k == 0 ? 0 : 2 * k - 1] = rd(p);
      if (k) vox[2 * k] = rd(p + 3072);
      vma[k] = lds0 + (unsigned)(L_VOM + sv * 1280 + t * 256 + (plane & 31) * 8);
      PIN_V(vma[k]);
    }
    __builtin_amdgcn_sched_barrier(0);
    if (F0A) { xsrc = x_src(a, s + 3, b); PIN_S(xsrc); }
    const unsigned iu = __builtin_amdgcn_readfirstlane(f2u(invv));
    if (__builtin_expect(iu != f2u(inv), 0)) {
      inv = u2f(iu);
#pragma unroll
      for (int i = 0; i < 8; ++i) {
        u32x4& m = i < 4 ? slo : shi;
        m[i & 3] = (c >> 1) == i ? ((c & 1) ? iu & 0xffff0000u : iu >> 16) : 0u;
      }
      k1 = KEXP * inv;
    }
    PIN_V(k1); PIN_V(slo); PIN_V(shi);
    asm volatile("" : "+v"(vox[0]), "+v"(vox[1]), "+v"(vox[2]), "+v"(vox[3]), "+v"(vox[4]));
    __builtin_amdgcn_sched_barrier(0);
  };
  auto rows = [&](int s) {
    const int dl = spin_until(tw);
    if (w < 2) tc0 = cyc32();
    __builtin_amdgcn_sched_barrier(0);
    STAMP(1);
    unsigned spins = 0;
    auto pass = [&](bool first) {
      u32x4 x[2 * NC];
#pragma unroll
      for (int f = 0; f < NC; ++f) {
        if (f && w < 2 && first) { prep(s); asm volatile(".rept %0\n\ts_nop 3\n\t.endr" :: "n"(P2_G1)); }
        else if (f) asm volatile(".rept %0\n\ts_nop 3\n\t.endr" :: "n"(w < 2 ? P2_G0 : (f == 1 ? P2_G2 : P2_G3)));
        __builtin_amdgcn_sched_barrier(0);
        x[2 * f] = SC1_LOADS(l2off[2 * f], o2p); x[2 * f + 1] = SC1_LOADS(l2off[2 * f + 1], o2p);
        __builtin_amdgcn_sched_barrier(0);
      }
      if (first) lds_inc<L_MISC + 92>(lds0);
      if (first && w < 2 && PR_P != 3) __builtin_amdgcn_s_setprio(PR_P);
      if (first) { if (w >= 2) prep(s); else prep2(s); }
      if (w >= 2 && first && P2_W) lds_spin<L_MISC + 92, 0>(lds0, 4u * (unsigned)s);
      if (w == 2 && first) tk.store(rs, o4, s > LAG3 ? 0u : 0x7ff00000u);
      if (w == 3 && first) t2.store(rs, t2o, o4);
      if (first && w < 2) {
        p1cur = p1v; offs = (offs - (unsigned)dl) & ~3u;
        PIN_V(p1cur); PIN_S(offs);
        tstar = tc0 + (unsigned)(e1 + D1_M + (w == 1 ? D1_W1 : 0)) - offs * 20u;
        dmin = offs < 40u ? D1_MIN : REC_D1;
        dmin = s < EARLY_N ? REC_D1 + EARLY_D1 : dmin;
        dmax = REC_D1;
        PIN_S(tstar); PIN_S(dmin); PIN_S(dmax);
      }
      __builtin_amdgcn_sched_barrier(0);
      unsigned ok = 32u;
#pragma unroll
      for (int t = 0; t < 3; ++t) { acc[t] = Z4; PIN_V(acc[t]); }
      if (first && w < 2 && PR_P != 3) __builtin_amdgcn_s_setprio(3);
      __builtin_amdgcn_sched_barrier(0);
      bool r = true;
#pragma unroll
      for (int f = 0; f < NC; ++f) {
        unsigned p[4];
#pragma unroll
        for (int k = 0; k < 2; ++k) {
          const u32x4 v = x[2 * f + k];
          p[2 * k] = pack2((float)(int)v[0], (float)(int)v[1]); p[2 * k + 1] = pack2((float)(int)v[2], (float)(int)v[3]);
          asm volatile("" : "+v"(p[2 * k]), "+v"(p[2 * k + 1]));
          __builtin_amdgcn_sched_barrier(0);
        }
        ok = (ok & x[2 * f][0] & x[2 * f][2]) & (x[2 * f + 1][0] & x[2 * f + 1][2]);
        PIN_V(ok);
        __builtin_amdgcn_sched_barrier(0);
        bm[f] = mk8(p[0], p[1], p[2], p[3]);
        acc[0] = smfma64(bm[f], Wqk[0][f], acc[0], sidx);
        __builtin_amdgcn_sched_barrier(0);
        bm_put(f);
        acc[1] = smfma64(bm[f], Wqk[1][f], acc[1], sidx);
        __builtin_amdgcn_sched_barrier(0);
        if (f == NC - 1) { u64 bad = __builtin_amdgcn_ballot_w64(ok == 0u); PIN_S(bad); r = bad == 0ull; }
        __builtin_amdgcn_sched_barrier(0);
        acc[2] = smfma64(bm[f], Wqk[2][f], acc[2], sidx);
        __builtin_amdgcn_sched_barrier(0);
      }
      red_put();
      return r;
    };
    if (__builtin_expect(!pass(true), 0)) {
      FC(0);
      do {
        if (dead || ++spins > SPIN_CAP) { dead = true; break; }
        asm volatile("" ::: "memory");
      } while (!pass(false));
    }
    STAMPV(12, spins); LOGF(1, spins);
    TSTAMP(2, now32()); TSTAMP(3, spins);
  };
  if (w >= 2) prep0(-1);
  prep(0);
  if (w < 2) prep2(0);
  VM_DRAIN();
  CODE_PAD((PADR >> (4 * w)) & 15);
#pragma unroll 1
  for (int s = 0;; ++s) {
    if (__builtin_expect(o2p != 0u, 1)) rows(s);
    if (w < 2 && __builtin_expect(s == NSEG - 1, 0)) break;
    STAMP(2);
    BAR();
    STAMP(3);
    if (w < 2) pub1(p1cur, w == 0 ? o1m : o1);
    if (w == 2) tk.load(rs, o3p);
    if (w == 3) pv2.load(rs, o3p);
    const unsigned tp1 = cyc32();
    __builtin_amdgcn_sched_barrier(0);
    if (w == 2) {
      __builtin_amdgcn_s_setprio(W2_P);
      prep0(s);
      if (W2_D) spin_cyc(tp1 + W2_D);
      fr = qk_frag(lds + qo4, lds + qo2, lds + qo2b);
      t2_mem(fr, qx[0], qx[1], rs, o3, h, j, invv * KEXP, lane);
      __builtin_amdgcn_sched_barrier(0);
      asm volatile("s_waitcnt vmcnt(1)" ::: "memory");
      lds_inc<L_MISC + 28>(lds0);
      kq_get(s + 1);
    }
    if (w == 3) {
      __builtin_amdgcn_s_setprio(T4_P);
      prep0(s);
      t2.get(lds, s + 1, lane); t2.run(0); t2.run(1);
      __builtin_amdgcn_sched_barrier(0);
    }
    if (w >= 2 && __builtin_expect(s == NSEG - 2, 0)) { VM_DRAIN(); lds_inc<L_MISC + 28>(lds0); }
    if (w >= 2) __builtin_amdgcn_s_setprio(UN_P);
    TSTAMP(0, now32());
    STAMP(4);
    __builtin_amdgcn_sched_barrier(0);
    unsigned tpub = 0;
    u32x2 sw;
    if (w == 2) {
      STAMP(5);
      __builtin_amdgcn_sched_barrier(0);
      STAMP(6);
      ARRIVE(0);
      tk.run(j, lane, SelLo);
      if (__builtin_expect(s == NSEG - 1, 0)) { tlast = tp1; break; }
      if (F2B) { lds_spin<L_MISC + 48, 1>(lds0, 2u * (unsigned)(s + 1)); x_fetch_n<F2B>(xsrc, lds0, s + 3, P2B, lane * 16); }
      __builtin_amdgcn_s_setprio(SP_P);
    }
    if (w == 3) {
      pv2.run<false>(lds, sv < 1 + LAG4 ? sv + NV - 1 - LAG4 : sv - 1 - LAG4, lane, rs, 0u);
      if (__builtin_expect(s == NSEG - 1, 0)) { tlast = tp1; break; }
      lds_inc<L_MISC + 112>(lds0);
      STAMP(11);
      __builtin_amdgcn_s_setprio(SP_P);
      STAMP(5);
      ARRIVE(0);
      if (F3) { lds_spin<L_MISC + 48, 1>(lds0, 2u * (unsigned)(s + 1)); x_fetch_n<F3>(xsrc, lds0, s + 3, 40 - F3C - F3, lane * 16); }
      STAMP(6);
    }
    if (w < 2) {
      __builtin_amdgcn_s_setprio(D1_P);
      x_fetch_n<F0A>(xsrc, lds0, s + 3, PA + w * F0A, lane * 16);
      int dq = imin((int)(tstar - tp1), dmax);
      dq = dq < -D1_LATE ? dmax : dq;
      PIN_S(dq);
      spin_exact(tp1 + (unsigned)imax(dq, dmin));
      STAMP(5);
      u32x32 S;
      auto sc_load = [&]() {
        asm volatile("buffer_load_dwordx4 v[224:227], %2, %4, %5 offen sc1\n\t.rept %7\n\ts_nop 3\n\t.endr\n\t"
                     "buffer_load_dwordx4 v[228:231], %3, %4, %6 offen sc1\n\tbuffer_load_dwordx4 v[232:235], %3, %4, %6 offen offset:2048 sc1\n\t"
                     "buffer_load_dwordx4 v[236:239], %3, %4, %6 offen offset:256 sc1\n\tbuffer_load_dwordx4 v[240:243], %3, %4, %6 offen offset:2304 sc1\n\t"
                     "v_mov_b32 v220, 0x42000000\n\tv_mov_b32 v221, 0\n\tv_mov_b32 v222, 0x42000000\n\tv_mov_b32 v223, 0"
                     : "=&{v[224:255]}"(S), "+{v[208:223]}"(P) : "v"(l1offm), "v"(l1off), "s"(rs), "s"(o1m), "s"(o1), "n"(P1_G) : "memory");
      };
      sc_load();
      __builtin_amdgcn_sched_barrier(0);
      lds_inc<L_MISC + 48>(lds0);
      STAMP(6);
      ARRIVE(0);
      lds_spin<L_MISC + 28, 0>(lds0, need2);
      __builtin_amdgcn_s_setprio(3);
      asm volatile("ds_read_b64 v[192:193], %3\n\tds_read_b64 v[196:197], %3 offset:768\n\tds_read_b64 v[200:201], %4\n\tds_read_b64 v[204:205], %4 offset:768\n\tds_read_b64 v[184:185], %2"
                   : "+{v[184:191]}"(V8), "+{v[192:207]}"(V) : "v"(vma[0]), "v"(vma[1]), "v"(vma[2]) : "memory");
      __builtin_amdgcn_sched_barrier(0);
      {
        const int de = imin((int)(tp1 - tc0) - e1, D1_C);
        e1 += s == 0 ? 0 : de >> (s < EARLY_N && de < -D1_C ? 0 : D1_G);
        PIN_S(e1);
      }
      unsigned sn = (unsigned)(s + 1);
      PIN_V(sn);
      STAMP(11);
      __builtin_amdgcn_sched_barrier(0);
      {
        unsigned spins = 0;
        u64 miss, bad;
#define MM(d, a, b, c) "v_mfma_f32_16x16x32_bf16 " d ", " a ", " b ", " c "\n\t"
#define FM(d, x, r, ctrl) "v_fmac_f32_dpp " d ", " x ", " r " " ctrl "\n\t"
#define ID_ "quad_perm:[0,1,2,3] row_mask:0xf bank_mask:"
#define HM_ "row_half_mirror row_mask:0xf bank_mask:"
#define R8_ "row_ror:8 row_mask:0xf bank_mask:"
#define MI_ "row_mirror row_mask:0xf bank_mask:"
        auto soft = [&]() {
          asm volatile(
            "s_waitcnt vmcnt(4)\n\t" MM("v[244:247]", "%6", "v[224:227]", "0") "v_min3_u32 v214, v224, v225, v226\n\t"
            "s_waitcnt vmcnt(3)\n\t" MM("v[248:251]", "%6", "v[228:231]", "0") "v_min3_u32 v214, v214, v227, v228\n\tv_min3_u32 v214, v214, v229, v230\n\t"
            "s_waitcnt vmcnt(2)\n\t" MM("v[248:251]", "%6", "v[232:235]", "v[248:251]") "v_min3_u32 v214, v214, v231, v232\n\t"
            "v_add_f32_dpp v212, v244, v244 " R8_ "0x3\n\tv_add_f32_dpp v213, v245, v245 " R8_ "0x3\n\t"
            "s_waitcnt vmcnt(1)\n\t" MM("v[248:251]", "%7", "v[236:239]", "v[248:251]")
            "v_add_f32_dpp v212, v246, v246 " R8_ "0xc\n\tv_add_f32_dpp v213, v247, v247 " R8_ "0xc\n\t"
            "s_waitcnt vmcnt(0) lgkmcnt(0)\n\t" MM("v[248:251]", "%7", "v[240:243]", "v[248:251]")
            "v_fma_f32 v212, v212, %14, %15\n\tv_fma_f32 v213, v213, %14, %15\n\tv_exp_f32 v216, v212\n\tv_exp_f32 v217, v213\n\t"
            "v_min3_u32 v214, v214, v233, v234\n\tv_cvt_pk_bf16_f32 v208, v216, v217\n\tv_min3_u32 v214, v214, v235, v236\n\tds_read_b96 v[188:190], %16 offset:%18\n\t"
            "v_exp_f32 v216, v248\n\tv_mov_b32_dpp v209, v208 " R8_ "0xf\n\tv_exp_f32 v217, v249\n\tv_exp_f32 v218, v250\n\t"
            MM("v[252:255]", "%8", "v[208:211]", "0") "v_exp_f32 v219, v251\n\tv_cvt_pk_bf16_f32 v232, v216, v217\n\t"
            MM("v[224:227]", "v[192:195]", "v[208:211]", "0") "v_cvt_pk_bf16_f32 v233, v218, v219\n\tv_min3_u32 v214, v214, v237, v238\n\tv_mov_b32_dpp v234, v232 " R8_ "0xf\n\t"
            MM("v[228:231]", "v[196:199]", "v[208:211]", "0") "v_mov_b32_dpp v235, v233 " R8_ "0xf\n\t"
            MM("v[244:247]", "v[204:207]", "v[208:211]", "0") "v_min3_u32 v214, v214, v239, v240\n\t"
            MM("v[252:255]", "%8", "v[232:235]", "v[252:255]") MM("v[224:227]", "%10", "v[232:235]", "v[224:227]") "v_min3_u32 v214, v214, v241, v242\n\t"
            MM("v[228:231]", "%11", "v[232:235]", "v[228:231]") "s_waitcnt lgkmcnt(0)\n\tv_sub_u32 v215, v190, %17 clamp\n\t"
            MM("v[236:239]", "v[200:203]", "v[208:211]", "0") "v_min3_u32 v214, v214, v243, v215\n\tv_cmp_eq_u32 %4, 0, v214\n\tv_rcp_f32 v216, v252\n\t"
            MM("v[240:243]", "v[184:187]", "v[208:211]", "0") "v_mul_f32 v216, v216, v188\n\tv_subrev_u32 v215, 0x2f800000, v252\n\t"
            MM("v[236:239]", "%12", "v[232:235]", "v[236:239]") FM("v220", "v224", "v216", ID_ "0x1") FM("v221", "v225", "v216", ID_ "0x1")
            "v_mov_b32_dpp v217, v216 " HM_ "0xf\n\t"
            MM("v[244:247]", "%13", "v[232:235]", "v[244:247]") FM("v220", "v230", "v216", ID_ "0x2") FM("v221", "v231", "v216", ID_ "0x2")
            MM("v[240:243]", "%9", "v[232:235]", "v[240:243]") FM("v220", "v228", "v217", HM_ "0x1") FM("v221", "v229", "v217", HM_ "0x1")
            "v_cmp_ge_u32 vcc, v215, v189\n\t" FM("v220", "v226", "v217", HM_ "0x2") FM("v221", "v227", "v217", HM_ "0x2")
            "s_or_b64 %5, %4, vcc"
            : "+{v[224:255]}"(S), "+{v[208:223]}"(P), "+{v[184:191]}"(V8), "+{v[192:207]}"(V), "=&s"(miss), "=&s"(bad)
            : "v"(slo), "v"(shi), "v"(ONES), "v"(vox[0]), "v"(vox[1]), "v"(vox[2]), "v"(vox[3]), "v"(vox[4]), "v"(k1), "v"(minf), "v"(lds0), "s"(s), "n"(L_MISC + 32)
            : "vcc", "scc", "memory");
        };
        soft();
        if (__builtin_expect(bad != 0ull, 0)) {
          if (miss) FC(1); else FC(2);
          while (miss != 0ull && !dead) {
            if (++spins > SPIN_CAP) dead = true;
            asm volatile("s_nop 7\n\ts_nop 7" ::: "memory");
            sc_load(); soft();
          }
          if (bad != 0ull) {
            u32x4 y[5];
#pragma unroll
            for (int k = 0; k < 4; ++k) y[k] = SC1_LOADS(l1off + (k & 1) * 2048 + (k >> 1) * 256, o1);
            y[4] = SC1_LOADS(l1offm, o1m);
            const f32x4 d0 = mfma32(as8(slo), as8(y[4]), Z4);
            f32x4 d1 = mfma32(as8(shi), as8(y[3]), mfma32(as8(shi), as8(y[2]), mfma32(as8(slo), as8(y[1]), mfma32(as8(slo), as8(y[0]), Z4))));
            const bool lo = c < 8;
            const float m0 = __builtin_fmaf((lo ? d0[0] : d0[2]) + dppf<DPP_ROR8>(lo ? d0[2] : d0[0]), k1, minf);
            const float m1 = __builtin_fmaf((lo ? d0[1] : d0[3]) + dppf<DPP_ROR8>(lo ? d0[3] : d0[1]), k1, minf);
            float mx = fmx(fmx(fmx(d1[0], d1[1]), d1[2]), fmx(fmx(d1[3], m0), m1));
            mx = fmx(mx, dppf<DPP_ROR8>(mx));
            mx = x32mx(x16mx(mx));
            d1 -= mx;
            const unsigned mm = pack2(__builtin_amdgcn_exp2f(m0 - mx), __builtin_amdgcn_exp2f(m1 - mx));
            const unsigned t01 = pack2(__builtin_amdgcn_exp2f(d1[0]), __builtin_amdgcn_exp2f(d1[1])), t23 = pack2(__builtin_amdgcn_exp2f(d1[2]), __builtin_amdgcn_exp2f(d1[3]));
            const bf16x8 pm = mk8(mm, dpp_ror8(mm), 0u, 0u), pt = mk8(t01, t23, dpp_ror8(t01), dpp_ror8(t23));
            const float r = __builtin_amdgcn_rcpf(mfma32(ONES, pt, mfma32(ONES, pm, Z4))[0]) * u2f(V8[4]), rm = dppf<0x141>(r);
            f32x4 o[5];
#pragma unroll
            for (int k = 0; k < 5; ++k) o[k] = mfma32(vox[k], pt, mfma32(k ? mk8(V[4 * k - 4], V[4 * k - 3], 0u, 0u) : mk8(V8[0], V8[1], 0u, 0u), pm, Z4));
#pragma unroll
            for (int i = 0; i < 2; ++i) {
              const float oa = dppf<0x141>(o[2][i]), ob = dppf<0x141>(o[1][2 + i]);
              P[12 + i] = f2u(__builtin_fmaf(hd ? ob : oa, rm, __builtin_fmaf(hd ? o[2][2 + i] : o[1][i], r, i ? 0.f : 32.0f)));
            }
            P[8] = f2u(r); P[9] = f2u(rm);
#pragma unroll
            for (int i = 0; i < 4; ++i) { S[12 + i] = f2u(o[3][i]); S[16 + i] = f2u(o[0][i]); S[20 + i] = f2u(o[4][i]); }
            asm volatile("s_nop 7\n\ts_nop 7" : "+{v[224:255]}"(S), "+{v[208:223]}"(P));
          }
        }
        STAMP(7); STAMPV(13, spins); LOGF(0, spins);
        {
          asm volatile(".if %11 == 0\n\tds_write_b32 %8, %12 offset:%10\n\t.endif\n\t"
                       "v_cvt_i32_f32 v218, v220\n\tv_cvt_i32_f32 v219, v221\n\tv_and_or_b32 v218, v218, %7, 2\n\tbuffer_atomic_add_x2 v[218:219], %3, %5, %6 offen\n\t"
                       ".if %11 == 1\n\tds_write_b32 %8, %12 offset:%10\n\t.endif\n\t"
                       FM("v222", "v236", "v216", R8_ "0x4") FM("v223", "v237", "v216", R8_ "0x4") "ds_read_b64 %2, %8 offset:%9\n\t"
                       FM("v222", "v238", "v217", MI_ "0x8") FM("v223", "v239", "v217", MI_ "0x8") "v_permlane32_swap_b32 v240, v242\n\t"
                       FM("v222", "v244", "v217", MI_ "0x4") FM("v223", "v245", "v217", MI_ "0x4") "v_permlane32_swap_b32 v241, v243\n\t"
                       FM("v222", "v246", "v216", R8_ "0x8") FM("v223", "v247", "v216", R8_ "0x8") ".if %11 == 2\n\tds_write_b32 %8, %12 offset:%10\n\t.endif\n\t"
                       FM("v222", "v240", "v216", ID_ "0x1") FM("v223", "v241", "v216", ID_ "0x1") FM("v222", "v242", "v217", HM_ "0x1") FM("v223", "v243", "v217", HM_ "0x1")
                       "v_cvt_i32_f32 v212, v222\n\tv_cvt_i32_f32 v213, v223\n\tv_and_or_b32 v212, v212, %7, 2\n\tbuffer_atomic_add_x2 v[212:213], %4, %5, %6 offen\n\t"
                       ".if %11 == 3\n\tds_write_b32 %8, %12 offset:%10\n\ts_waitcnt lgkmcnt(1)\n\t.else\n\ts_waitcnt lgkmcnt(0)\n\t.endif"
                       : "+{v[224:255]}"(S), "+{v[208:223]}"(P), "=&v"(sw) : "v"(acco), "v"(acco2), "s"(rs), "s"(o2), "s"(~63u), "v"(lds0), "n"(L_MISC + 96),
                         "n"(L_MISC + 104 + 4 * (w & 1)), "n"(REL_AT), "v"(sn) : "memory");
          __builtin_amdgcn_sched_barrier(0);
          tpub = now32();
          asm volatile("buffer_atomic_umax %0, %1, %2, %3 offen" :: "v"(tpub | 1u), "v"(tso), "s"(rs), "s"(o2) : "memory");
          STAMP(10);
        }
      }
    }
    STAMP(8);
    ARRIVE(1);
    o2p = o2;
    PIN_S(o2p);
    if (w < 2) {
      if (__builtin_expect(__builtin_amdgcn_readfirstlane(sw[1]) <= (unsigned)s, 0)) {
        lds_spin<L_MISC + 100, 0>(lds0, (unsigned)s + 1u);
        sw[0] = __hip_atomic_load((unsigned*)(misc + 24), __ATOMIC_RELAXED, __HIP_MEMORY_SCOPE_WORKGROUP);
      }
    } else {
      u32x4 q;
      asm volatile("1:\n\tds_read_b128 %0, %1 offset:%3\n\t.if %4\n\ts_sleep 1\n\t.endif\n\ts_waitcnt lgkmcnt(0)\n\tv_min3_u32 v201, v201, v202, v203\n\tv_cmp_ge_u32 vcc, %2, v201\n\ts_cbranch_vccnz 1b"
                   : "=&{v[200:203]}"(q) : "v"(lds0), "s"(s), "n"(L_MISC + 96), "n"(SP_SL) : "vcc", "memory");
      __builtin_amdgcn_s_setprio(3);
      tpub = now32();
      sw[0] = q[0];
    }
    STAMP(9);
    static_assert(F3C == 0 && F2C == 0 && F0C == 0, "nothing behind the atomics");
    LOGT(0, tpub); LOGT(1, tb2_);
    TSTAMP(1, tpub);
    ARRIVE_OUT();
    const unsigned an = __builtin_amdgcn_readfirstlane(sw[0]);
    const int ahead = (int)(an - tpub);
    tw = tpub + (unsigned)(an == 0u || ahead > 1000 ? SCH_FIRST : imax(ahead, w < 2 ? SCH_DMIN : 0));
    offs = tw - an;
  }
  STAMP_OUT();
  LOGP(4);
  __builtin_amdgcn_s_setprio(3);
  if (w < 2) { BAR(); tlast = cyc32(); }
  if (w == 2) tk.store(rs, o4);
  if (w == 3) {
    const unsigned oo = op_slot(NSEG - 2 - LAG4) + (unsigned)((m * 32 + gp) * 1280);
    const char* sp = lds + L_STG + 2048;
    const u32x4 v0 = *(const u32x4*)(sp + lane * 16), v1 = *(const u32x4*)(sp + 1280 + lane * 16), v2 = *(const u32x4*)(sp + (g & 1) * 1280 + 1024 + c * 16);
    SC1_STORE(v0, oo + lane * 16); SC1_STORE(v1, oo + 20480 + lane * 16); SC1_STORE(v2, lane < 32 ? oo + g * 20480 + 1024 + c * 16 : 0x7ffffff0u);
  }
  if (dead) tlast |= 1u; else tlast &= ~1u;
}

__device__ __forceinline__ void drain(const RecArgs& a, char* lds, const __amdgpu_buffer_rsrc_t rs, const int w, const unsigned tp1) {
  static_assert(LAG3 == 2 && LAG4 == 3 && LAG5 == 6, "drain schedule");
  const int lane = __builtin_amdgcn_workitem_id_x() & 63, b = __builtin_amdgcn_workgroup_id_x(), c = lane & 15;
  const int gp = (b & 7) * 2 + (b >> 7), m = (b >> 3) & 15, j = m & 7;
  unsigned* const misc = (unsigned*)(lds + L_MISC);
  const unsigned lds0 = (unsigned)(uintptr_t)(LAS char*)lds, ck = __builtin_amdgcn_readfirstlane(misc[11]);
  bool dead = tp1 & 1u;
  auto poll = [&](auto load, auto ready) {
    for (unsigned spins = 0;;) {
      asm volatile("" ::: "memory");
      load();
      if (ready() || dead) break;
      if (++spins > SPIN_CAP) dead = true;
      if (END_SL) __builtin_amdgcn_s_sleep(END_SL);
    }
  };
  LOGD(0);
#define TASK(ty, sg, tm) ((u64)((ty) | ((sg) - 56) << 2 | ((tm) / 100) << 5))
  u64 todo = w == 0 ? TASK(1, 63, END_E1) | TASK(2, 63, END_E2) << 12 | TASK(3, 62, END_E3) << 24
           : w == 1 ? TASK(1, 62, 0) | TASK(2, 62, END_E62) << 12 | TASK(3, 60, END_E3) << 24
           : w == 2 ? TASK(2, 60, 0) | TASK(3, 56, 0) << 12 : TASK(2, 61, END_E61) | TASK(3, 58, END_E59) << 12;
  TokTask tk;
  PvTask2 pv;
  OutTask ot;
  {
    const u32x4* p = (const u32x4*)(lds + L_DRN) + lane;
    const u32x4 q0 = p[0], q1 = p[64], q2 = p[128];
    tk.l0 = q0[0]; tk.l1 = q0[0] + 16384u; tk.lm = q0[1]; tk.s0 = q0[2]; tk.s1 = q0[3];
    pv.lo = q1[0]; pv.lm = q1[1]; pv.vr = q1[2]; pv.ma = q1[3]; pv.mz = q2[0]; pv.so = q2[1];
    pv.pa[0] = pv.pa[1] = pv.pb[0] = pv.pb[1] = (u32x4){0u, 0u, 0u, 0u};
    ot.t5f = q2[2]; ot.t5o = q2[3]; ot.sel = as8(p[192]);
  }
#pragma unroll 1
  for (int ti = 0; todo; todo >>= 12, ++ti) {
    const int ty = (int)todo & 3, sl = 56 + (((int)todo >> 2) & 7);
    const unsigned tm = tp1 + (((unsigned)todo >> 5) & 0x7fu) * 100u;
    if (ty == 1) {
      wait_cyc(tm);
      LOGD(3 * ti + 1);
      poll([&]() { tk.load(rs, ts_slot(ck, sl)); }, [&]() { return tk.ready(); });
      LOGD(3 * ti + 2);
      tk.run(j, lane, ot.sel); tk.store(rs, pt_slot(ck, sl));
    } else if (ty == 2) {
      const unsigned t0 = cyc32();
      if (sl >= NSEG - 2) { lds_wait(misc + 7, 4u * NSEG + 3u); wait_cyc(t0 + END_G2); }
      wait_cyc(tm);
      LOGD(3 * ti + 1);
      poll([&]() { pv.load(rs, pt_slot(ck, sl)); }, [&]() { return pv.ready(); });
      LOGD(3 * ti + 2);
      pv.run<true>(lds, sl % NV, lane, rs, op_slot(sl) + (unsigned)((m * 32 + gp) * 1280));
      if (sl == NSEG - 1) misc[14] = cyc32() | 1u;
    } else {
      if (sl >= NSEG - 4) { lds_wait(misc + 14, 1u); wait_cyc(__builtin_amdgcn_readfirstlane(misc[14]) + END_G3); }
      wait_cyc(tm);
      LOGD(3 * ti + 1);
      OutTask o2 = ot;
      poll([&]() { o2.load(a, rs, sl + 1, b); ot.load(a, rs, sl, b); }, [&]() { return ot.ready(b) & o2.ready(b); });
      LOGD(3 * ti + 2);
      o2.run(lane); o2.store(a, sl + 1, b, lane);
      ot.run(lane); ot.store(a, sl, b, lane);
    }
    LOGD(3 * ti + 3);
  }
  VM_DRAIN();
  if (w != 0) lds_inc<L_MISC + 64>(lds0);
  else {
    constexpr unsigned o2 = (unsigned)OFF_ACC + (unsigned)((NSEG - 1) % SR) * (unsigned)ACC_STEP;
    lds_wait(misc + 16, 3u);
    LOGD(10);
    if (lane == 0) __hip_atomic_fetch_add((gu64*)((GAS char*)(a.ws + o2) + acc_off(m * 4, 0)), (u64)1, RLX_AGENT);
    wait_until(now32() + SCH_DRAIN);
    wait_cyc(tp1 + END_E4);
    unsigned x;
    if (qkv_row(b) + 48 >= 55 && qkv_col(b) < 15) poll([&]() { x = __builtin_amdgcn_raw_buffer_load_b32(rs, acc_off(c * 4, 0), o2, 16); }, [&]() { return wave_all((x & 16u) != 0u); });
    lds_inc<L_MISC + 68>(lds0);
    LOGD(11);
  }
  if (qkv_row(b) + 48 >= 55 && qkv_col(b) < 15) {
    lds_wait(misc + 18, 8u);
    lds_spin<L_MISC + 68, 1>(lds0, 1u);
    x_fetch_n<10>(x_src(a, NSEG + 3, b), lds0, 3, 10 * w, lane * 16);
    VM_DRAIN();
    lds_inc<L_MISC + 68>(lds0);
  }
  LOGP(8);
}

__device__ __forceinline__ void proj_front(const RecArgs& a, char* lds, const __amdgpu_buffer_rsrc_t rs, const int v) {
  const int lane = __builtin_amdgcn_workitem_id_x() & 63, b = __builtin_amdgcn_workgroup_id_x(), c = lane & 15, g = lane >> 4;
  const int rb = (b & 7) + 8 * (b >> 5), cq = (b >> 3) & 3;
  const unsigned lds0 = (unsigned)(uintptr_t)(LAS char*)lds;
  unsigned* const cnt = (unsigned*)(lds + L_MISC) + 20;
  LOGF2(0);
  float* const red = (float*)(lds + L_RED1);
  float* const fin = red + 640;
  const f32x4 Z4 = {0.f, 0.f, 0.f, 0.f};
  x_fetch_n<8>((const u16*)(a.ws + OFF_OB) + (size_t)rb * 32 * DM, lds0, 0, 8 * v, lane * 16);
  bf16x8 W[2][20];
  const __amdgpu_buffer_rsrc_t rw = __builtin_amdgcn_make_buffer_rsrc(a.ws + OFF_WOBT + (size_t)(cq * 10 + 2 * v) * 20480, 0, 40960, 0x00020000);
  auto w_get = [&](int ks) {
#pragma unroll
    for (int i = 0; i < 2; ++i) W[i][ks] = as8(__builtin_amdgcn_raw_buffer_load_b128(rw, lane * 16 + (ks & 3) * 1024, (i * 20 + (ks & ~3)) * 1024, 0));
    __builtin_amdgcn_sched_barrier(0);
  };
#pragma unroll
  for (int ks = 0; ks < FW; ++ks) w_get(ks);
  const unsigned fo = (unsigned)(rb * 40960 + (cq * 5 + v) * 1024 + ((g >> 1) * 16 + c) * 16), f8 = fo + (g & 1) * 8, f16 = fo + (g & 1) * 20480;
  u32x2 xo[2][2];
#pragma unroll
  for (int k = 0; k < 4; ++k)
    xo[k >> 1][k & 1] = __builtin_bit_cast(u32x2, __hip_atomic_load((const gu64*)((const char*)a.xnew + f8 + (k >> 1) * 512 + (k & 1) * 20480), RLX_AGENT));
  asm volatile("s_waitcnt vmcnt(%0)" :: "n"(2 * FW + 4) : "memory");
  lds_inc<L_MISC + 80>(lds0);
  lds_wait(cnt, 5u);
  LOGF2(1);
  f32x4 acc[2][2] = {{Z4, Z4}, {Z4, Z4}};
  bf16x8 x[20][2];
#pragma unroll
  for (int ks = -3; ks < 20; ++ks) {
    if (ks >= 0 && ks + FW < 20) w_get(ks + FW);
#pragma unroll
    for (int t = 0; t < 2; ++t) {
      if (ks + 3 < 20) x[ks + 3][t] = *(const bf16x8*)(lds + L_XN + (t * 20 + ks + 3) * 1024 + lane * 16);
#pragma unroll
      for (int i = 0; i < 2 && ks >= 0; ++i) acc[i][t] = mfma32(W[i][ks < 0 ? 0 : ks], x[ks < 0 ? 0 : ks][t], acc[i][t]);
    }
    __builtin_amdgcn_sched_barrier(0);
  }
  u32x2 xp[2][2];
  PIN_V(acc[1][1]);
  LOGF2(2);
#pragma unroll
  for (int t = 0; t < 2; ++t) {
#pragma unroll
    for (int k = 0; k < 4; ++k) {
      const int i = k >> 1, q = k & 1;
      const unsigned r = pack2(acc[i][t][2 * q], acc[i][t][2 * q + 1]), x = xo[i][t][q];
      xp[i][t][q] = pack2(u2f(x << 16) + u2f(r << 16), u2f(x & 0xffff0000u) + u2f(r & 0xffff0000u));
      acc[i][t][2 * q] = u2f(xp[i][t][q] << 16); acc[i][t][2 * q + 1] = u2f(xp[i][t][q] & 0xffff0000u);
    }
    const f32x4 sm = acc[0][t] + acc[1][t];
    const float ml = x32sum(x16sum((sm[0] + sm[1]) + (sm[2] + sm[3]))) * (1.0f / 32.0f);
    float q = 0.f;
#pragma unroll
    for (int r = 0; r < 8; ++r) { const float d = acc[r >> 2][t][r & 3] - ml; q += d * d; }
    q = x32sum(x16sum(q));
    *(f32x2*)(red + (v * 32 + t * 16 + c) * 2) = (f32x2){ml, q};
  }
  lds_inc<L_MISC + 80>(lds0);
  if (v == 0) {
    lds_wait(cnt, 10u);
    LOGF2(3);
    float mw[5], ml = 0.f, m2 = 0.f;
#pragma unroll
    for (int i = 0; i < 5; ++i) { const f32x2 u = *(const f32x2*)(red + (i * 32 + (lane & 31)) * 2); mw[i] = u[0]; ml += u[0]; m2 += u[1]; }
    ml *= 0.2f;
#pragma unroll
    for (int i = 0; i < 5; ++i) m2 += 32.0f * (mw[i] - ml) * (mw[i] - ml);
    const unsigned xs = (unsigned)OFF_LNX + rb * 1024;
    if (lane < 32) SC1_STORE8S(((u32x2){f2u(ml), nz(f2u(m2))}), (cq * 32 + lane) * 8, xs);
    wait_cyc(cyc32() + FR_D);
    u32x2 x0, x1;
    for (unsigned spins = 0; spins < SPIN_CAP; ++spins) {
      x0 = __builtin_amdgcn_raw_buffer_load_b64(rs, lane * 8, xs, 16); x1 = __builtin_amdgcn_raw_buffer_load_b64(rs, 512 + lane * 8, xs, 16);
      if (wave_all(x0[1] != 0u && x1[1] != 0u)) break;
      asm volatile("" ::: "memory");
    }
    const float m0 = u2f(x0[0]), m1 = u2f(x1[0]);
    const float mean = x32sum(m0 + m1) * 0.25f;
    const float M2 = x32sum((u2f(x0[1]) + u2f(x1[1])) + 160.0f * ((m0 - mean) * (m0 - mean) + (m1 - mean) * (m1 - mean)));
    const float mb = rbf(mean), den = rbf(__builtin_sqrtf(rbf(rbf(M2 * (1.0f / DM)) + 1e-5f)));
    if (lane < 32) {
      fin[lane] = mb; fin[32 + lane] = 1.0f / den;
      if (cq == 0) __hip_atomic_store((GAS unsigned*)(a.ws + OFF_XNRM) + (rb * 32 + lane) * 4, f2u(1.02f * (M2 + DM * (mean - mb) * (mean - mb)) / (den * den)), RLX_AGENT);
    }
    lds_inc<L_MISC + 84>(lds0);
  }
  lds_wait(cnt + 1, 1u);
  LOGF2(4);
  const __amdgpu_buffer_rsrc_t rx = __builtin_amdgcn_make_buffer_rsrc(a.ws + OFF_XN, 0, (int)(OFF_QB - OFF_XN), 0x00020000);
  auto put = [&](const u32x2 p0, const u32x2 p1, unsigned so) {
    const auto r0 = __builtin_amdgcn_permlane16_swap(p0[0], p1[0], false, false), r1 = __builtin_amdgcn_permlane16_swap(p0[1], p1[1], false, false);
    __builtin_amdgcn_raw_buffer_store_b128((u32x4){r0[0], r1[0], r0[1], r1[1]}, rx, f16, so, 16);
  };
#pragma unroll
  for (int i = 0; i < 2; ++i) {
    u32x2 y[2];
#pragma unroll
    for (int t = 0; t < 2; ++t) {
      const float mb = fin[t * 16 + c], rden = fin[32 + t * 16 + c];
      y[t] = (u32x2){pack2(rbf(acc[i][t][0] - mb) * rden, rbf(acc[i][t][1] - mb) * rden), pack2(rbf(acc[i][t][2] - mb) * rden, rbf(acc[i][t][3] - mb) * rden)};
    }
    put(y[0], y[1], i * 512);
  }
  VM_DRAIN();
  lds_inc<L_MISC + 80>(lds0);
#pragma unroll
  for (int i = 0; i < 2; ++i) put(xp[i][0], xp[i][1], (unsigned)(OFF_XB - OFF_XN) + i * 512);
  if (v == 0) {
    lds_wait(cnt, 15u);
    if (rb < 4 && lane < 8) __hip_atomic_fetch_add((GAS unsigned*)(a.ws + OFF_RDY) + lane * 64, 1u, RLX_AGENT);
  }
  LOGF2(5);
}

__global__ __launch_bounds__(512, 2) void rec_kernel(RecArgs a) {
  __shared__ __attribute__((aligned(16))) char lds[L_BYTES];
  const int tid = __builtin_amdgcn_workitem_id_x(), wid = __builtin_amdgcn_readfirstlane(tid >> 6), w = wid & 3;
  const __amdgpu_buffer_rsrc_t rs = __builtin_amdgcn_make_buffer_rsrc(a.ws, 0, (int)COMM_BYTES, 0x00020000);
  if (tid < 32) ((float*)(lds + L_MISC))[tid] = (tid == 4 || tid == 5) ? 1.0f : 0.0f;
  BAR();
  if (a.layer && wid < 5) proj_front(a, lds, rs, wid);
  if (wid < 4) {
    unsigned tlast;
    if (w == 0) rec_waves<0>(a, lds, rs, tlast);
    else if (w == 1) rec_waves<1>(a, lds, rs, tlast);
    else if (w == 2) rec_waves<2>(a, lds, rs, tlast);
    else rec_waves<3>(a, lds, rs, tlast);
    drain(a, lds, rs, w, tlast);
  } else {
    unsigned vo[2][2];
    if (w == 0) token_waves<0>(a, lds, rs, vo);
    else if (w == 1) token_waves<1>(a, lds, rs, vo);
    else if (w == 2) token_waves<2>(a, lds, rs, vo);
    else token_waves<3>(a, lds, rs, vo);
    qkv_tail(a, lds, w, vo);
  }
}

#if defined(__HIP_DEVICE_COMPILE__)
#pragma clang attribute pop
#endif

constexpr int KQ = S_ / 4;
#define ATT_P1 6
#define ATT_P2 10
#define ATT_P3 14
#define ATT_THR 6.0f
#define ATT_NQ 4
#define ATT_LMAX 1.0e12f
#if defined(__HIP_DEVICE_COMPILE__)
#pragma clang attribute push(__attribute__((target("no-packed-fp32-ops"))), apply_to = function)
#endif
__global__ __launch_bounds__(256 * ATT_NQ, 4) void attb_kernel(const u16* __restrict__ Qp, const u16* __restrict__ KV, u16* __restrict__ O) {
  __shared__ f32x4 mo_[ATT_NQ][4][2][3][64];
  __shared__ float mm_[ATT_NQ][4][2][64];
  const int tid = __builtin_amdgcn_workitem_id_x(), bid = __builtin_amdgcn_workgroup_id_x();
  __shared__ unsigned sink[64];
  own_code<5000>(tid, sink);
  const int lane = tid & 63, wid = __builtin_amdgcn_readfirstlane(tid >> 6), kh = wid / ATT_NQ, r = lane & 31, hh = lane >> 5;
  const int hd = (bid & 7) * 4 + ((bid >> 3) & 3), qb = (bid >> 5) * ATT_NQ + wid % ATT_NQ;
  const int q0 = qb * 64;
  bf16x8 qa[2]; u32x2 qb2[2];
#pragma unroll
  for (int qt = 0; qt < 2; ++qt) {
    { const u32x2* qp = (const u32x2*)(Qp + ((size_t)hd * S_ + q0 + qt * 32 + r) * QST + 8 * hh); const u32x2 q0_ = qp[0], q1_ = qp[1]; qa[qt] = mk8(q0_[0], q0_[1], q1_[0], q1_[1]); }
    const u32x2 t = *(const u32x2*)(Qp + ((size_t)hd * S_ + q0 + qt * 32 + r) * QST + 16);
    qb2[qt] = t;
  }
  const __amdgpu_buffer_rsrc_t rk = __builtin_amdgcn_make_buffer_rsrc((void*)(KV + ((size_t)hd * 64 + kh * 16) * (KVT / 2)), 0, 16 * KVT, 0x00020000);
  unsigned ok1 = lane * 16, ok2 = hh ? 1600 : 1024 + r * 8, ov = r > 20 ? 1u << 30 : 1280 + (hh * 21 + r) * 16;
  f32x16 o[2];
  float m[2];
  const f32x16 Z16 = {0.f, 0.f, 0.f, 0.f, 0.f, 0.f, 0.f, 0.f, 0.f, 0.f, 0.f, 0.f, 0.f, 0.f, 0.f, 0.f};
  int odd = 0xDDDD;
  asm volatile("" : "+v"(odd));
  struct KvT { u32x4 k1, v0, v1; u32x2 k2; };
  auto fetch_k = [&](KvT& x, int t) {
    x.k1 = __builtin_amdgcn_raw_buffer_load_b128(rk, ok1, imin(t, KQ / 32 - 1) * KVT, 0); x.k2 = __builtin_amdgcn_raw_buffer_load_b64(rk, ok2, imin(t, KQ / 32 - 1) * KVT, 0);
    __builtin_amdgcn_sched_barrier(0);
  };
  auto fetch_v = [&](KvT& x, int t) {
    x.v0 = __builtin_amdgcn_raw_buffer_load_b128(rk, ov, imin(t, KQ / 32 - 1) * KVT, 0); x.v1 = __builtin_amdgcn_raw_buffer_load_b128(rk, ov + 672, imin(t, KQ / 32 - 1) * KVT, 0);
    __builtin_amdgcn_sched_barrier(0);
  };
  auto fetch = [&](int t) { KvT x; __builtin_amdgcn_sched_barrier(0); fetch_k(x, t); fetch_v(x, t); return x; };
  auto qk1 = [&](const KvT& x, int qt) { return __builtin_amdgcn_mfma_f32_32x32x16_bf16(as8(x.k1), qa[qt], Z16, 0, 0, 0); };
  auto qk2 = [&](const KvT& x, int qt, f32x16 c) { return __builtin_amdgcn_mfma_f32_32x32x8bf16_1k(__builtin_bit_cast(v4i16_t, x.k2), __builtin_bit_cast(v4i16_t, qb2[qt]), c, 0, 0, 0); };
  auto pv = [&](const u32x4 v, const f32x16 p, int hi, f32x16 c) {
    const f32x8 ph = hi ? __builtin_shufflevector(p, p, 8, 9, 10, 11, 12, 13, 14, 15) : __builtin_shufflevector(p, p, 0, 1, 2, 3, 4, 5, 6, 7);
    return __builtin_amdgcn_smfmac_f32_32x32x32_bf16(__builtin_bit_cast(hbf16x8, v), __builtin_bit_cast(hbf16x16, ph), c, odd, 0, 0);
  };
  auto tile = [&](const KvT& x, auto MODE) {
    f32x16 sa[2];
#pragma unroll
    for (int qt = 0; qt < 2; ++qt) sa[qt] = qk1(x, qt);
#pragma unroll
    for (int qt = 0; qt < 2; ++qt) sa[qt] = qk2(x, qt, sa[qt]);
#pragma unroll
    for (int qt = 0; qt < 2; ++qt) {
      if (MODE != 0) {
        float mx = fmx(fmx(sa[qt][0], sa[qt][1]), sa[qt][2]);
#pragma unroll
        for (int i = 3; i < 15; i += 2) mx = fmx(fmx(mx, sa[qt][i]), sa[qt][i + 1]);
        mx = fmx(mx, sa[qt][15]);
        if (MODE == 1 || !wave_all(mx <= ATT_THR)) {
          mx = x32mx(mx);
          if (MODE != 1) mx = fmx(mx, 0.f);
          const float mn = m[qt] + mx;
          const unsigned h1 = f2bf(mn); const float r1 = mn - bf2f(h1);
          const unsigned h2 = f2bf(r1); const float r2 = r1 - bf2f(h2);
          const unsigned h3 = f2bf(r2);
          const float me = (bf2f(h1) + bf2f(h2)) + bf2f(h3);
          const float dm = me - m[qt];
          m[qt] = me;
          if (hh) qb2[qt] = (u32x2){(h1 | (h2 << 16)) ^ 0x80008000u, h3 ^ 0x8000u};
          const float alpha = __builtin_amdgcn_exp2f(-dm);
#pragma unroll
          for (int i = 0; i < 16; ++i) { if (MODE != 1) o[qt][i] *= alpha; sa[qt][i] -= dm; }
        }
      }
#pragma unroll
      for (int i = 0; i < 16; ++i) sa[qt][i] = __builtin_amdgcn_exp2f(sa[qt][i]);
      asm volatile("" : "+v"(sa[qt]));
      if (MODE == 1) { o[qt] = Z16; asm volatile("" : "+v"(o[qt])); }
      o[qt] = pv(x.v1, sa[qt], 1, pv(x.v0, sa[qt], 0, o[qt]));
      __builtin_amdgcn_sched_barrier(0);
    }
  };
  typedef std::integral_constant<int, 1> I1; typedef std::integral_constant<int, 2> I2;
  auto reset = [&]() {
    asm volatile("" : "+v"(ok1), "+v"(ok2), "+v"(ov) :: "memory");
    m[0] = m[1] = 0.f;
    if (hh) qb2[0] = qb2[1] = (u32x2){0u, 0u};
  };
  auto slow = [&]() {
    reset();
    tile(fetch(0), I1{});
#pragma unroll 1
    for (int t = 1; t < KQ / 32; ++t) tile(fetch(t), I2{});
  };
  f32x16 sc[2];
  auto phase = [&](int qt, const u32x4 vp, const KvT& xk, const u32x4 v0, auto mid) {
    auto ex = [&](int i0, int i1) {
#pragma unroll
      for (int i = i0; i < i1; ++i) sc[qt][i] = __builtin_amdgcn_exp2f(sc[qt][i]);
      asm volatile("" : "+v"(sc[qt]));
      __builtin_amdgcn_sched_barrier(0);
    };
    o[1 - qt] = pv(vp, sc[1 - qt], 1, o[1 - qt]);
    __builtin_amdgcn_sched_barrier(0);
    ex(0, 3);
    sc[1 - qt] = qk1(xk, 1 - qt);
    __builtin_amdgcn_sched_barrier(0);
    ex(3, 6);
    sc[1 - qt] = qk2(xk, 1 - qt, sc[1 - qt]);
    __builtin_amdgcn_sched_barrier(0);
    ex(6, 10);
    o[qt] = pv(v0, sc[qt], 0, o[qt]);
    __builtin_amdgcn_sched_barrier(0);
    mid();
    ex(10, 16);
  };
  auto fast = [&]() {
    reset();
    KvT xa = fetch(0), xb = fetch(1);
    tile(xa, I1{});
    fetch_k(xa, 2);
    sc[0] = qk2(xb, 0, qk1(xb, 0)); sc[1] = Z16;
    asm volatile("" : "+v"(sc[1]));
    auto step = [&](KvT& x, KvT& y, int t) {
      phase(0, y.v1, x, x.v0, [&]() { fetch_v(y, t + 1); fetch_k(x, t + 2); });
      phase(1, x.v1, y, x.v0, []() {});
    };
    step(xb, xa, 1);
#pragma unroll 1
    for (int t = 2; t < KQ / 32; t += 2) {
      if (t == ATT_P1) __builtin_amdgcn_s_setprio(2);
      if (t == ATT_P2) __builtin_amdgcn_s_setprio(1);
      if (t == ATT_P3) __builtin_amdgcn_s_setprio(0);
      step(xa, xb, t);
      step(xb, xa, t + 1);
    }
    o[1] = pv(xb.v1, sc[1], 1, o[1]);
  };
  if (ATT_P1) __builtin_amdgcn_s_setprio(3);
  fast();
  {
    float big = 0.f;
#pragma unroll
    for (int qt = 0; qt < 2; ++qt) {
      big = fmx(big, hh ? o[qt][8] * (3.0e38f / ATT_LMAX) : __builtin_fabsf(o[qt][8]));
#pragma unroll
      for (int i = 0; i < 12; ++i) if (i != 8) big = fmx(big, (i > 8 && hh) ? 0.f : __builtin_fabsf(o[qt][i]));
    }
    if (__builtin_expect(!wave_all(big <= 3.0e38f), 0)) slow();
  }
  f32x4 (*mo)[2][3][64] = mo_[wid % ATT_NQ];
  float (*mm)[2][64] = mm_[wid % ATT_NQ];
#pragma unroll
  for (int qt = 0; qt < 2; ++qt) {
#pragma unroll
    for (int k = 0; k < 3; ++k) mo[kh][qt][k][lane] = (f32x4){o[qt][4 * k], o[qt][4 * k + 1], o[qt][4 * k + 2], o[qt][4 * k + 3]};
    mm[kh][qt][lane] = m[qt];
  }
  const int qt = kh & 1, grp = kh >> 1, row = q0 + qt * 32 + r, k0 = hd * BD + 4 * hh + 8 * grp;
  auto oaddr = [&](int k) { return O + (((size_t)(row >> 4) * 20 + (k >> 5)) * 64 + ((k & 31) >> 3) * 16 + (row & 15)) * 8 + (k & 7); };
  u16 *op0 = oaddr(k0), *op1 = oaddr(k0 + 16);
  asm volatile("" : "+v"(op0), "+v"(op1));
  asm volatile("s_waitcnt lgkmcnt(0)\n\ts_barrier" ::: "memory");
  {
    float a[4], l[4];
    f32x4 u[4], x[4];
#pragma unroll
    for (int i = 0; i < 4; ++i) { a[i] = mm[i][qt][lane]; l[i] = mo[i][qt][2][lane | 32][0]; u[i] = mo[i][qt][grp][lane]; x[i] = mo[i][qt][2][lane]; }
    const float mn = fmx(fmx(a[0], a[1]), fmx(a[2], a[3]));
#pragma unroll
    for (int i = 0; i < 4; ++i) a[i] = __builtin_amdgcn_exp2f(a[i] - mn);
    const float inv = __builtin_amdgcn_rcpf((l[0] * a[0] + l[1] * a[1]) + (l[2] * a[2] + l[3] * a[3]));
    const f32x4 v = ((u[0] * a[0] + u[1] * a[1]) + (u[2] * a[2] + u[3] * a[3])) * inv;
    *(GAS u32x2*)op0 = (u32x2){pack2(v[0], v[1]), pack2(v[2], v[3])};
    if (grp == 0) {
      const f32x4 y = ((x[0] * a[0] + x[1] * a[1]) + (x[2] * a[2] + x[3] * a[3])) * inv;
      if (hh == 0) *(GAS u32x2*)op1 = (u32x2){pack2(y[0], y[1]), pack2(y[2], y[3])};
    }
  }
}
#if defined(__HIP_DEVICE_COMPILE__)
#pragma clang attribute pop
#endif

__global__ __launch_bounds__(640, 3) void proj_kernel(const u16* __restrict__ Of, const u16* __restrict__ Wf, const u16* __restrict__ xold,
                                                      u16* __restrict__ xnew) {
  __shared__ __attribute__((aligned(16))) char ob[40960];
  const int tid = threadIdx.x, lane = tid & 63, w = __builtin_amdgcn_readfirstlane(tid >> 6), g = lane >> 4, c = lane & 15;
  __shared__ unsigned sink[64];
  own_code<1500>(tid, sink);
  const int rb = (blockIdx.x & 7) + 8 * (blockIdx.x >> 5), cb = (blockIdx.x >> 3) & 3;
  const int col = cb * 160 + w * 16 + 4 * g;
  const u16* wp = Wf + (size_t)(cb * 10 + w) * 20 * 512 + lane * 8;
  u32x4 st[4];
#pragma unroll
  for (int i = 0; i < 4; ++i) st[i] = *(const u32x4*)(Of + (size_t)rb * 40 * 512 + (i * 640 + tid) * 8);
  bf16x8 wf[20];
#pragma unroll
  for (int ks = 0; ks < 20; ++ks) wf[ks] = *(const bf16x8*)(wp + (size_t)ks * 512);
  u32x2 xo[2];
#pragma unroll
  for (int t = 0; t < 2; ++t) xo[t] = *(const u32x2*)(xold + (((size_t)(rb * 2 + t) * 20 + (col >> 5)) * 64 + ((col & 31) >> 3) * 16 + c) * 8 + (col & 7));
#pragma unroll
  for (int i = 0; i < 4; ++i) *(u32x4*)(ob + (i * 640 + tid) * 16) = st[i];
  BAR();
  f32x4 acc[2] = {{0.f, 0.f, 0.f, 0.f}, {0.f, 0.f, 0.f, 0.f}};
#pragma unroll
  for (int ks = 0; ks < 20; ++ks)
#pragma unroll
    for (int t = 0; t < 2; ++t) acc[t] = mfma32(wf[ks], *(const bf16x8*)(ob + (t * 20 + ks) * 1024 + lane * 16), acc[t]);
#pragma unroll
  for (int t = 0; t < 2; ++t) {
    const unsigned r01 = pack2(acc[t][0], acc[t][1]), r23 = pack2(acc[t][2], acc[t][3]);
    const unsigned p01 = pack2(u2f(xo[t][0] << 16) + u2f(r01 << 16), u2f(xo[t][0] & 0xffff0000u) + u2f(r01 & 0xffff0000u));
    const unsigned p23 = pack2(u2f(xo[t][1] << 16) + u2f(r23 << 16), u2f(xo[t][1] & 0xffff0000u) + u2f(r23 & 0xffff0000u));
    *(u32x2*)(xnew + (size_t)(rb * 32 + t * 16 + c) * DM + col) = (u32x2){p01, p23};
  }
}

extern "C" void kernel_launch(void* const* d_in, const int* in_sizes, int n_in, void* d_out, int out_size, void* d_ws,
                              size_t ws_size, hipStream_t stream) {
  const u16* A_x = (const u16*)d_in[0];
  const u16* A_mem0 = (const u16*)d_in[1];
  const u16* A_Wq = (const u16*)d_in[2];
  const u16* A_Wk = (const u16*)d_in[3];
  const u16* A_Wv = (const u16*)d_in[4];
  const u16* A_Wo = (const u16*)d_in[5];
  const u16* B_wqkv = (const u16*)d_in[8];
  const u16* B_wo = (const u16*)d_in[9];
  char* ws = (char*)(((uintptr_t)d_ws + ((size_t)WS_OFF << 20) + WS_AL - 1) & ~(uintptr_t)(WS_AL - 1));
  u16* xa = (u16*)(ws + OFF_XA); u16* Ob = (u16*)(ws + OFF_OB);

  prologue_kernel<<<256, 640, 0, stream>>>(ProArgs{A_x, A_Wq, A_Wk, A_Wv, A_Wo, B_wqkv, B_wo, ws});
  for (int layer = 0; layer < 2; ++layer) {
    rec_kernel<<<256, 512, 0, stream>>>(RecArgs{A_mem0, A_Wq, A_Wk, (const u16*)(ws + OFF_XB), xa, ws, (unsigned)layer, 0u});
    attb_kernel<<<BH * 32 / ATT_NQ, 256 * ATT_NQ, 0, stream>>>((const u16*)(ws + OFF_QB), (const u16*)(ws + OFF_KV), Ob);
  }
  proj_kernel<<<256, 640, 0, stream>>>(Ob, (const u16*)(ws + OFF_WOBT), xa, (u16*)d_out);
}
```

```cpp
#include <hip/hip_runtime.h>
#include <hip/hip_bf16.h>
#include <stdint.h>
#include <type_traits>

typedef unsigned short u16;
typedef unsigned long long u64;
typedef __attribute__((ext_vector_type(8))) short bf16x8;
typedef __attribute__((ext_vector_type(16))) short bf16x16;
typedef __attribute__((ext_vector_type(4))) float f32x4;
typedef __attribute__((ext_vector_type(2))) float f32x2;
typedef __attribute__((ext_vector_type(16))) float f32x16;
typedef __attribute__((ext_vector_type(8))) float f32x8;
typedef __attribute__((ext_vector_type(4))) unsigned u32x4;
typedef __attribute__((ext_vector_type(2))) unsigned u32x2;
typedef __attribute__((ext_vector_type(3))) unsigned u32x3;
typedef __attribute__((ext_vector_type(8))) unsigned u32x8;
typedef __attribute__((ext_vector_type(16))) unsigned u32x16;
typedef __attribute__((ext_vector_type(32))) unsigned u32x32;
#define GAS __attribute__((address_space(1)))
#define LAS __attribute__((address_space(3)))
typedef GAS unsigned long long gu64;
#define BAR() do { __builtin_amdgcn_s_waitcnt(0xC07F); asm volatile("s_waitcnt lgkmcnt(0)\n\ts_barrier" ::: "memory"); } while (0)

constexpr int S_ = 2048, DM = 640;
constexpr int NH = 32, DH = 192, HD = NH * DH;
constexpr int MEM = 8, SEG = 32, NSEG = S_ / SEG;
constexpr int BH = 32, BD = 20;

constexpr size_t al256(size_t x) { return (x + 255) & ~(size_t)255; }
constexpr int LAG3 = 2, LAG4 = 3, LAG5 = 6;
#define SR_ 8
constexpr int SR = SR_;
constexpr unsigned SCT_STEP = 131072, OFF_SCM = SR * SCT_STEP, SCM_STEP = 32768;
constexpr int ACC_NB = 192;
constexpr size_t ACC_STEP = 65536;
constexpr unsigned ACC_TS = ACC_NB * 256;
constexpr size_t ACC_LAYER = SR * ACC_STEP;
constexpr size_t OFF_ACC = 3 * 524288;
static_assert(SR == 8, "regions");
__device__ __forceinline__ unsigned ts_piece(int i) { return ACC_TS + (unsigned)(i < 128 ? (i >> 1) * 256 + (i & 1) * 16 : 128 + (i & 1) * 8192); }
constexpr size_t OFF_LNX = OFF_SCM + SR * SCM_STEP;
constexpr size_t OFF_RDY = OFF_LNX + 65536;
constexpr size_t PG = 2097152;
#define RING_ 4
constexpr int RING = RING_;
constexpr size_t TS_SLOT = (size_t)NH * 8 * 2560, PT_SLOT = (size_t)NH * 2560, OP_SLOT = (size_t)256 * 2560;
__device__ __forceinline__ unsigned ts_tok(int h, int j) { return (unsigned)((h & 15) * 32768 + (j >> 2) * 16384 + (j & 3) * 2048); }
__device__ __forceinline__ unsigned ts_mem(int h, int j) { return (unsigned)(524288 + ((h & 15) >> 1) * 16384 + (h & 1) * 4096 + j * 512); }
__device__ __forceinline__ unsigned pt_tok(int h) { return (unsigned)(((h & 15) >> 2) * 16384 + (h & 3) * 2048); }
__device__ __forceinline__ unsigned pt_mem(int h) { return (unsigned)(65536 + (h & 15) * 512); }
constexpr size_t OFF_X = 2 * PG + TS_SLOT + 3 * PT_SLOT, X_BYTES = 7 * OP_SLOT + 4 * PT_SLOT;
constexpr size_t OFF_OP = OFF_X + TS_SLOT + 4 * PT_SLOT;
constexpr size_t COMM_BYTES = OFF_X + X_BYTES + RING * OP_SLOT;
__device__ __forceinline__ unsigned near_half(unsigned ck, unsigned o) { return o + ((ck >> (o >= 2 * PG ? 8 : 4)) & 8192u); }
__device__ __forceinline__ unsigned ts_slot(unsigned ck, int sl) {
  return near_half(ck, sl == NSEG - 1 ? (unsigned)OFF_X : ((sl & 3) == 3 ? (unsigned)(2 * PG) : (unsigned)PG + (unsigned)(sl & 3) * (unsigned)TS_SLOT));
}
__device__ __forceinline__ unsigned pt_slot(unsigned ck, int sl) {
  return near_half(ck, sl >= NSEG - 4 ? (unsigned)(OFF_X + TS_SLOT) + (unsigned)(sl - (NSEG - 4)) * (unsigned)PT_SLOT
                                      : ((sl & 3) == 0 ? (unsigned)(PG + 3 * TS_SLOT) : (unsigned)(2 * PG + TS_SLOT - PT_SLOT) + (unsigned)(sl & 3) * (unsigned)PT_SLOT));
}
__device__ __forceinline__ unsigned op_slot(int sl) { return (unsigned)OFF_OP + (unsigned)(sl >= NSEG - 6 ? sl - (NSEG - 6) : 6 + sl % RING) * (unsigned)OP_SLOT; }
static_assert(RING == 4 && OFF_OP <= 3 * PG && OFF_RDY + 2048 <= OFF_ACC && OFF_ACC + ACC_LAYER == PG && TS_SLOT % 16384 == 0 && PT_SLOT % 16384 == 0, "pages");
static_assert(TS_SLOT == OP_SLOT && RING >= 4 && NSEG % SR == 0 && SR >= 5 && X_BYTES % 4096 == 0 && X_BYTES <= 256 * 20 * 1024, "slots");
constexpr size_t OFF_WB = (COMM_BYTES + 16383) / 16384 * 16384, WB_BYTES = (size_t)256 * 200 * 1024;
__device__ __forceinline__ unsigned wb_off(int F) { const unsigned o = (unsigned)((F >> 3) * 16384 + (F & 7) * 1024); return o | ((((unsigned)OFF_WB + o) >> 8) & 8192u); }
constexpr size_t OFF_PHI = OFF_WB + WB_BYTES;
constexpr size_t OFF_BNRM = OFF_PHI + 1024;
constexpr size_t OFF_LAT = OFF_BNRM + 1024;
constexpr size_t OFF_XNRM = OFF_LAT + 2048;
constexpr size_t OFF_WQKVBT = OFF_XNRM + al256(S_ * 16);
constexpr size_t OFF_WOBT = OFF_WQKVBT + al256((size_t)3 * DM * DM * 2);
constexpr size_t OFF_XN = OFF_WOBT + al256((size_t)DM * DM * 2);
constexpr int QST = BD;
constexpr size_t OFF_XA = OFF_XN + al256((size_t)S_ * DM * 2);
constexpr size_t OFF_XB = OFF_XA + al256((size_t)S_ * DM * 2);
constexpr size_t OFF_QB = OFF_XB + al256((size_t)S_ * DM * 2);
constexpr int KVT = 2624;
constexpr size_t OFF_KV = OFF_QB + al256((size_t)BH * S_ * QST * 2);
constexpr size_t OFF_OB = OFF_KV + al256((size_t)BH * 64 * KVT);
constexpr size_t OFF_DBG = OFF_OB + al256((size_t)S_ * DM * 2);
constexpr size_t WS_TOTAL = OFF_DBG + (size_t)2 * 64 * 8 * DM * 2;
#define WS_AL 2097152
#define WS_OFF 0
static_assert(WS_TOTAL + WS_AL + ((size_t)WS_OFF << 20) <= ((size_t)256 << 20), "workspace budget");

#if defined(__HIP_DEVICE_COMPILE__)
#pragma clang attribute push(__attribute__((target("no-packed-fp32-ops"))), apply_to = function)
#endif
__device__ __forceinline__ float u2f(unsigned v) { return __builtin_bit_cast(float, v); }
__device__ __forceinline__ unsigned f2u(float v) { return __builtin_bit_cast(unsigned, v); }
__device__ __forceinline__ float bf2f(unsigned v) { return __builtin_bit_cast(float, v << 16); }
__device__ __forceinline__ unsigned f2bf(float f) { __bf16 b = (__bf16)f; return (unsigned)__builtin_bit_cast(u16, b); }
__device__ __forceinline__ float rbf(float f) { return bf2f(f2bf(f)); }
typedef __attribute__((ext_vector_type(2))) __bf16 hbf16x2;
__device__ __forceinline__ unsigned pack2(float lo, float hi) { f32x2 v = {lo, hi}; hbf16x2 b = __builtin_convertvector(v, hbf16x2); return __builtin_bit_cast(unsigned, b); }
__device__ __forceinline__ f32x4 mfma32(bf16x8 a, bf16x8 b, f32x4 c) { return __builtin_amdgcn_mfma_f32_16x16x32_bf16(a, b, c, 0, 0, 0); }
typedef __attribute__((ext_vector_type(8))) __bf16 hbf16x8;
typedef __attribute__((ext_vector_type(16))) __bf16 hbf16x16;
__device__ __forceinline__ f32x4 smfma64(bf16x8 a, bf16x16 b, f32x4 c, int idx) {
  return __builtin_amdgcn_smfmac_f32_16x16x64_bf16(__builtin_bit_cast(hbf16x8, a), __builtin_bit_cast(hbf16x16, b), c, idx, 0, 0);
}
__device__ __forceinline__ bf16x16 cat16(bf16x8 a, bf16x8 b) { return __builtin_shufflevector(a, b, 0, 1, 2, 3, 4, 5, 6, 7, 8, 9, 10, 11, 12, 13, 14, 15); }
__device__ __forceinline__ bf16x8 half8(bf16x16 v, int hi) { return hi ? __builtin_shufflevector(v, v, 8, 9, 10, 11, 12, 13, 14, 15) : __builtin_shufflevector(v, v, 0, 1, 2, 3, 4, 5, 6, 7); }
__device__ __forceinline__ bf16x8 mk8(unsigned a, unsigned b, unsigned c, unsigned d) { u32x4 v = {a, b, c, d}; return __builtin_bit_cast(bf16x8, v); }
__device__ __forceinline__ unsigned dpp_ror8(unsigned v) { return (unsigned)__builtin_amdgcn_mov_dpp((int)v, 0x128, 0xf, 0xf, true); }
__device__ __forceinline__ float fmx(float a, float b) { return __builtin_elementwise_maximum(a, b); }

__device__ __forceinline__ int imax(int a, int b) { return a > b ? a : b; }
__device__ __forceinline__ int imin(int a, int b) { return a < b ? a : b; }
__device__ __forceinline__ unsigned umin(unsigned a, unsigned b) { return a < b ? a : b; }
__device__ __forceinline__ unsigned umax(unsigned a, unsigned b) { return a > b ? a : b; }
__device__ __forceinline__ bool wave_all(bool p) { return __builtin_amdgcn_ballot_w64(!p) == 0ull; }
__device__ __forceinline__ float x16mx(float x) { unsigned u = __builtin_bit_cast(unsigned, x); auto r = __builtin_amdgcn_permlane16_swap(u, u, false, false); return fmx(u2f(r[0]), u2f(r[1])); }
__device__ __forceinline__ float x32mx(float x) { unsigned u = __builtin_bit_cast(unsigned, x); auto r = __builtin_amdgcn_permlane32_swap(u, u, false, false); return fmx(u2f(r[0]), u2f(r[1])); }
__device__ __forceinline__ float x16sum(float x) { unsigned u = __builtin_bit_cast(unsigned, x); auto r = __builtin_amdgcn_permlane16_swap(u, u, false, false); return u2f(r[0]) + u2f(r[1]); }
__device__ __forceinline__ float x32sum(float x) { unsigned u = __builtin_bit_cast(unsigned, x); auto r = __builtin_amdgcn_permlane32_swap(u, u, false, false); return u2f(r[0]) + u2f(r[1]); }
template <int CTRL> __device__ __forceinline__ float dppf(float v) { return u2f((unsigned)__builtin_amdgcn_mov_dpp((int)f2u(v), CTRL, 0xf, 0xf, true)); }
#define OWN_PF 1
template <int BYTES> __device__ __forceinline__ void own_code(int tid, unsigned* sink) {
  if (!OWN_PF || __builtin_amdgcn_readfirstlane(tid >> 6) * 4096 >= BYTES) return;
  const char* pc; unsigned keep;
  asm volatile("s_getpc_b64 %0" : "=s"(pc));
  asm volatile("s_mov_b32 %0, m0\n\ts_mov_b32 m0, %3\n\ts_nop 0\n\tglobal_load_lds_dword %1, %2\n\ts_mov_b32 m0, %0"
               : "=&s"(keep) : "v"(tid * 64 < BYTES ? tid * 64 : 0), "s"(pc), "s"((unsigned)(uintptr_t)(LAS unsigned*)sink) : "memory");
}
constexpr int DPP_XOR1 = 0xB1, DPP_XOR2 = 0x4E, DPP_ROR4 = 0x124, DPP_ROR8 = 0x128;
#if defined(__HIP_DEVICE_COMPILE__)
#pragma clang attribute pop
#endif

typedef short v4i16_t __attribute__((ext_vector_type(4)));
__device__ __forceinline__ bf16x8 tr_frag(const char* lo_, const char* hi_) {
  const v4i16_t lo = __builtin_amdgcn_ds_read_tr16_b64_v4i16((LAS v4i16_t*)lo_), hi = __builtin_amdgcn_ds_read_tr16_b64_v4i16((LAS v4i16_t*)hi_);
  return (bf16x8){lo[0], lo[1], lo[2], lo[3], hi[0], hi[1], hi[2], hi[3]};
}
__device__ __forceinline__ int rec_wg(int gp, int m) { return (((gp & 1) * 16 + m) << 3) | (gp >> 1); }
__device__ __forceinline__ float wsum(float v) {
  v += dppf<DPP_XOR1>(v); v += dppf<DPP_XOR2>(v); v += dppf<DPP_ROR4>(v); v += dppf<DPP_ROR8>(v);
  return x32sum(x16sum(v));
}
__device__ __forceinline__ float ln_row(int lane, u32x4 (&xo)[2]) {
  const bool two = lane < 16;
  float v[16];
#pragma unroll
  for (int hf = 0; hf < 2; ++hf)
#pragma unroll
    for (int i = 0; i < 4; ++i) { const unsigned u = xo[hf][i]; v[hf * 8 + 2 * i] = bf2f(u & 0xffff); v[hf * 8 + 2 * i + 1] = bf2f(u >> 16); }
  float s0 = 0.f, s1 = 0.f;
#pragma unroll
  for (int i = 0; i < 8; ++i) { s0 += v[i]; s1 += v[8 + i]; }
  const float mean = wsum(two ? s0 + s1 : s0) * (1.0f / DM);
  s0 = s1 = 0.f;
#pragma unroll
  for (int i = 0; i < 8; ++i) { const float d0 = v[i] - mean, d1 = v[8 + i] - mean; s0 += d0 * d0; s1 += d1 * d1; }
  const float var = rbf(wsum(two ? s0 + s1 : s0) * (1.0f / DM));
  const float mb = rbf(mean);
  const float rden = 1.0f / rbf(sqrtf(rbf(var + 1e-5f)));
  float n2[2] = {0.f, 0.f};
#pragma unroll
  for (int hf = 0; hf < 2; ++hf) {
    float y[8];
#pragma unroll
    for (int i = 0; i < 8; ++i) { y[i] = rbf(rbf(v[hf * 8 + i] - mb) * rden); n2[hf] += y[i] * y[i]; }
    xo[hf] = (u32x4){pack2(y[0], y[1]), pack2(y[2], y[3]), pack2(y[4], y[5]), pack2(y[6], y[7])};
  }
  return wsum(two ? n2[0] + n2[1] : n2[0]);
}

#define PROT(k)
#define WARM_CODE 1
#define B_AUX 16
#define X_AUX 16
#define T_AUX 16
#define WB_NEAR 1
#define PF_POS 0
#define PRB_N0 3
#define PRB_N1 0
#define PRB_LOG(i, v)
#define PLD(p) __builtin_nontemporal_load((const u32x4*)(p))
struct ProArgs { const u16* x; const u16* wq; const u16* wk; const u16* wv; const u16* wo; const u16* wqkvb; const u16* wob; char* ws; };
__global__ __launch_bounds__(640, 3) void prologue_kernel(ProArgs a) {
  __shared__ __attribute__((aligned(16))) char img[192 * 320];
  __shared__ __attribute__((aligned(16))) char slots[10 * 6144];
  __shared__ float red[10][160];
  __shared__ unsigned latw[10];
  __shared__ u32x4 xst[2][640];
  const int b = blockIdx.x, tid = threadIdx.x, lane = tid & 63, v = __builtin_amdgcn_readfirstlane(tid >> 6), c = lane & 15, g = lane >> 4;
  const int h = (b & 7) * 4 + ((b >> 3) & 3), dh = (b >> 5) & 1, cq = b >> 6, ks = dh * 10 + v;
  char* const slot = slots + v * 6144;
  PROT(0);
  __shared__ unsigned sink[64];
  own_code<9600>(tid, sink);
  const unsigned zo = v < 8 ? (unsigned)(((b >> 6) * 512 + ((b & 7) * 2 + (v >> 2)) * 32 + ((b >> 3) & 7) * 4 + (v & 3)) * 1024) : (unsigned)(((v - 7) * 2048 + (b & 7) * 32 + ((b >> 3) & 7) * 4 + (b >> 6)) * 1024);
  __builtin_amdgcn_raw_buffer_store_b128((u32x4){0u, 0u, 0u, 0u}, __builtin_amdgcn_make_buffer_rsrc(a.ws, 0, (int)(3 * PG), 0x00020000), zo + lane * 16, 0, 16);
  __builtin_amdgcn_sched_barrier(0);
  u32x4 wa[2][6], wb[6], tr[4], xo[2];
  const int xrow = b * 8 + (v & 7);
  xo[0] = *(const u32x4*)(a.x + (size_t)xrow * DM + lane * 8);
  xo[1] = *(const u32x4*)(a.x + (size_t)xrow * DM + 512 + c * 8);
  const int job = imin(b + 256 * (9 - v), 800);
  const int jn = job < 600 ? job / 20 : (job % 200) / 20, jk = job % 20;
  {
    const u16* src = job < 600 ? a.wqkvb + (size_t)(32 * jk) * 3 * DM : a.wob + (size_t)(32 * jk) * DM;
#pragma unroll
    for (int i = 0; i < 4; ++i) tr[i] = *(const u32x4*)(src + (size_t)(i * 8 + (lane >> 3)) * (job < 600 ? 3 * DM : DM) + jn * 64 + (lane & 7) * 8);
  }
#pragma unroll
  for (int i = 0; i < 6; ++i) {
    const int idx = (v * 6 + i) * 64 + lane, row = idx / 20;
    wb[i] = PLD(a.wo + (size_t)(h * DH + row) * DM + cq * 160 + (idx - row * 20) * 8);
  }
#pragma unroll
  for (int i = 0; i < 6; ++i) {
    const int idx = i * 64 + lane, r = idx / 24;
#pragma unroll
    for (int u = 0; u < 2; ++u) wa[u][i] = PLD(a.wv + (size_t)(32 * ks + 8 * (r >> 2) + (r & 3) + 4 * u) * HD + h * DH + (idx - r * 24) * 8);
  }
  unsigned pf[2] = {0u, 0u};
  auto prefetch = [&]() {
#pragma unroll
    for (int i = 0; i < 2; ++i) pf[i] = *(const unsigned*)((i ? a.wq : a.wk) + ((size_t)(b * 10 + v) * 48 + imin(lane, 47)) * 32);
  };
  if (PF_POS == 0) prefetch();
  __builtin_amdgcn_sched_barrier(0);
  unsigned lat = ~0u;
  auto probe = [&](int n0, int n) {
    const char* p = a.ws + zo;
#pragma unroll 1
    for (int i = n0; i < n0 + n; ++i) {
      u64 t0, t1; unsigned d;
      asm volatile("s_memtime %0\n\ts_waitcnt lgkmcnt(0)\n\ts_load_dword %2, %3, %4\n\ts_waitcnt lgkmcnt(0)\n\ts_memtime %1\n\ts_waitcnt lgkmcnt(0)"
                   : "=&s"(t0), "=&s"(t1), "=&s"(d) : "s"(p), "s"((i & 3) * 256 + (i >> 2) * 128) : "memory");
      PRB_LOG(i, (unsigned)t1 - (unsigned)t0);
      lat = umin(lat, (unsigned)t1 - (unsigned)t0);
    }
  };
  asm volatile("s_waitcnt vmcnt(26)" ::: "memory");
  probe(0, PRB_N0);
  latw[v] = lat;
  __builtin_amdgcn_sched_barrier(0);
  auto xput = [&](int k) {
    if (v < 8) { xst[k][lane * 8 + (v ^ (lane & 7))] = xo[0]; if (lane < 16) xst[k][(64 + lane) * 8 + (v ^ (lane & 7))] = xo[1]; }
  };
  xput(0);
  const float xn2 = u2f(__builtin_amdgcn_readfirstlane(f2u(ln_row(lane, xo))));
  xput(1);
#pragma unroll
  for (int i = 0; i < 4; ++i) *(u32x4*)(slot + (i * 8 + (lane >> 3)) * 144 + (lane & 7) * 16) = tr[i];
  __builtin_amdgcn_fence(__ATOMIC_RELEASE, "wavefront"); __builtin_amdgcn_wave_barrier();
  bf16x8 tf[4];
#pragma unroll
  for (int t = 0; t < 4; ++t) {
    const char* bp = slot + (8 * g + (c >> 2)) * 144 + t * 32 + (c & 3) * 8;
    tf[t] = tr_frag(bp, bp + 4 * 144);
  }
  __builtin_amdgcn_fence(__ATOMIC_RELEASE, "wavefront"); __builtin_amdgcn_wave_barrier();
  __builtin_amdgcn_sched_barrier(0);
#pragma unroll
  for (int i = 0; i < 6; ++i) {
    const int idx = (v * 6 + i) * 64 + lane, row = idx / 20;
    *(u32x4*)(img + row * 320 + (((idx - row * 20) ^ ((row >> 2) & 2)) * 16)) = wb[i];
  }
  bf16x8 A[2][6];
#pragma unroll
  for (int u = 0; u < 2; ++u) {
#pragma unroll
    for (int i = 0; i < 6; ++i) {
      const int idx = i * 64 + lane, r = idx / 24;
      *(u32x4*)(slot + r * 384 + (((idx - r * 24) ^ (r >> 1)) * 16)) = wa[u][i];
    }
    __builtin_amdgcn_fence(__ATOMIC_RELEASE, "wavefront"); __builtin_amdgcn_wave_barrier();
#pragma unroll
    for (int e = 0; e < 6; ++e) A[u][e] = *(const bf16x8*)(slot + c * 384 + (((4 * e + g) ^ (c >> 1)) * 16));
    __builtin_amdgcn_fence(__ATOMIC_RELEASE, "wavefront"); __builtin_amdgcn_wave_barrier();
  }
  BAR();
  PROT(1);
  if (PF_POS == 0) asm volatile("" :: "v"(pf[0]), "v"(pf[1]));
  if (PF_POS == 1) prefetch();
  {
    const unsigned xf = (unsigned)((b >> 1) * 20480 + (tid >> 3) * 256 + (b & 1) * 128 + (tid & 7) * 16);
    const __amdgpu_buffer_rsrc_t rx = __builtin_amdgcn_make_buffer_rsrc(a.ws + OFF_XN, 0, (int)(OFF_QB - OFF_XN), 0x00020000);
    __builtin_amdgcn_raw_buffer_store_b128(xst[1][tid ^ ((tid >> 3) & 7)], rx, xf, 0, X_AUX);
    __builtin_amdgcn_raw_buffer_store_b128(xst[0][tid ^ ((tid >> 3) & 7)], rx, xf + (unsigned)(OFF_XB - OFF_XN), 0, X_AUX);
    if (v < 8 && lane < 4) ((float*)(a.ws + OFF_XNRM))[xrow * 4 + lane] = lane == 0 ? xn2 : 0.f;
  }
  if (job < 800) {
    const __amdgpu_buffer_rsrc_t rt = __builtin_amdgcn_make_buffer_rsrc(a.ws + (job < 600 ? OFF_WQKVBT : OFF_WOBT) + ((size_t)jn * 80 + jk) * 1024, 0, 81920, 0x00020000);
#pragma unroll
    for (int t = 0; t < 4; ++t) __builtin_amdgcn_raw_buffer_store_b128(__builtin_bit_cast(u32x4, tf[t]), rt, lane * 16 + t * 20480, 0, T_AUX);
  }
  const int gp = h >> 1, hh = h & 1;
  const __amdgpu_buffer_rsrc_t rwb = __builtin_amdgcn_make_buffer_rsrc(a.ws + OFF_WB, 0, (int)WB_BYTES, 0x00020000);
  const unsigned phi = WB_NEAR ? ((((unsigned)b >> 4) & 1u) ^ (unsigned)(latw[8] < latw[9]) ^ (WB_NEAR - 1)) << 13 : (unsigned)(b & 32) << 8;
  if (tid == 0) ((unsigned*)(a.ws + OFF_PHI))[b] = phi;
#pragma unroll
  for (int n = 0; n < 10; ++n) {
    const int off = n * 32 + 8 * (c & 3), T = cq * 10 + n;
    const char* bp = img + (8 * g + (c >> 2)) * 320 + (off ^ ((g & 1) << 5));
    f32x4 ac[2] = {{0.f, 0.f, 0.f, 0.f}, {0.f, 0.f, 0.f, 0.f}};
#pragma unroll
    for (int e = 0; e < 6; ++e) {
      const bf16x8 bf = tr_frag(bp + e * 32 * 320, bp + (e * 32 + 4) * 320);
      ac[0] = mfma32(A[0][e], bf, ac[0]); ac[1] = mfma32(A[1][e], bf, ac[1]);
    }
    const u32x4 o = {pack2(ac[0][0], ac[0][1]), pack2(ac[0][2], ac[0][3]), pack2(ac[1][0], ac[1][1]), pack2(ac[1][2], ac[1][3])};
    const unsigned oo = (T < 32 ? wb_off(rec_wg(gp, T & 15) * 100 + (hh * 3 + (T >> 4)) * 20 + ks) + (unsigned)(lane * 16)
                                : wb_off(rec_wg(gp, 2 * (T - 32) + (c >> 3)) * 100 + 40 + ks) + (unsigned)((g * 16 + hh * 8 + (c & 7)) * 16)) ^ phi;
    __builtin_amdgcn_raw_buffer_store_b128(o, rwb, oo, 0, B_AUX);
    float n2 = 0.f;
#pragma unroll
    for (int r = 0; r < 4; ++r) n2 += ac[0][r] * ac[0][r] + ac[1][r] * ac[1][r];
    n2 = x32sum(x16sum(n2));
    if (g == 0) red[v][n * 16 + c] = n2;
  }
  PROT(2);
  if (PF_POS == 1) asm volatile("" :: "v"(pf[0]), "v"(pf[1]));
  if (v >= 8 && b < 64 && lane == 0) ((unsigned*)(a.ws + OFF_LAT))[(v - 4) * 64 + b] = umin(lat, 60000u);
  if (WARM_CODE && v == 7) {
    const char* pc; int d0, d1;
    asm("s_getpc_b64 %0\n\ts_mov_b32 %1, _Z10rec_kernel7RecArgs@rel32@lo+4\n\ts_mov_b32 %2, _Z11proj_kernelPKtS0_S0_Pt@rel32@lo+12" : "=s"(pc), "=s"(d0), "=s"(d1));
    const char *c0 = pc + d0, *c1 = pc + d1;
    const long o = ((long)b * 64 + lane) * 64;
    if (o < c1 - c0) asm volatile("" :: "v"(*(const volatile GAS unsigned*)(c0 + o)));
  }
  if (v == 9 && lane < 32) {
    const int q = b * 32 + lane;
    *(u32x4*)(a.ws + OFF_KV + (size_t)(q >> 2) * KVT + 1280 + (q & 3) * 336 + 320) = (u32x4){0x3f803f80u, 0x3f803f80u, 0x3f803f80u, 0x3f803f80u};
  }
  BAR();
  if (v == 1 && lane == 0) ((unsigned*)(a.ws + OFF_LAT))[b] = umin(umin(umin(latw[0], latw[4]) + umin(latw[1], latw[5]), 30000u) + umin(umin(latw[2], latw[6]) + umin(latw[3], latw[7]), 30000u), 60000u);
  if (v == 0) {
    float mx = 0.f;
#pragma unroll
    for (int k = 0; k < 3; ++k) {
      const int col = k * 64 + lane;
      float sm = 0.f;
#pragma unroll
      for (int i = 0; i < 10; ++i) sm += red[i][col < 160 ? col : 0];
      mx = fmx(mx, sm);
    }
    mx = fmx(mx, dppf<DPP_XOR1>(mx)); mx = fmx(mx, dppf<DPP_XOR2>(mx)); mx = fmx(mx, dppf<DPP_ROR4>(mx)); mx = fmx(mx, dppf<DPP_ROR8>(mx));
    mx = x32mx(x16mx(mx));
    if (lane == 0) ((float*)(a.ws + OFF_BNRM))[(cq * 32 + h) * 2 + dh] = mx;
  }
  PROT(3);
}

struct RecArgs {
  const u16* mem0; const u16* wq; const u16* wk; const u16* xold; u16* xnew; char* ws;
  unsigned layer; unsigned pad;
};
#define RLX_AGENT __ATOMIC_RELAXED, __HIP_MEMORY_SCOPE_AGENT
constexpr unsigned SPIN_CAP = 1u << 20;
#define TSTAMP(k, v)
#define ARRIVE(k)
#define ARRIVE_OUT()
#define ARRIVE_DECL()
#define LOGV(k, v)
#define LOGF(k, v)
#define LOGP(k)
#define LOGQ(k)
#define LOGF2(k)
#define LOGD(k)
#define LOGT(k, v)
#define STAMP_DECL()
#define STAMP(k)
#define STAMPV(k, v)
#define STAMP_OUT()
#define REC_D1 520
#define SCH_V 10
#define SCH_VMIN 0
#define SCH_DMIN 4
#define SCH_PEN 150
#define SCH_VMIN2 (-4)
#define SCH_PEN2 40
#define SCH_GU 1
#define SCH_GD 2
#define SCH_UP 6
#define SCH_D0 196
#define SCH_FIRST 120
#define EXP_NODMA 0
#define BG_HOLD 220
#define QBATCH 0
#define WPF_N 5
#define WPF_W 0
#define WPF0 (63 - WPF_N)
#define BG_HOLD4 250
#define D1_W1 20
#define F3_D 1200
#define T2W 2
#define AT_ASM 1
#define PM_Z 1
#define P2_X 1
#define ST_KS 14
#define ST_KS0 ST_KS
#define ST_KS1 ST_KS
#define PADR 0x6666
#define PADT 0x000
#define CODE_PAD(n) asm volatile(".p2align 6\n.rept %0\ns_nop 0\n.endr" :: "n"(n))
#define ST_X 250
#define ST_ALL 4
#define CPF_S 61
#define FR_D 700
#define FR_PACE 6
#define END_E60 600
#define END_E1 1300
#define END_E2 3400
#define END_E62 2800
#define END_E3 6800
#define END_E4 11400
#define EARLY_N 6
#define EARLY_D1 0
#define EARLY_V 10
#define EARLY_V1 18
#define F3 0
#define F3A 0
#define F3A_D 400
#define T4_D 300
#define T4_P 1
#define P2_W 1
#define PS_SL 1
#define F2B 4
#define F0C 0
#define F2C 0
#define W2_P 1
#define SCH_W 1
#define SP_SL 0
#define D1_M 300
#define D1_MIN 260
#define D1_C 96
#define D1_G 2
#define BG_HOLD6 0
#define W2_D 0
#define END_E61 2600
#define D1_LATE 200
#define P2_G0 10
#define P2_G2 6
#define P2_G3 6
#define SCH_RL 0
#define SCH_P 3
#define MMA_P 1
#define SP_P 0
#define F6 8
#define F7 10
#define XPF_W 2
#define SCH_LATE 0
#define LATE_V 8
#define QW 3
#define FB_SL 0
#define P2_G1 1
#define P1_G 0
#define F4Z 0
#define F5Z 0
#define T5_Z 2
#define F0A 3
#define FB_E 0
#define EXACT 1
#define D1_P 3
#define VO_A0 9
#define VO_A2 10
#define VO_B0 3
#define VO_B1 0
#define VO_N2 7
#define VO_N3 10
#define VO_W -1
#define UN_P 2
#define VP_E 1
#define TS_B 0
#define ST_E 1
#define W1_SL 0
#define VO_A1 4
#define LAST_V 22
#define END_E59 3200
#define END_G2 600
#define END_G3 600
#define QR 4
#define WPF_S 128
#define END_SL 4
#define SCH_DR 182
#define SCH_DK 3
#define FR_HOLD 0
#define FW 8
#define PR_P 0
#define REL_AT 1
#define XO_T 4
#define Q_AUX 0
#define O_WT 0
#define P_WT 0
#define KP0 0
#define KP1 0
#define XPF 3
#define W1_D 0
#define F4 12
#define F5 0
#define F67 0
#define KP2 0
#define REC_RNG 70u
#define SCH_DRAIN 40
#define PL_X 0
#define FC(k)
#define PIN_V(x) asm volatile("" : "+v"(x))
#define PIN_S(x) asm volatile("" : "+s"(x))
#define VM_DRAIN() do { asm volatile("s_waitcnt vmcnt(0)" ::: "memory"); __builtin_amdgcn_s_waitcnt(0x0F70); } while (0)
#define SC1_LOAD(off) __builtin_amdgcn_raw_buffer_load_b128(rs, (off), 0, 16)
#define SC1_LOADS(off, soff) __builtin_amdgcn_raw_buffer_load_b128(rs, (off), (soff), 16)
#define SC1_STORE8S(v, off, soff) __builtin_amdgcn_raw_buffer_store_b64((v), rs, (off), (soff), 16)
#define SC1_LOAD8(off) __builtin_amdgcn_raw_buffer_load_b64(rs, (off), 0, 16)
#define SC1_STORE(v, off) __builtin_amdgcn_raw_buffer_store_b128((v), rs, (off), 0, 16)
#define SC1_STORE8(v, off) __builtin_amdgcn_raw_buffer_store_b64((v), rs, (off), 0, 16)
#if defined(__HIP_DEVICE_COMPILE__)
#pragma clang attribute push(__attribute__((target("no-packed-fp32-ops"))), apply_to = function)
#endif
__device__ __forceinline__ unsigned now32() { return (unsigned)__builtin_amdgcn_s_memrealtime(); }
__device__ __forceinline__ void wait_until(unsigned t) { for (;;) { const int d = (int)(t - now32()); if (d <= 0) break; if (d > 8) __builtin_amdgcn_s_sleep(1); } }
__device__ __forceinline__ unsigned cyc32() { return (unsigned)__builtin_amdgcn_s_memtime(); }
__device__ __forceinline__ void wait_cyc(unsigned t) { for (;;) { const int d = (int)(t - cyc32()); if (d <= 0) break; if (d > 200) __builtin_amdgcn_s_sleep(1); } }
#define WAIT_ASM(clk, far_) "1:\n\t" clk " vcc\n\ts_waitcnt lgkmcnt(0)\n\ts_sub_i32 %0, %1, vcc_lo\n\ts_cmp_lt_i32 %0, " far_ "\n\ts_cbranch_scc1 3f\n\ts_sleep 1\n\ts_branch 1b\n" \
                            "2:\n\t" clk " vcc\n\ts_waitcnt lgkmcnt(0)\n\ts_sub_i32 %0, %1, vcc_lo\n3:\n\ts_cmp_gt_i32 %0, 0\n\ts_cbranch_scc1 2b"
__device__ __forceinline__ int spin_until(unsigned t) { int d; asm volatile(WAIT_ASM("s_memrealtime", "9") : "=&s"(d) : "s"(t) : "vcc", "scc", "memory"); return d; }
__device__ __forceinline__ int spin_cyc(unsigned t) { int d; asm volatile(WAIT_ASM("s_memtime", "200") : "=&s"(d) : "s"(t) : "vcc", "scc", "memory"); return d; }
__device__ __forceinline__ void spin_exact(unsigned t) {
  if (!EXACT) { spin_cyc(t); return; }
  int d;
  asm volatile("1:\n\ts_memtime vcc\n\ts_waitcnt lgkmcnt(0)\n\ts_sub_i32 %0, %1, vcc_lo\n\ts_cmp_gt_i32 %0, 95\n\ts_cbranch_scc1 1b\n\t"
               "s_max_i32 %0, %0, 0\n\ts_andn2_b32 %0, %0, 3\n\ts_sub_i32 %0, 3f-2f, %0\n\ts_getpc_b64 vcc\n"
               "2:\n\ts_add_u32 vcc_lo, vcc_lo, %0\n\ts_addc_u32 vcc_hi, vcc_hi, 0\n\ts_setpc_b64 vcc\n\t.rept 24\n\ts_nop 0\n\t.endr\n3:"
               : "=&s"(d) : "s"(t) : "scc", "vcc", "memory");
}
template <int OFF, int SL> __device__ __forceinline__ void lds_spin(unsigned lds0, unsigned want) {
  unsigned v;
  asm volatile("1:\n\tds_read_b32 %0, %1 offset:%3\n\t.if %4\n\ts_sleep %4\n\t.endif\n\ts_waitcnt lgkmcnt(0)\n\tv_cmp_gt_u32 vcc, %2, %0\n\ts_cbranch_vccnz 1b"
               : "=&v"(v) : "v"(lds0), "s"(want), "n"(OFF), "n"(SL) : "vcc", "memory");
}
template <int CTRL> __device__ __forceinline__ int dppi(int v) { return __builtin_amdgcn_mov_dpp(v, CTRL, 0xf, 0xf, true); }
__device__ __forceinline__ int wave_imax(int l) {
  l = imax(l, dppi<DPP_XOR1>(l)); l = imax(l, dppi<DPP_XOR2>(l)); l = imax(l, dppi<DPP_ROR4>(l)); l = imax(l, dppi<DPP_ROR8>(l));
  int a = imax(__builtin_amdgcn_readlane(l, 0), __builtin_amdgcn_readlane(l, 16)), b = imax(__builtin_amdgcn_readlane(l, 32), __builtin_amdgcn_readlane(l, 48));
  PIN_S(a); PIN_S(b);
  return imax(a, b);
}
__device__ __forceinline__ void glds16(const void* gsrc, unsigned lds_dst) {
  unsigned keep;
  asm volatile("s_mov_b32 %0, m0\n\ts_mov_b32 m0, %2\n\ts_nop 0\n\tglobal_load_lds_dwordx4 %1, off\n\ts_mov_b32 m0, %0" : "=&s"(keep) : "v"(gsrc), "s"(lds_dst) : "memory");
}
__device__ __forceinline__ void glds16_sc1(const void* gsrc, unsigned lds_dst) {
  unsigned keep;
  asm volatile("s_mov_b32 %0, m0\n\ts_mov_b32 m0, %2\n\ts_nop 0\n\tglobal_load_lds_dwordx4 %1, off sc1\n\ts_mov_b32 m0, %0" : "=&s"(keep) : "v"(gsrc), "s"(lds_dst) : "memory");
}
__device__ __forceinline__ unsigned acc_off(int G, int row) {
  return (unsigned)(G < 128 ? ((G & 31) | ((G & 32) << 1) | ((G & 64) >> 1)) * 256 + row * 16 : (2 * G - 128 + (row >> 2)) * 256 + (row & 3) * 16);
}
__device__ __forceinline__ unsigned nz(unsigned v) { return v > 1u ? v : 1u; }
__device__ __forceinline__ unsigned pack2n(float lo, float hi) { return nz(pack2(lo, hi)); }
__device__ __forceinline__ unsigned min4(u32x4 v) { return umin(umin(v[0], v[1]), umin(v[2], v[3])); }
__device__ __forceinline__ bf16x8 as8(u32x4 v) { return __builtin_bit_cast(bf16x8, v); }

constexpr int NV = 7, NKQ = 2;
constexpr int L_RED1 = 0;
constexpr int L_BM = L_RED1 + 8 * 1536;
constexpr int L_STG = L_BM + 10240;
constexpr int L_ST = L_STG + 2048 + 2560;
constexpr int L_MISC = L_ST + 512;
constexpr int L_KX = L_MISC + 128;
constexpr int L_QX = L_KX + NKQ * 2048;
constexpr int L_XN = L_QX + NKQ * 2048;
constexpr int L_VOX = L_XN + 81920;
constexpr int L_VOM = L_VOX + NV * 5120;
constexpr int L_BYTES = L_VOM + NV * 1280;
constexpr int S_IMG = L_XN + 40960;
static_assert(S_IMG + 61440 <= L_VOM, "start-up image");
static_assert(L_BYTES <= 160 * 1024, "LDS budget");

__device__ __forceinline__ bf16x8 wqk_frag(const char* lds, int t, int ks, int lane) {
  const int c = lane & 15, g = lane >> 4, p = c & 3;
  const char* q = lds + S_IMG + (32 * ks + 8 * g + (c >> 2)) * 96 + 8 * p + (t == 0 ? 0 : (t == 1 ? 48 : (p < 2 ? 32 : 64)));
  const v4i16_t lo = __builtin_amdgcn_ds_read_tr16_b64_v4i16((LAS v4i16_t*)q), hi = __builtin_amdgcn_ds_read_tr16_b64_v4i16((LAS v4i16_t*)(q + 4 * 96));
  return (bf16x8){lo[0], lo[1], lo[2], lo[3], hi[0], hi[1], hi[2], hi[3]};
}
__device__ __forceinline__ bf16x8 sel_frag(int c, int base) {
  bf16x8 r;
#pragma unroll
  for (int i = 0; i < 8; ++i) r[i] = (c == base + i) ? (short)0x3f80 : (short)0;
  return r;
}
__device__ __forceinline__ bf16x8 qk_frag(const char* p4, const char* p2, const char* p2b) {
  f32x4 a0 = *(const f32x4*)(p4), a1 = *(const f32x4*)(p4 + 1792), a2 = *(const f32x4*)(p4 + 3584), a3 = *(const f32x4*)(p4 + 5376);
  f32x2 b0 = *(const f32x2*)(p2), b1 = *(const f32x2*)(p2 + 1792), b2 = *(const f32x2*)(p2b), b3 = *(const f32x2*)(p2b + 1792);
  f32x4 va = (a0 + a1) + (a2 + a3);
  f32x2 vb = (b0 + b1) + (b2 + b3);
  return mk8(pack2(va[0], va[1]), pack2(va[2], va[3]), pack2(vb[0], vb[1]), 0u);
}
__device__ __forceinline__ void x_fetch(const u16* src, unsigned lds0, int seg, int p, int lane) {
  if (EXP_NODMA) return;
  glds16_sc1(src + (size_t)p * 512 + lane * 8, lds0 + L_XN + (seg & 1) * 40960 + p * 1024);
}
template <int N> __device__ __forceinline__ void x_fetch_n(const u16* src, unsigned lds0, int seg, int p0, unsigned voff) {
  if (EXP_NODMA) return;
#pragma unroll
  for (int i = 0; i < N; i += 4) {
    unsigned keep;
    asm volatile("s_mov_b32 %0, m0\n\ts_mov_b32 m0, %3\n\ts_nop 0\n\tglobal_load_lds_dwordx4 %1, %2 sc1\n\t"
                 ".if %4 > 1\n\tglobal_load_lds_dwordx4 %1, %2 offset:1024 sc1\n\t.endif\n\t.if %4 > 2\n\tglobal_load_lds_dwordx4 %1, %2 offset:2048 sc1\n\t.endif\n\t"
                 ".if %4 > 3\n\tglobal_load_lds_dwordx4 %1, %2 offset:3072 sc1\n\t.endif\n\ts_mov_b32 m0, %0"
                 : "=&s"(keep) : "v"(voff), "s"(src + (size_t)(p0 + i) * 512), "s"(lds0 + L_XN + (seg & 1) * 40960 + (p0 + i) * 1024), "n"(N - i < 4 ? N - i : 4) : "memory");
  }
}
__device__ __forceinline__ int qkv_row(int b) { return (b & 7) / QR * (2 * QR) + (b >> 3) / (16 / QR); }
__device__ __forceinline__ int qkv_col(int b) { return (b & 7) % QR * (16 / QR) + (b >> 3) % (16 / QR); }
__device__ __forceinline__ const u16* x_src(const RecArgs& a, int seg, int b) {
  return seg < NSEG ? (const u16*)(a.ws + OFF_XN) + (size_t)seg * 32 * DM : a.xnew + (size_t)(qkv_row(b) + 16 * (seg - NSEG)) * 32 * DM;
}
template <int N>
__device__ __forceinline__ void img_fetch(const RecArgs& a, unsigned lds0, int h, int j, int part, int lane) {
#pragma unroll
  for (int i = 0; i < N; ++i) {
    const int n = part * N + i, idx = n * 64 + lane, row = idx / 6, pc = idx - row * 6;
    if (FR_PACE && N == 20) asm volatile("s_waitcnt vmcnt(%0)" :: "n"(FR_PACE) : "memory");
    glds16((pc < 3 ? a.wq : a.wk - 24) + (size_t)row * HD + h * DH + j * 24 + pc * 8, lds0 + S_IMG + n * 1024);
  }
}
constexpr int P2B = F4 + F5 + 2 * F67 + F6 + F7, PZ = P2B + F2B, PA = PZ, P0C = PA + 2 * F0A, F3C = 40 - F3A - F3 - P0C - 2 * F0C - F2C;
static_assert(F3C >= 0, "fetch plan");
template <int OFF> __device__ __forceinline__ void lds_inc(unsigned lds0) {
  asm volatile("s_mov_b64 exec, 1\n\tds_add_u32 %0, %1 offset:%2\n\ts_mov_b64 exec, -1" :: "v"(lds0), "v"(1u), "n"(OFF) : "memory");
}
__device__ __forceinline__ void lds_arrive(unsigned* p, int lane) {
  asm volatile("s_waitcnt lgkmcnt(0)" ::: "memory");
  if (lane == 0) __hip_atomic_fetch_add(p, 1u, __ATOMIC_RELAXED, __HIP_MEMORY_SCOPE_WORKGROUP);
}
__device__ __forceinline__ void lds_wait(unsigned* p, unsigned want) {
  while (__hip_atomic_load(p, __ATOMIC_RELAXED, __HIP_MEMORY_SCOPE_WORKGROUP) < want) __builtin_amdgcn_s_sleep(1);
  asm volatile("" ::: "memory");
}
constexpr float KEXP = 0.07216878364870322f * 1.4426950408889634f;
#define FRESH_LANE() int lane = lane0; asm volatile("" : "+v"(lane)); const int c = lane & 15, g = lane >> 4; (void)c; (void)g

struct OutTask {
  u32x4 y[4]; u32x2 xo, out;
  bf16x8 sel;
  unsigned t5f, t5o;
  __device__ __forceinline__ void init(int b, int lane) {
    const int um = b / 10, uq = (b / 5) & 1, up = b % 5, tok = lane & 15, k = up < 4 ? (up >> 1) * 256 + um * 16 + (up & 1) * 8 : 512 + um * 8, q4 = ((lane >> 4) & 1) * 4;
    t5f = (unsigned)(((uq * 20 + (k >> 5)) * 64 + ((k & 31) >> 3) * 16 + tok) * 8 + q4);
    t5o = (unsigned)(((um * 2 + uq) * 16 + (lane >> 4)) * 1280 + up * 256 + tok * 16);
    if (b >= 160) { t5f = 0u; t5o = 0x7ffffff0u; }
    sel = sel_frag(tok, 0);
    PIN_V(sel);
  }
  static __device__ __forceinline__ bool ok(int sl, int b) { return sl >= 0 && b < 160; }
  __device__ __forceinline__ void load(const RecArgs& a, const __amdgpu_buffer_rsrc_t rs, int sl, int b, int part = 3) {
    const unsigned o = t5o | (sl < 0 ? 0x7ff00000u : 0u);
    sl = imax(sl, 0);
#pragma unroll
    for (int k = 0; k < 4; ++k) if (part & 1) y[k] = SC1_LOADS(o, op_slot(sl) + k * 5120);
    if (part & 2) xo = __builtin_bit_cast(u32x2, __hip_atomic_load((const gu64*)(a.xold + (size_t)sl * 32 * DM + t5f), RLX_AGENT));
  }
  __device__ __forceinline__ bool ready(int b) { return b >= 160 || wave_all(umin(umin(min4(y[0]), min4(y[1])), umin(min4(y[2]), min4(y[3]))) != 0u); }
  __device__ __forceinline__ void run(int lane) {
    f32x4 d = {0.f, 0.f, 0.f, 0.f};
#pragma unroll
    for (int k = 0; k < 4; ++k) d = mfma32(sel, as8(y[k]), d);
#pragma unroll
    for (int i = 0; i < 2; ++i) {
      const unsigned r = pack2(d[2 * i], d[2 * i + 1]), x = xo[i];
      out[i] = pack2(u2f(x << 16) + u2f(r << 16), u2f(x & 0xffff0000u) + u2f(r & 0xffff0000u));
    }
    PIN_V(out);
  }
  __device__ __forceinline__ void store(const RecArgs& a, int sl, int b, int lane) {
    __builtin_amdgcn_raw_buffer_store_b64(out, __builtin_amdgcn_make_buffer_rsrc(a.xnew, 0, ok(sl, b) ? S_ * DM * 2 : 0, 0x00020000), (t5f + (unsigned)sl * 32 * DM) * 2u | (unsigned)(lane & 32) << 25, 0, 16);
  }
};

struct PvTask2 {
  u32x4 y[2][2], pa[2], pb[2];
  unsigned lo, lm, vr, ma, mz, so, sz;
  __device__ __forceinline__ void init(int gp, int lane) {
    const int hi = lane >> 5;
    lo = pt_tok(2 * gp) + (unsigned)(lane * 16); lm = pt_mem(2 * gp + hi) + (unsigned)((lane & 31) * 8);
    vr = (unsigned)(L_VOX + 2048 + (lane ^ 8) * 16);
    ma = (unsigned)(L_VOM + hi * 768 + (lane & 31) * 8); mz = (unsigned)(L_VOM + 512 + ((lane & 31) ^ (hi * 8)) * 8);
    so = (unsigned)(L_STG + 2048 + (hi * 16 + (lane & 15)) * 16 + ((lane >> 4) & 1) * 8);
    sz = hi ? (unsigned)(L_RED1 + 8704 + lane * 8) : so + 1024;
    PIN_V(lo); PIN_V(lm); PIN_V(vr); PIN_V(ma); PIN_V(mz); PIN_V(so); PIN_V(sz);
#pragma unroll
    for (int q = 0; q < 2; ++q) { pa[q] = pb[q] = y[q][0] = y[q][1] = (u32x4){0u, 0u, 0u, 0u}; PIN_V(pa[q]); PIN_V(pb[q]); }
  }
  __device__ __forceinline__ void load(const __amdgpu_buffer_rsrc_t rs, unsigned o) {
#pragma unroll
    for (int q = 0; q < 2; ++q) {
      y[q][0] = SC1_LOADS(lo + q * 1024, o); y[q][1] = SC1_LOADS(lo + 2048 + q * 1024, o);
      const u32x2 p = __builtin_amdgcn_raw_buffer_load_b64(rs, lm + q * 256, o, 16);
      pa[q][0] = p[0]; pa[q][1] = p[1];
    }
  }
  __device__ __forceinline__ bool ready() {
    return wave_all(umin(umin(umin(min4(y[0][0]), min4(y[0][1])), umin(min4(y[1][0]), min4(y[1][1]))), umin(umin(pa[0][0], pa[0][1]), umin(pa[1][0], pa[1][1]))) != 0u);
  }
  template <bool DIRECT> __device__ __forceinline__ void run(char* lds, int sv, int lane, const __amdgpu_buffer_rsrc_t rs, unsigned og) {
    const f32x4 Z4 = {0.f, 0.f, 0.f, 0.f};
    const char* vp = lds + L_VOX + sv * 5120 + lane * 16;
    bf16x8 vx[6];
#pragma unroll
    for (int t = 0; t < 5; ++t) vx[t] = *(const bf16x8*)(vp + t * 1024);
    vx[5] = *(const bf16x8*)(lds + vr + sv * 5120);
    const u32x2 a0 = *(const u32x2*)(lds + ma + sv * 1280), a1 = *(const u32x2*)(lds + ma + sv * 1280 + 256);
    const u32x2 z0 = *(const u32x2*)(lds + mz + sv * 1280), z1 = *(const u32x2*)(lds + mz + sv * 1280 + 256);
    const bf16x8 vm = mk8(a0[0], a0[1], a1[0], a1[1]), vz = mk8(z0[0], z0[1], z1[0], z1[1]);
#pragma unroll
    for (int q = 0; q < 2; ++q) { PIN_V(pa[q]); pb[q][2] = pa[q][0]; pb[q][3] = pa[q][1]; PIN_V(pb[q]); }
    __builtin_amdgcn_sched_barrier(0);
    f32x4 o[2][3];
#pragma unroll
    for (int q = 0; q < 2; ++q) {
#pragma unroll
      for (int t = 0; t < 3; ++t) o[q][t] = mfma32(vx[t], as8(y[q][0]), Z4);
#pragma unroll
      for (int t = 0; t < 3; ++t) o[q][t] = mfma32(vx[3 + t], as8(y[q][1]), o[q][t]);
      o[q][0] = mfma32(vm, as8(pa[q]), o[q][0]); o[q][1] = mfma32(vm, as8(pb[q]), o[q][1]); o[q][2] = mfma32(vz, as8(pa[q]), o[q][2]);
    }
#pragma unroll
    for (int q = 0; q < 2; ++q) {
#pragma unroll
      for (int t = 0; t < 3; ++t) {
        const u32x2 v = {pack2n(o[q][t][0], o[q][t][1]), pack2n(o[q][t][2], o[q][t][3])};
        if (DIRECT) SC1_STORE8S(v, (so - (L_STG + 2048) + t * 512) | (t == 2 ? (unsigned)(lane & 32) << 25 : 0u), og + q * 20480);
        else *(u32x2*)(lds + (t < 2 ? so + t * 512 : sz) + q * 1280) = v;
      }
    }
  }
};

struct TokScores {
  bf16x8 k[2], q[2]; u32x4 v[2];
  __device__ __forceinline__ void get(const char* lds, int seg, int lane) {
#pragma unroll
    for (int i = 0; i < 2; ++i) { k[i] = *(const bf16x8*)(lds + L_KX + (seg & 1) * 2048 + i * 1024 + lane * 16); q[i] = *(const bf16x8*)(lds + L_QX + (seg & 1) * 2048 + i * 1024 + lane * 16); }
  }
  __device__ __forceinline__ void run(int qt) {
    const f32x4 Z4 = {0.f, 0.f, 0.f, 0.f};
    const f32x4 d1 = mfma32(k[0], q[qt], Z4), d2 = mfma32(k[1], q[qt], Z4);
    v[qt] = (u32x4){pack2n(d1[0], d1[1]), pack2n(d1[2], d1[3]), pack2n(d2[0], d2[1]), pack2n(d2[2], d2[3])};
    PIN_V(v[qt]);
  }
  __device__ __forceinline__ void store(const __amdgpu_buffer_rsrc_t rs, unsigned off, unsigned o) {
    __builtin_amdgcn_raw_buffer_store_b128(v[0], rs, off, o, 16); __builtin_amdgcn_raw_buffer_store_b128(v[1], rs, off + 1024, o, 16);
    asm volatile("s_nop 1" ::: "memory");
  }
};

__device__ __forceinline__ void touch(const char* base, unsigned off, unsigned lds0) {
  unsigned keep;
  asm volatile("s_mov_b32 %0, m0\n\ts_mov_b32 m0, %3\n\ts_nop 0\n\tglobal_load_lds_dword %1, %2 sc1\n\ts_mov_b32 m0, %0" : "=&s"(keep) : "v"(off), "s"(base), "s"(lds0 + L_RED1 + 8192) : "memory");
}
template <int N> __device__ __forceinline__ void stouch(const char* p, unsigned& d) {
  asm volatile(".set pf_o, 0\n\t.rept %2\n\ts_load_dword %0, %1, pf_o\n\t.set pf_o, pf_o + 128\n\t.endr" : "+s"(d) : "s"(p), "n"(N) : "memory");
}
__device__ __forceinline__ void code_touch(int b, int lane, unsigned lds0) {
  const char* pc; int d0, d1;
  asm volatile("s_getpc_b64 %0\n\ts_mov_b32 %1, _Z10rec_kernel7RecArgs@rel32@lo+4\n\ts_mov_b32 %2, _Z11attb_kernelPKtS0_Pt@rel32@lo+12" : "=s"(pc), "=s"(d0), "=s"(d1));
  const int o = (lane * 32 + (b >> 3)) * 128;
  touch(pc + d0, (unsigned)(o < d1 - d0 ? o : 0), lds0);
}
__device__ __forceinline__ unsigned zero_base(int b, int h, int j, int ln) {
  return ln < 32 ? (unsigned)((h & 3) * 32768 + j * 512 + ln * 16) : ln < 40 ? (unsigned)((h & 2) * 1024 + j * 256 + (ln & 4) * 32 + (h & 1) * 64 + (ln & 3) * 16)
                 : (unsigned)OFF_ACC + (b < ACC_NB ? (unsigned)(b * 256 + (ln - 40) * 16) : ts_piece((b - ACC_NB) * 8 + ln - 40));
}
__device__ __forceinline__ unsigned sct_slot(unsigned ck, unsigned sl) { return sl * SCT_STEP + ((ck >> (sl & 4u)) & 7u) * 4096u; }
__device__ __forceinline__ unsigned scm_slot(unsigned ck, unsigned sl) { return OFF_SCM + sl * SCM_STEP + ((ck >> 8) & 7u) * 4096u; }
__device__ __forceinline__ int ck_rot(int x) { return ((x & 1) << 2) | (x >> 1); }
struct Place {
  unsigned t[8];
  __device__ __forceinline__ void load(const char* ws, int lane) {
#pragma unroll
    for (int x = 0; x < 8; ++x) t[x] = *(const unsigned*)(ws + OFF_LAT + ((imin(lane >> 3, 5) * 8 + (ck_rot(x) ^ (lane & 7))) * 8 + x) * 4);
  }
  __device__ __forceinline__ unsigned get(int b, int lane) {
    int c = (int)((((t[0] + t[1]) + (t[2] + t[3])) + ((t[4] + t[5]) + (t[6] + t[7]))) * 8u) + (lane & 7);
    c = imin(c, dppi<DPP_XOR1>(c)); c = imin(c, dppi<DPP_XOR2>(c)); c = imin(c, dppi<0x141>(c));
    unsigned k = 0u;
#pragma unroll
    for (int r = 0; r < 6; ++r) k |= (unsigned)(__builtin_amdgcn_readlane(c, 8 * r) & 7) << (4 * r);
    return (k ^ PL_X) ^ (unsigned)ck_rot(b & 7) * 0x111111u;
  }
};

struct Sched {
  u32x4 tsv, rec;
  unsigned side, o;
  __device__ __forceinline__ void pre(int s) { o = (unsigned)OFF_ACC + (unsigned)((s + SR - 1) % SR) * (unsigned)ACC_STEP + ACC_TS + side; PIN_S(o); }
  __device__ __forceinline__ void load(const __amdgpu_buffer_rsrc_t rs, int lane) {
    tsv = SC1_LOADS(ts_piece(lane & 63) - ACC_TS, o); rec = SC1_LOADS(128, o);
  }
  __device__ __forceinline__ unsigned last(unsigned ref) {
    const unsigned o = 0x40000000u - ref;
    int l = (int)umax(umax(tsv[0] ? tsv[0] + o : 0u, tsv[1] ? tsv[1] + o : 0u), umax(tsv[2] ? tsv[2] + o : 0u, tsv[3] ? tsv[3] + o : 0u));
    return (unsigned)wave_imax(l) - o;
  }
  __device__ __forceinline__ unsigned run(const RecArgs& a, const __amdgpu_buffer_rsrc_t rs, int s, int b, int lane) {
    constexpr int w = 3; (void)w;
    unsigned acur = __builtin_amdgcn_readfirstlane(rec[0]), aprev = __builtin_amdgcn_readfirstlane(rec[1]), hist = __builtin_amdgcn_readfirstlane(rec[3]);
    int D8 = (int)__builtin_amdgcn_readfirstlane(rec[2]);
    const int d = wave_imax((int)(umax(umax(tsv[0], tsv[1]), umax(tsv[2], tsv[3])) - aprev)), dd = 8 * d - D8;
    int Dn = D8 + (dd >> (dd > 0 ? SCH_GU : SCH_GD));
    unsigned anext = (acur + (unsigned)((Dn + 4 + imax(Dn - 8 * SCH_DR, 0) * SCH_DK / 8) >> 3) + SCH_V + (s < EARLY_N ? EARLY_V : (s >= WPF0 ? (s == NSEG - 2 ? LAST_V : LATE_V) : 0))) | 1u;
    if (__builtin_expect(hist < 2u || aprev == 0u || d < 1 || d + SCH_VMIN > (int)(acur - aprev), 0)) {
      if (s == 0) {
        for (int it = 0; it < 40 && !wave_all(min4(tsv) != 0u); ++it) {
          __builtin_amdgcn_s_sleep(ST_ALL);
          asm volatile("" ::: "memory");
          load(rs, lane);
        }
      }
      const unsigned now = now32(), L = last(now);
      if (acur == 0u) { D8 = 8 * SCH_D0; hist = 0u; }
      const bool late = acur == 0u || (int)(L + SCH_VMIN - acur) > 0;
      if (late) { if (b == 0) FC(3); if (acur) D8 += 8 * (hist ? SCH_UP : 3 * SCH_UP); }
      else if (aprev) { if (hist < 2u) D8 = 8 * imin(imax((int)(L - aprev), 1), 4000); hist = umin(hist + 1u, 2u); }
      const unsigned base = acur == 0u ? L + SCH_FIRST : (late ? L + SCH_V + ((int)(L + SCH_VMIN2 - acur) <= 0 ? SCH_PEN2 : SCH_PEN) : acur);
      anext = (base + (unsigned)((D8 + 4) >> 3) + SCH_V + (s == 1 ? EARLY_V1 : (s < EARLY_N ? EARLY_V : 0))) | 1u;
      if (s == 0) anext = (L + ST_X) | 1u;
      if ((int)(now - L) > 50000) anext = 0u;
      if (late) acur = 0u;
      Dn = D8;
    }
    if (__builtin_expect(b == 0, 0))
      __builtin_amdgcn_raw_buffer_store_b128((u32x4){anext, acur, (unsigned)Dn, hist}, rs, lane < 2 ? lane * 8192 : 0x7ffffff0, (unsigned)OFF_ACC + (unsigned)(s % SR) * (unsigned)ACC_STEP + ACC_TS + 128u, 16);
    if (XO_T && anext) anext += (b & 1) * XO_T;
    LOGV(0, anext); LOGV(1, acur == 0u); LOGV(2, (Dn + 4) >> 3); LOGV(3, d); LOGV(4, acur - aprev - d); LOGV(5, aprev);
    return anext;
  }
};

struct TokTask {
  u32x4 y[2]; u32x2 ym; u32x4 res;
  unsigned l0, l1, lm, s0, s1;
  static __device__ __forceinline__ bool ok(int sl) { return sl >= 0 && sl < NSEG; }
  __device__ __forceinline__ void init(int h, int j, int lane) {
    const int c = lane & 15, g = lane >> 4;
    l0 = ts_tok(h, g) + (unsigned)(j * 256 + c * 16); l1 = l0 + 16384u;
    lm = ts_mem(h, g + 4 * (c >> 3)) + (unsigned)(j * 64 + (c & 7) * 8);
    res = (u32x4){0u, 0u, 0u, 0u};
    const int qi = 4 * j + (c & 3);
    s0 = g < 2 ? pt_tok(h) + (unsigned)((qi >> 4) * 1024 + ((c >> 2) * 16 + (qi & 15)) * 16 + g * 8) : 0x7ffffff0u;
    s1 = lane < 8 ? pt_mem(h) + (unsigned)((qi >> 4) * 256 + ((c >> 2) * 16 + (qi & 15)) * 8) : 0x7ffffff0u;
    PIN_V(l0); PIN_V(l1); PIN_V(lm); PIN_V(s0); PIN_V(s1);
  }
  __device__ __forceinline__ void load(const __amdgpu_buffer_rsrc_t rs, unsigned o) {
    y[0] = SC1_LOADS(l0, o); y[1] = SC1_LOADS(l1, o);
    ym = __builtin_amdgcn_raw_buffer_load_b64(rs, lm, o, 16);
  }
  __device__ __forceinline__ bool ready() { return wave_all(umin(umin(min4(y[0]), min4(y[1])), umin(ym[0], ym[1])) != 0u); }
  __device__ __forceinline__ void run(int j, int lane, const bf16x8 SelLo) {
    const int c = lane & 15, g = lane >> 4;
    const f32x4 Z4 = {0.f, 0.f, 0.f, 0.f};
    const f32x4 d1 = mfma32(SelLo, as8(y[1]), mfma32(SelLo, as8(y[0]), Z4));
    const f32x4 d0 = mfma32(SelLo, mk8(ym[0], ym[1], 0u, 0u), Z4);
    const int qi = 4 * j + (c & 3), k0 = 16 * g + 4 * (c >> 2);
    float t[4], mv[4];
    float mx = -INFINITY;
#pragma unroll
    for (int r = 0; r < 4; ++r) {
      t[r] = (g < 2 && k0 + r <= qi) ? d1[r] : -INFINITY;
      const float mt = d0[r] + dppf<DPP_ROR8>(d0[r]);
      mv[r] = lane < 8 ? mt : -INFINITY;
      mx = fmx(mx, fmx(t[r], mv[r]));
    }
    mx = fmx(mx, dppf<DPP_ROR8>(mx)); mx = fmx(mx, dppf<DPP_ROR4>(mx));
    mx = x16mx(mx);
    float sum = 0.f;
#pragma unroll
    for (int r = 0; r < 4; ++r) {
      t[r] = __builtin_amdgcn_exp2f(t[r] - mx); mv[r] = __builtin_amdgcn_exp2f(mv[r] - mx);
      sum += t[r] + mv[r];
    }
    sum += dppf<DPP_ROR8>(sum); sum += dppf<DPP_ROR4>(sum);
    sum = x16sum(sum);
    const float rs_ = __builtin_amdgcn_rcpf(sum);
    res = (u32x4){pack2n(t[0] * rs_, t[1] * rs_), pack2n(t[2] * rs_, t[3] * rs_), pack2n(mv[0] * rs_, mv[1] * rs_), pack2n(mv[2] * rs_, mv[3] * rs_)};
    PIN_V(res);
  }
  __device__ __forceinline__ void store(const __amdgpu_buffer_rsrc_t rs, unsigned o, unsigned no = 0u) {
    SC1_STORE8S(((u32x2){res[0], res[1]}), s0 | no, o);
    SC1_STORE8S(((u32x2){res[2], res[3]}), s1 | no, o);
  }
};

constexpr int L_DRN = L_VOX + (NSEG % NV) * 5120;
__device__ __forceinline__ void drain_pre(char* lds, int b, int lane) {
  const int gp = (b & 7) * 2 + (b >> 7), m = (b >> 3) & 15;
  TokTask tk; PvTask2 pv; OutTask ot;
  tk.init(2 * gp + (m >> 3), m & 7, lane); pv.init(gp, lane); ot.init(b, lane);
  u32x4* p = (u32x4*)(lds + L_DRN) + lane;
  p[0] = (u32x4){tk.l0, tk.lm, tk.s0, tk.s1}; p[64] = (u32x4){pv.lo, pv.lm, pv.vr, pv.ma}; p[128] = (u32x4){pv.mz, pv.so, ot.t5f, ot.t5o};
  p[192] = __builtin_bit_cast(u32x4, ot.sel);
}
__device__ __forceinline__ void qkv_pre(unsigned (&vo)[2][2], int b, int w, int lane) {
  const int cb = qkv_col(b), c = lane & 15, g = lane >> 4, t0 = w & 1, col0 = (cb % 5) * 128 + w * 32;
  const bool SW = cb < 10;
#pragma unroll
  for (int i = 0; i < 2; ++i)
#pragma unroll
    for (int tt = 0; tt < 2; ++tt) {
      const int tq = tt ^ t0, tk = tq * 16 + c, cc = col0 + i * 16 + (SW ? 4 * g : c), hd = cc / BD, e = cc - hd * BD;
      const int kv = (int)(OFF_KV - OFF_QB) + hd * 64 * KVT;
      vo[i][tt] = (unsigned)(!SW ? kv + 1280 + tq * 672 + ((g >> 1) * 21 + e) * 16 + (g & 1) * 8
                                 : (cb < 5 ? (hd * S_ + tk) * 2 * QST + e * 2 : kv + (e < 16 ? (tk + 32 * (e >> 3)) * 16 + (e & 7) * 2 : 1024 + tk * 8)));
      PIN_V(vo[i][tt]);
    }
}

template <int w>
__device__ __forceinline__ void token_waves(const RecArgs& a, char* lds, const __amdgpu_buffer_rsrc_t rs, unsigned (&vo)[2][2]) {
  const int lane0 = __builtin_amdgcn_workitem_id_x() & 63, b = __builtin_amdgcn_workgroup_id_x(), wid = w + 4; (void)wid;
  const int gp = (b & 7) * 2 + (b >> 7), m = (b >> 3) & 15, h0 = 2 * gp, h = h0 + (m >> 3), j = m & 7;
  float* const misc = (float*)(lds + L_MISC);
  const f32x4 Z4 = {0.f, 0.f, 0.f, 0.f};
  const unsigned lds0 = (unsigned)(uintptr_t)(LAS char*)lds;
  unsigned* const cnt = (unsigned*)(misc + 6);
  const float* const xnrm = (const float*)(a.ws + OFF_XNRM);
  bf16x16 W[2][10];
  auto btile = [&](int i) { return w == 1 ? 0 : (w == 2 ? 1 + i : 3 + i); };
  if (w == 1) code_touch(b, lane0, lds0);
  Place pl;
  unsigned phi[2][2], ph[2][2][2];
#pragma unroll
  for (int k = 0; k < 8; ++k) {
    const int bt = btile(k >> 2), hw = h0 + (bt == 2 ? k & 1 : bt / 3), T = bt == 2 ? 32 + (m >> 1) : (bt % 3) * 16 + m;
    ph[k >> 2][(k >> 1) & 1][k & 1] = ((const __attribute__((address_space(4))) unsigned*)(a.ws + OFF_PHI))[(hw >> 2) | (hw & 3) << 3 | (k & 2) << 4 | (T / 10) << 6];
  }
  __builtin_amdgcn_sched_barrier(0);
  if (w == 3) pl.load(a.ws, lane0);
  if (a.layer) {
    if (w != 0 && FR_HOLD) wait_until(now32() + FR_HOLD);
    if (w != 0) img_fetch<20>(a, lds0, h, j, w - 1, lane0);
  } else {
    if (w == 0) img_fetch<12>(a, lds0, h, j, 4, lane0);
#pragma unroll
    for (int i = 0; i < 10; ++i) x_fetch(x_src(a, 0, b), lds0, 0, 10 * w + i, lane0);
  }
  const __amdgpu_buffer_rsrc_t rwb = __builtin_amdgcn_make_buffer_rsrc(a.ws + OFF_WB, 0, (int)WB_BYTES, 0x00020000);
#pragma unroll
  for (int k = 0; k < 4; ++k) phi[k >> 1][k & 1] = (((lane0 & 8) ? ph[k >> 1][k & 1][1] : ph[k >> 1][k & 1][0]) & 8192u) | (unsigned)(lane0 * 16);
#pragma unroll
  for (int i = 0; i < 2; ++i)
#pragma unroll
    for (int p = 0; p < 10; ++p)
      if (w >= 2 || (w == 1 && i == 1)) {
        const unsigned wo = wb_off((b * 5 + btile(i)) * 20 + 2 * p), wv = phi[i][p / 5] ^ (wo & 8192u);
        if (FR_PACE && a.layer) asm volatile("s_waitcnt vmcnt(%0)" :: "n"(FR_PACE) : "memory");
        W[i][p] = cat16(as8(__builtin_amdgcn_raw_buffer_load_b128(rwb, wv, wo & ~8192u, 0)), as8(__builtin_amdgcn_raw_buffer_load_b128(rwb, wv + 1024, wo & ~8192u, 0)));
      }
  f32x4 bn = Z4;
  if (w == 0) {
    bn = *(const f32x4*)(a.ws + OFF_BNRM + lane0 * 16);
    if (!a.layer) asm volatile("s_waitcnt vmcnt(0)" ::: "memory");
  } else asm volatile("s_waitcnt vmcnt(%0)" :: "n"(w == 1 ? 20 : 40) : "memory");
  unsigned ck = 0u;
  if (w == 3) { ck = pl.get(b, lane0); *(unsigned*)(misc + 11) = ck; }
  BAR();
#pragma unroll
  for (int p = 0; p < 10; ++p) {
    if (w < 2) W[0][p] = cat16(wqk_frag(lds, 2 * w, 2 * p, lane0), wqk_frag(lds, 2 * w, 2 * p + 1, lane0));
    if (w == 0) W[1][p] = cat16(wqk_frag(lds, 1, 2 * p, lane0), wqk_frag(lds, 1, 2 * p + 1, lane0));
  }
  float cb2 = 0.f;
  if (w == 0) {
    float n = __builtin_amdgcn_sqrtf(bn[0] + bn[1]) + __builtin_amdgcn_sqrtf(bn[2] + bn[3]);
    n += dppf<DPP_XOR1>(n); n += dppf<DPP_XOR2>(n); n += dppf<DPP_ROR4>(n); n += dppf<DPP_ROR8>(n);
    n = x32mx(x16mx(n));
    cb2 = u2f(__builtin_amdgcn_readfirstlane(f2u(1.15f * n * n)));
  }
  const float cm2 = u2f((f2u(cb2) & 0x7fffffu) | 0x40000000u);
  const int ek = imax((int)((f2u(cb2) >> 23) & 0xffu), 1) - 252;
  if (w >= 2) {
#pragma unroll
    for (int i = 0; i < 4; ++i) *(unsigned*)(lds + L_KX + (w - 2) * 4096 + i * 1024 + lane0 * 16 + 12) = 0u;
  }
  OutTask ot;
  const int zl = lane0 - 16;
  const bool zon = w == 3 && zl >= 0 && (zl < 40 || b < ACC_NB + 17);
  unsigned zo = zon ? zero_base(b, h, j, zl) + (zl < 32 ? sct_slot(ck, 0u) : (zl < 40 ? scm_slot(ck, 0u) : 0u)) : 0x7ffffff0u;
  unsigned zm = zon ? (zl < 32 ? SCT_STEP + 1u : (zl < 40 ? SCM_STEP : (unsigned)ACC_STEP)) : 0u;
  const unsigned zx = ((ck ^ (ck >> 4)) & 7u) * 4096u;
  if (w == 3) { PIN_V(zo); PIN_V(zm); }
  const __amdgpu_buffer_rsrc_t rx = __builtin_amdgcn_make_buffer_rsrc(a.ws + OFF_XNRM, 0, (int)(OFF_QB - OFF_XNRM), 0x00020000);
  u32x4 v0, v1;
  f32x4 d5;
  unsigned oo, za, xa, ya, n4, so_op, so_y, so_x;
  float xn4 = 0.f;
  auto w1_part = [&](int k, int s, int lane) {
    if (w < 2) return;
    if (w == 2) {
      if (k >= 1 && k <= 4) d5 = mfma32(ot.sel, as8(ot.y[k < 1 || k > 4 ? 0 : k - 1]), k == 1 ? Z4 : d5);
      if (k == 6 || k == 7) {
        const unsigned r = pack2(d5[2 * (k & 1)], d5[2 * (k & 1) + 1]), x = ot.xo[k & 1];
        ot.out[k & 1] = pack2(u2f(x << 16) + u2f(r << 16), u2f(x & 0xffff0000u) + u2f(r & 0xffff0000u));
        PIN_V(ot.out);
      }
      if (k == 8) xn4 = fmx(xn4, dppf<DPP_XOR1>(xn4));
      if (k == 9) xn4 = fmx(xn4, dppf<DPP_XOR2>(xn4));
      if (k == 10) xn4 = fmx(xn4, dppf<DPP_ROR4>(xn4));
      if (k == 11) xn4 = fmx(xn4, dppf<DPP_ROR8>(xn4));
      if (k >= 8 && k <= 11) PIN_V(xn4);
      if (k == 12) misc[s & 3] = x16mx(xn4);
      if (k == 5) { so_y = op_slot(imax(s - 1 - LAG5, 0)); so_x = (unsigned)imax(s - 1 - LAG5, 0) * 40960u; PIN_S(so_y); PIN_S(so_x); }
      if (k == 14) {
        ya = ot.t5o | (s <= LAG5 ? 0x7ff00000u : 0u);
        xa = (ot.t5f * 2u | (unsigned)(lane & 32) << 25) | (OutTask::ok(s - 2 - LAG5, b) ? 0u : 0x7ff00000u);
        PIN_V(ya); PIN_V(xa);
      }
    } else if (k == 8) {
      const unsigned zs = (unsigned)(s + 2) % SR;
      za = (zo + (s < 0 ? 1024u : zs) * (zm & ~1u)) ^ ((zm & 1u) * (zs & 4u ? zx : 0u));
      PIN_V(za);
    }
    if (k == 13) {
      n4 = __hip_atomic_load((unsigned*)(misc + 28), __ATOMIC_RELAXED, __HIP_MEMORY_SCOPE_WORKGROUP);
      so_op = s < 2 + LAG4 ? 0x7ff00000u : op_slot(s - 2 - LAG4) + (unsigned)((m * 32 + (w & 1) * 16 + gp) * 1280);
      PIN_S(so_op);
    }
    if (k == 15 && __builtin_expect((int)__builtin_amdgcn_readfirstlane(n4) < s, 0)) lds_spin<L_MISC + 112, 1>(lds0, (unsigned)s);
    if (k == 16) {
      asm volatile("" ::: "memory");
      const char* sp = lds + L_STG + 2048 + (w & 1) * 1280;
      v0 = *(const u32x4*)(sp + lane * 16); v1 = *(const u32x4*)(sp + 1024 + (lane & 15) * 16);
      oo = so_op + (unsigned)(lane * 16);
      za = lane < 16 ? oo + 1024 : (w == 3 ? za : 0x7ffffff0u);
      PIN_V(oo); PIN_V(za);
    }
    if (k == 19 && w == 3) {
#pragma unroll
      for (int i = 0; i < 4; ++i) { v1[i] = lane < 16 ? v1[i] : 0u; PIN_V(v1[i]); }
    }
    __builtin_amdgcn_sched_barrier(0);
  };
  auto w1_fire = [&](int s, int lane, auto last) {
    if (w < 2) return;
    lds_spin<L_MISC + 92, W1_SL>(lds0, 4u * (unsigned)imax(s, 0));
    SC1_STORE(v0, oo);
    SC1_STORE(v1, za);
    __builtin_amdgcn_sched_barrier(0);
    if (w == 2) {
      __builtin_amdgcn_raw_buffer_store_b64(ot.out, rx, xa, so_x + (unsigned)(OFF_XA - OFF_XNRM - 40960), 16);
      if (last == 0) {
#pragma unroll
        for (int k = 0; k < 4; ++k) ot.y[k] = SC1_LOADS(ya, so_y + k * 5120);
        ot.xo = __builtin_amdgcn_raw_buffer_load_b64(rx, ot.t5f * 2u, so_x + (unsigned)(OFF_XB - OFF_XNRM), 16);
        xn4 = u2f(__builtin_amdgcn_raw_buffer_load_b32(rx, (lane & 31) * 16, (s + 1) * 512, 16));
      }
    } else {
      const unsigned xo_ = (unsigned)((s - 8) * 1024 + lane * 16);
      if (__builtin_expect((unsigned)(s - 8) < 20u, 0)) SC1_STORE(((u32x4){0u, 0u, 0u, 0u}), xo_ < X_BYTES / 256 ? (unsigned)(OFF_X + b * (X_BYTES / 256)) + xo_ : 0x7ffffff0u);
    }
    __builtin_amdgcn_sched_barrier(0);
  };
  STAMP_DECL(); ARRIVE_DECL();
  auto mma_seg = [&](int seg, int sv, int lane, auto pause) {
    const int c = lane & 15, g = lane >> 4;
    const char* xp = lds + L_XN + (seg & 1) * 40960 + lane * 16;
    f32x4 acc[2][2] = {{Z4, Z4}, {Z4, Z4}};
    bf16x8 x[4][2];
#pragma unroll
    for (int p = 0; p < 3; ++p) { x[p][0] = *(const bf16x8*)(xp + p * 1024); x[p][1] = *(const bf16x8*)(xp + (20 + p) * 1024); }
    unsigned eo[2];
#pragma unroll
    for (int ks = 0; ks < 20; ++ks) {
      if (ks == (w == 0 ? KP0 : (w == 1 ? KP1 : KP2))) { pause(); __builtin_amdgcn_sched_barrier(0); }
      if (ks == 17) {
#pragma unroll
        for (int i = 0; i < 2; ++i) {
          eo[i] = (unsigned)(w == 0 ? (i ? L_KX : L_QX) + (seg % NKQ) * 2048 + lane * 16
                                    : (w == 1 && i == 0 ? ((g >> 1) ? L_KX : L_QX) + (seg % NKQ) * 2048 + (2 * (g & 1) * 16 + c) * 16 + 8 : L_VOX + sv * 5120 + btile(i) * 1024 + lane * 16));
          if (i == 0 || w == 1) PIN_V(eo[i]);
        }
        if (w == 0) eo[1] = eo[0] + (L_KX - L_QX);
        if (w >= 2) eo[1] = eo[0] + 1024;
      }
      __builtin_amdgcn_sched_barrier(0);
#pragma unroll
      for (int q = 0; q < 4; ++q) {
        const int tt = 1 - (q >> 1), i = q & 1;
        const bool isqk = w == 0 || (w == 1 && i == 0);
        const bf16x8 wf = half8(W[i][ks >> 1], ks & 1);
        acc[i][tt] = isqk ? mfma32(wf, x[ks & 3][tt], acc[i][tt]) : mfma32(x[ks & 3][tt], wf, acc[i][tt]);
        __builtin_amdgcn_sched_barrier(0);
        if (q < 2 && ks + 3 < 20) x[(ks + 3) & 3][q] = *(const bf16x8*)(xp + (q * 20 + ks + 3) * 1024);
        if (q == 2) w1_part(ks, seg - 1, lane);
        __builtin_amdgcn_sched_barrier(0);
      }
    }
#pragma unroll
    for (int i = 0; i < 2; ++i) {
      if (w + i == 1) { const float ks_ = (w == 0 || (g >> 1)) ? KEXP : 1.0f; acc[i][0] *= ks_; acc[i][1] *= ks_; }
      const u32x4 v = {pack2(acc[i][0][0], acc[i][0][1]), pack2(acc[i][0][2], acc[i][0][3]), pack2(acc[i][1][0], acc[i][1][1]), pack2(acc[i][1][2], acc[i][1][3])};
      char* p = lds + eo[i];
      if (w == 0) { *(u32x2*)(p) = (u32x2){v[0], v[1]}; *(u32x2*)(p + 1024) = (u32x2){v[2], v[3]}; }
      else if (w == 1 && i == 0) { *(unsigned*)(p) = v[0]; *(unsigned*)(p + 256) = v[1]; *(unsigned*)(p + 1024) = v[2]; *(unsigned*)(p + 1280) = v[3]; }
      else *(u32x4*)p = v;
    }
  };
  unsigned vp, mp, ip;
  int sidx, ei = 127;
  float inv;
  f32x4 acc[2];
  auto vo_pre = [&](int s, int sv, int lane) {
    vp = (unsigned)(w == 0 ? L_MISC + (s & 3) * 4 : L_VOM + sv * 1280 + (btile(0) * 32 + (w == 1 ? lane & 31 : lane)) * 8);
    mp = (unsigned)(L_BM + lane * 16);
    PIN_V(vp); PIN_V(mp);
    if (w == 0) { ip = (unsigned)(L_MISC + 16 + ((s + 1) & 1) * 4); PIN_V(ip); }
    else {
      inv = misc[4 + (s & 1)];
      sidx = (lane & 8) ? 0xEEEE : 0x4444;
      acc[0] = acc[1] = Z4;
      PIN_V(sidx); PIN_V(acc[1]);
      if (w >= 2) { PIN_V(acc[0]); PIN_V(inv); }
    }
    __builtin_amdgcn_sched_barrier(0);
  };
  auto vo_mem = [&](int s, auto last) {
    __builtin_amdgcn_s_setprio(2);
    asm volatile(".rept %0\n\ts_nop 3\n\t.endr" :: "n"(w == 0 ? VO_A0 : (w == 1 ? VO_A1 : VO_A2)) : "memory");
    float xmax = 0.f;
    if (w == 0) xmax = *(const float*)(lds + vp);
    constexpr int NX = w == 2 ? VO_N2 : (w == 3 ? VO_N3 : 10);
    bf16x8 x[10];
#pragma unroll
    for (int p = 0; p < NX; ++p) x[p] = *(const bf16x8*)(lds + mp + p * 1024);
    __builtin_amdgcn_sched_barrier(0);
    if (w < 2) asm volatile(".rept %0\n\ts_nop 3\n\t.endr" :: "n"(w == 0 ? VO_B0 : VO_B1) : "memory");
    if (VO_W >= 0) asm volatile("s_waitcnt lgkmcnt(%0)" :: "n"(VO_W < 0 ? 0 : VO_W) : "memory");
    STAMP(5);
    __builtin_amdgcn_sched_barrier(0);
    int ex = 0;
#pragma unroll
    for (int p = 0; p < 10; ++p) {
      if (w == 0) acc[0] = mfma32(x[p], x[p], p ? acc[0] : Z4);
      if (w == 0 && p == 9) { ex = imax((int)((__builtin_amdgcn_readfirstlane(f2u(__builtin_amdgcn_sqrtf(cb2 * xmax))) >> 23) & 0xffu) + 1, 32); PIN_S(ex); }
#pragma unroll
      for (int i = (w == 1); i < 2 && w != 0; ++i) acc[i] = smfma64(x[p], W[i][p], acc[i], sidx);
      __builtin_amdgcn_sched_barrier(0);
      if (p + NX < 10) x[p + NX] = *(const bf16x8*)(lds + mp + (p + NX) * 1024);
      __builtin_amdgcn_sched_barrier(0);
    }
    STAMP(7);
    if (w != 0) {
      float v[4];
#pragma unroll
      for (int r = 0; r < 4; ++r) {
        const auto sw = __builtin_amdgcn_permlane32_swap(f2u(acc[w == 1][r]), f2u(acc[1][r]), false, false);
        v[r] = (u2f(sw[0]) + u2f(sw[1])) * inv;
      }
      *(u32x2*)(lds + vp) = (u32x2){pack2(v[0], v[1]), pack2(v[2], v[3])};
    } else {
      const int t = (int)f2u(fmx(fmx(acc[0][0], acc[0][1]), fmx(acc[0][2], acc[0][3])) * cm2);
      const int et = wave_imax(t) >> 23, em = ((et + ek) >> 1) + ei;
      int e = imax(et == 255 ? 256 : em, ex);
      PIN_S(e);
      e = imin(e, 256);
      ei = e - 31;
      *(unsigned*)(lds + ip) = (unsigned)ei << 23;
      const u32x3 sr = {(unsigned)(285 - e) << 23, (unsigned)(e - 87) < 79u ? REC_RNG << 23 : 0u, (unsigned)(s + 1)};
      asm volatile("ds_write_b96 %0, %1 offset:%2" :: "v"(lds0), "v"(sr), "n"(L_MISC + 32) : "memory");
    }
    if (last == 0 && w >= 2) asm volatile("s_waitcnt vmcnt(%0)" :: "n"(w == SCH_W ? 2 : 0) : "memory");
    if (w != 0) lds_inc<L_MISC + 28>(lds0);
    STAMP(8);
    if (last) __builtin_amdgcn_s_setprio(0);
    if (last && w == 2) { VM_DRAIN(); lds_inc<L_MISC + 28>(lds0); }
  };
  unsigned* const cntq = (unsigned*)(misc + 12);
  unsigned* const cntt = (unsigned*)(misc + 26 + (w & 1));
  unsigned* const cntp = (unsigned*)(misc + 23);
  unsigned* const cntx2 = (unsigned*)(misc + 28);
  auto wpf = [&](int s, int lane) {
    constexpr int PER = 81920 / QR / WPF_N;
    static_assert(PER % WPF_S == 0 && PER / WPF_S <= 64, "prefetch");
    touch(a.ws + OFF_WQKVBT + (size_t)imin(qkv_col(b), 14) * 163840 + (qkv_row(b) % (2 * QR)) * (81920 / QR), (s - WPF0) * PER + imin(lane, PER / WPF_S - 1) * WPF_S, lds0);
  };
  Sched sch;
  auto late = [&](int s, int lane) {
    if (__builtin_expect(s < 0, 0)) return;
    if (w == SCH_W && SCH_LATE && __builtin_expect(s >= 0, 1)) *(u32x2*)(misc + 24) = (u32x2){sch.run(a, rs, s, b, lane), (unsigned)(s + 1)};
    if ((w == 2 && F6) || (w == 0 && F4) || (w == 1 && F5) || (w == 3 && F7)) { lds_spin<L_MISC + 48, 1>(lds0, 2u * (unsigned)imax(s + 1, 0)); if (FB_SL && w != 2) __builtin_amdgcn_s_sleep(FB_SL); }
    if (w >= 2) { x_fetch_n<w == 2 ? F6 : F7>(x_src(a, s + 3, b), lds0, s + 3, F4 + F5 + 2 * F67 + (w == 3 ? F6 : 0), lane * 16); return; }
    unsigned sd = 0u;
    if ((w == 0 && XPF == 3) || XPF == 4 || (w == 1 && XPF == 5)) stouch<10>(a.ws + OFF_XN + (size_t)imin(s + 4, NSEG - 1) * (s < 1 ? 0 : 40960) + (b >> 3) * 1280 + w * 64, sd);
    x_fetch_n<w == 0 ? F4 : F5>(x_src(a, s + 3, b), lds0, s + 3, w == 0 ? 0 : F4, lane * 16);
    if (WPF_W == 0 && w == 0 && __builtin_expect(s >= WPF0 && s < WPF0 + WPF_N, 0)) wpf(s, lane);
    if (w == 1 && __builtin_expect(s == CPF_S, 0)) code_touch(b, lane, lds0);
    lds_spin<L_MISC + 104 + 4 * (w & 1), PS_SL>(lds0, (unsigned)imax(s + 1, 0));
    asm volatile("" :: "s"(sd));
  };
  if (w == 2) { ot.init(b, lane0); ot.out = (u32x2){0u, 0u}; ot.xo = ot.out; ot.y[0] = ot.y[1] = ot.y[2] = ot.y[3] = (u32x4){0u, 0u, 0u, 0u}; }
  VM_DRAIN();
  lds_arrive(cnt, lane0);
  lds_wait(cnt, 8u);
  sch.tsv = sch.rec = (u32x4){0u, 0u, 0u, 0u};
  sch.side = __builtin_amdgcn_readfirstlane(*(const unsigned*)(misc + 11)) & 8192u;
  sch.o = 0u;
  int sv = NV - 1;
  CODE_PAD((PADT >> (4 * w)) & 15);
#pragma unroll 1
  for (int s = -1; s < NSEG - 1; ++s) {
    const int sv1 = sv == NV - 1 ? 0 : sv + 1;
    FRESH_LANE();
    STAMP(0);
    mma_seg(s + 1, sv1, lane, [&]() { STAMP(1); late(s - 1, lane); if (w == SCH_W) sch.pre(s); STAMP(9); });
    STAMP(2);
    __builtin_amdgcn_sched_barrier(0);
    VM_DRAIN();
    STAMP(10);
    w1_fire(s, lane, std::integral_constant<int, 0>{});
    if (w >= 2) vo_pre(s, sv, lane);
    STAMP(3);
    BAR();
    STAMP(4);
    if (__builtin_expect(s >= 0, 1)) {
      if (w == SCH_W) { sch.load(rs, lane0); __builtin_amdgcn_sched_barrier(0); }
      if (w < 2) vo_pre(s, sv, lane);
      vo_mem(s, std::integral_constant<int, 0>{});
      STAMP(6);
      if (w == SCH_W && !SCH_LATE) {
        __builtin_amdgcn_s_setprio(SCH_P);
        *(u32x2*)(misc + 24) = (u32x2){sch.run(a, rs, s, b, lane0), (unsigned)(s + 1)};
      }
      STAMP(11);
    }
    __builtin_amdgcn_s_setprio(MMA_P);
    if (w == 2) VM_DRAIN();
    __builtin_amdgcn_sched_barrier(0);
    sv = sv1;
  }
  __builtin_amdgcn_s_setprio(0);
  STAMP_OUT();
  late(NSEG - 2, lane0);
  { FRESH_LANE(); if (w == 0) drain_pre(lds, b, lane); qkv_pre(vo, b, w, lane); }
#pragma unroll
  for (int k = 0; k < 20; ++k) w1_part(k, NSEG - 1, lane0);
  w1_fire(NSEG - 1, lane0, std::integral_constant<int, 1>{});
  if (w != 0) vo_pre(NSEG - 1, (NSEG - 1) % NV, lane0);
  BAR();
  if (w != 0) vo_mem(NSEG - 1, std::integral_constant<int, 1>{});
}

__device__ __forceinline__ void qkv_tail(const RecArgs& a, char* lds, const int w, const unsigned (&vo)[2][2]) {
  const int lane0 = __builtin_amdgcn_workitem_id_x() & 63, b = __builtin_amdgcn_workgroup_id_x();
  const int r = qkv_row(b), cb = qkv_col(b);
  if (cb >= 15) return;
  const unsigned lds0 = (unsigned)(uintptr_t)(LAS char*)lds;
  unsigned* const stage = (unsigned*)(lds + L_MISC) + 17;
  unsigned* const cnty = stage + 1;
  const f32x4 Z4 = {0.f, 0.f, 0.f, 0.f};
  auto fetch = [&](int i) {
    x_fetch_n<10>(x_src(a, NSEG + i, b), lds0, i, 10 * w, lane0 * 16);
  };
  unsigned want = 0;
  auto sync = [&]() { VM_DRAIN(); want += 4u; lds_inc<L_MISC + 72>(lds0); while (__hip_atomic_load(cnty, __ATOMIC_RELAXED, __HIP_MEMORY_SCOPE_WORKGROUP) < want) { } asm volatile("" ::: "memory"); };
  LOGQ(0);
  bf16x8 W[2][20];
  const __amdgpu_buffer_rsrc_t rw = __builtin_amdgcn_make_buffer_rsrc(a.ws + OFF_WQKVBT + (size_t)(cb * 8 + 2 * w) * 20480, 0, 40960, 0x00020000);
  auto w_get = [&](int ks) {
#pragma unroll
    for (int i = 0; i < 2; ++i) W[i][ks] = as8(__builtin_amdgcn_raw_buffer_load_b128(rw, lane0 * 16 + (ks & 3) * 1024, (i * 20 + (ks & ~3)) * 1024, 0));
    __builtin_amdgcn_sched_barrier(0);
  };
#pragma unroll
  for (int ks = 0; ks < QW; ++ks) w_get(ks);
  bf16x8 ones = mk8(0x3f803f80u, 0x3f803f80u, 0x3f803f80u, 0x3f803f80u);
  PIN_V(ones);
  const __amdgpu_buffer_rsrc_t ro = __builtin_amdgcn_make_buffer_rsrc(a.ws + OFF_QB, 0, (int)(OFF_OB - OFF_QB), 0x00020000);
  const int t0 = w & 1;
  auto body = [&](auto SW, auto SQ) {
    const float qs = cb < 5 ? 0.22360679774997896f * 1.4426950408889634f : 1.0f;
    f32x4 cs[2] = {Z4, Z4};
    LOGQ(1);
    f32x4 acc[2][2], st;
    auto mma = [&](int it, auto FIRST) {
      const char* xp0 = lds + L_XN + (it & 1) * 40960 + t0 * 20480 + lane0 * 16;
      const char* xp1 = lds + L_XN + (it & 1) * 40960 + (1 - t0) * 20480 + lane0 * 16;
      acc[0][0] = acc[0][1] = acc[1][0] = acc[1][1] = st = Z4;
      bf16x8 x[4][2];
#pragma unroll
      for (int p = 0; p < 3; ++p) { x[p][0] = *(const bf16x8*)(xp0 + p * 1024); x[p][1] = *(const bf16x8*)(xp1 + p * 1024); }
      __builtin_amdgcn_sched_barrier(0);
#pragma unroll
      for (int ks = 0; ks < 20; ++ks) {
        if (FIRST && ks + QW < 20) w_get(ks + QW);
#pragma unroll
        for (int q = 0; q < 4; ++q) {
          const int tt = 1 - (q >> 1), i = 1 - (q & 1);
          acc[i][tt] = SW ? mfma32(W[i][ks], x[ks & 3][tt], acc[i][tt]) : mfma32(x[ks & 3][tt], W[i][ks], acc[i][tt]);
          __builtin_amdgcn_sched_barrier(0);
          if (q < 2 && ks + 3 < 20) x[(ks + 3) & 3][q] = *(const bf16x8*)((q ? xp1 : xp0) + (ks + 3) * 1024);
          __builtin_amdgcn_sched_barrier(0);
        }
        st = mfma32(SQ ? x[ks & 3][0] : ones, x[ks & 3][0], st);
        if (FIRST) {
#pragma unroll
          for (int i = 0; i < 2; ++i) cs[i] = SW ? mfma32(W[i][ks], ones, cs[i]) : mfma32(ones, W[i][ks], cs[i]);
        }
        __builtin_amdgcn_sched_barrier(0);
      }
    };
    mma(0, std::integral_constant<bool, true>{});
#pragma unroll 1
    for (int it = 0; it < 4; ++it) {
      if (it) mma(it, std::integral_constant<bool, false>{});
      LOGQ(2 + 3 * it);
      FRESH_LANE();
      float* const sp = (float*)(lds + L_ST) + (it & 1) * 64;
      if (SQ) { if ((c >> 2) == g) sp[32 + t0 * 16 + c] = (c & 3) == 0 ? st[0] : ((c & 3) == 1 ? st[1] : ((c & 3) == 2 ? st[2] : st[3])); }
      else if (g == 0) sp[t0 * 16 + c] = st[0];
      sync();
      LOGQ(3 + 3 * it);
      if (it == 0) fetch(2);
      const bool late = r + 48 >= 55;
      if (it == 1 && !late) fetch(3);
      const unsigned so = (unsigned)((r + 16 * it) * (cb < 5 ? 64 * QST : KVT));
      auto stat = [&](float sm, float sq, float& mean, float& rstd) {
        mean = sm * (1.0f / DM);
        rstd = __builtin_amdgcn_rsqf(__builtin_fmaxf(sq * (1.0f / DM) - mean * mean, 0.f) + 1e-5f);
      };
#pragma unroll
      for (int tt = 0; tt < 2; ++tt) {
        const int tq = tt ^ t0;
        float mean[4], rstd[4];
        if (SW) {
          stat(sp[tq * 16 + c], sp[32 + tq * 16 + c], mean[0], rstd[0]);
          rstd[0] *= qs;
        } else {
          const f32x4 sm = *(const f32x4*)(sp + tq * 16 + 4 * g), sq = *(const f32x4*)(sp + 32 + tq * 16 + 4 * g);
#pragma unroll
          for (int k = 0; k < 4; ++k) stat(sm[k], sq[k], mean[k], rstd[k]);
        }
#pragma unroll
        for (int i = 0; i < 2; ++i) {
          float v[4];
#pragma unroll
          for (int k = 0; k < 4; ++k) v[k] = (acc[i][tt][k] - mean[SW ? 0 : k] * cs[i][SW ? k : 0]) * rstd[SW ? 0 : k];
          __builtin_amdgcn_raw_buffer_store_b64((u32x2){pack2(v[0], v[1]), pack2(v[2], v[3])}, ro, vo[i][tt], so, Q_AUX);
        }
      }
      LOGQ(4 + 3 * it);
      if (it == 2 && late) lds_spin<L_MISC + 68, 0>(lds0, 5u);
    }
  };
  typedef std::integral_constant<bool, false> F_; typedef std::integral_constant<bool, true> T_;
  if (cb < 10) { if (w >> 1) body(T_{}, T_{}); else body(T_{}, F_{}); }
  else { if (w >> 1) body(F_{}, T_{}); else body(F_{}, F_{}); }
}

__device__ __forceinline__ void t2_mem(const bf16x8 fr, const bf16x8 q0, const bf16x8 q1, const __amdgpu_buffer_rsrc_t rs, unsigned o, int h, int j, float inv, int lane) {
  const f32x4 Z4 = {0.f, 0.f, 0.f, 0.f};
  const f32x4 d0 = mfma32(fr, q0, Z4) * inv, d1 = mfma32(fr, q1, Z4) * inv;
  const unsigned a0 = pack2n(d0[0], d0[1]), a1 = pack2n(d0[2], d0[3]), b0 = pack2n(d1[0], d1[1]), b1 = pack2n(d1[2], d1[3]);
  const auto r0 = __builtin_amdgcn_permlane32_swap(a0, b0, false, false), r1 = __builtin_amdgcn_permlane32_swap(a1, b1, false, false);
  SC1_STORE8S(((u32x2){r0[1], r1[1]}), ts_mem(h, j) + (lane >> 5) * 256 + (lane & 12) * 16 + (lane & 16) * 2 + (lane & 3) * 8, o);
}

template <int w>
__device__ __forceinline__ void rec_waves(const RecArgs& a, char* lds, __amdgpu_buffer_rsrc_t rs, unsigned& tlast) {
  PIN_S(rs);
  const int lane = __builtin_amdgcn_workitem_id_x() & 63, b = __builtin_amdgcn_workgroup_id_x(), c = lane & 15, g = lane >> 4, wid = w; (void)wid;
  const int gp = (b & 7) * 2 + (b >> 7), m = (b >> 3) & 15, h0 = 2 * gp, h = h0 + (m >> 3), j = m & 7;
  float* const misc = (float*)(lds + L_MISC);
  const f32x4 Z4 = {0.f, 0.f, 0.f, 0.f};
  LOGP(0);
  __builtin_amdgcn_s_setprio(3);
  const unsigned lds0 = (unsigned)(uintptr_t)(LAS char*)lds;
  if (!a.layer) img_fetch<12>(a, lds0, h, j, w, lane);
  constexpr int NC = w < 2 ? 2 : 3;
  const int far = ((b >> 2) & 1) ^ 1;
  auto chunk = [&](int f) { return f == 0 ? 4 * far + w : (w == 2 && f == 1 ? 8 + far : (w == 3 && f == 2 ? 9 - far : 4 - 4 * far + w)); };
  bf16x16 Wqk[3][NC];
  u32x4 bm0[NC][2];
#pragma unroll
  for (int f = 0; f < 2 * NC; ++f) bm0[f >> 1][f & 1] = *(const u32x4*)(a.mem0 + (size_t)(c & 7) * DM + 64 * chunk(f >> 1) + 16 * g + 8 * (f & 1));
  if (!a.layer) asm volatile("s_waitcnt vmcnt(0)" ::: "memory");
  BAR();
  LOGP(9);
  const unsigned ck = __builtin_amdgcn_readfirstlane(*(const unsigned*)(misc + 11));
  const unsigned flip = (ck ^ (unsigned)(b << 11)) & 8192u;
#pragma unroll
  for (int t = 0; t < 3; ++t)
#pragma unroll
    for (int f = 0; f < NC; ++f) Wqk[t][f] = cat16(wqk_frag(lds, t, 2 * chunk(f), lane), wqk_frag(lds, t, 2 * chunk(f) + 1, lane));
  if (a.layer) {
    if (w == 0) {
      for (unsigned spins = 0; spins < SPIN_CAP; ++spins) {
        if (__builtin_amdgcn_raw_buffer_load_b32(rs, (b & 7) * 256, (unsigned)OFF_RDY, 16) >= 16u) break;
        __builtin_amdgcn_s_sleep(4);
        asm volatile("" ::: "memory");
      }
      lds_inc<L_MISC + 84>(lds0);
    }
    lds_wait((unsigned*)(misc + 21), 2u);
#pragma unroll
    for (int f = 0; f < 2 * NC; ++f) PIN_V(bm0[f >> 1][f & 1]);
#pragma unroll
    for (int i = 0; i < 10; ++i) x_fetch(x_src(a, 0, b), lds0, 0, 10 * w + i, lane);
  }
  const bf16x8 SelLo = sel_frag(c, 0);
  bf16x8 ONES = mk8(0x3f803f80u, 0x3f803f80u, 0x3f803f80u, 0x3f803f80u);
  if (w < 2) PIN_V(ONES);
  bool dead = false;
  float inv = 0.f;
  unsigned* const cnt2 = (unsigned*)(misc + 7);
  unsigned l2off[2 * NC];
#pragma unroll
  for (int f = 0; f < 2 * NC; ++f) { l2off[f] = acc_off(16 * chunk(f >> 1) + 4 * g + 2 * (f & 1) + (c >> 3), c & 7) ^ flip; PIN_V(l2off[f]); }
  int sidx = (c >> 3) ? 0xEEEE : 0x4444;
  PIN_V(sidx);
  const unsigned p1off = (unsigned)((w == 0 ? (h & 2) * 1024 + j * 256 + (h & 1) * 64 + (g & 1) * 8 : (h & 3) * 32768 + j * 512 + (g >> 1) * 256 + (g & 1) * 64) + (c & 4) * 32 + (c & 3) * 16);
  unsigned p1v = (c < 8) && (w != 0 || g >= 2) ? p1off : 0x7ffffff0u, p1cur = 0x7ffffff0u;
  PIN_V(p1v); PIN_V(p1cur);
  const int hd = (c >> 2) & 1, mr = hd ? 3 - (c & 3) : (c & 3);
  const unsigned l1off = (unsigned)(((h0 + hd) & 3) * 32768 + g * 512 + (w & 1) * 128 + (c >> 3) * 64 + mr * 16);
  const unsigned l1offm = (unsigned)((h0 & 2) * 1024 + (g + 4 * (c >> 3)) * 256 + (w & 1) * 128 + hd * 64 + mr * 16);
  const int plane = (lane & ~6) | ((lane & 2) << 1) | ((lane & 4) >> 1);
  unsigned acco = c < 8 ? (acc_off(64 * far + m * 4 + g, 4 * (w & 1) + mr) + (unsigned)(hd * 8)) ^ flip : 0x7ffffff0u;
  unsigned acco2 = c >= 8 ? (acc_off(64 - 64 * far + m * 4 + g, 4 * (w & 1) + mr) + (unsigned)(hd * 8)) ^ flip
                          : (c < 4 ? (acc_off(128 + m * 2 + (g & 1), 4 * (w & 1) + mr) + (unsigned)((g >> 1) * 8)) ^ flip : 0x7ffffff0u);
  if (w < 2) { PIN_V(acco); PIN_V(acco2); }
  unsigned bmo[NC];
#pragma unroll
  for (int f = 0; f < NC; ++f) { bmo[f] = (unsigned)(chunk(f) * 1024 + lane * 16); PIN_V(bmo[f]); }
  bf16x8 bm[NC];
  auto bm_put = [&](int f) { *(bf16x8*)(lds + L_BM + bmo[f]) = bm[f]; __builtin_amdgcn_sched_barrier(0); };
  f32x4 acc[3] = {Z4, Z4, Z4};
  const int rt2 = ((c >> 3) * 8 + 4 * (g & 1)) * 28 + 16 + (c & 7);
  unsigned redo[3] = {(unsigned)(4 * g * 28 + c), (unsigned)(g < 2 ? (4 * g + 2) * 28 + c : rt2), (unsigned)(g < 2 ? (4 * g + 10) * 28 + c : rt2 + 56)};
#pragma unroll
  for (int k = 0; k < 3; ++k) { redo[k] = L_RED1 + w * 1792 + redo[k] * 4; PIN_V(redo[k]); }
  auto red_put = [&]() {
    auto hs = [&](float x, float y) { const auto sw = __builtin_amdgcn_permlane32_swap(f2u(x), f2u(y), false, false); return u2f(sw[0]) + u2f(sw[1]); };
#pragma unroll
    for (int k = 0; k < 3; ++k) {
#pragma unroll
      for (int i = 0; i < 2; ++i) *(float*)(lds + redo[k] + i * 112) = k == 0 ? hs(acc[0][i], acc[1][i]) : hs(acc[k - 1][2 + i], acc[2][2 * k - 2 + i]);
      __builtin_amdgcn_sched_barrier(0);
    }
  };
#pragma unroll
  for (int f = 0; f < NC; ++f) {
    const u32x4 u = bm0[f][0], v = bm0[f][1];
    bm[f] = c < 8 ? mk8(u[0], u[2], v[0], v[2]) : mk8(u[1], u[3], v[1], v[3]);
    bm_put(f);
#pragma unroll
    for (int t = 0; t < 3; ++t) acc[t] = smfma64(bm[f], Wqk[t][f], acc[t], sidx);
  }
  red_put();
  VM_DRAIN();
  lds_arrive((unsigned*)(misc + 6), lane);
  lds_wait((unsigned*)(misc + 6), 8u);
  x_fetch_n<10>(x_src(a, 1, b), lds0, 1, 10 * w, lane * 16);
  LOGP(1);
  STAMP_DECL(); ARRIVE_DECL();
  bf16x8 fr, qx[2];
  auto kq_get = [&](int seg) {
    const char* p = lds + (w == 1 ? L_KX : L_QX) + (seg % NKQ) * 2048 + lane * 16;
    if (w == 1 || w == 2) { qx[0] = *(const bf16x8*)p; qx[1] = *(const bf16x8*)(p + 1024); }
  };
  unsigned qo4 = (unsigned)(L_RED1 + (w == 1 ? (c & 7) * 112 + (c >> 3) * 3584 : c * 112) + g * 16), qo2 = qo4 + 64u - (unsigned)(g * 8), qo2b = qo2 + 3584u;
  if (w < 3) { PIN_V(qo4); PIN_V(qo2); if (w != 1) PIN_V(qo2b); }
  auto pub1 = [&](unsigned off, unsigned o1) {
    if (w == 0) fr = qk_frag(lds + qo4, lds + qo2, lds + qo2b);
    else {
      const f32x4 a0 = *(const f32x4*)(lds + qo4), a1 = *(const f32x4*)(lds + qo4 + 1792);
      const f32x2 b0 = *(const f32x2*)(lds + qo2), b1 = *(const f32x2*)(lds + qo2 + 1792);
      float v[6];
#pragma unroll
      for (int i = 0; i < 6; ++i) v[i] = i < 4 ? a0[i] + a1[i] : b0[i - 4] + b1[i - 4];
#pragma unroll
      for (int i = 0; i < 6; ++i) v[i] += dppf<DPP_ROR8>(v[i]);
      fr = mk8(pack2(v[0], v[1]), pack2(v[2], v[3]), pack2(v[4], v[5]), 0u);
    }
    const f32x4 sc = mfma32(w == 0 ? fr : qx[0], fr, Z4);
    if (w == 0) SC1_STORE8S(((u32x2){pack2n(sc[0], sc[1]), pack2n(sc[2], sc[3])}), off, o1);
    else {
      const f32x4 s2 = mfma32(qx[1], fr, Z4);
      __builtin_amdgcn_raw_buffer_store_b128((u32x4){pack2n(sc[0], sc[1]), pack2n(sc[2], sc[3]), pack2n(s2[0], s2[1]), pack2n(s2[2], s2[3])}, rs, off, o1, 16);
    }
  };
  unsigned o1 = sct_slot(ck, 0u), o1m = scm_slot(ck, 0u);
  unsigned t2o = ts_tok(h, j) + (unsigned)((c >> 2) * 256 + g * 64 + (c & 3) * 16);
  TokScores t2;
  TokTask tk;
  PvTask2 pv2;
  if (w == 2) tk.init(h, j, lane);
  if (w == 3) { PIN_V(t2o); pv2.init(gp, lane); }
  if (w == 0) pub1(p1v, o1m);
  VM_DRAIN();
  BAR();
  const unsigned tst = now32() | 1u;
  if (w == 3 && ST_X) __builtin_amdgcn_raw_buffer_store_b32(tst, rs, lane < 2 ? ts_piece(b >> 2) - ACC_TS + (b & 3) * 4 + lane * 8192 : 0x7ffffff0, (unsigned)(OFF_ACC + (SR - 1) * ACC_STEP) + ACC_TS, 16);
  LOGP(2);
  kq_get(0);
  if (w == 1) pub1(p1v, o1);
  x_fetch_n<10>(x_src(a, 2, b), lds0, 2, 10 * w, lane * 16);
  if (w == 3) { t2.get(lds, 0, lane); t2.run(0); t2.run(1); t2.store(rs, t2o, ts_slot(ck, 0)); }
  LOGP(3);
  unsigned o2 = (unsigned)OFF_ACC, o2p = 0u, tw = 0u;
  asm volatile("" : "+s"(o2p));
  unsigned tc0 = 0u, tstar = 0u;
  int dmin = 0, dmax = 0;
  int e1 = 100000;
  unsigned offs = 100u;
  unsigned o4 = 0u;
  unsigned o3, o3p;
  u32x16 P = {0u, 0u, 0u, 0u, 0u, 0u, 0u, 0u, 0u, 0u, 0u, 0u, 0u, 0u, 0u, 0u}, V = P;
  u32x8 V8 = {0u, 0u, 0u, 0u, 0u, 0u, 0u, 0u};
  if (w < 2) asm volatile("" : "+{v[208:223]}"(P), "+{v[184:191]}"(V8), "+{v[192:207]}"(V));
  unsigned tso = lane < 2 ? ts_piece(b >> 2) + (unsigned)((b & 3) * 4 + lane * 8192) : 0x7ffffff0u;
  if (w < 2) PIN_V(tso);
  float minf = g >= 2 ? -INFINITY : 0.f;
  if (w < 2) PIN_V(minf);
  int sv = NV - 1;
  bf16x8 vox[5];
  unsigned vma[3], need2;
  u32x4 slo = {0u, 0u, 0u, 0u}, shi = slo;
  float k1 = 0.f;
  const u16* xsrc;
  float invv = 1.0f;
  auto prep0 = [&](int s) {
    xsrc = x_src(a, s + 3, b);
    if (w == 2) { o3p = ts_slot(ck, (s + 1 - LAG3) & 3); o3 = ts_slot(ck, imax(s, 0)); o4 = pt_slot(ck, imax(s - LAG3, 0)); PIN_S(o3); }
    if (w == 3) { o3p = pt_slot(ck, (s - LAG4) & 3); o4 = ts_slot(ck, s + 1); }
    PIN_S(xsrc); PIN_S(o3p); PIN_S(o4);
    __builtin_amdgcn_sched_barrier(0);
  };
  auto prep = [&](int s) {
    o2 = (unsigned)OFF_ACC + (unsigned)(s % SR) * (unsigned)ACC_STEP;
    sv = sv == NV - 1 ? 0 : sv + 1;
    PIN_S(o2); PIN_S(sv);
    if (w < 2) {
      PIN_S(tc0);
      o1 = sct_slot(ck, s % SR); o1m = scm_slot(ck, s % SR);
      need2 = 4u * (unsigned)(s + 1) + 2u * ((unsigned)(s + 2) / NSEG);
      PIN_S(o1); PIN_S(o1m); PIN_S(need2);
      __builtin_amdgcn_sched_barrier(0);
      kq_get(s);
    }
    invv = misc[4 + (s & 1)];
  };
  auto prep2 = [&](int s) {
#pragma unroll
    for (int k = 0; k < 3; ++k) {
      const int t = k == 0 ? 2 : (k == 1 ? far : 1 - far);
      const char* p = lds + L_VOX + sv * 5120 + t * 1024 + (plane & 47) * 16 + (g & 1) * 8;
      auto rd = [&](const char* q) { const u32x2 a0 = *(const u32x2*)q, a1 = *(const u32x2*)(q + 256); return mk8(a0[0], a0[1], a1[0], a1[1]); };
      vox[k == 0 ? 0 : 2 * k - 1] = rd(p);
      if (k) vox[2 * k] = rd(p + 3072);
      vma[k] = lds0 + (unsigned)(L_VOM + sv * 1280 + t * 256 + (plane & 31) * 8);
      PIN_V(vma[k]);
    }
    __builtin_amdgcn_sched_barrier(0);
    if (F0A) { xsrc = x_src(a, s + 3, b); PIN_S(xsrc); }
    const unsigned iu = __builtin_amdgcn_readfirstlane(f2u(invv));
    if (__builtin_expect(iu != f2u(inv), 0)) {
      inv = u2f(iu);
#pragma unroll
      for (int i = 0; i < 8; ++i) {
        u32x4& m = i < 4 ? slo : shi;
        m[i & 3] = (c >> 1) == i ? ((c & 1) ? iu & 0xffff0000u : iu >> 16) : 0u;
      }
      k1 = KEXP * inv;
    }
    PIN_V(k1); PIN_V(slo); PIN_V(shi);
    asm volatile("" : "+v"(vox[0]), "+v"(vox[1]), "+v"(vox[2]), "+v"(vox[3]), "+v"(vox[4]));
    __builtin_amdgcn_sched_barrier(0);
  };
  auto rows = [&](int s) {
    const int dl = spin_until(tw);
    if (w < 2) tc0 = cyc32();
    __builtin_amdgcn_sched_barrier(0);
    STAMP(1);
    unsigned spins = 0;
    auto pass = [&](bool first) {
      u32x4 x[2 * NC];
#pragma unroll
      for (int f = 0; f < NC; ++f) {
        if (f && w < 2 && first) { prep(s); asm volatile(".rept %0\n\ts_nop 3\n\t.endr" :: "n"(P2_G1)); }
        else if (f) asm volatile(".rept %0\n\ts_nop 3\n\t.endr" :: "n"(w < 2 ? P2_G0 : (f == 1 ? P2_G2 : P2_G3)));
        __builtin_amdgcn_sched_barrier(0);
        x[2 * f] = SC1_LOADS(l2off[2 * f], o2p); x[2 * f + 1] = SC1_LOADS(l2off[2 * f + 1], o2p);
        __builtin_amdgcn_sched_barrier(0);
      }
      if (first) lds_inc<L_MISC + 92>(lds0);
      if (first && w < 2 && PR_P != 3) __builtin_amdgcn_s_setprio(PR_P);
      if (first) { if (w >= 2) prep(s); else prep2(s); }
      if (w >= 2 && first && P2_W) lds_spin<L_MISC + 92, 0>(lds0, 4u * (unsigned)s);
      if (w == 2 && first) tk.store(rs, o4, s > LAG3 ? 0u : 0x7ff00000u);
      if (w == 3 && first) t2.store(rs, t2o, o4);
      if (first && w < 2) {
        p1cur = p1v; offs = (offs - (unsigned)dl) & ~3u;
        PIN_V(p1cur); PIN_S(offs);
        tstar = tc0 + (unsigned)(e1 + D1_M + (w == 1 ? D1_W1 : 0)) - offs * 20u;
        dmin = offs < 40u ? D1_MIN : REC_D1;
        dmin = s < EARLY_N ? REC_D1 + EARLY_D1 : dmin;
        dmax = REC_D1;
        PIN_S(tstar); PIN_S(dmin); PIN_S(dmax);
      }
      __builtin_amdgcn_sched_barrier(0);
      unsigned ok = 32u;
#pragma unroll
      for (int t = 0; t < 3; ++t) { acc[t] = Z4; PIN_V(acc[t]); }
      if (first && w < 2 && PR_P != 3) __builtin_amdgcn_s_setprio(3);
      __builtin_amdgcn_sched_barrier(0);
      bool r = true;
#pragma unroll
      for (int f = 0; f < NC; ++f) {
        unsigned p[4];
#pragma unroll
        for (int k = 0; k < 2; ++k) {
          const u32x4 v = x[2 * f + k];
          p[2 * k] = pack2((float)(int)v[0], (float)(int)v[1]); p[2 * k + 1] = pack2((float)(int)v[2], (float)(int)v[3]);
          asm volatile("" : "+v"(p[2 * k]), "+v"(p[2 * k + 1]));
          __builtin_amdgcn_sched_barrier(0);
        }
        ok = (ok & x[2 * f][0] & x[2 * f][2]) & (x[2 * f + 1][0] & x[2 * f + 1][2]);
        PIN_V(ok);
        __builtin_amdgcn_sched_barrier(0);
        bm[f] = mk8(p[0], p[1], p[2], p[3]);
        acc[0] = smfma64(bm[f], Wqk[0][f], acc[0], sidx);
        __builtin_amdgcn_sched_barrier(0);
        bm_put(f);
        acc[1] = smfma64(bm[f], Wqk[1][f], acc[1], sidx);
        __builtin_amdgcn_sched_barrier(0);
        if (f == NC - 1) { u64 bad = __builtin_amdgcn_ballot_w64(ok == 0u); PIN_S(bad); r = bad == 0ull; }
        __builtin_amdgcn_sched_barrier(0);
        acc[2] = smfma64(bm[f], Wqk[2][f], acc[2], sidx);
        __builtin_amdgcn_sched_barrier(0);
      }
      red_put();
      return r;
    };
    if (__builtin_expect(!pass(true), 0)) {
      FC(0);
      do {
        if (dead || ++spins > SPIN_CAP) { dead = true; break; }
        asm volatile("" ::: "memory");
      } while (!pass(false));
    }
    STAMPV(12, spins); LOGF(1, spins);
    TSTAMP(2, now32()); TSTAMP(3, spins);
  };
  if (w >= 2) prep0(-1);
  prep(0);
  if (w < 2) prep2(0);
  VM_DRAIN();
  CODE_PAD((PADR >> (4 * w)) & 15);
#pragma unroll 1
  for (int s = 0;; ++s) {
    if (__builtin_expect(o2p != 0u, 1)) rows(s);
    if (w < 2 && __builtin_expect(s == NSEG - 1, 0)) break;
    STAMP(2);
    BAR();
    STAMP(3);
    if (w < 2) pub1(p1cur, w == 0 ? o1m : o1);
    if (w == 2) tk.load(rs, o3p);
    if (w == 3) pv2.load(rs, o3p);
    const unsigned tp1 = cyc32();
    __builtin_amdgcn_sched_barrier(0);
    if (w == 2) {
      __builtin_amdgcn_s_setprio(W2_P);
      prep0(s);
      if (W2_D) spin_cyc(tp1 + W2_D);
      fr = qk_frag(lds + qo4, lds + qo2, lds + qo2b);
      t2_mem(fr, qx[0], qx[1], rs, o3, h, j, invv * KEXP, lane);
      __builtin_amdgcn_sched_barrier(0);
      asm volatile("s_waitcnt vmcnt(1)" ::: "memory");
      lds_inc<L_MISC + 28>(lds0);
      kq_get(s + 1);
    }
    if (w == 3) {
      __builtin_amdgcn_s_setprio(T4_P);
      prep0(s);
      t2.get(lds, s + 1, lane); t2.run(0); t2.run(1);
      __builtin_amdgcn_sched_barrier(0);
    }
    if (w >= 2 && __builtin_expect(s == NSEG - 2, 0)) { VM_DRAIN(); lds_inc<L_MISC + 28>(lds0); }
    if (w >= 2) __builtin_amdgcn_s_setprio(UN_P);
    TSTAMP(0, now32());
    STAMP(4);
    __builtin_amdgcn_sched_barrier(0);
    unsigned tpub = 0;
    u32x2 sw;
    if (w == 2) {
      STAMP(5);
      __builtin_amdgcn_sched_barrier(0);
      STAMP(6);
      ARRIVE(0);
      tk.run(j, lane, SelLo);
      if (__builtin_expect(s == NSEG - 1, 0)) { tlast = tp1; break; }
      if (F2B) { lds_spin<L_MISC + 48, 1>(lds0, 2u * (unsigned)(s + 1)); x_fetch_n<F2B>(xsrc, lds0, s + 3, P2B, lane * 16); }
      __builtin_amdgcn_s_setprio(SP_P);
    }
    if (w == 3) {
      pv2.run<false>(lds, sv < 1 + LAG4 ? sv + NV - 1 - LAG4 : sv - 1 - LAG4, lane, rs, 0u);
      if (__builtin_expect(s == NSEG - 1, 0)) { tlast = tp1; break; }
      lds_inc<L_MISC + 112>(lds0);
      STAMP(11);
      __builtin_amdgcn_s_setprio(SP_P);
      STAMP(5);
      ARRIVE(0);
      if (F3) { lds_spin<L_MISC + 48, 1>(lds0, 2u * (unsigned)(s + 1)); x_fetch_n<F3>(xsrc, lds0, s + 3, 40 - F3C - F3, lane * 16); }
      STAMP(6);
    }
    if (w < 2) {
      __builtin_amdgcn_s_setprio(D1_P);
      x_fetch_n<F0A>(xsrc, lds0, s + 3, PA + w * F0A, lane * 16);
      int dq = imin((int)(tstar - tp1), dmax);
      dq = dq < -D1_LATE ? dmax : dq;
      PIN_S(dq);
      spin_exact(tp1 + (unsigned)imax(dq, dmin));
      STAMP(5);
      u32x32 S;
      auto sc_load = [&]() {
        asm volatile("buffer_load_dwordx4 v[224:227], %2, %4, %5 offen sc1\n\t.rept %7\n\ts_nop 3\n\t.endr\n\t"
                     "buffer_load_dwordx4 v[228:231], %3, %4, %6 offen sc1\n\tbuffer_load_dwordx4 v[232:235], %3, %4, %6 offen offset:2048 sc1\n\t"
                     "buffer_load_dwordx4 v[236:239], %3, %4, %6 offen offset:256 sc1\n\tbuffer_load_dwordx4 v[240:243], %3, %4, %6 offen offset:2304 sc1\n\t"
                     "v_mov_b32 v220, 0x42000000\n\tv_mov_b32 v221, 0\n\tv_mov_b32 v222, 0x42000000\n\tv_mov_b32 v223, 0"
                     : "=&{v[224:255]}"(S), "+{v[208:223]}"(P) : "v"(l1offm), "v"(l1off), "s"(rs), "s"(o1m), "s"(o1), "n"(P1_G) : "memory");
      };
      sc_load();
      __builtin_amdgcn_sched_barrier(0);
      lds_inc<L_MISC + 48>(lds0);
      STAMP(6);
      ARRIVE(0);
      lds_spin<L_MISC + 28, 0>(lds0, need2);
      __builtin_amdgcn_s_setprio(3);
      asm volatile("ds_read_b64 v[192:193], %3\n\tds_read_b64 v[196:197], %3 offset:768\n\tds_read_b64 v[200:201], %4\n\tds_read_b64 v[204:205], %4 offset:768\n\tds_read_b64 v[184:185], %2"
                   : "+{v[184:191]}"(V8), "+{v[192:207]}"(V) : "v"(vma[0]), "v"(vma[1]), "v"(vma[2]) : "memory");
      __builtin_amdgcn_sched_barrier(0);
      {
        const int de = imin((int)(tp1 - tc0) - e1, D1_C);
        e1 += s == 0 ? 0 : de >> (s < EARLY_N && de < -D1_C ? 0 : D1_G);
        PIN_S(e1);
      }
      unsigned sn = (unsigned)(s + 1);
      PIN_V(sn);
      STAMP(11);
      __builtin_amdgcn_sched_barrier(0);
      {
        unsigned spins = 0;
        u64 miss, bad;
#define MM(d, a, b, c) "v_mfma_f32_16x16x32_bf16 " d ", " a ", " b ", " c "\n\t"
#define FM(d, x, r, ctrl) "v_fmac_f32_dpp " d ", " x ", " r " " ctrl "\n\t"
#define ID_ "quad_perm:[0,1,2,3] row_mask:0xf bank_mask:"
#define HM_ "row_half_mirror row_mask:0xf bank_mask:"
#define R8_ "row_ror:8 row_mask:0xf bank_mask:"
#define MI_ "row_mirror row_mask:0xf bank_mask:"
        auto soft = [&]() {
          asm volatile(
            "s_waitcnt vmcnt(4)\n\t" MM("v[244:247]", "%6", "v[224:227]", "0") "v_min3_u32 v214, v224, v225, v226\n\t"
            "s_waitcnt vmcnt(3)\n\t" MM("v[248:251]", "%6", "v[228:231]", "0") "v_min3_u32 v214, v214, v227, v228\n\tv_min3_u32 v214, v214, v229, v230\n\t"
            "s_waitcnt vmcnt(2)\n\t" MM("v[248:251]", "%6", "v[232:235]", "v[248:251]") "v_min3_u32 v214, v214, v231, v232\n\t"
            "v_add_f32_dpp v212, v244, v244 " R8_ "0x3\n\tv_add_f32_dpp v213, v245, v245 " R8_ "0x3\n\t"
            "s_waitcnt vmcnt(1)\n\t" MM("v[248:251]", "%7", "v[236:239]", "v[248:251]")
            "v_add_f32_dpp v212, v246, v246 " R8_ "0xc\n\tv_add_f32_dpp v213, v247, v247 " R8_ "0xc\n\t"
            "s_waitcnt vmcnt(0) lgkmcnt(0)\n\t" MM("v[248:251]", "%7", "v[240:243]", "v[248:251]")
            "v_fma_f32 v212, v212, %14, %15\n\tv_fma_f32 v213, v213, %14, %15\n\tv_exp_f32 v216, v212\n\tv_exp_f32 v217, v213\n\t"
            "v_min3_u32 v214, v214, v233, v234\n\tv_cvt_pk_bf16_f32 v208, v216, v217\n\tv_min3_u32 v214, v214, v235, v236\n\tds_read_b96 v[188:190], %16 offset:%18\n\t"
            "v_exp_f32 v216, v248\n\tv_mov_b32_dpp v209, v208 " R8_ "0xf\n\tv_exp_f32 v217, v249\n\tv_exp_f32 v218, v250\n\t"
            MM("v[252:255]", "%8", "v[208:211]", "0") "v_exp_f32 v219, v251\n\tv_cvt_pk_bf16_f32 v232, v216, v217\n\t"
            MM("v[224:227]", "v[192:195]", "v[208:211]", "0") "v_cvt_pk_bf16_f32 v233, v218, v219\n\tv_min3_u32 v214, v214, v237, v238\n\tv_mov_b32_dpp v234, v232 " R8_ "0xf\n\t"
            MM("v[228:231]", "v[196:199]", "v[208:211]", "0") "v_mov_b32_dpp v235, v233 " R8_ "0xf\n\t"
            MM("v[244:247]", "v[204:207]", "v[208:211]", "0") "v_min3_u32 v214, v214, v239, v240\n\t"
            MM("v[252:255]", "%8", "v[232:235]", "v[252:255]") MM("v[224:227]", "%10", "v[232:235]", "v[224:227]") "v_min3_u32 v214, v214, v241, v242\n\t"
            MM("v[228:231]", "%11", "v[232:235]", "v[228:231]") "s_waitcnt lgkmcnt(0)\n\tv_sub_u32 v215, v190, %17 clamp\n\t"
            MM("v[236:239]", "v[200:203]", "v[208:211]", "0") "v_min3_u32 v214, v214, v243, v215\n\tv_cmp_eq_u32 %4, 0, v214\n\tv_rcp_f32 v216, v252\n\t"
            MM("v[240:243]", "v[184:187]", "v[208:211]", "0") "v_mul_f32 v216, v216, v188\n\tv_subrev_u32 v215, 0x2f800000, v252\n\t"
            MM("v[236:239]", "%12", "v[232:235]", "v[236:239]") FM("v220", "v224", "v216", ID_ "0x1") FM("v221", "v225", "v216", ID_ "0x1")
            "v_mov_b32_dpp v217, v216 " HM_ "0xf\n\t"
            MM("v[244:247]", "%13", "v[232:235]", "v[244:247]") FM("v220", "v230", "v216", ID_ "0x2") FM("v221", "v231", "v216", ID_ "0x2")
            MM("v[240:243]", "%9", "v[232:235]", "v[240:243]") FM("v220", "v228", "v217", HM_ "0x1") FM("v221", "v229", "v217", HM_ "0x1")
            "v_cmp_ge_u32 vcc, v215, v189\n\t" FM("v220", "v226", "v217", HM_ "0x2") FM("v221", "v227", "v217", HM_ "0x2")
            "s_or_b64 %5, %4, vcc"
            : "+{v[224:255]}"(S), "+{v[208:223]}"(P), "+{v[184:191]}"(V8), "+{v[192:207]}"(V), "=&s"(miss), "=&s"(bad)
            : "v"(slo), "v"(shi), "v"(ONES), "v"(vox[0]), "v"(vox[1]), "v"(vox[2]), "v"(vox[3]), "v"(vox[4]), "v"(k1), "v"(minf), "v"(lds0), "s"(s), "n"(L_MISC + 32)
            : "vcc", "scc", "memory");
        };
        soft();
        if (__builtin_expect(bad != 0ull, 0)) {
          if (miss) FC(1); else FC(2);
          while (miss != 0ull && !dead) {
            if (++spins > SPIN_CAP) dead = true;
            asm volatile("s_nop 7\n\ts_nop 7" ::: "memory");
            sc_load(); soft();
          }
          if (bad != 0ull) {
            u32x4 y[5];
#pragma unroll
            for (int k = 0; k < 4; ++k) y[k] = SC1_LOADS(l1off + (k & 1) * 2048 + (k >> 1) * 256, o1);
            y[4] = SC1_LOADS(l1offm, o1m);
            const f32x4 d0 = mfma32(as8(slo), as8(y[4]), Z4);
            f32x4 d1 = mfma32(as8(shi), as8(y[3]), mfma32(as8(shi), as8(y[2]), mfma32(as8(slo), as8(y[1]), mfma32(as8(slo), as8(y[0]), Z4))));
            const bool lo = c < 8;
            const float m0 = __builtin_fmaf((lo ? d0[0] : d0[2]) + dppf<DPP_ROR8>(lo ? d0[2] : d0[0]), k1, minf);
            const float m1 = __builtin_fmaf((lo ? d0[1] : d0[3]) + dppf<DPP_ROR8>(lo ? d0[3] : d0[1]), k1, minf);
            float mx = fmx(fmx(fmx(d1[0], d1[1]), d1[2]), fmx(fmx(d1[3], m0), m1));
            mx = fmx(mx, dppf<DPP_ROR8>(mx));
            mx = x32mx(x16mx(mx));
            d1 -= mx;
            const unsigned mm = pack2(__builtin_amdgcn_exp2f(m0 - mx), __builtin_amdgcn_exp2f(m1 - mx));
            const unsigned t01 = pack2(__builtin_amdgcn_exp2f(d1[0]), __builtin_amdgcn_exp2f(d1[1])), t23 = pack2(__builtin_amdgcn_exp2f(d1[2]), __builtin_amdgcn_exp2f(d1[3]));
            const bf16x8 pm = mk8(mm, dpp_ror8(mm), 0u, 0u), pt = mk8(t01, t23, dpp_ror8(t01), dpp_ror8(t23));
            const float r = __builtin_amdgcn_rcpf(mfma32(ONES, pt, mfma32(ONES, pm, Z4))[0]) * u2f(V8[4]), rm = dppf<0x141>(r);
            f32x4 o[5];
#pragma unroll
            for (int k = 0; k < 5; ++k) o[k] = mfma32(vox[k], pt, mfma32(k ? mk8(V[4 * k - 4], V[4 * k - 3], 0u, 0u) : mk8(V8[0], V8[1], 0u, 0u), pm, Z4));
#pragma unroll
            for (int i = 0; i < 2; ++i) {
              const float oa = dppf<0x141>(o[2][i]), ob = dppf<0x141>(o[1][2 + i]);
              P[12 + i] = f2u(__builtin_fmaf(hd ? ob : oa, rm, __builtin_fmaf(hd ? o[2][2 + i] : o[1][i], r, i ? 0.f : 32.0f)));
            }
            P[8] = f2u(r); P[9] = f2u(rm);
#pragma unroll
            for (int i = 0; i < 4; ++i) { S[12 + i] = f2u(o[3][i]); S[16 + i] = f2u(o[0][i]); S[20 + i] = f2u(o[4][i]); }
            asm volatile("s_nop 7\n\ts_nop 7" : "+{v[224:255]}"(S), "+{v[208:223]}"(P));
          }
        }
        STAMP(7); STAMPV(13, spins); LOGF(0, spins);
        {
          asm volatile(".if %11 == 0\n\tds_write_b32 %8, %12 offset:%10\n\t.endif\n\t"
                       "v_cvt_i32_f32 v218, v220\n\tv_cvt_i32_f32 v219, v221\n\tv_and_or_b32 v218, v218, %7, 2\n\tbuffer_atomic_add_x2 v[218:219], %3, %5, %6 offen\n\t"
                       ".if %11 == 1\n\tds_write_b32 %8, %12 offset:%10\n\t.endif\n\t"
                       FM("v222", "v236", "v216", R8_ "0x4") FM("v223", "v237", "v216", R8_ "0x4") "ds_read_b64 %2, %8 offset:%9\n\t"
                       FM("v222", "v238", "v217", MI_ "0x8") FM("v223", "v239", "v217", MI_ "0x8") "v_permlane32_swap_b32 v240, v242\n\t"
                       FM("v222", "v244", "v217", MI_ "0x4") FM("v223", "v245", "v217", MI_ "0x4") "v_permlane32_swap_b32 v241, v243\n\t"
                       FM("v222", "v246", "v216", R8_ "0x8") FM("v223", "v247", "v216", R8_ "0x8") ".if %11 == 2\n\tds_write_b32 %8, %12 offset:%10\n\t.endif\n\t"
                       FM("v222", "v240", "v216", ID_ "0x1") FM("v223", "v241", "v216", ID_ "0x1") FM("v222", "v242", "v217", HM_ "0x1") FM("v223", "v243", "v217", HM_ "0x1")
                       "v_cvt_i32_f32 v212, v222\n\tv_cvt_i32_f32 v213, v223\n\tv_and_or_b32 v212, v212, %7, 2\n\tbuffer_atomic_add_x2 v[212:213], %4, %5, %6 offen\n\t"
                       ".if %11 == 3\n\tds_write_b32 %8, %12 offset:%10\n\ts_waitcnt lgkmcnt(1)\n\t.else\n\ts_waitcnt lgkmcnt(0)\n\t.endif"
                       : "+{v[224:255]}"(S), "+{v[208:223]}"(P), "=&v"(sw) : "v"(acco), "v"(acco2), "s"(rs), "s"(o2), "s"(~63u), "v"(lds0), "n"(L_MISC + 96),
                         "n"(L_MISC + 104 + 4 * (w & 1)), "n"(REL_AT), "v"(sn) : "memory");
          __builtin_amdgcn_sched_barrier(0);
          tpub = now32();
          asm volatile("buffer_atomic_umax %0, %1, %2, %3 offen" :: "v"(tpub | 1u), "v"(tso), "s"(rs), "s"(o2) : "memory");
          STAMP(10);
        }
      }
    }
    STAMP(8);
    ARRIVE(1);
    o2p = o2;
    PIN_S(o2p);
    if (w < 2) {
      if (__builtin_expect(__builtin_amdgcn_readfirstlane(sw[1]) <= (unsigned)s, 0)) {
        lds_spin<L_MISC + 100, 0>(lds0, (unsigned)s + 1u);
        sw[0] = __hip_atomic_load((unsigned*)(misc + 24), __ATOMIC_RELAXED, __HIP_MEMORY_SCOPE_WORKGROUP);
      }
    } else {
      u32x4 q;
      asm volatile("1:\n\tds_read_b128 %0, %1 offset:%3\n\t.if %4\n\ts_sleep 1\n\t.endif\n\ts_waitcnt lgkmcnt(0)\n\tv_min3_u32 v201, v201, v202, v203\n\tv_cmp_ge_u32 vcc, %2, v201\n\ts_cbranch_vccnz 1b"
                   : "=&{v[200:203]}"(q) : "v"(lds0), "s"(s), "n"(L_MISC + 96), "n"(SP_SL) : "vcc", "memory");
      __builtin_amdgcn_s_setprio(3);
      tpub = now32();
      sw[0] = q[0];
    }
    STAMP(9);
    static_assert(F3C == 0 && F2C == 0 && F0C == 0, "nothing behind the atomics");
    LOGT(0, tpub); LOGT(1, tb2_);
    TSTAMP(1, tpub);
    ARRIVE_OUT();
    const unsigned an = __builtin_amdgcn_readfirstlane(sw[0]);
    const int ahead = (int)(an - tpub);
    tw = tpub + (unsigned)(an == 0u || ahead > 1000 ? SCH_FIRST : imax(ahead, w < 2 ? SCH_DMIN : 0));
    offs = tw - an;
  }
  STAMP_OUT();
  LOGP(4);
  __builtin_amdgcn_s_setprio(3);
  if (w < 2) { BAR(); tlast = cyc32(); }
  if (w == 2) tk.store(rs, o4);
  if (w == 3) {
    const unsigned oo = op_slot(NSEG - 2 - LAG4) + (unsigned)((m * 32 + gp) * 1280);
    const char* sp = lds + L_STG + 2048;
    const u32x4 v0 = *(const u32x4*)(sp + lane * 16), v1 = *(const u32x4*)(sp + 1280 + lane * 16), v2 = *(const u32x4*)(sp + (g & 1) * 1280 + 1024 + c * 16);
    SC1_STORE(v0, oo + lane * 16); SC1_STORE(v1, oo + 20480 + lane * 16); SC1_STORE(v2, lane < 32 ? oo + g * 20480 + 1024 + c * 16 : 0x7ffffff0u);
  }
  if (dead) tlast |= 1u; else tlast &= ~1u;
}

__device__ __forceinline__ void drain(const RecArgs& a, char* lds, const __amdgpu_buffer_rsrc_t rs, const int w, const unsigned tp1) {
  static_assert(LAG3 == 2 && LAG4 == 3 && LAG5 == 6, "drain schedule");
  const int lane = __builtin_amdgcn_workitem_id_x() & 63, b = __builtin_amdgcn_workgroup_id_x(), c = lane & 15;
  const int gp = (b & 7) * 2 + (b >> 7), m = (b >> 3) & 15, j = m & 7;
  unsigned* const misc = (unsigned*)(lds + L_MISC);
  const unsigned lds0 = (unsigned)(uintptr_t)(LAS char*)lds, ck = __builtin_amdgcn_readfirstlane(misc[11]);
  bool dead = tp1 & 1u;
  auto poll = [&](auto load, auto ready) {
    for (unsigned spins = 0;;) {
      asm volatile("" ::: "memory");
      load();
      if (ready() || dead) break;
      if (++spins > SPIN_CAP) dead = true;
      if (END_SL) __builtin_amdgcn_s_sleep(END_SL);
    }
  };
  LOGD(0);
#define TASK(ty, sg, tm) ((u64)((ty) | ((sg) - 56) << 2 | ((tm) / 100) << 5))
  u64 todo = w == 0 ? TASK(1, 63, END_E1) | TASK(2, 63, END_E2) << 12 | TASK(3, 62, END_E3) << 24
           : w == 1 ? TASK(1, 62, 0) | TASK(2, 62, END_E62) << 12 | TASK(3, 60, END_E3) << 24
           : w == 2 ? TASK(2, 60, 0) | TASK(3, 56, 0) << 12 : TASK(2, 61, END_E61) | TASK(3, 58, END_E59) << 12;
  TokTask tk;
  PvTask2 pv;
  OutTask ot;
  {
    const u32x4* p = (const u32x4*)(lds + L_DRN) + lane;
    const u32x4 q0 = p[0], q1 = p[64], q2 = p[128];
    tk.l0 = q0[0]; tk.l1 = q0[0] + 16384u; tk.lm = q0[1]; tk.s0 = q0[2]; tk.s1 = q0[3];
    pv.lo = q1[0]; pv.lm = q1[1]; pv.vr = q1[2]; pv.ma = q1[3]; pv.mz = q2[0]; pv.so = q2[1];
    pv.pa[0] = pv.pa[1] = pv.pb[0] = pv.pb[1] = (u32x4){0u, 0u, 0u, 0u};
    ot.t5f = q2[2]; ot.t5o = q2[3]; ot.sel = as8(p[192]);
  }
#pragma unroll 1
  for (int ti = 0; todo; todo >>= 12, ++ti) {
    const int ty = (int)todo & 3, sl = 56 + (((int)todo >> 2) & 7);
    const unsigned tm = tp1 + (((unsigned)todo >> 5) & 0x7fu) * 100u;
    if (ty == 1) {
      wait_cyc(tm);
      LOGD(3 * ti + 1);
      poll([&]() { tk.load(rs, ts_slot(ck, sl)); }, [&]() { return tk.ready(); });
      LOGD(3 * ti + 2);
      tk.run(j, lane, ot.sel); tk.store(rs, pt_slot(ck, sl));
    } else if (ty == 2) {
      const unsigned t0 = cyc32();
      if (sl >= NSEG - 2) { lds_wait(misc + 7, 4u * NSEG + 3u); wait_cyc(t0 + END_G2); }
      wait_cyc(tm);
      LOGD(3 * ti + 1);
      poll([&]() { pv.load(rs, pt_slot(ck, sl)); }, [&]() { return pv.ready(); });
      LOGD(3 * ti + 2);
      pv.run<true>(lds, sl % NV, lane, rs, op_slot(sl) + (unsigned)((m * 32 + gp) * 1280));
      if (sl == NSEG - 1) misc[14] = cyc32() | 1u;
    } else {
      if (sl >= NSEG - 4) { lds_wait(misc + 14, 1u); wait_cyc(__builtin_amdgcn_readfirstlane(misc[14]) + END_G3); }
      wait_cyc(tm);
      LOGD(3 * ti + 1);
      OutTask o2 = ot;
      poll([&]() { o2.load(a, rs, sl + 1, b); ot.load(a, rs, sl, b); }, [&]() { return ot.ready(b) & o2.ready(b); });
      LOGD(3 * ti + 2);
      o2.run(lane); o2.store(a, sl + 1, b, lane);
      ot.run(lane); ot.store(a, sl, b, lane);
    }
    LOGD(3 * ti + 3);
  }
  VM_DRAIN();
  if (w != 0) lds_inc<L_MISC + 64>(lds0);
  else {
    constexpr unsigned o2 = (unsigned)OFF_ACC + (unsigned)((NSEG - 1) % SR) * (unsigned)ACC_STEP;
    lds_wait(misc + 16, 3u);
    LOGD(10);
    if (lane == 0) __hip_atomic_fetch_add((gu64*)((GAS char*)(a.ws + o2) + acc_off(m * 4, 0)), (u64)1, RLX_AGENT);
    wait_until(now32() + SCH_DRAIN);
    wait_cyc(tp1 + END_E4);
    unsigned x;
    if (qkv_row(b) + 48 >= 55 && qkv_col(b) < 15) poll([&]() { x = __builtin_amdgcn_raw_buffer_load_b32(rs, acc_off(c * 4, 0), o2, 16); }, [&]() { return wave_all((x & 16u) != 0u); });
    lds_inc<L_MISC + 68>(lds0);
    LOGD(11);
  }
  if (qkv_row(b) + 48 >= 55 && qkv_col(b) < 15) {
    lds_wait(misc + 18, 8u);
    lds_spin<L_MISC + 68, 1>(lds0, 1u);
    x_fetch_n<10>(x_src(a, NSEG + 3, b), lds0, 3, 10 * w, lane * 16);
    VM_DRAIN();
    lds_inc<L_MISC + 68>(lds0);
  }
  LOGP(8);
}

__device__ __forceinline__ void proj_front(const RecArgs& a, char* lds, const __amdgpu_buffer_rsrc_t rs, const int v) {
  const int lane = __builtin_amdgcn_workitem_id_x() & 63, b = __builtin_amdgcn_workgroup_id_x(), c = lane & 15, g = lane >> 4;
  const int rb = (b & 7) + 8 * (b >> 5), cq = (b >> 3) & 3;
  const unsigned lds0 = (unsigned)(uintptr_t)(LAS char*)lds;
  unsigned* const cnt = (unsigned*)(lds + L_MISC) + 20;
  LOGF2(0);
  float* const red = (float*)(lds + L_RED1);
  float* const fin = red + 640;
  const f32x4 Z4 = {0.f, 0.f, 0.f, 0.f};
  x_fetch_n<8>((const u16*)(a.ws + OFF_OB) + (size_t)rb * 32 * DM, lds0, 0, 8 * v, lane * 16);
  bf16x8 W[2][20];
  const __amdgpu_buffer_rsrc_t rw = __builtin_amdgcn_make_buffer_rsrc(a.ws + OFF_WOBT + (size_t)(cq * 10 + 2 * v) * 20480, 0, 40960, 0x00020000);
  auto w_get = [&](int ks) {
#pragma unroll
    for (int i = 0; i < 2; ++i) W[i][ks] = as8(__builtin_amdgcn_raw_buffer_load_b128(rw, lane * 16 + (ks & 3) * 1024, (i * 20 + (ks & ~3)) * 1024, 0));
    __builtin_amdgcn_sched_barrier(0);
  };
#pragma unroll
  for (int ks = 0; ks < FW; ++ks) w_get(ks);
  const unsigned fo = (unsigned)(rb * 40960 + (cq * 5 + v) * 1024 + ((g >> 1) * 16 + c) * 16), f8 = fo + (g & 1) * 8, f16 = fo + (g & 1) * 20480;
  u32x2 xo[2][2];
#pragma unroll
  for (int k = 0; k < 4; ++k)
    xo[k >> 1][k & 1] = __builtin_bit_cast(u32x2, __hip_atomic_load((const gu64*)((const char*)a.xnew + f8 + (k >> 1) * 512 + (k & 1) * 20480), RLX_AGENT));
  asm volatile("s_waitcnt vmcnt(%0)" :: "n"(2 * FW + 4) : "memory");
  lds_inc<L_MISC + 80>(lds0);
  lds_wait(cnt, 5u);
  LOGF2(1);
  f32x4 acc[2][2] = {{Z4, Z4}, {Z4, Z4}};
  bf16x8 x[20][2];
#pragma unroll
  for (int ks = -3; ks < 20; ++ks) {
    if (ks >= 0 && ks + FW < 20) w_get(ks + FW);
#pragma unroll
    for (int t = 0; t < 2; ++t) {
      if (ks + 3 < 20) x[ks + 3][t] = *(const bf16x8*)(lds + L_XN + (t * 20 + ks + 3) * 1024 + lane * 16);
#pragma unroll
      for (int i = 0; i < 2 && ks >= 0; ++i) acc[i][t] = mfma32(W[i][ks < 0 ? 0 : ks], x[ks < 0 ? 0 : ks][t], acc[i][t]);
    }
    __builtin_amdgcn_sched_barrier(0);
  }
  u32x2 xp[2][2];
  PIN_V(acc[1][1]);
  LOGF2(2);
#pragma unroll
  for (int t = 0; t < 2; ++t) {
#pragma unroll
    for (int k = 0; k < 4; ++k) {
      const int i = k >> 1, q = k & 1;
      const unsigned r = pack2(acc[i][t][2 * q], acc[i][t][2 * q + 1]), x = xo[i][t][q];
      xp[i][t][q] = pack2(u2f(x << 16) + u2f(r << 16), u2f(x & 0xffff0000u) + u2f(r & 0xffff0000u));
      acc[i][t][2 * q] = u2f(xp[i][t][q] << 16); acc[i][t][2 * q + 1] = u2f(xp[i][t][q] & 0xffff0000u);
    }
    const f32x4 sm = acc[0][t] + acc[1][t];
    const float ml = x32sum(x16sum((sm[0] + sm[1]) + (sm[2] + sm[3]))) * (1.0f / 32.0f);
    float q = 0.f;
#pragma unroll
    for (int r = 0; r < 8; ++r) { const float d = acc[r >> 2][t][r & 3] - ml; q += d * d; }
    q = x32sum(x16sum(q));
    *(f32x2*)(red + (v * 32 + t * 16 + c) * 2) = (f32x2){ml, q};
  }
  lds_inc<L_MISC + 80>(lds0);
  if (v == 0) {
    lds_wait(cnt, 10u);
    LOGF2(3);
    float mw[5], ml = 0.f, m2 = 0.f;
#pragma unroll
    for (int i = 0; i < 5; ++i) { const f32x2 u = *(const f32x2*)(red + (i * 32 + (lane & 31)) * 2); mw[i] = u[0]; ml += u[0]; m2 += u[1]; }
    ml *= 0.2f;
#pragma unroll
    for (int i = 0; i < 5; ++i) m2 += 32.0f * (mw[i] - ml) * (mw[i] - ml);
    const unsigned xs = (unsigned)OFF_LNX + rb * 1024;
    if (lane < 32) SC1_STORE8S(((u32x2){f2u(ml), nz(f2u(m2))}), (cq * 32 + lane) * 8, xs);
    wait_cyc(cyc32() + FR_D);
    u32x2 x0, x1;
    for (unsigned spins = 0; spins < SPIN_CAP; ++spins) {
      x0 = __builtin_amdgcn_raw_buffer_load_b64(rs, lane * 8, xs, 16); x1 = __builtin_amdgcn_raw_buffer_load_b64(rs, 512 + lane * 8, xs, 16);
      if (wave_all(x0[1] != 0u && x1[1] != 0u)) break;
      asm volatile("" ::: "memory");
    }
    const float m0 = u2f(x0[0]), m1 = u2f(x1[0]);
    const float mean = x32sum(m0 + m1) * 0.25f;
    const float M2 = x32sum((u2f(x0[1]) + u2f(x1[1])) + 160.0f * ((m0 - mean) * (m0 - mean) + (m1 - mean) * (m1 - mean)));
    const float mb = rbf(mean), den = rbf(__builtin_sqrtf(rbf(rbf(M2 * (1.0f / DM)) + 1e-5f)));
    if (lane < 32) {
      fin[lane] = mb; fin[32 + lane] = 1.0f / den;
      if (cq == 0) __hip_atomic_store((GAS unsigned*)(a.ws + OFF_XNRM) + (rb * 32 + lane) * 4, f2u(1.02f * (M2 + DM * (mean - mb) * (mean - mb)) / (den * den)), RLX_AGENT);
    }
    lds_inc<L_MISC + 84>(lds0);
  }
  lds_wait(cnt + 1, 1u);
  LOGF2(4);
  const __amdgpu_buffer_rsrc_t rx = __builtin_amdgcn_make_buffer_rsrc(a.ws + OFF_XN, 0, (int)(OFF_QB - OFF_XN), 0x00020000);
  auto put = [&](const u32x2 p0, const u32x2 p1, unsigned so) {
    const auto r0 = __builtin_amdgcn_permlane16_swap(p0[0], p1[0], false, false), r1 = __builtin_amdgcn_permlane16_swap(p0[1], p1[1], false, false);
    __builtin_amdgcn_raw_buffer_store_b128((u32x4){r0[0], r1[0], r0[1], r1[1]}, rx, f16, so, 16);
  };
#pragma unroll
  for (int i = 0; i < 2; ++i) {
    u32x2 y[2];
#pragma unroll
    for (int t = 0; t < 2; ++t) {
      const float mb = fin[t * 16 + c], rden = fin[32 + t * 16 + c];
      y[t] = (u32x2){pack2(rbf(acc[i][t][0] - mb) * rden, rbf(acc[i][t][1] - mb) * rden), pack2(rbf(acc[i][t][2] - mb) * rden, rbf(acc[i][t][3] - mb) * rden)};
    }
    put(y[0], y[1], i * 512);
  }
  VM_DRAIN();
  lds_inc<L_MISC + 80>(lds0);
#pragma unroll
  for (int i = 0; i < 2; ++i) put(xp[i][0], xp[i][1], (unsigned)(OFF_XB - OFF_XN) + i * 512);
  if (v == 0) {
    lds_wait(cnt, 15u);
    if (rb < 4 && lane < 8) __hip_atomic_fetch_add((GAS unsigned*)(a.ws + OFF_RDY) + lane * 64, 1u, RLX_AGENT);
  }
  LOGF2(5);
}

__global__ __launch_bounds__(512, 2) void rec_kernel(RecArgs a) {
  __shared__ __attribute__((aligned(16))) char lds[L_BYTES];
  const int tid = __builtin_amdgcn_workitem_id_x(), wid = __builtin_amdgcn_readfirstlane(tid >> 6), w = wid & 3;
  const __amdgpu_buffer_rsrc_t rs = __builtin_amdgcn_make_buffer_rsrc(a.ws, 0, (int)COMM_BYTES, 0x00020000);
  if (tid < 32) ((float*)(lds + L_MISC))[tid] = (tid == 4 || tid == 5) ? 1.0f : 0.0f;
  BAR();
  if (a.layer && wid < 5) proj_front(a, lds, rs, wid);
  if (wid < 4) {
    unsigned tlast;
    if (w == 0) rec_waves<0>(a, lds, rs, tlast);
    else if (w == 1) rec_waves<1>(a, lds, rs, tlast);
    else if (w == 2) rec_waves<2>(a, lds, rs, tlast);
    else rec_waves<3>(a, lds, rs, tlast);
    drain(a, lds, rs, w, tlast);
  } else {
    unsigned vo[2][2];
    if (w == 0) token_waves<0>(a, lds, rs, vo);
    else if (w == 1) token_waves<1>(a, lds, rs, vo);
    else if (w == 2) token_waves<2>(a, lds, rs, vo);
    else token_waves<3>(a, lds, rs, vo);
    qkv_tail(a, lds, w, vo);
  }
}

#if defined(__HIP_DEVICE_COMPILE__)
#pragma clang attribute pop
#endif

constexpr int KQ = S_ / 4;
#define ATT_P1 6
#define ATT_P2 10
#define ATT_P3 14
#define ATT_THR 6.0f
#define ATT_NQ 4
#define ATT_LMAX 1.0e12f
#if defined(__HIP_DEVICE_COMPILE__)
#pragma clang attribute push(__attribute__((target("no-packed-fp32-ops"))), apply_to = function)
#endif
__global__ __launch_bounds__(256 * ATT_NQ, 4) void attb_kernel(const u16* __restrict__ Qp, const u16* __restrict__ KV, u16* __restrict__ O) {
  __shared__ f32x4 mo_[ATT_NQ][4][2][3][64];
  __shared__ float mm_[ATT_NQ][4][2][64];
  const int tid = __builtin_amdgcn_workitem_id_x(), bid = __builtin_amdgcn_workgroup_id_x();
  __shared__ unsigned sink[64];
  own_code<5000>(tid, sink);
  const int lane = tid & 63, wid = __builtin_amdgcn_readfirstlane(tid >> 6), kh = wid / ATT_NQ, r = lane & 31, hh = lane >> 5;
  const int hd = (bid & 7) * 4 + ((bid >> 3) & 3), qb = (bid >> 5) * ATT_NQ + wid % ATT_NQ;
  const int q0 = qb * 64;
  bf16x8 qa[2]; u32x2 qb2[2];
#pragma unroll
  for (int qt = 0; qt < 2; ++qt) {
    { const u32x2* qp = (const u32x2*)(Qp + ((size_t)hd * S_ + q0 + qt * 32 + r) * QST + 8 * hh); const u32x2 q0_ = qp[0], q1_ = qp[1]; qa[qt] = mk8(q0_[0], q0_[1], q1_[0], q1_[1]); }
    const u32x2 t = *(const u32x2*)(Qp + ((size_t)hd * S_ + q0 + qt * 32 + r) * QST + 16);
    qb2[qt] = t;
  }
  const __amdgpu_buffer_rsrc_t rk = __builtin_amdgcn_make_buffer_rsrc((void*)(KV + ((size_t)hd * 64 + kh * 16) * (KVT / 2)), 0, 16 * KVT, 0x00020000);
  unsigned ok1 = lane * 16, ok2 = hh ? 1600 : 1024 + r * 8, ov = r > 20 ? 1u << 30 : 1280 + (hh * 21 + r) * 16;
  f32x16 o[2];
  float m[2];
  const f32x16 Z16 = {0.f, 0.f, 0.f, 0.f, 0.f, 0.f, 0.f, 0.f, 0.f, 0.f, 0.f, 0.f, 0.f, 0.f, 0.f, 0.f};
  int odd = 0xDDDD;
  asm volatile("" : "+v"(odd));
  struct KvT { u32x4 k1, v0, v1; u32x2 k2; };
  auto fetch_k = [&](KvT& x, int t) {
    x.k1 = __builtin_amdgcn_raw_buffer_load_b128(rk, ok1, imin(t, KQ / 32 - 1) * KVT, 0); x.k2 = __builtin_amdgcn_raw_buffer_load_b64(rk, ok2, imin(t, KQ / 32 - 1) * KVT, 0);
    __builtin_amdgcn_sched_barrier(0);
  };
  auto fetch_v = [&](KvT& x, int t) {
    x.v0 = __builtin_amdgcn_raw_buffer_load_b128(rk, ov, imin(t, KQ / 32 - 1) * KVT, 0); x.v1 = __builtin_amdgcn_raw_buffer_load_b128(rk, ov + 672, imin(t, KQ / 32 - 1) * KVT, 0);
    __builtin_amdgcn_sched_barrier(0);
  };
  auto fetch = [&](int t) { KvT x; __builtin_amdgcn_sched_barrier(0); fetch_k(x, t); fetch_v(x, t); return x; };
  auto qk1 = [&](const KvT& x, int qt) { return __builtin_amdgcn_mfma_f32_32x32x16_bf16(as8(x.k1), qa[qt], Z16, 0, 0, 0); };
  auto qk2 = [&](const KvT& x, int qt, f32x16 c) { return __builtin_amdgcn_mfma_f32_32x32x8bf16_1k(__builtin_bit_cast(v4i16_t, x.k2), __builtin_bit_cast(v4i16_t, qb2[qt]), c, 0, 0, 0); };
  auto pv = [&](const u32x4 v, const f32x16 p, int hi, f32x16 c) {
    const f32x8 ph = hi ? __builtin_shufflevector(p, p, 8, 9, 10, 11, 12, 13, 14, 15) : __builtin_shufflevector(p, p, 0, 1, 2, 3, 4, 5, 6, 7);
    return __builtin_amdgcn_smfmac_f32_32x32x32_bf16(__builtin_bit_cast(hbf16x8, v), __builtin_bit_cast(hbf16x16, ph), c, odd, 0, 0);
  };
  auto tile = [&](const KvT& x, auto MODE) {
    f32x16 sa[2];
#pragma unroll
    for (int qt = 0; qt < 2; ++qt) sa[qt] = qk1(x, qt);
#pragma unroll
    for (int qt = 0; qt < 2; ++qt) sa[qt] = qk2(x, qt, sa[qt]);
#pragma unroll
    for (int qt = 0; qt < 2; ++qt) {
      if (MODE != 0) {
        float mx = fmx(fmx(sa[qt][0], sa[qt][1]), sa[qt][2]);
#pragma unroll
        for (int i = 3; i < 15; i += 2) mx = fmx(fmx(mx, sa[qt][i]), sa[qt][i + 1]);
        mx = fmx(mx, sa[qt][15]);
        if (MODE == 1 || !wave_all(mx <= ATT_THR)) {
          mx = x32mx(mx);
          if (MODE != 1) mx = fmx(mx, 0.f);
          const float mn = m[qt] + mx;
          const unsigned h1 = f2bf(mn); const float r1 = mn - bf2f(h1);
          const unsigned h2 = f2bf(r1); const float r2 = r1 - bf2f(h2);
          const unsigned h3 = f2bf(r2);
          const float me = (bf2f(h1) + bf2f(h2)) + bf2f(h3);
          const float dm = me - m[qt];
          m[qt] = me;
          if (hh) qb2[qt] = (u32x2){(h1 | (h2 << 16)) ^ 0x80008000u, h3 ^ 0x8000u};
          const float alpha = __builtin_amdgcn_exp2f(-dm);
#pragma unroll
          for (int i = 0; i < 16; ++i) { if (MODE != 1) o[qt][i] *= alpha; sa[qt][i] -= dm; }
        }
      }
#pragma unroll
      for (int i = 0; i < 16; ++i) sa[qt][i] = __builtin_amdgcn_exp2f(sa[qt][i]);
      asm volatile("" : "+v"(sa[qt]));
      if (MODE == 1) { o[qt] = Z16; asm volatile("" : "+v"(o[qt])); }
      o[qt] = pv(x.v1, sa[qt], 1, pv(x.v0, sa[qt], 0, o[qt]));
      __builtin_amdgcn_sched_barrier(0);
    }
  };
  typedef std::integral_constant<int, 1> I1; typedef std::integral_constant<int, 2> I2;
  auto reset = [&]() {
    asm volatile("" : "+v"(ok1), "+v"(ok2), "+v"(ov) :: "memory");
    m[0] = m[1] = 0.f;
    if (hh) qb2[0] = qb2[1] = (u32x2){0u, 0u};
  };
  auto slow = [&]() {
    reset();
    tile(fetch(0), I1{});
#pragma unroll 1
    for (int t = 1; t < KQ / 32; ++t) tile(fetch(t), I2{});
  };
  f32x16 sc[2];
  auto phase = [&](int qt, const u32x4 vp, const KvT& xk, const u32x4 v0, auto mid) {
    auto ex = [&](int i0, int i1) {
#pragma unroll
      for (int i = i0; i < i1; ++i) sc[qt][i] = __builtin_amdgcn_exp2f(sc[qt][i]);
      asm volatile("" : "+v"(sc[qt]));
      __builtin_amdgcn_sched_barrier(0);
    };
    o[1 - qt] = pv(vp, sc[1 - qt], 1, o[1 - qt]);
    __builtin_amdgcn_sched_barrier(0);
    ex(0, 3);
    sc[1 - qt] = qk1(xk, 1 - qt);
    __builtin_amdgcn_sched_barrier(0);
    ex(3, 6);
    sc[1 - qt] = qk2(xk, 1 - qt, sc[1 - qt]);
    __builtin_amdgcn_sched_barrier(0);
    ex(6, 10);
    o[qt] = pv(v0, sc[qt], 0, o[qt]);
    __builtin_amdgcn_sched_barrier(0);
    mid();
    ex(10, 16);
  };
  auto fast = [&]() {
    reset();
    KvT xa = fetch(0), xb = fetch(1);
    tile(xa, I1{});
    fetch_k(xa, 2);
    sc[0] = qk2(xb, 0, qk1(xb, 0)); sc[1] = Z16;
    asm volatile("" : "+v"(sc[1]));
    auto step = [&](KvT& x, KvT& y, int t) {
      phase(0, y.v1, x, x.v0, [&]() { fetch_v(y, t + 1); fetch_k(x, t + 2); });
      phase(1, x.v1, y, x.v0, []() {});
    };
    step(xb, xa, 1);
#pragma unroll 1
    for (int t = 2; t < KQ / 32; t += 2) {
      if (t == ATT_P1) __builtin_amdgcn_s_setprio(2);
      if (t == ATT_P2) __builtin_amdgcn_s_setprio(1);
      if (t == ATT_P3) __builtin_amdgcn_s_setprio(0);
      step(xa, xb, t);
      step(xb, xa, t + 1);
    }
    o[1] = pv(xb.v1, sc[1], 1, o[1]);
  };
  if (ATT_P1) __builtin_amdgcn_s_setprio(3);
  fast();
  {
    float big = 0.f;
#pragma unroll
    for (int qt = 0; qt < 2; ++qt) {
      big = fmx(big, hh ? o[qt][8] * (3.0e38f / ATT_LMAX) : __builtin_fabsf(o[qt][8]));
#pragma unroll
      for (int i = 0; i < 12; ++i) if (i != 8) big = fmx(big, (i > 8 && hh) ? 0.f : __builtin_fabsf(o[qt][i]));
    }
    if (__builtin_expect(!wave_all(big <= 3.0e38f), 0)) slow();
  }
  f32x4 (*mo)[2][3][64] = mo_[wid % ATT_NQ];
  float (*mm)[2][64] = mm_[wid % ATT_NQ];
#pragma unroll
  for (int qt = 0; qt < 2; ++qt) {
#pragma unroll
    for (int k = 0; k < 3; ++k) mo[kh][qt][k][lane] = (f32x4){o[qt][4 * k], o[qt][4 * k + 1], o[qt][4 * k + 2], o[qt][4 * k + 3]};
    mm[kh][qt][lane] = m[qt];
  }
  const int qt = kh & 1, grp = kh >> 1, row = q0 + qt * 32 + r, k0 = hd * BD + 4 * hh + 8 * grp;
  auto oaddr = [&](int k) { return O + (((size_t)(row >> 4) * 20 + (k >> 5)) * 64 + ((k & 31) >> 3) * 16 + (row & 15)) * 8 + (k & 7); };
  u16 *op0 = oaddr(k0), *op1 = oaddr(k0 + 16);
  asm volatile("" : "+v"(op0), "+v"(op1));
  asm volatile("s_waitcnt lgkmcnt(0)\n\ts_barrier" ::: "memory");
  {
    float a[4], l[4];
    f32x4 u[4], x[4];
#pragma unroll
    for (int i = 0; i < 4; ++i) { a[i] = mm[i][qt][lane]; l[i] = mo[i][qt][2][lane | 32][0]; u[i] = mo[i][qt][grp][lane]; x[i] = mo[i][qt][2][lane]; }
    const float mn = fmx(fmx(a[0], a[1]), fmx(a[2], a[3]));
#pragma unroll
    for (int i = 0; i < 4; ++i) a[i] = __builtin_amdgcn_exp2f(a[i] - mn);
    const float inv = __builtin_amdgcn_rcpf((l[0] * a[0] + l[1] * a[1]) + (l[2] * a[2] + l[3] * a[3]));
    const f32x4 v = ((u[0] * a[0] + u[1] * a[1]) + (u[2] * a[2] + u[3] * a[3])) * inv;
    auto put = [&](u16* p, const f32x4 z) {
      const u32x2 t = {pack2(z[0], z[1]), pack2(z[2], z[3])};
      if (O_WT) __hip_atomic_store((gu64*)p, __builtin_bit_cast(u64, t), RLX_AGENT); else *(GAS u32x2*)p = t;
    };
    put(op0, v);
    if (grp == 0) {
      const f32x4 y = ((x[0] * a[0] + x[1] * a[1]) + (x[2] * a[2] + x[3] * a[3])) * inv;
      if (hh == 0) put(op1, y);
    }
  }
}
#if defined(__HIP_DEVICE_COMPILE__)
#pragma clang attribute pop
#endif

__global__ __launch_bounds__(640, 3) void proj_kernel(const u16* __restrict__ Of, const u16* __restrict__ Wf, const u16* __restrict__ xold,
                                                      u16* __restrict__ xnew) {
  __shared__ __attribute__((aligned(16))) char ob[40960];
  const int tid = threadIdx.x, lane = tid & 63, w = __builtin_amdgcn_readfirstlane(tid >> 6), g = lane >> 4, c = lane & 15;
  __shared__ unsigned sink[64];
  own_code<1500>(tid, sink);
  const int rb = (blockIdx.x & 7) + 8 * (blockIdx.x >> 5), cb = (blockIdx.x >> 3) & 3;
  const int col = cb * 160 + w * 16 + 4 * g;
  const u16* wp = Wf + (size_t)(cb * 10 + w) * 20 * 512 + lane * 8;
  u32x4 st[4];
#pragma unroll
  for (int i = 0; i < 4; ++i) st[i] = *(const u32x4*)(Of + (size_t)rb * 40 * 512 + (i * 640 + tid) * 8);
  bf16x8 wf[20];
#pragma unroll
  for (int ks = 0; ks < 20; ++ks) wf[ks] = *(const bf16x8*)(wp + (size_t)ks * 512);
  u32x2 xo[2];
#pragma unroll
  for (int t = 0; t < 2; ++t) xo[t] = *(const u32x2*)(xold + (((size_t)(rb * 2 + t) * 20 + (col >> 5)) * 64 + ((col & 31) >> 3) * 16 + c) * 8 + (col & 7));
#pragma unroll
  for (int i = 0; i < 4; ++i) *(u32x4*)(ob + (i * 640 + tid) * 16) = st[i];
  BAR();
  f32x4 acc[2] = {{0.f, 0.f, 0.f, 0.f}, {0.f, 0.f, 0.f, 0.f}};
#pragma unroll
  for (int ks = 0; ks < 20; ++ks)
#pragma unroll
    for (int t = 0; t < 2; ++t) acc[t] = mfma32(wf[ks], *(const bf16x8*)(ob + (t * 20 + ks) * 1024 + lane * 16), acc[t]);
#pragma unroll
  for (int t = 0; t < 2; ++t) {
    const unsigned r01 = pack2(acc[t][0], acc[t][1]), r23 = pack2(acc[t][2], acc[t][3]);
    const unsigned p01 = pack2(u2f(xo[t][0] << 16) + u2f(r01 << 16), u2f(xo[t][0] & 0xffff0000u) + u2f(r01 & 0xffff0000u));
    const unsigned p23 = pack2(u2f(xo[t][1] << 16) + u2f(r23 << 16), u2f(xo[t][1] & 0xffff0000u) + u2f(r23 & 0xffff0000u));
    if (P_WT) __hip_atomic_store((gu64*)(xnew + (size_t)(rb * 32 + t * 16 + c) * DM + col), (u64)p01 | (u64)p23 << 32, RLX_AGENT);
    else *(u32x2*)(xnew + (size_t)(rb * 32 + t * 16 + c) * DM + col) = (u32x2){p01, p23};
  }
}

extern "C" void kernel_launch(void* const* d_in, const int* in_sizes, int n_in, void* d_out, int out_size, void* d_ws,
                              size_t ws_size, hipStream_t stream) {
  const u16* A_x = (const u16*)d_in[0];
  const u16* A_mem0 = (const u16*)d_in[1];
  const u16* A_Wq = (const u16*)d_in[2];
  const u16* A_Wk = (const u16*)d_in[3];
  const u16* A_Wv = (const u16*)d_in[4];
  const u16* A_Wo = (const u16*)d_in[5];
  const u16* B_wqkv = (const u16*)d_in[8];
  const u16* B_wo = (const u16*)d_in[9];
  char* ws = (char*)(((uintptr_t)d_ws + ((size_t)WS_OFF << 20) + WS_AL - 1) & ~(uintptr_t)(WS_AL - 1));
  u16* xa = (u16*)(ws + OFF_XA); u16* Ob = (u16*)(ws + OFF_OB);

  prologue_kernel<<<256, 640, 0, stream>>>(ProArgs{A_x, A_Wq, A_Wk, A_Wv, A_Wo, B_wqkv, B_wo, ws});
  for (int layer = 0; layer < 2; ++layer) {
    rec_kernel<<<256, 512, 0, stream>>>(RecArgs{A_mem0, A_Wq, A_Wk, (const u16*)(ws + OFF_XB), xa, ws, (unsigned)layer, 0u});
    attb_kernel<<<BH * 32 / ATT_NQ, 256 * ATT_NQ, 0, stream>>>((const u16*)(ws + OFF_QB), (const u16*)(ws + OFF_KV), Ob);
  }
  proj_kernel<<<256, 640, 0, stream>>>(Ob, (const u16*)(ws + OFF_WOBT), xa, (u16*)d_out);
}
```

```cpp
#include <hip/hip_runtime.h>
#include <hip/hip_bf16.h>
#include <stdint.h>
#include <type_traits>

typedef unsigned short u16;
typedef unsigned long long u64;
typedef __attribute__((ext_vector_type(8))) short bf16x8;
typedef __attribute__((ext_vector_type(16))) short bf16x16;
typedef __attribute__((ext_vector_type(4))) float f32x4;
typedef __attribute__((ext_vector_type(2))) float f32x2;
typedef __attribute__((ext_vector_type(16))) float f32x16;
typedef __attribute__((ext_vector_type(8))) float f32x8;
typedef __attribute__((ext_vector_type(4))) unsigned u32x4;
typedef __attribute__((ext_vector_type(2))) unsigned u32x2;
typedef __attribute__((ext_vector_type(3))) unsigned u32x3;
typedef __attribute__((ext_vector_type(8))) unsigned u32x8;
typedef __attribute__((ext_vector_type(16))) unsigned u32x16;
typedef __attribute__((ext_vector_type(32))) unsigned u32x32;
#define GAS __attribute__((address_space(1)))
#define LAS __attribute__((address_space(3)))
typedef GAS unsigned long long gu64;
#define BAR() do { __builtin_amdgcn_s_waitcnt(0xC07F); asm volatile("s_waitcnt lgkmcnt(0)\n\ts_barrier" ::: "memory"); } while (0)

constexpr int S_ = 2048, DM = 640;
constexpr int NH = 32, DH = 192, HD = NH * DH;
constexpr int MEM = 8, SEG = 32, NSEG = S_ / SEG;
constexpr int BH = 32, BD = 20;

constexpr size_t al256(size_t x) { return (x + 255) & ~(size_t)255; }
constexpr int LAG3 = 2, LAG4 = 3, LAG5 = 6;
#define SR_ 8
constexpr int SR = SR_;
constexpr unsigned SCT_STEP = 131072, OFF_SCM = SR * SCT_STEP, SCM_STEP = 32768;
constexpr int ACC_NB = 192;
constexpr size_t ACC_STEP = 65536;
constexpr unsigned ACC_TS = ACC_NB * 256;
constexpr size_t ACC_LAYER = SR * ACC_STEP;
constexpr size_t OFF_ACC = 3 * 524288;
static_assert(SR == 8, "regions");
__device__ __forceinline__ unsigned ts_piece(int i) { return ACC_TS + (unsigned)(i < 128 ? (i >> 1) * 256 + (i & 1) * 16 : 128 + (i & 1) * 8192); }
constexpr size_t OFF_LNX = OFF_SCM + SR * SCM_STEP;
constexpr size_t OFF_RDY = OFF_LNX + 65536;
constexpr size_t PG = 2097152;
#define RING_ 4
constexpr int RING = RING_;
constexpr size_t TS_SLOT = (size_t)NH * 8 * 2560, PT_SLOT = (size_t)NH * 2560, OP_SLOT = (size_t)256 * 2560;
__device__ __forceinline__ unsigned ts_tok(int h, int j) { return (unsigned)((h & 15) * 32768 + (j >> 2) * 16384 + (j & 3) * 2048); }
__device__ __forceinline__ unsigned ts_mem(int h, int j) { return (unsigned)(524288 + ((h & 15) >> 1) * 16384 + (h & 1) * 4096 + j * 512); }
__device__ __forceinline__ unsigned pt_tok(int h) { return (unsigned)(((h & 15) >> 2) * 16384 + (h & 3) * 2048); }
__device__ __forceinline__ unsigned pt_mem(int h) { return (unsigned)(65536 + (h & 15) * 512); }
constexpr size_t OFF_X = 2 * PG + TS_SLOT + 3 * PT_SLOT, X_BYTES = 7 * OP_SLOT + 4 * PT_SLOT;
constexpr size_t OFF_OP = OFF_X + TS_SLOT + 4 * PT_SLOT;
constexpr size_t COMM_BYTES = OFF_X + X_BYTES + RING * OP_SLOT;
__device__ __forceinline__ unsigned near_half(unsigned ck, unsigned o) { return o + ((ck >> (o >= 2 * PG ? 8 : 4)) & 8192u); }
__device__ __forceinline__ unsigned ts_slot(unsigned ck, int sl) {
  return near_half(ck, sl == NSEG - 1 ? (unsigned)OFF_X : ((sl & 3) == 3 ? (unsigned)(2 * PG) : (unsigned)PG + (unsigned)(sl & 3) * (unsigned)TS_SLOT));
}
__device__ __forceinline__ unsigned pt_slot(unsigned ck, int sl) {
  return near_half(ck, sl >= NSEG - 4 ? (unsigned)(OFF_X + TS_SLOT) + (unsigned)(sl - (NSEG - 4)) * (unsigned)PT_SLOT
                                      : ((sl & 3) == 0 ? (unsigned)(PG + 3 * TS_SLOT) : (unsigned)(2 * PG + TS_SLOT - PT_SLOT) + (unsigned)(sl & 3) * (unsigned)PT_SLOT));
}
__device__ __forceinline__ unsigned op_slot(int sl) { return (unsigned)OFF_OP + (unsigned)(sl >= NSEG - 6 ? sl - (NSEG - 6) : 6 + sl % RING) * (unsigned)OP_SLOT; }
static_assert(RING == 4 && OFF_OP <= 3 * PG && OFF_RDY + 2048 <= OFF_ACC && OFF_ACC + ACC_LAYER == PG && TS_SLOT % 16384 == 0 && PT_SLOT % 16384 == 0, "pages");
static_assert(TS_SLOT == OP_SLOT && RING >= 4 && NSEG % SR == 0 && SR >= 5 && X_BYTES % 4096 == 0 && X_BYTES <= 256 * 20 * 1024, "slots");
constexpr size_t OFF_WB = (COMM_BYTES + 16383) / 16384 * 16384, WB_BYTES = (size_t)256 * 200 * 1024;
__device__ __forceinline__ unsigned wb_off(int F) { const unsigned o = (unsigned)((F >> 3) * 16384 + (F & 7) * 1024); return o | ((((unsigned)OFF_WB + o) >> 8) & 8192u); }
constexpr size_t OFF_PHI = OFF_WB + WB_BYTES;
constexpr size_t OFF_BNRM = OFF_PHI + 1024;
constexpr size_t OFF_LAT = OFF_BNRM + 1024;
constexpr size_t OFF_XNRM = OFF_LAT + 2048;
constexpr size_t OFF_WQKVBT = OFF_XNRM + al256(S_ * 16);
constexpr size_t OFF_WOBT = OFF_WQKVBT + al256((size_t)3 * DM * DM * 2);
constexpr size_t OFF_XN = OFF_WOBT + al256((size_t)DM * DM * 2);
constexpr int QST = BD;
constexpr size_t OFF_XA = OFF_XN + al256((size_t)S_ * DM * 2);
constexpr size_t OFF_XB = OFF_XA + al256((size_t)S_ * DM * 2);
constexpr size_t OFF_QB = OFF_XB + al256((size_t)S_ * DM * 2);
constexpr int KVT = 2624;
constexpr size_t OFF_KV = OFF_QB + al256((size_t)BH * S_ * QST * 2);
constexpr size_t OFF_OB = OFF_KV + al256((size_t)BH * 64 * KVT);
constexpr size_t OFF_DBG = OFF_OB + al256((size_t)S_ * DM * 2);
constexpr size_t WS_TOTAL = OFF_DBG + (size_t)2 * 64 * 8 * DM * 2;
#define WS_AL 2097152
#define WS_OFF 0
static_assert(WS_TOTAL + WS_AL + ((size_t)WS_OFF << 20) <= ((size_t)256 << 20), "workspace budget");

#if defined(__HIP_DEVICE_COMPILE__)
#pragma clang attribute push(__attribute__((target("no-packed-fp32-ops"))), apply_to = function)
#endif
__device__ __forceinline__ float u2f(unsigned v) { return __builtin_bit_cast(float, v); }
__device__ __forceinline__ unsigned f2u(float v) { return __builtin_bit_cast(unsigned, v); }
__device__ __forceinline__ float bf2f(unsigned v) { return __builtin_bit_cast(float, v << 16); }
__device__ __forceinline__ unsigned f2bf(float f) { __bf16 b = (__bf16)f; return (unsigned)__builtin_bit_cast(u16, b); }
__device__ __forceinline__ float rbf(float f) { return bf2f(f2bf(f)); }
typedef __attribute__((ext_vector_type(2))) __bf16 hbf16x2;
__device__ __forceinline__ unsigned pack2(float lo, float hi) { f32x2 v = {lo, hi}; hbf16x2 b = __builtin_convertvector(v, hbf16x2); return __builtin_bit_cast(unsigned, b); }
__device__ __forceinline__ f32x4 mfma32(bf16x8 a, bf16x8 b, f32x4 c) { return __builtin_amdgcn_mfma_f32_16x16x32_bf16(a, b, c, 0, 0, 0); }
typedef __attribute__((ext_vector_type(8))) __bf16 hbf16x8;
typedef __attribute__((ext_vector_type(16))) __bf16 hbf16x16;
__device__ __forceinline__ f32x4 smfma64(bf16x8 a, bf16x16 b, f32x4 c, int idx) {
  return __builtin_amdgcn_smfmac_f32_16x16x64_bf16(__builtin_bit_cast(hbf16x8, a), __builtin_bit_cast(hbf16x16, b), c, idx, 0, 0);
}
__device__ __forceinline__ bf16x16 cat16(bf16x8 a, bf16x8 b) { return __builtin_shufflevector(a, b, 0, 1, 2, 3, 4, 5, 6, 7, 8, 9, 10, 11, 12, 13, 14, 15); }
__device__ __forceinline__ bf16x8 half8(bf16x16 v, int hi) { return hi ? __builtin_shufflevector(v, v, 8, 9, 10, 11, 12, 13, 14, 15) : __builtin_shufflevector(v, v, 0, 1, 2, 3, 4, 5, 6, 7); }
__device__ __forceinline__ bf16x8 mk8(unsigned a, unsigned b, unsigned c, unsigned d) { u32x4 v = {a, b, c, d}; return __builtin_bit_cast(bf16x8, v); }
__device__ __forceinline__ unsigned dpp_ror8(unsigned v) { return (unsigned)__builtin_amdgcn_mov_dpp((int)v, 0x128, 0xf, 0xf, true); }
__device__ __forceinline__ float fmx(float a, float b) { return __builtin_elementwise_maximum(a, b); }

__device__ __forceinline__ int imax(int a, int b) { return a > b ? a : b; }
__device__ __forceinline__ int imin(int a, int b) { return a < b ? a : b; }
__device__ __forceinline__ unsigned umin(unsigned a, unsigned b) { return a < b ? a : b; }
__device__ __forceinline__ unsigned umax(unsigned a, unsigned b) { return a > b ? a : b; }
__device__ __forceinline__ bool wave_all(bool p) { return __builtin_amdgcn_ballot_w64(!p) == 0ull; }
__device__ __forceinline__ float x16mx(float x) { unsigned u = __builtin_bit_cast(unsigned, x); auto r = __builtin_amdgcn_permlane16_swap(u, u, false, false); return fmx(u2f(r[0]), u2f(r[1])); }
__device__ __forceinline__ float x32mx(float x) { unsigned u = __builtin_bit_cast(unsigned, x); auto r = __builtin_amdgcn_permlane32_swap(u, u, false, false); return fmx(u2f(r[0]), u2f(r[1])); }
__device__ __forceinline__ float x16sum(float x) { unsigned u = __builtin_bit_cast(unsigned, x); auto r = __builtin_amdgcn_permlane16_swap(u, u, false, false); return u2f(r[0]) + u2f(r[1]); }
__device__ __forceinline__ float x32sum(float x) { unsigned u = __builtin_bit_cast(unsigned, x); auto r = __builtin_amdgcn_permlane32_swap(u, u, false, false); return u2f(r[0]) + u2f(r[1]); }
template <int CTRL> __device__ __forceinline__ float dppf(float v) { return u2f((unsigned)__builtin_amdgcn_mov_dpp((int)f2u(v), CTRL, 0xf, 0xf, true)); }
#define OWN_PF 1
template <int BYTES> __device__ __forceinline__ void own_code(int tid, unsigned* sink) {
  if (!OWN_PF || __builtin_amdgcn_readfirstlane(tid >> 6) * 4096 >= BYTES) return;
  const char* pc; unsigned keep;
  asm volatile("s_getpc_b64 %0" : "=s"(pc));
  asm volatile("s_mov_b32 %0, m0\n\ts_mov_b32 m0, %3\n\ts_nop 0\n\tglobal_load_lds_dword %1, %2\n\ts_mov_b32 m0, %0"
               : "=&s"(keep) : "v"(tid * 64 < BYTES ? tid * 64 : 0), "s"(pc), "s"((unsigned)(uintptr_t)(LAS unsigned*)sink) : "memory");
}
constexpr int DPP_XOR1 = 0xB1, DPP_XOR2 = 0x4E, DPP_ROR4 = 0x124, DPP_ROR8 = 0x128;
#if defined(__HIP_DEVICE_COMPILE__)
#pragma clang attribute pop
#endif

typedef short v4i16_t __attribute__((ext_vector_type(4)));
__device__ __forceinline__ bf16x8 tr_frag(const char* lo_, const char* hi_) {
  const v4i16_t lo = __builtin_amdgcn_ds_read_tr16_b64_v4i16((LAS v4i16_t*)lo_), hi = __builtin_amdgcn_ds_read_tr16_b64_v4i16((LAS v4i16_t*)hi_);
  return (bf16x8){lo[0], lo[1], lo[2], lo[3], hi[0], hi[1], hi[2], hi[3]};
}
__device__ __forceinline__ int rec_wg(int gp, int m) { return (((gp & 1) * 16 + m) << 3) | (gp >> 1); }
__device__ __forceinline__ float wsum(float v) {
  v += dppf<DPP_XOR1>(v); v += dppf<DPP_XOR2>(v); v += dppf<DPP_ROR4>(v); v += dppf<DPP_ROR8>(v);
  return x32sum(x16sum(v));
}
__device__ __forceinline__ float ln_row(int lane, u32x4 (&xo)[2]) {
  const bool two = lane < 16;
  float v[16];
#pragma unroll
  for (int hf = 0; hf < 2; ++hf)
#pragma unroll
    for (int i = 0; i < 4; ++i) { const unsigned u = xo[hf][i]; v[hf * 8 + 2 * i] = bf2f(u & 0xffff); v[hf * 8 + 2 * i + 1] = bf2f(u >> 16); }
  float s0 = 0.f, s1 = 0.f;
#pragma unroll
  for (int i = 0; i < 8; ++i) { s0 += v[i]; s1 += v[8 + i]; }
  const float mean = wsum(two ? s0 + s1 : s0) * (1.0f / DM);
  s0 = s1 = 0.f;
#pragma unroll
  for (int i = 0; i < 8; ++i) { const float d0 = v[i] - mean, d1 = v[8 + i] - mean; s0 += d0 * d0; s1 += d1 * d1; }
  const float var = rbf(wsum(two ? s0 + s1 : s0) * (1.0f / DM));
  const float mb = rbf(mean);
  const float rden = 1.0f / rbf(sqrtf(rbf(var + 1e-5f)));
  float n2[2] = {0.f, 0.f};
#pragma unroll
  for (int hf = 0; hf < 2; ++hf) {
    float y[8];
#pragma unroll
    for (int i = 0; i < 8; ++i) { y[i] = rbf(rbf(v[hf * 8 + i] - mb) * rden); n2[hf] += y[i] * y[i]; }
    xo[hf] = (u32x4){pack2(y[0], y[1]), pack2(y[2], y[3]), pack2(y[4], y[5]), pack2(y[6], y[7])};
  }
  return wsum(two ? n2[0] + n2[1] : n2[0]);
}

#define PROT(k)
#define WARM_CODE 1
#define B_AUX 16
#define X_AUX 16
#define T_AUX 16
#define WB_NEAR 1
#define PF_POS 0
#define PRB_N0 3
#define PRB_N1 0
#define PRB_LOG(i, v)
#define PLD(p) __builtin_nontemporal_load((const u32x4*)(p))
struct ProArgs { const u16* x; const u16* wq; const u16* wk; const u16* wv; const u16* wo; const u16* wqkvb; const u16* wob; char* ws; };
__global__ __launch_bounds__(640, 3) void prologue_kernel(ProArgs a) {
  __shared__ __attribute__((aligned(16))) char img[192 * 320];
  __shared__ __attribute__((aligned(16))) char slots[10 * 6144];
  __shared__ float red[10][160];
  __shared__ unsigned latw[10];
  __shared__ u32x4 xst[2][640];
  const int b = blockIdx.x, tid = threadIdx.x, lane = tid & 63, v = __builtin_amdgcn_readfirstlane(tid >> 6), c = lane & 15, g = lane >> 4;
  const int h = (b & 7) * 4 + ((b >> 3) & 3), dh = (b >> 5) & 1, cq = b >> 6, ks = dh * 10 + v;
  char* const slot = slots + v * 6144;
  PROT(0);
  __shared__ unsigned sink[64];
  own_code<9600>(tid, sink);
  const unsigned zo = v < 8 ? (unsigned)(((b >> 6) * 512 + ((b & 7) * 2 + (v >> 2)) * 32 + ((b >> 3) & 7) * 4 + (v & 3)) * 1024) : (unsigned)(((v - 7) * 2048 + (b & 7) * 32 + ((b >> 3) & 7) * 4 + (b >> 6)) * 1024);
  __builtin_amdgcn_raw_buffer_store_b128((u32x4){0u, 0u, 0u, 0u}, __builtin_amdgcn_make_buffer_rsrc(a.ws, 0, (int)(3 * PG), 0x00020000), zo + lane * 16, 0, 16);
  __builtin_amdgcn_sched_barrier(0);
  u32x4 wa[2][6], wb[6], tr[4], xo[2];
  const int xrow = b * 8 + (v & 7);
  xo[0] = *(const u32x4*)(a.x + (size_t)xrow * DM + lane * 8);
  xo[1] = *(const u32x4*)(a.x + (size_t)xrow * DM + 512 + c * 8);
  const int job = imin(b + 256 * (9 - v), 800);
  const int jn = job < 600 ? job / 20 : (job % 200) / 20, jk = job % 20;
  {
    const u16* src = job < 600 ? a.wqkvb + (size_t)(32 * jk) * 3 * DM : a.wob + (size_t)(32 * jk) * DM;
#pragma unroll
    for (int i = 0; i < 4; ++i) tr[i] = *(const u32x4*)(src + (size_t)(i * 8 + (lane >> 3)) * (job < 600 ? 3 * DM : DM) + jn * 64 + (lane & 7) * 8);
  }
#pragma unroll
  for (int i = 0; i < 6; ++i) {
    const int idx = (v * 6 + i) * 64 + lane, row = idx / 20;
    wb[i] = PLD(a.wo + (size_t)(h * DH + row) * DM + cq * 160 + (idx - row * 20) * 8);
  }
#pragma unroll
  for (int i = 0; i < 6; ++i) {
    const int idx = i * 64 + lane, r = idx / 24;
#pragma unroll
    for (int u = 0; u < 2; ++u) wa[u][i] = PLD(a.wv + (size_t)(32 * ks + 8 * (r >> 2) + (r & 3) + 4 * u) * HD + h * DH + (idx - r * 24) * 8);
  }
  unsigned pf[2] = {0u, 0u};
  auto prefetch = [&]() {
#pragma unroll
    for (int i = 0; i < 2; ++i) pf[i] = *(const unsigned*)((i ? a.wq : a.wk) + ((size_t)(b * 10 + v) * 48 + imin(lane, 47)) * 32);
  };
  if (PF_POS == 0) prefetch();
  __builtin_amdgcn_sched_barrier(0);
  unsigned lat = ~0u;
  auto probe = [&](int n0, int n) {
    const char* p = a.ws + zo;
#pragma unroll 1
    for (int i = n0; i < n0 + n; ++i) {
      u64 t0, t1; unsigned d;
      asm volatile("s_memtime %0\n\ts_waitcnt lgkmcnt(0)\n\ts_load_dword %2, %3, %4\n\ts_waitcnt lgkmcnt(0)\n\ts_memtime %1\n\ts_waitcnt lgkmcnt(0)"
                   : "=&s"(t0), "=&s"(t1), "=&s"(d) : "s"(p), "s"((i & 3) * 256 + (i >> 2) * 128) : "memory");
      PRB_LOG(i, (unsigned)t1 - (unsigned)t0);
      lat = umin(lat, (unsigned)t1 - (unsigned)t0);
    }
  };
  asm volatile("s_waitcnt vmcnt(26)" ::: "memory");
  probe(0, PRB_N0);
  latw[v] = lat;
  __builtin_amdgcn_sched_barrier(0);
  auto xput = [&](int k) {
    if (v < 8) { xst[k][lane * 8 + (v ^ (lane & 7))] = xo[0]; if (lane < 16) xst[k][(64 + lane) * 8 + (v ^ (lane & 7))] = xo[1]; }
  };
  xput(0);
  const float xn2 = u2f(__builtin_amdgcn_readfirstlane(f2u(ln_row(lane, xo))));
  xput(1);
#pragma unroll
  for (int i = 0; i < 4; ++i) *(u32x4*)(slot + (i * 8 + (lane >> 3)) * 144 + (lane & 7) * 16) = tr[i];
  __builtin_amdgcn_fence(__ATOMIC_RELEASE, "wavefront"); __builtin_amdgcn_wave_barrier();
  bf16x8 tf[4];
#pragma unroll
  for (int t = 0; t < 4; ++t) {
    const char* bp = slot + (8 * g + (c >> 2)) * 144 + t * 32 + (c & 3) * 8;
    tf[t] = tr_frag(bp, bp + 4 * 144);
  }
  __builtin_amdgcn_fence(__ATOMIC_RELEASE, "wavefront"); __builtin_amdgcn_wave_barrier();
  __builtin_amdgcn_sched_barrier(0);
#pragma unroll
  for (int i = 0; i < 6; ++i) {
    const int idx = (v * 6 + i) * 64 + lane, row = idx / 20;
    *(u32x4*)(img + row * 320 + (((idx - row * 20) ^ ((row >> 2) & 2)) * 16)) = wb[i];
  }
  bf16x8 A[2][6];
#pragma unroll
  for (int u = 0; u < 2; ++u) {
#pragma unroll
    for (int i = 0; i < 6; ++i) {
      const int idx = i * 64 + lane, r = idx / 24;
      *(u32x4*)(slot + r * 384 + (((idx - r * 24) ^ (r >> 1)) * 16)) = wa[u][i];
    }
    __builtin_amdgcn_fence(__ATOMIC_RELEASE, "wavefront"); __builtin_amdgcn_wave_barrier();
#pragma unroll
    for (int e = 0; e < 6; ++e) A[u][e] = *(const bf16x8*)(slot + c * 384 + (((4 * e + g) ^ (c >> 1)) * 16));
    __builtin_amdgcn_fence(__ATOMIC_RELEASE, "wavefront"); __builtin_amdgcn_wave_barrier();
  }
  BAR();
  PROT(1);
  if (PF_POS == 0) asm volatile("" :: "v"(pf[0]), "v"(pf[1]));
  if (PF_POS == 1) prefetch();
  {
    const unsigned xf = (unsigned)((b >> 1) * 20480 + (tid >> 3) * 256 + (b & 1) * 128 + (tid & 7) * 16);
    const __amdgpu_buffer_rsrc_t rx = __builtin_amdgcn_make_buffer_rsrc(a.ws + OFF_XN, 0, (int)(OFF_QB - OFF_XN), 0x00020000);
    __builtin_amdgcn_raw_buffer_store_b128(xst[1][tid ^ ((tid >> 3) & 7)], rx, xf, 0, X_AUX);
    __builtin_amdgcn_raw_buffer_store_b128(xst[0][tid ^ ((tid >> 3) & 7)], rx, xf + (unsigned)(OFF_XB - OFF_XN), 0, X_AUX);
    if (v < 8 && lane < 4) ((float*)(a.ws + OFF_XNRM))[xrow * 4 + lane] = lane == 0 ? xn2 : 0.f;
  }
  if (job < 800) {
    const __amdgpu_buffer_rsrc_t rt = __builtin_amdgcn_make_buffer_rsrc(a.ws + (job < 600 ? OFF_WQKVBT : OFF_WOBT) + ((size_t)jn * 80 + jk) * 1024, 0, 81920, 0x00020000);
#pragma unroll
    for (int t = 0; t < 4; ++t) __builtin_amdgcn_raw_buffer_store_b128(__builtin_bit_cast(u32x4, tf[t]), rt, lane * 16 + t * 20480, 0, T_AUX);
  }
  const int gp = h >> 1, hh = h & 1;
  const __amdgpu_buffer_rsrc_t rwb = __builtin_amdgcn_make_buffer_rsrc(a.ws + OFF_WB, 0, (int)WB_BYTES, 0x00020000);
  const unsigned phi = WB_NEAR ? ((((unsigned)b >> 4) & 1u) ^ (unsigned)(latw[8] < latw[9]) ^ (WB_NEAR - 1)) << 13 : (unsigned)(b & 32) << 8;
  if (tid == 0) ((unsigned*)(a.ws + OFF_PHI))[b] = phi;
#pragma unroll
  for (int n = 0; n < 10; ++n) {
    const int off = n * 32 + 8 * (c & 3), T = cq * 10 + n;
    const char* bp = img + (8 * g + (c >> 2)) * 320 + (off ^ ((g & 1) << 5));
    f32x4 ac[2] = {{0.f, 0.f, 0.f, 0.f}, {0.f, 0.f, 0.f, 0.f}};
#pragma unroll
    for (int e = 0; e < 6; ++e) {
      const bf16x8 bf = tr_frag(bp + e * 32 * 320, bp + (e * 32 + 4) * 320);
      ac[0] = mfma32(A[0][e], bf, ac[0]); ac[1] = mfma32(A[1][e], bf, ac[1]);
    }
    const u32x4 o = {pack2(ac[0][0], ac[0][1]), pack2(ac[0][2], ac[0][3]), pack2(ac[1][0], ac[1][1]), pack2(ac[1][2], ac[1][3])};
    const unsigned oo = (T < 32 ? wb_off(rec_wg(gp, T & 15) * 100 + (hh * 3 + (T >> 4)) * 20 + ks) + (unsigned)(lane * 16)
                                : wb_off(rec_wg(gp, 2 * (T - 32) + (c >> 3)) * 100 + 40 + ks) + (unsigned)((g * 16 + hh * 8 + (c & 7)) * 16)) ^ phi;
    __builtin_amdgcn_raw_buffer_store_b128(o, rwb, oo, 0, B_AUX);
    float n2 = 0.f;
#pragma unroll
    for (int r = 0; r < 4; ++r) n2 += ac[0][r] * ac[0][r] + ac[1][r] * ac[1][r];
    n2 = x32sum(x16sum(n2));
    if (g == 0) red[v][n * 16 + c] = n2;
  }
  PROT(2);
  if (PF_POS == 1) asm volatile("" :: "v"(pf[0]), "v"(pf[1]));
  if (v >= 8 && b < 64 && lane == 0) ((unsigned*)(a.ws + OFF_LAT))[(v - 4) * 64 + b] = umin(lat, 60000u);
  if (WARM_CODE && v == 7) {
    const char* pc; int d0, d1;
    asm("s_getpc_b64 %0\n\ts_mov_b32 %1, _Z10rec_kernel7RecArgs@rel32@lo+4\n\ts_mov_b32 %2, _Z11proj_kernelPKtS0_S0_Pt@rel32@lo+12" : "=s"(pc), "=s"(d0), "=s"(d1));
    const char *c0 = pc + d0, *c1 = pc + d1;
    const long o = ((long)b * 64 + lane) * 64;
    if (o < c1 - c0) asm volatile("" :: "v"(*(const volatile GAS unsigned*)(c0 + o)));
  }
  if (v == 9 && lane < 32) {
    const int q = b * 32 + lane;
    *(u32x4*)(a.ws + OFF_KV + (size_t)(q >> 2) * KVT + 1280 + (q & 3) * 336 + 320) = (u32x4){0x3f803f80u, 0x3f803f80u, 0x3f803f80u, 0x3f803f80u};
  }
  BAR();
  if (v == 1 && lane == 0) ((unsigned*)(a.ws + OFF_LAT))[b] = umin(umin(umin(latw[0], latw[4]) + umin(latw[1], latw[5]), 30000u) + umin(umin(latw[2], latw[6]) + umin(latw[3], latw[7]), 30000u), 60000u);
  if (v == 0) {
    float mx = 0.f;
#pragma unroll
    for (int k = 0; k < 3; ++k) {
      const int col = k * 64 + lane;
      float sm = 0.f;
#pragma unroll
      for (int i = 0; i < 10; ++i) sm += red[i][col < 160 ? col : 0];
      mx = fmx(mx, sm);
    }
    mx = fmx(mx, dppf<DPP_XOR1>(mx)); mx = fmx(mx, dppf<DPP_XOR2>(mx)); mx = fmx(mx, dppf<DPP_ROR4>(mx)); mx = fmx(mx, dppf<DPP_ROR8>(mx));
    mx = x32mx(x16mx(mx));
    if (lane == 0) ((float*)(a.ws + OFF_BNRM))[(cq * 32 + h) * 2 + dh] = mx;
  }
  PROT(3);
}

struct RecArgs {
  const u16* mem0; const u16* wq; const u16* wk; const u16* xold; u16* xnew; char* ws;
  unsigned layer; unsigned pad;
};
#define RLX_AGENT __ATOMIC_RELAXED, __HIP_MEMORY_SCOPE_AGENT
constexpr unsigned SPIN_CAP = 1u << 20;
#define TSTAMP(k, v)
#define ARRIVE(k)
#define ARRIVE_OUT()
#define ARRIVE_DECL()
#define LOGV(k, v)
#define LOGF(k, v)
#define LOGP(k)
#define LOGQ(k)
#define LOGF2(k)
#define LOGD(k)
#define LOGT(k, v)
#define STAMP_DECL()
#define STAMP(k)
#define STAMPV(k, v)
#define STAMP_OUT()
#define REC_D1 520
#define SCH_V 10
#define SCH_VMIN 0
#define SCH_DMIN 4
#define SCH_PEN 150
#define SCH_VMIN2 (-4)
#define SCH_PEN2 40
#define SCH_GU 1
#define SCH_GD 2
#define SCH_UP 6
#define SCH_D0 196
#define SCH_FIRST 120
#define EXP_NODMA 0
#define BG_HOLD 220
#define QBATCH 0
#define WPF_N 5
#define WPF_W 0
#define WPF0 (63 - WPF_N)
#define BG_HOLD4 250
#define D1_W1 20
#define F3_D 1200
#define T2W 2
#define AT_ASM 1
#define PM_Z 1
#define P2_X 1
#define ST_KS 14
#define ST_KS0 ST_KS
#define ST_KS1 ST_KS
#define PADR 0x6666
#define PADT 0x000
#define CODE_PAD(n) asm volatile(".p2align 6\n.rept %0\ns_nop 0\n.endr" :: "n"(n))
#define ST_X 250
#define ST_ALL 4
#define CPF_S 61
#define FR_D 700
#define FR_PACE 6
#define END_E60 600
#define END_E1 1300
#define END_E2 3400
#define END_E62 2800
#define END_E3 6800
#define END_E4 11400
#define EARLY_N 6
#define EARLY_D1 0
#define EARLY_V 10
#define EARLY_V1 18
#define F3 0
#define F3A 0
#define F3A_D 400
#define T4_D 300
#define T4_P 1
#define P2_W 1
#define PS_SL 1
#define F2B 4
#define F0C 0
#define F2C 0
#define W2_P 1
#define SCH_W 1
#define SP_SL 0
#define D1_M 300
#define D1_MIN 260
#define D1_C 96
#define D1_G 2
#define BG_HOLD6 0
#define W2_D 0
#define END_E61 2600
#define D1_LATE 200
#define P2_G0 10
#define P2_G2 6
#define P2_G3 6
#define SCH_RL 0
#define SCH_P 3
#define MMA_P 1
#define SP_P 0
#define F6 8
#define F7 10
#define XPF_W 2
#define SCH_LATE 0
#define LATE_V 8
#define QW 3
#define FB_SL 0
#define P2_G1 1
#define P1_G 0
#define F4Z 0
#define F5Z 0
#define T5_Z 2
#define F0A 3
#define FB_E 0
#define EXACT 1
#define D1_P 3
#define VO_A0 9
#define VO_A2 10
#define VO_B0 3
#define VO_B1 0
#define VO_N2 7
#define VO_N3 10
#define VO_W -1
#define UN_P 2
#define VP_E 1
#define TS_B 0
#define ST_E 1
#define W1_SL 0
#define VO_A1 4
#define LAST_V 22
#define END_E59 3200
#define END_G2 600
#define END_G3 600
#define QR 4
#define WPF_S 128
#define END_SL 4
#define SCH_DR 182
#define SCH_DK 3
#define FR_HOLD 0
#define FW 8
#define PR_P 0
#define REL_AT 1
#define XO_T 4
#define Q_AUX 0
#define O_WT 0
#define P_WT 0
#define KP0 0
#define KP1 0
#define XPF 3
#define W1_D 0
#define F4 12
#define F5 0
#define F67 0
#define KP2 0
#define REC_RNG 70u
#define SCH_DRAIN 40
#define PL_X 0
#define FC(k)
#define PIN_V(x) asm volatile("" : "+v"(x))
#define PIN_S(x) asm volatile("" : "+s"(x))
#define VM_DRAIN() do { asm volatile("s_waitcnt vmcnt(0)" ::: "memory"); __builtin_amdgcn_s_waitcnt(0x0F70); } while (0)
#define SC1_LOAD(off) __builtin_amdgcn_raw_buffer_load_b128(rs, (off), 0, 16)
#define SC1_LOADS(off, soff) __builtin_amdgcn_raw_buffer_load_b128(rs, (off), (soff), 16)
#define SC1_STORE8S(v, off, soff) __builtin_amdgcn_raw_buffer_store_b64((v), rs, (off), (soff), 16)
#define SC1_LOAD8(off) __builtin_amdgcn_raw_buffer_load_b64(rs, (off), 0, 16)
#define SC1_STORE(v, off) __builtin_amdgcn_raw_buffer_store_b128((v), rs, (off), 0, 16)
#define SC1_STORE8(v, off) __builtin_amdgcn_raw_buffer_store_b64((v), rs, (off), 0, 16)
#if defined(__HIP_DEVICE_COMPILE__)
#pragma clang attribute push(__attribute__((target("no-packed-fp32-ops"))), apply_to = function)
#endif
__device__ __forceinline__ unsigned now32() { return (unsigned)__builtin_amdgcn_s_memrealtime(); }
__device__ __forceinline__ void wait_until(unsigned t) { for (;;) { const int d = (int)(t - now32()); if (d <= 0) break; if (d > 8) __builtin_amdgcn_s_sleep(1); } }
__device__ __forceinline__ unsigned cyc32() { return (unsigned)__builtin_amdgcn_s_memtime(); }
__device__ __forceinline__ void wait_cyc(unsigned t) { for (;;) { const int d = (int)(t - cyc32()); if (d <= 0) break; if (d > 200) __builtin_amdgcn_s_sleep(1); } }
#define WAIT_ASM(clk, far_) "1:\n\t" clk " vcc\n\ts_waitcnt lgkmcnt(0)\n\ts_sub_i32 %0, %1, vcc_lo\n\ts_cmp_lt_i32 %0, " far_ "\n\ts_cbranch_scc1 3f\n\ts_sleep 1\n\ts_branch 1b\n" \
                            "2:\n\t" clk " vcc\n\ts_waitcnt lgkmcnt(0)\n\ts_sub_i32 %0, %1, vcc_lo\n3:\n\ts_cmp_gt_i32 %0, 0\n\ts_cbranch_scc1 2b"
__device__ __forceinline__ int spin_until(unsigned t) { int d; asm volatile(WAIT_ASM("s_memrealtime", "9") : "=&s"(d) : "s"(t) : "vcc", "scc", "memory"); return d; }
__device__ __forceinline__ int spin_cyc(unsigned t) { int d; asm volatile(WAIT_ASM("s_memtime", "200") : "=&s"(d) : "s"(t) : "vcc", "scc", "memory"); return d; }
__device__ __forceinline__ void spin_exact(unsigned t) {
  if (!EXACT) { spin_cyc(t); return; }
  int d;
  asm volatile("1:\n\ts_memtime vcc\n\ts_waitcnt lgkmcnt(0)\n\ts_sub_i32 %0, %1, vcc_lo\n\ts_cmp_gt_i32 %0, 95\n\ts_cbranch_scc1 1b\n\t"
               "s_max_i32 %0, %0, 0\n\ts_andn2_b32 %0, %0, 3\n\ts_sub_i32 %0, 3f-2f, %0\n\ts_getpc_b64 vcc\n"
               "2:\n\ts_add_u32 vcc_lo, vcc_lo, %0\n\ts_addc_u32 vcc_hi, vcc_hi, 0\n\ts_setpc_b64 vcc\n\t.rept 24\n\ts_nop 0\n\t.endr\n3:"
               : "=&s"(d) : "s"(t) : "scc", "vcc", "memory");
}
template <int OFF, int SL> __device__ __forceinline__ void lds_spin(unsigned lds0, unsigned want) {
  unsigned v;
  asm volatile("1:\n\tds_read_b32 %0, %1 offset:%3\n\t.if %4\n\ts_sleep %4\n\t.endif\n\ts_waitcnt lgkmcnt(0)\n\tv_cmp_gt_u32 vcc, %2, %0\n\ts_cbranch_vccnz 1b"
               : "=&v"(v) : "v"(lds0), "s"(want), "n"(OFF), "n"(SL) : "vcc", "memory");
}
template <int CTRL> __device__ __forceinline__ int dppi(int v) { return __builtin_amdgcn_mov_dpp(v, CTRL, 0xf, 0xf, true); }
__device__ __forceinline__ int wave_imax(int l) {
  l = imax(l, dppi<DPP_XOR1>(l)); l = imax(l, dppi<DPP_XOR2>(l)); l = imax(l, dppi<DPP_ROR4>(l)); l = imax(l, dppi<DPP_ROR8>(l));
  int a = imax(__builtin_amdgcn_readlane(l, 0), __builtin_amdgcn_readlane(l, 16)), b = imax(__builtin_amdgcn_readlane(l, 32), __builtin_amdgcn_readlane(l, 48));
  PIN_S(a); PIN_S(b);
  return imax(a, b);
}
__device__ __forceinline__ void glds16(const void* gsrc, unsigned lds_dst) {
  unsigned keep;
  asm volatile("s_mov_b32 %0, m0\n\ts_mov_b32 m0, %2\n\ts_nop 0\n\tglobal_load_lds_dwordx4 %1, off\n\ts_mov_b32 m0, %0" : "=&s"(keep) : "v"(gsrc), "s"(lds_dst) : "memory");
}
__device__ __forceinline__ void glds16_sc1(const void* gsrc, unsigned lds_dst) {
  unsigned keep;
  asm volatile("s_mov_b32 %0, m0\n\ts_mov_b32 m0, %2\n\ts_nop 0\n\tglobal_load_lds_dwordx4 %1, off sc1\n\ts_mov_b32 m0, %0" : "=&s"(keep) : "v"(gsrc), "s"(lds_dst) : "memory");
}
__device__ __forceinline__ unsigned acc_off(int G, int row) {
  return (unsigned)(G < 128 ? ((G & 31) | ((G & 32) << 1) | ((G & 64) >> 1)) * 256 + row * 16 : (2 * G - 128 + (row >> 2)) * 256 + (row & 3) * 16);
}
__device__ __forceinline__ unsigned nz(unsigned v) { return v > 1u ? v : 1u; }
__device__ __forceinline__ unsigned pack2n(float lo, float hi) { return nz(pack2(lo, hi)); }
__device__ __forceinline__ unsigned min4(u32x4 v) { return umin(umin(v[0], v[1]), umin(v[2], v[3])); }
__device__ __forceinline__ bf16x8 as8(u32x4 v) { return __builtin_bit_cast(bf16x8, v); }

constexpr int NV = 7, NKQ = 2;
constexpr int L_RED1 = 0;
constexpr int L_BM = L_RED1 + 8 * 1536;
constexpr int L_STG = L_BM + 10240;
constexpr int L_ST = L_STG + 2048 + 2560;
constexpr int L_MISC = L_ST + 512;
constexpr int L_KX = L_MISC + 128;
constexpr int L_QX = L_KX + NKQ * 2048;
constexpr int L_XN = L_QX + NKQ * 2048;
constexpr int L_VOX = L_XN + 81920;
constexpr int L_VOM = L_VOX + NV * 5120;
constexpr int L_BYTES = L_VOM + NV * 1280;
constexpr int S_IMG = L_XN + 40960;
static_assert(S_IMG + 61440 <= L_VOM, "start-up image");
static_assert(L_BYTES <= 160 * 1024, "LDS budget");

__device__ __forceinline__ bf16x8 wqk_frag(const char* lds, int t, int ks, int lane) {
  const int c = lane & 15, g = lane >> 4, p = c & 3;
  const char* q = lds + S_IMG + (32 * ks + 8 * g + (c >> 2)) * 96 + 8 * p + (t == 0 ? 0 : (t == 1 ? 48 : (p < 2 ? 32 : 64)));
  const v4i16_t lo = __builtin_amdgcn_ds_read_tr16_b64_v4i16((LAS v4i16_t*)q), hi = __builtin_amdgcn_ds_read_tr16_b64_v4i16((LAS v4i16_t*)(q + 4 * 96));
  return (bf16x8){lo[0], lo[1], lo[2], lo[3], hi[0], hi[1], hi[2], hi[3]};
}
__device__ __forceinline__ bf16x8 sel_frag(int c, int base) {
  bf16x8 r;
#pragma unroll
  for (int i = 0; i < 8; ++i) r[i] = (c == base + i) ? (short)0x3f80 : (short)0;
  return r;
}
__device__ __forceinline__ bf16x8 qk_frag(const char* p4, const char* p2, const char* p2b) {
  f32x4 a0 = *(const f32x4*)(p4), a1 = *(const f32x4*)(p4 + 1792), a2 = *(const f32x4*)(p4 + 3584), a3 = *(const f32x4*)(p4 + 5376);
  f32x2 b0 = *(const f32x2*)(p2), b1 = *(const f32x2*)(p2 + 1792), b2 = *(const f32x2*)(p2b), b3 = *(const f32x2*)(p2b + 1792);
  f32x4 va = (a0 + a1) + (a2 + a3);
  f32x2 vb = (b0 + b1) + (b2 + b3);
  return mk8(pack2(va[0], va[1]), pack2(va[2], va[3]), pack2(vb[0], vb[1]), 0u);
}
__device__ __forceinline__ void x_fetch(const u16* src, unsigned lds0, int seg, int p, int lane) {
  if (EXP_NODMA) return;
  glds16_sc1(src + (size_t)p * 512 + lane * 8, lds0 + L_XN + (seg & 1) * 40960 + p * 1024);
}
template <int N> __device__ __forceinline__ void x_fetch_n(const u16* src, unsigned lds0, int seg, int p0, unsigned voff) {
  if (EXP_NODMA) return;
#pragma unroll
  for (int i = 0; i < N; i += 4) {
    unsigned keep;
    asm volatile("s_mov_b32 %0, m0\n\ts_mov_b32 m0, %3\n\ts_nop 0\n\tglobal_load_lds_dwordx4 %1, %2 sc1\n\t"
                 ".if %4 > 1\n\tglobal_load_lds_dwordx4 %1, %2 offset:1024 sc1\n\t.endif\n\t.if %4 > 2\n\tglobal_load_lds_dwordx4 %1, %2 offset:2048 sc1\n\t.endif\n\t"
                 ".if %4 > 3\n\tglobal_load_lds_dwordx4 %1, %2 offset:3072 sc1\n\t.endif\n\ts_mov_b32 m0, %0"
                 : "=&s"(keep) : "v"(voff), "s"(src + (size_t)(p0 + i) * 512), "s"(lds0 + L_XN + (seg & 1) * 40960 + (p0 + i) * 1024), "n"(N - i < 4 ? N - i : 4) : "memory");
  }
}
__device__ __forceinline__ int qkv_row(int b) { return (b & 7) / QR * (2 * QR) + (b >> 3) / (16 / QR); }
__device__ __forceinline__ int qkv_col(int b) { return (b & 7) % QR * (16 / QR) + (b >> 3) % (16 / QR); }
__device__ __forceinline__ const u16* x_src(const RecArgs& a, int seg, int b) {
  return seg < NSEG ? (const u16*)(a.ws + OFF_XN) + (size_t)seg * 32 * DM : a.xnew + (size_t)(qkv_row(b) + 16 * (seg - NSEG)) * 32 * DM;
}
template <int N>
__device__ __forceinline__ void img_fetch(const RecArgs& a, unsigned lds0, int h, int j, int part, int lane) {
#pragma unroll
  for (int i = 0; i < N; ++i) {
    const int n = part * N + i, idx = n * 64 + lane, row = idx / 6, pc = idx - row * 6;
    if (FR_PACE && N == 20) asm volatile("s_waitcnt vmcnt(%0)" :: "n"(FR_PACE) : "memory");
    glds16((pc < 3 ? a.wq : a.wk - 24) + (size_t)row * HD + h * DH + j * 24 + pc * 8, lds0 + S_IMG + n * 1024);
  }
}
constexpr int P2B = F4 + F5 + 2 * F67 + F6 + F7, PZ = P2B + F2B, PA = PZ, P0C = PA + 2 * F0A, F3C = 40 - F3A - F3 - P0C - 2 * F0C - F2C;
static_assert(F3C >= 0, "fetch plan");
template <int OFF> __device__ __forceinline__ void lds_inc(unsigned lds0) {
  asm volatile("s_mov_b64 exec, 1\n\tds_add_u32 %0, %1 offset:%2\n\ts_mov_b64 exec, -1" :: "v"(lds0), "v"(1u), "n"(OFF) : "memory");
}
__device__ __forceinline__ void lds_arrive(unsigned* p, int lane) {
  asm volatile("s_waitcnt lgkmcnt(0)" ::: "memory");
  if (lane == 0) __hip_atomic_fetch_add(p, 1u, __ATOMIC_RELAXED, __HIP_MEMORY_SCOPE_WORKGROUP);
}
__device__ __forceinline__ void lds_wait(unsigned* p, unsigned want) {
  while (__hip_atomic_load(p, __ATOMIC_RELAXED, __HIP_MEMORY_SCOPE_WORKGROUP) < want) __builtin_amdgcn_s_sleep(1);
  asm volatile("" ::: "memory");
}
constexpr float KEXP = 0.07216878364870322f * 1.4426950408889634f;
#define FRESH_LANE() int lane = lane0; asm volatile("" : "+v"(lane)); const int c = lane & 15, g = lane >> 4; (void)c; (void)g

struct OutTask {
  u32x4 y[4]; u32x2 xo, out;
  bf16x8 sel;
  unsigned t5f, t5o;
  __device__ __forceinline__ void init(int b, int lane) {
    const int um = b / 10, uq = (b / 5) & 1, up = b % 5, tok = lane & 15, k = up < 4 ? (up >> 1) * 256 + um * 16 + (up & 1) * 8 : 512 + um * 8, q4 = ((lane >> 4) & 1) * 4;
    t5f = (unsigned)(((uq * 20 + (k >> 5)) * 64 + ((k & 31) >> 3) * 16 + tok) * 8 + q4);
    t5o = (unsigned)(((um * 2 + uq) * 16 + (lane >> 4)) * 1280 + up * 256 + tok * 16);
    if (b >= 160) { t5f = 0u; t5o = 0x7ffffff0u; }
    sel = sel_frag(tok, 0);
    PIN_V(sel);
  }
  static __device__ __forceinline__ bool ok(int sl, int b) { return sl >= 0 && b < 160; }
  __device__ __forceinline__ void load(const RecArgs& a, const __amdgpu_buffer_rsrc_t rs, int sl, int b, int part = 3) {
    const unsigned o = t5o | (sl < 0 ? 0x7ff00000u : 0u);
    sl = imax(sl, 0);
#pragma unroll
    for (int k = 0; k < 4; ++k) if (part & 1) y[k] = SC1_LOADS(o, op_slot(sl) + k * 5120);
    if (part & 2) xo = __builtin_bit_cast(u32x2, __hip_atomic_load((const gu64*)(a.xold + (size_t)sl * 32 * DM + t5f), RLX_AGENT));
  }
  __device__ __forceinline__ bool ready(int b) { return b >= 160 || wave_all(umin(umin(min4(y[0]), min4(y[1])), umin(min4(y[2]), min4(y[3]))) != 0u); }
  __device__ __forceinline__ void run(int lane) {
    f32x4 d = {0.f, 0.f, 0.f, 0.f};
#pragma unroll
    for (int k = 0; k < 4; ++k) d = mfma32(sel, as8(y[k]), d);
#pragma unroll
    for (int i = 0; i < 2; ++i) {
      const unsigned r = pack2(d[2 * i], d[2 * i + 1]), x = xo[i];
      out[i] = pack2(u2f(x << 16) + u2f(r << 16), u2f(x & 0xffff0000u) + u2f(r & 0xffff0000u));
    }
    PIN_V(out);
  }
  __device__ __forceinline__ void store(const RecArgs& a, int sl, int b, int lane) {
    __builtin_amdgcn_raw_buffer_store_b64(out, __builtin_amdgcn_make_buffer_rsrc(a.xnew, 0, ok(sl, b) ? S_ * DM * 2 : 0, 0x00020000), (t5f + (unsigned)sl * 32 * DM) * 2u | (unsigned)(lane & 32) << 25, 0, 16);
  }
};

struct PvTask2 {
  u32x4 y[2][2], pa[2], pb[2];
  unsigned lo, lm, vr, ma, mz, so, sz;
  __device__ __forceinline__ void init(int gp, int lane) {
    const int hi = lane >> 5;
    lo = pt_tok(2 * gp) + (unsigned)(lane * 16); lm = pt_mem(2 * gp + hi) + (unsigned)((lane & 31) * 8);
    vr = (unsigned)(L_VOX + 2048 + (lane ^ 8) * 16);
    ma = (unsigned)(L_VOM + hi * 768 + (lane & 31) * 8); mz = (unsigned)(L_VOM + 512 + ((lane & 31) ^ (hi * 8)) * 8);
    so = (unsigned)(L_STG + 2048 + (hi * 16 + (lane & 15)) * 16 + ((lane >> 4) & 1) * 8);
    sz = hi ? (unsigned)(L_RED1 + 8704 + lane * 8) : so + 1024;
    PIN_V(lo); PIN_V(lm); PIN_V(vr); PIN_V(ma); PIN_V(mz); PIN_V(so); PIN_V(sz);
#pragma unroll
    for (int q = 0; q < 2; ++q) { pa[q] = pb[q] = y[q][0] = y[q][1] = (u32x4){0u, 0u, 0u, 0u}; PIN_V(pa[q]); PIN_V(pb[q]); }
  }
  __device__ __forceinline__ void load(const __amdgpu_buffer_rsrc_t rs, unsigned o) {
#pragma unroll
    for (int q = 0; q < 2; ++q) {
      y[q][0] = SC1_LOADS(lo + q * 1024, o); y[q][1] = SC1_LOADS(lo + 2048 + q * 1024, o);
      const u32x2 p = __builtin_amdgcn_raw_buffer_load_b64(rs, lm + q * 256, o, 16);
      pa[q][0] = p[0]; pa[q][1] = p[1];
    }
  }
  __device__ __forceinline__ bool ready() {
    return wave_all(umin(umin(umin(min4(y[0][0]), min4(y[0][1])), umin(min4(y[1][0]), min4(y[1][1]))), umin(umin(pa[0][0], pa[0][1]), umin(pa[1][0], pa[1][1]))) != 0u);
  }
  template <bool DIRECT> __device__ __forceinline__ void run(char* lds, int sv, int lane, const __amdgpu_buffer_rsrc_t rs, unsigned og) {
    const f32x4 Z4 = {0.f, 0.f, 0.f, 0.f};
    const char* vp = lds + L_VOX + sv * 5120 + lane * 16;
    bf16x8 vx[6];
#pragma unroll
    for (int t = 0; t < 5; ++t) vx[t] = *(const bf16x8*)(vp + t * 1024);
    vx[5] = *(const bf16x8*)(lds + vr + sv * 5120);
    const u32x2 a0 = *(const u32x2*)(lds + ma + sv * 1280), a1 = *(const u32x2*)(lds + ma + sv * 1280 + 256);
    const u32x2 z0 = *(const u32x2*)(lds + mz + sv * 1280), z1 = *(const u32x2*)(lds + mz + sv * 1280 + 256);
    const bf16x8 vm = mk8(a0[0], a0[1], a1[0], a1[1]), vz = mk8(z0[0], z0[1], z1[0], z1[1]);
#pragma unroll
    for (int q = 0; q < 2; ++q) { PIN_V(pa[q]); pb[q][2] = pa[q][0]; pb[q][3] = pa[q][1]; PIN_V(pb[q]); }
    __builtin_amdgcn_sched_barrier(0);
    f32x4 o[2][3];
#pragma unroll
    for (int q = 0; q < 2; ++q) {
#pragma unroll
      for (int t = 0; t < 3; ++t) o[q][t] = mfma32(vx[t], as8(y[q][0]), Z4);
#pragma unroll
      for (int t = 0; t < 3; ++t) o[q][t] = mfma32(vx[3 + t], as8(y[q][1]), o[q][t]);
      o[q][0] = mfma32(vm, as8(pa[q]), o[q][0]); o[q][1] = mfma32(vm, as8(pb[q]), o[q][1]); o[q][2] = mfma32(vz, as8(pa[q]), o[q][2]);
    }
#pragma unroll
    for (int q = 0; q < 2; ++q) {
#pragma unroll
      for (int t = 0; t < 3; ++t) {
        const u32x2 v = {pack2n(o[q][t][0], o[q][t][1]), pack2n(o[q][t][2], o[q][t][3])};
        if (DIRECT) SC1_STORE8S(v, (so - (L_STG + 2048) + t * 512) | (t == 2 ? (unsigned)(lane & 32) << 25 : 0u), og + q * 20480);
        else *(u32x2*)(lds + (t < 2 ? so + t * 512 : sz) + q * 1280) = v;
      }
    }
  }
};

struct TokScores {
  bf16x8 k[2], q[2]; u32x4 v[2];
  __device__ __forceinline__ void get(const char* lds, int seg, int lane) {
#pragma unroll
    for (int i = 0; i < 2; ++i) { k[i] = *(const bf16x8*)(lds + L_KX + (seg & 1) * 2048 + i * 1024 + lane * 16); q[i] = *(const bf16x8*)(lds + L_QX + (seg & 1) * 2048 + i * 1024 + lane * 16); }
  }
  __device__ __forceinline__ void run(int qt) {
    const f32x4 Z4 = {0.f, 0.f, 0.f, 0.f};
    const f32x4 d1 = mfma32(k[0], q[qt], Z4), d2 = mfma32(k[1], q[qt], Z4);
    v[qt] = (u32x4){pack2n(d1[0], d1[1]), pack2n(d1[2], d1[3]), pack2n(d2[0], d2[1]), pack2n(d2[2], d2[3])};
    PIN_V(v[qt]);
  }
  __device__ __forceinline__ void store(const __amdgpu_buffer_rsrc_t rs, unsigned off, unsigned o) {
    __builtin_amdgcn_raw_buffer_store_b128(v[0], rs, off, o, 16); __builtin_amdgcn_raw_buffer_store_b128(v[1], rs, off + 1024, o, 16);
    asm volatile("s_nop 1" ::: "memory");
  }
};

__device__ __forceinline__ void touch(const char* base, unsigned off, unsigned lds0) {
  unsigned keep;
  asm volatile("s_mov_b32 %0, m0\n\ts_mov_b32 m0, %3\n\ts_nop 0\n\tglobal_load_lds_dword %1, %2 sc1\n\ts_mov_b32 m0, %0" : "=&s"(keep) : "v"(off), "s"(base), "s"(lds0 + L_RED1 + 8192) : "memory");
}
template <int N> __device__ __forceinline__ void stouch(const char* p, unsigned& d) {
  asm volatile(".set pf_o, 0\n\t.rept %2\n\ts_load_dword %0, %1, pf_o\n\t.set pf_o, pf_o + 128\n\t.endr" : "+s"(d) : "s"(p), "n"(N) : "memory");
}
__device__ __forceinline__ void code_touch(int b, int lane, unsigned lds0) {
  const char* pc; int d0, d1;
  asm volatile("s_getpc_b64 %0\n\ts_mov_b32 %1, _Z10rec_kernel7RecArgs@rel32@lo+4\n\ts_mov_b32 %2, _Z11attb_kernelPKtS0_Pt@rel32@lo+12" : "=s"(pc), "=s"(d0), "=s"(d1));
  const int o = (lane * 32 + (b >> 3)) * 128;
  touch(pc + d0, (unsigned)(o < d1 - d0 ? o : 0), lds0);
}
__device__ __forceinline__ unsigned zero_base(int b, int h, int j, int ln) {
  return ln < 32 ? (unsigned)((h & 3) * 32768 + j * 512 + ln * 16) : ln < 40 ? (unsigned)((h & 2) * 1024 + j * 256 + (ln & 4) * 32 + (h & 1) * 64 + (ln & 3) * 16)
                 : (unsigned)OFF_ACC + (b < ACC_NB ? (unsigned)(b * 256 + (ln - 40) * 16) : ts_piece((b - ACC_NB) * 8 + ln - 40));
}
__device__ __forceinline__ unsigned sct_slot(unsigned ck, unsigned sl) { return sl * SCT_STEP + ((ck >> (sl & 4u)) & 7u) * 4096u; }
__device__ __forceinline__ unsigned scm_slot(unsigned ck, unsigned sl) { return OFF_SCM + sl * SCM_STEP + ((ck >> 8) & 7u) * 4096u; }
__device__ __forceinline__ int ck_rot(int x) { return ((x & 1) << 2) | (x >> 1); }
struct Place {
  unsigned t[8];
  __device__ __forceinline__ void load(const char* ws, int lane) {
#pragma unroll
    for (int x = 0; x < 8; ++x) t[x] = *(const unsigned*)(ws + OFF_LAT + ((imin(lane >> 3, 5) * 8 + (ck_rot(x) ^ (lane & 7))) * 8 + x) * 4);
  }
  __device__ __forceinline__ unsigned get(int b, int lane) {
    int c = (int)((((t[0] + t[1]) + (t[2] + t[3])) + ((t[4] + t[5]) + (t[6] + t[7]))) * 8u) + (lane & 7);
    c = imin(c, dppi<DPP_XOR1>(c)); c = imin(c, dppi<DPP_XOR2>(c)); c = imin(c, dppi<0x141>(c));
    unsigned k = 0u;
#pragma unroll
    for (int r = 0; r < 6; ++r) k |= (unsigned)(__builtin_amdgcn_readlane(c, 8 * r) & 7) << (4 * r);
    return (k ^ PL_X) ^ (unsigned)ck_rot(b & 7) * 0x111111u;
  }
};

struct Sched {
  u32x4 tsv, rec;
  unsigned side, o;
  __device__ __forceinline__ void pre(int s) { o = (unsigned)OFF_ACC + (unsigned)((s + SR - 1) % SR) * (unsigned)ACC_STEP + ACC_TS + side; PIN_S(o); }
  __device__ __forceinline__ void load(const __amdgpu_buffer_rsrc_t rs, int lane) {
    tsv = SC1_LOADS(ts_piece(lane & 63) - ACC_TS, o); rec = SC1_LOADS(128, o);
  }
  __device__ __forceinline__ unsigned last(unsigned ref) {
    const unsigned o = 0x40000000u - ref;
    int l = (int)umax(umax(tsv[0] ? tsv[0] + o : 0u, tsv[1] ? tsv[1] + o : 0u), umax(tsv[2] ? tsv[2] + o : 0u, tsv[3] ? tsv[3] + o : 0u));
    return (unsigned)wave_imax(l) - o;
  }
  __device__ __forceinline__ unsigned run(const RecArgs& a, const __amdgpu_buffer_rsrc_t rs, int s, int b, int lane) {
    constexpr int w = 3; (void)w;
    unsigned acur = __builtin_amdgcn_readfirstlane(rec[0]), aprev = __builtin_amdgcn_readfirstlane(rec[1]), hist = __builtin_amdgcn_readfirstlane(rec[3]);
    int D8 = (int)__builtin_amdgcn_readfirstlane(rec[2]);
    const int d = wave_imax((int)(umax(umax(tsv[0], tsv[1]), umax(tsv[2], tsv[3])) - aprev)), dd = 8 * d - D8;
    int Dn = D8 + (dd >> (dd > 0 ? SCH_GU : SCH_GD));
    unsigned anext = (acur + (unsigned)((Dn + 4 + imax(Dn - 8 * SCH_DR, 0) * SCH_DK / 8) >> 3) + SCH_V + (s < EARLY_N ? EARLY_V : (s >= WPF0 ? (s == NSEG - 2 ? LAST_V : LATE_V) : 0))) | 1u;
    if (__builtin_expect(hist < 2u || aprev == 0u || d < 1 || d + SCH_VMIN > (int)(acur - aprev), 0)) {
      if (s == 0) {
        for (int it = 0; it < 40 && !wave_all(min4(tsv) != 0u); ++it) {
          __builtin_amdgcn_s_sleep(ST_ALL);
          asm volatile("" ::: "memory");
          load(rs, lane);
        }
      }
      const unsigned now = now32(), L = last(now);
      if (acur == 0u) { D8 = 8 * SCH_D0; hist = 0u; }
      const bool late = acur == 0u || (int)(L + SCH_VMIN - acur) > 0;
      if (late) { if (b == 0) FC(3); if (acur) D8 += 8 * (hist ? SCH_UP : 3 * SCH_UP); }
      else if (aprev) { if (hist < 2u) D8 = 8 * imin(imax((int)(L - aprev), 1), 4000); hist = umin(hist + 1u, 2u); }
      const unsigned base = acur == 0u ? L + SCH_FIRST : (late ? L + SCH_V + ((int)(L + SCH_VMIN2 - acur) <= 0 ? SCH_PEN2 : SCH_PEN) : acur);
      anext = (base + (unsigned)((D8 + 4) >> 3) + SCH_V + (s == 1 ? EARLY_V1 : (s < EARLY_N ? EARLY_V : 0))) | 1u;
      if (s == 0) anext = (L + ST_X) | 1u;
      if ((int)(now - L) > 50000) anext = 0u;
      if (late) acur = 0u;
      Dn = D8;
    }
    if (__builtin_expect(b == 0, 0))
      __builtin_amdgcn_raw_buffer_store_b128((u32x4){anext, acur, (unsigned)Dn, hist}, rs, lane < 2 ? lane * 8192 : 0x7ffffff0, (unsigned)OFF_ACC + (unsigned)(s % SR) * (unsigned)ACC_STEP + ACC_TS + 128u, 16);
    if (XO_T && anext) anext += (b & 1) * XO_T;
    LOGV(0, anext); LOGV(1, acur == 0u); LOGV(2, (Dn + 4) >> 3); LOGV(3, d); LOGV(4, acur - aprev - d); LOGV(5, aprev);
    return anext;
  }
};

struct TokTask {
  u32x4 y[2]; u32x2 ym; u32x4 res;
  unsigned l0, l1, lm, s0, s1;
  static __device__ __forceinline__ bool ok(int sl) { return sl >= 0 && sl < NSEG; }
  __device__ __forceinline__ void init(int h, int j, int lane) {
    const int c = lane & 15, g = lane >> 4;
    l0 = ts_tok(h, g) + (unsigned)(j * 256 + c * 16); l1 = l0 + 16384u;
    lm = ts_mem(h, g + 4 * (c >> 3)) + (unsigned)(j * 64 + (c & 7) * 8);
    res = (u32x4){0u, 0u, 0u, 0u};
    const int qi = 4 * j + (c & 3);
    s0 = g < 2 ? pt_tok(h) + (unsigned)((qi >> 4) * 1024 + ((c >> 2) * 16 + (qi & 15)) * 16 + g * 8) : 0x7ffffff0u;
    s1 = lane < 8 ? pt_mem(h) + (unsigned)((qi >> 4) * 256 + ((c >> 2) * 16 + (qi & 15)) * 8) : 0x7ffffff0u;
    PIN_V(l0); PIN_V(l1); PIN_V(lm); PIN_V(s0); PIN_V(s1);
  }
  __device__ __forceinline__ void load(const __amdgpu_buffer_rsrc_t rs, unsigned o) {
    y[0] = SC1_LOADS(l0, o); y[1] = SC1_LOADS(l1, o);
    ym = __builtin_amdgcn_raw_buffer_load_b64(rs, lm, o, 16);
  }
  __device__ __forceinline__ bool ready() { return wave_all(umin(umin(min4(y[0]), min4(y[1])), umin(ym[0], ym[1])) != 0u); }
  __device__ __forceinline__ void run(int j, int lane, const bf16x8 SelLo) {
    const int c = lane & 15, g = lane >> 4;
    const f32x4 Z4 = {0.f, 0.f, 0.f, 0.f};
    const f32x4 d1 = mfma32(SelLo, as8(y[1]), mfma32(SelLo, as8(y[0]), Z4));
    const f32x4 d0 = mfma32(SelLo, mk8(ym[0], ym[1], 0u, 0u), Z4);
    const int qi = 4 * j + (c & 3), k0 = 16 * g + 4 * (c >> 2);
    float t[4], mv[4];
    float mx = -INFINITY;
#pragma unroll
    for (int r = 0; r < 4; ++r) {
      t[r] = (g < 2 && k0 + r <= qi) ? d1[r] : -INFINITY;
      const float mt = d0[r] + dppf<DPP_ROR8>(d0[r]);
      mv[r] = lane < 8 ? mt : -INFINITY;
      mx = fmx(mx, fmx(t[r], mv[r]));
    }
    mx = fmx(mx, dppf<DPP_ROR8>(mx)); mx = fmx(mx, dppf<DPP_ROR4>(mx));
    mx = x16mx(mx);
    float sum = 0.f;
#pragma unroll
    for (int r = 0; r < 4; ++r) {
      t[r] = __builtin_amdgcn_exp2f(t[r] - mx); mv[r] = __builtin_amdgcn_exp2f(mv[r] - mx);
      sum += t[r] + mv[r];
    }
    sum += dppf<DPP_ROR8>(sum); sum += dppf<DPP_ROR4>(sum);
    sum = x16sum(sum);
    const float rs_ = __builtin_amdgcn_rcpf(sum);
    res = (u32x4){pack2n(t[0] * rs_, t[1] * rs_), pack2n(t[2] * rs_, t[3] * rs_), pack2n(mv[0] * rs_, mv[1] * rs_), pack2n(mv[2] * rs_, mv[3] * rs_)};
    PIN_V(res);
  }
  __device__ __forceinline__ void store(const __amdgpu_buffer_rsrc_t rs, unsigned o, unsigned no = 0u) {
    SC1_STORE8S(((u32x2){res[0], res[1]}), s0 | no, o);
    SC1_STORE8S(((u32x2){res[2], res[3]}), s1 | no, o);
  }
};

constexpr int L_DRN = L_VOX + (NSEG % NV) * 5120;
__device__ __forceinline__ void drain_pre(char* lds, int b, int lane) {
  const int gp = (b & 7) * 2 + (b >> 7), m = (b >> 3) & 15;
  TokTask tk; PvTask2 pv; OutTask ot;
  tk.init(2 * gp + (m >> 3), m & 7, lane); pv.init(gp, lane); ot.init(b, lane);
  u32x4* p = (u32x4*)(lds + L_DRN) + lane;
  p[0] = (u32x4){tk.l0, tk.lm, tk.s0, tk.s1}; p[64] = (u32x4){pv.lo, pv.lm, pv.vr, pv.ma}; p[128] = (u32x4){pv.mz, pv.so, ot.t5f, ot.t5o};
  p[192] = __builtin_bit_cast(u32x4, ot.sel);
}
__device__ __forceinline__ void qkv_pre(unsigned (&vo)[2][2], int b, int w, int lane) {
  const int cb = qkv_col(b), c = lane & 15, g = lane >> 4, t0 = w & 1, col0 = (cb % 5) * 128 + w * 32;
  const bool SW = cb < 10;
#pragma unroll
  for (int i = 0; i < 2; ++i)
#pragma unroll
    for (int tt = 0; tt < 2; ++tt) {
      const int tq = tt ^ t0, tk = tq * 16 + c, cc = col0 + i * 16 + (SW ? 4 * g : c), hd = cc / BD, e = cc - hd * BD;
      const int kv = (int)(OFF_KV - OFF_QB) + hd * 64 * KVT;
      vo[i][tt] = (unsigned)(!SW ? kv + 1280 + tq * 672 + ((g >> 1) * 21 + e) * 16 + (g & 1) * 8
                                 : (cb < 5 ? (hd * S_ + tk) * 2 * QST + e * 2 : kv + (e < 16 ? (tk + 32 * (e >> 3)) * 16 + (e & 7) * 2 : 1024 + tk * 8)));
      PIN_V(vo[i][tt]);
    }
}

template <int w>
__device__ __forceinline__ void token_waves(const RecArgs& a, char* lds, const __amdgpu_buffer_rsrc_t rs, unsigned (&vo)[2][2]) {
  const int lane0 = __builtin_amdgcn_workitem_id_x() & 63, b = __builtin_amdgcn_workgroup_id_x(), wid = w + 4; (void)wid;
  const int gp = (b & 7) * 2 + (b >> 7), m = (b >> 3) & 15, h0 = 2 * gp, h = h0 + (m >> 3), j = m & 7;
  float* const misc = (float*)(lds + L_MISC);
  const f32x4 Z4 = {0.f, 0.f, 0.f, 0.f};
  const unsigned lds0 = (unsigned)(uintptr_t)(LAS char*)lds;
  unsigned* const cnt = (unsigned*)(misc + 6);
  const float* const xnrm = (const float*)(a.ws + OFF_XNRM);
  bf16x16 W[2][10];
  auto btile = [&](int i) { return w == 1 ? 0 : (w == 2 ? 1 + i : 3 + i); };
  if (w == 1) code_touch(b, lane0, lds0);
  Place pl;
  unsigned phi[2][2], ph[2][2][2];
#pragma unroll
  for (int k = 0; k < 8; ++k) {
    const int bt = btile(k >> 2), hw = h0 + (bt == 2 ? k & 1 : bt / 3), T = bt == 2 ? 32 + (m >> 1) : (bt % 3) * 16 + m;
    ph[k >> 2][(k >> 1) & 1][k & 1] = ((const __attribute__((address_space(4))) unsigned*)(a.ws + OFF_PHI))[(hw >> 2) | (hw & 3) << 3 | (k & 2) << 4 | (T / 10) << 6];
  }
  __builtin_amdgcn_sched_barrier(0);
  if (w == 3) pl.load(a.ws, lane0);
  if (a.layer) {
    if (w != 0 && FR_HOLD) wait_until(now32() + FR_HOLD);
    if (w != 0) img_fetch<20>(a, lds0, h, j, w - 1, lane0);
  } else {
    if (w == 0) img_fetch<12>(a, lds0, h, j, 4, lane0);
#pragma unroll
    for (int i = 0; i < 10; ++i) x_fetch(x_src(a, 0, b), lds0, 0, 10 * w + i, lane0);
  }
  const __amdgpu_buffer_rsrc_t rwb = __builtin_amdgcn_make_buffer_rsrc(a.ws + OFF_WB, 0, (int)WB_BYTES, 0x00020000);
#pragma unroll
  for (int k = 0; k < 4; ++k) phi[k >> 1][k & 1] = (((lane0 & 8) ? ph[k >> 1][k & 1][1] : ph[k >> 1][k & 1][0]) & 8192u) | (unsigned)(lane0 * 16);
#pragma unroll
  for (int i = 0; i < 2; ++i)
#pragma unroll
    for (int p = 0; p < 10; ++p)
      if (w >= 2 || (w == 1 && i == 1)) {
        const unsigned wo = wb_off((b * 5 + btile(i)) * 20 + 2 * p), wv = phi[i][p / 5] ^ (wo & 8192u);
        if (FR_PACE && a.layer) asm volatile("s_waitcnt vmcnt(%0)" :: "n"(FR_PACE) : "memory");
        W[i][p] = cat16(as8(__builtin_amdgcn_raw_buffer_load_b128(rwb, wv, wo & ~8192u, 0)), as8(__builtin_amdgcn_raw_buffer_load_b128(rwb, wv + 1024, wo & ~8192u, 0)));
      }
  f32x4 bn = Z4;
  if (w == 0) {
    bn = *(const f32x4*)(a.ws + OFF_BNRM + lane0 * 16);
    if (!a.layer) asm volatile("s_waitcnt vmcnt(0)" ::: "memory");
  } else asm volatile("s_waitcnt vmcnt(%0)" :: "n"(w == 1 ? 20 : 40) : "memory");
  unsigned ck = 0u;
  if (w == 3) { ck = pl.get(b, lane0); *(unsigned*)(misc + 11) = ck; }
  BAR();
#pragma unroll
  for (int p = 0; p < 10; ++p) {
    if (w < 2) W[0][p] = cat16(wqk_frag(lds, 2 * w, 2 * p, lane0), wqk_frag(lds, 2 * w, 2 * p + 1, lane0));
    if (w == 0) W[1][p] = cat16(wqk_frag(lds, 1, 2 * p, lane0), wqk_frag(lds, 1, 2 * p + 1, lane0));
  }
  float cb2 = 0.f;
  if (w == 0) {
    float n = __builtin_amdgcn_sqrtf(bn[0] + bn[1]) + __builtin_amdgcn_sqrtf(bn[2] + bn[3]);
    n += dppf<DPP_XOR1>(n); n += dppf<DPP_XOR2>(n); n += dppf<DPP_ROR4>(n); n += dppf<DPP_ROR8>(n);
    n = x32mx(x16mx(n));
    cb2 = u2f(__builtin_amdgcn_readfirstlane(f2u(1.15f * n * n)));
  }
  const float cm2 = u2f((f2u(cb2) & 0x7fffffu) | 0x40000000u);
  const int ek = imax((int)((f2u(cb2) >> 23) & 0xffu), 1) - 252;
  if (w >= 2) {
#pragma unroll
    for (int i = 0; i < 4; ++i) *(unsigned*)(lds + L_KX + (w - 2) * 4096 + i * 1024 + lane0 * 16 + 12) = 0u;
  }
  OutTask ot;
  const int zl = lane0 - 16;
  const bool zon = w == 3 && zl >= 0 && (zl < 40 || b < ACC_NB + 17);
  unsigned zo = zon ? zero_base(b, h, j, zl) + (zl < 32 ? sct_slot(ck, 0u) : (zl < 40 ? scm_slot(ck, 0u) : 0u)) : 0x7ffffff0u;
  unsigned zm = zon ? (zl < 32 ? SCT_STEP + 1u : (zl < 40 ? SCM_STEP : (unsigned)ACC_STEP)) : 0u;
  const unsigned zx = ((ck ^ (ck >> 4)) & 7u) * 4096u;
  if (w == 3) { PIN_V(zo); PIN_V(zm); }
  const __amdgpu_buffer_rsrc_t rx = __builtin_amdgcn_make_buffer_rsrc(a.ws + OFF_XNRM, 0, (int)(OFF_QB - OFF_XNRM), 0x00020000);
  u32x4 v0, v1;
  f32x4 d5;
  unsigned oo, za, xa, ya, n4, so_op, so_y, so_x;
  float xn4 = 0.f;
  auto w1_part = [&](int k, int s, int lane) {
    if (w < 2) return;
    if (w == 2) {
      if (k >= 1 && k <= 4) d5 = mfma32(ot.sel, as8(ot.y[k < 1 || k > 4 ? 0 : k - 1]), k == 1 ? Z4 : d5);
      if (k == 6 || k == 7) {
        const unsigned r = pack2(d5[2 * (k & 1)], d5[2 * (k & 1) + 1]), x = ot.xo[k & 1];
        ot.out[k & 1] = pack2(u2f(x << 16) + u2f(r << 16), u2f(x & 0xffff0000u) + u2f(r & 0xffff0000u));
        PIN_V(ot.out);
      }
      if (k == 8) xn4 = fmx(xn4, dppf<DPP_XOR1>(xn4));
      if (k == 9) xn4 = fmx(xn4, dppf<DPP_XOR2>(xn4));
      if (k == 10) xn4 = fmx(xn4, dppf<DPP_ROR4>(xn4));
      if (k == 11) xn4 = fmx(xn4, dppf<DPP_ROR8>(xn4));
      if (k >= 8 && k <= 11) PIN_V(xn4);
      if (k == 12) misc[s & 3] = x16mx(xn4);
      if (k == 5) { so_y = op_slot(imax(s - 1 - LAG5, 0)); so_x = (unsigned)imax(s - 1 - LAG5, 0) * 40960u; PIN_S(so_y); PIN_S(so_x); }
      if (k == 14) {
        ya = ot.t5o | (s <= LAG5 ? 0x7ff00000u : 0u);
        xa = (ot.t5f * 2u | (unsigned)(lane & 32) << 25) | (OutTask::ok(s - 2 - LAG5, b) ? 0u : 0x7ff00000u);
        PIN_V(ya); PIN_V(xa);
      }
    } else if (k == 8) {
      const unsigned zs = (unsigned)(s + 2) % SR;
      za = (zo + (s < 0 ? 1024u : zs) * (zm & ~1u)) ^ ((zm & 1u) * (zs & 4u ? zx : 0u));
      PIN_V(za);
    }
    if (k == 13) {
      n4 = __hip_atomic_load((unsigned*)(misc + 28), __ATOMIC_RELAXED, __HIP_MEMORY_SCOPE_WORKGROUP);
      so_op = s < 2 + LAG4 ? 0x7ff00000u : op_slot(s - 2 - LAG4) + (unsigned)((m * 32 + (w & 1) * 16 + gp) * 1280);
      PIN_S(so_op);
    }
    if (k == 15 && __builtin_expect((int)__builtin_amdgcn_readfirstlane(n4) < s, 0)) lds_spin<L_MISC + 112, 1>(lds0, (unsigned)s);
    if (k == 16) {
      asm volatile("" ::: "memory");
      const char* sp = lds + L_STG + 2048 + (w & 1) * 1280;
      v0 = *(const u32x4*)(sp + lane * 16); v1 = *(const u32x4*)(sp + 1024 + (lane & 15) * 16);
      oo = so_op + (unsigned)(lane * 16);
      za = lane < 16 ? oo + 1024 : (w == 3 ? za : 0x7ffffff0u);
      PIN_V(oo); PIN_V(za);
    }
    if (k == 19 && w == 3) {
#pragma unroll
      for (int i = 0; i < 4; ++i) { v1[i] = lane < 16 ? v1[i] : 0u; PIN_V(v1[i]); }
    }
    __builtin_amdgcn_sched_barrier(0);
  };
  auto w1_fire = [&](int s, int lane, auto last) {
    if (w < 2) return;
    lds_spin<L_MISC + 92, W1_SL>(lds0, 4u * (unsigned)imax(s, 0));
    SC1_STORE(v0, oo);
    SC1_STORE(v1, za);
    __builtin_amdgcn_sched_barrier(0);
    if (w == 2) {
      __builtin_amdgcn_raw_buffer_store_b64(ot.out, rx, xa, so_x + (unsigned)(OFF_XA - OFF_XNRM - 40960), 16);
      if (last == 0) {
#pragma unroll
        for (int k = 0; k < 4; ++k) ot.y[k] = SC1_LOADS(ya, so_y + k * 5120);
        ot.xo = __builtin_amdgcn_raw_buffer_load_b64(rx, ot.t5f * 2u, so_x + (unsigned)(OFF_XB - OFF_XNRM), 16);
        xn4 = u2f(__builtin_amdgcn_raw_buffer_load_b32(rx, (lane & 31) * 16, (s + 1) * 512, 16));
      }
    } else {
      const unsigned xo_ = (unsigned)((s - 8) * 1024 + lane * 16);
      if (__builtin_expect((unsigned)(s - 8) < 20u, 0)) SC1_STORE(((u32x4){0u, 0u, 0u, 0u}), xo_ < X_BYTES / 256 ? (unsigned)(OFF_X + b * (X_BYTES / 256)) + xo_ : 0x7ffffff0u);
    }
    __builtin_amdgcn_sched_barrier(0);
  };
  STAMP_DECL(); ARRIVE_DECL();
  auto mma_seg = [&](int seg, int sv, int lane, auto pause) {
    const int c = lane & 15, g = lane >> 4;
    const char* xp = lds + L_XN + (seg & 1) * 40960 + lane * 16;
    f32x4 acc[2][2] = {{Z4, Z4}, {Z4, Z4}};
    bf16x8 x[4][2];
#pragma unroll
    for (int p = 0; p < 3; ++p) { x[p][0] = *(const bf16x8*)(xp + p * 1024); x[p][1] = *(const bf16x8*)(xp + (20 + p) * 1024); }
    unsigned eo[2];
#pragma unroll
    for (int ks = 0; ks < 20; ++ks) {
      if (ks == (w == 0 ? KP0 : (w == 1 ? KP1 : KP2))) { pause(); __builtin_amdgcn_sched_barrier(0); }
      if (ks == 17) {
#pragma unroll
        for (int i = 0; i < 2; ++i) {
          eo[i] = (unsigned)(w == 0 ? (i ? L_KX : L_QX) + (seg % NKQ) * 2048 + lane * 16
                                    : (w == 1 && i == 0 ? ((g >> 1) ? L_KX : L_QX) + (seg % NKQ) * 2048 + (2 * (g & 1) * 16 + c) * 16 + 8 : L_VOX + sv * 5120 + btile(i) * 1024 + lane * 16));
          if (i == 0 || w == 1) PIN_V(eo[i]);
        }
        if (w == 0) eo[1] = eo[0] + (L_KX - L_QX);
        if (w >= 2) eo[1] = eo[0] + 1024;
      }
      __builtin_amdgcn_sched_barrier(0);
#pragma unroll
      for (int q = 0; q < 4; ++q) {
        const int tt = 1 - (q >> 1), i = q & 1;
        const bool isqk = w == 0 || (w == 1 && i == 0);
        const bf16x8 wf = half8(W[i][ks >> 1], ks & 1);
        acc[i][tt] = isqk ? mfma32(wf, x[ks & 3][tt], acc[i][tt]) : mfma32(x[ks & 3][tt], wf, acc[i][tt]);
        __builtin_amdgcn_sched_barrier(0);
        if (q < 2 && ks + 3 < 20) x[(ks + 3) & 3][q] = *(const bf16x8*)(xp + (q * 20 + ks + 3) * 1024);
        if (q == 2) w1_part(ks, seg - 1, lane);
        __builtin_amdgcn_sched_barrier(0);
      }
    }
#pragma unroll
    for (int i = 0; i < 2; ++i) {
      if (w + i == 1) { const float ks_ = (w == 0 || (g >> 1)) ? KEXP : 1.0f; acc[i][0] *= ks_; acc[i][1] *= ks_; }
      const u32x4 v = {pack2(acc[i][0][0], acc[i][0][1]), pack2(acc[i][0][2], acc[i][0][3]), pack2(acc[i][1][0], acc[i][1][1]), pack2(acc[i][1][2], acc[i][1][3])};
      char* p = lds + eo[i];
      if (w == 0) { *(u32x2*)(p) = (u32x2){v[0], v[1]}; *(u32x2*)(p + 1024) = (u32x2){v[2], v[3]}; }
      else if (w == 1 && i == 0) { *(unsigned*)(p) = v[0]; *(unsigned*)(p + 256) = v[1]; *(unsigned*)(p + 1024) = v[2]; *(unsigned*)(p + 1280) = v[3]; }
      else *(u32x4*)p = v;
    }
  };
  unsigned vp, mp, ip;
  int sidx, ei = 127;
  float inv;
  f32x4 acc[2];
  auto vo_pre = [&](int s, int sv, int lane) {
    vp = (unsigned)(w == 0 ? L_MISC + (s & 3) * 4 : L_VOM + sv * 1280 + (btile(0) * 32 + (w == 1 ? lane & 31 : lane)) * 8);
    mp = (unsigned)(L_BM + lane * 16);
    PIN_V(vp); PIN_V(mp);
    if (w == 0) { ip = (unsigned)(L_MISC + 16 + ((s + 1) & 1) * 4); PIN_V(ip); }
    else {
      inv = misc[4 + (s & 1)];
      sidx = (lane & 8) ? 0xEEEE : 0x4444;
      acc[0] = acc[1] = Z4;
      PIN_V(sidx); PIN_V(acc[1]);
      if (w >= 2) { PIN_V(acc[0]); PIN_V(inv); }
    }
    __builtin_amdgcn_sched_barrier(0);
  };
  auto vo_mem = [&](int s, auto last) {
    __builtin_amdgcn_s_setprio(2);
    asm volatile(".rept %0\n\ts_nop 3\n\t.endr" :: "n"(w == 0 ? VO_A0 : (w == 1 ? VO_A1 : VO_A2)) : "memory");
    float xmax = 0.f;
    if (w == 0) xmax = *(const float*)(lds + vp);
    constexpr int NX = w == 2 ? VO_N2 : (w == 3 ? VO_N3 : 10);
    bf16x8 x[10];
#pragma unroll
    for (int p = 0; p < NX; ++p) x[p] = *(const bf16x8*)(lds + mp + p * 1024);
    __builtin_amdgcn_sched_barrier(0);
    if (w < 2) asm volatile(".rept %0\n\ts_nop 3\n\t.endr" :: "n"(w == 0 ? VO_B0 : VO_B1) : "memory");
    if (VO_W >= 0) asm volatile("s_waitcnt lgkmcnt(%0)" :: "n"(VO_W < 0 ? 0 : VO_W) : "memory");
    STAMP(5);
    __builtin_amdgcn_sched_barrier(0);
    int ex = 0;
#pragma unroll
    for (int p = 0; p < 10; ++p) {
      if (w == 0) acc[0] = mfma32(x[p], x[p], p ? acc[0] : Z4);
      if (w == 0 && p == 9) { ex = imax((int)((__builtin_amdgcn_readfirstlane(f2u(__builtin_amdgcn_sqrtf(cb2 * xmax))) >> 23) & 0xffu) + 1, 32); PIN_S(ex); }
#pragma unroll
      for (int i = (w == 1); i < 2 && w != 0; ++i) acc[i] = smfma64(x[p], W[i][p], acc[i], sidx);
      __builtin_amdgcn_sched_barrier(0);
      if (p + NX < 10) x[p + NX] = *(const bf16x8*)(lds + mp + (p + NX) * 1024);
      __builtin_amdgcn_sched_barrier(0);
    }
    STAMP(7);
    if (w != 0) {
      float v[4];
#pragma unroll
      for (int r = 0; r < 4; ++r) {
        const auto sw = __builtin_amdgcn_permlane32_swap(f2u(acc[w == 1][r]), f2u(acc[1][r]), false, false);
        v[r] = (u2f(sw[0]) + u2f(sw[1])) * inv;
      }
      *(u32x2*)(lds + vp) = (u32x2){pack2(v[0], v[1]), pack2(v[2], v[3])};
    } else {
      const int t = (int)f2u(fmx(fmx(acc[0][0], acc[0][1]), fmx(acc[0][2], acc[0][3])) * cm2);
      const int et = wave_imax(t) >> 23, em = ((et + ek) >> 1) + ei;
      int e = imax(et == 255 ? 256 : em, ex);
      PIN_S(e);
      e = imin(e, 256);
      ei = e - 31;
      *(unsigned*)(lds + ip) = (unsigned)ei << 23;
      const u32x3 sr = {(unsigned)(285 - e) << 23, (unsigned)(e - 87) < 79u ? REC_RNG << 23 : 0u, (unsigned)(s + 1)};
      asm volatile("ds_write_b96 %0, %1 offset:%2" :: "v"(lds0), "v"(sr), "n"(L_MISC + 32) : "memory");
    }
    if (last == 0 && w >= 2) asm volatile("s_waitcnt vmcnt(%0)" :: "n"(w == SCH_W ? 2 : 0) : "memory");
    if (w != 0) lds_inc<L_MISC + 28>(lds0);
    STAMP(8);
    if (last) __builtin_amdgcn_s_setprio(0);
    if (last && w == 2) { VM_DRAIN(); lds_inc<L_MISC + 28>(lds0); }
  };
  unsigned* const cntq = (unsigned*)(misc + 12);
  unsigned* const cntt = (unsigned*)(misc + 26 + (w & 1));
  unsigned* const cntp = (unsigned*)(misc + 23);
  unsigned* const cntx2 = (unsigned*)(misc + 28);
  auto wpf = [&](int s, int lane) {
    constexpr int PER = 81920 / QR / WPF_N;
    static_assert(PER % WPF_S == 0 && PER / WPF_S <= 64, "prefetch");
    touch(a.ws + OFF_WQKVBT + (size_t)imin(qkv_col(b), 14) * 163840 + (qkv_row(b) % (2 * QR)) * (81920 / QR), (s - WPF0) * PER + imin(lane, PER / WPF_S - 1) * WPF_S, lds0);
  };
  Sched sch;
  auto late = [&](int s, int lane) {
    if (__builtin_expect(s < 0, 0)) return;
    if (w == SCH_W && SCH_LATE && __builtin_expect(s >= 0, 1)) *(u32x2*)(misc + 24) = (u32x2){sch.run(a, rs, s, b, lane), (unsigned)(s + 1)};
    if ((w == 2 && F6) || (w == 0 && F4) || (w == 1 && F5) || (w == 3 && F7)) { lds_spin<L_MISC + 48, 1>(lds0, 2u * (unsigned)imax(s + 1, 0)); if (FB_SL && w != 2) __builtin_amdgcn_s_sleep(FB_SL); }
    if (w >= 2) { x_fetch_n<w == 2 ? F6 : F7>(x_src(a, s + 3, b), lds0, s + 3, F4 + F5 + 2 * F67 + (w == 3 ? F6 : 0), lane * 16); return; }
    unsigned sd = 0u;
    if ((w == 0 && XPF == 3) || XPF == 4 || (w == 1 && XPF == 5)) stouch<10>(a.ws + OFF_XN + (size_t)imin(s + 4, NSEG - 1) * (s < 1 ? 0 : 40960) + (b >> 3) * 1280 + w * 64, sd);
    x_fetch_n<w == 0 ? F4 : F5>(x_src(a, s + 3, b), lds0, s + 3, w == 0 ? 0 : F4, lane * 16);
    if (WPF_W == 0 && w == 0 && __builtin_expect(s >= WPF0 && s < WPF0 + WPF_N, 0)) wpf(s, lane);
    if (w == 1 && __builtin_expect(s == CPF_S, 0)) code_touch(b, lane, lds0);
    lds_spin<L_MISC + 104 + 4 * (w & 1), PS_SL>(lds0, (unsigned)imax(s + 1, 0));
    asm volatile("" :: "s"(sd));
  };
  if (w == 2) { ot.init(b, lane0); ot.out = (u32x2){0u, 0u}; ot.xo = ot.out; ot.y[0] = ot.y[1] = ot.y[2] = ot.y[3] = (u32x4){0u, 0u, 0u, 0u}; }
  VM_DRAIN();
  lds_arrive(cnt, lane0);
  lds_wait(cnt, 8u);
  sch.tsv = sch.rec = (u32x4){0u, 0u, 0u, 0u};
  sch.side = __builtin_amdgcn_readfirstlane(*(const unsigned*)(misc + 11)) & 8192u;
  sch.o = 0u;
  int sv = NV - 1;
  CODE_PAD((PADT >> (4 * w)) & 15);
#pragma unroll 1
  for (int s = -1; s < NSEG - 1; ++s) {
    const int sv1 = sv == NV - 1 ? 0 : sv + 1;
    FRESH_LANE();
    STAMP(0);
    mma_seg(s + 1, sv1, lane, [&]() { STAMP(1); late(s - 1, lane); if (w == SCH_W) sch.pre(s); STAMP(9); });
    STAMP(2);
    __builtin_amdgcn_sched_barrier(0);
    VM_DRAIN();
    STAMP(10);
    w1_fire(s, lane, std::integral_constant<int, 0>{});
    if (w >= 2) vo_pre(s, sv, lane);
    STAMP(3);
    BAR();
    STAMP(4);
    if (__builtin_expect(s >= 0, 1)) {
      if (w == SCH_W) { sch.load(rs, lane0); __builtin_amdgcn_sched_barrier(0); }
      if (w < 2) vo_pre(s, sv, lane);
      vo_mem(s, std::integral_constant<int, 0>{});
      STAMP(6);
      if (w == SCH_W && !SCH_LATE) {
        __builtin_amdgcn_s_setprio(SCH_P);
        *(u32x2*)(misc + 24) = (u32x2){sch.run(a, rs, s, b, lane0), (unsigned)(s + 1)};
      }
      STAMP(11);
    }
    __builtin_amdgcn_s_setprio(MMA_P);
    if (w == 2) VM_DRAIN();
    __builtin_amdgcn_sched_barrier(0);
    sv = sv1;
  }
  __builtin_amdgcn_s_setprio(0);
  STAMP_OUT();
  late(NSEG - 2, lane0);
  { FRESH_LANE(); if (w == 0) drain_pre(lds, b, lane); qkv_pre(vo, b, w, lane); }
#pragma unroll
  for (int k = 0; k < 20; ++k) w1_part(k, NSEG - 1, lane0);
  w1_fire(NSEG - 1, lane0, std::integral_constant<int, 1>{});
  if (w != 0) vo_pre(NSEG - 1, (NSEG - 1) % NV, lane0);
  BAR();
  if (w != 0) vo_mem(NSEG - 1, std::integral_constant<int, 1>{});
}

__device__ __forceinline__ void qkv_tail(const RecArgs& a, char* lds, const int w, const unsigned (&vo)[2][2]) {
  const int lane0 = __builtin_amdgcn_workitem_id_x() & 63, b = __builtin_amdgcn_workgroup_id_x();
  const int r = qkv_row(b), cb = qkv_col(b);
  if (cb >= 15) return;
  const unsigned lds0 = (unsigned)(uintptr_t)(LAS char*)lds;
  unsigned* const stage = (unsigned*)(lds + L_MISC) + 17;
  unsigned* const cnty = stage + 1;
  const f32x4 Z4 = {0.f, 0.f, 0.f, 0.f};
  auto fetch = [&](int i) {
    x_fetch_n<10>(x_src(a, NSEG + i, b), lds0, i, 10 * w, lane0 * 16);
  };
  unsigned want = 0;
  auto sync = [&]() { VM_DRAIN(); want += 4u; lds_inc<L_MISC + 72>(lds0); while (__hip_atomic_load(cnty, __ATOMIC_RELAXED, __HIP_MEMORY_SCOPE_WORKGROUP) < want) { } asm volatile("" ::: "memory"); };
  LOGQ(0);
  bf16x8 W[2][20];
  const __amdgpu_buffer_rsrc_t rw = __builtin_amdgcn_make_buffer_rsrc(a.ws + OFF_WQKVBT + (size_t)(cb * 8 + 2 * w) * 20480, 0, 40960, 0x00020000);
  auto w_get = [&](int ks) {
#pragma unroll
    for (int i = 0; i < 2; ++i) W[i][ks] = as8(__builtin_amdgcn_raw_buffer_load_b128(rw, lane0 * 16 + (ks & 3) * 1024, (i * 20 + (ks & ~3)) * 1024, 0));
    __builtin_amdgcn_sched_barrier(0);
  };
#pragma unroll
  for (int ks = 0; ks < QW; ++ks) w_get(ks);
  bf16x8 ones = mk8(0x3f803f80u, 0x3f803f80u, 0x3f803f80u, 0x3f803f80u);
  PIN_V(ones);
  const __amdgpu_buffer_rsrc_t ro = __builtin_amdgcn_make_buffer_rsrc(a.ws + OFF_QB, 0, (int)(OFF_OB - OFF_QB), 0x00020000);
  const int t0 = w & 1;
  auto body = [&](auto SW, auto SQ) {
    const float qs = cb < 5 ? 0.22360679774997896f * 1.4426950408889634f : 1.0f;
    f32x4 cs[2] = {Z4, Z4};
    LOGQ(1);
    f32x4 acc[2][2], st;
    auto mma = [&](int it, auto FIRST) {
      const char* xp0 = lds + L_XN + (it & 1) * 40960 + t0 * 20480 + lane0 * 16;
      const char* xp1 = lds + L_XN + (it & 1) * 40960 + (1 - t0) * 20480 + lane0 * 16;
      acc[0][0] = acc[0][1] = acc[1][0] = acc[1][1] = st = Z4;
      bf16x8 x[4][2];
#pragma unroll
      for (int p = 0; p < 3; ++p) { x[p][0] = *(const bf16x8*)(xp0 + p * 1024); x[p][1] = *(const bf16x8*)(xp1 + p * 1024); }
      __builtin_amdgcn_sched_barrier(0);
#pragma unroll
      for (int ks = 0; ks < 20; ++ks) {
        if (FIRST && ks + QW < 20) w_get(ks + QW);
#pragma unroll
        for (int q = 0; q < 4; ++q) {
          const int tt = 1 - (q >> 1), i = 1 - (q & 1);
          acc[i][tt] = SW ? mfma32(W[i][ks], x[ks & 3][tt], acc[i][tt]) : mfma32(x[ks & 3][tt], W[i][ks], acc[i][tt]);
          __builtin_amdgcn_sched_barrier(0);
          if (q < 2 && ks + 3 < 20) x[(ks + 3) & 3][q] = *(const bf16x8*)((q ? xp1 : xp0) + (ks + 3) * 1024);
          __builtin_amdgcn_sched_barrier(0);
        }
        st = mfma32(SQ ? x[ks & 3][0] : ones, x[ks & 3][0], st);
        if (FIRST) {
#pragma unroll
          for (int i = 0; i < 2; ++i) cs[i] = SW ? mfma32(W[i][ks], ones, cs[i]) : mfma32(ones, W[i][ks], cs[i]);
        }
        __builtin_amdgcn_sched_barrier(0);
      }
    };
    mma(0, std::integral_constant<bool, true>{});
#pragma unroll 1
    for (int it = 0; it < 4; ++it) {
      if (it) mma(it, std::integral_constant<bool, false>{});
      LOGQ(2 + 3 * it);
      FRESH_LANE();
      float* const sp = (float*)(lds + L_ST) + (it & 1) * 64;
      if (SQ) { if ((c >> 2) == g) sp[32 + t0 * 16 + c] = (c & 3) == 0 ? st[0] : ((c & 3) == 1 ? st[1] : ((c & 3) == 2 ? st[2] : st[3])); }
      else if (g == 0) sp[t0 * 16 + c] = st[0];
      sync();
      LOGQ(3 + 3 * it);
      if (it == 0) fetch(2);
      const bool late = r + 48 >= 55;
      if (it == 1 && !late) fetch(3);
      const unsigned so = (unsigned)((r + 16 * it) * (cb < 5 ? 64 * QST : KVT));
      auto stat = [&](float sm, float sq, float& mean, float& rstd) {
        mean = sm * (1.0f / DM);
        rstd = __builtin_amdgcn_rsqf(__builtin_fmaxf(sq * (1.0f / DM) - mean * mean, 0.f) + 1e-5f);
      };
#pragma unroll
      for (int tt = 0; tt < 2; ++tt) {
        const int tq = tt ^ t0;
        float mean[4], rstd[4];
        if (SW) {
          stat(sp[tq * 16 + c], sp[32 + tq * 16 + c], mean[0], rstd[0]);
          rstd[0] *= qs;
        } else {
          const f32x4 sm = *(const f32x4*)(sp + tq * 16 + 4 * g), sq = *(const f32x4*)(sp + 32 + tq * 16 + 4 * g);
#pragma unroll
          for (int k = 0; k < 4; ++k) stat(sm[k], sq[k], mean[k], rstd[k]);
        }
#pragma unroll
        for (int i = 0; i < 2; ++i) {
          float v[4];
#pragma unroll
          for (int k = 0; k < 4; ++k) v[k] = (acc[i][tt][k] - mean[SW ? 0 : k] * cs[i][SW ? k : 0]) * rstd[SW ? 0 : k];
          __builtin_amdgcn_raw_buffer_store_b64((u32x2){pack2(v[0], v[1]), pack2(v[2], v[3])}, ro, vo[i][tt], so, Q_AUX);
        }
      }
      LOGQ(4 + 3 * it);
      if (it == 2 && late) lds_spin<L_MISC + 68, 0>(lds0, 5u);
    }
  };
  typedef std::integral_constant<bool, false> F_; typedef std::integral_constant<bool, true> T_;
  if (cb < 10) { if (w >> 1) body(T_{}, T_{}); else body(T_{}, F_{}); }
  else { if (w >> 1) body(F_{}, T_{}); else body(F_{}, F_{}); }
}

__device__ __forceinline__ void t2_mem(const bf16x8 fr, const bf16x8 q0, const bf16x8 q1, const __amdgpu_buffer_rsrc_t rs, unsigned o, int h, int j, float inv, int lane) {
  const f32x4 Z4 = {0.f, 0.f, 0.f, 0.f};
  const f32x4 d0 = mfma32(fr, q0, Z4) * inv, d1 = mfma32(fr, q1, Z4) * inv;
  const unsigned a0 = pack2n(d0[0], d0[1]), a1 = pack2n(d0[2], d0[3]), b0 = pack2n(d1[0], d1[1]), b1 = pack2n(d1[2], d1[3]);
  const auto r0 = __builtin_amdgcn_permlane32_swap(a0, b0, false, false), r1 = __builtin_amdgcn_permlane32_swap(a1, b1, false, false);
  SC1_STORE8S(((u32x2){r0[1], r1[1]}), ts_mem(h, j) + (lane >> 5) * 256 + (lane & 12) * 16 + (lane & 16) * 2 + (lane & 3) * 8, o);
}

template <int w>
__device__ __forceinline__ void rec_waves(const RecArgs& a, char* lds, __amdgpu_buffer_rsrc_t rs, unsigned& tlast) {
  PIN_S(rs);
  const int lane = __builtin_amdgcn_workitem_id_x() & 63, b = __builtin_amdgcn_workgroup_id_x(), c = lane & 15, g = lane >> 4, wid = w; (void)wid;
  const int gp = (b & 7) * 2 + (b >> 7), m = (b >> 3) & 15, h0 = 2 * gp, h = h0 + (m >> 3), j = m & 7;
  float* const misc = (float*)(lds + L_MISC);
  const f32x4 Z4 = {0.f, 0.f, 0.f, 0.f};
  LOGP(0);
  __builtin_amdgcn_s_setprio(3);
  const unsigned lds0 = (unsigned)(uintptr_t)(LAS char*)lds;
  if (!a.layer) img_fetch<12>(a, lds0, h, j, w, lane);
  constexpr int NC = w < 2 ? 2 : 3;
  const int far = ((b >> 2) & 1) ^ 1;
  auto chunk = [&](int f) { return f == 0 ? 4 * far + w : (w == 2 && f == 1 ? 8 + far : (w == 3 && f == 2 ? 9 - far : 4 - 4 * far + w)); };
  bf16x16 Wqk[3][NC];
  u32x4 bm0[NC][2];
#pragma unroll
  for (int f = 0; f < 2 * NC; ++f) bm0[f >> 1][f & 1] = *(const u32x4*)(a.mem0 + (size_t)(c & 7) * DM + 64 * chunk(f >> 1) + 16 * g + 8 * (f & 1));
  if (!a.layer) asm volatile("s_waitcnt vmcnt(0)" ::: "memory");
  BAR();
  LOGP(9);
  const unsigned ck = __builtin_amdgcn_readfirstlane(*(const unsigned*)(misc + 11));
  const unsigned flip = (ck ^ (unsigned)(b << 11)) & 8192u;
#pragma unroll
  for (int t = 0; t < 3; ++t)
#pragma unroll
    for (int f = 0; f < NC; ++f) Wqk[t][f] = cat16(wqk_frag(lds, t, 2 * chunk(f), lane), wqk_frag(lds, t, 2 * chunk(f) + 1, lane));
  if (a.layer) {
    if (w == 0) {
      for (unsigned spins = 0; spins < SPIN_CAP; ++spins) {
        if (__builtin_amdgcn_raw_buffer_load_b32(rs, (b & 7) * 256, (unsigned)OFF_RDY, 16) >= 16u) break;
        __builtin_amdgcn_s_sleep(4);
        asm volatile("" ::: "memory");
      }
      lds_inc<L_MISC + 84>(lds0);
    }
    lds_wait((unsigned*)(misc + 21), 2u);
#pragma unroll
    for (int f = 0; f < 2 * NC; ++f) PIN_V(bm0[f >> 1][f & 1]);
#pragma unroll
    for (int i = 0; i < 10; ++i) x_fetch(x_src(a, 0, b), lds0, 0, 10 * w + i, lane);
  }
  const bf16x8 SelLo = sel_frag(c, 0);
  bf16x8 ONES = mk8(0x3f803f80u, 0x3f803f80u, 0x3f803f80u, 0x3f803f80u);
  if (w < 2) PIN_V(ONES);
  bool dead = false;
  float inv = 0.f;
  unsigned* const cnt2 = (unsigned*)(misc + 7);
  unsigned l2off[2 * NC];
#pragma unroll
  for (int f = 0; f < 2 * NC; ++f) { l2off[f] = acc_off(16 * chunk(f >> 1) + 4 * g + 2 * (f & 1) + (c >> 3), c & 7) ^ flip; PIN_V(l2off[f]); }
  int sidx = (c >> 3) ? 0xEEEE : 0x4444;
  PIN_V(sidx);
  const unsigned p1off = (unsigned)((w == 0 ? (h & 2) * 1024 + j * 256 + (h & 1) * 64 + (g & 1) * 8 : (h & 3) * 32768 + j * 512 + (g >> 1) * 256 + (g & 1) * 64) + (c & 4) * 32 + (c & 3) * 16);
  unsigned p1v = (c < 8) && (w != 0 || g >= 2) ? p1off : 0x7ffffff0u, p1cur = 0x7ffffff0u;
  PIN_V(p1v); PIN_V(p1cur);
  const int hd = (c >> 2) & 1, mr = hd ? 3 - (c & 3) : (c & 3);
  const unsigned l1off = (unsigned)(((h0 + hd) & 3) * 32768 + g * 512 + (w & 1) * 128 + (c >> 3) * 64 + mr * 16);
  const unsigned l1offm = (unsigned)((h0 & 2) * 1024 + (g + 4 * (c >> 3)) * 256 + (w & 1) * 128 + hd * 64 + mr * 16);
  const int plane = (lane & ~6) | ((lane & 2) << 1) | ((lane & 4) >> 1);
  unsigned acco = c < 8 ? (acc_off(64 * far + m * 4 + g, 4 * (w & 1) + mr) + (unsigned)(hd * 8)) ^ flip : 0x7ffffff0u;
  unsigned acco2 = c >= 8 ? (acc_off(64 - 64 * far + m * 4 + g, 4 * (w & 1) + mr) + (unsigned)(hd * 8)) ^ flip
                          : (c < 4 ? (acc_off(128 + m * 2 + (g & 1), 4 * (w & 1) + mr) + (unsigned)((g >> 1) * 8)) ^ flip : 0x7ffffff0u);
  if (w < 2) { PIN_V(acco); PIN_V(acco2); }
  unsigned bmo[NC];
#pragma unroll
  for (int f = 0; f < NC; ++f) { bmo[f] = (unsigned)(chunk(f) * 1024 + lane * 16); PIN_V(bmo[f]); }
  bf16x8 bm[NC];
  auto bm_put = [&](int f) { *(bf16x8*)(lds + L_BM + bmo[f]) = bm[f]; __builtin_amdgcn_sched_barrier(0); };
  f32x4 acc[3] = {Z4, Z4, Z4};
  const int rt2 = ((c >> 3) * 8 + 4 * (g & 1)) * 28 + 16 + (c & 7);
  unsigned redo[3] = {(unsigned)(4 * g * 28 + c), (unsigned)(g < 2 ? (4 * g + 2) * 28 + c : rt2), (unsigned)(g < 2 ? (4 * g + 10) * 28 + c : rt2 + 56)};
#pragma unroll
  for (int k = 0; k < 3; ++k) { redo[k] = L_RED1 + w * 1792 + redo[k] * 4; PIN_V(redo[k]); }
  auto red_put = [&]() {
    auto hs = [&](float x, float y) { const auto sw = __builtin_amdgcn_permlane32_swap(f2u(x), f2u(y), false, false); return u2f(sw[0]) + u2f(sw[1]); };
#pragma unroll
    for (int k = 0; k < 3; ++k) {
#pragma unroll
      for (int i = 0; i < 2; ++i) *(float*)(lds + redo[k] + i * 112) = k == 0 ? hs(acc[0][i], acc[1][i]) : hs(acc[k - 1][2 + i], acc[2][2 * k - 2 + i]);
      __builtin_amdgcn_sched_barrier(0);
    }
  };
#pragma unroll
  for (int f = 0; f < NC; ++f) {
    const u32x4 u = bm0[f][0], v = bm0[f][1];
    bm[f] = c < 8 ? mk8(u[0], u[2], v[0], v[2]) : mk8(u[1], u[3], v[1], v[3]);
    bm_put(f);
#pragma unroll
    for (int t = 0; t < 3; ++t) acc[t] = smfma64(bm[f], Wqk[t][f], acc[t], sidx);
  }
  red_put();
  VM_DRAIN();
  lds_arrive((unsigned*)(misc + 6), lane);
  lds_wait((unsigned*)(misc + 6), 8u);
  x_fetch_n<10>(x_src(a, 1, b), lds0, 1, 10 * w, lane * 16);
  LOGP(1);
  STAMP_DECL(); ARRIVE_DECL();
  bf16x8 fr, qx[2];
  auto kq_get = [&](int seg) {
    const char* p = lds + (w == 1 ? L_KX : L_QX) + (seg % NKQ) * 2048 + lane * 16;
    if (w == 1 || w == 2) { qx[0] = *(const bf16x8*)p; qx[1] = *(const bf16x8*)(p + 1024); }
  };
  unsigned qo4 = (unsigned)(L_RED1 + (w == 1 ? (c & 7) * 112 + (c >> 3) * 3584 : c * 112) + g * 16), qo2 = qo4 + 64u - (unsigned)(g * 8), qo2b = qo2 + 3584u;
  if (w < 3) { PIN_V(qo4); PIN_V(qo2); if (w != 1) PIN_V(qo2b); }
  auto pub1 = [&](unsigned off, unsigned o1) {
    if (w == 0) fr = qk_frag(lds + qo4, lds + qo2, lds + qo2b);
    else {
      const f32x4 a0 = *(const f32x4*)(lds + qo4), a1 = *(const f32x4*)(lds + qo4 + 1792);
      const f32x2 b0 = *(const f32x2*)(lds + qo2), b1 = *(const f32x2*)(lds + qo2 + 1792);
      float v[6];
#pragma unroll
      for (int i = 0; i < 6; ++i) v[i] = i < 4 ? a0[i] + a1[i] : b0[i - 4] + b1[i - 4];
#pragma unroll
      for (int i = 0; i < 6; ++i) v[i] += dppf<DPP_ROR8>(v[i]);
      fr = mk8(pack2(v[0], v[1]), pack2(v[2], v[3]), pack2(v[4], v[5]), 0u);
    }
    const f32x4 sc = mfma32(w == 0 ? fr : qx[0], fr, Z4);
    if (w == 0) SC1_STORE8S(((u32x2){pack2n(sc[0], sc[1]), pack2n(sc[2], sc[3])}), off, o1);
    else {
      const f32x4 s2 = mfma32(qx[1], fr, Z4);
      __builtin_amdgcn_raw_buffer_store_b128((u32x4){pack2n(sc[0], sc[1]), pack2n(sc[2], sc[3]), pack2n(s2[0], s2[1]), pack2n(s2[2], s2[3])}, rs, off, o1, 16);
    }
  };
  unsigned o1 = sct_slot(ck, 0u), o1m = scm_slot(ck, 0u);
  unsigned t2o = ts_tok(h, j) + (unsigned)((c >> 2) * 256 + g * 64 + (c & 3) * 16);
  TokScores t2;
  TokTask tk;
  PvTask2 pv2;
  if (w == 2) tk.init(h, j, lane);
  if (w == 3) { PIN_V(t2o); pv2.init(gp, lane); }
  if (w == 0) pub1(p1v, o1m);
  VM_DRAIN();
  BAR();
  const unsigned tst = now32() | 1u;
  if (w == 3 && ST_X) __builtin_amdgcn_raw_buffer_store_b32(tst, rs, lane < 2 ? ts_piece(b >> 2) - ACC_TS + (b & 3) * 4 + lane * 8192 : 0x7ffffff0, (unsigned)(OFF_ACC + (SR - 1) * ACC_STEP) + ACC_TS, 16);
  LOGP(2);
  kq_get(0);
  if (w == 1) pub1(p1v, o1);
  x_fetch_n<10>(x_src(a, 2, b), lds0, 2, 10 * w, lane * 16);
  if (w == 3) { t2.get(lds, 0, lane); t2.run(0); t2.run(1); t2.store(rs, t2o, ts_slot(ck, 0)); }
  LOGP(3);
  unsigned o2 = (unsigned)OFF_ACC, o2p = 0u, tw = 0u;
  asm volatile("" : "+s"(o2p));
  unsigned tc0 = 0u, tstar = 0u;
  int dmin = 0, dmax = 0;
  int e1 = 100000;
  unsigned offs = 100u;
  unsigned o4 = 0u;
  unsigned o3, o3p;
  u32x16 P = {0u, 0u, 0u, 0u, 0u, 0u, 0u, 0u, 0u, 0u, 0u, 0u, 0u, 0u, 0u, 0u}, V = P;
  u32x8 V8 = {0u, 0u, 0u, 0u, 0u, 0u, 0u, 0u};
  if (w < 2) asm volatile("" : "+{v[208:223]}"(P), "+{v[184:191]}"(V8), "+{v[192:207]}"(V));
  unsigned tso = lane < 2 ? ts_piece(b >> 2) + (unsigned)((b & 3) * 4 + lane * 8192) : 0x7ffffff0u;
  if (w < 2) PIN_V(tso);
  float minf = g >= 2 ? -INFINITY : 0.f;
  if (w < 2) PIN_V(minf);
  int sv = NV - 1;
  bf16x8 vox[5];
  unsigned vma[3], need2;
  u32x4 slo = {0u, 0u, 0u, 0u}, shi = slo;
  float k1 = 0.f;
  const u16* xsrc;
  float invv = 1.0f;
  auto prep0 = [&](int s) {
    xsrc = x_src(a, s + 3, b);
    if (w == 2) { o3p = ts_slot(ck, (s + 1 - LAG3) & 3); o3 = ts_slot(ck, imax(s, 0)); o4 = pt_slot(ck, imax(s - LAG3, 0)); PIN_S(o3); }
    if (w == 3) { o3p = pt_slot(ck, (s - LAG4) & 3); o4 = ts_slot(ck, s + 1); }
    PIN_S(xsrc); PIN_S(o3p); PIN_S(o4);
    __builtin_amdgcn_sched_barrier(0);
  };
  auto prep = [&](int s) {
    o2 = (unsigned)OFF_ACC + (unsigned)(s % SR) * (unsigned)ACC_STEP;
    sv = sv == NV - 1 ? 0 : sv + 1;
    PIN_S(o2); PIN_S(sv);
    if (w < 2) {
      PIN_S(tc0);
      o1 = sct_slot(ck, s % SR); o1m = scm_slot(ck, s % SR);
      need2 = 4u * (unsigned)(s + 1) + 2u * ((unsigned)(s + 2) / NSEG);
      PIN_S(o1); PIN_S(o1m); PIN_S(need2);
      __builtin_amdgcn_sched_barrier(0);
      kq_get(s);
    }
    invv = misc[4 + (s & 1)];
  };
  auto prep2 = [&](int s) {
#pragma unroll
    for (int k = 0; k < 3; ++k) {
      const int t = k == 0 ? 2 : (k == 1 ? far : 1 - far);
      const char* p = lds + L_VOX + sv * 5120 + t * 1024 + (plane & 47) * 16 + (g & 1) * 8;
      auto rd = [&](const char* q) { const u32x2 a0 = *(const u32x2*)q, a1 = *(const u32x2*)(q + 256); return mk8(a0[0], a0[1], a1[0], a1[1]); };
      vox[k == 0 ? 0 : 2 * k - 1] = rd(p);
      if (k) vox[2 * k] = rd(p + 3072);
      vma[k] = lds0 + (unsigned)(L_VOM + sv * 1280 + t * 256 + (plane & 31) * 8);
      PIN_V(vma[k]);
    }
    __builtin_amdgcn_sched_barrier(0);
    if (F0A) { xsrc = x_src(a, s + 3, b); PIN_S(xsrc); }
    const unsigned iu = __builtin_amdgcn_readfirstlane(f2u(invv));
    if (__builtin_expect(iu != f2u(inv), 0)) {
      inv = u2f(iu);
#pragma unroll
      for (int i = 0; i < 8; ++i) {
        u32x4& m = i < 4 ? slo : shi;
        m[i & 3] = (c >> 1) == i ? ((c & 1) ? iu & 0xffff0000u : iu >> 16) : 0u;
      }
      k1 = KEXP * inv;
    }
    PIN_V(k1); PIN_V(slo); PIN_V(shi);
    asm volatile("" : "+v"(vox[0]), "+v"(vox[1]), "+v"(vox[2]), "+v"(vox[3]), "+v"(vox[4]));
    __builtin_amdgcn_sched_barrier(0);
  };
  auto rows = [&](int s) {
    const int dl = spin_until(tw);
    if (w < 2) tc0 = cyc32();
    __builtin_amdgcn_sched_barrier(0);
    STAMP(1);
    unsigned spins = 0;
    auto pass = [&](bool first) {
      u32x4 x[2 * NC];
#pragma unroll
      for (int f = 0; f < NC; ++f) {
        if (f && w < 2 && first) { prep(s); asm volatile(".rept %0\n\ts_nop 3\n\t.endr" :: "n"(P2_G1)); }
        else if (f) asm volatile(".rept %0\n\ts_nop 3\n\t.endr" :: "n"(w < 2 ? P2_G0 : (f == 1 ? P2_G2 : P2_G3)));
        __builtin_amdgcn_sched_barrier(0);
        x[2 * f] = SC1_LOADS(l2off[2 * f], o2p); x[2 * f + 1] = SC1_LOADS(l2off[2 * f + 1], o2p);
        __builtin_amdgcn_sched_barrier(0);
      }
      if (first) lds_inc<L_MISC + 92>(lds0);
      if (first && w < 2 && PR_P != 3) __builtin_amdgcn_s_setprio(PR_P);
      if (first) { if (w >= 2) prep(s); else prep2(s); }
      if (w >= 2 && first && P2_W) lds_spin<L_MISC + 92, 0>(lds0, 4u * (unsigned)s);
      if (w == 2 && first) tk.store(rs, o4, s > LAG3 ? 0u : 0x7ff00000u);
      if (w == 3 && first) t2.store(rs, t2o, o4);
      if (first && w < 2) {
        p1cur = p1v; offs = (offs - (unsigned)dl) & ~3u;
        PIN_V(p1cur); PIN_S(offs);
        tstar = tc0 + (unsigned)(e1 + D1_M + (w == 1 ? D1_W1 : 0)) - offs * 20u;
        dmin = offs < 40u ? D1_MIN : REC_D1;
        dmin = s < EARLY_N ? REC_D1 + EARLY_D1 : dmin;
        dmax = REC_D1;
        PIN_S(tstar); PIN_S(dmin); PIN_S(dmax);
      }
      __builtin_amdgcn_sched_barrier(0);
      unsigned ok = 32u;
#pragma unroll
      for (int t = 0; t < 3; ++t) { acc[t] = Z4; PIN_V(acc[t]); }
      if (first && w < 2 && PR_P != 3) __builtin_amdgcn_s_setprio(3);
      __builtin_amdgcn_sched_barrier(0);
      bool r = true;
#pragma unroll
      for (int f = 0; f < NC; ++f) {
        unsigned p[4];
#pragma unroll
        for (int k = 0; k < 2; ++k) {
          const u32x4 v = x[2 * f + k];
          p[2 * k] = pack2((float)(int)v[0], (float)(int)v[1]); p[2 * k + 1] = pack2((float)(int)v[2], (float)(int)v[3]);
          asm volatile("" : "+v"(p[2 * k]), "+v"(p[2 * k + 1]));
          __builtin_amdgcn_sched_barrier(0);
        }
        ok = (ok & x[2 * f][0] & x[2 * f][2]) & (x[2 * f + 1][0] & x[2 * f + 1][2]);
        PIN_V(ok);
        __builtin_amdgcn_sched_barrier(0);
        bm[f] = mk8(p[0], p[1], p[2], p[3]);
        acc[0] = smfma64(bm[f], Wqk[0][f], acc[0], sidx);
        __builtin_amdgcn_sched_barrier(0);
        bm_put(f);
        acc[1] = smfma64(bm[f], Wqk[1][f], acc[1], sidx);
        __builtin_amdgcn_sched_barrier(0);
        if (f == NC - 1) { u64 bad = __builtin_amdgcn_ballot_w64(ok == 0u); PIN_S(bad); r = bad == 0ull; }
        __builtin_amdgcn_sched_barrier(0);
        acc[2] = smfma64(bm[f], Wqk[2][f], acc[2], sidx);
        __builtin_amdgcn_sched_barrier(0);
      }
      red_put();
      return r;
    };
    if (__builtin_expect(!pass(true), 0)) {
      FC(0);
      do {
        if (dead || ++spins > SPIN_CAP) { dead = true; break; }
        asm volatile("" ::: "memory");
      } while (!pass(false));
    }
    STAMPV(12, spins); LOGF(1, spins);
    TSTAMP(2, now32()); TSTAMP(3, spins);
  };
  if (w >= 2) prep0(-1);
  prep(0);
  if (w < 2) prep2(0);
  VM_DRAIN();
  CODE_PAD((PADR >> (4 * w)) & 15);
#pragma unroll 1
  for (int s = 0;; ++s) {
    if (__builtin_expect(o2p != 0u, 1)) rows(s);
    if (w < 2 && __builtin_expect(s == NSEG - 1, 0)) break;
    STAMP(2);
    BAR();
    STAMP(3);
    if (w < 2) pub1(p1cur, w == 0 ? o1m : o1);
    if (w == 2) tk.load(rs, o3p);
    if (w == 3) pv2.load(rs, o3p);
    const unsigned tp1 = cyc32();
    __builtin_amdgcn_sched_barrier(0);
    if (w == 2) {
      __builtin_amdgcn_s_setprio(W2_P);
      prep0(s);
      if (W2_D) spin_cyc(tp1 + W2_D);
      fr = qk_frag(lds + qo4, lds + qo2, lds + qo2b);
      t2_mem(fr, qx[0], qx[1], rs, o3, h, j, invv * KEXP, lane);
      __builtin_amdgcn_sched_barrier(0);
      asm volatile("s_waitcnt vmcnt(1)" ::: "memory");
      lds_inc<L_MISC + 28>(lds0);
      kq_get(s + 1);
    }
    if (w == 3) {
      __builtin_amdgcn_s_setprio(T4_P);
      prep0(s);
      t2.get(lds, s + 1, lane); t2.run(0); t2.run(1);
      __builtin_amdgcn_sched_barrier(0);
    }
    if (w >= 2 && __builtin_expect(s == NSEG - 2, 0)) { VM_DRAIN(); lds_inc<L_MISC + 28>(lds0); }
    if (w >= 2) __builtin_amdgcn_s_setprio(UN_P);
    TSTAMP(0, now32());
    STAMP(4);
    __builtin_amdgcn_sched_barrier(0);
    unsigned tpub = 0;
    u32x2 sw;
    if (w == 2) {
      STAMP(5);
      __builtin_amdgcn_sched_barrier(0);
      STAMP(6);
      ARRIVE(0);
      tk.run(j, lane, SelLo);
      if (__builtin_expect(s == NSEG - 1, 0)) { tlast = tp1; break; }
      if (F2B) { lds_spin<L_MISC + 48, 1>(lds0, 2u * (unsigned)(s + 1)); x_fetch_n<F2B>(xsrc, lds0, s + 3, P2B, lane * 16); }
      __builtin_amdgcn_s_setprio(SP_P);
    }
    if (w == 3) {
      pv2.run<false>(lds, sv < 1 + LAG4 ? sv + NV - 1 - LAG4 : sv - 1 - LAG4, lane, rs, 0u);
      if (__builtin_expect(s == NSEG - 1, 0)) { tlast = tp1; break; }
      lds_inc<L_MISC + 112>(lds0);
      STAMP(11);
      __builtin_amdgcn_s_setprio(SP_P);
      STAMP(5);
      ARRIVE(0);
      if (F3) { lds_spin<L_MISC + 48, 1>(lds0, 2u * (unsigned)(s + 1)); x_fetch_n<F3>(xsrc, lds0, s + 3, 40 - F3C - F3, lane * 16); }
      STAMP(6);
    }
    if (w < 2) {
      __builtin_amdgcn_s_setprio(D1_P);
      x_fetch_n<F0A>(xsrc, lds0, s + 3, PA + w * F0A, lane * 16);
      int dq = imin((int)(tstar - tp1), dmax);
      dq = dq < -D1_LATE ? dmax : dq;
      PIN_S(dq);
      spin_exact(tp1 + (unsigned)imax(dq, dmin));
      STAMP(5);
      u32x32 S;
      auto sc_load = [&]() {
        asm volatile("buffer_load_dwordx4 v[224:227], %2, %4, %5 offen sc1\n\t.rept %7\n\ts_nop 3\n\t.endr\n\t"
                     "buffer_load_dwordx4 v[228:231], %3, %4, %6 offen sc1\n\tbuffer_load_dwordx4 v[232:235], %3, %4, %6 offen offset:2048 sc1\n\t"
                     "buffer_load_dwordx4 v[236:239], %3, %4, %6 offen offset:256 sc1\n\tbuffer_load_dwordx4 v[240:243], %3, %4, %6 offen offset:2304 sc1\n\t"
                     "v_mov_b32 v220, 0x42000000\n\tv_mov_b32 v221, 0\n\tv_mov_b32 v222, 0x42000000\n\tv_mov_b32 v223, 0"
                     : "=&{v[224:255]}"(S), "+{v[208:223]}"(P) : "v"(l1offm), "v"(l1off), "s"(rs), "s"(o1m), "s"(o1), "n"(P1_G) : "memory");
      };
      sc_load();
      __builtin_amdgcn_sched_barrier(0);
      lds_inc<L_MISC + 48>(lds0);
      STAMP(6);
      ARRIVE(0);
      lds_spin<L_MISC + 28, 0>(lds0, need2);
      __builtin_amdgcn_s_setprio(3);
      asm volatile("ds_read_b64 v[192:193], %3\n\tds_read_b64 v[196:197], %3 offset:768\n\tds_read_b64 v[200:201], %4\n\tds_read_b64 v[204:205], %4 offset:768\n\tds_read_b64 v[184:185], %2"
                   : "+{v[184:191]}"(V8), "+{v[192:207]}"(V) : "v"(vma[0]), "v"(vma[1]), "v"(vma[2]) : "memory");
      __builtin_amdgcn_sched_barrier(0);
      {
        const int de = imin((int)(tp1 - tc0) - e1, D1_C);
        e1 += s == 0 ? 0 : de >> (s < EARLY_N && de < -D1_C ? 0 : D1_G);
        PIN_S(e1);
      }
      unsigned sn = (unsigned)(s + 1);
      PIN_V(sn);
      STAMP(11);
      __builtin_amdgcn_sched_barrier(0);
      {
        unsigned spins = 0;
        u64 miss, bad;
#define MM(d, a, b, c) "v_mfma_f32_16x16x32_bf16 " d ", " a ", " b ", " c "\n\t"
#define FM(d, x, r, ctrl) "v_fmac_f32_dpp " d ", " x ", " r " " ctrl "\n\t"
#define ID_ "quad_perm:[0,1,2,3] row_mask:0xf bank_mask:"
#define HM_ "row_half_mirror row_mask:0xf bank_mask:"
#define R8_ "row_ror:8 row_mask:0xf bank_mask:"
#define MI_ "row_mirror row_mask:0xf bank_mask:"
        auto soft = [&]() {
          asm volatile(
            "s_waitcnt vmcnt(4)\n\t" MM("v[244:247]", "%6", "v[224:227]", "0") "v_min3_u32 v214, v224, v225, v226\n\t"
            "s_waitcnt vmcnt(3)\n\t" MM("v[248:251]", "%6", "v[228:231]", "0") "v_min3_u32 v214, v214, v227, v228\n\tv_min3_u32 v214, v214, v229, v230\n\t"
            "s_waitcnt vmcnt(2)\n\t" MM("v[248:251]", "%6", "v[232:235]", "v[248:251]") "v_min3_u32 v214, v214, v231, v232\n\t"
            "v_add_f32_dpp v212, v244, v244 " R8_ "0x3\n\tv_add_f32_dpp v213, v245, v245 " R8_ "0x3\n\t"
            "s_waitcnt vmcnt(1)\n\t" MM("v[248:251]", "%7", "v[236:239]", "v[248:251]")
            "v_add_f32_dpp v212, v246, v246 " R8_ "0xc\n\tv_add_f32_dpp v213, v247, v247 " R8_ "0xc\n\t"
            "s_waitcnt vmcnt(0) lgkmcnt(0)\n\t" MM("v[248:251]", "%7", "v[240:243]", "v[248:251]")
            "v_fma_f32 v212, v212, %14, %15\n\tv_fma_f32 v213, v213, %14, %15\n\tv_exp_f32 v216, v212\n\tv_exp_f32 v217, v213\n\t"
            "v_min3_u32 v214, v214, v233, v234\n\tv_cvt_pk_bf16_f32 v208, v216, v217\n\tv_min3_u32 v214, v214, v235, v236\n\tds_read_b96 v[188:190], %16 offset:%18\n\t"
            "v_exp_f32 v216, v248\n\tv_mov_b32_dpp v209, v208 " R8_ "0xf\n\tv_exp_f32 v217, v249\n\tv_exp_f32 v218, v250\n\t"
            MM("v[252:255]", "%8", "v[208:211]", "0") "v_exp_f32 v219, v251\n\tv_cvt_pk_bf16_f32 v232, v216, v217\n\t"
            MM("v[224:227]", "v[192:195]", "v[208:211]", "0") "v_cvt_pk_bf16_f32 v233, v218, v219\n\tv_min3_u32 v214, v214, v237, v238\n\tv_mov_b32_dpp v234, v232 " R8_ "0xf\n\t"
            MM("v[228:231]", "v[196:199]", "v[208:211]", "0") "v_mov_b32_dpp v235, v233 " R8_ "0xf\n\t"
            MM("v[244:247]", "v[204:207]", "v[208:211]", "0") "v_min3_u32 v214, v214, v239, v240\n\t"
            MM("v[252:255]", "%8", "v[232:235]", "v[252:255]") MM("v[224:227]", "%10", "v[232:235]", "v[224:227]") "v_min3_u32 v214, v214, v241, v242\n\t"
            MM("v[228:231]", "%11", "v[232:235]", "v[228:231]") "s_waitcnt lgkmcnt(0)\n\tv_sub_u32 v215, v190, %17 clamp\n\t"
            MM("v[236:239]", "v[200:203]", "v[208:211]", "0") "v_min3_u32 v214, v214, v243, v215\n\tv_cmp_eq_u32 %4, 0, v214\n\tv_rcp_f32 v216, v252\n\t"
            MM("v[240:243]", "v[184:187]", "v[208:211]", "0") "v_mul_f32 v216, v216, v188\n\tv_subrev_u32 v215, 0x2f800000, v252\n\t"
            MM("v[236:239]", "%12", "v[232:235]", "v[236:239]") FM("v220", "v224", "v216", ID_ "0x1") FM("v221", "v225", "v216", ID_ "0x1")
            "v_mov_b32_dpp v217, v216 " HM_ "0xf\n\t"
            MM("v[244:247]", "%13", "v[232:235]", "v[244:247]") FM("v220", "v230", "v216", ID_ "0x2") FM("v221", "v231", "v216", ID_ "0x2")
            MM("v[240:243]", "%9", "v[232:235]", "v[240:243]") FM("v220", "v228", "v217", HM_ "0x1") FM("v221", "v229", "v217", HM_ "0x1")
            "v_cmp_ge_u32 vcc, v215, v189\n\t" FM("v220", "v226", "v217", HM_ "0x2") FM("v221", "v227", "v217", HM_ "0x2")
            "s_or_b64 %5, %4, vcc"
            : "+{v[224:255]}"(S), "+{v[208:223]}"(P), "+{v[184:191]}"(V8), "+{v[192:207]}"(V), "=&s"(miss), "=&s"(bad)
            : "v"(slo), "v"(shi), "v"(ONES), "v"(vox[0]), "v"(vox[1]), "v"(vox[2]), "v"(vox[3]), "v"(vox[4]), "v"(k1), "v"(minf), "v"(lds0), "s"(s), "n"(L_MISC + 32)
            : "vcc", "scc", "memory");
        };
        soft();
        if (__builtin_expect(bad != 0ull, 0)) {
          if (miss) FC(1); else FC(2);
          while (miss != 0ull && !dead) {
            if (++spins > SPIN_CAP) dead = true;
            asm volatile("s_nop 7\n\ts_nop 7" ::: "memory");
            sc_load(); soft();
          }
          if (bad != 0ull) {
            u32x4 y[5];
#pragma unroll
            for (int k = 0; k < 4; ++k) y[k] = SC1_LOADS(l1off + (k & 1) * 2048 + (k >> 1) * 256, o1);
            y[4] = SC1_LOADS(l1offm, o1m);
            const f32x4 d0 = mfma32(as8(slo), as8(y[4]), Z4);
            f32x4 d1 = mfma32(as8(shi), as8(y[3]), mfma32(as8(shi), as8(y[2]), mfma32(as8(slo), as8(y[1]), mfma32(as8(slo), as8(y[0]), Z4))));
            const bool lo = c < 8;
            const float m0 = __builtin_fmaf((lo ? d0[0] : d0[2]) + dppf<DPP_ROR8>(lo ? d0[2] : d0[0]), k1, minf);
            const float m1 = __builtin_fmaf((lo ? d0[1] : d0[3]) + dppf<DPP_ROR8>(lo ? d0[3] : d0[1]), k1, minf);
            float mx = fmx(fmx(fmx(d1[0], d1[1]), d1[2]), fmx(fmx(d1[3], m0), m1));
            mx = fmx(mx, dppf<DPP_ROR8>(mx));
            mx = x32mx(x16mx(mx));
            d1 -= mx;
            const unsigned mm = pack2(__builtin_amdgcn_exp2f(m0 - mx), __builtin_amdgcn_exp2f(m1 - mx));
            const unsigned t01 = pack2(__builtin_amdgcn_exp2f(d1[0]), __builtin_amdgcn_exp2f(d1[1])), t23 = pack2(__builtin_amdgcn_exp2f(d1[2]), __builtin_amdgcn_exp2f(d1[3]));
            const bf16x8 pm = mk8(mm, dpp_ror8(mm), 0u, 0u), pt = mk8(t01, t23, dpp_ror8(t01), dpp_ror8(t23));
            const float r = __builtin_amdgcn_rcpf(mfma32(ONES, pt, mfma32(ONES, pm, Z4))[0]) * u2f(V8[4]), rm = dppf<0x141>(r);
            f32x4 o[5];
#pragma unroll
            for (int k = 0; k < 5; ++k) o[k] = mfma32(vox[k], pt, mfma32(k ? mk8(V[4 * k - 4], V[4 * k - 3], 0u, 0u) : mk8(V8[0], V8[1], 0u, 0u), pm, Z4));
#pragma unroll
            for (int i = 0; i < 2; ++i) {
              const float oa = dppf<0x141>(o[2][i]), ob = dppf<0x141>(o[1][2 + i]);
              P[12 + i] = f2u(__builtin_fmaf(hd ? ob : oa, rm, __builtin_fmaf(hd ? o[2][2 + i] : o[1][i], r, i ? 0.f : 32.0f)));
            }
            P[8] = f2u(r); P[9] = f2u(rm);
#pragma unroll
            for (int i = 0; i < 4; ++i) { S[12 + i] = f2u(o[3][i]); S[16 + i] = f2u(o[0][i]); S[20 + i] = f2u(o[4][i]); }
            asm volatile("s_nop 7\n\ts_nop 7" : "+{v[224:255]}"(S), "+{v[208:223]}"(P));
          }
        }
        STAMP(7); STAMPV(13, spins); LOGF(0, spins);
        {
          asm volatile(".if %11 == 0\n\tds_write_b32 %8, %12 offset:%10\n\t.endif\n\t"
                       "v_cvt_i32_f32 v218, v220\n\tv_cvt_i32_f32 v219, v221\n\tv_and_or_b32 v218, v218, %7, 2\n\tbuffer_atomic_add_x2 v[218:219], %3, %5, %6 offen\n\t"
                       ".if %11 == 1\n\tds_write_b32 %8, %12 offset:%10\n\t.endif\n\t"
                       FM("v222", "v236", "v216", R8_ "0x4") FM("v223", "v237", "v216", R8_ "0x4") "ds_read_b64 %2, %8 offset:%9\n\t"
                       FM("v222", "v238", "v217", MI_ "0x8") FM("v223", "v239", "v217", MI_ "0x8") "v_permlane32_swap_b32 v240, v242\n\t"
                       FM("v222", "v244", "v217", MI_ "0x4") FM("v223", "v245", "v217", MI_ "0x4") "v_permlane32_swap_b32 v241, v243\n\t"
                       FM("v222", "v246", "v216", R8_ "0x8") FM("v223", "v247", "v216", R8_ "0x8") ".if %11 == 2\n\tds_write_b32 %8, %12 offset:%10\n\t.endif\n\t"
                       FM("v222", "v240", "v216", ID_ "0x1") FM("v223", "v241", "v216", ID_ "0x1") FM("v222", "v242", "v217", HM_ "0x1") FM("v223", "v243", "v217", HM_ "0x1")
                       "v_cvt_i32_f32 v212, v222\n\tv_cvt_i32_f32 v213, v223\n\tv_and_or_b32 v212, v212, %7, 2\n\tbuffer_atomic_add_x2 v[212:213], %4, %5, %6 offen\n\t"
                       ".if %11 == 3\n\tds_write_b32 %8, %12 offset:%10\n\ts_waitcnt lgkmcnt(1)\n\t.else\n\ts_waitcnt lgkmcnt(0)\n\t.endif"
                       : "+{v[224:255]}"(S), "+{v[208:223]}"(P), "=&v"(sw) : "v"(acco), "v"(acco2), "s"(rs), "s"(o2), "s"(~63u), "v"(lds0), "n"(L_MISC + 96),
                         "n"(L_MISC + 104 + 4 * (w & 1)), "n"(REL_AT), "v"(sn) : "memory");
          __builtin_amdgcn_sched_barrier(0);
          tpub = now32();
          asm volatile("buffer_atomic_umax %0, %1, %2, %3 offen" :: "v"(tpub | 1u), "v"(tso), "s"(rs), "s"(o2) : "memory");
          STAMP(10);
        }
      }
    }
    STAMP(8);
    ARRIVE(1);
    o2p = o2;
    PIN_S(o2p);
    if (w < 2) {
      if (__builtin_expect(__builtin_amdgcn_readfirstlane(sw[1]) <= (unsigned)s, 0)) {
        lds_spin<L_MISC + 100, 0>(lds0, (unsigned)s + 1u);
        sw[0] = __hip_atomic_load((unsigned*)(misc + 24), __ATOMIC_RELAXED, __HIP_MEMORY_SCOPE_WORKGROUP);
      }
    } else {
      u32x4 q;
      asm volatile("1:\n\tds_read_b128 %0, %1 offset:%3\n\t.if %4\n\ts_sleep 1\n\t.endif\n\ts_waitcnt lgkmcnt(0)\n\tv_min3_u32 v201, v201, v202, v203\n\tv_cmp_ge_u32 vcc, %2, v201\n\ts_cbranch_vccnz 1b"
                   : "=&{v[200:203]}"(q) : "v"(lds0), "s"(s), "n"(L_MISC + 96), "n"(SP_SL) : "vcc", "memory");
      __builtin_amdgcn_s_setprio(3);
      tpub = now32();
      sw[0] = q[0];
    }
    STAMP(9);
    static_assert(F3C == 0 && F2C == 0 && F0C == 0, "nothing behind the atomics");
    LOGT(0, tpub); LOGT(1, tb2_);
    TSTAMP(1, tpub);
    ARRIVE_OUT();
    const unsigned an = __builtin_amdgcn_readfirstlane(sw[0]);
    const int ahead = (int)(an - tpub);
    tw = tpub + (unsigned)(an == 0u || ahead > 1000 ? SCH_FIRST : imax(ahead, w < 2 ? SCH_DMIN : 0));
    offs = tw - an;
  }
  STAMP_OUT();
  LOGP(4);
  __builtin_amdgcn_s_setprio(3);
  if (w < 2) { BAR(); tlast = cyc32(); }
  if (w == 2) tk.store(rs, o4);
  if (w == 3) {
    const unsigned oo = op_slot(NSEG - 2 - LAG4) + (unsigned)((m * 32 + gp) * 1280);
    const char* sp = lds + L_STG + 2048;
    const u32x4 v0 = *(const u32x4*)(sp + lane * 16), v1 = *(const u32x4*)(sp + 1280 + lane * 16), v2 = *(const u32x4*)(sp + (g & 1) * 1280 + 1024 + c * 16);
    SC1_STORE(v0, oo + lane * 16); SC1_STORE(v1, oo + 20480 + lane * 16); SC1_STORE(v2, lane < 32 ? oo + g * 20480 + 1024 + c * 16 : 0x7ffffff0u);
  }
  if (dead) tlast |= 1u; else tlast &= ~1u;
}

__device__ __forceinline__ void drain(const RecArgs& a, char* lds, const __amdgpu_buffer_rsrc_t rs, const int w, const unsigned tp1) {
  static_assert(LAG3 == 2 && LAG4 == 3 && LAG5 == 6, "drain schedule");
  const int lane = __builtin_amdgcn_workitem_id_x() & 63, b = __builtin_amdgcn_workgroup_id_x(), c = lane & 15;
  const int gp = (b & 7) * 2 + (b >> 7), m = (b >> 3) & 15, j = m & 7;
  unsigned* const misc = (unsigned*)(lds + L_MISC);
  const unsigned lds0 = (unsigned)(uintptr_t)(LAS char*)lds, ck = __builtin_amdgcn_readfirstlane(misc[11]);
  bool dead = tp1 & 1u;
  auto poll = [&](auto load, auto ready) {
    for (unsigned spins = 0;;) {
      asm volatile("" ::: "memory");
      load();
      if (ready() || dead) break;
      if (++spins > SPIN_CAP) dead = true;
      if (END_SL) __builtin_amdgcn_s_sleep(END_SL);
    }
  };
  LOGD(0);
#define TASK(ty, sg, tm) ((u64)((ty) | ((sg) - 56) << 2 | ((tm) / 100) << 5))
  u64 todo = w == 0 ? TASK(1, 63, END_E1) | TASK(2, 63, END_E2) << 12 | TASK(3, 62, END_E3) << 24
           : w == 1 ? TASK(1, 62, 0) | TASK(2, 62, END_E62) << 12 | TASK(3, 60, END_E3) << 24
           : w == 2 ? TASK(2, 60, 0) | TASK(3, 56, 0) << 12 : TASK(2, 61, END_E61) | TASK(3, 58, END_E59) << 12;
  TokTask tk;
  PvTask2 pv;
  OutTask ot;
  {
    const u32x4* p = (const u32x4*)(lds + L_DRN) + lane;
    const u32x4 q0 = p[0], q1 = p[64], q2 = p[128];
    tk.l0 = q0[0]; tk.l1 = q0[0] + 16384u; tk.lm = q0[1]; tk.s0 = q0[2]; tk.s1 = q0[3];
    pv.lo = q1[0]; pv.lm = q1[1]; pv.vr = q1[2]; pv.ma = q1[3]; pv.mz = q2[0]; pv.so = q2[1];
    pv.pa[0] = pv.pa[1] = pv.pb[0] = pv.pb[1] = (u32x4){0u, 0u, 0u, 0u};
    ot.t5f = q2[2]; ot.t5o = q2[3]; ot.sel = as8(p[192]);
  }
#pragma unroll 1
  for (int ti = 0; todo; todo >>= 12, ++ti) {
    const int ty = (int)todo & 3, sl = 56 + (((int)todo >> 2) & 7);
    const unsigned tm = tp1 + (((unsigned)todo >> 5) & 0x7fu) * 100u;
    if (ty == 1) {
      wait_cyc(tm);
      LOGD(3 * ti + 1);
      poll([&]() { tk.load(rs, ts_slot(ck, sl)); }, [&]() { return tk.ready(); });
      LOGD(3 * ti + 2);
      tk.run(j, lane, ot.sel); tk.store(rs, pt_slot(ck, sl));
    } else if (ty == 2) {
      const unsigned t0 = cyc32();
      if (sl >= NSEG - 2) { lds_wait(misc + 7, 4u * NSEG + 3u); wait_cyc(t0 + END_G2); }
      wait_cyc(tm);
      LOGD(3 * ti + 1);
      poll([&]() { pv.load(rs, pt_slot(ck, sl)); }, [&]() { return pv.ready(); });
      LOGD(3 * ti + 2);
      pv.run<true>(lds, sl % NV, lane, rs, op_slot(sl) + (unsigned)((m * 32 + gp) * 1280));
      if (sl == NSEG - 1) misc[14] = cyc32() | 1u;
    } else {
      if (sl >= NSEG - 4) { lds_wait(misc + 14, 1u); wait_cyc(__builtin_amdgcn_readfirstlane(misc[14]) + END_G3); }
      wait_cyc(tm);
      LOGD(3 * ti + 1);
      OutTask o2 = ot;
      poll([&]() { o2.load(a, rs, sl + 1, b); ot.load(a, rs, sl, b); }, [&]() { return ot.ready(b) & o2.ready(b); });
      LOGD(3 * ti + 2);
      o2.run(lane); o2.store(a, sl + 1, b, lane);
      ot.run(lane); ot.store(a, sl, b, lane);
    }
    LOGD(3 * ti + 3);
  }
  VM_DRAIN();
  if (w != 0) lds_inc<L_MISC + 64>(lds0);
  else {
    constexpr unsigned o2 = (unsigned)OFF_ACC + (unsigned)((NSEG - 1) % SR) * (unsigned)ACC_STEP;
    lds_wait(misc + 16, 3u);
    LOGD(10);
    if (lane == 0) __hip_atomic_fetch_add((gu64*)((GAS char*)(a.ws + o2) + acc_off(m * 4, 0)), (u64)1, RLX_AGENT);
    wait_until(now32() + SCH_DRAIN);
    wait_cyc(tp1 + END_E4);
    unsigned x;
    if (qkv_row(b) + 48 >= 55 && qkv_col(b) < 15) poll([&]() { x = __builtin_amdgcn_raw_buffer_load_b32(rs, acc_off(c * 4, 0), o2, 16); }, [&]() { return wave_all((x & 16u) != 0u); });
    lds_inc<L_MISC + 68>(lds0);
    LOGD(11);
  }
  if (qkv_row(b) + 48 >= 55 && qkv_col(b) < 15) {
    lds_wait(misc + 18, 8u);
    lds_spin<L_MISC + 68, 1>(lds0, 1u);
    x_fetch_n<10>(x_src(a, NSEG + 3, b), lds0, 3, 10 * w, lane * 16);
    VM_DRAIN();
    lds_inc<L_MISC + 68>(lds0);
  }
  LOGP(8);
}

__device__ __forceinline__ void proj_front(const RecArgs& a, char* lds, const __amdgpu_buffer_rsrc_t rs, const int v) {
  const int lane = __builtin_amdgcn_workitem_id_x() & 63, b = __builtin_amdgcn_workgroup_id_x(), c = lane & 15, g = lane >> 4;
  const int rb = (b & 7) + 8 * (b >> 5), cq = (b >> 3) & 3;
  const unsigned lds0 = (unsigned)(uintptr_t)(LAS char*)lds;
  unsigned* const cnt = (unsigned*)(lds + L_MISC) + 20;
  LOGF2(0);
  float* const red = (float*)(lds + L_RED1);
  float* const fin = red + 640;
  const f32x4 Z4 = {0.f, 0.f, 0.f, 0.f};
  x_fetch_n<8>((const u16*)(a.ws + OFF_OB) + (size_t)rb * 32 * DM, lds0, 0, 8 * v, lane * 16);
  bf16x8 W[2][20];
  const __amdgpu_buffer_rsrc_t rw = __builtin_amdgcn_make_buffer_rsrc(a.ws + OFF_WOBT + (size_t)(cq * 10 + 2 * v) * 20480, 0, 40960, 0x00020000);
  auto w_get = [&](int ks) {
#pragma unroll
    for (int i = 0; i < 2; ++i) W[i][ks] = as8(__builtin_amdgcn_raw_buffer_load_b128(rw, lane * 16 + (ks & 3) * 1024, (i * 20 + (ks & ~3)) * 1024, 0));
    __builtin_amdgcn_sched_barrier(0);
  };
#pragma unroll
  for (int ks = 0; ks < FW; ++ks) w_get(ks);
  const unsigned fo = (unsigned)(rb * 40960 + (cq * 5 + v) * 1024 + ((g >> 1) * 16 + c) * 16), f8 = fo + (g & 1) * 8, f16 = fo + (g & 1) * 20480;
  u32x2 xo[2][2];
#pragma unroll
  for (int k = 0; k < 4; ++k)
    xo[k >> 1][k & 1] = __builtin_bit_cast(u32x2, __hip_atomic_load((const gu64*)((const char*)a.xnew + f8 + (k >> 1) * 512 + (k & 1) * 20480), RLX_AGENT));
  asm volatile("s_waitcnt vmcnt(%0)" :: "n"(2 * FW + 4) : "memory");
  lds_inc<L_MISC + 80>(lds0);
  lds_wait(cnt, 5u);
  LOGF2(1);
  f32x4 acc[2][2] = {{Z4, Z4}, {Z4, Z4}};
  bf16x8 x[20][2];
#pragma unroll
  for (int ks = -3; ks < 20; ++ks) {
    if (ks >= 0 && ks + FW < 20) w_get(ks + FW);
#pragma unroll
    for (int t = 0; t < 2; ++t) {
      if (ks + 3 < 20) x[ks + 3][t] = *(const bf16x8*)(lds + L_XN + (t * 20 + ks + 3) * 1024 + lane * 16);
#pragma unroll
      for (int i = 0; i < 2 && ks >= 0; ++i) acc[i][t] = mfma32(W[i][ks < 0 ? 0 : ks], x[ks < 0 ? 0 : ks][t], acc[i][t]);
    }
    __builtin_amdgcn_sched_barrier(0);
  }
  u32x2 xp[2][2];
  PIN_V(acc[1][1]);
  LOGF2(2);
#pragma unroll
  for (int t = 0; t < 2; ++t) {
#pragma unroll
    for (int k = 0; k < 4; ++k) {
      const int i = k >> 1, q = k & 1;
      const unsigned r = pack2(acc[i][t][2 * q], acc[i][t][2 * q + 1]), x = xo[i][t][q];
      xp[i][t][q] = pack2(u2f(x << 16) + u2f(r << 16), u2f(x & 0xffff0000u) + u2f(r & 0xffff0000u));
      acc[i][t][2 * q] = u2f(xp[i][t][q] << 16); acc[i][t][2 * q + 1] = u2f(xp[i][t][q] & 0xffff0000u);
    }
    const f32x4 sm = acc[0][t] + acc[1][t];
    const float ml = x32sum(x16sum((sm[0] + sm[1]) + (sm[2] + sm[3]))) * (1.0f / 32.0f);
    float q = 0.f;
#pragma unroll
    for (int r = 0; r < 8; ++r) { const float d = acc[r >> 2][t][r & 3] - ml; q += d * d; }
    q = x32sum(x16sum(q));
    *(f32x2*)(red + (v * 32 + t * 16 + c) * 2) = (f32x2){ml, q};
  }
  lds_inc<L_MISC + 80>(lds0);
  if (v == 0) {
    lds_wait(cnt, 10u);
    LOGF2(3);
    float mw[5], ml = 0.f, m2 = 0.f;
#pragma unroll
    for (int i = 0; i < 5; ++i) { const f32x2 u = *(const f32x2*)(red + (i * 32 + (lane & 31)) * 2); mw[i] = u[0]; ml += u[0]; m2 += u[1]; }
    ml *= 0.2f;
#pragma unroll
    for (int i = 0; i < 5; ++i) m2 += 32.0f * (mw[i] - ml) * (mw[i] - ml);
    const unsigned xs = (unsigned)OFF_LNX + rb * 1024;
    if (lane < 32) SC1_STORE8S(((u32x2){f2u(ml), nz(f2u(m2))}), (cq * 32 + lane) * 8, xs);
    wait_cyc(cyc32() + FR_D);
    u32x2 x0, x1;
    for (unsigned spins = 0; spins < SPIN_CAP; ++spins) {
      x0 = __builtin_amdgcn_raw_buffer_load_b64(rs, lane * 8, xs, 16); x1 = __builtin_amdgcn_raw_buffer_load_b64(rs, 512 + lane * 8, xs, 16);
      if (wave_all(x0[1] != 0u && x1[1] != 0u)) break;
      asm volatile("" ::: "memory");
    }
    const float m0 = u2f(x0[0]), m1 = u2f(x1[0]);
    const float mean = x32sum(m0 + m1) * 0.25f;
    const float M2 = x32sum((u2f(x0[1]) + u2f(x1[1])) + 160.0f * ((m0 - mean) * (m0 - mean) + (m1 - mean) * (m1 - mean)));
    const float mb = rbf(mean), den = rbf(__builtin_sqrtf(rbf(rbf(M2 * (1.0f / DM)) + 1e-5f)));
    if (lane < 32) {
      fin[lane] = mb; fin[32 + lane] = 1.0f / den;
      if (cq == 0) __hip_atomic_store((GAS unsigned*)(a.ws + OFF_XNRM) + (rb * 32 + lane) * 4, f2u(1.02f * (M2 + DM * (mean - mb) * (mean - mb)) / (den * den)), RLX_AGENT);
    }
    lds_inc<L_MISC + 84>(lds0);
  }
  lds_wait(cnt + 1, 1u);
  LOGF2(4);
  const __amdgpu_buffer_rsrc_t rx = __builtin_amdgcn_make_buffer_rsrc(a.ws + OFF_XN, 0, (int)(OFF_QB - OFF_XN), 0x00020000);
  auto put = [&](const u32x2 p0, const u32x2 p1, unsigned so) {
    const auto r0 = __builtin_amdgcn_permlane16_swap(p0[0], p1[0], false, false), r1 = __builtin_amdgcn_permlane16_swap(p0[1], p1[1], false, false);
    __builtin_amdgcn_raw_buffer_store_b128((u32x4){r0[0], r1[0], r0[1], r1[1]}, rx, f16, so, 16);
  };
#pragma unroll
  for (int i = 0; i < 2; ++i) {
    u32x2 y[2];
#pragma unroll
    for (int t = 0; t < 2; ++t) {
      const float mb = fin[t * 16 + c], rden = fin[32 + t * 16 + c];
      y[t] = (u32x2){pack2(rbf(acc[i][t][0] - mb) * rden, rbf(acc[i][t][1] - mb) * rden), pack2(rbf(acc[i][t][2] - mb) * rden, rbf(acc[i][t][3] - mb) * rden)};
    }
    put(y[0], y[1], i * 512);
  }
  VM_DRAIN();
  lds_inc<L_MISC + 80>(lds0);
#pragma unroll
  for (int i = 0; i < 2; ++i) put(xp[i][0], xp[i][1], (unsigned)(OFF_XB - OFF_XN) + i * 512);
  if (v == 0) {
    lds_wait(cnt, 15u);
    if (rb < 4 && lane < 8) __hip_atomic_fetch_add((GAS unsigned*)(a.ws + OFF_RDY) + lane * 64, 1u, RLX_AGENT);
  }
  LOGF2(5);
}

__global__ __launch_bounds__(512, 2) void rec_kernel(RecArgs a) {
  __shared__ __attribute__((aligned(16))) char lds[L_BYTES];
  const int tid = __builtin_amdgcn_workitem_id_x(), wid = __builtin_amdgcn_readfirstlane(tid >> 6), w = wid & 3;
  const __amdgpu_buffer_rsrc_t rs = __builtin_amdgcn_make_buffer_rsrc(a.ws, 0, (int)COMM_BYTES, 0x00020000);
  if (tid < 32) ((float*)(lds + L_MISC))[tid] = (tid == 4 || tid == 5) ? 1.0f : 0.0f;
  BAR();
  if (a.layer && wid < 5) proj_front(a, lds, rs, wid);
  if (wid < 4) {
    unsigned tlast;
    if (w == 0) rec_waves<0>(a, lds, rs, tlast);
    else if (w == 1) rec_waves<1>(a, lds, rs, tlast);
    else if (w == 2) rec_waves<2>(a, lds, rs, tlast);
    else rec_waves<3>(a, lds, rs, tlast);
    drain(a, lds, rs, w, tlast);
  } else {
    unsigned vo[2][2];
    if (w == 0) token_waves<0>(a, lds, rs, vo);
    else if (w == 1) token_waves<1>(a, lds, rs, vo);
    else if (w == 2) token_waves<2>(a, lds, rs, vo);
    else token_waves<3>(a, lds, rs, vo);
    qkv_tail(a, lds, w, vo);
  }
}

#if defined(__HIP_DEVICE_COMPILE__)
#pragma clang attribute pop
#endif

#define ATT_P1 6
#define ATT_P2 10
#define ATT_P3 14
#define ATT_THR 6.0f
#define ATT_NT 4
constexpr int NT = ATT_NT, ATT_NQ = 8 / NT, KQ = S_ / NT;
#define ATT_LMAX 1.0e12f
#if defined(__HIP_DEVICE_COMPILE__)
#pragma clang attribute push(__attribute__((target("no-packed-fp32-ops"))), apply_to = function)
#endif
__global__ __launch_bounds__(512, 2) void attb_kernel(const u16* __restrict__ Qp, const u16* __restrict__ KV, u16* __restrict__ O) {
  __shared__ f32x4 mo_[8][NT - 1][3][64];
  const int tid = __builtin_amdgcn_workitem_id_x(), bid = __builtin_amdgcn_workgroup_id_x();
  __shared__ unsigned sink[64];
  own_code<1300 * NT>(tid, sink);
  const int lane = tid & 63, wid = __builtin_amdgcn_readfirstlane(tid >> 6), kh = wid / ATT_NQ, r = lane & 31, hh = lane >> 5;
  const int hd = (bid & 7) * 4 + ((bid >> 3) & 3), qb = (bid >> 5) * ATT_NQ + wid % ATT_NQ;
  const int q0 = qb * 32 * NT;
  bf16x8 qa[NT]; u32x2 qb2[NT];
#pragma unroll
  for (int qt = 0; qt < NT; ++qt) {
    const u16* qr = Qp + ((size_t)hd * S_ + q0 + (qt + kh) % NT * 32 + r) * QST;
    { const u32x2* qp = (const u32x2*)(qr + 8 * hh); const u32x2 q0_ = qp[0], q1_ = qp[1]; qa[qt] = mk8(q0_[0], q0_[1], q1_[0], q1_[1]); }
    const u32x2 t = *(const u32x2*)(qr + 16);
    qb2[qt] = hh ? (u32x2){0u, 0u} : t;
  }
  const __amdgpu_buffer_rsrc_t rk = __builtin_amdgcn_make_buffer_rsrc((void*)(KV + ((size_t)hd * 64 + kh * (KQ / 32)) * (KVT / 2)), 0, KQ / 32 * KVT, 0x00020000);
  unsigned ok1 = lane * 16, ok2 = hh ? 1600 : 1024 + r * 8, ov = r > 20 ? 1u << 30 : 1280 + (hh * 21 + r) * 16;
  const __amdgpu_buffer_rsrc_t ro = __builtin_amdgcn_make_buffer_rsrc(O, 0, S_ * DM * 2, 0x00020000);
  unsigned oo[3];
#pragma unroll
  for (int i = 0; i < 3; ++i) {
    const int row = q0 + kh * 32 + r, k = hd * BD + 4 * hh + 8 * i;
    oo[i] = i == 2 && hh ? 1u << 30 : (unsigned)(((((row >> 4) * 20 + (k >> 5)) * 64 + ((k & 31) >> 3) * 16 + (row & 15)) * 8 + (k & 7)) * 2);
  }
  f32x16 o[NT];
  float m[NT];
  const f32x16 Z16 = {0.f, 0.f, 0.f, 0.f, 0.f, 0.f, 0.f, 0.f, 0.f, 0.f, 0.f, 0.f, 0.f, 0.f, 0.f, 0.f};
  int odd = 0xDDDD;
  asm volatile("" : "+v"(odd));
  struct KvT { u32x4 k1, v0, v1; u32x2 k2; };
  auto fetch_k = [&](KvT& x, int t) {
    x.k1 = __builtin_amdgcn_raw_buffer_load_b128(rk, ok1, imin(t, KQ / 32 - 1) * KVT, 0); x.k2 = __builtin_amdgcn_raw_buffer_load_b64(rk, ok2, imin(t, KQ / 32 - 1) * KVT, 0);
    __builtin_amdgcn_sched_barrier(0);
  };
  auto fetch_v = [&](KvT& x, int t) {
    x.v0 = __builtin_amdgcn_raw_buffer_load_b128(rk, ov, imin(t, KQ / 32 - 1) * KVT, 0); x.v1 = __builtin_amdgcn_raw_buffer_load_b128(rk, ov + 672, imin(t, KQ / 32 - 1) * KVT, 0);
    __builtin_amdgcn_sched_barrier(0);
  };
  auto fetch = [&](int t) { KvT x; __builtin_amdgcn_sched_barrier(0); fetch_k(x, t); fetch_v(x, t); return x; };
  auto qk1 = [&](const KvT& x, int qt) { return __builtin_amdgcn_mfma_f32_32x32x16_bf16(as8(x.k1), qa[qt], Z16, 0, 0, 0); };
  auto qk2 = [&](const KvT& x, int qt, f32x16 c) { return __builtin_amdgcn_mfma_f32_32x32x8bf16_1k(__builtin_bit_cast(v4i16_t, x.k2), __builtin_bit_cast(v4i16_t, qb2[qt]), c, 0, 0, 0); };
  auto pv = [&](const u32x4 v, const f32x16 p, int hi, f32x16 c) {
    const f32x8 ph = hi ? __builtin_shufflevector(p, p, 8, 9, 10, 11, 12, 13, 14, 15) : __builtin_shufflevector(p, p, 0, 1, 2, 3, 4, 5, 6, 7);
    return __builtin_amdgcn_smfmac_f32_32x32x32_bf16(__builtin_bit_cast(hbf16x8, v), __builtin_bit_cast(hbf16x16, ph), c, odd, 0, 0);
  };
  auto slow = [&]() {
    asm volatile("" : "+v"(ok1), "+v"(ok2), "+v"(ov) :: "memory");
#pragma unroll
    for (int qt = 0; qt < NT; ++qt) o[qt] = Z16;
#pragma unroll 1
    for (int t = 0; t < KQ / 32; ++t) {
      const KvT x = fetch(t);
#pragma unroll
      for (int qt = 0; qt < NT; ++qt) {
        f32x16 sa = qk2(x, qt, qk1(x, qt));
        float mx = fmx(fmx(sa[0], sa[1]), sa[2]);
#pragma unroll
        for (int i = 3; i < 15; i += 2) mx = fmx(fmx(mx, sa[i]), sa[i + 1]);
        mx = fmx(mx, sa[15]);
        if (t == 0 || !wave_all(mx <= ATT_THR)) {
          mx = x32mx(mx);
          if (t) mx = fmx(mx, 0.f);
          const float mn = m[qt] + mx;
          const unsigned h1 = f2bf(mn); const float r1 = mn - bf2f(h1);
          const unsigned h2 = f2bf(r1); const float r2 = r1 - bf2f(h2);
          const unsigned h3 = f2bf(r2);
          const float me = (bf2f(h1) + bf2f(h2)) + bf2f(h3);
          const float dm = me - m[qt];
          m[qt] = me;
          if (hh) qb2[qt] = (u32x2){(h1 | (h2 << 16)) ^ 0x80008000u, h3 ^ 0x8000u};
          const float alpha = t ? __builtin_amdgcn_exp2f(-dm) : 0.f;
#pragma unroll
          for (int i = 0; i < 16; ++i) { o[qt][i] *= alpha; sa[i] -= dm; }
        }
#pragma unroll
        for (int i = 0; i < 16; ++i) sa[i] = __builtin_amdgcn_exp2f(sa[i]);
        asm volatile("" : "+v"(sa));
        o[qt] = pv(x.v1, sa, 1, pv(x.v0, sa, 0, o[qt]));
        __builtin_amdgcn_sched_barrier(0);
      }
    }
  };
  f32x16 sc[2];
  auto phase = [&](int qt, const u32x4 vp, const KvT& xk, const u32x4 v0, auto mid) {
    const int u = qt & 1;
    auto ex = [&](int i0, int i1) {
#pragma unroll
      for (int i = i0; i < i1; ++i) sc[u][i] = __builtin_amdgcn_exp2f(sc[u][i]);
      asm volatile("" : "+v"(sc[u]));
      __builtin_amdgcn_sched_barrier(0);
    };
    o[(qt + NT - 1) % NT] = pv(vp, sc[1 - u], 1, o[(qt + NT - 1) % NT]);
    __builtin_amdgcn_sched_barrier(0);
    ex(0, 3);
    sc[1 - u] = qk1(xk, (qt + 1) % NT);
    __builtin_amdgcn_sched_barrier(0);
    ex(3, 6);
    sc[1 - u] = qk2(xk, (qt + 1) % NT, sc[1 - u]);
    __builtin_amdgcn_sched_barrier(0);
    ex(6, 10);
    o[qt] = pv(v0, sc[u], 0, o[qt]);
    __builtin_amdgcn_sched_barrier(0);
    mid();
    ex(10, 16);
  };
  if (ATT_P1) __builtin_amdgcn_s_setprio(3);
  {
    KvT xa = fetch(0), xb;
    fetch_k(xb, 1);
    xb.v1 = (u32x4){0u, 0u, 0u, 0u};
    sc[1] = Z16;
    asm volatile("" : "+v"(sc[1]), "+v"(xb.v1), "+v"(oo[0]), "+v"(oo[1]), "+v"(oo[2]));
#pragma unroll
    for (int qt = 0; qt < NT; ++qt) { m[qt] = 0.f; o[qt] = Z16; asm volatile("" : "+v"(o[qt])); }
    sc[0] = qk2(xa, 0, qk1(xa, 0));
    auto step = [&](KvT& x, KvT& y, int t) {
#pragma unroll
      for (int qt = 0; qt < NT; ++qt)
        phase(qt, qt ? x.v1 : y.v1, qt < NT - 1 ? x : y, x.v0, [&]() { if (qt == 0) fetch_v(y, t + 1); if (qt == NT - 2) fetch_k(x, t + 2); });
    };
#pragma unroll 1
    for (int t = 0; t < KQ / 32; t += 2) {
      if (t == ATT_P1 * 4 / NT) __builtin_amdgcn_s_setprio(2);
      if (t == ATT_P2 * 4 / NT) __builtin_amdgcn_s_setprio(1);
      if (t == ATT_P3 * 4 / NT) __builtin_amdgcn_s_setprio(0);
      step(xa, xb, t);
      step(xb, xa, t + 1);
    }
    o[NT - 1] = pv(xb.v1, sc[1], 1, o[NT - 1]);
    float big = 0.f;
#pragma unroll
    for (int qt = 0; qt < NT; ++qt) {
      big = fmx(big, hh ? (o[qt][8] >= 1.0f / ATT_LMAX ? o[qt][8] * (3.0e38f / ATT_LMAX) : INFINITY) : __builtin_fabsf(o[qt][8]));
#pragma unroll
      for (int i = 0; i < 12; ++i) if (i != 8) big = fmx(big, __builtin_fabsf(o[qt][i]));
    }
    if (__builtin_expect(!wave_all(big <= 3.0e38f), 0)) slow();
  }
#pragma unroll
  for (int qt = 1; qt < NT; ++qt) {
    f32x4 (*mo)[64] = mo_[(qt + kh) % NT * ATT_NQ + wid % ATT_NQ][qt - 1];
#pragma unroll
    for (int k = 0; k < 3; ++k) mo[k][lane] = (f32x4){o[qt][4 * k], k == 2 && hh ? m[qt] : o[qt][4 * k + 1], o[qt][4 * k + 2], o[qt][4 * k + 3]};
  }
  asm volatile("s_waitcnt lgkmcnt(0)\n\ts_barrier" ::: "memory");
  {
    f32x2 lm[NT - 1];
    f32x4 u[NT - 1][3];
#pragma unroll
    for (int i = 0; i < NT - 1; ++i) {
      lm[i] = *(const f32x2*)&mo_[wid][i][2][lane | 32];
#pragma unroll
      for (int k = 0; k < 3; ++k) u[i][k] = mo_[wid][i][k][lane];
    }
    float mn = m[0];
#pragma unroll
    for (int i = 0; i < NT - 1; ++i) mn = fmx(mn, lm[i][1]);
    float a0 = __builtin_amdgcn_exp2f(m[0] - mn), a[NT - 1];
    float l = u2f(__builtin_amdgcn_permlane32_swap(f2u(o[0][8]), f2u(o[0][8]), false, false)[1]) * a0;
#pragma unroll
    for (int i = 0; i < NT - 1; ++i) { a[i] = __builtin_amdgcn_exp2f(lm[i][1] - mn); l += lm[i][0] * a[i]; }
    const float inv = __builtin_amdgcn_rcpf(l);
    a0 *= inv;
#pragma unroll
    for (int i = 0; i < NT - 1; ++i) a[i] *= inv;
#pragma unroll
    for (int k = 0; k < 3; ++k) {
      f32x4 v = (f32x4){o[0][4 * k], o[0][4 * k + 1], o[0][4 * k + 2], o[0][4 * k + 3]} * a0;
#pragma unroll
      for (int i = 0; i < NT - 1; ++i) v += u[i][k] * a[i];
      __builtin_amdgcn_raw_buffer_store_b64((u32x2){pack2(v[0], v[1]), pack2(v[2], v[3])}, ro, oo[k], 0, 0);
    }
  }
}
#if defined(__HIP_DEVICE_COMPILE__)
#pragma clang attribute pop
#endif

__global__ __launch_bounds__(640, 3) void proj_kernel(const u16* __restrict__ Of, const u16* __restrict__ Wf, const u16* __restrict__ xold,
                                                      u16* __restrict__ xnew) {
  __shared__ __attribute__((aligned(16))) char ob[40960];
  const int tid = threadIdx.x, lane = tid & 63, w = __builtin_amdgcn_readfirstlane(tid >> 6), g = lane >> 4, c = lane & 15;
  __shared__ unsigned sink[64];
  own_code<1500>(tid, sink);
  const int rb = (blockIdx.x & 7) + 8 * (blockIdx.x >> 5), cb = (blockIdx.x >> 3) & 3;
  const int col = cb * 160 + w * 16 + 4 * g;
  const u16* wp = Wf + (size_t)(cb * 10 + w) * 20 * 512 + lane * 8;
  u32x4 st[4];
#pragma unroll
  for (int i = 0; i < 4; ++i) st[i] = *(const u32x4*)(Of + (size_t)rb * 40 * 512 + (i * 640 + tid) * 8);
  bf16x8 wf[20];
#pragma unroll
  for (int ks = 0; ks < 20; ++ks) wf[ks] = *(const bf16x8*)(wp + (size_t)ks * 512);
  u32x2 xo[2];
#pragma unroll
  for (int t = 0; t < 2; ++t) xo[t] = *(const u32x2*)(xold + (((size_t)(rb * 2 + t) * 20 + (col >> 5)) * 64 + ((col & 31) >> 3) * 16 + c) * 8 + (col & 7));
#pragma unroll
  for (int i = 0; i < 4; ++i) *(u32x4*)(ob + (i * 640 + tid) * 16) = st[i];
  BAR();
  f32x4 acc[2] = {{0.f, 0.f, 0.f, 0.f}, {0.f, 0.f, 0.f, 0.f}};
#pragma unroll
  for (int ks = 0; ks < 20; ++ks)
#pragma unroll
    for (int t = 0; t < 2; ++t) acc[t] = mfma32(wf[ks], *(const bf16x8*)(ob + (t * 20 + ks) * 1024 + lane * 16), acc[t]);
#pragma unroll
  for (int t = 0; t < 2; ++t) {
    const unsigned r01 = pack2(acc[t][0], acc[t][1]), r23 = pack2(acc[t][2], acc[t][3]);
    const unsigned p01 = pack2(u2f(xo[t][0] << 16) + u2f(r01 << 16), u2f(xo[t][0] & 0xffff0000u) + u2f(r01 & 0xffff0000u));
    const unsigned p23 = pack2(u2f(xo[t][1] << 16) + u2f(r23 << 16), u2f(xo[t][1] & 0xffff0000u) + u2f(r23 & 0xffff0000u));
    if (P_WT) __hip_atomic_store((gu64*)(xnew + (size_t)(rb * 32 + t * 16 + c) * DM + col), (u64)p01 | (u64)p23 << 32, RLX_AGENT);
    else *(u32x2*)(xnew + (size_t)(rb * 32 + t * 16 + c) * DM + col) = (u32x2){p01, p23};
  }
}

extern "C" void kernel_launch(void* const* d_in, const int* in_sizes, int n_in, void* d_out, int out_size, void* d_ws,
                              size_t ws_size, hipStream_t stream) {
  const u16* A_x = (const u16*)d_in[0];
  const u16* A_mem0 = (const u16*)d_in[1];
  const u16* A_Wq = (const u16*)d_in[2];
  const u16* A_Wk = (const u16*)d_in[3];
  const u16* A_Wv = (const u16*)d_in[4];
  const u16* A_Wo = (const u16*)d_in[5];
  const u16* B_wqkv = (const u16*)d_in[8];
  const u16* B_wo = (const u16*)d_in[9];
  char* ws = (char*)(((uintptr_t)d_ws + ((size_t)WS_OFF << 20) + WS_AL - 1) & ~(uintptr_t)(WS_AL - 1));
  u16* xa = (u16*)(ws + OFF_XA); u16* Ob = (u16*)(ws + OFF_OB);

  prologue_kernel<<<256, 640, 0, stream>>>(ProArgs{A_x, A_Wq, A_Wk, A_Wv, A_Wo, B_wqkv, B_wo, ws});
  for (int layer = 0; layer < 2; ++layer) {
    rec_kernel<<<256, 512, 0, stream>>>(RecArgs{A_mem0, A_Wq, A_Wk, (const u16*)(ws + OFF_XB), xa, ws, (unsigned)layer, 0u});
    attb_kernel<<<BH * 8, 512, 0, stream>>>((const u16*)(ws + OFF_QB), (const u16*)(ws + OFF_KV), Ob);
  }
  proj_kernel<<<256, 640, 0, stream>>>(Ob, (const u16*)(ws + OFF_WOBT), xa, (u16*)d_out);
}
```

```cpp
#include <hip/hip_runtime.h>
#include <hip/hip_bf16.h>
#include <stdint.h>
#include <type_traits>

typedef unsigned short u16;
typedef unsigned long long u64;
typedef __attribute__((ext_vector_type(8))) short bf16x8;
typedef __attribute__((ext_vector_type(16))) short bf16x16;
typedef __attribute__((ext_vector_type(4))) float f32x4;
typedef __attribute__((ext_vector_type(2))) float f32x2;
typedef __attribute__((ext_vector_type(16))) float f32x16;
typedef __attribute__((ext_vector_type(8))) float f32x8;
typedef __attribute__((ext_vector_type(4))) unsigned u32x4;
typedef __attribute__((ext_vector_type(2))) unsigned u32x2;
typedef __attribute__((ext_vector_type(3))) unsigned u32x3;
typedef __attribute__((ext_vector_type(8))) unsigned u32x8;
typedef __attribute__((ext_vector_type(16))) unsigned u32x16;
typedef __attribute__((ext_vector_type(32))) unsigned u32x32;
#define GAS __attribute__((address_space(1)))
#define LAS __attribute__((address_space(3)))
typedef GAS unsigned long long gu64;
#define BAR() do { __builtin_amdgcn_s_waitcnt(0xC07F); asm volatile("s_waitcnt lgkmcnt(0)\n\ts_barrier" ::: "memory"); } while (0)

constexpr int S_ = 2048, DM = 640;
constexpr int NH = 32, DH = 192, HD = NH * DH;
constexpr int MEM = 8, SEG = 32, NSEG = S_ / SEG;
constexpr int BH = 32, BD = 20;

constexpr size_t al256(size_t x) { return (x + 255) & ~(size_t)255; }
constexpr int LAG3 = 2, LAG4 = 3, LAG5 = 6;
#define SR_ 8
constexpr int SR = SR_;
constexpr unsigned SCT_STEP = 131072, OFF_SCM = SR * SCT_STEP, SCM_STEP = 32768;
constexpr int ACC_NB = 192;
constexpr size_t ACC_STEP = 65536;
constexpr unsigned ACC_TS = ACC_NB * 256;
constexpr size_t ACC_LAYER = SR * ACC_STEP;
constexpr size_t OFF_ACC = 3 * 524288;
static_assert(SR == 8, "regions");
__device__ __forceinline__ unsigned ts_piece(int i) { return ACC_TS + (unsigned)(i < 128 ? (i >> 1) * 256 + (i & 1) * 16 : 128 + (i & 1) * 8192); }
constexpr size_t OFF_LNX = OFF_SCM + SR * SCM_STEP;
constexpr size_t OFF_RDY = OFF_LNX + 65536;
constexpr size_t OFF_FIN = OFF_RDY + 2048;
constexpr size_t PG = 2097152;
#define RING_ 4
constexpr int RING = RING_;
constexpr size_t TS_SLOT = (size_t)NH * 8 * 2560, PT_SLOT = (size_t)NH * 2560, OP_SLOT = (size_t)256 * 2560;
__device__ __forceinline__ unsigned ts_tok(int h, int j) { return (unsigned)((h & 15) * 32768 + (j >> 2) * 16384 + (j & 3) * 2048); }
__device__ __forceinline__ unsigned ts_mem(int h, int j) { return (unsigned)(524288 + ((h & 15) >> 1) * 16384 + (h & 1) * 4096 + j * 512); }
__device__ __forceinline__ unsigned pt_tok(int h) { return (unsigned)(((h & 15) >> 2) * 16384 + (h & 3) * 2048); }
__device__ __forceinline__ unsigned pt_mem(int h) { return (unsigned)(65536 + (h & 15) * 512); }
constexpr size_t OFF_X = 2 * PG + TS_SLOT + 3 * PT_SLOT, X_BYTES = 7 * OP_SLOT + 4 * PT_SLOT;
constexpr size_t OFF_OP = OFF_X + TS_SLOT + 4 * PT_SLOT;
constexpr size_t COMM_BYTES = OFF_X + X_BYTES + RING * OP_SLOT;
__device__ __forceinline__ unsigned near_half(unsigned ck, unsigned o) { return o + ((ck >> (o >= 2 * PG ? 8 : 4)) & 8192u); }
__device__ __forceinline__ unsigned ts_slot(unsigned ck, int sl) {
  return near_half(ck, sl == NSEG - 1 ? (unsigned)OFF_X : ((sl & 3) == 3 ? (unsigned)(2 * PG) : (unsigned)PG + (unsigned)(sl & 3) * (unsigned)TS_SLOT));
}
__device__ __forceinline__ unsigned pt_slot(unsigned ck, int sl) {
  return near_half(ck, sl >= NSEG - 4 ? (unsigned)(OFF_X + TS_SLOT) + (unsigned)(sl - (NSEG - 4)) * (unsigned)PT_SLOT
                                      : ((sl & 3) == 0 ? (unsigned)(PG + 3 * TS_SLOT) : (unsigned)(2 * PG + TS_SLOT - PT_SLOT) + (unsigned)(sl & 3) * (unsigned)PT_SLOT));
}
__device__ __forceinline__ unsigned op_slot(int sl) { return (unsigned)OFF_OP + (unsigned)(sl >= NSEG - 6 ? sl - (NSEG - 6) : 6 + sl % RING) * (unsigned)OP_SLOT; }
static_assert(RING == 4 && OFF_OP <= 3 * PG && OFF_FIN + 16384 <= OFF_ACC && OFF_ACC + ACC_LAYER == PG && TS_SLOT % 16384 == 0 && PT_SLOT % 16384 == 0, "pages");
static_assert(TS_SLOT == OP_SLOT && RING >= 4 && NSEG % SR == 0 && SR >= 5 && X_BYTES % 4096 == 0 && X_BYTES <= 256 * 20 * 1024, "slots");
constexpr size_t OFF_WB = (COMM_BYTES + 16383) / 16384 * 16384, WB_BYTES = (size_t)256 * 200 * 1024;
__device__ __forceinline__ unsigned wb_off(int F) { const unsigned o = (unsigned)((F >> 3) * 16384 + (F & 7) * 1024); return o | ((((unsigned)OFF_WB + o) >> 8) & 8192u); }
constexpr size_t OFF_PHI = OFF_WB + WB_BYTES;
constexpr size_t OFF_BNRM = OFF_PHI + 1024;
constexpr size_t OFF_LAT = OFF_BNRM + 1024;
constexpr size_t OFF_XNRM = OFF_LAT + 2048;
constexpr size_t OFF_WQKVBT = OFF_XNRM + al256(S_ * 16);
constexpr size_t OFF_WOBT = OFF_WQKVBT + al256((size_t)3 * DM * DM * 2);
constexpr size_t OFF_XN = OFF_WOBT + al256((size_t)DM * DM * 2);
constexpr int QST = BD;
constexpr size_t OFF_XA = OFF_XN + al256((size_t)S_ * DM * 2);
constexpr size_t OFF_XB = OFF_XA + al256((size_t)S_ * DM * 2);
constexpr size_t OFF_QB = OFF_XB + al256((size_t)S_ * DM * 2);
constexpr int KVT = 2624;
constexpr size_t OFF_KV = OFF_QB + al256((size_t)BH * S_ * QST * 2);
constexpr size_t OFF_OB = OFF_KV + al256((size_t)BH * 64 * KVT);
constexpr size_t OFF_DBG = OFF_OB + al256((size_t)S_ * DM * 2);
constexpr size_t WS_TOTAL = OFF_DBG + (size_t)2 * 64 * 8 * DM * 2;
#define WS_AL 2097152
#define WS_OFF 0
static_assert(WS_TOTAL + WS_AL + ((size_t)WS_OFF << 20) <= ((size_t)256 << 20), "workspace budget");

#if defined(__HIP_DEVICE_COMPILE__)
#pragma clang attribute push(__attribute__((target("no-packed-fp32-ops"))), apply_to = function)
#endif
__device__ __forceinline__ float u2f(unsigned v) { return __builtin_bit_cast(float, v); }
__device__ __forceinline__ unsigned f2u(float v) { return __builtin_bit_cast(unsigned, v); }
__device__ __forceinline__ float bf2f(unsigned v) { return __builtin_bit_cast(float, v << 16); }
__device__ __forceinline__ unsigned f2bf(float f) { __bf16 b = (__bf16)f; return (unsigned)__builtin_bit_cast(u16, b); }
__device__ __forceinline__ float rbf(float f) { return bf2f(f2bf(f)); }
typedef __attribute__((ext_vector_type(2))) __bf16 hbf16x2;
__device__ __forceinline__ unsigned pack2(float lo, float hi) { f32x2 v = {lo, hi}; hbf16x2 b = __builtin_convertvector(v, hbf16x2); return __builtin_bit_cast(unsigned, b); }
__device__ __forceinline__ f32x4 mfma32(bf16x8 a, bf16x8 b, f32x4 c) { return __builtin_amdgcn_mfma_f32_16x16x32_bf16(a, b, c, 0, 0, 0); }
typedef __attribute__((ext_vector_type(8))) __bf16 hbf16x8;
typedef __attribute__((ext_vector_type(16))) __bf16 hbf16x16;
__device__ __forceinline__ f32x4 smfma64(bf16x8 a, bf16x16 b, f32x4 c, int idx) {
  return __builtin_amdgcn_smfmac_f32_16x16x64_bf16(__builtin_bit_cast(hbf16x8, a), __builtin_bit_cast(hbf16x16, b), c, idx, 0, 0);
}
__device__ __forceinline__ bf16x16 cat16(bf16x8 a, bf16x8 b) { return __builtin_shufflevector(a, b, 0, 1, 2, 3, 4, 5, 6, 7, 8, 9, 10, 11, 12, 13, 14, 15); }
__device__ __forceinline__ bf16x8 half8(bf16x16 v, int hi) { return hi ? __builtin_shufflevector(v, v, 8, 9, 10, 11, 12, 13, 14, 15) : __builtin_shufflevector(v, v, 0, 1, 2, 3, 4, 5, 6, 7); }
__device__ __forceinline__ bf16x8 mk8(unsigned a, unsigned b, unsigned c, unsigned d) { u32x4 v = {a, b, c, d}; return __builtin_bit_cast(bf16x8, v); }
__device__ __forceinline__ unsigned dpp_ror8(unsigned v) { return (unsigned)__builtin_amdgcn_mov_dpp((int)v, 0x128, 0xf, 0xf, true); }
__device__ __forceinline__ float fmx(float a, float b) { return __builtin_elementwise_maximum(a, b); }

__device__ __forceinline__ int imax(int a, int b) { return a > b ? a : b; }
__device__ __forceinline__ int imin(int a, int b) { return a < b ? a : b; }
__device__ __forceinline__ unsigned umin(unsigned a, unsigned b) { return a < b ? a : b; }
__device__ __forceinline__ unsigned umax(unsigned a, unsigned b) { return a > b ? a : b; }
__device__ __forceinline__ bool wave_all(bool p) { return __builtin_amdgcn_ballot_w64(!p) == 0ull; }
__device__ __forceinline__ float x16mx(float x) { unsigned u = __builtin_bit_cast(unsigned, x); auto r = __builtin_amdgcn_permlane16_swap(u, u, false, false); return fmx(u2f(r[0]), u2f(r[1])); }
__device__ __forceinline__ float x32mx(float x) { unsigned u = __builtin_bit_cast(unsigned, x); auto r = __builtin_amdgcn_permlane32_swap(u, u, false, false); return fmx(u2f(r[0]), u2f(r[1])); }
__device__ __forceinline__ float x16sum(float x) { unsigned u = __builtin_bit_cast(unsigned, x); auto r = __builtin_amdgcn_permlane16_swap(u, u, false, false); return u2f(r[0]) + u2f(r[1]); }
__device__ __forceinline__ float x32sum(float x) { unsigned u = __builtin_bit_cast(unsigned, x); auto r = __builtin_amdgcn_permlane32_swap(u, u, false, false); return u2f(r[0]) + u2f(r[1]); }
template <int CTRL> __device__ __forceinline__ float dppf(float v) { return u2f((unsigned)__builtin_amdgcn_mov_dpp((int)f2u(v), CTRL, 0xf, 0xf, true)); }
#define OWN_PF 1
template <int BYTES> __device__ __forceinline__ void own_code(int tid, unsigned* sink) {
  if (!OWN_PF || __builtin_amdgcn_readfirstlane(tid >> 6) * 4096 >= BYTES) return;
  const char* pc; unsigned keep;
  asm volatile("s_getpc_b64 %0" : "=s"(pc));
  asm volatile("s_mov_b32 %0, m0\n\ts_mov_b32 m0, %3\n\ts_nop 0\n\tglobal_load_lds_dword %1, %2\n\ts_mov_b32 m0, %0"
               : "=&s"(keep) : "v"(tid * 64 < BYTES ? tid * 64 : 0), "s"(pc), "s"((unsigned)(uintptr_t)(LAS unsigned*)sink) : "memory");
}
constexpr int DPP_XOR1 = 0xB1, DPP_XOR2 = 0x4E, DPP_ROR4 = 0x124, DPP_ROR8 = 0x128;
#if defined(__HIP_DEVICE_COMPILE__)
#pragma clang attribute pop
#endif

typedef short v4i16_t __attribute__((ext_vector_type(4)));
__device__ __forceinline__ bf16x8 tr_frag(const char* lo_, const char* hi_) {
  const v4i16_t lo = __builtin_amdgcn_ds_read_tr16_b64_v4i16((LAS v4i16_t*)lo_), hi = __builtin_amdgcn_ds_read_tr16_b64_v4i16((LAS v4i16_t*)hi_);
  return (bf16x8){lo[0], lo[1], lo[2], lo[3], hi[0], hi[1], hi[2], hi[3]};
}
__device__ __forceinline__ int rec_wg(int gp, int m) { return (((gp & 1) * 16 + m) << 3) | (gp >> 1); }
__device__ __forceinline__ float wsum(float v) {
  v += dppf<DPP_XOR1>(v); v += dppf<DPP_XOR2>(v); v += dppf<DPP_ROR4>(v); v += dppf<DPP_ROR8>(v);
  return x32sum(x16sum(v));
}
__device__ __forceinline__ float ln_row(int lane, u32x4 (&xo)[2]) {
  const bool two = lane < 16;
  float v[16];
#pragma unroll
  for (int hf = 0; hf < 2; ++hf)
#pragma unroll
    for (int i = 0; i < 4; ++i) { const unsigned u = xo[hf][i]; v[hf * 8 + 2 * i] = bf2f(u & 0xffff); v[hf * 8 + 2 * i + 1] = bf2f(u >> 16); }
  float s0 = 0.f, s1 = 0.f;
#pragma unroll
  for (int i = 0; i < 8; ++i) { s0 += v[i]; s1 += v[8 + i]; }
  const float mean = wsum(two ? s0 + s1 : s0) * (1.0f / DM);
  s0 = s1 = 0.f;
#pragma unroll
  for (int i = 0; i < 8; ++i) { const float d0 = v[i] - mean, d1 = v[8 + i] - mean; s0 += d0 * d0; s1 += d1 * d1; }
  const float var = rbf(wsum(two ? s0 + s1 : s0) * (1.0f / DM));
  const float mb = rbf(mean);
  const float rden = 1.0f / rbf(sqrtf(rbf(var + 1e-5f)));
  float n2[2] = {0.f, 0.f};
#pragma unroll
  for (int hf = 0; hf < 2; ++hf) {
    float y[8];
#pragma unroll
    for (int i = 0; i < 8; ++i) { y[i] = rbf(rbf(v[hf * 8 + i] - mb) * rden); n2[hf] += y[i] * y[i]; }
    xo[hf] = (u32x4){pack2(y[0], y[1]), pack2(y[2], y[3]), pack2(y[4], y[5]), pack2(y[6], y[7])};
  }
  return wsum(two ? n2[0] + n2[1] : n2[0]);
}

#define PROT(k)
#define WARM_CODE 1
#define B_AUX 16
#define X_AUX 16
#define T_AUX 16
#define ATT_CODE 15000
#define FIN_D 4
#define FIN_SL 2
#define WB_NEAR 1
#define PF_POS 0
#define PRB_N0 3
#define PRB_N1 0
#define PRB_LOG(i, v)
#define PLD(p) __builtin_nontemporal_load((const u32x4*)(p))
struct ProArgs { const u16* x; const u16* wq; const u16* wk; const u16* wv; const u16* wo; const u16* wqkvb; const u16* wob; char* ws; };
__global__ __launch_bounds__(640, 3) void prologue_kernel(ProArgs a) {
  __shared__ __attribute__((aligned(16))) char img[192 * 320];
  __shared__ __attribute__((aligned(16))) char slots[10 * 6144];
  __shared__ float red[10][160];
  __shared__ unsigned latw[10];
  __shared__ u32x4 xst[2][640];
  const int b = blockIdx.x, tid = threadIdx.x, lane = tid & 63, v = __builtin_amdgcn_readfirstlane(tid >> 6), c = lane & 15, g = lane >> 4;
  const int h = (b & 7) * 4 + ((b >> 3) & 3), dh = (b >> 5) & 1, cq = b >> 6, ks = dh * 10 + v;
  char* const slot = slots + v * 6144;
  PROT(0);
  __shared__ unsigned sink[64];
  own_code<9600>(tid, sink);
  const unsigned zo = v < 8 ? (unsigned)(((b >> 6) * 512 + ((b & 7) * 2 + (v >> 2)) * 32 + ((b >> 3) & 7) * 4 + (v & 3)) * 1024) : (unsigned)(((v - 7) * 2048 + (b & 7) * 32 + ((b >> 3) & 7) * 4 + (b >> 6)) * 1024);
  __builtin_amdgcn_raw_buffer_store_b128((u32x4){0u, 0u, 0u, 0u}, __builtin_amdgcn_make_buffer_rsrc(a.ws, 0, (int)(3 * PG), 0x00020000), zo + lane * 16, 0, 16);
  __builtin_amdgcn_sched_barrier(0);
  u32x4 wa[2][6], wb[6], tr[4], xo[2];
  const int xrow = b * 8 + (v & 7);
  xo[0] = *(const u32x4*)(a.x + (size_t)xrow * DM + lane * 8);
  xo[1] = *(const u32x4*)(a.x + (size_t)xrow * DM + 512 + c * 8);
  const int job = imin(b + 256 * (9 - v), 800);
  const int jn = job < 600 ? job / 20 : (job % 200) / 20, jk = job % 20;
  {
    const u16* src = job < 600 ? a.wqkvb + (size_t)(32 * jk) * 3 * DM : a.wob + (size_t)(32 * jk) * DM;
#pragma unroll
    for (int i = 0; i < 4; ++i) tr[i] = *(const u32x4*)(src + (size_t)(i * 8 + (lane >> 3)) * (job < 600 ? 3 * DM : DM) + jn * 64 + (lane & 7) * 8);
  }
#pragma unroll
  for (int i = 0; i < 6; ++i) {
    const int idx = (v * 6 + i) * 64 + lane, row = idx / 20;
    wb[i] = PLD(a.wo + (size_t)(h * DH + row) * DM + cq * 160 + (idx - row * 20) * 8);
  }
#pragma unroll
  for (int i = 0; i < 6; ++i) {
    const int idx = i * 64 + lane, r = idx / 24;
#pragma unroll
    for (int u = 0; u < 2; ++u) wa[u][i] = PLD(a.wv + (size_t)(32 * ks + 8 * (r >> 2) + (r & 3) + 4 * u) * HD + h * DH + (idx - r * 24) * 8);
  }
  unsigned pf[2] = {0u, 0u};
  auto prefetch = [&]() {
#pragma unroll
    for (int i = 0; i < 2; ++i) pf[i] = *(const unsigned*)((i ? a.wq : a.wk) + ((size_t)(b * 10 + v) * 48 + imin(lane, 47)) * 32);
  };
  if (PF_POS == 0) prefetch();
  __builtin_amdgcn_sched_barrier(0);
  unsigned lat = ~0u;
  auto probe = [&](int n0, int n) {
    const char* p = a.ws + zo;
#pragma unroll 1
    for (int i = n0; i < n0 + n; ++i) {
      u64 t0, t1; unsigned d;
      asm volatile("s_memtime %0\n\ts_waitcnt lgkmcnt(0)\n\ts_load_dword %2, %3, %4\n\ts_waitcnt lgkmcnt(0)\n\ts_memtime %1\n\ts_waitcnt lgkmcnt(0)"
                   : "=&s"(t0), "=&s"(t1), "=&s"(d) : "s"(p), "s"((i & 3) * 256 + (i >> 2) * 128) : "memory");
      PRB_LOG(i, (unsigned)t1 - (unsigned)t0);
      lat = umin(lat, (unsigned)t1 - (unsigned)t0);
    }
  };
  asm volatile("s_waitcnt vmcnt(26)" ::: "memory");
  probe(0, PRB_N0);
  latw[v] = lat;
  __builtin_amdgcn_sched_barrier(0);
  auto xput = [&](int k) {
    if (v < 8) { xst[k][lane * 8 + (v ^ (lane & 7))] = xo[0]; if (lane < 16) xst[k][(64 + lane) * 8 + (v ^ (lane & 7))] = xo[1]; }
  };
  xput(0);
  const float xn2 = u2f(__builtin_amdgcn_readfirstlane(f2u(ln_row(lane, xo))));
  xput(1);
#pragma unroll
  for (int i = 0; i < 4; ++i) *(u32x4*)(slot + (i * 8 + (lane >> 3)) * 144 + (lane & 7) * 16) = tr[i];
  __builtin_amdgcn_fence(__ATOMIC_RELEASE, "wavefront"); __builtin_amdgcn_wave_barrier();
  bf16x8 tf[4];
#pragma unroll
  for (int t = 0; t < 4; ++t) {
    const char* bp = slot + (8 * g + (c >> 2)) * 144 + t * 32 + (c & 3) * 8;
    tf[t] = tr_frag(bp, bp + 4 * 144);
  }
  __builtin_amdgcn_fence(__ATOMIC_RELEASE, "wavefront"); __builtin_amdgcn_wave_barrier();
  __builtin_amdgcn_sched_barrier(0);
#pragma unroll
  for (int i = 0; i < 6; ++i) {
    const int idx = (v * 6 + i) * 64 + lane, row = idx / 20;
    *(u32x4*)(img + row * 320 + (((idx - row * 20) ^ ((row >> 2) & 2)) * 16)) = wb[i];
  }
  bf16x8 A[2][6];
#pragma unroll
  for (int u = 0; u < 2; ++u) {
#pragma unroll
    for (int i = 0; i < 6; ++i) {
      const int idx = i * 64 + lane, r = idx / 24;
      *(u32x4*)(slot + r * 384 + (((idx - r * 24) ^ (r >> 1)) * 16)) = wa[u][i];
    }
    __builtin_amdgcn_fence(__ATOMIC_RELEASE, "wavefront"); __builtin_amdgcn_wave_barrier();
#pragma unroll
    for (int e = 0; e < 6; ++e) A[u][e] = *(const bf16x8*)(slot + c * 384 + (((4 * e + g) ^ (c >> 1)) * 16));
    __builtin_amdgcn_fence(__ATOMIC_RELEASE, "wavefront"); __builtin_amdgcn_wave_barrier();
  }
  BAR();
  PROT(1);
  if (PF_POS == 0) asm volatile("" :: "v"(pf[0]), "v"(pf[1]));
  if (PF_POS == 1) prefetch();
  {
    const unsigned xf = (unsigned)((b >> 1) * 20480 + (tid >> 3) * 256 + (b & 1) * 128 + (tid & 7) * 16);
    const __amdgpu_buffer_rsrc_t rx = __builtin_amdgcn_make_buffer_rsrc(a.ws + OFF_XN, 0, (int)(OFF_QB - OFF_XN), 0x00020000);
    __builtin_amdgcn_raw_buffer_store_b128(xst[1][tid ^ ((tid >> 3) & 7)], rx, xf, 0, X_AUX);
    __builtin_amdgcn_raw_buffer_store_b128(xst[0][tid ^ ((tid >> 3) & 7)], rx, xf + (unsigned)(OFF_XB - OFF_XN), 0, X_AUX);
    if (v < 8 && lane < 4) ((float*)(a.ws + OFF_XNRM))[xrow * 4 + lane] = lane == 0 ? xn2 : 0.f;
  }
  if (job < 800) {
    const __amdgpu_buffer_rsrc_t rt = __builtin_amdgcn_make_buffer_rsrc(a.ws + (job < 600 ? OFF_WQKVBT : OFF_WOBT) + ((size_t)jn * 80 + jk) * 1024, 0, 81920, 0x00020000);
#pragma unroll
    for (int t = 0; t < 4; ++t) __builtin_amdgcn_raw_buffer_store_b128(__builtin_bit_cast(u32x4, tf[t]), rt, lane * 16 + t * 20480, 0, T_AUX);
  }
  const int gp = h >> 1, hh = h & 1;
  const __amdgpu_buffer_rsrc_t rwb = __builtin_amdgcn_make_buffer_rsrc(a.ws + OFF_WB, 0, (int)WB_BYTES, 0x00020000);
  const unsigned phi = WB_NEAR ? ((((unsigned)b >> 4) & 1u) ^ (unsigned)(latw[8] < latw[9]) ^ (WB_NEAR - 1)) << 13 : (unsigned)(b & 32) << 8;
  if (tid == 0) ((unsigned*)(a.ws + OFF_PHI))[b] = phi;
#pragma unroll
  for (int n = 0; n < 10; ++n) {
    const int off = n * 32 + 8 * (c & 3), T = cq * 10 + n;
    const char* bp = img + (8 * g + (c >> 2)) * 320 + (off ^ ((g & 1) << 5));
    f32x4 ac[2] = {{0.f, 0.f, 0.f, 0.f}, {0.f, 0.f, 0.f, 0.f}};
#pragma unroll
    for (int e = 0; e < 6; ++e) {
      const bf16x8 bf = tr_frag(bp + e * 32 * 320, bp + (e * 32 + 4) * 320);
      ac[0] = mfma32(A[0][e], bf, ac[0]); ac[1] = mfma32(A[1][e], bf, ac[1]);
    }
    const u32x4 o = {pack2(ac[0][0], ac[0][1]), pack2(ac[0][2], ac[0][3]), pack2(ac[1][0], ac[1][1]), pack2(ac[1][2], ac[1][3])};
    const unsigned oo = (T < 32 ? wb_off(rec_wg(gp, T & 15) * 100 + (hh * 3 + (T >> 4)) * 20 + ks) + (unsigned)(lane * 16)
                                : wb_off(rec_wg(gp, 2 * (T - 32) + (c >> 3)) * 100 + 40 + ks) + (unsigned)((g * 16 + hh * 8 + (c & 7)) * 16)) ^ phi;
    __builtin_amdgcn_raw_buffer_store_b128(o, rwb, oo, 0, B_AUX);
    float n2 = 0.f;
#pragma unroll
    for (int r = 0; r < 4; ++r) n2 += ac[0][r] * ac[0][r] + ac[1][r] * ac[1][r];
    n2 = x32sum(x16sum(n2));
    if (g == 0) red[v][n * 16 + c] = n2;
  }
  PROT(2);
  if (PF_POS == 1) asm volatile("" :: "v"(pf[0]), "v"(pf[1]));
  if (v >= 8 && b < 64 && lane == 0) ((unsigned*)(a.ws + OFF_LAT))[(v - 4) * 64 + b] = umin(lat, 60000u);
  if (WARM_CODE && v == 7) {
    const char* pc; int d0, d1;
    asm("s_getpc_b64 %0\n\ts_mov_b32 %1, _Z10rec_kernel7RecArgs@rel32@lo+4\n\ts_mov_b32 %2, _Z11attb_kernelPcPt@rel32@lo+12" : "=s"(pc), "=s"(d0), "=s"(d1));
    const char *c0 = pc + d0, *c1 = pc + d1 + ATT_CODE;
    const long o = ((long)b * 64 + lane) * 64;
    if (o < c1 - c0) asm volatile("" :: "v"(*(const volatile GAS unsigned*)(c0 + o)));
  }
  if (v == 9 && lane < 32) {
    const int q = b * 32 + lane;
    *(u32x4*)(a.ws + OFF_KV + (size_t)(q >> 2) * KVT + 1280 + (q & 3) * 336 + 320) = (u32x4){0x3f803f80u, 0x3f803f80u, 0x3f803f80u, 0x3f803f80u};
  }
  BAR();
  if (v == 1 && lane == 0) ((unsigned*)(a.ws + OFF_LAT))[b] = umin(umin(umin(latw[0], latw[4]) + umin(latw[1], latw[5]), 30000u) + umin(umin(latw[2], latw[6]) + umin(latw[3], latw[7]), 30000u), 60000u);
  if (v == 0) {
    float mx = 0.f;
#pragma unroll
    for (int k = 0; k < 3; ++k) {
      const int col = k * 64 + lane;
      float sm = 0.f;
#pragma unroll
      for (int i = 0; i < 10; ++i) sm += red[i][col < 160 ? col : 0];
      mx = fmx(mx, sm);
    }
    mx = fmx(mx, dppf<DPP_XOR1>(mx)); mx = fmx(mx, dppf<DPP_XOR2>(mx)); mx = fmx(mx, dppf<DPP_ROR4>(mx)); mx = fmx(mx, dppf<DPP_ROR8>(mx));
    mx = x32mx(x16mx(mx));
    if (lane == 0) ((float*)(a.ws + OFF_BNRM))[(cq * 32 + h) * 2 + dh] = mx;
  }
  PROT(3);
}

struct RecArgs {
  const u16* mem0; const u16* wq; const u16* wk; const u16* xold; u16* xnew; char* ws;
  unsigned layer; unsigned pad;
};
#define RLX_AGENT __ATOMIC_RELAXED, __HIP_MEMORY_SCOPE_AGENT
constexpr unsigned SPIN_CAP = 1u << 20;
#define TSTAMP(k, v)
#define ARRIVE(k)
#define ARRIVE_OUT()
#define ARRIVE_DECL()
#define LOGV(k, v)
#define LOGF(k, v)
#define LOGP(k)
#define LOGQ(k)
#define LOGF2(k)
#define LOGD(k)
#define LOGT(k, v)
#define STAMP_DECL()
#define STAMP(k)
#define STAMPV(k, v)
#define STAMP_OUT()
#define REC_D1 520
#define SCH_V 10
#define SCH_VMIN 0
#define SCH_DMIN 4
#define SCH_PEN 150
#define SCH_VMIN2 (-4)
#define SCH_PEN2 40
#define SCH_GU 1
#define SCH_GD 2
#define SCH_UP 6
#define SCH_D0 196
#define SCH_FIRST 120
#define EXP_NODMA 0
#define BG_HOLD 220
#define QBATCH 0
#define WPF_N 5
#define WPF_W 0
#define WPF0 (63 - WPF_N)
#define BG_HOLD4 250
#define D1_W1 20
#define F3_D 1200
#define T2W 2
#define AT_ASM 1
#define PM_Z 1
#define P2_X 1
#define ST_KS 14
#define ST_KS0 ST_KS
#define ST_KS1 ST_KS
#define PADR 0x6666
#define PADT 0x000
#define CODE_PAD(n) asm volatile(".p2align 6\n.rept %0\ns_nop 0\n.endr" :: "n"(n))
#define ST_X 250
#define ST_ALL 4
#define CPF_S 61
#define FR_D 700
#define FR_PACE 6
#define END_E60 600
#define END_E1 1300
#define END_E2 3400
#define END_E62 2800
#define END_E3 6800
#define END_E4 11400
#define EARLY_N 6
#define EARLY_D1 0
#define EARLY_V 10
#define EARLY_V1 18
#define F3 0
#define F3A 0
#define F3A_D 400
#define T4_D 300
#define T4_P 1
#define P2_W 1
#define PS_SL 1
#define F2B 4
#define F0C 0
#define F2C 0
#define W2_P 1
#define SCH_W 1
#define SP_SL 0
#define D1_M 300
#define D1_MIN 260
#define D1_C 96
#define D1_G 2
#define BG_HOLD6 0
#define W2_D 0
#define END_E61 2600
#define D1_LATE 200
#define P2_G0 10
#define P2_G2 6
#define P2_G3 6
#define SCH_RL 0
#define SCH_P 3
#define MMA_P 1
#define SP_P 0
#define F6 8
#define F7 10
#define XPF_W 2
#define SCH_LATE 0
#define LATE_V 8
#define QW 3
#define FB_SL 0
#define P2_G1 1
#define P1_G 0
#define F4Z 0
#define F5Z 0
#define T5_Z 2
#define F0A 3
#define FB_E 0
#define EXACT 1
#define D1_P 3
#define VO_A0 9
#define VO_A2 10
#define VO_B0 3
#define VO_B1 0
#define VO_N2 7
#define VO_N3 10
#define VO_W -1
#define UN_P 2
#define VP_E 1
#define TS_B 0
#define ST_E 1
#define W1_SL 0
#define VO_A1 4
#define LAST_V 22
#define END_E59 3200
#define END_G2 600
#define END_G3 600
#define QR 4
#define WPF_S 128
#define END_SL 4
#define SCH_DR 182
#define SCH_DK 3
#define FR_HOLD 0
#define FW 8
#define PR_P 0
#define REL_AT 1
#define XO_T 4
#define Q_AUX 0
#define O_WT 0
#define P_WT 0
#define KP0 0
#define KP1 0
#define XPF 3
#define W1_D 0
#define F4 12
#define F5 0
#define F67 0
#define KP2 0
#define REC_RNG 70u
#define SCH_DRAIN 40
#define PL_X 0
#define FC(k)
#define PIN_V(x) asm volatile("" : "+v"(x))
#define PIN_S(x) asm volatile("" : "+s"(x))
#define VM_DRAIN() do { asm volatile("s_waitcnt vmcnt(0)" ::: "memory"); __builtin_amdgcn_s_waitcnt(0x0F70); } while (0)
#define SC1_LOAD(off) __builtin_amdgcn_raw_buffer_load_b128(rs, (off), 0, 16)
#define SC1_LOADS(off, soff) __builtin_amdgcn_raw_buffer_load_b128(rs, (off), (soff), 16)
#define SC1_STORE8S(v, off, soff) __builtin_amdgcn_raw_buffer_store_b64((v), rs, (off), (soff), 16)
#define SC1_LOAD8(off) __builtin_amdgcn_raw_buffer_load_b64(rs, (off), 0, 16)
#define SC1_STORE(v, off) __builtin_amdgcn_raw_buffer_store_b128((v), rs, (off), 0, 16)
#define SC1_STORE8(v, off) __builtin_amdgcn_raw_buffer_store_b64((v), rs, (off), 0, 16)
#if defined(__HIP_DEVICE_COMPILE__)
#pragma clang attribute push(__attribute__((target("no-packed-fp32-ops"))), apply_to = function)
#endif
__device__ __forceinline__ unsigned now32() { return (unsigned)__builtin_amdgcn_s_memrealtime(); }
__device__ __forceinline__ void wait_until(unsigned t) { for (;;) { const int d = (int)(t - now32()); if (d <= 0) break; if (d > 8) __builtin_amdgcn_s_sleep(1); } }
__device__ __forceinline__ unsigned cyc32() { return (unsigned)__builtin_amdgcn_s_memtime(); }
__device__ __forceinline__ void wait_cyc(unsigned t) { for (;;) { const int d = (int)(t - cyc32()); if (d <= 0) break; if (d > 200) __builtin_amdgcn_s_sleep(1); } }
#define WAIT_ASM(clk, far_) "1:\n\t" clk " vcc\n\ts_waitcnt lgkmcnt(0)\n\ts_sub_i32 %0, %1, vcc_lo\n\ts_cmp_lt_i32 %0, " far_ "\n\ts_cbranch_scc1 3f\n\ts_sleep 1\n\ts_branch 1b\n" \
                            "2:\n\t" clk " vcc\n\ts_waitcnt lgkmcnt(0)\n\ts_sub_i32 %0, %1, vcc_lo\n3:\n\ts_cmp_gt_i32 %0, 0\n\ts_cbranch_scc1 2b"
__device__ __forceinline__ int spin_until(unsigned t) { int d; asm volatile(WAIT_ASM("s_memrealtime", "9") : "=&s"(d) : "s"(t) : "vcc", "scc", "memory"); return d; }
__device__ __forceinline__ int spin_cyc(unsigned t) { int d; asm volatile(WAIT_ASM("s_memtime", "200") : "=&s"(d) : "s"(t) : "vcc", "scc", "memory"); return d; }
__device__ __forceinline__ void spin_exact(unsigned t) {
  if (!EXACT) { spin_cyc(t); return; }
  int d;
  asm volatile("1:\n\ts_memtime vcc\n\ts_waitcnt lgkmcnt(0)\n\ts_sub_i32 %0, %1, vcc_lo\n\ts_cmp_gt_i32 %0, 95\n\ts_cbranch_scc1 1b\n\t"
               "s_max_i32 %0, %0, 0\n\ts_andn2_b32 %0, %0, 3\n\ts_sub_i32 %0, 3f-2f, %0\n\ts_getpc_b64 vcc\n"
               "2:\n\ts_add_u32 vcc_lo, vcc_lo, %0\n\ts_addc_u32 vcc_hi, vcc_hi, 0\n\ts_setpc_b64 vcc\n\t.rept 24\n\ts_nop 0\n\t.endr\n3:"
               : "=&s"(d) : "s"(t) : "scc", "vcc", "memory");
}
template <int OFF, int SL> __device__ __forceinline__ void lds_spin(unsigned lds0, unsigned want) {
  unsigned v;
  asm volatile("1:\n\tds_read_b32 %0, %1 offset:%3\n\t.if %4\n\ts_sleep %4\n\t.endif\n\ts_waitcnt lgkmcnt(0)\n\tv_cmp_gt_u32 vcc, %2, %0\n\ts_cbranch_vccnz 1b"
               : "=&v"(v) : "v"(lds0), "s"(want), "n"(OFF), "n"(SL) : "vcc", "memory");
}
template <int CTRL> __device__ __forceinline__ int dppi(int v) { return __builtin_amdgcn_mov_dpp(v, CTRL, 0xf, 0xf, true); }
__device__ __forceinline__ int wave_imax(int l) {
  l = imax(l, dppi<DPP_XOR1>(l)); l = imax(l, dppi<DPP_XOR2>(l)); l = imax(l, dppi<DPP_ROR4>(l)); l = imax(l, dppi<DPP_ROR8>(l));
  int a = imax(__builtin_amdgcn_readlane(l, 0), __builtin_amdgcn_readlane(l, 16)), b = imax(__builtin_amdgcn_readlane(l, 32), __builtin_amdgcn_readlane(l, 48));
  PIN_S(a); PIN_S(b);
  return imax(a, b);
}
__device__ __forceinline__ void glds16(const void* gsrc, unsigned lds_dst) {
  unsigned keep;
  asm volatile("s_mov_b32 %0, m0\n\ts_mov_b32 m0, %2\n\ts_nop 0\n\tglobal_load_lds_dwordx4 %1, off\n\ts_mov_b32 m0, %0" : "=&s"(keep) : "v"(gsrc), "s"(lds_dst) : "memory");
}
__device__ __forceinline__ void glds16_sc1(const void* gsrc, unsigned lds_dst) {
  unsigned keep;
  asm volatile("s_mov_b32 %0, m0\n\ts_mov_b32 m0, %2\n\ts_nop 0\n\tglobal_load_lds_dwordx4 %1, off sc1\n\ts_mov_b32 m0, %0" : "=&s"(keep) : "v"(gsrc), "s"(lds_dst) : "memory");
}
__device__ __forceinline__ unsigned acc_off(int G, int row) {
  return (unsigned)(G < 128 ? ((G & 31) | ((G & 32) << 1) | ((G & 64) >> 1)) * 256 + row * 16 : (2 * G - 128 + (row >> 2)) * 256 + (row & 3) * 16);
}
__device__ __forceinline__ unsigned nz(unsigned v) { return v > 1u ? v : 1u; }
__device__ __forceinline__ unsigned pack2n(float lo, float hi) { return nz(pack2(lo, hi)); }
__device__ __forceinline__ unsigned min4(u32x4 v) { return umin(umin(v[0], v[1]), umin(v[2], v[3])); }
__device__ __forceinline__ bf16x8 as8(u32x4 v) { return __builtin_bit_cast(bf16x8, v); }

constexpr int NV = 7, NKQ = 2;
constexpr int L_RED1 = 0;
constexpr int L_BM = L_RED1 + 8 * 1536;
constexpr int L_STG = L_BM + 10240;
constexpr int L_ST = L_STG + 2048 + 2560;
constexpr int L_MISC = L_ST + 512;
constexpr int L_KX = L_MISC + 128;
constexpr int L_QX = L_KX + NKQ * 2048;
constexpr int L_XN = L_QX + NKQ * 2048;
constexpr int L_VOX = L_XN + 81920;
constexpr int L_VOM = L_VOX + NV * 5120;
constexpr int L_BYTES = L_VOM + NV * 1280;
constexpr int S_IMG = L_XN + 40960;
static_assert(S_IMG + 61440 <= L_VOM, "start-up image");
static_assert(L_BYTES <= 160 * 1024, "LDS budget");

__device__ __forceinline__ bf16x8 wqk_frag(const char* lds, int t, int ks, int lane) {
  const int c = lane & 15, g = lane >> 4, p = c & 3;
  const char* q = lds + S_IMG + (32 * ks + 8 * g + (c >> 2)) * 96 + 8 * p + (t == 0 ? 0 : (t == 1 ? 48 : (p < 2 ? 32 : 64)));
  const v4i16_t lo = __builtin_amdgcn_ds_read_tr16_b64_v4i16((LAS v4i16_t*)q), hi = __builtin_amdgcn_ds_read_tr16_b64_v4i16((LAS v4i16_t*)(q + 4 * 96));
  return (bf16x8){lo[0], lo[1], lo[2], lo[3], hi[0], hi[1], hi[2], hi[3]};
}
__device__ __forceinline__ bf16x8 sel_frag(int c, int base) {
  bf16x8 r;
#pragma unroll
  for (int i = 0; i < 8; ++i) r[i] = (c == base + i) ? (short)0x3f80 : (short)0;
  return r;
}
__device__ __forceinline__ bf16x8 qk_frag(const char* p4, const char* p2, const char* p2b) {
  f32x4 a0 = *(const f32x4*)(p4), a1 = *(const f32x4*)(p4 + 1792), a2 = *(const f32x4*)(p4 + 3584), a3 = *(const f32x4*)(p4 + 5376);
  f32x2 b0 = *(const f32x2*)(p2), b1 = *(const f32x2*)(p2 + 1792), b2 = *(const f32x2*)(p2b), b3 = *(const f32x2*)(p2b + 1792);
  f32x4 va = (a0 + a1) + (a2 + a3);
  f32x2 vb = (b0 + b1) + (b2 + b3);
  return mk8(pack2(va[0], va[1]), pack2(va[2], va[3]), pack2(vb[0], vb[1]), 0u);
}
__device__ __forceinline__ void x_fetch(const u16* src, unsigned lds0, int seg, int p, int lane) {
  if (EXP_NODMA) return;
  glds16_sc1(src + (size_t)p * 512 + lane * 8, lds0 + L_XN + (seg & 1) * 40960 + p * 1024);
}
template <int N> __device__ __forceinline__ void x_fetch_n(const u16* src, unsigned lds0, int seg, int p0, unsigned voff) {
  if (EXP_NODMA) return;
#pragma unroll
  for (int i = 0; i < N; i += 4) {
    unsigned keep;
    asm volatile("s_mov_b32 %0, m0\n\ts_mov_b32 m0, %3\n\ts_nop 0\n\tglobal_load_lds_dwordx4 %1, %2 sc1\n\t"
                 ".if %4 > 1\n\tglobal_load_lds_dwordx4 %1, %2 offset:1024 sc1\n\t.endif\n\t.if %4 > 2\n\tglobal_load_lds_dwordx4 %1, %2 offset:2048 sc1\n\t.endif\n\t"
                 ".if %4 > 3\n\tglobal_load_lds_dwordx4 %1, %2 offset:3072 sc1\n\t.endif\n\ts_mov_b32 m0, %0"
                 : "=&s"(keep) : "v"(voff), "s"(src + (size_t)(p0 + i) * 512), "s"(lds0 + L_XN + (seg & 1) * 40960 + (p0 + i) * 1024), "n"(N - i < 4 ? N - i : 4) : "memory");
  }
}
__device__ __forceinline__ int qkv_row(int b) { return (b & 7) / QR * (2 * QR) + (b >> 3) / (16 / QR); }
__device__ __forceinline__ int qkv_col(int b) { return (b & 7) % QR * (16 / QR) + (b >> 3) % (16 / QR); }
__device__ __forceinline__ const u16* x_src(const RecArgs& a, int seg, int b) {
  return seg < NSEG ? (const u16*)(a.ws + OFF_XN) + (size_t)seg * 32 * DM : a.xnew + (size_t)(qkv_row(b) + 16 * (seg - NSEG)) * 32 * DM;
}
template <int N>
__device__ __forceinline__ void img_fetch(const RecArgs& a, unsigned lds0, int h, int j, int part, int lane) {
#pragma unroll
  for (int i = 0; i < N; ++i) {
    const int n = part * N + i, idx = n * 64 + lane, row = idx / 6, pc = idx - row * 6;
    if (FR_PACE && N == 20) asm volatile("s_waitcnt vmcnt(%0)" :: "n"(FR_PACE) : "memory");
    glds16((pc < 3 ? a.wq : a.wk - 24) + (size_t)row * HD + h * DH + j * 24 + pc * 8, lds0 + S_IMG + n * 1024);
  }
}
constexpr int P2B = F4 + F5 + 2 * F67 + F6 + F7, PZ = P2B + F2B, PA = PZ, P0C = PA + 2 * F0A, F3C = 40 - F3A - F3 - P0C - 2 * F0C - F2C;
static_assert(F3C >= 0, "fetch plan");
template <int OFF> __device__ __forceinline__ void lds_inc(unsigned lds0) {
  asm volatile("s_mov_b64 exec, 1\n\tds_add_u32 %0, %1 offset:%2\n\ts_mov_b64 exec, -1" :: "v"(lds0), "v"(1u), "n"(OFF) : "memory");
}
__device__ __forceinline__ void lds_arrive(unsigned* p, int lane) {
  asm volatile("s_waitcnt lgkmcnt(0)" ::: "memory");
  if (lane == 0) __hip_atomic_fetch_add(p, 1u, __ATOMIC_RELAXED, __HIP_MEMORY_SCOPE_WORKGROUP);
}
__device__ __forceinline__ void lds_wait(unsigned* p, unsigned want) {
  while (__hip_atomic_load(p, __ATOMIC_RELAXED, __HIP_MEMORY_SCOPE_WORKGROUP) < want) __builtin_amdgcn_s_sleep(1);
  asm volatile("" ::: "memory");
}
constexpr float KEXP = 0.07216878364870322f * 1.4426950408889634f;
#define FRESH_LANE() int lane = lane0; asm volatile("" : "+v"(lane)); const int c = lane & 15, g = lane >> 4; (void)c; (void)g

struct OutTask {
  u32x4 y[4]; u32x2 xo, out;
  bf16x8 sel;
  unsigned t5f, t5o;
  __device__ __forceinline__ void init(int b, int lane) {
    const int um = b / 10, uq = (b / 5) & 1, up = b % 5, tok = lane & 15, k = up < 4 ? (up >> 1) * 256 + um * 16 + (up & 1) * 8 : 512 + um * 8, q4 = ((lane >> 4) & 1) * 4;
    t5f = (unsigned)(((uq * 20 + (k >> 5)) * 64 + ((k & 31) >> 3) * 16 + tok) * 8 + q4);
    t5o = (unsigned)(((um * 2 + uq) * 16 + (lane >> 4)) * 1280 + up * 256 + tok * 16);
    if (b >= 160) { t5f = 0u; t5o = 0x7ffffff0u; }
    sel = sel_frag(tok, 0);
    PIN_V(sel);
  }
  static __device__ __forceinline__ bool ok(int sl, int b) { return sl >= 0 && b < 160; }
  __device__ __forceinline__ void load(const RecArgs& a, const __amdgpu_buffer_rsrc_t rs, int sl, int b, int part = 3) {
    const unsigned o = t5o | (sl < 0 ? 0x7ff00000u : 0u);
    sl = imax(sl, 0);
#pragma unroll
    for (int k = 0; k < 4; ++k) if (part & 1) y[k] = SC1_LOADS(o, op_slot(sl) + k * 5120);
    if (part & 2) xo = __builtin_bit_cast(u32x2, __hip_atomic_load((const gu64*)(a.xold + (size_t)sl * 32 * DM + t5f), RLX_AGENT));
  }
  __device__ __forceinline__ bool ready(int b) { return b >= 160 || wave_all(umin(umin(min4(y[0]), min4(y[1])), umin(min4(y[2]), min4(y[3]))) != 0u); }
  __device__ __forceinline__ void run(int lane) {
    f32x4 d = {0.f, 0.f, 0.f, 0.f};
#pragma unroll
    for (int k = 0; k < 4; ++k) d = mfma32(sel, as8(y[k]), d);
#pragma unroll
    for (int i = 0; i < 2; ++i) {
      const unsigned r = pack2(d[2 * i], d[2 * i + 1]), x = xo[i];
      out[i] = pack2(u2f(x << 16) + u2f(r << 16), u2f(x & 0xffff0000u) + u2f(r & 0xffff0000u));
    }
    PIN_V(out);
  }
  __device__ __forceinline__ void store(const RecArgs& a, int sl, int b, int lane) {
    __builtin_amdgcn_raw_buffer_store_b64(out, __builtin_amdgcn_make_buffer_rsrc(a.xnew, 0, ok(sl, b) ? S_ * DM * 2 : 0, 0x00020000), (t5f + (unsigned)sl * 32 * DM) * 2u | (unsigned)(lane & 32) << 25, 0, 16);
  }
};

struct PvTask2 {
  u32x4 y[2][2], pa[2], pb[2];
  unsigned lo, lm, vr, ma, mz, so, sz;
  __device__ __forceinline__ void init(int gp, int lane) {
    const int hi = lane >> 5;
    lo = pt_tok(2 * gp) + (unsigned)(lane * 16); lm = pt_mem(2 * gp + hi) + (unsigned)((lane & 31) * 8);
    vr = (unsigned)(L_VOX + 2048 + (lane ^ 8) * 16);
    ma = (unsigned)(L_VOM + hi * 768 + (lane & 31) * 8); mz = (unsigned)(L_VOM + 512 + ((lane & 31) ^ (hi * 8)) * 8);
    so = (unsigned)(L_STG + 2048 + (hi * 16 + (lane & 15)) * 16 + ((lane >> 4) & 1) * 8);
    sz = hi ? (unsigned)(L_RED1 + 8704 + lane * 8) : so + 1024;
    PIN_V(lo); PIN_V(lm); PIN_V(vr); PIN_V(ma); PIN_V(mz); PIN_V(so); PIN_V(sz);
#pragma unroll
    for (int q = 0; q < 2; ++q) { pa[q] = pb[q] = y[q][0] = y[q][1] = (u32x4){0u, 0u, 0u, 0u}; PIN_V(pa[q]); PIN_V(pb[q]); }
  }
  __device__ __forceinline__ void load(const __amdgpu_buffer_rsrc_t rs, unsigned o) {
#pragma unroll
    for (int q = 0; q < 2; ++q) {
      y[q][0] = SC1_LOADS(lo + q * 1024, o); y[q][1] = SC1_LOADS(lo + 2048 + q * 1024, o);
      const u32x2 p = __builtin_amdgcn_raw_buffer_load_b64(rs, lm + q * 256, o, 16);
      pa[q][0] = p[0]; pa[q][1] = p[1];
    }
  }
  __device__ __forceinline__ bool ready() {
    return wave_all(umin(umin(umin(min4(y[0][0]), min4(y[0][1])), umin(min4(y[1][0]), min4(y[1][1]))), umin(umin(pa[0][0], pa[0][1]), umin(pa[1][0], pa[1][1]))) != 0u);
  }
  template <bool DIRECT> __device__ __forceinline__ void run(char* lds, int sv, int lane, const __amdgpu_buffer_rsrc_t rs, unsigned og) {
    const f32x4 Z4 = {0.f, 0.f, 0.f, 0.f};
    const char* vp = lds + L_VOX + sv * 5120 + lane * 16;
    bf16x8 vx[6];
#pragma unroll
    for (int t = 0; t < 5; ++t) vx[t] = *(const bf16x8*)(vp + t * 1024);
    vx[5] = *(const bf16x8*)(lds + vr + sv * 5120);
    const u32x2 a0 = *(const u32x2*)(lds + ma + sv * 1280), a1 = *(const u32x2*)(lds + ma + sv * 1280 + 256);
    const u32x2 z0 = *(const u32x2*)(lds + mz + sv * 1280), z1 = *(const u32x2*)(lds + mz + sv * 1280 + 256);
    const bf16x8 vm = mk8(a0[0], a0[1], a1[0], a1[1]), vz = mk8(z0[0], z0[1], z1[0], z1[1]);
#pragma unroll
    for (int q = 0; q < 2; ++q) { PIN_V(pa[q]); pb[q][2] = pa[q][0]; pb[q][3] = pa[q][1]; PIN_V(pb[q]); }
    __builtin_amdgcn_sched_barrier(0);
    f32x4 o[2][3];
#pragma unroll
    for (int q = 0; q < 2; ++q) {
#pragma unroll
      for (int t = 0; t < 3; ++t) o[q][t] = mfma32(vx[t], as8(y[q][0]), Z4);
#pragma unroll
      for (int t = 0; t < 3; ++t) o[q][t] = mfma32(vx[3 + t], as8(y[q][1]), o[q][t]);
      o[q][0] = mfma32(vm, as8(pa[q]), o[q][0]); o[q][1] = mfma32(vm, as8(pb[q]), o[q][1]); o[q][2] = mfma32(vz, as8(pa[q]), o[q][2]);
    }
#pragma unroll
    for (int q = 0; q < 2; ++q) {
#pragma unroll
      for (int t = 0; t < 3; ++t) {
        const u32x2 v = {pack2n(o[q][t][0], o[q][t][1]), pack2n(o[q][t][2], o[q][t][3])};
        if (DIRECT) SC1_STORE8S(v, (so - (L_STG + 2048) + t * 512) | (t == 2 ? (unsigned)(lane & 32) << 25 : 0u), og + q * 20480);
        else *(u32x2*)(lds + (t < 2 ? so + t * 512 : sz) + q * 1280) = v;
      }
    }
  }
};

struct TokScores {
  bf16x8 k[2], q[2]; u32x4 v[2];
  __device__ __forceinline__ void get(const char* lds, int seg, int lane) {
#pragma unroll
    for (int i = 0; i < 2; ++i) { k[i] = *(const bf16x8*)(lds + L_KX + (seg & 1) * 2048 + i * 1024 + lane * 16); q[i] = *(const bf16x8*)(lds + L_QX + (seg & 1) * 2048 + i * 1024 + lane * 16); }
  }
  __device__ __forceinline__ void run(int qt) {
    const f32x4 Z4 = {0.f, 0.f, 0.f, 0.f};
    const f32x4 d1 = mfma32(k[0], q[qt], Z4), d2 = mfma32(k[1], q[qt], Z4);
    v[qt] = (u32x4){pack2n(d1[0], d1[1]), pack2n(d1[2], d1[3]), pack2n(d2[0], d2[1]), pack2n(d2[2], d2[3])};
    PIN_V(v[qt]);
  }
  __device__ __forceinline__ void store(const __amdgpu_buffer_rsrc_t rs, unsigned off, unsigned o) {
    __builtin_amdgcn_raw_buffer_store_b128(v[0], rs, off, o, 16); __builtin_amdgcn_raw_buffer_store_b128(v[1], rs, off + 1024, o, 16);
    asm volatile("s_nop 1" ::: "memory");
  }
};

__device__ __forceinline__ void touch(const char* base, unsigned off, unsigned lds0) {
  unsigned keep;
  asm volatile("s_mov_b32 %0, m0\n\ts_mov_b32 m0, %3\n\ts_nop 0\n\tglobal_load_lds_dword %1, %2 sc1\n\ts_mov_b32 m0, %0" : "=&s"(keep) : "v"(off), "s"(base), "s"(lds0 + L_RED1 + 8192) : "memory");
}
template <int N> __device__ __forceinline__ void stouch(const char* p, unsigned& d) {
  asm volatile(".set pf_o, 0\n\t.rept %2\n\ts_load_dword %0, %1, pf_o\n\t.set pf_o, pf_o + 128\n\t.endr" : "+s"(d) : "s"(p), "n"(N) : "memory");
}
__device__ __forceinline__ void code_touch(int b, int lane, unsigned lds0) {
  const char* pc; int d0, d1;
  asm volatile("s_getpc_b64 %0\n\ts_mov_b32 %1, _Z10rec_kernel7RecArgs@rel32@lo+4\n\ts_mov_b32 %2, _Z11attb_kernelPcPt@rel32@lo+12" : "=s"(pc), "=s"(d0), "=s"(d1));
  const int o = (lane * 32 + (b >> 3)) * 128;
  touch(pc + d0, (unsigned)(o < d1 - d0 ? o : 0), lds0);
}
__device__ __forceinline__ unsigned zero_base(int b, int h, int j, int ln) {
  return ln < 32 ? (unsigned)((h & 3) * 32768 + j * 512 + ln * 16) : ln < 40 ? (unsigned)((h & 2) * 1024 + j * 256 + (ln & 4) * 32 + (h & 1) * 64 + (ln & 3) * 16)
                 : (unsigned)OFF_ACC + (b < ACC_NB ? (unsigned)(b * 256 + (ln - 40) * 16) : ts_piece((b - ACC_NB) * 8 + ln - 40));
}
__device__ __forceinline__ unsigned sct_slot(unsigned ck, unsigned sl) { return sl * SCT_STEP + ((ck >> (sl & 4u)) & 7u) * 4096u; }
__device__ __forceinline__ unsigned scm_slot(unsigned ck, unsigned sl) { return OFF_SCM + sl * SCM_STEP + ((ck >> 8) & 7u) * 4096u; }
__device__ __forceinline__ int ck_rot(int x) { return ((x & 1) << 2) | (x >> 1); }
struct Place {
  unsigned t[8];
  __device__ __forceinline__ void load(const char* ws, int lane) {
#pragma unroll
    for (int x = 0; x < 8; ++x) t[x] = *(const unsigned*)(ws + OFF_LAT + ((imin(lane >> 3, 5) * 8 + (ck_rot(x) ^ (lane & 7))) * 8 + x) * 4);
  }
  __device__ __forceinline__ unsigned get(int b, int lane) {
    int c = (int)((((t[0] + t[1]) + (t[2] + t[3])) + ((t[4] + t[5]) + (t[6] + t[7]))) * 8u) + (lane & 7);
    c = imin(c, dppi<DPP_XOR1>(c)); c = imin(c, dppi<DPP_XOR2>(c)); c = imin(c, dppi<0x141>(c));
    unsigned k = 0u;
#pragma unroll
    for (int r = 0; r < 6; ++r) k |= (unsigned)(__builtin_amdgcn_readlane(c, 8 * r) & 7) << (4 * r);
    return (k ^ PL_X) ^ (unsigned)ck_rot(b & 7) * 0x111111u;
  }
};

struct Sched {
  u32x4 tsv, rec;
  unsigned side, o;
  __device__ __forceinline__ void pre(int s) { o = (unsigned)OFF_ACC + (unsigned)((s + SR - 1) % SR) * (unsigned)ACC_STEP + ACC_TS + side; PIN_S(o); }
  __device__ __forceinline__ void load(const __amdgpu_buffer_rsrc_t rs, int lane) {
    tsv = SC1_LOADS(ts_piece(lane & 63) - ACC_TS, o); rec = SC1_LOADS(128, o);
  }
  __device__ __forceinline__ unsigned last(unsigned ref) {
    const unsigned o = 0x40000000u - ref;
    int l = (int)umax(umax(tsv[0] ? tsv[0] + o : 0u, tsv[1] ? tsv[1] + o : 0u), umax(tsv[2] ? tsv[2] + o : 0u, tsv[3] ? tsv[3] + o : 0u));
    return (unsigned)wave_imax(l) - o;
  }
  __device__ __forceinline__ unsigned run(const RecArgs& a, const __amdgpu_buffer_rsrc_t rs, int s, int b, int lane) {
    constexpr int w = 3; (void)w;
    unsigned acur = __builtin_amdgcn_readfirstlane(rec[0]), aprev = __builtin_amdgcn_readfirstlane(rec[1]), hist = __builtin_amdgcn_readfirstlane(rec[3]);
    int D8 = (int)__builtin_amdgcn_readfirstlane(rec[2]);
    const int d = wave_imax((int)(umax(umax(tsv[0], tsv[1]), umax(tsv[2], tsv[3])) - aprev)), dd = 8 * d - D8;
    int Dn = D8 + (dd >> (dd > 0 ? SCH_GU : SCH_GD));
    unsigned anext = (acur + (unsigned)((Dn + 4 + imax(Dn - 8 * SCH_DR, 0) * SCH_DK / 8) >> 3) + SCH_V + (s < EARLY_N ? EARLY_V : (s >= WPF0 ? (s == NSEG - 2 ? LAST_V : LATE_V) : 0))) | 1u;
    if (__builtin_expect(hist < 2u || aprev == 0u || d < 1 || d + SCH_VMIN > (int)(acur - aprev), 0)) {
      if (s == 0) {
        for (int it = 0; it < 40 && !wave_all(min4(tsv) != 0u); ++it) {
          __builtin_amdgcn_s_sleep(ST_ALL);
          asm volatile("" ::: "memory");
          load(rs, lane);
        }
      }
      const unsigned now = now32(), L = last(now);
      if (acur == 0u) { D8 = 8 * SCH_D0; hist = 0u; }
      const bool late = acur == 0u || (int)(L + SCH_VMIN - acur) > 0;
      if (late) { if (b == 0) FC(3); if (acur) D8 += 8 * (hist ? SCH_UP : 3 * SCH_UP); }
      else if (aprev) { if (hist < 2u) D8 = 8 * imin(imax((int)(L - aprev), 1), 4000); hist = umin(hist + 1u, 2u); }
      const unsigned base = acur == 0u ? L + SCH_FIRST : (late ? L + SCH_V + ((int)(L + SCH_VMIN2 - acur) <= 0 ? SCH_PEN2 : SCH_PEN) : acur);
      anext = (base + (unsigned)((D8 + 4) >> 3) + SCH_V + (s == 1 ? EARLY_V1 : (s < EARLY_N ? EARLY_V : 0))) | 1u;
      if (s == 0) anext = (L + ST_X) | 1u;
      if ((int)(now - L) > 50000) anext = 0u;
      if (late) acur = 0u;
      Dn = D8;
    }
    if (__builtin_expect(b == 0, 0))
      __builtin_amdgcn_raw_buffer_store_b128((u32x4){anext, acur, (unsigned)Dn, hist}, rs, lane < 2 ? lane * 8192 : 0x7ffffff0, (unsigned)OFF_ACC + (unsigned)(s % SR) * (unsigned)ACC_STEP + ACC_TS + 128u, 16);
    if (XO_T && anext) anext += (b & 1) * XO_T;
    LOGV(0, anext); LOGV(1, acur == 0u); LOGV(2, (Dn + 4) >> 3); LOGV(3, d); LOGV(4, acur - aprev - d); LOGV(5, aprev);
    return anext;
  }
};

struct TokTask {
  u32x4 y[2]; u32x2 ym; u32x4 res;
  unsigned l0, l1, lm, s0, s1;
  static __device__ __forceinline__ bool ok(int sl) { return sl >= 0 && sl < NSEG; }
  __device__ __forceinline__ void init(int h, int j, int lane) {
    const int c = lane & 15, g = lane >> 4;
    l0 = ts_tok(h, g) + (unsigned)(j * 256 + c * 16); l1 = l0 + 16384u;
    lm = ts_mem(h, g + 4 * (c >> 3)) + (unsigned)(j * 64 + (c & 7) * 8);
    res = (u32x4){0u, 0u, 0u, 0u};
    const int qi = 4 * j + (c & 3);
    s0 = g < 2 ? pt_tok(h) + (unsigned)((qi >> 4) * 1024 + ((c >> 2) * 16 + (qi & 15)) * 16 + g * 8) : 0x7ffffff0u;
    s1 = lane < 8 ? pt_mem(h) + (unsigned)((qi >> 4) * 256 + ((c >> 2) * 16 + (qi & 15)) * 8) : 0x7ffffff0u;
    PIN_V(l0); PIN_V(l1); PIN_V(lm); PIN_V(s0); PIN_V(s1);
  }
  __device__ __forceinline__ void load(const __amdgpu_buffer_rsrc_t rs, unsigned o) {
    y[0] = SC1_LOADS(l0, o); y[1] = SC1_LOADS(l1, o);
    ym = __builtin_amdgcn_raw_buffer_load_b64(rs, lm, o, 16);
  }
  __device__ __forceinline__ bool ready() { return wave_all(umin(umin(min4(y[0]), min4(y[1])), umin(ym[0], ym[1])) != 0u); }
  __device__ __forceinline__ void run(int j, int lane, const bf16x8 SelLo) {
    const int c = lane & 15, g = lane >> 4;
    const f32x4 Z4 = {0.f, 0.f, 0.f, 0.f};
    const f32x4 d1 = mfma32(SelLo, as8(y[1]), mfma32(SelLo, as8(y[0]), Z4));
    const f32x4 d0 = mfma32(SelLo, mk8(ym[0], ym[1], 0u, 0u), Z4);
    const int qi = 4 * j + (c & 3), k0 = 16 * g + 4 * (c >> 2);
    float t[4], mv[4];
    float mx = -INFINITY;
#pragma unroll
    for (int r = 0; r < 4; ++r) {
      t[r] = (g < 2 && k0 + r <= qi) ? d1[r] : -INFINITY;
      const float mt = d0[r] + dppf<DPP_ROR8>(d0[r]);
      mv[r] = lane < 8 ? mt : -INFINITY;
      mx = fmx(mx, fmx(t[r], mv[r]));
    }
    mx = fmx(mx, dppf<DPP_ROR8>(mx)); mx = fmx(mx, dppf<DPP_ROR4>(mx));
    mx = x16mx(mx);
    float sum = 0.f;
#pragma unroll
    for (int r = 0; r < 4; ++r) {
      t[r] = __builtin_amdgcn_exp2f(t[r] - mx); mv[r] = __builtin_amdgcn_exp2f(mv[r] - mx);
      sum += t[r] + mv[r];
    }
    sum += dppf<DPP_ROR8>(sum); sum += dppf<DPP_ROR4>(sum);
    sum = x16sum(sum);
    const float rs_ = __builtin_amdgcn_rcpf(sum);
    res = (u32x4){pack2n(t[0] * rs_, t[1] * rs_), pack2n(t[2] * rs_, t[3] * rs_), pack2n(mv[0] * rs_, mv[1] * rs_), pack2n(mv[2] * rs_, mv[3] * rs_)};
    PIN_V(res);
  }
  __device__ __forceinline__ void store(const __amdgpu_buffer_rsrc_t rs, unsigned o, unsigned no = 0u) {
    SC1_STORE8S(((u32x2){res[0], res[1]}), s0 | no, o);
    SC1_STORE8S(((u32x2){res[2], res[3]}), s1 | no, o);
  }
};

constexpr int L_DRN = L_VOX + (NSEG % NV) * 5120;
__device__ __forceinline__ void drain_pre(char* lds, int b, int lane) {
  const int gp = (b & 7) * 2 + (b >> 7), m = (b >> 3) & 15;
  TokTask tk; PvTask2 pv; OutTask ot;
  tk.init(2 * gp + (m >> 3), m & 7, lane); pv.init(gp, lane); ot.init(b, lane);
  u32x4* p = (u32x4*)(lds + L_DRN) + lane;
  p[0] = (u32x4){tk.l0, tk.lm, tk.s0, tk.s1}; p[64] = (u32x4){pv.lo, pv.lm, pv.vr, pv.ma}; p[128] = (u32x4){pv.mz, pv.so, ot.t5f, ot.t5o};
  p[192] = __builtin_bit_cast(u32x4, ot.sel);
}
__device__ __forceinline__ void qkv_pre(unsigned (&vo)[2][2], int b, int w, int lane) {
  const int cb = qkv_col(b), c = lane & 15, g = lane >> 4, t0 = w & 1, col0 = (cb % 5) * 128 + w * 32;
  const bool SW = cb < 10;
#pragma unroll
  for (int i = 0; i < 2; ++i)
#pragma unroll
    for (int tt = 0; tt < 2; ++tt) {
      const int tq = tt ^ t0, tk = tq * 16 + c, cc = col0 + i * 16 + (SW ? 4 * g : c), hd = cc / BD, e = cc - hd * BD;
      const int kv = (int)(OFF_KV - OFF_QB) + hd * 64 * KVT;
      vo[i][tt] = (unsigned)(!SW ? kv + 1280 + tq * 672 + ((g >> 1) * 21 + e) * 16 + (g & 1) * 8
                                 : (cb < 5 ? (hd * S_ + tk) * 2 * QST + e * 2 : kv + (e < 16 ? (tk + 32 * (e >> 3)) * 16 + (e & 7) * 2 : 1024 + tk * 8)));
      PIN_V(vo[i][tt]);
    }
}

template <int w>
__device__ __forceinline__ void token_waves(const RecArgs& a, char* lds, const __amdgpu_buffer_rsrc_t rs, unsigned (&vo)[2][2]) {
  const int lane0 = __builtin_amdgcn_workitem_id_x() & 63, b = __builtin_amdgcn_workgroup_id_x(), wid = w + 4; (void)wid;
  const int gp = (b & 7) * 2 + (b >> 7), m = (b >> 3) & 15, h0 = 2 * gp, h = h0 + (m >> 3), j = m & 7;
  float* const misc = (float*)(lds + L_MISC);
  const f32x4 Z4 = {0.f, 0.f, 0.f, 0.f};
  const unsigned lds0 = (unsigned)(uintptr_t)(LAS char*)lds;
  unsigned* const cnt = (unsigned*)(misc + 6);
  const float* const xnrm = (const float*)(a.ws + OFF_XNRM);
  bf16x16 W[2][10];
  auto btile = [&](int i) { return w == 1 ? 0 : (w == 2 ? 1 + i : 3 + i); };
  if (w == 1) code_touch(b, lane0, lds0);
  Place pl;
  unsigned phi[2][2], ph[2][2][2];
#pragma unroll
  for (int k = 0; k < 8; ++k) {
    const int bt = btile(k >> 2), hw = h0 + (bt == 2 ? k & 1 : bt / 3), T = bt == 2 ? 32 + (m >> 1) : (bt % 3) * 16 + m;
    ph[k >> 2][(k >> 1) & 1][k & 1] = ((const __attribute__((address_space(4))) unsigned*)(a.ws + OFF_PHI))[(hw >> 2) | (hw & 3) << 3 | (k & 2) << 4 | (T / 10) << 6];
  }
  __builtin_amdgcn_sched_barrier(0);
  if (w == 3) pl.load(a.ws, lane0);
  if (a.layer) {
    if (w != 0 && FR_HOLD) wait_until(now32() + FR_HOLD);
    if (w != 0) img_fetch<20>(a, lds0, h, j, w - 1, lane0);
  } else {
    if (w == 0) img_fetch<12>(a, lds0, h, j, 4, lane0);
#pragma unroll
    for (int i = 0; i < 10; ++i) x_fetch(x_src(a, 0, b), lds0, 0, 10 * w + i, lane0);
  }
  const __amdgpu_buffer_rsrc_t rwb = __builtin_amdgcn_make_buffer_rsrc(a.ws + OFF_WB, 0, (int)WB_BYTES, 0x00020000);
#pragma unroll
  for (int k = 0; k < 4; ++k) phi[k >> 1][k & 1] = (((lane0 & 8) ? ph[k >> 1][k & 1][1] : ph[k >> 1][k & 1][0]) & 8192u) | (unsigned)(lane0 * 16);
#pragma unroll
  for (int i = 0; i < 2; ++i)
#pragma unroll
    for (int p = 0; p < 10; ++p)
      if (w >= 2 || (w == 1 && i == 1)) {
        const unsigned wo = wb_off((b * 5 + btile(i)) * 20 + 2 * p), wv = phi[i][p / 5] ^ (wo & 8192u);
        if (FR_PACE && a.layer) asm volatile("s_waitcnt vmcnt(%0)" :: "n"(FR_PACE) : "memory");
        W[i][p] = cat16(as8(__builtin_amdgcn_raw_buffer_load_b128(rwb, wv, wo & ~8192u, 0)), as8(__builtin_amdgcn_raw_buffer_load_b128(rwb, wv + 1024, wo & ~8192u, 0)));
      }
  f32x4 bn = Z4;
  if (w == 0) {
    bn = *(const f32x4*)(a.ws + OFF_BNRM + lane0 * 16);
    if (!a.layer) asm volatile("s_waitcnt vmcnt(0)" ::: "memory");
  } else asm volatile("s_waitcnt vmcnt(%0)" :: "n"(w == 1 ? 20 : 40) : "memory");
  unsigned ck = 0u;
  if (w == 3) { ck = pl.get(b, lane0); *(unsigned*)(misc + 11) = ck; }
  BAR();
#pragma unroll
  for (int p = 0; p < 10; ++p) {
    if (w < 2) W[0][p] = cat16(wqk_frag(lds, 2 * w, 2 * p, lane0), wqk_frag(lds, 2 * w, 2 * p + 1, lane0));
    if (w == 0) W[1][p] = cat16(wqk_frag(lds, 1, 2 * p, lane0), wqk_frag(lds, 1, 2 * p + 1, lane0));
  }
  float cb2 = 0.f;
  if (w == 0) {
    float n = __builtin_amdgcn_sqrtf(bn[0] + bn[1]) + __builtin_amdgcn_sqrtf(bn[2] + bn[3]);
    n += dppf<DPP_XOR1>(n); n += dppf<DPP_XOR2>(n); n += dppf<DPP_ROR4>(n); n += dppf<DPP_ROR8>(n);
    n = x32mx(x16mx(n));
    cb2 = u2f(__builtin_amdgcn_readfirstlane(f2u(1.15f * n * n)));
  }
  const float cm2 = u2f((f2u(cb2) & 0x7fffffu) | 0x40000000u);
  const int ek = imax((int)((f2u(cb2) >> 23) & 0xffu), 1) - 252;
  if (w >= 2) {
#pragma unroll
    for (int i = 0; i < 4; ++i) *(unsigned*)(lds + L_KX + (w - 2) * 4096 + i * 1024 + lane0 * 16 + 12) = 0u;
  }
  OutTask ot;
  const int zl = lane0 - 16;
  const bool zon = w == 3 && zl >= 0 && (zl < 40 || b < ACC_NB + 17);
  unsigned zo = zon ? zero_base(b, h, j, zl) + (zl < 32 ? sct_slot(ck, 0u) : (zl < 40 ? scm_slot(ck, 0u) : 0u)) : 0x7ffffff0u;
  unsigned zm = zon ? (zl < 32 ? SCT_STEP + 1u : (zl < 40 ? SCM_STEP : (unsigned)ACC_STEP)) : 0u;
  const unsigned zx = ((ck ^ (ck >> 4)) & 7u) * 4096u;
  if (w == 3) { PIN_V(zo); PIN_V(zm); }
  const __amdgpu_buffer_rsrc_t rx = __builtin_amdgcn_make_buffer_rsrc(a.ws + OFF_XNRM, 0, (int)(OFF_QB - OFF_XNRM), 0x00020000);
  u32x4 v0, v1;
  f32x4 d5;
  unsigned oo, za, xa, ya, n4, so_op, so_y, so_x;
  float xn4 = 0.f;
  auto w1_part = [&](int k, int s, int lane) {
    if (w < 2) return;
    if (w == 2) {
      if (k >= 1 && k <= 4) d5 = mfma32(ot.sel, as8(ot.y[k < 1 || k > 4 ? 0 : k - 1]), k == 1 ? Z4 : d5);
      if (k == 6 || k == 7) {
        const unsigned r = pack2(d5[2 * (k & 1)], d5[2 * (k & 1) + 1]), x = ot.xo[k & 1];
        ot.out[k & 1] = pack2(u2f(x << 16) + u2f(r << 16), u2f(x & 0xffff0000u) + u2f(r & 0xffff0000u));
        PIN_V(ot.out);
      }
      if (k == 8) xn4 = fmx(xn4, dppf<DPP_XOR1>(xn4));
      if (k == 9) xn4 = fmx(xn4, dppf<DPP_XOR2>(xn4));
      if (k == 10) xn4 = fmx(xn4, dppf<DPP_ROR4>(xn4));
      if (k == 11) xn4 = fmx(xn4, dppf<DPP_ROR8>(xn4));
      if (k >= 8 && k <= 11) PIN_V(xn4);
      if (k == 12) misc[s & 3] = x16mx(xn4);
      if (k == 5) { so_y = op_slot(imax(s - 1 - LAG5, 0)); so_x = (unsigned)imax(s - 1 - LAG5, 0) * 40960u; PIN_S(so_y); PIN_S(so_x); }
      if (k == 14) {
        ya = ot.t5o | (s <= LAG5 ? 0x7ff00000u : 0u);
        xa = (ot.t5f * 2u | (unsigned)(lane & 32) << 25) | (OutTask::ok(s - 2 - LAG5, b) ? 0u : 0x7ff00000u);
        PIN_V(ya); PIN_V(xa);
      }
    } else if (k == 8) {
      const unsigned zs = (unsigned)(s + 2) % SR;
      za = (zo + (s < 0 ? 1024u : zs) * (zm & ~1u)) ^ ((zm & 1u) * (zs & 4u ? zx : 0u));
      PIN_V(za);
    }
    if (k == 13) {
      n4 = __hip_atomic_load((unsigned*)(misc + 28), __ATOMIC_RELAXED, __HIP_MEMORY_SCOPE_WORKGROUP);
      so_op = s < 2 + LAG4 ? 0x7ff00000u : op_slot(s - 2 - LAG4) + (unsigned)((m * 32 + (w & 1) * 16 + gp) * 1280);
      PIN_S(so_op);
    }
    if (k == 15 && __builtin_expect((int)__builtin_amdgcn_readfirstlane(n4) < s, 0)) lds_spin<L_MISC + 112, 1>(lds0, (unsigned)s);
    if (k == 16) {
      asm volatile("" ::: "memory");
      const char* sp = lds + L_STG + 2048 + (w & 1) * 1280;
      v0 = *(const u32x4*)(sp + lane * 16); v1 = *(const u32x4*)(sp + 1024 + (lane & 15) * 16);
      oo = so_op + (unsigned)(lane * 16);
      za = lane < 16 ? oo + 1024 : (w == 3 ? za : 0x7ffffff0u);
      PIN_V(oo); PIN_V(za);
    }
    if (k == 19 && w == 3) {
#pragma unroll
      for (int i = 0; i < 4; ++i) { v1[i] = lane < 16 ? v1[i] : 0u; PIN_V(v1[i]); }
    }
    __builtin_amdgcn_sched_barrier(0);
  };
  auto w1_fire = [&](int s, int lane, auto last) {
    if (w < 2) return;
    lds_spin<L_MISC + 92, W1_SL>(lds0, 4u * (unsigned)imax(s, 0));
    SC1_STORE(v0, oo);
    SC1_STORE(v1, za);
    __builtin_amdgcn_sched_barrier(0);
    if (w == 2) {
      __builtin_amdgcn_raw_buffer_store_b64(ot.out, rx, xa, so_x + (unsigned)(OFF_XA - OFF_XNRM - 40960), 16);
      if (last == 0) {
#pragma unroll
        for (int k = 0; k < 4; ++k) ot.y[k] = SC1_LOADS(ya, so_y + k * 5120);
        ot.xo = __builtin_amdgcn_raw_buffer_load_b64(rx, ot.t5f * 2u, so_x + (unsigned)(OFF_XB - OFF_XNRM), 16);
        xn4 = u2f(__builtin_amdgcn_raw_buffer_load_b32(rx, (lane & 31) * 16, (s + 1) * 512, 16));
      }
    } else {
      const unsigned xo_ = (unsigned)((s - 8) * 1024 + lane * 16);
      if (__builtin_expect((unsigned)(s - 8) < 20u, 0)) SC1_STORE(((u32x4){0u, 0u, 0u, 0u}), xo_ < X_BYTES / 256 ? (unsigned)(OFF_X + b * (X_BYTES / 256)) + xo_ : 0x7ffffff0u);
    }
    __builtin_amdgcn_sched_barrier(0);
  };
  STAMP_DECL(); ARRIVE_DECL();
  auto mma_seg = [&](int seg, int sv, int lane, auto pause) {
    const int c = lane & 15, g = lane >> 4;
    const char* xp = lds + L_XN + (seg & 1) * 40960 + lane * 16;
    f32x4 acc[2][2] = {{Z4, Z4}, {Z4, Z4}};
    bf16x8 x[4][2];
#pragma unroll
    for (int p = 0; p < 3; ++p) { x[p][0] = *(const bf16x8*)(xp + p * 1024); x[p][1] = *(const bf16x8*)(xp + (20 + p) * 1024); }
    unsigned eo[2];
#pragma unroll
    for (int ks = 0; ks < 20; ++ks) {
      if (ks == (w == 0 ? KP0 : (w == 1 ? KP1 : KP2))) { pause(); __builtin_amdgcn_sched_barrier(0); }
      if (ks == 17) {
#pragma unroll
        for (int i = 0; i < 2; ++i) {
          eo[i] = (unsigned)(w == 0 ? (i ? L_KX : L_QX) + (seg % NKQ) * 2048 + lane * 16
                                    : (w == 1 && i == 0 ? ((g >> 1) ? L_KX : L_QX) + (seg % NKQ) * 2048 + (2 * (g & 1) * 16 + c) * 16 + 8 : L_VOX + sv * 5120 + btile(i) * 1024 + lane * 16));
          if (i == 0 || w == 1) PIN_V(eo[i]);
        }
        if (w == 0) eo[1] = eo[0] + (L_KX - L_QX);
        if (w >= 2) eo[1] = eo[0] + 1024;
      }
      __builtin_amdgcn_sched_barrier(0);
#pragma unroll
      for (int q = 0; q < 4; ++q) {
        const int tt = 1 - (q >> 1), i = q & 1;
        const bool isqk = w == 0 || (w == 1 && i == 0);
        const bf16x8 wf = half8(W[i][ks >> 1], ks & 1);
        acc[i][tt] = isqk ? mfma32(wf, x[ks & 3][tt], acc[i][tt]) : mfma32(x[ks & 3][tt], wf, acc[i][tt]);
        __builtin_amdgcn_sched_barrier(0);
        if (q < 2 && ks + 3 < 20) x[(ks + 3) & 3][q] = *(const bf16x8*)(xp + (q * 20 + ks + 3) * 1024);
        if (q == 2) w1_part(ks, seg - 1, lane);
        __builtin_amdgcn_sched_barrier(0);
      }
    }
#pragma unroll
    for (int i = 0; i < 2; ++i) {
      if (w + i == 1) { const float ks_ = (w == 0 || (g >> 1)) ? KEXP : 1.0f; acc[i][0] *= ks_; acc[i][1] *= ks_; }
      const u32x4 v = {pack2(acc[i][0][0], acc[i][0][1]), pack2(acc[i][0][2], acc[i][0][3]), pack2(acc[i][1][0], acc[i][1][1]), pack2(acc[i][1][2], acc[i][1][3])};
      char* p = lds + eo[i];
      if (w == 0) { *(u32x2*)(p) = (u32x2){v[0], v[1]}; *(u32x2*)(p + 1024) = (u32x2){v[2], v[3]}; }
      else if (w == 1 && i == 0) { *(unsigned*)(p) = v[0]; *(unsigned*)(p + 256) = v[1]; *(unsigned*)(p + 1024) = v[2]; *(unsigned*)(p + 1280) = v[3]; }
      else *(u32x4*)p = v;
    }
  };
  unsigned vp, mp, ip;
  int sidx, ei = 127;
  float inv;
  f32x4 acc[2];
  auto vo_pre = [&](int s, int sv, int lane) {
    vp = (unsigned)(w == 0 ? L_MISC + (s & 3) * 4 : L_VOM + sv * 1280 + (btile(0) * 32 + (w == 1 ? lane & 31 : lane)) * 8);
    mp = (unsigned)(L_BM + lane * 16);
    PIN_V(vp); PIN_V(mp);
    if (w == 0) { ip = (unsigned)(L_MISC + 16 + ((s + 1) & 1) * 4); PIN_V(ip); }
    else {
      inv = misc[4 + (s & 1)];
      sidx = (lane & 8) ? 0xEEEE : 0x4444;
      acc[0] = acc[1] = Z4;
      PIN_V(sidx); PIN_V(acc[1]);
      if (w >= 2) { PIN_V(acc[0]); PIN_V(inv); }
    }
    __builtin_amdgcn_sched_barrier(0);
  };
  auto vo_mem = [&](int s, auto last) {
    __builtin_amdgcn_s_setprio(2);
    asm volatile(".rept %0\n\ts_nop 3\n\t.endr" :: "n"(w == 0 ? VO_A0 : (w == 1 ? VO_A1 : VO_A2)) : "memory");
    float xmax = 0.f;
    if (w == 0) xmax = *(const float*)(lds + vp);
    constexpr int NX = w == 2 ? VO_N2 : (w == 3 ? VO_N3 : 10);
    bf16x8 x[10];
#pragma unroll
    for (int p = 0; p < NX; ++p) x[p] = *(const bf16x8*)(lds + mp + p * 1024);
    __builtin_amdgcn_sched_barrier(0);
    if (w < 2) asm volatile(".rept %0\n\ts_nop 3\n\t.endr" :: "n"(w == 0 ? VO_B0 : VO_B1) : "memory");
    if (VO_W >= 0) asm volatile("s_waitcnt lgkmcnt(%0)" :: "n"(VO_W < 0 ? 0 : VO_W) : "memory");
    STAMP(5);
    __builtin_amdgcn_sched_barrier(0);
    int ex = 0;
#pragma unroll
    for (int p = 0; p < 10; ++p) {
      if (w == 0) acc[0] = mfma32(x[p], x[p], p ? acc[0] : Z4);
      if (w == 0 && p == 9) { ex = imax((int)((__builtin_amdgcn_readfirstlane(f2u(__builtin_amdgcn_sqrtf(cb2 * xmax))) >> 23) & 0xffu) + 1, 32); PIN_S(ex); }
#pragma unroll
      for (int i = (w == 1); i < 2 && w != 0; ++i) acc[i] = smfma64(x[p], W[i][p], acc[i], sidx);
      __builtin_amdgcn_sched_barrier(0);
      if (p + NX < 10) x[p + NX] = *(const bf16x8*)(lds + mp + (p + NX) * 1024);
      __builtin_amdgcn_sched_barrier(0);
    }
    STAMP(7);
    if (w != 0) {
      float v[4];
#pragma unroll
      for (int r = 0; r < 4; ++r) {
        const auto sw = __builtin_amdgcn_permlane32_swap(f2u(acc[w == 1][r]), f2u(acc[1][r]), false, false);
        v[r] = (u2f(sw[0]) + u2f(sw[1])) * inv;
      }
      *(u32x2*)(lds + vp) = (u32x2){pack2(v[0], v[1]), pack2(v[2], v[3])};
    } else {
      const int t = (int)f2u(fmx(fmx(acc[0][0], acc[0][1]), fmx(acc[0][2], acc[0][3])) * cm2);
      const int et = wave_imax(t) >> 23, em = ((et + ek) >> 1) + ei;
      int e = imax(et == 255 ? 256 : em, ex);
      PIN_S(e);
      e = imin(e, 256);
      ei = e - 31;
      *(unsigned*)(lds + ip) = (unsigned)ei << 23;
      const u32x3 sr = {(unsigned)(285 - e) << 23, (unsigned)(e - 87) < 79u ? REC_RNG << 23 : 0u, (unsigned)(s + 1)};
      asm volatile("ds_write_b96 %0, %1 offset:%2" :: "v"(lds0), "v"(sr), "n"(L_MISC + 32) : "memory");
    }
    if (last == 0 && w >= 2) asm volatile("s_waitcnt vmcnt(%0)" :: "n"(w == SCH_W ? 2 : 0) : "memory");
    if (w != 0) lds_inc<L_MISC + 28>(lds0);
    STAMP(8);
    if (last) __builtin_amdgcn_s_setprio(0);
    if (last && w == 2) { VM_DRAIN(); lds_inc<L_MISC + 28>(lds0); }
  };
  unsigned* const cntq = (unsigned*)(misc + 12);
  unsigned* const cntt = (unsigned*)(misc + 26 + (w & 1));
  unsigned* const cntp = (unsigned*)(misc + 23);
  unsigned* const cntx2 = (unsigned*)(misc + 28);
  auto wpf = [&](int s, int lane) {
    constexpr int PER = 81920 / QR / WPF_N;
    static_assert(PER % WPF_S == 0 && PER / WPF_S <= 64, "prefetch");
    touch(a.ws + OFF_WQKVBT + (size_t)imin(qkv_col(b), 14) * 163840 + (qkv_row(b) % (2 * QR)) * (81920 / QR), (s - WPF0) * PER + imin(lane, PER / WPF_S - 1) * WPF_S, lds0);
  };
  Sched sch;
  auto late = [&](int s, int lane) {
    if (__builtin_expect(s < 0, 0)) return;
    if (w == SCH_W && SCH_LATE && __builtin_expect(s >= 0, 1)) *(u32x2*)(misc + 24) = (u32x2){sch.run(a, rs, s, b, lane), (unsigned)(s + 1)};
    if ((w == 2 && F6) || (w == 0 && F4) || (w == 1 && F5) || (w == 3 && F7)) { lds_spin<L_MISC + 48, 1>(lds0, 2u * (unsigned)imax(s + 1, 0)); if (FB_SL && w != 2) __builtin_amdgcn_s_sleep(FB_SL); }
    if (w >= 2) { x_fetch_n<w == 2 ? F6 : F7>(x_src(a, s + 3, b), lds0, s + 3, F4 + F5 + 2 * F67 + (w == 3 ? F6 : 0), lane * 16); return; }
    unsigned sd = 0u;
    if ((w == 0 && XPF == 3) || XPF == 4 || (w == 1 && XPF == 5)) stouch<10>(a.ws + OFF_XN + (size_t)imin(s + 4, NSEG - 1) * (s < 1 ? 0 : 40960) + (b >> 3) * 1280 + w * 64, sd);
    x_fetch_n<w == 0 ? F4 : F5>(x_src(a, s + 3, b), lds0, s + 3, w == 0 ? 0 : F4, lane * 16);
    if (WPF_W == 0 && w == 0 && __builtin_expect(s >= WPF0 && s < WPF0 + WPF_N, 0)) wpf(s, lane);
    if (w == 1 && __builtin_expect(s == CPF_S, 0)) code_touch(b, lane, lds0);
    lds_spin<L_MISC + 104 + 4 * (w & 1), PS_SL>(lds0, (unsigned)imax(s + 1, 0));
    asm volatile("" :: "s"(sd));
  };
  if (w == 2) { ot.init(b, lane0); ot.out = (u32x2){0u, 0u}; ot.xo = ot.out; ot.y[0] = ot.y[1] = ot.y[2] = ot.y[3] = (u32x4){0u, 0u, 0u, 0u}; }
  VM_DRAIN();
  lds_arrive(cnt, lane0);
  lds_wait(cnt, 8u);
  sch.tsv = sch.rec = (u32x4){0u, 0u, 0u, 0u};
  sch.side = __builtin_amdgcn_readfirstlane(*(const unsigned*)(misc + 11)) & 8192u;
  sch.o = 0u;
  int sv = NV - 1;
  CODE_PAD((PADT >> (4 * w)) & 15);
#pragma unroll 1
  for (int s = -1; s < NSEG - 1; ++s) {
    const int sv1 = sv == NV - 1 ? 0 : sv + 1;
    FRESH_LANE();
    STAMP(0);
    mma_seg(s + 1, sv1, lane, [&]() { STAMP(1); late(s - 1, lane); if (w == SCH_W) sch.pre(s); STAMP(9); });
    STAMP(2);
    __builtin_amdgcn_sched_barrier(0);
    VM_DRAIN();
    STAMP(10);
    w1_fire(s, lane, std::integral_constant<int, 0>{});
    if (w >= 2) vo_pre(s, sv, lane);
    STAMP(3);
    BAR();
    STAMP(4);
    if (__builtin_expect(s >= 0, 1)) {
      if (w == SCH_W) { sch.load(rs, lane0); __builtin_amdgcn_sched_barrier(0); }
      if (w < 2) vo_pre(s, sv, lane);
      vo_mem(s, std::integral_constant<int, 0>{});
      STAMP(6);
      if (w == SCH_W && !SCH_LATE) {
        __builtin_amdgcn_s_setprio(SCH_P);
        *(u32x2*)(misc + 24) = (u32x2){sch.run(a, rs, s, b, lane0), (unsigned)(s + 1)};
      }
      STAMP(11);
    }
    __builtin_amdgcn_s_setprio(MMA_P);
    if (w == 2) VM_DRAIN();
    __builtin_amdgcn_sched_barrier(0);
    sv = sv1;
  }
  __builtin_amdgcn_s_setprio(0);
  STAMP_OUT();
  late(NSEG - 2, lane0);
  { FRESH_LANE(); if (w == 0) drain_pre(lds, b, lane); qkv_pre(vo, b, w, lane); }
#pragma unroll
  for (int k = 0; k < 20; ++k) w1_part(k, NSEG - 1, lane0);
  w1_fire(NSEG - 1, lane0, std::integral_constant<int, 1>{});
  if (w != 0) vo_pre(NSEG - 1, (NSEG - 1) % NV, lane0);
  BAR();
  if (w != 0) vo_mem(NSEG - 1, std::integral_constant<int, 1>{});
}

__device__ __forceinline__ void qkv_tail(const RecArgs& a, char* lds, const int w, const unsigned (&vo)[2][2]) {
  const int lane0 = __builtin_amdgcn_workitem_id_x() & 63, b = __builtin_amdgcn_workgroup_id_x();
  const int r = qkv_row(b), cb = qkv_col(b);
  if (cb >= 15) return;
  const unsigned lds0 = (unsigned)(uintptr_t)(LAS char*)lds;
  unsigned* const stage = (unsigned*)(lds + L_MISC) + 17;
  unsigned* const cnty = stage + 1;
  const f32x4 Z4 = {0.f, 0.f, 0.f, 0.f};
  auto fetch = [&](int i) {
    x_fetch_n<10>(x_src(a, NSEG + i, b), lds0, i, 10 * w, lane0 * 16);
  };
  unsigned want = 0;
  auto sync = [&]() { VM_DRAIN(); want += 4u; lds_inc<L_MISC + 72>(lds0); while (__hip_atomic_load(cnty, __ATOMIC_RELAXED, __HIP_MEMORY_SCOPE_WORKGROUP) < want) { } asm volatile("" ::: "memory"); };
  LOGQ(0);
  bf16x8 W[2][20];
  const __amdgpu_buffer_rsrc_t rw = __builtin_amdgcn_make_buffer_rsrc(a.ws + OFF_WQKVBT + (size_t)(cb * 8 + 2 * w) * 20480, 0, 40960, 0x00020000);
  auto w_get = [&](int ks) {
#pragma unroll
    for (int i = 0; i < 2; ++i) W[i][ks] = as8(__builtin_amdgcn_raw_buffer_load_b128(rw, lane0 * 16 + (ks & 3) * 1024, (i * 20 + (ks & ~3)) * 1024, 0));
    __builtin_amdgcn_sched_barrier(0);
  };
#pragma unroll
  for (int ks = 0; ks < QW; ++ks) w_get(ks);
  bf16x8 ones = mk8(0x3f803f80u, 0x3f803f80u, 0x3f803f80u, 0x3f803f80u);
  PIN_V(ones);
  const __amdgpu_buffer_rsrc_t ro = __builtin_amdgcn_make_buffer_rsrc(a.ws + OFF_QB, 0, (int)(OFF_OB - OFF_QB), 0x00020000);
  const int t0 = w & 1;
  auto body = [&](auto SW, auto SQ) {
    const float qs = cb < 5 ? 0.22360679774997896f * 1.4426950408889634f : 1.0f;
    f32x4 cs[2] = {Z4, Z4};
    LOGQ(1);
    f32x4 acc[2][2], st;
    auto mma = [&](int it, auto FIRST) {
      const char* xp0 = lds + L_XN + (it & 1) * 40960 + t0 * 20480 + lane0 * 16;
      const char* xp1 = lds + L_XN + (it & 1) * 40960 + (1 - t0) * 20480 + lane0 * 16;
      acc[0][0] = acc[0][1] = acc[1][0] = acc[1][1] = st = Z4;
      bf16x8 x[4][2];
#pragma unroll
      for (int p = 0; p < 3; ++p) { x[p][0] = *(const bf16x8*)(xp0 + p * 1024); x[p][1] = *(const bf16x8*)(xp1 + p * 1024); }
      __builtin_amdgcn_sched_barrier(0);
#pragma unroll
      for (int ks = 0; ks < 20; ++ks) {
        if (FIRST && ks + QW < 20) w_get(ks + QW);
#pragma unroll
        for (int q = 0; q < 4; ++q) {
          const int tt = 1 - (q >> 1), i = 1 - (q & 1);
          acc[i][tt] = SW ? mfma32(W[i][ks], x[ks & 3][tt], acc[i][tt]) : mfma32(x[ks & 3][tt], W[i][ks], acc[i][tt]);
          __builtin_amdgcn_sched_barrier(0);
          if (q < 2 && ks + 3 < 20) x[(ks + 3) & 3][q] = *(const bf16x8*)((q ? xp1 : xp0) + (ks + 3) * 1024);
          __builtin_amdgcn_sched_barrier(0);
        }
        st = mfma32(SQ ? x[ks & 3][0] : ones, x[ks & 3][0], st);
        if (FIRST) {
#pragma unroll
          for (int i = 0; i < 2; ++i) cs[i] = SW ? mfma32(W[i][ks], ones, cs[i]) : mfma32(ones, W[i][ks], cs[i]);
        }
        __builtin_amdgcn_sched_barrier(0);
      }
    };
    mma(0, std::integral_constant<bool, true>{});
#pragma unroll 1
    for (int it = 0; it < 4; ++it) {
      if (it) mma(it, std::integral_constant<bool, false>{});
      LOGQ(2 + 3 * it);
      FRESH_LANE();
      float* const sp = (float*)(lds + L_ST) + (it & 1) * 64;
      if (SQ) { if ((c >> 2) == g) sp[32 + t0 * 16 + c] = (c & 3) == 0 ? st[0] : ((c & 3) == 1 ? st[1] : ((c & 3) == 2 ? st[2] : st[3])); }
      else if (g == 0) sp[t0 * 16 + c] = st[0];
      sync();
      LOGQ(3 + 3 * it);
      if (it == 0) fetch(2);
      const bool late = r + 48 >= 55;
      if (it == 1 && !late) fetch(3);
      const unsigned so = (unsigned)((r + 16 * it) * (cb < 5 ? 64 * QST : KVT));
      auto stat = [&](float sm, float sq, float& mean, float& rstd) {
        mean = sm * (1.0f / DM);
        rstd = __builtin_amdgcn_rsqf(__builtin_fmaxf(sq * (1.0f / DM) - mean * mean, 0.f) + 1e-5f);
      };
#pragma unroll
      for (int tt = 0; tt < 2; ++tt) {
        const int tq = tt ^ t0;
        float mean[4], rstd[4];
        if (SW) {
          stat(sp[tq * 16 + c], sp[32 + tq * 16 + c], mean[0], rstd[0]);
          rstd[0] *= qs;
        } else {
          const f32x4 sm = *(const f32x4*)(sp + tq * 16 + 4 * g), sq = *(const f32x4*)(sp + 32 + tq * 16 + 4 * g);
#pragma unroll
          for (int k = 0; k < 4; ++k) stat(sm[k], sq[k], mean[k], rstd[k]);
        }
#pragma unroll
        for (int i = 0; i < 2; ++i) {
          float v[4];
#pragma unroll
          for (int k = 0; k < 4; ++k) v[k] = (acc[i][tt][k] - mean[SW ? 0 : k] * cs[i][SW ? k : 0]) * rstd[SW ? 0 : k];
          __builtin_amdgcn_raw_buffer_store_b64((u32x2){pack2(v[0], v[1]), pack2(v[2], v[3])}, ro, vo[i][tt], so, Q_AUX);
        }
      }
      LOGQ(4 + 3 * it);
      if (it == 2 && late) lds_spin<L_MISC + 68, 0>(lds0, 5u);
    }
  };
  typedef std::integral_constant<bool, false> F_; typedef std::integral_constant<bool, true> T_;
  if (cb < 10) { if (w >> 1) body(T_{}, T_{}); else body(T_{}, F_{}); }
  else { if (w >> 1) body(F_{}, T_{}); else body(F_{}, F_{}); }
}

__device__ __forceinline__ void t2_mem(const bf16x8 fr, const bf16x8 q0, const bf16x8 q1, const __amdgpu_buffer_rsrc_t rs, unsigned o, int h, int j, float inv, int lane) {
  const f32x4 Z4 = {0.f, 0.f, 0.f, 0.f};
  const f32x4 d0 = mfma32(fr, q0, Z4) * inv, d1 = mfma32(fr, q1, Z4) * inv;
  const unsigned a0 = pack2n(d0[0], d0[1]), a1 = pack2n(d0[2], d0[3]), b0 = pack2n(d1[0], d1[1]), b1 = pack2n(d1[2], d1[3]);
  const auto r0 = __builtin_amdgcn_permlane32_swap(a0, b0, false, false), r1 = __builtin_amdgcn_permlane32_swap(a1, b1, false, false);
  SC1_STORE8S(((u32x2){r0[1], r1[1]}), ts_mem(h, j) + (lane >> 5) * 256 + (lane & 12) * 16 + (lane & 16) * 2 + (lane & 3) * 8, o);
}

template <int w>
__device__ __forceinline__ void rec_waves(const RecArgs& a, char* lds, __amdgpu_buffer_rsrc_t rs, unsigned& tlast) {
  PIN_S(rs);
  const int lane = __builtin_amdgcn_workitem_id_x() & 63, b = __builtin_amdgcn_workgroup_id_x(), c = lane & 15, g = lane >> 4, wid = w; (void)wid;
  const int gp = (b & 7) * 2 + (b >> 7), m = (b >> 3) & 15, h0 = 2 * gp, h = h0 + (m >> 3), j = m & 7;
  float* const misc = (float*)(lds + L_MISC);
  const f32x4 Z4 = {0.f, 0.f, 0.f, 0.f};
  LOGP(0);
  __builtin_amdgcn_s_setprio(3);
  const unsigned lds0 = (unsigned)(uintptr_t)(LAS char*)lds;
  if (!a.layer) img_fetch<12>(a, lds0, h, j, w, lane);
  constexpr int NC = w < 2 ? 2 : 3;
  const int far = ((b >> 2) & 1) ^ 1;
  auto chunk = [&](int f) { return f == 0 ? 4 * far + w : (w == 2 && f == 1 ? 8 + far : (w == 3 && f == 2 ? 9 - far : 4 - 4 * far + w)); };
  bf16x16 Wqk[3][NC];
  u32x4 bm0[NC][2];
#pragma unroll
  for (int f = 0; f < 2 * NC; ++f) bm0[f >> 1][f & 1] = *(const u32x4*)(a.mem0 + (size_t)(c & 7) * DM + 64 * chunk(f >> 1) + 16 * g + 8 * (f & 1));
  if (!a.layer) asm volatile("s_waitcnt vmcnt(0)" ::: "memory");
  BAR();
  LOGP(9);
  const unsigned ck = __builtin_amdgcn_readfirstlane(*(const unsigned*)(misc + 11));
  const unsigned flip = (ck ^ (unsigned)(b << 11)) & 8192u;
#pragma unroll
  for (int t = 0; t < 3; ++t)
#pragma unroll
    for (int f = 0; f < NC; ++f) Wqk[t][f] = cat16(wqk_frag(lds, t, 2 * chunk(f), lane), wqk_frag(lds, t, 2 * chunk(f) + 1, lane));
  if (a.layer) {
    if (w == 0) {
      for (unsigned spins = 0; spins < SPIN_CAP; ++spins) {
        if (__builtin_amdgcn_raw_buffer_load_b32(rs, (b & 7) * 256, (unsigned)OFF_RDY, 16) >= 16u) break;
        __builtin_amdgcn_s_sleep(4);
        asm volatile("" ::: "memory");
      }
      lds_inc<L_MISC + 84>(lds0);
    }
    lds_wait((unsigned*)(misc + 21), 2u);
#pragma unroll
    for (int f = 0; f < 2 * NC; ++f) PIN_V(bm0[f >> 1][f & 1]);
#pragma unroll
    for (int i = 0; i < 10; ++i) x_fetch(x_src(a, 0, b), lds0, 0, 10 * w + i, lane);
  }
  const bf16x8 SelLo = sel_frag(c, 0);
  bf16x8 ONES = mk8(0x3f803f80u, 0x3f803f80u, 0x3f803f80u, 0x3f803f80u);
  if (w < 2) PIN_V(ONES);
  bool dead = false;
  float inv = 0.f;
  unsigned* const cnt2 = (unsigned*)(misc + 7);
  unsigned l2off[2 * NC];
#pragma unroll
  for (int f = 0; f < 2 * NC; ++f) { l2off[f] = acc_off(16 * chunk(f >> 1) + 4 * g + 2 * (f & 1) + (c >> 3), c & 7) ^ flip; PIN_V(l2off[f]); }
  int sidx = (c >> 3) ? 0xEEEE : 0x4444;
  PIN_V(sidx);
  const unsigned p1off = (unsigned)((w == 0 ? (h & 2) * 1024 + j * 256 + (h & 1) * 64 + (g & 1) * 8 : (h & 3) * 32768 + j * 512 + (g >> 1) * 256 + (g & 1) * 64) + (c & 4) * 32 + (c & 3) * 16);
  unsigned p1v = (c < 8) && (w != 0 || g >= 2) ? p1off : 0x7ffffff0u, p1cur = 0x7ffffff0u;
  PIN_V(p1v); PIN_V(p1cur);
  const int hd = (c >> 2) & 1, mr = hd ? 3 - (c & 3) : (c & 3);
  const unsigned l1off = (unsigned)(((h0 + hd) & 3) * 32768 + g * 512 + (w & 1) * 128 + (c >> 3) * 64 + mr * 16);
  const unsigned l1offm = (unsigned)((h0 & 2) * 1024 + (g + 4 * (c >> 3)) * 256 + (w & 1) * 128 + hd * 64 + mr * 16);
  const int plane = (lane & ~6) | ((lane & 2) << 1) | ((lane & 4) >> 1);
  unsigned acco = c < 8 ? (acc_off(64 * far + m * 4 + g, 4 * (w & 1) + mr) + (unsigned)(hd * 8)) ^ flip : 0x7ffffff0u;
  unsigned acco2 = c >= 8 ? (acc_off(64 - 64 * far + m * 4 + g, 4 * (w & 1) + mr) + (unsigned)(hd * 8)) ^ flip
                          : (c < 4 ? (acc_off(128 + m * 2 + (g & 1), 4 * (w & 1) + mr) + (unsigned)((g >> 1) * 8)) ^ flip : 0x7ffffff0u);
  if (w < 2) { PIN_V(acco); PIN_V(acco2); }
  unsigned bmo[NC];
#pragma unroll
  for (int f = 0; f < NC; ++f) { bmo[f] = (unsigned)(chunk(f) * 1024 + lane * 16); PIN_V(bmo[f]); }
  bf16x8 bm[NC];
  auto bm_put = [&](int f) { *(bf16x8*)(lds + L_BM + bmo[f]) = bm[f]; __builtin_amdgcn_sched_barrier(0); };
  f32x4 acc[3] = {Z4, Z4, Z4};
  const int rt2 = ((c >> 3) * 8 + 4 * (g & 1)) * 28 + 16 + (c & 7);
  unsigned redo[3] = {(unsigned)(4 * g * 28 + c), (unsigned)(g < 2 ? (4 * g + 2) * 28 + c : rt2), (unsigned)(g < 2 ? (4 * g + 10) * 28 + c : rt2 + 56)};
#pragma unroll
  for (int k = 0; k < 3; ++k) { redo[k] = L_RED1 + w * 1792 + redo[k] * 4; PIN_V(redo[k]); }
  auto red_put = [&]() {
    auto hs = [&](float x, float y) { const auto sw = __builtin_amdgcn_permlane32_swap(f2u(x), f2u(y), false, false); return u2f(sw[0]) + u2f(sw[1]); };
#pragma unroll
    for (int k = 0; k < 3; ++k) {
#pragma unroll
      for (int i = 0; i < 2; ++i) *(float*)(lds + redo[k] + i * 112) = k == 0 ? hs(acc[0][i], acc[1][i]) : hs(acc[k - 1][2 + i], acc[2][2 * k - 2 + i]);
      __builtin_amdgcn_sched_barrier(0);
    }
  };
#pragma unroll
  for (int f = 0; f < NC; ++f) {
    const u32x4 u = bm0[f][0], v = bm0[f][1];
    bm[f] = c < 8 ? mk8(u[0], u[2], v[0], v[2]) : mk8(u[1], u[3], v[1], v[3]);
    bm_put(f);
#pragma unroll
    for (int t = 0; t < 3; ++t) acc[t] = smfma64(bm[f], Wqk[t][f], acc[t], sidx);
  }
  red_put();
  VM_DRAIN();
  lds_arrive((unsigned*)(misc + 6), lane);
  lds_wait((unsigned*)(misc + 6), 8u);
  x_fetch_n<10>(x_src(a, 1, b), lds0, 1, 10 * w, lane * 16);
  LOGP(1);
  STAMP_DECL(); ARRIVE_DECL();
  bf16x8 fr, qx[2];
  auto kq_get = [&](int seg) {
    const char* p = lds + (w == 1 ? L_KX : L_QX) + (seg % NKQ) * 2048 + lane * 16;
    if (w == 1 || w == 2) { qx[0] = *(const bf16x8*)p; qx[1] = *(const bf16x8*)(p + 1024); }
  };
  unsigned qo4 = (unsigned)(L_RED1 + (w == 1 ? (c & 7) * 112 + (c >> 3) * 3584 : c * 112) + g * 16), qo2 = qo4 + 64u - (unsigned)(g * 8), qo2b = qo2 + 3584u;
  if (w < 3) { PIN_V(qo4); PIN_V(qo2); if (w != 1) PIN_V(qo2b); }
  auto pub1 = [&](unsigned off, unsigned o1) {
    if (w == 0) fr = qk_frag(lds + qo4, lds + qo2, lds + qo2b);
    else {
      const f32x4 a0 = *(const f32x4*)(lds + qo4), a1 = *(const f32x4*)(lds + qo4 + 1792);
      const f32x2 b0 = *(const f32x2*)(lds + qo2), b1 = *(const f32x2*)(lds + qo2 + 1792);
      float v[6];
#pragma unroll
      for (int i = 0; i < 6; ++i) v[i] = i < 4 ? a0[i] + a1[i] : b0[i - 4] + b1[i - 4];
#pragma unroll
      for (int i = 0; i < 6; ++i) v[i] += dppf<DPP_ROR8>(v[i]);
      fr = mk8(pack2(v[0], v[1]), pack2(v[2], v[3]), pack2(v[4], v[5]), 0u);
    }
    const f32x4 sc = mfma32(w == 0 ? fr : qx[0], fr, Z4);
    if (w == 0) SC1_STORE8S(((u32x2){pack2n(sc[0], sc[1]), pack2n(sc[2], sc[3])}), off, o1);
    else {
      const f32x4 s2 = mfma32(qx[1], fr, Z4);
      __builtin_amdgcn_raw_buffer_store_b128((u32x4){pack2n(sc[0], sc[1]), pack2n(sc[2], sc[3]), pack2n(s2[0], s2[1]), pack2n(s2[2], s2[3])}, rs, off, o1, 16);
    }
  };
  unsigned o1 = sct_slot(ck, 0u), o1m = scm_slot(ck, 0u);
  unsigned t2o = ts_tok(h, j) + (unsigned)((c >> 2) * 256 + g * 64 + (c & 3) * 16);
  TokScores t2;
  TokTask tk;
  PvTask2 pv2;
  if (w == 2) tk.init(h, j, lane);
  if (w == 3) { PIN_V(t2o); pv2.init(gp, lane); }
  if (w == 0) pub1(p1v, o1m);
  VM_DRAIN();
  BAR();
  const unsigned tst = now32() | 1u;
  if (w == 3 && ST_X) __builtin_amdgcn_raw_buffer_store_b32(tst, rs, lane < 2 ? ts_piece(b >> 2) - ACC_TS + (b & 3) * 4 + lane * 8192 : 0x7ffffff0, (unsigned)(OFF_ACC + (SR - 1) * ACC_STEP) + ACC_TS, 16);
  LOGP(2);
  kq_get(0);
  if (w == 1) pub1(p1v, o1);
  x_fetch_n<10>(x_src(a, 2, b), lds0, 2, 10 * w, lane * 16);
  if (w == 3) { t2.get(lds, 0, lane); t2.run(0); t2.run(1); t2.store(rs, t2o, ts_slot(ck, 0)); }
  LOGP(3);
  unsigned o2 = (unsigned)OFF_ACC, o2p = 0u, tw = 0u;
  asm volatile("" : "+s"(o2p));
  unsigned tc0 = 0u, tstar = 0u;
  int dmin = 0, dmax = 0;
  int e1 = 100000;
  unsigned offs = 100u;
  unsigned o4 = 0u;
  unsigned o3, o3p;
  u32x16 P = {0u, 0u, 0u, 0u, 0u, 0u, 0u, 0u, 0u, 0u, 0u, 0u, 0u, 0u, 0u, 0u}, V = P;
  u32x8 V8 = {0u, 0u, 0u, 0u, 0u, 0u, 0u, 0u};
  if (w < 2) asm volatile("" : "+{v[208:223]}"(P), "+{v[184:191]}"(V8), "+{v[192:207]}"(V));
  unsigned tso = lane < 2 ? ts_piece(b >> 2) + (unsigned)((b & 3) * 4 + lane * 8192) : 0x7ffffff0u;
  if (w < 2) PIN_V(tso);
  float minf = g >= 2 ? -INFINITY : 0.f;
  if (w < 2) PIN_V(minf);
  int sv = NV - 1;
  bf16x8 vox[5];
  unsigned vma[3], need2;
  u32x4 slo = {0u, 0u, 0u, 0u}, shi = slo;
  float k1 = 0.f;
  const u16* xsrc;
  float invv = 1.0f;
  auto prep0 = [&](int s) {
    xsrc = x_src(a, s + 3, b);
    if (w == 2) { o3p = ts_slot(ck, (s + 1 - LAG3) & 3); o3 = ts_slot(ck, imax(s, 0)); o4 = pt_slot(ck, imax(s - LAG3, 0)); PIN_S(o3); }
    if (w == 3) { o3p = pt_slot(ck, (s - LAG4) & 3); o4 = ts_slot(ck, s + 1); }
    PIN_S(xsrc); PIN_S(o3p); PIN_S(o4);
    __builtin_amdgcn_sched_barrier(0);
  };
  auto prep = [&](int s) {
    o2 = (unsigned)OFF_ACC + (unsigned)(s % SR) * (unsigned)ACC_STEP;
    sv = sv == NV - 1 ? 0 : sv + 1;
    PIN_S(o2); PIN_S(sv);
    if (w < 2) {
      PIN_S(tc0);
      o1 = sct_slot(ck, s % SR); o1m = scm_slot(ck, s % SR);
      need2 = 4u * (unsigned)(s + 1) + 2u * ((unsigned)(s + 2) / NSEG);
      PIN_S(o1); PIN_S(o1m); PIN_S(need2);
      __builtin_amdgcn_sched_barrier(0);
      kq_get(s);
    }
    invv = misc[4 + (s & 1)];
  };
  auto prep2 = [&](int s) {
#pragma unroll
    for (int k = 0; k < 3; ++k) {
      const int t = k == 0 ? 2 : (k == 1 ? far : 1 - far);
      const char* p = lds + L_VOX + sv * 5120 + t * 1024 + (plane & 47) * 16 + (g & 1) * 8;
      auto rd = [&](const char* q) { const u32x2 a0 = *(const u32x2*)q, a1 = *(const u32x2*)(q + 256); return mk8(a0[0], a0[1], a1[0], a1[1]); };
      vox[k == 0 ? 0 : 2 * k - 1] = rd(p);
      if (k) vox[2 * k] = rd(p + 3072);
      vma[k] = lds0 + (unsigned)(L_VOM + sv * 1280 + t * 256 + (plane & 31) * 8);
      PIN_V(vma[k]);
    }
    __builtin_amdgcn_sched_barrier(0);
    if (F0A) { xsrc = x_src(a, s + 3, b); PIN_S(xsrc); }
    const unsigned iu = __builtin_amdgcn_readfirstlane(f2u(invv));
    if (__builtin_expect(iu != f2u(inv), 0)) {
      inv = u2f(iu);
#pragma unroll
      for (int i = 0; i < 8; ++i) {
        u32x4& m = i < 4 ? slo : shi;
        m[i & 3] = (c >> 1) == i ? ((c & 1) ? iu & 0xffff0000u : iu >> 16) : 0u;
      }
      k1 = KEXP * inv;
    }
    PIN_V(k1); PIN_V(slo); PIN_V(shi);
    asm volatile("" : "+v"(vox[0]), "+v"(vox[1]), "+v"(vox[2]), "+v"(vox[3]), "+v"(vox[4]));
    __builtin_amdgcn_sched_barrier(0);
  };
  auto rows = [&](int s) {
    const int dl = spin_until(tw);
    if (w < 2) tc0 = cyc32();
    __builtin_amdgcn_sched_barrier(0);
    STAMP(1);
    unsigned spins = 0;
    auto pass = [&](bool first) {
      u32x4 x[2 * NC];
#pragma unroll
      for (int f = 0; f < NC; ++f) {
        if (f && w < 2 && first) { prep(s); asm volatile(".rept %0\n\ts_nop 3\n\t.endr" :: "n"(P2_G1)); }
        else if (f) asm volatile(".rept %0\n\ts_nop 3\n\t.endr" :: "n"(w < 2 ? P2_G0 : (f == 1 ? P2_G2 : P2_G3)));
        __builtin_amdgcn_sched_barrier(0);
        x[2 * f] = SC1_LOADS(l2off[2 * f], o2p); x[2 * f + 1] = SC1_LOADS(l2off[2 * f + 1], o2p);
        __builtin_amdgcn_sched_barrier(0);
      }
      if (first) lds_inc<L_MISC + 92>(lds0);
      if (first && w < 2 && PR_P != 3) __builtin_amdgcn_s_setprio(PR_P);
      if (first) { if (w >= 2) prep(s); else prep2(s); }
      if (w >= 2 && first && P2_W) lds_spin<L_MISC + 92, 0>(lds0, 4u * (unsigned)s);
      if (w == 2 && first) tk.store(rs, o4, s > LAG3 ? 0u : 0x7ff00000u);
      if (w == 3 && first) t2.store(rs, t2o, o4);
      if (first && w < 2) {
        p1cur = p1v; offs = (offs - (unsigned)dl) & ~3u;
        PIN_V(p1cur); PIN_S(offs);
        tstar = tc0 + (unsigned)(e1 + D1_M + (w == 1 ? D1_W1 : 0)) - offs * 20u;
        dmin = offs < 40u ? D1_MIN : REC_D1;
        dmin = s < EARLY_N ? REC_D1 + EARLY_D1 : dmin;
        dmax = REC_D1;
        PIN_S(tstar); PIN_S(dmin); PIN_S(dmax);
      }
      __builtin_amdgcn_sched_barrier(0);
      unsigned ok = 32u;
#pragma unroll
      for (int t = 0; t < 3; ++t) { acc[t] = Z4; PIN_V(acc[t]); }
      if (first && w < 2 && PR_P != 3) __builtin_amdgcn_s_setprio(3);
      __builtin_amdgcn_sched_barrier(0);
      bool r = true;
#pragma unroll
      for (int f = 0; f < NC; ++f) {
        unsigned p[4];
#pragma unroll
        for (int k = 0; k < 2; ++k) {
          const u32x4 v = x[2 * f + k];
          p[2 * k] = pack2((float)(int)v[0], (float)(int)v[1]); p[2 * k + 1] = pack2((float)(int)v[2], (float)(int)v[3]);
          asm volatile("" : "+v"(p[2 * k]), "+v"(p[2 * k + 1]));
          __builtin_amdgcn_sched_barrier(0);
        }
        ok = (ok & x[2 * f][0] & x[2 * f][2]) & (x[2 * f + 1][0] & x[2 * f + 1][2]);
        PIN_V(ok);
        __builtin_amdgcn_sched_barrier(0);
        bm[f] = mk8(p[0], p[1], p[2], p[3]);
        acc[0] = smfma64(bm[f], Wqk[0][f], acc[0], sidx);
        __builtin_amdgcn_sched_barrier(0);
        bm_put(f);
        acc[1] = smfma64(bm[f], Wqk[1][f], acc[1], sidx);
        __builtin_amdgcn_sched_barrier(0);
        if (f == NC - 1) { u64 bad = __builtin_amdgcn_ballot_w64(ok == 0u); PIN_S(bad); r = bad == 0ull; }
        __builtin_amdgcn_sched_barrier(0);
        acc[2] = smfma64(bm[f], Wqk[2][f], acc[2], sidx);
        __builtin_amdgcn_sched_barrier(0);
      }
      red_put();
      return r;
    };
    if (__builtin_expect(!pass(true), 0)) {
      FC(0);
      do {
        if (dead || ++spins > SPIN_CAP) { dead = true; break; }
        asm volatile("" ::: "memory");
      } while (!pass(false));
    }
    STAMPV(12, spins); LOGF(1, spins);
    TSTAMP(2, now32()); TSTAMP(3, spins);
  };
  if (w >= 2) prep0(-1);
  prep(0);
  if (w < 2) prep2(0);
  VM_DRAIN();
  CODE_PAD((PADR >> (4 * w)) & 15);
#pragma unroll 1
  for (int s = 0;; ++s) {
    if (__builtin_expect(o2p != 0u, 1)) rows(s);
    if (w < 2 && __builtin_expect(s == NSEG - 1, 0)) break;
    STAMP(2);
    BAR();
    STAMP(3);
    if (w < 2) pub1(p1cur, w == 0 ? o1m : o1);
    if (w == 2) tk.load(rs, o3p);
    if (w == 3) pv2.load(rs, o3p);
    const unsigned tp1 = cyc32();
    __builtin_amdgcn_sched_barrier(0);
    if (w == 2) {
      __builtin_amdgcn_s_setprio(W2_P);
      prep0(s);
      if (W2_D) spin_cyc(tp1 + W2_D);
      fr = qk_frag(lds + qo4, lds + qo2, lds + qo2b);
      t2_mem(fr, qx[0], qx[1], rs, o3, h, j, invv * KEXP, lane);
      __builtin_amdgcn_sched_barrier(0);
      asm volatile("s_waitcnt vmcnt(1)" ::: "memory");
      lds_inc<L_MISC + 28>(lds0);
      kq_get(s + 1);
    }
    if (w == 3) {
      __builtin_amdgcn_s_setprio(T4_P);
      prep0(s);
      t2.get(lds, s + 1, lane); t2.run(0); t2.run(1);
      __builtin_amdgcn_sched_barrier(0);
    }
    if (w >= 2 && __builtin_expect(s == NSEG - 2, 0)) { VM_DRAIN(); lds_inc<L_MISC + 28>(lds0); }
    if (w >= 2) __builtin_amdgcn_s_setprio(UN_P);
    TSTAMP(0, now32());
    STAMP(4);
    __builtin_amdgcn_sched_barrier(0);
    unsigned tpub = 0;
    u32x2 sw;
    if (w == 2) {
      STAMP(5);
      __builtin_amdgcn_sched_barrier(0);
      STAMP(6);
      ARRIVE(0);
      tk.run(j, lane, SelLo);
      if (__builtin_expect(s == NSEG - 1, 0)) { tlast = tp1; break; }
      if (F2B) { lds_spin<L_MISC + 48, 1>(lds0, 2u * (unsigned)(s + 1)); x_fetch_n<F2B>(xsrc, lds0, s + 3, P2B, lane * 16); }
      __builtin_amdgcn_s_setprio(SP_P);
    }
    if (w == 3) {
      pv2.run<false>(lds, sv < 1 + LAG4 ? sv + NV - 1 - LAG4 : sv - 1 - LAG4, lane, rs, 0u);
      if (__builtin_expect(s == NSEG - 1, 0)) { tlast = tp1; break; }
      lds_inc<L_MISC + 112>(lds0);
      STAMP(11);
      __builtin_amdgcn_s_setprio(SP_P);
      STAMP(5);
      ARRIVE(0);
      if (F3) { lds_spin<L_MISC + 48, 1>(lds0, 2u * (unsigned)(s + 1)); x_fetch_n<F3>(xsrc, lds0, s + 3, 40 - F3C - F3, lane * 16); }
      STAMP(6);
    }
    if (w < 2) {
      __builtin_amdgcn_s_setprio(D1_P);
      x_fetch_n<F0A>(xsrc, lds0, s + 3, PA + w * F0A, lane * 16);
      int dq = imin((int)(tstar - tp1), dmax);
      dq = dq < -D1_LATE ? dmax : dq;
      PIN_S(dq);
      spin_exact(tp1 + (unsigned)imax(dq, dmin));
      STAMP(5);
      u32x32 S;
      auto sc_load = [&]() {
        asm volatile("buffer_load_dwordx4 v[224:227], %2, %4, %5 offen sc1\n\t.rept %7\n\ts_nop 3\n\t.endr\n\t"
                     "buffer_load_dwordx4 v[228:231], %3, %4, %6 offen sc1\n\tbuffer_load_dwordx4 v[232:235], %3, %4, %6 offen offset:2048 sc1\n\t"
                     "buffer_load_dwordx4 v[236:239], %3, %4, %6 offen offset:256 sc1\n\tbuffer_load_dwordx4 v[240:243], %3, %4, %6 offen offset:2304 sc1\n\t"
                     "v_mov_b32 v220, 0x42000000\n\tv_mov_b32 v221, 0\n\tv_mov_b32 v222, 0x42000000\n\tv_mov_b32 v223, 0"
                     : "=&{v[224:255]}"(S), "+{v[208:223]}"(P) : "v"(l1offm), "v"(l1off), "s"(rs), "s"(o1m), "s"(o1), "n"(P1_G) : "memory");
      };
      sc_load();
      __builtin_amdgcn_sched_barrier(0);
      lds_inc<L_MISC + 48>(lds0);
      STAMP(6);
      ARRIVE(0);
      lds_spin<L_MISC + 28, 0>(lds0, need2);
      __builtin_amdgcn_s_setprio(3);
      asm volatile("ds_read_b64 v[192:193], %3\n\tds_read_b64 v[196:197], %3 offset:768\n\tds_read_b64 v[200:201], %4\n\tds_read_b64 v[204:205], %4 offset:768\n\tds_read_b64 v[184:185], %2"
                   : "+{v[184:191]}"(V8), "+{v[192:207]}"(V) : "v"(vma[0]), "v"(vma[1]), "v"(vma[2]) : "memory");
      __builtin_amdgcn_sched_barrier(0);
      {
        const int de = imin((int)(tp1 - tc0) - e1, D1_C);
        e1 += s == 0 ? 0 : de >> (s < EARLY_N && de < -D1_C ? 0 : D1_G);
        PIN_S(e1);
      }
      unsigned sn = (unsigned)(s + 1);
      PIN_V(sn);
      STAMP(11);
      __builtin_amdgcn_sched_barrier(0);
      {
        unsigned spins = 0;
        u64 miss, bad;
#define MM(d, a, b, c) "v_mfma_f32_16x16x32_bf16 " d ", " a ", " b ", " c "\n\t"
#define FM(d, x, r, ctrl) "v_fmac_f32_dpp " d ", " x ", " r " " ctrl "\n\t"
#define ID_ "quad_perm:[0,1,2,3] row_mask:0xf bank_mask:"
#define HM_ "row_half_mirror row_mask:0xf bank_mask:"
#define R8_ "row_ror:8 row_mask:0xf bank_mask:"
#define MI_ "row_mirror row_mask:0xf bank_mask:"
        auto soft = [&]() {
          asm volatile(
            "s_waitcnt vmcnt(4)\n\t" MM("v[244:247]", "%6", "v[224:227]", "0") "v_min3_u32 v214, v224, v225, v226\n\t"
            "s_waitcnt vmcnt(3)\n\t" MM("v[248:251]", "%6", "v[228:231]", "0") "v_min3_u32 v214, v214, v227, v228\n\tv_min3_u32 v214, v214, v229, v230\n\t"
            "s_waitcnt vmcnt(2)\n\t" MM("v[248:251]", "%6", "v[232:235]", "v[248:251]") "v_min3_u32 v214, v214, v231, v232\n\t"
            "v_add_f32_dpp v212, v244, v244 " R8_ "0x3\n\tv_add_f32_dpp v213, v245, v245 " R8_ "0x3\n\t"
            "s_waitcnt vmcnt(1)\n\t" MM("v[248:251]", "%7", "v[236:239]", "v[248:251]")
            "v_add_f32_dpp v212, v246, v246 " R8_ "0xc\n\tv_add_f32_dpp v213, v247, v247 " R8_ "0xc\n\t"
            "s_waitcnt vmcnt(0) lgkmcnt(0)\n\t" MM("v[248:251]", "%7", "v[240:243]", "v[248:251]")
            "v_fma_f32 v212, v212, %14, %15\n\tv_fma_f32 v213, v213, %14, %15\n\tv_exp_f32 v216, v212\n\tv_exp_f32 v217, v213\n\t"
            "v_min3_u32 v214, v214, v233, v234\n\tv_cvt_pk_bf16_f32 v208, v216, v217\n\tv_min3_u32 v214, v214, v235, v236\n\tds_read_b96 v[188:190], %16 offset:%18\n\t"
            "v_exp_f32 v216, v248\n\tv_mov_b32_dpp v209, v208 " R8_ "0xf\n\tv_exp_f32 v217, v249\n\tv_exp_f32 v218, v250\n\t"
            MM("v[252:255]", "%8", "v[208:211]", "0") "v_exp_f32 v219, v251\n\tv_cvt_pk_bf16_f32 v232, v216, v217\n\t"
            MM("v[224:227]", "v[192:195]", "v[208:211]", "0") "v_cvt_pk_bf16_f32 v233, v218, v219\n\tv_min3_u32 v214, v214, v237, v238\n\tv_mov_b32_dpp v234, v232 " R8_ "0xf\n\t"
            MM("v[228:231]", "v[196:199]", "v[208:211]", "0") "v_mov_b32_dpp v235, v233 " R8_ "0xf\n\t"
            MM("v[244:247]", "v[204:207]", "v[208:211]", "0") "v_min3_u32 v214, v214, v239, v240\n\t"
            MM("v[252:255]", "%8", "v[232:235]", "v[252:255]") MM("v[224:227]", "%10", "v[232:235]", "v[224:227]") "v_min3_u32 v214, v214, v241, v242\n\t"
            MM("v[228:231]", "%11", "v[232:235]", "v[228:231]") "s_waitcnt lgkmcnt(0)\n\tv_sub_u32 v215, v190, %17 clamp\n\t"
            MM("v[236:239]", "v[200:203]", "v[208:211]", "0") "v_min3_u32 v214, v214, v243, v215\n\tv_cmp_eq_u32 %4, 0, v214\n\tv_rcp_f32 v216, v252\n\t"
            MM("v[240:243]", "v[184:187]", "v[208:211]", "0") "v_mul_f32 v216, v216, v188\n\tv_subrev_u32 v215, 0x2f800000, v252\n\t"
            MM("v[236:239]", "%12", "v[232:235]", "v[236:239]") FM("v220", "v224", "v216", ID_ "0x1") FM("v221", "v225", "v216", ID_ "0x1")
            "v_mov_b32_dpp v217, v216 " HM_ "0xf\n\t"
            MM("v[244:247]", "%13", "v[232:235]", "v[244:247]") FM("v220", "v230", "v216", ID_ "0x2") FM("v221", "v231", "v216", ID_ "0x2")
            MM("v[240:243]", "%9", "v[232:235]", "v[240:243]") FM("v220", "v228", "v217", HM_ "0x1") FM("v221", "v229", "v217", HM_ "0x1")
            "v_cmp_ge_u32 vcc, v215, v189\n\t" FM("v220", "v226", "v217", HM_ "0x2") FM("v221", "v227", "v217", HM_ "0x2")
            "s_or_b64 %5, %4, vcc"
            : "+{v[224:255]}"(S), "+{v[208:223]}"(P), "+{v[184:191]}"(V8), "+{v[192:207]}"(V), "=&s"(miss), "=&s"(bad)
            : "v"(slo), "v"(shi), "v"(ONES), "v"(vox[0]), "v"(vox[1]), "v"(vox[2]), "v"(vox[3]), "v"(vox[4]), "v"(k1), "v"(minf), "v"(lds0), "s"(s), "n"(L_MISC + 32)
            : "vcc", "scc", "memory");
        };
        soft();
        if (__builtin_expect(bad != 0ull, 0)) {
          if (miss) FC(1); else FC(2);
          while (miss != 0ull && !dead) {
            if (++spins > SPIN_CAP) dead = true;
            asm volatile("s_nop 7\n\ts_nop 7" ::: "memory");
            sc_load(); soft();
          }
          if (bad != 0ull) {
            u32x4 y[5];
#pragma unroll
            for (int k = 0; k < 4; ++k) y[k] = SC1_LOADS(l1off + (k & 1) * 2048 + (k >> 1) * 256, o1);
            y[4] = SC1_LOADS(l1offm, o1m);
            const f32x4 d0 = mfma32(as8(slo), as8(y[4]), Z4);
            f32x4 d1 = mfma32(as8(shi), as8(y[3]), mfma32(as8(shi), as8(y[2]), mfma32(as8(slo), as8(y[1]), mfma32(as8(slo), as8(y[0]), Z4))));
            const bool lo = c < 8;
            const float m0 = __builtin_fmaf((lo ? d0[0] : d0[2]) + dppf<DPP_ROR8>(lo ? d0[2] : d0[0]), k1, minf);
            const float m1 = __builtin_fmaf((lo ? d0[1] : d0[3]) + dppf<DPP_ROR8>(lo ? d0[3] : d0[1]), k1, minf);
            float mx = fmx(fmx(fmx(d1[0], d1[1]), d1[2]), fmx(fmx(d1[3], m0), m1));
            mx = fmx(mx, dppf<DPP_ROR8>(mx));
            mx = x32mx(x16mx(mx));
            d1 -= mx;
            const unsigned mm = pack2(__builtin_amdgcn_exp2f(m0 - mx), __builtin_amdgcn_exp2f(m1 - mx));
            const unsigned t01 = pack2(__builtin_amdgcn_exp2f(d1[0]), __builtin_amdgcn_exp2f(d1[1])), t23 = pack2(__builtin_amdgcn_exp2f(d1[2]), __builtin_amdgcn_exp2f(d1[3]));
            const bf16x8 pm = mk8(mm, dpp_ror8(mm), 0u, 0u), pt = mk8(t01, t23, dpp_ror8(t01), dpp_ror8(t23));
            const float r = __builtin_amdgcn_rcpf(mfma32(ONES, pt, mfma32(ONES, pm, Z4))[0]) * u2f(V8[4]), rm = dppf<0x141>(r);
            f32x4 o[5];
#pragma unroll
            for (int k = 0; k < 5; ++k) o[k] = mfma32(vox[k], pt, mfma32(k ? mk8(V[4 * k - 4], V[4 * k - 3], 0u, 0u) : mk8(V8[0], V8[1], 0u, 0u), pm, Z4));
#pragma unroll
            for (int i = 0; i < 2; ++i) {
              const float oa = dppf<0x141>(o[2][i]), ob = dppf<0x141>(o[1][2 + i]);
              P[12 + i] = f2u(__builtin_fmaf(hd ? ob : oa, rm, __builtin_fmaf(hd ? o[2][2 + i] : o[1][i], r, i ? 0.f : 32.0f)));
            }
            P[8] = f2u(r); P[9] = f2u(rm);
#pragma unroll
            for (int i = 0; i < 4; ++i) { S[12 + i] = f2u(o[3][i]); S[16 + i] = f2u(o[0][i]); S[20 + i] = f2u(o[4][i]); }
            asm volatile("s_nop 7\n\ts_nop 7" : "+{v[224:255]}"(S), "+{v[208:223]}"(P));
          }
        }
        STAMP(7); STAMPV(13, spins); LOGF(0, spins);
        {
          asm volatile(".if %11 == 0\n\tds_write_b32 %8, %12 offset:%10\n\t.endif\n\t"
                       "v_cvt_i32_f32 v218, v220\n\tv_cvt_i32_f32 v219, v221\n\tv_and_or_b32 v218, v218, %7, 2\n\tbuffer_atomic_add_x2 v[218:219], %3, %5, %6 offen\n\t"
                       ".if %11 == 1\n\tds_write_b32 %8, %12 offset:%10\n\t.endif\n\t"
                       FM("v222", "v236", "v216", R8_ "0x4") FM("v223", "v237", "v216", R8_ "0x4") "ds_read_b64 %2, %8 offset:%9\n\t"
                       FM("v222", "v238", "v217", MI_ "0x8") FM("v223", "v239", "v217", MI_ "0x8") "v_permlane32_swap_b32 v240, v242\n\t"
                       FM("v222", "v244", "v217", MI_ "0x4") FM("v223", "v245", "v217", MI_ "0x4") "v_permlane32_swap_b32 v241, v243\n\t"
                       FM("v222", "v246", "v216", R8_ "0x8") FM("v223", "v247", "v216", R8_ "0x8") ".if %11 == 2\n\tds_write_b32 %8, %12 offset:%10\n\t.endif\n\t"
                       FM("v222", "v240", "v216", ID_ "0x1") FM("v223", "v241", "v216", ID_ "0x1") FM("v222", "v242", "v217", HM_ "0x1") FM("v223", "v243", "v217", HM_ "0x1")
                       "v_cvt_i32_f32 v212, v222\n\tv_cvt_i32_f32 v213, v223\n\tv_and_or_b32 v212, v212, %7, 2\n\tbuffer_atomic_add_x2 v[212:213], %4, %5, %6 offen\n\t"
                       ".if %11 == 3\n\tds_write_b32 %8, %12 offset:%10\n\ts_waitcnt lgkmcnt(1)\n\t.else\n\ts_waitcnt lgkmcnt(0)\n\t.endif"
                       : "+{v[224:255]}"(S), "+{v[208:223]}"(P), "=&v"(sw) : "v"(acco), "v"(acco2), "s"(rs), "s"(o2), "s"(~63u), "v"(lds0), "n"(L_MISC + 96),
                         "n"(L_MISC + 104 + 4 * (w & 1)), "n"(REL_AT), "v"(sn) : "memory");
          __builtin_amdgcn_sched_barrier(0);
          tpub = now32();
          asm volatile("buffer_atomic_umax %0, %1, %2, %3 offen" :: "v"(tpub | 1u), "v"(tso), "s"(rs), "s"(o2) : "memory");
          STAMP(10);
        }
      }
    }
    STAMP(8);
    ARRIVE(1);
    o2p = o2;
    PIN_S(o2p);
    if (w < 2) {
      if (__builtin_expect(__builtin_amdgcn_readfirstlane(sw[1]) <= (unsigned)s, 0)) {
        lds_spin<L_MISC + 100, 0>(lds0, (unsigned)s + 1u);
        sw[0] = __hip_atomic_load((unsigned*)(misc + 24), __ATOMIC_RELAXED, __HIP_MEMORY_SCOPE_WORKGROUP);
      }
    } else {
      u32x4 q;
      asm volatile("1:\n\tds_read_b128 %0, %1 offset:%3\n\t.if %4\n\ts_sleep 1\n\t.endif\n\ts_waitcnt lgkmcnt(0)\n\tv_min3_u32 v201, v201, v202, v203\n\tv_cmp_ge_u32 vcc, %2, v201\n\ts_cbranch_vccnz 1b"
                   : "=&{v[200:203]}"(q) : "v"(lds0), "s"(s), "n"(L_MISC + 96), "n"(SP_SL) : "vcc", "memory");
      __builtin_amdgcn_s_setprio(3);
      tpub = now32();
      sw[0] = q[0];
    }
    STAMP(9);
    static_assert(F3C == 0 && F2C == 0 && F0C == 0, "nothing behind the atomics");
    LOGT(0, tpub); LOGT(1, tb2_);
    TSTAMP(1, tpub);
    ARRIVE_OUT();
    const unsigned an = __builtin_amdgcn_readfirstlane(sw[0]);
    const int ahead = (int)(an - tpub);
    tw = tpub + (unsigned)(an == 0u || ahead > 1000 ? SCH_FIRST : imax(ahead, w < 2 ? SCH_DMIN : 0));
    offs = tw - an;
  }
  STAMP_OUT();
  LOGP(4);
  __builtin_amdgcn_s_setprio(3);
  if (w < 2) { BAR(); tlast = cyc32(); }
  if (w == 2) tk.store(rs, o4);
  if (w == 3) {
    const unsigned oo = op_slot(NSEG - 2 - LAG4) + (unsigned)((m * 32 + gp) * 1280);
    const char* sp = lds + L_STG + 2048;
    const u32x4 v0 = *(const u32x4*)(sp + lane * 16), v1 = *(const u32x4*)(sp + 1280 + lane * 16), v2 = *(const u32x4*)(sp + (g & 1) * 1280 + 1024 + c * 16);
    SC1_STORE(v0, oo + lane * 16); SC1_STORE(v1, oo + 20480 + lane * 16); SC1_STORE(v2, lane < 32 ? oo + g * 20480 + 1024 + c * 16 : 0x7ffffff0u);
  }
  if (dead) tlast |= 1u; else tlast &= ~1u;
}

__device__ __forceinline__ void drain(const RecArgs& a, char* lds, const __amdgpu_buffer_rsrc_t rs, const int w, const unsigned tp1) {
  static_assert(LAG3 == 2 && LAG4 == 3 && LAG5 == 6, "drain schedule");
  const int lane = __builtin_amdgcn_workitem_id_x() & 63, b = __builtin_amdgcn_workgroup_id_x(), c = lane & 15;
  const int gp = (b & 7) * 2 + (b >> 7), m = (b >> 3) & 15, j = m & 7;
  unsigned* const misc = (unsigned*)(lds + L_MISC);
  const unsigned lds0 = (unsigned)(uintptr_t)(LAS char*)lds, ck = __builtin_amdgcn_readfirstlane(misc[11]);
  bool dead = tp1 & 1u;
  auto poll = [&](auto load, auto ready) {
    for (unsigned spins = 0;;) {
      asm volatile("" ::: "memory");
      load();
      if (ready() || dead) break;
      if (++spins > SPIN_CAP) dead = true;
      if (END_SL) __builtin_amdgcn_s_sleep(END_SL);
    }
  };
  LOGD(0);
#define TASK(ty, sg, tm) ((u64)((ty) | ((sg) - 56) << 2 | ((tm) / 100) << 5))
  u64 todo = w == 0 ? TASK(1, 63, END_E1) | TASK(2, 63, END_E2) << 12 | TASK(3, 62, END_E3) << 24
           : w == 1 ? TASK(1, 62, 0) | TASK(2, 62, END_E62) << 12 | TASK(3, 60, END_E3) << 24
           : w == 2 ? TASK(2, 60, 0) | TASK(3, 56, 0) << 12 : TASK(2, 61, END_E61) | TASK(3, 58, END_E59) << 12;
  TokTask tk;
  PvTask2 pv;
  OutTask ot;
  {
    const u32x4* p = (const u32x4*)(lds + L_DRN) + lane;
    const u32x4 q0 = p[0], q1 = p[64], q2 = p[128];
    tk.l0 = q0[0]; tk.l1 = q0[0] + 16384u; tk.lm = q0[1]; tk.s0 = q0[2]; tk.s1 = q0[3];
    pv.lo = q1[0]; pv.lm = q1[1]; pv.vr = q1[2]; pv.ma = q1[3]; pv.mz = q2[0]; pv.so = q2[1];
    pv.pa[0] = pv.pa[1] = pv.pb[0] = pv.pb[1] = (u32x4){0u, 0u, 0u, 0u};
    ot.t5f = q2[2]; ot.t5o = q2[3]; ot.sel = as8(p[192]);
  }
#pragma unroll 1
  for (int ti = 0; todo; todo >>= 12, ++ti) {
    const int ty = (int)todo & 3, sl = 56 + (((int)todo >> 2) & 7);
    const unsigned tm = tp1 + (((unsigned)todo >> 5) & 0x7fu) * 100u;
    if (ty == 1) {
      wait_cyc(tm);
      LOGD(3 * ti + 1);
      poll([&]() { tk.load(rs, ts_slot(ck, sl)); }, [&]() { return tk.ready(); });
      LOGD(3 * ti + 2);
      tk.run(j, lane, ot.sel); tk.store(rs, pt_slot(ck, sl));
    } else if (ty == 2) {
      const unsigned t0 = cyc32();
      if (sl >= NSEG - 2) { lds_wait(misc + 7, 4u * NSEG + 3u); wait_cyc(t0 + END_G2); }
      wait_cyc(tm);
      LOGD(3 * ti + 1);
      poll([&]() { pv.load(rs, pt_slot(ck, sl)); }, [&]() { return pv.ready(); });
      LOGD(3 * ti + 2);
      pv.run<true>(lds, sl % NV, lane, rs, op_slot(sl) + (unsigned)((m * 32 + gp) * 1280));
      if (sl == NSEG - 1) misc[14] = cyc32() | 1u;
    } else {
      if (sl >= NSEG - 4) { lds_wait(misc + 14, 1u); wait_cyc(__builtin_amdgcn_readfirstlane(misc[14]) + END_G3); }
      wait_cyc(tm);
      LOGD(3 * ti + 1);
      OutTask o2 = ot;
      poll([&]() { o2.load(a, rs, sl + 1, b); ot.load(a, rs, sl, b); }, [&]() { return ot.ready(b) & o2.ready(b); });
      LOGD(3 * ti + 2);
      o2.run(lane); o2.store(a, sl + 1, b, lane);
      ot.run(lane); ot.store(a, sl, b, lane);
    }
    LOGD(3 * ti + 3);
  }
  VM_DRAIN();
  if (w != 0) lds_inc<L_MISC + 64>(lds0);
  else {
    constexpr unsigned o2 = (unsigned)OFF_ACC + (unsigned)((NSEG - 1) % SR) * (unsigned)ACC_STEP;
    lds_wait(misc + 16, 3u);
    LOGD(10);
    if (lane == 0) __hip_atomic_fetch_add((gu64*)((GAS char*)(a.ws + o2) + acc_off(m * 4, 0)), (u64)1, RLX_AGENT);
    wait_until(now32() + SCH_DRAIN);
    wait_cyc(tp1 + END_E4);
    unsigned x;
    if (qkv_row(b) + 48 >= 55 && qkv_col(b) < 15) poll([&]() { x = __builtin_amdgcn_raw_buffer_load_b32(rs, acc_off(c * 4, 0), o2, 16); }, [&]() { return wave_all((x & 16u) != 0u); });
    lds_inc<L_MISC + 68>(lds0);
    LOGD(11);
  }
  if (qkv_row(b) + 48 >= 55 && qkv_col(b) < 15) {
    lds_wait(misc + 18, 8u);
    lds_spin<L_MISC + 68, 1>(lds0, 1u);
    x_fetch_n<10>(x_src(a, NSEG + 3, b), lds0, 3, 10 * w, lane * 16);
    VM_DRAIN();
    lds_inc<L_MISC + 68>(lds0);
  }
  LOGP(8);
}

__device__ __forceinline__ void proj_front(const RecArgs& a, char* lds, const __amdgpu_buffer_rsrc_t rs, const int v) {
  const int lane = __builtin_amdgcn_workitem_id_x() & 63, b = __builtin_amdgcn_workgroup_id_x(), c = lane & 15, g = lane >> 4;
  const int rb = (b & 7) + 8 * (b >> 5), cq = (b >> 3) & 3;
  const unsigned lds0 = (unsigned)(uintptr_t)(LAS char*)lds;
  unsigned* const cnt = (unsigned*)(lds + L_MISC) + 20;
  LOGF2(0);
  float* const red = (float*)(lds + L_RED1);
  float* const fin = red + 640;
  const f32x4 Z4 = {0.f, 0.f, 0.f, 0.f};
  x_fetch_n<8>((const u16*)(a.ws + OFF_OB) + (size_t)rb * 32 * DM, lds0, 0, 8 * v, lane * 16);
  bf16x8 W[2][20];
  const __amdgpu_buffer_rsrc_t rw = __builtin_amdgcn_make_buffer_rsrc(a.ws + OFF_WOBT + (size_t)(cq * 10 + 2 * v) * 20480, 0, 40960, 0x00020000);
  auto w_get = [&](int ks) {
#pragma unroll
    for (int i = 0; i < 2; ++i) W[i][ks] = as8(__builtin_amdgcn_raw_buffer_load_b128(rw, lane * 16 + (ks & 3) * 1024, (i * 20 + (ks & ~3)) * 1024, 0));
    __builtin_amdgcn_sched_barrier(0);
  };
#pragma unroll
  for (int ks = 0; ks < FW; ++ks) w_get(ks);
  const unsigned fo = (unsigned)(rb * 40960 + (cq * 5 + v) * 1024 + ((g >> 1) * 16 + c) * 16), f8 = fo + (g & 1) * 8, f16 = fo + (g & 1) * 20480;
  u32x2 xo[2][2];
#pragma unroll
  for (int k = 0; k < 4; ++k)
    xo[k >> 1][k & 1] = __builtin_bit_cast(u32x2, __hip_atomic_load((const gu64*)((const char*)a.xnew + f8 + (k >> 1) * 512 + (k & 1) * 20480), RLX_AGENT));
  asm volatile("s_waitcnt vmcnt(%0)" :: "n"(2 * FW + 4) : "memory");
  lds_inc<L_MISC + 80>(lds0);
  lds_wait(cnt, 5u);
  LOGF2(1);
  f32x4 acc[2][2] = {{Z4, Z4}, {Z4, Z4}};
  bf16x8 x[20][2];
#pragma unroll
  for (int ks = -3; ks < 20; ++ks) {
    if (ks >= 0 && ks + FW < 20) w_get(ks + FW);
#pragma unroll
    for (int t = 0; t < 2; ++t) {
      if (ks + 3 < 20) x[ks + 3][t] = *(const bf16x8*)(lds + L_XN + (t * 20 + ks + 3) * 1024 + lane * 16);
#pragma unroll
      for (int i = 0; i < 2 && ks >= 0; ++i) acc[i][t] = mfma32(W[i][ks < 0 ? 0 : ks], x[ks < 0 ? 0 : ks][t], acc[i][t]);
    }
    __builtin_amdgcn_sched_barrier(0);
  }
  u32x2 xp[2][2];
  PIN_V(acc[1][1]);
  LOGF2(2);
#pragma unroll
  for (int t = 0; t < 2; ++t) {
#pragma unroll
    for (int k = 0; k < 4; ++k) {
      const int i = k >> 1, q = k & 1;
      const unsigned r = pack2(acc[i][t][2 * q], acc[i][t][2 * q + 1]), x = xo[i][t][q];
      xp[i][t][q] = pack2(u2f(x << 16) + u2f(r << 16), u2f(x & 0xffff0000u) + u2f(r & 0xffff0000u));
      acc[i][t][2 * q] = u2f(xp[i][t][q] << 16); acc[i][t][2 * q + 1] = u2f(xp[i][t][q] & 0xffff0000u);
    }
    const f32x4 sm = acc[0][t] + acc[1][t];
    const float ml = x32sum(x16sum((sm[0] + sm[1]) + (sm[2] + sm[3]))) * (1.0f / 32.0f);
    float q = 0.f;
#pragma unroll
    for (int r = 0; r < 8; ++r) { const float d = acc[r >> 2][t][r & 3] - ml; q += d * d; }
    q = x32sum(x16sum(q));
    *(f32x2*)(red + (v * 32 + t * 16 + c) * 2) = (f32x2){ml, q};
  }
  lds_inc<L_MISC + 80>(lds0);
  if (v == 0) {
    lds_wait(cnt, 10u);
    LOGF2(3);
    float mw[5], ml = 0.f, m2 = 0.f;
#pragma unroll
    for (int i = 0; i < 5; ++i) { const f32x2 u = *(const f32x2*)(red + (i * 32 + (lane & 31)) * 2); mw[i] = u[0]; ml += u[0]; m2 += u[1]; }
    ml *= 0.2f;
#pragma unroll
    for (int i = 0; i < 5; ++i) m2 += 32.0f * (mw[i] - ml) * (mw[i] - ml);
    const unsigned xs = (unsigned)OFF_LNX + rb * 1024;
    if (lane < 32) SC1_STORE8S(((u32x2){f2u(ml), nz(f2u(m2))}), (cq * 32 + lane) * 8, xs);
    wait_cyc(cyc32() + FR_D);
    u32x2 x0, x1;
    for (unsigned spins = 0; spins < SPIN_CAP; ++spins) {
      x0 = __builtin_amdgcn_raw_buffer_load_b64(rs, lane * 8, xs, 16); x1 = __builtin_amdgcn_raw_buffer_load_b64(rs, 512 + lane * 8, xs, 16);
      if (wave_all(x0[1] != 0u && x1[1] != 0u)) break;
      asm volatile("" ::: "memory");
    }
    const float m0 = u2f(x0[0]), m1 = u2f(x1[0]);
    const float mean = x32sum(m0 + m1) * 0.25f;
    const float M2 = x32sum((u2f(x0[1]) + u2f(x1[1])) + 160.0f * ((m0 - mean) * (m0 - mean) + (m1 - mean) * (m1 - mean)));
    const float mb = rbf(mean), den = rbf(__builtin_sqrtf(rbf(rbf(M2 * (1.0f / DM)) + 1e-5f)));
    if (lane < 32) {
      fin[lane] = mb; fin[32 + lane] = 1.0f / den;
      if (cq == 0) __hip_atomic_store((GAS unsigned*)(a.ws + OFF_XNRM) + (rb * 32 + lane) * 4, f2u(1.02f * (M2 + DM * (mean - mb) * (mean - mb)) / (den * den)), RLX_AGENT);
    }
    lds_inc<L_MISC + 84>(lds0);
  }
  lds_wait(cnt + 1, 1u);
  LOGF2(4);
  const __amdgpu_buffer_rsrc_t rx = __builtin_amdgcn_make_buffer_rsrc(a.ws + OFF_XN, 0, (int)(OFF_QB - OFF_XN), 0x00020000);
  auto put = [&](const u32x2 p0, const u32x2 p1, unsigned so) {
    const auto r0 = __builtin_amdgcn_permlane16_swap(p0[0], p1[0], false, false), r1 = __builtin_amdgcn_permlane16_swap(p0[1], p1[1], false, false);
    __builtin_amdgcn_raw_buffer_store_b128((u32x4){r0[0], r1[0], r0[1], r1[1]}, rx, f16, so, 16);
  };
#pragma unroll
  for (int i = 0; i < 2; ++i) {
    u32x2 y[2];
#pragma unroll
    for (int t = 0; t < 2; ++t) {
      const float mb = fin[t * 16 + c], rden = fin[32 + t * 16 + c];
      y[t] = (u32x2){pack2(rbf(acc[i][t][0] - mb) * rden, rbf(acc[i][t][1] - mb) * rden), pack2(rbf(acc[i][t][2] - mb) * rden, rbf(acc[i][t][3] - mb) * rden)};
    }
    put(y[0], y[1], i * 512);
  }
  VM_DRAIN();
  lds_inc<L_MISC + 80>(lds0);
#pragma unroll
  for (int i = 0; i < 2; ++i) put(xp[i][0], xp[i][1], (unsigned)(OFF_XB - OFF_XN) + i * 512);
  if (v == 0) {
    lds_wait(cnt, 15u);
    if (rb < 4 && lane < 8) __hip_atomic_fetch_add((GAS unsigned*)(a.ws + OFF_RDY) + lane * 64, 1u, RLX_AGENT);
  }
  LOGF2(5);
}

__global__ __launch_bounds__(512, 2) void rec_kernel(RecArgs a) {
  __shared__ __attribute__((aligned(16))) char lds[L_BYTES];
  const int tid = __builtin_amdgcn_workitem_id_x(), wid = __builtin_amdgcn_readfirstlane(tid >> 6), w = wid & 3;
  const __amdgpu_buffer_rsrc_t rs = __builtin_amdgcn_make_buffer_rsrc(a.ws, 0, (int)COMM_BYTES, 0x00020000);
  if (tid < 32) ((float*)(lds + L_MISC))[tid] = (tid == 4 || tid == 5) ? 1.0f : 0.0f;
  BAR();
  if (a.layer && wid < 5) proj_front(a, lds, rs, wid);
  if (wid < 4) {
    unsigned tlast;
    if (w == 0) rec_waves<0>(a, lds, rs, tlast);
    else if (w == 1) rec_waves<1>(a, lds, rs, tlast);
    else if (w == 2) rec_waves<2>(a, lds, rs, tlast);
    else rec_waves<3>(a, lds, rs, tlast);
    drain(a, lds, rs, w, tlast);
  } else {
    unsigned vo[2][2];
    if (w == 0) token_waves<0>(a, lds, rs, vo);
    else if (w == 1) token_waves<1>(a, lds, rs, vo);
    else if (w == 2) token_waves<2>(a, lds, rs, vo);
    else token_waves<3>(a, lds, rs, vo);
    qkv_tail(a, lds, w, vo);
  }
}

#if defined(__HIP_DEVICE_COMPILE__)
#pragma clang attribute pop
#endif

#define ATT_P1 6
#define ATT_P2 10
#define ATT_P3 14
#define ATT_THR 6.0f
#define ATT_NT 4
constexpr int NT = ATT_NT, ATT_NQ = 8 / NT, KQ = S_ / NT;
#define ATT_LMAX 1.0e12f
#if defined(__HIP_DEVICE_COMPILE__)
#pragma clang attribute push(__attribute__((target("no-packed-fp32-ops"))), apply_to = function)
#endif
__global__ __launch_bounds__(512, 2) void attb_kernel(char* __restrict__ ws, u16* __restrict__ out) {
  const u16* const Qp = (const u16*)(ws + OFF_QB);
  const u16* const KV = (const u16*)(ws + OFF_KV);
  u16* const O = (u16*)(ws + OFF_OB);
  __shared__ __attribute__((aligned(16))) char wl[40960], ob[40960];
  __shared__ f32x4 mo_[8][NT - 1][3][64];
  const int tid = __builtin_amdgcn_workitem_id_x(), bid = __builtin_amdgcn_workgroup_id_x();
  __shared__ unsigned sink[64];
  own_code<ATT_CODE>(tid, sink);
  const int lane = tid & 63, wid = __builtin_amdgcn_readfirstlane(tid >> 6), kh = wid / ATT_NQ, r = lane & 31, hh = lane >> 5;
  const int hd = (bid & 7) * 4 + ((bid >> 3) & 3), qb = (bid >> 5) * ATT_NQ + wid % ATT_NQ;
  const int q0 = qb * 32 * NT;
  bf16x8 qa[NT]; u32x2 qb2[NT];
#pragma unroll
  for (int qt = 0; qt < NT; ++qt) {
    const u16* qr = Qp + ((size_t)hd * S_ + q0 + (qt + kh) % NT * 32 + r) * QST;
    { const u32x2* qp = (const u32x2*)(qr + 8 * hh); const u32x2 q0_ = qp[0], q1_ = qp[1]; qa[qt] = mk8(q0_[0], q0_[1], q1_[0], q1_[1]); }
    const u32x2 t = *(const u32x2*)(qr + 16);
    qb2[qt] = hh ? (u32x2){0u, 0u} : t;
  }
  const __amdgpu_buffer_rsrc_t rk = __builtin_amdgcn_make_buffer_rsrc((void*)(KV + ((size_t)hd * 64 + kh * (KQ / 32)) * (KVT / 2)), 0, KQ / 32 * KVT, 0x00020000);
  unsigned ok1 = lane * 16, ok2 = hh ? 1600 : 1024 + r * 8, ov = r > 20 ? 1u << 30 : 1280 + (hh * 21 + r) * 16;
  const __amdgpu_buffer_rsrc_t ro = __builtin_amdgcn_make_buffer_rsrc(O, 0, S_ * DM * 2, 0x00020000);
  unsigned oo[3];
#pragma unroll
  for (int i = 0; i < 3; ++i) {
    const int row = q0 + kh * 32 + r, k = hd * BD + 4 * hh + 8 * i;
    oo[i] = i == 2 && hh ? 1u << 30 : (unsigned)(((((row >> 4) * 20 + (k >> 5)) * 64 + ((k & 31) >> 3) * 16 + (row & 15)) * 8 + (k & 7)) * 2);
  }
  f32x16 o[NT];
  float m[NT];
  const f32x16 Z16 = {0.f, 0.f, 0.f, 0.f, 0.f, 0.f, 0.f, 0.f, 0.f, 0.f, 0.f, 0.f, 0.f, 0.f, 0.f, 0.f};
  int odd = 0xDDDD;
  asm volatile("" : "+v"(odd));
  struct KvT { u32x4 k1, v0, v1; u32x2 k2; };
  auto fetch_k = [&](KvT& x, int t) {
    x.k1 = __builtin_amdgcn_raw_buffer_load_b128(rk, ok1, imin(t, KQ / 32 - 1) * KVT, 0); x.k2 = __builtin_amdgcn_raw_buffer_load_b64(rk, ok2, imin(t, KQ / 32 - 1) * KVT, 0);
    __builtin_amdgcn_sched_barrier(0);
  };
  auto fetch_v = [&](KvT& x, int t) {
    x.v0 = __builtin_amdgcn_raw_buffer_load_b128(rk, ov, imin(t, KQ / 32 - 1) * KVT, 0); x.v1 = __builtin_amdgcn_raw_buffer_load_b128(rk, ov + 672, imin(t, KQ / 32 - 1) * KVT, 0);
    __builtin_amdgcn_sched_barrier(0);
  };
  auto fetch = [&](int t) { KvT x; __builtin_amdgcn_sched_barrier(0); fetch_k(x, t); fetch_v(x, t); return x; };
  auto qk1 = [&](const KvT& x, int qt) { return __builtin_amdgcn_mfma_f32_32x32x16_bf16(as8(x.k1), qa[qt], Z16, 0, 0, 0); };
  auto qk2 = [&](const KvT& x, int qt, f32x16 c) { return __builtin_amdgcn_mfma_f32_32x32x8bf16_1k(__builtin_bit_cast(v4i16_t, x.k2), __builtin_bit_cast(v4i16_t, qb2[qt]), c, 0, 0, 0); };
  auto pv = [&](const u32x4 v, const f32x16 p, int hi, f32x16 c) {
    const f32x8 ph = hi ? __builtin_shufflevector(p, p, 8, 9, 10, 11, 12, 13, 14, 15) : __builtin_shufflevector(p, p, 0, 1, 2, 3, 4, 5, 6, 7);
    return __builtin_amdgcn_smfmac_f32_32x32x32_bf16(__builtin_bit_cast(hbf16x8, v), __builtin_bit_cast(hbf16x16, ph), c, odd, 0, 0);
  };
  auto slow = [&]() {
    asm volatile("" : "+v"(ok1), "+v"(ok2), "+v"(ov) :: "memory");
#pragma unroll
    for (int qt = 0; qt < NT; ++qt) o[qt] = Z16;
#pragma unroll 1
    for (int t = 0; t < KQ / 32; ++t) {
      const KvT x = fetch(t);
#pragma unroll
      for (int qt = 0; qt < NT; ++qt) {
        f32x16 sa = qk2(x, qt, qk1(x, qt));
        float mx = fmx(fmx(sa[0], sa[1]), sa[2]);
#pragma unroll
        for (int i = 3; i < 15; i += 2) mx = fmx(fmx(mx, sa[i]), sa[i + 1]);
        mx = fmx(mx, sa[15]);
        if (t == 0 || !wave_all(mx <= ATT_THR)) {
          mx = x32mx(mx);
          if (t) mx = fmx(mx, 0.f);
          const float mn = m[qt] + mx;
          const unsigned h1 = f2bf(mn); const float r1 = mn - bf2f(h1);
          const unsigned h2 = f2bf(r1); const float r2 = r1 - bf2f(h2);
          const unsigned h3 = f2bf(r2);
          const float me = (bf2f(h1) + bf2f(h2)) + bf2f(h3);
          const float dm = me - m[qt];
          m[qt] = me;
          if (hh) qb2[qt] = (u32x2){(h1 | (h2 << 16)) ^ 0x80008000u, h3 ^ 0x8000u};
          const float alpha = t ? __builtin_amdgcn_exp2f(-dm) : 0.f;
#pragma unroll
          for (int i = 0; i < 16; ++i) { o[qt][i] *= alpha; sa[i] -= dm; }
        }
#pragma unroll
        for (int i = 0; i < 16; ++i) sa[i] = __builtin_amdgcn_exp2f(sa[i]);
        asm volatile("" : "+v"(sa));
        o[qt] = pv(x.v1, sa, 1, pv(x.v0, sa, 0, o[qt]));
        __builtin_amdgcn_sched_barrier(0);
      }
    }
  };
  f32x16 sc[2];
  auto phase = [&](int qt, const u32x4 vp, const KvT& xk, const u32x4 v0, auto mid) {
    const int u = qt & 1;
    auto ex = [&](int i0, int i1) {
#pragma unroll
      for (int i = i0; i < i1; ++i) sc[u][i] = __builtin_amdgcn_exp2f(sc[u][i]);
      asm volatile("" : "+v"(sc[u]));
      __builtin_amdgcn_sched_barrier(0);
    };
    o[(qt + NT - 1) % NT] = pv(vp, sc[1 - u], 1, o[(qt + NT - 1) % NT]);
    __builtin_amdgcn_sched_barrier(0);
    ex(0, 3);
    sc[1 - u] = qk1(xk, (qt + 1) % NT);
    __builtin_amdgcn_sched_barrier(0);
    ex(3, 6);
    sc[1 - u] = qk2(xk, (qt + 1) % NT, sc[1 - u]);
    __builtin_amdgcn_sched_barrier(0);
    ex(6, 10);
    o[qt] = pv(v0, sc[u], 0, o[qt]);
    __builtin_amdgcn_sched_barrier(0);
    mid();
    ex(10, 16);
  };
  static_assert(NT == 4, "projection");
  const int rb = (bid >> 5) * 8 + (bid & 7), cb = (bid >> 3) & 3, g = lane >> 4, c = lane & 15;
  const __amdgpu_buffer_rsrc_t rw = __builtin_amdgcn_make_buffer_rsrc(ws + OFF_WOBT + (size_t)cb * 204800, 0, out ? 204800 : 0, 0x00020000);
  const __amdgpu_buffer_rsrc_t rx = __builtin_amdgcn_make_buffer_rsrc(ws + OFF_XA + (size_t)rb * 40960, 0, out ? 40960 : 0, 0x00020000);
  bf16x8 W[20];
  u32x2 xo[3];
  auto w_get = [&](int t) {
    if (t >= 2 && t < 12) {
#pragma unroll
      for (int i = 0; i < 2; ++i) W[t < 2 || t >= 12 ? 0 : 2 * t - 4 + i] = as8(__builtin_amdgcn_raw_buffer_load_b128(rw, ok1 + i * 1024, (wid * 20 + 2 * t - 4) * 1024, 0));
    }
    if (t >= 4 && t < 9) {
      unsigned keep;
      asm volatile("s_mov_b32 %0, m0\n\ts_mov_b32 m0, %4\n\ts_nop 0\n\tbuffer_load_dwordx4 %1, %2, %3 offen lds\n\ts_mov_b32 m0, %0"
                   : "=&s"(keep) : "v"(ok1), "s"(rw), "s"((160 + wid * 5 + t - 4) * 1024), "s"((unsigned)(uintptr_t)(LAS char*)wl + (wid * 5 + t - 4) * 1024) : "memory");
    }
    if (t == 12) {
#pragma unroll
      for (int i = 0; i < 3; ++i) {
        const int col = cb * 160 + (i < 2 ? wid : 8 + ((wid >> 1) & 1)) * 16 + 4 * g, tt = (i ^ wid) & 1;
        xo[i] = __builtin_amdgcn_raw_buffer_load_b64(rx, (((tt * 20 + (col >> 5)) * 64 + ((col & 31) >> 3) * 16 + c) * 8 + (col & 7)) * 2, 0, 0);
      }
    }
    __builtin_amdgcn_sched_barrier(0);
  };
  if (ATT_P1) __builtin_amdgcn_s_setprio(3);
  {
    KvT xa = fetch(0), xb;
    fetch_k(xb, 1);
    xb.v1 = (u32x4){0u, 0u, 0u, 0u};
    sc[1] = Z16;
    asm volatile("" : "+v"(sc[1]), "+v"(xb.v1), "+v"(oo[0]), "+v"(oo[1]), "+v"(oo[2]));
#pragma unroll
    for (int qt = 0; qt < NT; ++qt) { m[qt] = 0.f; o[qt] = Z16; asm volatile("" : "+v"(o[qt])); }
    sc[0] = qk2(xa, 0, qk1(xa, 0));
    auto step = [&](KvT& x, KvT& y, int t) {
#pragma unroll
      for (int qt = 0; qt < NT; ++qt)
        phase(qt, qt ? x.v1 : y.v1, qt < NT - 1 ? x : y, x.v0, [&]() {
          if (qt == 0 && t + 1 < KQ / 32) fetch_v(y, t + 1);
          if (qt == NT - 2 && t + 2 < KQ / 32) fetch_k(x, t + 2);
          if (qt == NT - 1) w_get(t);
        });
    };
#pragma unroll
    for (int t = 0; t < KQ / 32; t += 2) {
      if (t == ATT_P1 * 4 / NT) __builtin_amdgcn_s_setprio(2);
      if (t == ATT_P2 * 4 / NT) __builtin_amdgcn_s_setprio(1);
      if (t == ATT_P3 * 4 / NT) __builtin_amdgcn_s_setprio(0);
      step(xa, xb, t);
      step(xb, xa, t + 1);
    }
    o[NT - 1] = pv(xb.v1, sc[1], 1, o[NT - 1]);
    float big = 0.f;
#pragma unroll
    for (int qt = 0; qt < NT; ++qt) {
      big = fmx(big, hh ? (o[qt][8] >= 1.0f / ATT_LMAX ? o[qt][8] * (3.0e38f / ATT_LMAX) : INFINITY) : __builtin_fabsf(o[qt][8]));
#pragma unroll
      for (int i = 0; i < 12; ++i) if (i != 8) big = fmx(big, __builtin_fabsf(o[qt][i]));
    }
    if (__builtin_expect(!wave_all(big <= 3.0e38f), 0)) slow();
  }
#pragma unroll
  for (int qt = 1; qt < NT; ++qt) {
    f32x4 (*mo)[64] = mo_[(qt + kh) % NT * ATT_NQ + wid % ATT_NQ][qt - 1];
#pragma unroll
    for (int k = 0; k < 3; ++k) mo[k][lane] = (f32x4){o[qt][4 * k], k == 2 && hh ? m[qt] : o[qt][4 * k + 1], o[qt][4 * k + 2], o[qt][4 * k + 3]};
  }
  asm volatile("s_waitcnt lgkmcnt(0)\n\ts_barrier" ::: "memory");
  {
    f32x2 lm[NT - 1];
    f32x4 u[NT - 1][3];
#pragma unroll
    for (int i = 0; i < NT - 1; ++i) {
      lm[i] = *(const f32x2*)&mo_[wid][i][2][lane | 32];
#pragma unroll
      for (int k = 0; k < 3; ++k) u[i][k] = mo_[wid][i][k][lane];
    }
    float mn = m[0];
#pragma unroll
    for (int i = 0; i < NT - 1; ++i) mn = fmx(mn, lm[i][1]);
    float a0 = __builtin_amdgcn_exp2f(m[0] - mn), a[NT - 1];
    float l = u2f(__builtin_amdgcn_permlane32_swap(f2u(o[0][8]), f2u(o[0][8]), false, false)[1]) * a0;
#pragma unroll
    for (int i = 0; i < NT - 1; ++i) { a[i] = __builtin_amdgcn_exp2f(lm[i][1] - mn); l += lm[i][0] * a[i]; }
    const float inv = __builtin_amdgcn_rcpf(l);
    a0 *= inv;
#pragma unroll
    for (int i = 0; i < NT - 1; ++i) a[i] *= inv;
#pragma unroll
    for (int k = 0; k < 3; ++k) {
      f32x4 v = (f32x4){o[0][4 * k], o[0][4 * k + 1], o[0][4 * k + 2], o[0][4 * k + 3]} * a0;
#pragma unroll
      for (int i = 0; i < NT - 1; ++i) v += u[i][k] * a[i];
      __builtin_amdgcn_raw_buffer_store_b64((u32x2){pack2(v[0], v[1]), pack2(v[2], v[3])}, ro, oo[k], 0, 16);
    }
  }
  if (!out) return;
  asm volatile("s_waitcnt vmcnt(0)\n\ts_barrier" ::: "memory");
  GAS unsigned* const cnt = (GAS unsigned*)(ws + OFF_FIN) + (bid >> 5) * 512;
  if (wid == 0) {
    if (lane < 8) __hip_atomic_fetch_add(cnt + lane * 64, 1u, RLX_AGENT);
  }
  bf16x8 W2[20];
  const __amdgpu_buffer_rsrc_t rf = __builtin_amdgcn_make_buffer_rsrc(out + (size_t)rb * 32 * DM, 0, 32 * DM * 2, 0x00020000);
  unsigned xa0 = (wid & 1) * 20480 + lane * 16, xa1 = 20480 - (wid & 1) * 20480 + lane * 16;
#pragma unroll
  for (int i = 0; i < 3; ++i) oo[i] = (unsigned)(((((i ^ wid) & 1) * 16 + c) * DM + cb * 160 + (i < 2 ? wid : 8 + ((wid >> 1) & 1)) * 16 + 4 * g) * 2);
  asm volatile("" : "+v"(xa0), "+v"(xa1), "+v"(oo[0]), "+v"(oo[1]), "+v"(oo[2]));
#pragma unroll
  for (int ks = 0; ks < 20; ++ks) W2[ks] = *(const bf16x8*)(wl + (((wid >> 1) & 1) * 20 + ks) * 1024 + lane * 16);
  __builtin_amdgcn_sched_barrier(0);
#pragma unroll
  for (int ks = 0; ks < 20; ks += 4) asm volatile("" : "+v"(W2[ks]), "+v"(W2[ks + 1]), "+v"(W2[ks + 2]), "+v"(W2[ks + 3]));
  if (wid == 0) {
    __builtin_amdgcn_s_sleep(FIN_D);
    for (unsigned spins = 0; __hip_atomic_load(cnt + (bid & 7) * 64, RLX_AGENT) < 32u && spins < SPIN_CAP; ++spins) __builtin_amdgcn_s_sleep(FIN_SL);
  }
  asm volatile("s_barrier" ::: "memory");
  x_fetch_n<5>(O + (size_t)rb * 32 * DM, (unsigned)(uintptr_t)(LAS char*)ob - L_XN, 0, wid * 5, ok1);
  asm volatile("s_waitcnt vmcnt(0)\n\ts_barrier" ::: "memory");
  auto fin = [&](auto N) {
    f32x4 acc[3] = {{0.f, 0.f, 0.f, 0.f}, {0.f, 0.f, 0.f, 0.f}, {0.f, 0.f, 0.f, 0.f}};
    bf16x8 x[20][2];
#pragma unroll
    for (int ks = -4; ks < 20; ++ks) {
      if (ks + 4 < 20) { x[ks + 4][0] = *(const bf16x8*)(ob + xa0 + (ks + 4) * 1024); x[ks + 4][1] = *(const bf16x8*)(ob + xa1 + (ks + 4) * 1024); }
      if (ks >= 0) {
        acc[0] = mfma32(W[ks < 0 ? 0 : ks], x[ks < 0 ? 0 : ks][0], acc[0]); acc[1] = mfma32(W[ks < 0 ? 0 : ks], x[ks < 0 ? 0 : ks][1], acc[1]);
        if (N == 3) acc[2] = mfma32(W2[ks < 0 ? 0 : ks], x[ks < 0 ? 0 : ks][0], acc[2]);
      }
      __builtin_amdgcn_sched_barrier(0);
    }
#pragma unroll
    for (int i = 0; i < N; ++i) {
      const unsigned r01 = pack2(acc[i][0], acc[i][1]), r23 = pack2(acc[i][2], acc[i][3]);
      const unsigned p01 = pack2(u2f(xo[i][0] << 16) + u2f(r01 << 16), u2f(xo[i][0] & 0xffff0000u) + u2f(r01 & 0xffff0000u));
      const unsigned p23 = pack2(u2f(xo[i][1] << 16) + u2f(r23 << 16), u2f(xo[i][1] & 0xffff0000u) + u2f(r23 & 0xffff0000u));
      __builtin_amdgcn_raw_buffer_store_b64((u32x2){p01, p23}, rf, oo[i], 0, 0);
    }
  };
  if (wid >= 4) fin(std::integral_constant<int, 3>{}); else fin(std::integral_constant<int, 2>{});
}
#if defined(__HIP_DEVICE_COMPILE__)
#pragma clang attribute pop
#endif

extern "C" void kernel_launch(void* const* d_in, const int* in_sizes, int n_in, void* d_out, int out_size, void* d_ws,
                              size_t ws_size, hipStream_t stream) {
  const u16* A_x = (const u16*)d_in[0];
  const u16* A_mem0 = (const u16*)d_in[1];
  const u16* A_Wq = (const u16*)d_in[2];
  const u16* A_Wk = (const u16*)d_in[3];
  const u16* A_Wv = (const u16*)d_in[4];
  const u16* A_Wo = (const u16*)d_in[5];
  const u16* B_wqkv = (const u16*)d_in[8];
  const u16* B_wo = (const u16*)d_in[9];
  char* ws = (char*)(((uintptr_t)d_ws + ((size_t)WS_OFF << 20) + WS_AL - 1) & ~(uintptr_t)(WS_AL - 1));
  u16* xa = (u16*)(ws + OFF_XA);

  prologue_kernel<<<256, 640, 0, stream>>>(ProArgs{A_x, A_Wq, A_Wk, A_Wv, A_Wo, B_wqkv, B_wo, ws});
  for (int layer = 0; layer < 2; ++layer) {
    rec_kernel<<<256, 512, 0, stream>>>(RecArgs{A_mem0, A_Wq, A_Wk, (const u16*)(ws + OFF_XB), xa, ws, (unsigned)layer, 0u});
    attb_kernel<<<BH * 8, 512, 0, stream>>>(ws, layer ? (u16*)d_out : nullptr);
  }
}
```

```cpp
#include <hip/hip_runtime.h>
__device__ unsigned char g_fc[2 * 4 * 64];
#define FC(k) do { g_fc[(a.layer * 4 + (k)) * 64 + s] = 1; } while (0)
__global__ void spin_kernel(int L, int k, int lo, int hi) {
  unsigned t0 = (unsigned)__builtin_amdgcn_s_memrealtime(), n = 0;
  if (lo >= 0) for (int s = lo; s <= hi; ++s) n += g_fc[(L * 4 + k) * 64 + s]; else for (int s = hi; s >= -lo; --s) if (g_fc[(L * 4 + k) * 64 + s]) n = s;
  for (int i = 0; i < 512; ++i) g_fc[i] = 0;
  while ((unsigned)__builtin_amdgcn_s_memrealtime() - t0 < 1000u + n * 2000u) { }
}
#include <hip/hip_runtime.h>
#include <hip/hip_bf16.h>
#include <stdint.h>
#include <type_traits>

typedef unsigned short u16;
typedef unsigned long long u64;
typedef __attribute__((ext_vector_type(8))) short bf16x8;
typedef __attribute__((ext_vector_type(16))) short bf16x16;
typedef __attribute__((ext_vector_type(4))) float f32x4;
typedef __attribute__((ext_vector_type(2))) float f32x2;
typedef __attribute__((ext_vector_type(16))) float f32x16;
typedef __attribute__((ext_vector_type(8))) float f32x8;
typedef __attribute__((ext_vector_type(4))) unsigned u32x4;
typedef __attribute__((ext_vector_type(2))) unsigned u32x2;
typedef __attribute__((ext_vector_type(3))) unsigned u32x3;
typedef __attribute__((ext_vector_type(8))) unsigned u32x8;
typedef __attribute__((ext_vector_type(16))) unsigned u32x16;
typedef __attribute__((ext_vector_type(32))) unsigned u32x32;
#define GAS __attribute__((address_space(1)))
#define LAS __attribute__((address_space(3)))
typedef GAS unsigned long long gu64;
#define BAR() do { __builtin_amdgcn_s_waitcnt(0xC07F); asm volatile("s_waitcnt lgkmcnt(0)\n\ts_barrier" ::: "memory"); } while (0)

constexpr int S_ = 2048, DM = 640;
constexpr int NH = 32, DH = 192, HD = NH * DH;
constexpr int MEM = 8, SEG = 32, NSEG = S_ / SEG;
constexpr int BH = 32, BD = 20;

constexpr size_t al256(size_t x) { return (x + 255) & ~(size_t)255; }
constexpr int LAG3 = 2, LAG4 = 3, LAG5 = 6;
#define SR_ 8
constexpr int SR = SR_;
constexpr unsigned SCT_STEP = 131072, OFF_SCM = SR * SCT_STEP, SCM_STEP = 32768;
constexpr int ACC_NB = 192;
constexpr size_t ACC_STEP = 65536;
constexpr unsigned ACC_TS = ACC_NB * 256;
constexpr size_t ACC_LAYER = SR * ACC_STEP;
constexpr size_t OFF_ACC = 3 * 524288;
static_assert(SR == 8, "regions");
__device__ __forceinline__ unsigned ts_piece(int i) { return ACC_TS + (unsigned)(i < 128 ? (i >> 1) * 256 + (i & 1) * 16 : 128 + (i & 1) * 8192); }
constexpr size_t OFF_LNX = OFF_SCM + SR * SCM_STEP;
constexpr size_t OFF_RDY = OFF_LNX + 65536;
constexpr size_t OFF_FIN = OFF_RDY + 2048;
constexpr size_t PG = 2097152;
#define RING_ 4
constexpr int RING = RING_;
constexpr size_t TS_SLOT = (size_t)NH * 8 * 2560, PT_SLOT = (size_t)NH * 2560, OP_SLOT = (size_t)256 * 2560;
__device__ __forceinline__ unsigned ts_tok(int h, int j) { return (unsigned)((h & 15) * 32768 + (j >> 2) * 16384 + (j & 3) * 2048); }
__device__ __forceinline__ unsigned ts_mem(int h, int j) { return (unsigned)(524288 + ((h & 15) >> 1) * 16384 + (h & 1) * 4096 + j * 512); }
__device__ __forceinline__ unsigned pt_tok(int h) { return (unsigned)(((h & 15) >> 2) * 16384 + (h & 3) * 2048); }
__device__ __forceinline__ unsigned pt_mem(int h) { return (unsigned)(65536 + (h & 15) * 512); }
constexpr size_t OFF_X = 2 * PG + TS_SLOT + 3 * PT_SLOT, X_BYTES = 7 * OP_SLOT + 4 * PT_SLOT;
constexpr size_t OFF_OP = OFF_X + TS_SLOT + 4 * PT_SLOT;
constexpr size_t COMM_BYTES = OFF_X + X_BYTES + RING * OP_SLOT;
__device__ __forceinline__ unsigned near_half(unsigned ck, unsigned o) { return o + ((ck >> (o >= 2 * PG ? 8 : 4)) & 8192u); }
__device__ __forceinline__ unsigned ts_slot(unsigned ck, int sl) {
  return near_half(ck, sl == NSEG - 1 ? (unsigned)OFF_X : ((sl & 3) == 3 ? (unsigned)(2 * PG) : (unsigned)PG + (unsigned)(sl & 3) * (unsigned)TS_SLOT));
}
__device__ __forceinline__ unsigned pt_slot(unsigned ck, int sl) {
  return near_half(ck, sl >= NSEG - 4 ? (unsigned)(OFF_X + TS_SLOT) + (unsigned)(sl - (NSEG - 4)) * (unsigned)PT_SLOT
                                      : ((sl & 3) == 0 ? (unsigned)(PG + 3 * TS_SLOT) : (unsigned)(2 * PG + TS_SLOT - PT_SLOT) + (unsigned)(sl & 3) * (unsigned)PT_SLOT));
}
__device__ __forceinline__ unsigned op_slot(int sl) { return (unsigned)OFF_OP + (unsigned)(sl >= NSEG - 6 ? sl - (NSEG - 6) : 6 + sl % RING) * (unsigned)OP_SLOT; }
static_assert(RING == 4 && OFF_OP <= 3 * PG && OFF_FIN + 16384 <= OFF_ACC && OFF_ACC + ACC_LAYER == PG && TS_SLOT % 16384 == 0 && PT_SLOT % 16384 == 0, "pages");
static_assert(TS_SLOT == OP_SLOT && RING >= 4 && NSEG % SR == 0 && SR >= 5 && X_BYTES % 4096 == 0 && X_BYTES <= 256 * 20 * 1024, "slots");
constexpr size_t OFF_WB = (COMM_BYTES + 16383) / 16384 * 16384, WB_BYTES = (size_t)256 * 200 * 1024;
__device__ __forceinline__ unsigned wb_off(int F) { const unsigned o = (unsigned)((F >> 3) * 16384 + (F & 7) * 1024); return o | ((((unsigned)OFF_WB + o) >> 8) & 8192u); }
constexpr size_t OFF_PHI = OFF_WB + WB_BYTES;
constexpr size_t OFF_BNRM = OFF_PHI + 1024;
constexpr size_t OFF_LAT = OFF_BNRM + 1024;
constexpr size_t OFF_XNRM = OFF_LAT + 2048;
constexpr size_t OFF_WQKVBT = OFF_XNRM + al256(S_ * 16);
constexpr size_t OFF_WOBT = OFF_WQKVBT + al256((size_t)3 * DM * DM * 2);
constexpr size_t OFF_XN = OFF_WOBT + al256((size_t)DM * DM * 2);
constexpr int QST = BD;
constexpr size_t OFF_XA = OFF_XN + al256((size_t)S_ * DM * 2);
constexpr size_t OFF_XB = OFF_XA + al256((size_t)S_ * DM * 2);
constexpr size_t OFF_QB = OFF_XB + al256((size_t)S_ * DM * 2);
constexpr int KVT = 2624;
constexpr size_t OFF_KV = OFF_QB + al256((size_t)BH * S_ * QST * 2);
constexpr size_t OFF_OB = OFF_KV + al256((size_t)BH * 64 * KVT);
constexpr size_t OFF_DBG = OFF_OB + al256((size_t)S_ * DM * 2);
constexpr size_t WS_TOTAL = OFF_DBG + (size_t)2 * 64 * 8 * DM * 2;
#define WS_AL 2097152
#define WS_OFF 0
static_assert(WS_TOTAL + WS_AL + ((size_t)WS_OFF << 20) <= ((size_t)256 << 20), "workspace budget");

#if defined(__HIP_DEVICE_COMPILE__)
#pragma clang attribute push(__attribute__((target("no-packed-fp32-ops"))), apply_to = function)
#endif
__device__ __forceinline__ float u2f(unsigned v) { return __builtin_bit_cast(float, v); }
__device__ __forceinline__ unsigned f2u(float v) { return __builtin_bit_cast(unsigned, v); }
__device__ __forceinline__ float bf2f(unsigned v) { return __builtin_bit_cast(float, v << 16); }
__device__ __forceinline__ unsigned f2bf(float f) { __bf16 b = (__bf16)f; return (unsigned)__builtin_bit_cast(u16, b); }
__device__ __forceinline__ float rbf(float f) { return bf2f(f2bf(f)); }
typedef __attribute__((ext_vector_type(2))) __bf16 hbf16x2;
__device__ __forceinline__ unsigned pack2(float lo, float hi) { f32x2 v = {lo, hi}; hbf16x2 b = __builtin_convertvector(v, hbf16x2); return __builtin_bit_cast(unsigned, b); }
__device__ __forceinline__ f32x4 mfma32(bf16x8 a, bf16x8 b, f32x4 c) { return __builtin_amdgcn_mfma_f32_16x16x32_bf16(a, b, c, 0, 0, 0); }
typedef __attribute__((ext_vector_type(8))) __bf16 hbf16x8;
typedef __attribute__((ext_vector_type(16))) __bf16 hbf16x16;
__device__ __forceinline__ f32x4 smfma64(bf16x8 a, bf16x16 b, f32x4 c, int idx) {
  return __builtin_amdgcn_smfmac_f32_16x16x64_bf16(__builtin_bit_cast(hbf16x8, a), __builtin_bit_cast(hbf16x16, b), c, idx, 0, 0);
}
__device__ __forceinline__ bf16x16 cat16(bf16x8 a, bf16x8 b) { return __builtin_shufflevector(a, b, 0, 1, 2, 3, 4, 5, 6, 7, 8, 9, 10, 11, 12, 13, 14, 15); }
__device__ __forceinline__ bf16x8 half8(bf16x16 v, int hi) { return hi ? __builtin_shufflevector(v, v, 8, 9, 10, 11, 12, 13, 14, 15) : __builtin_shufflevector(v, v, 0, 1, 2, 3, 4, 5, 6, 7); }
__device__ __forceinline__ bf16x8 mk8(unsigned a, unsigned b, unsigned c, unsigned d) { u32x4 v = {a, b, c, d}; return __builtin_bit_cast(bf16x8, v); }
__device__ __forceinline__ unsigned dpp_ror8(unsigned v) { return (unsigned)__builtin_amdgcn_mov_dpp((int)v, 0x128, 0xf, 0xf, true); }
__device__ __forceinline__ float fmx(float a, float b) { return __builtin_elementwise_maximum(a, b); }

__device__ __forceinline__ int imax(int a, int b) { return a > b ? a : b; }
__device__ __forceinline__ int imin(int a, int b) { return a < b ? a : b; }
__device__ __forceinline__ unsigned umin(unsigned a, unsigned b) { return a < b ? a : b; }
__device__ __forceinline__ unsigned umax(unsigned a, unsigned b) { return a > b ? a : b; }
__device__ __forceinline__ bool wave_all(bool p) { return __builtin_amdgcn_ballot_w64(!p) == 0ull; }
__device__ __forceinline__ float x16mx(float x) { unsigned u = __builtin_bit_cast(unsigned, x); auto r = __builtin_amdgcn_permlane16_swap(u, u, false, false); return fmx(u2f(r[0]), u2f(r[1])); }
__device__ __forceinline__ float x32mx(float x) { unsigned u = __builtin_bit_cast(unsigned, x); auto r = __builtin_amdgcn_permlane32_swap(u, u, false, false); return fmx(u2f(r[0]), u2f(r[1])); }
__device__ __forceinline__ float x16sum(float x) { unsigned u = __builtin_bit_cast(unsigned, x); auto r = __builtin_amdgcn_permlane16_swap(u, u, false, false); return u2f(r[0]) + u2f(r[1]); }
__device__ __forceinline__ float x32sum(float x) { unsigned u = __builtin_bit_cast(unsigned, x); auto r = __builtin_amdgcn_permlane32_swap(u, u, false, false); return u2f(r[0]) + u2f(r[1]); }
template <int CTRL> __device__ __forceinline__ float dppf(float v) { return u2f((unsigned)__builtin_amdgcn_mov_dpp((int)f2u(v), CTRL, 0xf, 0xf, true)); }
#define OWN_PF 1
template <int BYTES> __device__ __forceinline__ void own_code(int tid, unsigned* sink) {
  if (!OWN_PF || __builtin_amdgcn_readfirstlane(tid >> 6) * 4096 >= BYTES) return;
  const char* pc; unsigned keep;
  asm volatile("s_getpc_b64 %0" : "=s"(pc));
  asm volatile("s_mov_b32 %0, m0\n\ts_mov_b32 m0, %3\n\ts_nop 0\n\tglobal_load_lds_dword %1, %2\n\ts_mov_b32 m0, %0"
               : "=&s"(keep) : "v"(tid * 64 < BYTES ? tid * 64 : 0), "s"(pc), "s"((unsigned)(uintptr_t)(LAS unsigned*)sink) : "memory");
}
constexpr int DPP_XOR1 = 0xB1, DPP_XOR2 = 0x4E, DPP_ROR4 = 0x124, DPP_ROR8 = 0x128;
#if defined(__HIP_DEVICE_COMPILE__)
#pragma clang attribute pop
#endif

typedef short v4i16_t __attribute__((ext_vector_type(4)));
__device__ __forceinline__ bf16x8 tr_frag(const char* lo_, const char* hi_) {
  const v4i16_t lo = __builtin_amdgcn_ds_read_tr16_b64_v4i16((LAS v4i16_t*)lo_), hi = __builtin_amdgcn_ds_read_tr16_b64_v4i16((LAS v4i16_t*)hi_);
  return (bf16x8){lo[0], lo[1], lo[2], lo[3], hi[0], hi[1], hi[2], hi[3]};
}
__device__ __forceinline__ int rec_wg(int gp, int m) { return (((gp & 1) * 16 + m) << 3) | (gp >> 1); }
__device__ __forceinline__ float wsum(float v) {
  v += dppf<DPP_XOR1>(v); v += dppf<DPP_XOR2>(v); v += dppf<DPP_ROR4>(v); v += dppf<DPP_ROR8>(v);
  return x32sum(x16sum(v));
}
__device__ __forceinline__ float ln_row(int lane, u32x4 (&xo)[2]) {
  const bool two = lane < 16;
  float v[16];
#pragma unroll
  for (int hf = 0; hf < 2; ++hf)
#pragma unroll
    for (int i = 0; i < 4; ++i) { const unsigned u = xo[hf][i]; v[hf * 8 + 2 * i] = bf2f(u & 0xffff); v[hf * 8 + 2 * i + 1] = bf2f(u >> 16); }
  float s0 = 0.f, s1 = 0.f;
#pragma unroll
  for (int i = 0; i < 8; ++i) { s0 += v[i]; s1 += v[8 + i]; }
  const float mean = wsum(two ? s0 + s1 : s0) * (1.0f / DM);
  s0 = s1 = 0.f;
#pragma unroll
  for (int i = 0; i < 8; ++i) { const float d0 = v[i] - mean, d1 = v[8 + i] - mean; s0 += d0 * d0; s1 += d1 * d1; }
  const float var = rbf(wsum(two ? s0 + s1 : s0) * (1.0f / DM));
  const float mb = rbf(mean);
  const float rden = 1.0f / rbf(sqrtf(rbf(var + 1e-5f)));
  float n2[2] = {0.f, 0.f};
#pragma unroll
  for (int hf = 0; hf < 2; ++hf) {
    float y[8];
#pragma unroll
    for (int i = 0; i < 8; ++i) { y[i] = rbf(rbf(v[hf * 8 + i] - mb) * rden); n2[hf] += y[i] * y[i]; }
    xo[hf] = (u32x4){pack2(y[0], y[1]), pack2(y[2], y[3]), pack2(y[4], y[5]), pack2(y[6], y[7])};
  }
  return wsum(two ? n2[0] + n2[1] : n2[0]);
}

#define PROT(k)
#define WARM_CODE 1
#define B_AUX 16
#define X_AUX 16
#define T_AUX 16
#define ATT_CODE 15000
#define FIN_D 4
#define FIN_SL 2
#define WB_NEAR 1
#define PF_POS 0
#define PRB_N0 3
#define PRB_N1 0
#define PRB_LOG(i, v)
#define PLD(p) __builtin_nontemporal_load((const u32x4*)(p))
struct ProArgs { const u16* x; const u16* wq; const u16* wk; const u16* wv; const u16* wo; const u16* wqkvb; const u16* wob; char* ws; };
__global__ __launch_bounds__(640, 3) void prologue_kernel(ProArgs a) {
  __shared__ __attribute__((aligned(16))) char img[192 * 320];
  __shared__ __attribute__((aligned(16))) char slots[10 * 6144];
  __shared__ float red[10][160];
  __shared__ unsigned latw[10];
  __shared__ u32x4 xst[2][640];
  const int b = blockIdx.x, tid = threadIdx.x, lane = tid & 63, v = __builtin_amdgcn_readfirstlane(tid >> 6), c = lane & 15, g = lane >> 4;
  const int h = (b & 7) * 4 + ((b >> 3) & 3), dh = (b >> 5) & 1, cq = b >> 6, ks = dh * 10 + v;
  char* const slot = slots + v * 6144;
  PROT(0);
  __shared__ unsigned sink[64];
  own_code<9600>(tid, sink);
  const unsigned zo = v < 8 ? (unsigned)(((b >> 6) * 512 + ((b & 7) * 2 + (v >> 2)) * 32 + ((b >> 3) & 7) * 4 + (v & 3)) * 1024) : (unsigned)(((v - 7) * 2048 + (b & 7) * 32 + ((b >> 3) & 7) * 4 + (b >> 6)) * 1024);
  __builtin_amdgcn_raw_buffer_store_b128((u32x4){0u, 0u, 0u, 0u}, __builtin_amdgcn_make_buffer_rsrc(a.ws, 0, (int)(3 * PG), 0x00020000), zo + lane * 16, 0, 16);
  __builtin_amdgcn_sched_barrier(0);
  u32x4 wa[2][6], wb[6], tr[4], xo[2];
  const int xrow = b * 8 + (v & 7);
  xo[0] = *(const u32x4*)(a.x + (size_t)xrow * DM + lane * 8);
  xo[1] = *(const u32x4*)(a.x + (size_t)xrow * DM + 512 + c * 8);
  const int job = imin(b + 256 * (9 - v), 800);
  const int jn = job < 600 ? job / 20 : (job % 200) / 20, jk = job % 20;
  {
    const u16* src = job < 600 ? a.wqkvb + (size_t)(32 * jk) * 3 * DM : a.wob + (size_t)(32 * jk) * DM;
#pragma unroll
    for (int i = 0; i < 4; ++i) tr[i] = *(const u32x4*)(src + (size_t)(i * 8 + (lane >> 3)) * (job < 600 ? 3 * DM : DM) + jn * 64 + (lane & 7) * 8);
  }
#pragma unroll
  for (int i = 0; i < 6; ++i) {
    const int idx = (v * 6 + i) * 64 + lane, row = idx / 20;
    wb[i] = PLD(a.wo + (size_t)(h * DH + row) * DM + cq * 160 + (idx - row * 20) * 8);
  }
#pragma unroll
  for (int i = 0; i < 6; ++i) {
    const int idx = i * 64 + lane, r = idx / 24;
#pragma unroll
    for (int u = 0; u < 2; ++u) wa[u][i] = PLD(a.wv + (size_t)(32 * ks + 8 * (r >> 2) + (r & 3) + 4 * u) * HD + h * DH + (idx - r * 24) * 8);
  }
  unsigned pf[2] = {0u, 0u};
  auto prefetch = [&]() {
#pragma unroll
    for (int i = 0; i < 2; ++i) pf[i] = *(const unsigned*)((i ? a.wq : a.wk) + ((size_t)(b * 10 + v) * 48 + imin(lane, 47)) * 32);
  };
  if (PF_POS == 0) prefetch();
  __builtin_amdgcn_sched_barrier(0);
  unsigned lat = ~0u;
  auto probe = [&](int n0, int n) {
    const char* p = a.ws + zo;
#pragma unroll 1
    for (int i = n0; i < n0 + n; ++i) {
      u64 t0, t1; unsigned d;
      asm volatile("s_memtime %0\n\ts_waitcnt lgkmcnt(0)\n\ts_load_dword %2, %3, %4\n\ts_waitcnt lgkmcnt(0)\n\ts_memtime %1\n\ts_waitcnt lgkmcnt(0)"
                   : "=&s"(t0), "=&s"(t1), "=&s"(d) : "s"(p), "s"((i & 3) * 256 + (i >> 2) * 128) : "memory");
      PRB_LOG(i, (unsigned)t1 - (unsigned)t0);
      lat = umin(lat, (unsigned)t1 - (unsigned)t0);
    }
  };
  asm volatile("s_waitcnt vmcnt(26)" ::: "memory");
  probe(0, PRB_N0);
  latw[v] = lat;
  __builtin_amdgcn_sched_barrier(0);
  auto xput = [&](int k) {
    if (v < 8) { xst[k][lane * 8 + (v ^ (lane & 7))] = xo[0]; if (lane < 16) xst[k][(64 + lane) * 8 + (v ^ (lane & 7))] = xo[1]; }
  };
  xput(0);
  const float xn2 = u2f(__builtin_amdgcn_readfirstlane(f2u(ln_row(lane, xo))));
  xput(1);
#pragma unroll
  for (int i = 0; i < 4; ++i) *(u32x4*)(slot + (i * 8 + (lane >> 3)) * 144 + (lane & 7) * 16) = tr[i];
  __builtin_amdgcn_fence(__ATOMIC_RELEASE, "wavefront"); __builtin_amdgcn_wave_barrier();
  bf16x8 tf[4];
#pragma unroll
  for (int t = 0; t < 4; ++t) {
    const char* bp = slot + (8 * g + (c >> 2)) * 144 + t * 32 + (c & 3) * 8;
    tf[t] = tr_frag(bp, bp + 4 * 144);
  }
  __builtin_amdgcn_fence(__ATOMIC_RELEASE, "wavefront"); __builtin_amdgcn_wave_barrier();
  __builtin_amdgcn_sched_barrier(0);
#pragma unroll
  for (int i = 0; i < 6; ++i) {
    const int idx = (v * 6 + i) * 64 + lane, row = idx / 20;
    *(u32x4*)(img + row * 320 + (((idx - row * 20) ^ ((row >> 2) & 2)) * 16)) = wb[i];
  }
  bf16x8 A[2][6];
#pragma unroll
  for (int u = 0; u < 2; ++u) {
#pragma unroll
    for (int i = 0; i < 6; ++i) {
      const int idx = i * 64 + lane, r = idx / 24;
      *(u32x4*)(slot + r * 384 + (((idx - r * 24) ^ (r >> 1)) * 16)) = wa[u][i];
    }
    __builtin_amdgcn_fence(__ATOMIC_RELEASE, "wavefront"); __builtin_amdgcn_wave_barrier();
#pragma unroll
    for (int e = 0; e < 6; ++e) A[u][e] = *(const bf16x8*)(slot + c * 384 + (((4 * e + g) ^ (c >> 1)) * 16));
    __builtin_amdgcn_fence(__ATOMIC_RELEASE, "wavefront"); __builtin_amdgcn_wave_barrier();
  }
  BAR();
  PROT(1);
  if (PF_POS == 0) asm volatile("" :: "v"(pf[0]), "v"(pf[1]));
  if (PF_POS == 1) prefetch();
  {
    const unsigned xf = (unsigned)((b >> 1) * 20480 + (tid >> 3) * 256 + (b & 1) * 128 + (tid & 7) * 16);
    const __amdgpu_buffer_rsrc_t rx = __builtin_amdgcn_make_buffer_rsrc(a.ws + OFF_XN, 0, (int)(OFF_QB - OFF_XN), 0x00020000);
    __builtin_amdgcn_raw_buffer_store_b128(xst[1][tid ^ ((tid >> 3) & 7)], rx, xf, 0, X_AUX);
    __builtin_amdgcn_raw_buffer_store_b128(xst[0][tid ^ ((tid >> 3) & 7)], rx, xf + (unsigned)(OFF_XB - OFF_XN), 0, X_AUX);
    if (v < 8 && lane < 4) ((float*)(a.ws + OFF_XNRM))[xrow * 4 + lane] = lane == 0 ? xn2 : 0.f;
  }
  if (job < 800) {
    const __amdgpu_buffer_rsrc_t rt = __builtin_amdgcn_make_buffer_rsrc(a.ws + (job < 600 ? OFF_WQKVBT : OFF_WOBT) + ((size_t)jn * 80 + jk) * 1024, 0, 81920, 0x00020000);
#pragma unroll
    for (int t = 0; t < 4; ++t) __builtin_amdgcn_raw_buffer_store_b128(__builtin_bit_cast(u32x4, tf[t]), rt, lane * 16 + t * 20480, 0, T_AUX);
  }
  const int gp = h >> 1, hh = h & 1;
  const __amdgpu_buffer_rsrc_t rwb = __builtin_amdgcn_make_buffer_rsrc(a.ws + OFF_WB, 0, (int)WB_BYTES, 0x00020000);
  const unsigned phi = WB_NEAR ? ((((unsigned)b >> 4) & 1u) ^ (unsigned)(latw[8] < latw[9]) ^ (WB_NEAR - 1)) << 13 : (unsigned)(b & 32) << 8;
  if (tid == 0) ((unsigned*)(a.ws + OFF_PHI))[b] = phi;
#pragma unroll
  for (int n = 0; n < 10; ++n) {
    const int off = n * 32 + 8 * (c & 3), T = cq * 10 + n;
    const char* bp = img + (8 * g + (c >> 2)) * 320 + (off ^ ((g & 1) << 5));
    f32x4 ac[2] = {{0.f, 0.f, 0.f, 0.f}, {0.f, 0.f, 0.f, 0.f}};
#pragma unroll
    for (int e = 0; e < 6; ++e) {
      const bf16x8 bf = tr_frag(bp + e * 32 * 320, bp + (e * 32 + 4) * 320);
      ac[0] = mfma32(A[0][e], bf, ac[0]); ac[1] = mfma32(A[1][e], bf, ac[1]);
    }
    const u32x4 o = {pack2(ac[0][0], ac[0][1]), pack2(ac[0][2], ac[0][3]), pack2(ac[1][0], ac[1][1]), pack2(ac[1][2], ac[1][3])};
    const unsigned oo = (T < 32 ? wb_off(rec_wg(gp, T & 15) * 100 + (hh * 3 + (T >> 4)) * 20 + ks) + (unsigned)(lane * 16)
                                : wb_off(rec_wg(gp, 2 * (T - 32) + (c >> 3)) * 100 + 40 + ks) + (unsigned)((g * 16 + hh * 8 + (c & 7)) * 16)) ^ phi;
    __builtin_amdgcn_raw_buffer_store_b128(o, rwb, oo, 0, B_AUX);
    float n2 = 0.f;
#pragma unroll
    for (int r = 0; r < 4; ++r) n2 += ac[0][r] * ac[0][r] + ac[1][r] * ac[1][r];
    n2 = x32sum(x16sum(n2));
    if (g == 0) red[v][n * 16 + c] = n2;
  }
  PROT(2);
  if (PF_POS == 1) asm volatile("" :: "v"(pf[0]), "v"(pf[1]));
  if (v >= 8 && b < 64 && lane == 0) ((unsigned*)(a.ws + OFF_LAT))[(v - 4) * 64 + b] = umin(lat, 60000u);
  if (WARM_CODE && v == 7) {
    const char* pc; int d0, d1;
    asm("s_getpc_b64 %0\n\ts_mov_b32 %1, _Z10rec_kernel7RecArgs@rel32@lo+4\n\ts_mov_b32 %2, _Z11attb_kernelPcPt@rel32@lo+12" : "=s"(pc), "=s"(d0), "=s"(d1));
    const char *c0 = pc + d0, *c1 = pc + d1 + ATT_CODE;
    const long o = ((long)b * 64 + lane) * 64;
    if (o < c1 - c0) asm volatile("" :: "v"(*(const volatile GAS unsigned*)(c0 + o)));
  }
  if (v == 9 && lane < 32) {
    const int q = b * 32 + lane;
    *(u32x4*)(a.ws + OFF_KV + (size_t)(q >> 2) * KVT + 1280 + (q & 3) * 336 + 320) = (u32x4){0x3f803f80u, 0x3f803f80u, 0x3f803f80u, 0x3f803f80u};
  }
  BAR();
  if (v == 1 && lane == 0) ((unsigned*)(a.ws + OFF_LAT))[b] = umin(umin(umin(latw[0], latw[4]) + umin(latw[1], latw[5]), 30000u) + umin(umin(latw[2], latw[6]) + umin(latw[3], latw[7]), 30000u), 60000u);
  if (v == 0) {
    float mx = 0.f;
#pragma unroll
    for (int k = 0; k < 3; ++k) {
      const int col = k * 64 + lane;
      float sm = 0.f;
#pragma unroll
      for (int i = 0; i < 10; ++i) sm += red[i][col < 160 ? col : 0];
      mx = fmx(mx, sm);
    }
    mx = fmx(mx, dppf<DPP_XOR1>(mx)); mx = fmx(mx, dppf<DPP_XOR2>(mx)); mx = fmx(mx, dppf<DPP_ROR4>(mx)); mx = fmx(mx, dppf<DPP_ROR8>(mx));
    mx = x32mx(x16mx(mx));
    if (lane == 0) ((float*)(a.ws + OFF_BNRM))[(cq * 32 + h) * 2 + dh] = mx;
  }
  PROT(3);
}

struct RecArgs {
  const u16* mem0; const u16* wq; const u16* wk; const u16* xold; u16* xnew; char* ws;
  unsigned layer; unsigned pad;
};
#define RLX_AGENT __ATOMIC_RELAXED, __HIP_MEMORY_SCOPE_AGENT
constexpr unsigned SPIN_CAP = 1u << 20;
#define TSTAMP(k, v)
#define ARRIVE(k)
#define ARRIVE_OUT()
#define ARRIVE_DECL()
#define LOGV(k, v)
#define LOGF(k, v)
#define LOGP(k)
#define LOGQ(k)
#define LOGF2(k)
#define LOGD(k)
#define LOGT(k, v)
#define STAMP_DECL()
#define STAMP(k)
#define STAMPV(k, v)
#define STAMP_OUT()
#define REC_D1 520
#define SCH_V 10
#define SCH_VMIN 0
#define SCH_DMIN 4
#define SCH_PEN 150
#define SCH_VMIN2 (-4)
#define SCH_PEN2 40
#define SCH_GU 1
#define SCH_GD 2
#define SCH_UP 6
#define SCH_D0 196
#define SCH_FIRST 120
#define EXP_NODMA 0
#define BG_HOLD 220
#define QBATCH 0
#define WPF_N 5
#define WPF_W 0
#define WPF0 (63 - WPF_N)
#define BG_HOLD4 250
#define D1_W1 20
#define F3_D 1200
#define T2W 2
#define AT_ASM 1
#define PM_Z 1
#define P2_X 1
#define ST_KS 14
#define ST_KS0 ST_KS
#define ST_KS1 ST_KS
#define PADR 0x6666
#define PADT 0x000
#define CODE_PAD(n) asm volatile(".p2align 6\n.rept %0\ns_nop 0\n.endr" :: "n"(n))
#define ST_X 270
#define ST_ALL 4
#define CPF_S 61
#define FR_D 700
#define FR_PACE 6
#define END_E60 600
#define END_E1 1300
#define END_E2 3400
#define END_E62 2800
#define END_E3 6800
#define END_E4 11400
#define EARLY_N 6
#define EARLY_D1 80
#define EARLY_V 10
#define EARLY_V1 18
#define F3 0
#define F3A 0
#define F3A_D 400
#define T4_D 300
#define T4_P 1
#define P2_W 1
#define PS_SL 1
#define F2B 4
#define F0C 0
#define F2C 0
#define W2_P 1
#define SCH_W 1
#define SP_SL 0
#define D1_M 310
#define D1_MIN 280
#define D1_C 96
#define D1_G 2
#define BG_HOLD6 0
#define W2_D 0
#define END_E61 2600
#define D1_LATE 200
#define P2_G0 10
#define P2_G2 6
#define P2_G3 6
#define SCH_RL 0
#define SCH_P 3
#define MMA_P 1
#define SP_P 0
#define F6 8
#define F7 10
#define XPF_W 2
#define SCH_LATE 0
#define LATE_V 8
#define QW 3
#define FB_SL 0
#define P2_G1 1
#define P1_G 0
#define F4Z 0
#define F5Z 0
#define T5_Z 2
#define F0A 3
#define FB_E 0
#define EXACT 1
#define D1_P 3
#define VO_A0 9
#define VO_A2 10
#define VO_B0 3
#define VO_B1 0
#define VO_N2 7
#define VO_N3 10
#define VO_W -1
#define UN_P 2
#define VP_E 1
#define TS_B 0
#define ST_E 1
#define W1_SL 0
#define VO_A1 4
#define LAST_V 22
#define END_E59 3200
#define END_G2 600
#define END_G3 600
#define QR 4
#define WPF_S 128
#define END_SL 4
#define SCH_DR 182
#define SCH_DK 3
#define FR_HOLD 0
#define FW 8
#define PR_P 0
#define REL_AT 1
#define XO_T 4
#define Q_AUX 0
#define O_WT 0
#define P_WT 0
#define KP0 0
#define KP1 0
#define XPF 3
#define W1_D 0
#define F4 12
#define F5 0
#define F67 0
#define KP2 0
#define REC_RNG 70u
#define SCH_DRAIN 40
#define PL_X 0
#define PIN_V(x) asm volatile("" : "+v"(x))
#define PIN_S(x) asm volatile("" : "+s"(x))
#define VM_DRAIN() do { asm volatile("s_waitcnt vmcnt(0)" ::: "memory"); __builtin_amdgcn_s_waitcnt(0x0F70); } while (0)
#define SC1_LOAD(off) __builtin_amdgcn_raw_buffer_load_b128(rs, (off), 0, 16)
#define SC1_LOADS(off, soff) __builtin_amdgcn_raw_buffer_load_b128(rs, (off), (soff), 16)
#define SC1_STORE8S(v, off, soff) __builtin_amdgcn_raw_buffer_store_b64((v), rs, (off), (soff), 16)
#define SC1_LOAD8(off) __builtin_amdgcn_raw_buffer_load_b64(rs, (off), 0, 16)
#define SC1_STORE(v, off) __builtin_amdgcn_raw_buffer_store_b128((v), rs, (off), 0, 16)
#define SC1_STORE8(v, off) __builtin_amdgcn_raw_buffer_store_b64((v), rs, (off), 0, 16)
#if defined(__HIP_DEVICE_COMPILE__)
#pragma clang attribute push(__attribute__((target("no-packed-fp32-ops"))), apply_to = function)
#endif
__device__ __forceinline__ unsigned now32() { return (unsigned)__builtin_amdgcn_s_memrealtime(); }
__device__ __forceinline__ void wait_until(unsigned t) { for (;;) { const int d = (int)(t - now32()); if (d <= 0) break; if (d > 8) __builtin_amdgcn_s_sleep(1); } }
__device__ __forceinline__ unsigned cyc32() { return (unsigned)__builtin_amdgcn_s_memtime(); }
__device__ __forceinline__ void wait_cyc(unsigned t) { for (;;) { const int d = (int)(t - cyc32()); if (d <= 0) break; if (d > 200) __builtin_amdgcn_s_sleep(1); } }
#define WAIT_ASM(clk, far_) "1:\n\t" clk " vcc\n\ts_waitcnt lgkmcnt(0)\n\ts_sub_i32 %0, %1, vcc_lo\n\ts_cmp_lt_i32 %0, " far_ "\n\ts_cbranch_scc1 3f\n\ts_sleep 1\n\ts_branch 1b\n" \
                            "2:\n\t" clk " vcc\n\ts_waitcnt lgkmcnt(0)\n\ts_sub_i32 %0, %1, vcc_lo\n3:\n\ts_cmp_gt_i32 %0, 0\n\ts_cbranch_scc1 2b"
__device__ __forceinline__ int spin_until(unsigned t) { int d; asm volatile(WAIT_ASM("s_memrealtime", "9") : "=&s"(d) : "s"(t) : "vcc", "scc", "memory"); return d; }
__device__ __forceinline__ int spin_cyc(unsigned t) { int d; asm volatile(WAIT_ASM("s_memtime", "200") : "=&s"(d) : "s"(t) : "vcc", "scc", "memory"); return d; }
__device__ __forceinline__ void spin_exact(unsigned t) {
  if (!EXACT) { spin_cyc(t); return; }
  int d;
  asm volatile("1:\n\ts_memtime vcc\n\ts_waitcnt lgkmcnt(0)\n\ts_sub_i32 %0, %1, vcc_lo\n\ts_cmp_gt_i32 %0, 95\n\ts_cbranch_scc1 1b\n\t"
               "s_max_i32 %0, %0, 0\n\ts_andn2_b32 %0, %0, 3\n\ts_sub_i32 %0, 3f-2f, %0\n\ts_getpc_b64 vcc\n"
               "2:\n\ts_add_u32 vcc_lo, vcc_lo, %0\n\ts_addc_u32 vcc_hi, vcc_hi, 0\n\ts_setpc_b64 vcc\n\t.rept 24\n\ts_nop 0\n\t.endr\n3:"
               : "=&s"(d) : "s"(t) : "scc", "vcc", "memory");
}
template <int OFF, int SL> __device__ __forceinline__ void lds_spin(unsigned lds0, unsigned want) {
  unsigned v;
  asm volatile("1:\n\tds_read_b32 %0, %1 offset:%3\n\t.if %4\n\ts_sleep %4\n\t.endif\n\ts_waitcnt lgkmcnt(0)\n\tv_cmp_gt_u32 vcc, %2, %0\n\ts_cbranch_vccnz 1b"
               : "=&v"(v) : "v"(lds0), "s"(want), "n"(OFF), "n"(SL) : "vcc", "memory");
}
template <int CTRL> __device__ __forceinline__ int dppi(int v) { return __builtin_amdgcn_mov_dpp(v, CTRL, 0xf, 0xf, true); }
__device__ __forceinline__ int wave_imax(int l) {
  l = imax(l, dppi<DPP_XOR1>(l)); l = imax(l, dppi<DPP_XOR2>(l)); l = imax(l, dppi<DPP_ROR4>(l)); l = imax(l, dppi<DPP_ROR8>(l));
  int a = imax(__builtin_amdgcn_readlane(l, 0), __builtin_amdgcn_readlane(l, 16)), b = imax(__builtin_amdgcn_readlane(l, 32), __builtin_amdgcn_readlane(l, 48));
  PIN_S(a); PIN_S(b);
  return imax(a, b);
}
__device__ __forceinline__ void glds16(const void* gsrc, unsigned lds_dst) {
  unsigned keep;
  asm volatile("s_mov_b32 %0, m0\n\ts_mov_b32 m0, %2\n\ts_nop 0\n\tglobal_load_lds_dwordx4 %1, off\n\ts_mov_b32 m0, %0" : "=&s"(keep) : "v"(gsrc), "s"(lds_dst) : "memory");
}
__device__ __forceinline__ void glds16_sc1(const void* gsrc, unsigned lds_dst) {
  unsigned keep;
  asm volatile("s_mov_b32 %0, m0\n\ts_mov_b32 m0, %2\n\ts_nop 0\n\tglobal_load_lds_dwordx4 %1, off sc1\n\ts_mov_b32 m0, %0" : "=&s"(keep) : "v"(gsrc), "s"(lds_dst) : "memory");
}
__device__ __forceinline__ unsigned acc_off(int G, int row) {
  return (unsigned)(G < 128 ? ((G & 31) | ((G & 32) << 1) | ((G & 64) >> 1)) * 256 + row * 16 : (2 * G - 128 + (row >> 2)) * 256 + (row & 3) * 16);
}
__device__ __forceinline__ unsigned nz(unsigned v) { return v > 1u ? v : 1u; }
__device__ __forceinline__ unsigned pack2n(float lo, float hi) { return nz(pack2(lo, hi)); }
__device__ __forceinline__ unsigned min4(u32x4 v) { return umin(umin(v[0], v[1]), umin(v[2], v[3])); }
__device__ __forceinline__ bf16x8 as8(u32x4 v) { return __builtin_bit_cast(bf16x8, v); }

constexpr int NV = 7, NKQ = 2;
constexpr int L_RED1 = 0;
constexpr int L_BM = L_RED1 + 8 * 1536;
constexpr int L_STG = L_BM + 10240;
constexpr int L_ST = L_STG + 2048 + 2560;
constexpr int L_MISC = L_ST + 512;
constexpr int L_KX = L_MISC + 128;
constexpr int L_QX = L_KX + NKQ * 2048;
constexpr int L_XN = L_QX + NKQ * 2048;
constexpr int L_VOX = L_XN + 81920;
constexpr int L_VOM = L_VOX + NV * 5120;
constexpr int L_BYTES = L_VOM + NV * 1280;
constexpr int S_IMG = L_XN + 40960;
static_assert(S_IMG + 61440 <= L_VOM, "start-up image");
static_assert(L_BYTES <= 160 * 1024, "LDS budget");

__device__ __forceinline__ bf16x8 wqk_frag(const char* lds, int t, int ks, int lane) {
  const int c = lane & 15, g = lane >> 4, p = c & 3;
  const char* q = lds + S_IMG + (32 * ks + 8 * g + (c >> 2)) * 96 + 8 * p + (t == 0 ? 0 : (t == 1 ? 48 : (p < 2 ? 32 : 64)));
  const v4i16_t lo = __builtin_amdgcn_ds_read_tr16_b64_v4i16((LAS v4i16_t*)q), hi = __builtin_amdgcn_ds_read_tr16_b64_v4i16((LAS v4i16_t*)(q + 4 * 96));
  return (bf16x8){lo[0], lo[1], lo[2], lo[3], hi[0], hi[1], hi[2], hi[3]};
}
__device__ __forceinline__ bf16x8 sel_frag(int c, int base) {
  bf16x8 r;
#pragma unroll
  for (int i = 0; i < 8; ++i) r[i] = (c == base + i) ? (short)0x3f80 : (short)0;
  return r;
}
__device__ __forceinline__ bf16x8 qk_frag(const char* p4, const char* p2, const char* p2b) {
  f32x4 a0 = *(const f32x4*)(p4), a1 = *(const f32x4*)(p4 + 1792), a2 = *(const f32x4*)(p4 + 3584), a3 = *(const f32x4*)(p4 + 5376);
  f32x2 b0 = *(const f32x2*)(p2), b1 = *(const f32x2*)(p2 + 1792), b2 = *(const f32x2*)(p2b), b3 = *(const f32x2*)(p2b + 1792);
  f32x4 va = (a0 + a1) + (a2 + a3);
  f32x2 vb = (b0 + b1) + (b2 + b3);
  return mk8(pack2(va[0], va[1]), pack2(va[2], va[3]), pack2(vb[0], vb[1]), 0u);
}
__device__ __forceinline__ void x_fetch(const u16* src, unsigned lds0, int seg, int p, int lane) {
  if (EXP_NODMA) return;
  glds16_sc1(src + (size_t)p * 512 + lane * 8, lds0 + L_XN + (seg & 1) * 40960 + p * 1024);
}
template <int N> __device__ __forceinline__ void x_fetch_n(const u16* src, unsigned lds0, int seg, int p0, unsigned voff) {
  if (EXP_NODMA) return;
#pragma unroll
  for (int i = 0; i < N; i += 4) {
    unsigned keep;
    asm volatile("s_mov_b32 %0, m0\n\ts_mov_b32 m0, %3\n\ts_nop 0\n\tglobal_load_lds_dwordx4 %1, %2 sc1\n\t"
                 ".if %4 > 1\n\tglobal_load_lds_dwordx4 %1, %2 offset:1024 sc1\n\t.endif\n\t.if %4 > 2\n\tglobal_load_lds_dwordx4 %1, %2 offset:2048 sc1\n\t.endif\n\t"
                 ".if %4 > 3\n\tglobal_load_lds_dwordx4 %1, %2 offset:3072 sc1\n\t.endif\n\ts_mov_b32 m0, %0"
                 : "=&s"(keep) : "v"(voff), "s"(src + (size_t)(p0 + i) * 512), "s"(lds0 + L_XN + (seg & 1) * 40960 + (p0 + i) * 1024), "n"(N - i < 4 ? N - i : 4) : "memory");
  }
}
__device__ __forceinline__ int qkv_row(int b) { return (b & 7) / QR * (2 * QR) + (b >> 3) / (16 / QR); }
__device__ __forceinline__ int qkv_col(int b) { return (b & 7) % QR * (16 / QR) + (b >> 3) % (16 / QR); }
__device__ __forceinline__ const u16* x_src(const RecArgs& a, int seg, int b) {
  return seg < NSEG ? (const u16*)(a.ws + OFF_XN) + (size_t)seg * 32 * DM : a.xnew + (size_t)(qkv_row(b) + 16 * (seg - NSEG)) * 32 * DM;
}
template <int N>
__device__ __forceinline__ void img_fetch(const RecArgs& a, unsigned lds0, int h, int j, int part, int lane) {
#pragma unroll
  for (int i = 0; i < N; ++i) {
    const int n = part * N + i, idx = n * 64 + lane, row = idx / 6, pc = idx - row * 6;
    if (FR_PACE && N == 20) asm volatile("s_waitcnt vmcnt(%0)" :: "n"(FR_PACE) : "memory");
    glds16((pc < 3 ? a.wq : a.wk - 24) + (size_t)row * HD + h * DH + j * 24 + pc * 8, lds0 + S_IMG + n * 1024);
  }
}
constexpr int P2B = F4 + F5 + 2 * F67 + F6 + F7, PZ = P2B + F2B, PA = PZ, P0C = PA + 2 * F0A, F3C = 40 - F3A - F3 - P0C - 2 * F0C - F2C;
static_assert(F3C >= 0, "fetch plan");
template <int OFF> __device__ __forceinline__ void lds_inc(unsigned lds0) {
  asm volatile("s_mov_b64 exec, 1\n\tds_add_u32 %0, %1 offset:%2\n\ts_mov_b64 exec, -1" :: "v"(lds0), "v"(1u), "n"(OFF) : "memory");
}
__device__ __forceinline__ void lds_arrive(unsigned* p, int lane) {
  asm volatile("s_waitcnt lgkmcnt(0)" ::: "memory");
  if (lane == 0) __hip_atomic_fetch_add(p, 1u, __ATOMIC_RELAXED, __HIP_MEMORY_SCOPE_WORKGROUP);
}
__device__ __forceinline__ void lds_wait(unsigned* p, unsigned want) {
  while (__hip_atomic_load(p, __ATOMIC_RELAXED, __HIP_MEMORY_SCOPE_WORKGROUP) < want) __builtin_amdgcn_s_sleep(1);
  asm volatile("" ::: "memory");
}
constexpr float KEXP = 0.07216878364870322f * 1.4426950408889634f;
#define FRESH_LANE() int lane = lane0; asm volatile("" : "+v"(lane)); const int c = lane & 15, g = lane >> 4; (void)c; (void)g

struct OutTask {
  u32x4 y[4]; u32x2 xo, out;
  bf16x8 sel;
  unsigned t5f, t5o;
  __device__ __forceinline__ void init(int b, int lane) {
    const int um = b / 10, uq = (b / 5) & 1, up = b % 5, tok = lane & 15, k = up < 4 ? (up >> 1) * 256 + um * 16 + (up & 1) * 8 : 512 + um * 8, q4 = ((lane >> 4) & 1) * 4;
    t5f = (unsigned)(((uq * 20 + (k >> 5)) * 64 + ((k & 31) >> 3) * 16 + tok) * 8 + q4);
    t5o = (unsigned)(((um * 2 + uq) * 16 + (lane >> 4)) * 1280 + up * 256 + tok * 16);
    if (b >= 160) { t5f = 0u; t5o = 0x7ffffff0u; }
    sel = sel_frag(tok, 0);
    PIN_V(sel);
  }
  static __device__ __forceinline__ bool ok(int sl, int b) { return sl >= 0 && b < 160; }
  __device__ __forceinline__ void load(const RecArgs& a, const __amdgpu_buffer_rsrc_t rs, int sl, int b, int part = 3) {
    const unsigned o = t5o | (sl < 0 ? 0x7ff00000u : 0u);
    sl = imax(sl, 0);
#pragma unroll
    for (int k = 0; k < 4; ++k) if (part & 1) y[k] = SC1_LOADS(o, op_slot(sl) + k * 5120);
    if (part & 2) xo = __builtin_bit_cast(u32x2, __hip_atomic_load((const gu64*)(a.xold + (size_t)sl * 32 * DM + t5f), RLX_AGENT));
  }
  __device__ __forceinline__ bool ready(int b) { return b >= 160 || wave_all(umin(umin(min4(y[0]), min4(y[1])), umin(min4(y[2]), min4(y[3]))) != 0u); }
  __device__ __forceinline__ void run(int lane) {
    f32x4 d = {0.f, 0.f, 0.f, 0.f};
#pragma unroll
    for (int k = 0; k < 4; ++k) d = mfma32(sel, as8(y[k]), d);
#pragma unroll
    for (int i = 0; i < 2; ++i) {
      const unsigned r = pack2(d[2 * i], d[2 * i + 1]), x = xo[i];
      out[i] = pack2(u2f(x << 16) + u2f(r << 16), u2f(x & 0xffff0000u) + u2f(r & 0xffff0000u));
    }
    PIN_V(out);
  }
  __device__ __forceinline__ void store(const RecArgs& a, int sl, int b, int lane) {
    __builtin_amdgcn_raw_buffer_store_b64(out, __builtin_amdgcn_make_buffer_rsrc(a.xnew, 0, ok(sl, b) ? S_ * DM * 2 : 0, 0x00020000), (t5f + (unsigned)sl * 32 * DM) * 2u | (unsigned)(lane & 32) << 25, 0, 16);
  }
};

struct PvTask2 {
  u32x4 y[2][2], pa[2], pb[2];
  unsigned lo, lm, vr, ma, mz, so, sz;
  __device__ __forceinline__ void init(int gp, int lane) {
    const int hi = lane >> 5;
    lo = pt_tok(2 * gp) + (unsigned)(lane * 16); lm = pt_mem(2 * gp + hi) + (unsigned)((lane & 31) * 8);
    vr = (unsigned)(L_VOX + 2048 + (lane ^ 8) * 16);
    ma = (unsigned)(L_VOM + hi * 768 + (lane & 31) * 8); mz = (unsigned)(L_VOM + 512 + ((lane & 31) ^ (hi * 8)) * 8);
    so = (unsigned)(L_STG + 2048 + (hi * 16 + (lane & 15)) * 16 + ((lane >> 4) & 1) * 8);
    sz = hi ? (unsigned)(L_RED1 + 8704 + lane * 8) : so + 1024;
    PIN_V(lo); PIN_V(lm); PIN_V(vr); PIN_V(ma); PIN_V(mz); PIN_V(so); PIN_V(sz);
#pragma unroll
    for (int q = 0; q < 2; ++q) { pa[q] = pb[q] = y[q][0] = y[q][1] = (u32x4){0u, 0u, 0u, 0u}; PIN_V(pa[q]); PIN_V(pb[q]); }
  }
  __device__ __forceinline__ void load(const __amdgpu_buffer_rsrc_t rs, unsigned o) {
#pragma unroll
    for (int q = 0; q < 2; ++q) {
      y[q][0] = SC1_LOADS(lo + q * 1024, o); y[q][1] = SC1_LOADS(lo + 2048 + q * 1024, o);
      const u32x2 p = __builtin_amdgcn_raw_buffer_load_b64(rs, lm + q * 256, o, 16);
      pa[q][0] = p[0]; pa[q][1] = p[1];
    }
  }
  __device__ __forceinline__ bool ready() {
    return wave_all(umin(umin(umin(min4(y[0][0]), min4(y[0][1])), umin(min4(y[1][0]), min4(y[1][1]))), umin(umin(pa[0][0], pa[0][1]), umin(pa[1][0], pa[1][1]))) != 0u);
  }
  template <bool DIRECT> __device__ __forceinline__ void run(char* lds, int sv, int lane, const __amdgpu_buffer_rsrc_t rs, unsigned og) {
    const f32x4 Z4 = {0.f, 0.f, 0.f, 0.f};
    const char* vp = lds + L_VOX + sv * 5120 + lane * 16;
    bf16x8 vx[6];
#pragma unroll
    for (int t = 0; t < 5; ++t) vx[t] = *(const bf16x8*)(vp + t * 1024);
    vx[5] = *(const bf16x8*)(lds + vr + sv * 5120);
    const u32x2 a0 = *(const u32x2*)(lds + ma + sv * 1280), a1 = *(const u32x2*)(lds + ma + sv * 1280 + 256);
    const u32x2 z0 = *(const u32x2*)(lds + mz + sv * 1280), z1 = *(const u32x2*)(lds + mz + sv * 1280 + 256);
    const bf16x8 vm = mk8(a0[0], a0[1], a1[0], a1[1]), vz = mk8(z0[0], z0[1], z1[0], z1[1]);
#pragma unroll
    for (int q = 0; q < 2; ++q) { PIN_V(pa[q]); pb[q][2] = pa[q][0]; pb[q][3] = pa[q][1]; PIN_V(pb[q]); }
    __builtin_amdgcn_sched_barrier(0);
    f32x4 o[2][3];
#pragma unroll
    for (int q = 0; q < 2; ++q) {
#pragma unroll
      for (int t = 0; t < 3; ++t) o[q][t] = mfma32(vx[t], as8(y[q][0]), Z4);
#pragma unroll
      for (int t = 0; t < 3; ++t) o[q][t] = mfma32(vx[3 + t], as8(y[q][1]), o[q][t]);
      o[q][0] = mfma32(vm, as8(pa[q]), o[q][0]); o[q][1] = mfma32(vm, as8(pb[q]), o[q][1]); o[q][2] = mfma32(vz, as8(pa[q]), o[q][2]);
    }
#pragma unroll
    for (int q = 0; q < 2; ++q) {
#pragma unroll
      for (int t = 0; t < 3; ++t) {
        const u32x2 v = {pack2n(o[q][t][0], o[q][t][1]), pack2n(o[q][t][2], o[q][t][3])};
        if (DIRECT) SC1_STORE8S(v, (so - (L_STG + 2048) + t * 512) | (t == 2 ? (unsigned)(lane & 32) << 25 : 0u), og + q * 20480);
        else *(u32x2*)(lds + (t < 2 ? so + t * 512 : sz) + q * 1280) = v;
      }
    }
  }
};

struct TokScores {
  bf16x8 k[2], q[2]; u32x4 v[2];
  __device__ __forceinline__ void get(const char* lds, int seg, int lane) {
#pragma unroll
    for (int i = 0; i < 2; ++i) { k[i] = *(const bf16x8*)(lds + L_KX + (seg & 1) * 2048 + i * 1024 + lane * 16); q[i] = *(const bf16x8*)(lds + L_QX + (seg & 1) * 2048 + i * 1024 + lane * 16); }
  }
  __device__ __forceinline__ void run(int qt) {
    const f32x4 Z4 = {0.f, 0.f, 0.f, 0.f};
    const f32x4 d1 = mfma32(k[0], q[qt], Z4), d2 = mfma32(k[1], q[qt], Z4);
    v[qt] = (u32x4){pack2n(d1[0], d1[1]), pack2n(d1[2], d1[3]), pack2n(d2[0], d2[1]), pack2n(d2[2], d2[3])};
    PIN_V(v[qt]);
  }
  __device__ __forceinline__ void store(const __amdgpu_buffer_rsrc_t rs, unsigned off, unsigned o) {
    __builtin_amdgcn_raw_buffer_store_b128(v[0], rs, off, o, 16); __builtin_amdgcn_raw_buffer_store_b128(v[1], rs, off + 1024, o, 16);
    asm volatile("s_nop 1" ::: "memory");
  }
};

__device__ __forceinline__ void touch(const char* base, unsigned off, unsigned lds0) {
  unsigned keep;
  asm volatile("s_mov_b32 %0, m0\n\ts_mov_b32 m0, %3\n\ts_nop 0\n\tglobal_load_lds_dword %1, %2 sc1\n\ts_mov_b32 m0, %0" : "=&s"(keep) : "v"(off), "s"(base), "s"(lds0 + L_RED1 + 8192) : "memory");
}
template <int N> __device__ __forceinline__ void stouch(const char* p, unsigned& d) {
  asm volatile(".set pf_o, 0\n\t.rept %2\n\ts_load_dword %0, %1, pf_o\n\t.set pf_o, pf_o + 128\n\t.endr" : "+s"(d) : "s"(p), "n"(N) : "memory");
}
__device__ __forceinline__ void code_touch(int b, int lane, unsigned lds0) {
  const char* pc; int d0, d1;
  asm volatile("s_getpc_b64 %0\n\ts_mov_b32 %1, _Z10rec_kernel7RecArgs@rel32@lo+4\n\ts_mov_b32 %2, _Z11attb_kernelPcPt@rel32@lo+12" : "=s"(pc), "=s"(d0), "=s"(d1));
  const int o = (lane * 32 + (b >> 3)) * 128;
  touch(pc + d0, (unsigned)(o < d1 - d0 ? o : 0), lds0);
}
__device__ __forceinline__ unsigned zero_base(int b, int h, int j, int ln) {
  return ln < 32 ? (unsigned)((h & 3) * 32768 + j * 512 + ln * 16) : ln < 40 ? (unsigned)((h & 2) * 1024 + j * 256 + (ln & 4) * 32 + (h & 1) * 64 + (ln & 3) * 16)
                 : (unsigned)OFF_ACC + (b < ACC_NB ? (unsigned)(b * 256 + (ln - 40) * 16) : ts_piece((b - ACC_NB) * 8 + ln - 40));
}
__device__ __forceinline__ unsigned sct_slot(unsigned ck, unsigned sl) { return sl * SCT_STEP + ((ck >> (sl & 4u)) & 7u) * 4096u; }
__device__ __forceinline__ unsigned scm_slot(unsigned ck, unsigned sl) { return OFF_SCM + sl * SCM_STEP + ((ck >> 8) & 7u) * 4096u; }
__device__ __forceinline__ int ck_rot(int x) { return ((x & 1) << 2) | (x >> 1); }
struct Place {
  unsigned t[8];
  __device__ __forceinline__ void load(const char* ws, int lane) {
#pragma unroll
    for (int x = 0; x < 8; ++x) t[x] = *(const unsigned*)(ws + OFF_LAT + ((imin(lane >> 3, 5) * 8 + (ck_rot(x) ^ (lane & 7))) * 8 + x) * 4);
  }
  __device__ __forceinline__ unsigned get(int b, int lane) {
    int c = (int)((((t[0] + t[1]) + (t[2] + t[3])) + ((t[4] + t[5]) + (t[6] + t[7]))) * 8u) + (lane & 7);
    c = imin(c, dppi<DPP_XOR1>(c)); c = imin(c, dppi<DPP_XOR2>(c)); c = imin(c, dppi<0x141>(c));
    unsigned k = 0u;
#pragma unroll
    for (int r = 0; r < 6; ++r) k |= (unsigned)(__builtin_amdgcn_readlane(c, 8 * r) & 7) << (4 * r);
    return (k ^ PL_X) ^ (unsigned)ck_rot(b & 7) * 0x111111u;
  }
};

struct Sched {
  u32x4 tsv, rec;
  unsigned side, o;
  __device__ __forceinline__ void pre(int s) { o = (unsigned)OFF_ACC + (unsigned)((s + SR - 1) % SR) * (unsigned)ACC_STEP + ACC_TS + side; PIN_S(o); }
  __device__ __forceinline__ void load(const __amdgpu_buffer_rsrc_t rs, int lane) {
    tsv = SC1_LOADS(ts_piece(lane & 63) - ACC_TS, o); rec = SC1_LOADS(128, o);
  }
  __device__ __forceinline__ unsigned last(unsigned ref) {
    const unsigned o = 0x40000000u - ref;
    int l = (int)umax(umax(tsv[0] ? tsv[0] + o : 0u, tsv[1] ? tsv[1] + o : 0u), umax(tsv[2] ? tsv[2] + o : 0u, tsv[3] ? tsv[3] + o : 0u));
    return (unsigned)wave_imax(l) - o;
  }
  __device__ __forceinline__ unsigned run(const RecArgs& a, const __amdgpu_buffer_rsrc_t rs, int s, int b, int lane) {
    constexpr int w = 3; (void)w;
    unsigned acur = __builtin_amdgcn_readfirstlane(rec[0]), aprev = __builtin_amdgcn_readfirstlane(rec[1]), hist = __builtin_amdgcn_readfirstlane(rec[3]);
    int D8 = (int)__builtin_amdgcn_readfirstlane(rec[2]);
    const int d = wave_imax((int)(umax(umax(tsv[0], tsv[1]), umax(tsv[2], tsv[3])) - aprev)), dd = 8 * d - D8;
    int Dn = D8 + (dd >> (dd > 0 ? SCH_GU : SCH_GD));
    unsigned anext = (acur + (unsigned)((Dn + 4 + imax(Dn - 8 * SCH_DR, 0) * SCH_DK / 8) >> 3) + SCH_V + (s < EARLY_N ? EARLY_V : (s >= WPF0 ? (s == NSEG - 2 ? LAST_V : LATE_V) : 0))) | 1u;
    if (__builtin_expect(hist < 2u || aprev == 0u || d < 1 || d + SCH_VMIN > (int)(acur - aprev), 0)) {
      if (s == 0) {
        for (int it = 0; it < 40 && !wave_all(min4(tsv) != 0u); ++it) {
          __builtin_amdgcn_s_sleep(ST_ALL);
          asm volatile("" ::: "memory");
          load(rs, lane);
        }
      }
      const unsigned now = now32(), L = last(now);
      if (acur == 0u) { D8 = 8 * SCH_D0; hist = 0u; }
      const bool late = acur == 0u || (int)(L + SCH_VMIN - acur) > 0;
      if (late) { if (b == 0) FC(3); if (acur) D8 += 8 * (hist ? SCH_UP : 3 * SCH_UP); }
      else if (aprev) { if (hist < 2u) D8 = 8 * imin(imax((int)(L - aprev), 1), 4000); hist = umin(hist + 1u, 2u); }
      const unsigned base = acur == 0u ? L + SCH_FIRST : (late ? L + SCH_V + ((int)(L + SCH_VMIN2 - acur) <= 0 ? SCH_PEN2 : SCH_PEN) : acur);
      anext = (base + (unsigned)((D8 + 4) >> 3) + SCH_V + (s == 1 ? EARLY_V1 : (s < EARLY_N ? EARLY_V : 0))) | 1u;
      if (s == 0) anext = (L + ST_X) | 1u;
      if ((int)(now - L) > 50000) anext = 0u;
      if (late) acur = 0u;
      Dn = D8;
    }
    if (__builtin_expect(b == 0, 0))
      __builtin_amdgcn_raw_buffer_store_b128((u32x4){anext, acur, (unsigned)Dn, hist}, rs, lane < 2 ? lane * 8192 : 0x7ffffff0, (unsigned)OFF_ACC + (unsigned)(s % SR) * (unsigned)ACC_STEP + ACC_TS + 128u, 16);
    if (XO_T && anext) anext += (b & 1) * XO_T;
    LOGV(0, anext); LOGV(1, acur == 0u); LOGV(2, (Dn + 4) >> 3); LOGV(3, d); LOGV(4, acur - aprev - d); LOGV(5, aprev);
    return anext;
  }
};

struct TokTask {
  u32x4 y[2]; u32x2 ym; u32x4 res;
  unsigned l0, l1, lm, s0, s1;
  static __device__ __forceinline__ bool ok(int sl) { return sl >= 0 && sl < NSEG; }
  __device__ __forceinline__ void init(int h, int j, int lane) {
    const int c = lane & 15, g = lane >> 4;
    l0 = ts_tok(h, g) + (unsigned)(j * 256 + c * 16); l1 = l0 + 16384u;
    lm = ts_mem(h, g + 4 * (c >> 3)) + (unsigned)(j * 64 + (c & 7) * 8);
    res = (u32x4){0u, 0u, 0u, 0u};
    const int qi = 4 * j + (c & 3);
    s0 = g < 2 ? pt_tok(h) + (unsigned)((qi >> 4) * 1024 + ((c >> 2) * 16 + (qi & 15)) * 16 + g * 8) : 0x7ffffff0u;
    s1 = lane < 8 ? pt_mem(h) + (unsigned)((qi >> 4) * 256 + ((c >> 2) * 16 + (qi & 15)) * 8) : 0x7ffffff0u;
    PIN_V(l0); PIN_V(l1); PIN_V(lm); PIN_V(s0); PIN_V(s1);
  }
  __device__ __forceinline__ void load(const __amdgpu_buffer_rsrc_t rs, unsigned o) {
    y[0] = SC1_LOADS(l0, o); y[1] = SC1_LOADS(l1, o);
    ym = __builtin_amdgcn_raw_buffer_load_b64(rs, lm, o, 16);
  }
  __device__ __forceinline__ bool ready() { return wave_all(umin(umin(min4(y[0]), min4(y[1])), umin(ym[0], ym[1])) != 0u); }
  __device__ __forceinline__ void run(int j, int lane, const bf16x8 SelLo) {
    const int c = lane & 15, g = lane >> 4;
    const f32x4 Z4 = {0.f, 0.f, 0.f, 0.f};
    const f32x4 d1 = mfma32(SelLo, as8(y[1]), mfma32(SelLo, as8(y[0]), Z4));
    const f32x4 d0 = mfma32(SelLo, mk8(ym[0], ym[1], 0u, 0u), Z4);
    const int qi = 4 * j + (c & 3), k0 = 16 * g + 4 * (c >> 2);
    float t[4], mv[4];
    float mx = -INFINITY;
#pragma unroll
    for (int r = 0; r < 4; ++r) {
      t[r] = (g < 2 && k0 + r <= qi) ? d1[r] : -INFINITY;
      const float mt = d0[r] + dppf<DPP_ROR8>(d0[r]);
      mv[r] = lane < 8 ? mt : -INFINITY;
      mx = fmx(mx, fmx(t[r], mv[r]));
    }
    mx = fmx(mx, dppf<DPP_ROR8>(mx)); mx = fmx(mx, dppf<DPP_ROR4>(mx));
    mx = x16mx(mx);
    float sum = 0.f;
#pragma unroll
    for (int r = 0; r < 4; ++r) {
      t[r] = __builtin_amdgcn_exp2f(t[r] - mx); mv[r] = __builtin_amdgcn_exp2f(mv[r] - mx);
      sum += t[r] + mv[r];
    }
    sum += dppf<DPP_ROR8>(sum); sum += dppf<DPP_ROR4>(sum);
    sum = x16sum(sum);
    const float rs_ = __builtin_amdgcn_rcpf(sum);
    res = (u32x4){pack2n(t[0] * rs_, t[1] * rs_), pack2n(t[2] * rs_, t[3] * rs_), pack2n(mv[0] * rs_, mv[1] * rs_), pack2n(mv[2] * rs_, mv[3] * rs_)};
    PIN_V(res);
  }
  __device__ __forceinline__ void store(const __amdgpu_buffer_rsrc_t rs, unsigned o, unsigned no = 0u) {
    SC1_STORE8S(((u32x2){res[0], res[1]}), s0 | no, o);
    SC1_STORE8S(((u32x2){res[2], res[3]}), s1 | no, o);
  }
};

constexpr int L_DRN = L_VOX + (NSEG % NV) * 5120;
__device__ __forceinline__ void drain_pre(char* lds, int b, int lane) {
  const int gp = (b & 7) * 2 + (b >> 7), m = (b >> 3) & 15;
  TokTask tk; PvTask2 pv; OutTask ot;
  tk.init(2 * gp + (m >> 3), m & 7, lane); pv.init(gp, lane); ot.init(b, lane);
  u32x4* p = (u32x4*)(lds + L_DRN) + lane;
  p[0] = (u32x4){tk.l0, tk.lm, tk.s0, tk.s1}; p[64] = (u32x4){pv.lo, pv.lm, pv.vr, pv.ma}; p[128] = (u32x4){pv.mz, pv.so, ot.t5f, ot.t5o};
  p[192] = __builtin_bit_cast(u32x4, ot.sel);
}
__device__ __forceinline__ void qkv_pre(unsigned (&vo)[2][2], int b, int w, int lane) {
  const int cb = qkv_col(b), c = lane & 15, g = lane >> 4, t0 = w & 1, col0 = (cb % 5) * 128 + w * 32;
  const bool SW = cb < 10;
#pragma unroll
  for (int i = 0; i < 2; ++i)
#pragma unroll
    for (int tt = 0; tt < 2; ++tt) {
      const int tq = tt ^ t0, tk = tq * 16 + c, cc = col0 + i * 16 + (SW ? 4 * g : c), hd = cc / BD, e = cc - hd * BD;
      const int kv = (int)(OFF_KV - OFF_QB) + hd * 64 * KVT;
      vo[i][tt] = (unsigned)(!SW ? kv + 1280 + tq * 672 + ((g >> 1) * 21 + e) * 16 + (g & 1) * 8
                                 : (cb < 5 ? (hd * S_ + tk) * 2 * QST + e * 2 : kv + (e < 16 ? (tk + 32 * (e >> 3)) * 16 + (e & 7) * 2 : 1024 + tk * 8)));
      PIN_V(vo[i][tt]);
    }
}

template <int w>
__device__ __forceinline__ void token_waves(const RecArgs& a, char* lds, const __amdgpu_buffer_rsrc_t rs, unsigned (&vo)[2][2]) {
  const int lane0 = __builtin_amdgcn_workitem_id_x() & 63, b = __builtin_amdgcn_workgroup_id_x(), wid = w + 4; (void)wid;
  const int gp = (b & 7) * 2 + (b >> 7), m = (b >> 3) & 15, h0 = 2 * gp, h = h0 + (m >> 3), j = m & 7;
  float* const misc = (float*)(lds + L_MISC);
  const f32x4 Z4 = {0.f, 0.f, 0.f, 0.f};
  const unsigned lds0 = (unsigned)(uintptr_t)(LAS char*)lds;
  unsigned* const cnt = (unsigned*)(misc + 6);
  const float* const xnrm = (const float*)(a.ws + OFF_XNRM);
  bf16x16 W[2][10];
  auto btile = [&](int i) { return w == 1 ? 0 : (w == 2 ? 1 + i : 3 + i); };
  if (w == 1) code_touch(b, lane0, lds0);
  Place pl;
  unsigned phi[2][2], ph[2][2][2];
#pragma unroll
  for (int k = 0; k < 8; ++k) {
    const int bt = btile(k >> 2), hw = h0 + (bt == 2 ? k & 1 : bt / 3), T = bt == 2 ? 32 + (m >> 1) : (bt % 3) * 16 + m;
    ph[k >> 2][(k >> 1) & 1][k & 1] = ((const __attribute__((address_space(4))) unsigned*)(a.ws + OFF_PHI))[(hw >> 2) | (hw & 3) << 3 | (k & 2) << 4 | (T / 10) << 6];
  }
  __builtin_amdgcn_sched_barrier(0);
  if (w == 3) pl.load(a.ws, lane0);
  if (a.layer) {
    if (w != 0 && FR_HOLD) wait_until(now32() + FR_HOLD);
    if (w != 0) img_fetch<20>(a, lds0, h, j, w - 1, lane0);
  } else {
    if (w == 0) img_fetch<12>(a, lds0, h, j, 4, lane0);
#pragma unroll
    for (int i = 0; i < 10; ++i) x_fetch(x_src(a, 0, b), lds0, 0, 10 * w + i, lane0);
  }
  const __amdgpu_buffer_rsrc_t rwb = __builtin_amdgcn_make_buffer_rsrc(a.ws + OFF_WB, 0, (int)WB_BYTES, 0x00020000);
#pragma unroll
  for (int k = 0; k < 4; ++k) phi[k >> 1][k & 1] = (((lane0 & 8) ? ph[k >> 1][k & 1][1] : ph[k >> 1][k & 1][0]) & 8192u) | (unsigned)(lane0 * 16);
#pragma unroll
  for (int i = 0; i < 2; ++i)
#pragma unroll
    for (int p = 0; p < 10; ++p)
      if (w >= 2 || (w == 1 && i == 1)) {
        const unsigned wo = wb_off((b * 5 + btile(i)) * 20 + 2 * p), wv = phi[i][p / 5] ^ (wo & 8192u);
        if (FR_PACE && a.layer) asm volatile("s_waitcnt vmcnt(%0)" :: "n"(FR_PACE) : "memory");
        W[i][p] = cat16(as8(__builtin_amdgcn_raw_buffer_load_b128(rwb, wv, wo & ~8192u, 0)), as8(__builtin_amdgcn_raw_buffer_load_b128(rwb, wv + 1024, wo & ~8192u, 0)));
      }
  f32x4 bn = Z4;
  if (w == 0) {
    bn = *(const f32x4*)(a.ws + OFF_BNRM + lane0 * 16);
    if (!a.layer) asm volatile("s_waitcnt vmcnt(0)" ::: "memory");
  } else asm volatile("s_waitcnt vmcnt(%0)" :: "n"(w == 1 ? 20 : 40) : "memory");
  unsigned ck = 0u;
  if (w == 3) { ck = pl.get(b, lane0); *(unsigned*)(misc + 11) = ck; }
  BAR();
#pragma unroll
  for (int p = 0; p < 10; ++p) {
    if (w < 2) W[0][p] = cat16(wqk_frag(lds, 2 * w, 2 * p, lane0), wqk_frag(lds, 2 * w, 2 * p + 1, lane0));
    if (w == 0) W[1][p] = cat16(wqk_frag(lds, 1, 2 * p, lane0), wqk_frag(lds, 1, 2 * p + 1, lane0));
  }
  float cb2 = 0.f;
  if (w == 0) {
    float n = __builtin_amdgcn_sqrtf(bn[0] + bn[1]) + __builtin_amdgcn_sqrtf(bn[2] + bn[3]);
    n += dppf<DPP_XOR1>(n); n += dppf<DPP_XOR2>(n); n += dppf<DPP_ROR4>(n); n += dppf<DPP_ROR8>(n);
    n = x32mx(x16mx(n));
    cb2 = u2f(__builtin_amdgcn_readfirstlane(f2u(1.15f * n * n)));
  }
  const float cm2 = u2f((f2u(cb2) & 0x7fffffu) | 0x40000000u);
  const int ek = imax((int)((f2u(cb2) >> 23) & 0xffu), 1) - 252;
  if (w >= 2) {
#pragma unroll
    for (int i = 0; i < 4; ++i) *(unsigned*)(lds + L_KX + (w - 2) * 4096 + i * 1024 + lane0 * 16 + 12) = 0u;
  }
  OutTask ot;
  const int zl = lane0 - 16;
  const bool zon = w == 3 && zl >= 0 && (zl < 40 || b < ACC_NB + 17);
  unsigned zo = zon ? zero_base(b, h, j, zl) + (zl < 32 ? sct_slot(ck, 0u) : (zl < 40 ? scm_slot(ck, 0u) : 0u)) : 0x7ffffff0u;
  unsigned zm = zon ? (zl < 32 ? SCT_STEP + 1u : (zl < 40 ? SCM_STEP : (unsigned)ACC_STEP)) : 0u;
  const unsigned zx = ((ck ^ (ck >> 4)) & 7u) * 4096u;
  if (w == 3) { PIN_V(zo); PIN_V(zm); }
  const __amdgpu_buffer_rsrc_t rx = __builtin_amdgcn_make_buffer_rsrc(a.ws + OFF_XNRM, 0, (int)(OFF_QB - OFF_XNRM), 0x00020000);
  u32x4 v0, v1;
  f32x4 d5;
  unsigned oo, za, xa, ya, n4, so_op, so_y, so_x;
  float xn4 = 0.f;
  auto w1_part = [&](int k, int s, int lane) {
    if (w < 2) return;
    if (w == 2) {
      if (k >= 1 && k <= 4) d5 = mfma32(ot.sel, as8(ot.y[k < 1 || k > 4 ? 0 : k - 1]), k == 1 ? Z4 : d5);
      if (k == 6 || k == 7) {
        const unsigned r = pack2(d5[2 * (k & 1)], d5[2 * (k & 1) + 1]), x = ot.xo[k & 1];
        ot.out[k & 1] = pack2(u2f(x << 16) + u2f(r << 16), u2f(x & 0xffff0000u) + u2f(r & 0xffff0000u));
        PIN_V(ot.out);
      }
      if (k == 8) xn4 = fmx(xn4, dppf<DPP_XOR1>(xn4));
      if (k == 9) xn4 = fmx(xn4, dppf<DPP_XOR2>(xn4));
      if (k == 10) xn4 = fmx(xn4, dppf<DPP_ROR4>(xn4));
      if (k == 11) xn4 = fmx(xn4, dppf<DPP_ROR8>(xn4));
      if (k >= 8 && k <= 11) PIN_V(xn4);
      if (k == 12) misc[s & 3] = x16mx(xn4);
      if (k == 5) { so_y = op_slot(imax(s - 1 - LAG5, 0)); so_x = (unsigned)imax(s - 1 - LAG5, 0) * 40960u; PIN_S(so_y); PIN_S(so_x); }
      if (k == 14) {
        ya = ot.t5o | (s <= LAG5 ? 0x7ff00000u : 0u);
        xa = (ot.t5f * 2u | (unsigned)(lane & 32) << 25) | (OutTask::ok(s - 2 - LAG5, b) ? 0u : 0x7ff00000u);
        PIN_V(ya); PIN_V(xa);
      }
    } else if (k == 8) {
      const unsigned zs = (unsigned)(s + 2) % SR;
      za = (zo + (s < 0 ? 1024u : zs) * (zm & ~1u)) ^ ((zm & 1u) * (zs & 4u ? zx : 0u));
      PIN_V(za);
    }
    if (k == 13) {
      n4 = __hip_atomic_load((unsigned*)(misc + 28), __ATOMIC_RELAXED, __HIP_MEMORY_SCOPE_WORKGROUP);
      so_op = s < 2 + LAG4 ? 0x7ff00000u : op_slot(s - 2 - LAG4) + (unsigned)((m * 32 + (w & 1) * 16 + gp) * 1280);
      PIN_S(so_op);
    }
    if (k == 15 && __builtin_expect((int)__builtin_amdgcn_readfirstlane(n4) < s, 0)) lds_spin<L_MISC + 112, 1>(lds0, (unsigned)s);
    if (k == 16) {
      asm volatile("" ::: "memory");
      const char* sp = lds + L_STG + 2048 + (w & 1) * 1280;
      v0 = *(const u32x4*)(sp + lane * 16); v1 = *(const u32x4*)(sp + 1024 + (lane & 15) * 16);
      oo = so_op + (unsigned)(lane * 16);
      za = lane < 16 ? oo + 1024 : (w == 3 ? za : 0x7ffffff0u);
      PIN_V(oo); PIN_V(za);
    }
    if (k == 19 && w == 3) {
#pragma unroll
      for (int i = 0; i < 4; ++i) { v1[i] = lane < 16 ? v1[i] : 0u; PIN_V(v1[i]); }
    }
    __builtin_amdgcn_sched_barrier(0);
  };
  auto w1_fire = [&](int s, int lane, auto last) {
    if (w < 2) return;
    lds_spin<L_MISC + 92, W1_SL>(lds0, 4u * (unsigned)imax(s, 0));
    SC1_STORE(v0, oo);
    SC1_STORE(v1, za);
    __builtin_amdgcn_sched_barrier(0);
    if (w == 2) {
      __builtin_amdgcn_raw_buffer_store_b64(ot.out, rx, xa, so_x + (unsigned)(OFF_XA - OFF_XNRM - 40960), 16);
      if (last == 0) {
#pragma unroll
        for (int k = 0; k < 4; ++k) ot.y[k] = SC1_LOADS(ya, so_y + k * 5120);
        ot.xo = __builtin_amdgcn_raw_buffer_load_b64(rx, ot.t5f * 2u, so_x + (unsigned)(OFF_XB - OFF_XNRM), 16);
        xn4 = u2f(__builtin_amdgcn_raw_buffer_load_b32(rx, (lane & 31) * 16, (s + 1) * 512, 16));
      }
    } else {
      const unsigned xo_ = (unsigned)((s - 8) * 1024 + lane * 16);
      if (__builtin_expect((unsigned)(s - 8) < 20u, 0)) SC1_STORE(((u32x4){0u, 0u, 0u, 0u}), xo_ < X_BYTES / 256 ? (unsigned)(OFF_X + b * (X_BYTES / 256)) + xo_ : 0x7ffffff0u);
    }
    __builtin_amdgcn_sched_barrier(0);
  };
  STAMP_DECL(); ARRIVE_DECL();
  auto mma_seg = [&](int seg, int sv, int lane, auto pause) {
    const int c = lane & 15, g = lane >> 4;
    const char* xp = lds + L_XN + (seg & 1) * 40960 + lane * 16;
    f32x4 acc[2][2] = {{Z4, Z4}, {Z4, Z4}};
    bf16x8 x[4][2];
#pragma unroll
    for (int p = 0; p < 3; ++p) { x[p][0] = *(const bf16x8*)(xp + p * 1024); x[p][1] = *(const bf16x8*)(xp + (20 + p) * 1024); }
    unsigned eo[2];
#pragma unroll
    for (int ks = 0; ks < 20; ++ks) {
      if (ks == (w == 0 ? KP0 : (w == 1 ? KP1 : KP2))) { pause(); __builtin_amdgcn_sched_barrier(0); }
      if (ks == 17) {
#pragma unroll
        for (int i = 0; i < 2; ++i) {
          eo[i] = (unsigned)(w == 0 ? (i ? L_KX : L_QX) + (seg % NKQ) * 2048 + lane * 16
                                    : (w == 1 && i == 0 ? ((g >> 1) ? L_KX : L_QX) + (seg % NKQ) * 2048 + (2 * (g & 1) * 16 + c) * 16 + 8 : L_VOX + sv * 5120 + btile(i) * 1024 + lane * 16));
          if (i == 0 || w == 1) PIN_V(eo[i]);
        }
        if (w == 0) eo[1] = eo[0] + (L_KX - L_QX);
        if (w >= 2) eo[1] = eo[0] + 1024;
      }
      __builtin_amdgcn_sched_barrier(0);
#pragma unroll
      for (int q = 0; q < 4; ++q) {
        const int tt = 1 - (q >> 1), i = q & 1;
        const bool isqk = w == 0 || (w == 1 && i == 0);
        const bf16x8 wf = half8(W[i][ks >> 1], ks & 1);
        acc[i][tt] = isqk ? mfma32(wf, x[ks & 3][tt], acc[i][tt]) : mfma32(x[ks & 3][tt], wf, acc[i][tt]);
        __builtin_amdgcn_sched_barrier(0);
        if (q < 2 && ks + 3 < 20) x[(ks + 3) & 3][q] = *(const bf16x8*)(xp + (q * 20 + ks + 3) * 1024);
        if (q == 2) w1_part(ks, seg - 1, lane);
        __builtin_amdgcn_sched_barrier(0);
      }
    }
#pragma unroll
    for (int i = 0; i < 2; ++i) {
      if (w + i == 1) { const float ks_ = (w == 0 || (g >> 1)) ? KEXP : 1.0f; acc[i][0] *= ks_; acc[i][1] *= ks_; }
      const u32x4 v = {pack2(acc[i][0][0], acc[i][0][1]), pack2(acc[i][0][2], acc[i][0][3]), pack2(acc[i][1][0], acc[i][1][1]), pack2(acc[i][1][2], acc[i][1][3])};
      char* p = lds + eo[i];
      if (w == 0) { *(u32x2*)(p) = (u32x2){v[0], v[1]}; *(u32x2*)(p + 1024) = (u32x2){v[2], v[3]}; }
      else if (w == 1 && i == 0) { *(unsigned*)(p) = v[0]; *(unsigned*)(p + 256) = v[1]; *(unsigned*)(p + 1024) = v[2]; *(unsigned*)(p + 1280) = v[3]; }
      else *(u32x4*)p = v;
    }
  };
  unsigned vp, mp, ip;
  int sidx, ei = 127;
  float inv;
  f32x4 acc[2];
  auto vo_pre = [&](int s, int sv, int lane) {
    vp = (unsigned)(w == 0 ? L_MISC + (s & 3) * 4 : L_VOM + sv * 1280 + (btile(0) * 32 + (w == 1 ? lane & 31 : lane)) * 8);
    mp = (unsigned)(L_BM + lane * 16);
    PIN_V(vp); PIN_V(mp);
    if (w == 0) { ip = (unsigned)(L_MISC + 16 + ((s + 1) & 1) * 4); PIN_V(ip); }
    else {
      inv = misc[4 + (s & 1)];
      sidx = (lane & 8) ? 0xEEEE : 0x4444;
      acc[0] = acc[1] = Z4;
      PIN_V(sidx); PIN_V(acc[1]);
      if (w >= 2) { PIN_V(acc[0]); PIN_V(inv); }
    }
    __builtin_amdgcn_sched_barrier(0);
  };
  auto vo_mem = [&](int s, auto last) {
    __builtin_amdgcn_s_setprio(2);
    asm volatile(".rept %0\n\ts_nop 3\n\t.endr" :: "n"(w == 0 ? VO_A0 : (w == 1 ? VO_A1 : VO_A2)) : "memory");
    float xmax = 0.f;
    if (w == 0) xmax = *(const float*)(lds + vp);
    constexpr int NX = w == 2 ? VO_N2 : (w == 3 ? VO_N3 : 10);
    bf16x8 x[10];
#pragma unroll
    for (int p = 0; p < NX; ++p) x[p] = *(const bf16x8*)(lds + mp + p * 1024);
    __builtin_amdgcn_sched_barrier(0);
    if (w < 2) asm volatile(".rept %0\n\ts_nop 3\n\t.endr" :: "n"(w == 0 ? VO_B0 : VO_B1) : "memory");
    if (VO_W >= 0) asm volatile("s_waitcnt lgkmcnt(%0)" :: "n"(VO_W < 0 ? 0 : VO_W) : "memory");
    STAMP(5);
    __builtin_amdgcn_sched_barrier(0);
    int ex = 0;
#pragma unroll
    for (int p = 0; p < 10; ++p) {
      if (w == 0) acc[0] = mfma32(x[p], x[p], p ? acc[0] : Z4);
      if (w == 0 && p == 9) { ex = imax((int)((__builtin_amdgcn_readfirstlane(f2u(__builtin_amdgcn_sqrtf(cb2 * xmax))) >> 23) & 0xffu) + 1, 32); PIN_S(ex); }
#pragma unroll
      for (int i = (w == 1); i < 2 && w != 0; ++i) acc[i] = smfma64(x[p], W[i][p], acc[i], sidx);
      __builtin_amdgcn_sched_barrier(0);
      if (p + NX < 10) x[p + NX] = *(const bf16x8*)(lds + mp + (p + NX) * 1024);
      __builtin_amdgcn_sched_barrier(0);
    }
    STAMP(7);
    if (w != 0) {
      float v[4];
#pragma unroll
      for (int r = 0; r < 4; ++r) {
        const auto sw = __builtin_amdgcn_permlane32_swap(f2u(acc[w == 1][r]), f2u(acc[1][r]), false, false);
        v[r] = (u2f(sw[0]) + u2f(sw[1])) * inv;
      }
      *(u32x2*)(lds + vp) = (u32x2){pack2(v[0], v[1]), pack2(v[2], v[3])};
    } else {
      const int t = (int)f2u(fmx(fmx(acc[0][0], acc[0][1]), fmx(acc[0][2], acc[0][3])) * cm2);
      const int et = wave_imax(t) >> 23, em = ((et + ek) >> 1) + ei;
      int e = imax(et == 255 ? 256 : em, ex);
      PIN_S(e);
      e = imin(e, 256);
      ei = e - 31;
      *(unsigned*)(lds + ip) = (unsigned)ei << 23;
      const u32x3 sr = {(unsigned)(285 - e) << 23, (unsigned)(e - 87) < 79u ? REC_RNG << 23 : 0u, (unsigned)(s + 1)};
      asm volatile("ds_write_b96 %0, %1 offset:%2" :: "v"(lds0), "v"(sr), "n"(L_MISC + 32) : "memory");
    }
    if (last == 0 && w >= 2) asm volatile("s_waitcnt vmcnt(%0)" :: "n"(w == SCH_W ? 2 : 0) : "memory");
    if (w != 0) lds_inc<L_MISC + 28>(lds0);
    STAMP(8);
    if (last) __builtin_amdgcn_s_setprio(0);
    if (last && w == 2) { VM_DRAIN(); lds_inc<L_MISC + 28>(lds0); }
  };
  unsigned* const cntq = (unsigned*)(misc + 12);
  unsigned* const cntt = (unsigned*)(misc + 26 + (w & 1));
  unsigned* const cntp = (unsigned*)(misc + 23);
  unsigned* const cntx2 = (unsigned*)(misc + 28);
  auto wpf = [&](int s, int lane) {
    constexpr int PER = 81920 / QR / WPF_N;
    static_assert(PER % WPF_S == 0 && PER / WPF_S <= 64, "prefetch");
    touch(a.ws + OFF_WQKVBT + (size_t)imin(qkv_col(b), 14) * 163840 + (qkv_row(b) % (2 * QR)) * (81920 / QR), (s - WPF0) * PER + imin(lane, PER / WPF_S - 1) * WPF_S, lds0);
  };
  Sched sch;
  auto late = [&](int s, int lane) {
    if (__builtin_expect(s < 0, 0)) return;
    if (w == SCH_W && SCH_LATE && __builtin_expect(s >= 0, 1)) *(u32x2*)(misc + 24) = (u32x2){sch.run(a, rs, s, b, lane), (unsigned)(s + 1)};
    if ((w == 2 && F6) || (w == 0 && F4) || (w == 1 && F5) || (w == 3 && F7)) { lds_spin<L_MISC + 48, 1>(lds0, 2u * (unsigned)imax(s + 1, 0)); if (FB_SL && w != 2) __builtin_amdgcn_s_sleep(FB_SL); }
    if (w >= 2) { x_fetch_n<w == 2 ? F6 : F7>(x_src(a, s + 3, b), lds0, s + 3, F4 + F5 + 2 * F67 + (w == 3 ? F6 : 0), lane * 16); return; }
    unsigned sd = 0u;
    if ((w == 0 && XPF == 3) || XPF == 4 || (w == 1 && XPF == 5)) stouch<10>(a.ws + OFF_XN + (size_t)imin(s + 4, NSEG - 1) * (s < 1 ? 0 : 40960) + (b >> 3) * 1280 + w * 64, sd);
    x_fetch_n<w == 0 ? F4 : F5>(x_src(a, s + 3, b), lds0, s + 3, w == 0 ? 0 : F4, lane * 16);
    if (WPF_W == 0 && w == 0 && __builtin_expect(s >= WPF0 && s < WPF0 + WPF_N, 0)) wpf(s, lane);
    if (w == 1 && __builtin_expect(s == CPF_S, 0)) code_touch(b, lane, lds0);
    lds_spin<L_MISC + 104 + 4 * (w & 1), PS_SL>(lds0, (unsigned)imax(s + 1, 0));
    asm volatile("" :: "s"(sd));
  };
  if (w == 2) { ot.init(b, lane0); ot.out = (u32x2){0u, 0u}; ot.xo = ot.out; ot.y[0] = ot.y[1] = ot.y[2] = ot.y[3] = (u32x4){0u, 0u, 0u, 0u}; }
  VM_DRAIN();
  lds_arrive(cnt, lane0);
  lds_wait(cnt, 8u);
  sch.tsv = sch.rec = (u32x4){0u, 0u, 0u, 0u};
  sch.side = __builtin_amdgcn_readfirstlane(*(const unsigned*)(misc + 11)) & 8192u;
  sch.o = 0u;
  int sv = NV - 1;
  CODE_PAD((PADT >> (4 * w)) & 15);
#pragma unroll 1
  for (int s = -1; s < NSEG - 1; ++s) {
    const int sv1 = sv == NV - 1 ? 0 : sv + 1;
    FRESH_LANE();
    STAMP(0);
    mma_seg(s + 1, sv1, lane, [&]() { STAMP(1); late(s - 1, lane); if (w == SCH_W) sch.pre(s); STAMP(9); });
    STAMP(2);
    __builtin_amdgcn_sched_barrier(0);
    VM_DRAIN();
    STAMP(10);
    w1_fire(s, lane, std::integral_constant<int, 0>{});
    if (w >= 2) vo_pre(s, sv, lane);
    STAMP(3);
    BAR();
    STAMP(4);
    if (__builtin_expect(s >= 0, 1)) {
      if (w == SCH_W) { sch.load(rs, lane0); __builtin_amdgcn_sched_barrier(0); }
      if (w < 2) vo_pre(s, sv, lane);
      vo_mem(s, std::integral_constant<int, 0>{});
      STAMP(6);
      if (w == SCH_W && !SCH_LATE) {
        __builtin_amdgcn_s_setprio(SCH_P);
        *(u32x2*)(misc + 24) = (u32x2){sch.run(a, rs, s, b, lane0), (unsigned)(s + 1)};
      }
      STAMP(11);
    }
    __builtin_amdgcn_s_setprio(MMA_P);
    if (w == 2) VM_DRAIN();
    __builtin_amdgcn_sched_barrier(0);
    sv = sv1;
  }
  __builtin_amdgcn_s_setprio(0);
  STAMP_OUT();
  late(NSEG - 2, lane0);
  { FRESH_LANE(); if (w == 0) drain_pre(lds, b, lane); qkv_pre(vo, b, w, lane); }
#pragma unroll
  for (int k = 0; k < 20; ++k) w1_part(k, NSEG - 1, lane0);
  w1_fire(NSEG - 1, lane0, std::integral_constant<int, 1>{});
  if (w != 0) vo_pre(NSEG - 1, (NSEG - 1) % NV, lane0);
  BAR();
  if (w != 0) vo_mem(NSEG - 1, std::integral_constant<int, 1>{});
}

__device__ __forceinline__ void qkv_tail(const RecArgs& a, char* lds, const int w, const unsigned (&vo)[2][2]) {
  const int lane0 = __builtin_amdgcn_workitem_id_x() & 63, b = __builtin_amdgcn_workgroup_id_x();
  const int r = qkv_row(b), cb = qkv_col(b);
  if (cb >= 15) return;
  const unsigned lds0 = (unsigned)(uintptr_t)(LAS char*)lds;
  unsigned* const stage = (unsigned*)(lds + L_MISC) + 17;
  unsigned* const cnty = stage + 1;
  const f32x4 Z4 = {0.f, 0.f, 0.f, 0.f};
  auto fetch = [&](int i) {
    x_fetch_n<10>(x_src(a, NSEG + i, b), lds0, i, 10 * w, lane0 * 16);
  };
  unsigned want = 0;
  auto sync = [&]() { VM_DRAIN(); want += 4u; lds_inc<L_MISC + 72>(lds0); while (__hip_atomic_load(cnty, __ATOMIC_RELAXED, __HIP_MEMORY_SCOPE_WORKGROUP) < want) { } asm volatile("" ::: "memory"); };
  LOGQ(0);
  bf16x8 W[2][20];
  const __amdgpu_buffer_rsrc_t rw = __builtin_amdgcn_make_buffer_rsrc(a.ws + OFF_WQKVBT + (size_t)(cb * 8 + 2 * w) * 20480, 0, 40960, 0x00020000);
  auto w_get = [&](int ks) {
#pragma unroll
    for (int i = 0; i < 2; ++i) W[i][ks] = as8(__builtin_amdgcn_raw_buffer_load_b128(rw, lane0 * 16 + (ks & 3) * 1024, (i * 20 + (ks & ~3)) * 1024, 0));
    __builtin_amdgcn_sched_barrier(0);
  };
#pragma unroll
  for (int ks = 0; ks < QW; ++ks) w_get(ks);
  bf16x8 ones = mk8(0x3f803f80u, 0x3f803f80u, 0x3f803f80u, 0x3f803f80u);
  PIN_V(ones);
  const __amdgpu_buffer_rsrc_t ro = __builtin_amdgcn_make_buffer_rsrc(a.ws + OFF_QB, 0, (int)(OFF_OB - OFF_QB), 0x00020000);
  const int t0 = w & 1;
  auto body = [&](auto SW, auto SQ) {
    const float qs = cb < 5 ? 0.22360679774997896f * 1.4426950408889634f : 1.0f;
    f32x4 cs[2] = {Z4, Z4};
    LOGQ(1);
    f32x4 acc[2][2], st;
    auto mma = [&](int it, auto FIRST) {
      const char* xp0 = lds + L_XN + (it & 1) * 40960 + t0 * 20480 + lane0 * 16;
      const char* xp1 = lds + L_XN + (it & 1) * 40960 + (1 - t0) * 20480 + lane0 * 16;
      acc[0][0] = acc[0][1] = acc[1][0] = acc[1][1] = st = Z4;
      bf16x8 x[4][2];
#pragma unroll
      for (int p = 0; p < 3; ++p) { x[p][0] = *(const bf16x8*)(xp0 + p * 1024); x[p][1] = *(const bf16x8*)(xp1 + p * 1024); }
      __builtin_amdgcn_sched_barrier(0);
#pragma unroll
      for (int ks = 0; ks < 20; ++ks) {
        if (FIRST && ks + QW < 20) w_get(ks + QW);
#pragma unroll
        for (int q = 0; q < 4; ++q) {
          const int tt = 1 - (q >> 1), i = 1 - (q & 1);
          acc[i][tt] = SW ? mfma32(W[i][ks], x[ks & 3][tt], acc[i][tt]) : mfma32(x[ks & 3][tt], W[i][ks], acc[i][tt]);
          __builtin_amdgcn_sched_barrier(0);
          if (q < 2 && ks + 3 < 20) x[(ks + 3) & 3][q] = *(const bf16x8*)((q ? xp1 : xp0) + (ks + 3) * 1024);
          __builtin_amdgcn_sched_barrier(0);
        }
        st = mfma32(SQ ? x[ks & 3][0] : ones, x[ks & 3][0], st);
        if (FIRST) {
#pragma unroll
          for (int i = 0; i < 2; ++i) cs[i] = SW ? mfma32(W[i][ks], ones, cs[i]) : mfma32(ones, W[i][ks], cs[i]);
        }
        __builtin_amdgcn_sched_barrier(0);
      }
    };
    mma(0, std::integral_constant<bool, true>{});
#pragma unroll 1
    for (int it = 0; it < 4; ++it) {
      if (it) mma(it, std::integral_constant<bool, false>{});
      LOGQ(2 + 3 * it);
      FRESH_LANE();
      float* const sp = (float*)(lds + L_ST) + (it & 1) * 64;
      if (SQ) { if ((c >> 2) == g) sp[32 + t0 * 16 + c] = (c & 3) == 0 ? st[0] : ((c & 3) == 1 ? st[1] : ((c & 3) == 2 ? st[2] : st[3])); }
      else if (g == 0) sp[t0 * 16 + c] = st[0];
      sync();
      LOGQ(3 + 3 * it);
      if (it == 0) fetch(2);
      const bool late = r + 48 >= 55;
      if (it == 1 && !late) fetch(3);
      const unsigned so = (unsigned)((r + 16 * it) * (cb < 5 ? 64 * QST : KVT));
      auto stat = [&](float sm, float sq, float& mean, float& rstd) {
        mean = sm * (1.0f / DM);
        rstd = __builtin_amdgcn_rsqf(__builtin_fmaxf(sq * (1.0f / DM) - mean * mean, 0.f) + 1e-5f);
      };
#pragma unroll
      for (int tt = 0; tt < 2; ++tt) {
        const int tq = tt ^ t0;
        float mean[4], rstd[4];
        if (SW) {
          stat(sp[tq * 16 + c], sp[32 + tq * 16 + c], mean[0], rstd[0]);
          rstd[0] *= qs;
        } else {
          const f32x4 sm = *(const f32x4*)(sp + tq * 16 + 4 * g), sq = *(const f32x4*)(sp + 32 + tq * 16 + 4 * g);
#pragma unroll
          for (int k = 0; k < 4; ++k) stat(sm[k], sq[k], mean[k], rstd[k]);
        }
#pragma unroll
        for (int i = 0; i < 2; ++i) {
          float v[4];
#pragma unroll
          for (int k = 0; k < 4; ++k) v[k] = (acc[i][tt][k] - mean[SW ? 0 : k] * cs[i][SW ? k : 0]) * rstd[SW ? 0 : k];
          __builtin_amdgcn_raw_buffer_store_b64((u32x2){pack2(v[0], v[1]), pack2(v[2], v[3])}, ro, vo[i][tt], so, Q_AUX);
        }
      }
      LOGQ(4 + 3 * it);
      if (it == 2 && late) lds_spin<L_MISC + 68, 0>(lds0, 5u);
    }
  };
  typedef std::integral_constant<bool, false> F_; typedef std::integral_constant<bool, true> T_;
  if (cb < 10) { if (w >> 1) body(T_{}, T_{}); else body(T_{}, F_{}); }
  else { if (w >> 1) body(F_{}, T_{}); else body(F_{}, F_{}); }
}

__device__ __forceinline__ void t2_mem(const bf16x8 fr, const bf16x8 q0, const bf16x8 q1, const __amdgpu_buffer_rsrc_t rs, unsigned o, int h, int j, float inv, int lane) {
  const f32x4 Z4 = {0.f, 0.f, 0.f, 0.f};
  const f32x4 d0 = mfma32(fr, q0, Z4) * inv, d1 = mfma32(fr, q1, Z4) * inv;
  const unsigned a0 = pack2n(d0[0], d0[1]), a1 = pack2n(d0[2], d0[3]), b0 = pack2n(d1[0], d1[1]), b1 = pack2n(d1[2], d1[3]);
  const auto r0 = __builtin_amdgcn_permlane32_swap(a0, b0, false, false), r1 = __builtin_amdgcn_permlane32_swap(a1, b1, false, false);
  SC1_STORE8S(((u32x2){r0[1], r1[1]}), ts_mem(h, j) + (lane >> 5) * 256 + (lane & 12) * 16 + (lane & 16) * 2 + (lane & 3) * 8, o);
}

template <int w>
__device__ __forceinline__ void rec_waves(const RecArgs& a, char* lds, __amdgpu_buffer_rsrc_t rs, unsigned& tlast) {
  PIN_S(rs);
  const int lane = __builtin_amdgcn_workitem_id_x() & 63, b = __builtin_amdgcn_workgroup_id_x(), c = lane & 15, g = lane >> 4, wid = w; (void)wid;
  const int gp = (b & 7) * 2 + (b >> 7), m = (b >> 3) & 15, h0 = 2 * gp, h = h0 + (m >> 3), j = m & 7;
  float* const misc = (float*)(lds + L_MISC);
  const f32x4 Z4 = {0.f, 0.f, 0.f, 0.f};
  LOGP(0);
  __builtin_amdgcn_s_setprio(3);
  const unsigned lds0 = (unsigned)(uintptr_t)(LAS char*)lds;
  if (!a.layer) img_fetch<12>(a, lds0, h, j, w, lane);
  constexpr int NC = w < 2 ? 2 : 3;
  const int far = ((b >> 2) & 1) ^ 1;
  auto chunk = [&](int f) { return f == 0 ? 4 * far + w : (w == 2 && f == 1 ? 8 + far : (w == 3 && f == 2 ? 9 - far : 4 - 4 * far + w)); };
  bf16x16 Wqk[3][NC];
  u32x4 bm0[NC][2];
#pragma unroll
  for (int f = 0; f < 2 * NC; ++f) bm0[f >> 1][f & 1] = *(const u32x4*)(a.mem0 + (size_t)(c & 7) * DM + 64 * chunk(f >> 1) + 16 * g + 8 * (f & 1));
  if (!a.layer) asm volatile("s_waitcnt vmcnt(0)" ::: "memory");
  BAR();
  LOGP(9);
  const unsigned ck = __builtin_amdgcn_readfirstlane(*(const unsigned*)(misc + 11));
  const unsigned flip = (ck ^ (unsigned)(b << 11)) & 8192u;
#pragma unroll
  for (int t = 0; t < 3; ++t)
#pragma unroll
    for (int f = 0; f < NC; ++f) Wqk[t][f] = cat16(wqk_frag(lds, t, 2 * chunk(f), lane), wqk_frag(lds, t, 2 * chunk(f) + 1, lane));
  if (a.layer) {
    if (w == 0) {
      for (unsigned spins = 0; spins < SPIN_CAP; ++spins) {
        if (__builtin_amdgcn_raw_buffer_load_b32(rs, (b & 7) * 256, (unsigned)OFF_RDY, 16) >= 16u) break;
        __builtin_amdgcn_s_sleep(4);
        asm volatile("" ::: "memory");
      }
      lds_inc<L_MISC + 84>(lds0);
    }
    lds_wait((unsigned*)(misc + 21), 2u);
#pragma unroll
    for (int f = 0; f < 2 * NC; ++f) PIN_V(bm0[f >> 1][f & 1]);
#pragma unroll
    for (int i = 0; i < 10; ++i) x_fetch(x_src(a, 0, b), lds0, 0, 10 * w + i, lane);
  }
  const bf16x8 SelLo = sel_frag(c, 0);
  bf16x8 ONES = mk8(0x3f803f80u, 0x3f803f80u, 0x3f803f80u, 0x3f803f80u);
  if (w < 2) PIN_V(ONES);
  bool dead = false;
  float inv = 0.f;
  unsigned* const cnt2 = (unsigned*)(misc + 7);
  unsigned l2off[2 * NC];
#pragma unroll
  for (int f = 0; f < 2 * NC; ++f) { l2off[f] = acc_off(16 * chunk(f >> 1) + 4 * g + 2 * (f & 1) + (c >> 3), c & 7) ^ flip; PIN_V(l2off[f]); }
  int sidx = (c >> 3) ? 0xEEEE : 0x4444;
  PIN_V(sidx);
  const unsigned p1off = (unsigned)((w == 0 ? (h & 2) * 1024 + j * 256 + (h & 1) * 64 + (g & 1) * 8 : (h & 3) * 32768 + j * 512 + (g >> 1) * 256 + (g & 1) * 64) + (c & 4) * 32 + (c & 3) * 16);
  unsigned p1v = (c < 8) && (w != 0 || g >= 2) ? p1off : 0x7ffffff0u, p1cur = 0x7ffffff0u;
  PIN_V(p1v); PIN_V(p1cur);
  const int hd = (c >> 2) & 1, mr = hd ? 3 - (c & 3) : (c & 3);
  const unsigned l1off = (unsigned)(((h0 + hd) & 3) * 32768 + g * 512 + (w & 1) * 128 + (c >> 3) * 64 + mr * 16);
  const unsigned l1offm = (unsigned)((h0 & 2) * 1024 + (g + 4 * (c >> 3)) * 256 + (w & 1) * 128 + hd * 64 + mr * 16);
  const int plane = (lane & ~6) | ((lane & 2) << 1) | ((lane & 4) >> 1);
  unsigned acco = c < 8 ? (acc_off(64 * far + m * 4 + g, 4 * (w & 1) + mr) + (unsigned)(hd * 8)) ^ flip : 0x7ffffff0u;
  unsigned acco2 = c >= 8 ? (acc_off(64 - 64 * far + m * 4 + g, 4 * (w & 1) + mr) + (unsigned)(hd * 8)) ^ flip
                          : (c < 4 ? (acc_off(128 + m * 2 + (g & 1), 4 * (w & 1) + mr) + (unsigned)((g >> 1) * 8)) ^ flip : 0x7ffffff0u);
  if (w < 2) { PIN_V(acco); PIN_V(acco2); }
  unsigned bmo[NC];
#pragma unroll
  for (int f = 0; f < NC; ++f) { bmo[f] = (unsigned)(chunk(f) * 1024 + lane * 16); PIN_V(bmo[f]); }
  bf16x8 bm[NC];
  auto bm_put = [&](int f) { *(bf16x8*)(lds + L_BM + bmo[f]) = bm[f]; __builtin_amdgcn_sched_barrier(0); };
  f32x4 acc[3] = {Z4, Z4, Z4};
  const int rt2 = ((c >> 3) * 8 + 4 * (g & 1)) * 28 + 16 + (c & 7);
  unsigned redo[3] = {(unsigned)(4 * g * 28 + c), (unsigned)(g < 2 ? (4 * g + 2) * 28 + c : rt2), (unsigned)(g < 2 ? (4 * g + 10) * 28 + c : rt2 + 56)};
#pragma unroll
  for (int k = 0; k < 3; ++k) { redo[k] = L_RED1 + w * 1792 + redo[k] * 4; PIN_V(redo[k]); }
  auto red_put = [&]() {
    auto hs = [&](float x, float y) { const auto sw = __builtin_amdgcn_permlane32_swap(f2u(x), f2u(y), false, false); return u2f(sw[0]) + u2f(sw[1]); };
#pragma unroll
    for (int k = 0; k < 3; ++k) {
#pragma unroll
      for (int i = 0; i < 2; ++i) *(float*)(lds + redo[k] + i * 112) = k == 0 ? hs(acc[0][i], acc[1][i]) : hs(acc[k - 1][2 + i], acc[2][2 * k - 2 + i]);
      __builtin_amdgcn_sched_barrier(0);
    }
  };
#pragma unroll
  for (int f = 0; f < NC; ++f) {
    const u32x4 u = bm0[f][0], v = bm0[f][1];
    bm[f] = c < 8 ? mk8(u[0], u[2], v[0], v[2]) : mk8(u[1], u[3], v[1], v[3]);
    bm_put(f);
#pragma unroll
    for (int t = 0; t < 3; ++t) acc[t] = smfma64(bm[f], Wqk[t][f], acc[t], sidx);
  }
  red_put();
  VM_DRAIN();
  lds_arrive((unsigned*)(misc + 6), lane);
  lds_wait((unsigned*)(misc + 6), 8u);
  x_fetch_n<10>(x_src(a, 1, b), lds0, 1, 10 * w, lane * 16);
  LOGP(1);
  STAMP_DECL(); ARRIVE_DECL();
  bf16x8 fr, qx[2];
  auto kq_get = [&](int seg) {
    const char* p = lds + (w == 1 ? L_KX : L_QX) + (seg % NKQ) * 2048 + lane * 16;
    if (w == 1 || w == 2) { qx[0] = *(const bf16x8*)p; qx[1] = *(const bf16x8*)(p + 1024); }
  };
  unsigned qo4 = (unsigned)(L_RED1 + (w == 1 ? (c & 7) * 112 + (c >> 3) * 3584 : c * 112) + g * 16), qo2 = qo4 + 64u - (unsigned)(g * 8), qo2b = qo2 + 3584u;
  if (w < 3) { PIN_V(qo4); PIN_V(qo2); if (w != 1) PIN_V(qo2b); }
  auto pub1 = [&](unsigned off, unsigned o1) {
    if (w == 0) fr = qk_frag(lds + qo4, lds + qo2, lds + qo2b);
    else {
      const f32x4 a0 = *(const f32x4*)(lds + qo4), a1 = *(const f32x4*)(lds + qo4 + 1792);
      const f32x2 b0 = *(const f32x2*)(lds + qo2), b1 = *(const f32x2*)(lds + qo2 + 1792);
      float v[6];
#pragma unroll
      for (int i = 0; i < 6; ++i) v[i] = i < 4 ? a0[i] + a1[i] : b0[i - 4] + b1[i - 4];
#pragma unroll
      for (int i = 0; i < 6; ++i) v[i] += dppf<DPP_ROR8>(v[i]);
      fr = mk8(pack2(v[0], v[1]), pack2(v[2], v[3]), pack2(v[4], v[5]), 0u);
    }
    const f32x4 sc = mfma32(w == 0 ? fr : qx[0], fr, Z4);
    if (w == 0) SC1_STORE8S(((u32x2){pack2n(sc[0], sc[1]), pack2n(sc[2], sc[3])}), off, o1);
    else {
      const f32x4 s2 = mfma32(qx[1], fr, Z4);
      __builtin_amdgcn_raw_buffer_store_b128((u32x4){pack2n(sc[0], sc[1]), pack2n(sc[2], sc[3]), pack2n(s2[0], s2[1]), pack2n(s2[2], s2[3])}, rs, off, o1, 16);
    }
  };
  unsigned o1 = sct_slot(ck, 0u), o1m = scm_slot(ck, 0u);
  unsigned t2o = ts_tok(h, j) + (unsigned)((c >> 2) * 256 + g * 64 + (c & 3) * 16);
  TokScores t2;
  TokTask tk;
  PvTask2 pv2;
  if (w == 2) tk.init(h, j, lane);
  if (w == 3) { PIN_V(t2o); pv2.init(gp, lane); }
  if (w == 0) pub1(p1v, o1m);
  VM_DRAIN();
  BAR();
  const unsigned tst = now32() | 1u;
  if (w == 3 && ST_X) __builtin_amdgcn_raw_buffer_store_b32(tst, rs, lane < 2 ? ts_piece(b >> 2) - ACC_TS + (b & 3) * 4 + lane * 8192 : 0x7ffffff0, (unsigned)(OFF_ACC + (SR - 1) * ACC_STEP) + ACC_TS, 16);
  LOGP(2);
  kq_get(0);
  if (w == 1) pub1(p1v, o1);
  x_fetch_n<10>(x_src(a, 2, b), lds0, 2, 10 * w, lane * 16);
  if (w == 3) { t2.get(lds, 0, lane); t2.run(0); t2.run(1); t2.store(rs, t2o, ts_slot(ck, 0)); }
  LOGP(3);
  unsigned o2 = (unsigned)OFF_ACC, o2p = 0u, tw = 0u;
  asm volatile("" : "+s"(o2p));
  unsigned tc0 = 0u, tstar = 0u;
  int dmin = 0, dmax = 0;
  int e1 = 100000;
  unsigned offs = 100u;
  unsigned o4 = 0u;
  unsigned o3, o3p;
  u32x16 P = {0u, 0u, 0u, 0u, 0u, 0u, 0u, 0u, 0u, 0u, 0u, 0u, 0u, 0u, 0u, 0u}, V = P;
  u32x8 V8 = {0u, 0u, 0u, 0u, 0u, 0u, 0u, 0u};
  if (w < 2) asm volatile("" : "+{v[208:223]}"(P), "+{v[184:191]}"(V8), "+{v[192:207]}"(V));
  unsigned tso = lane < 2 ? ts_piece(b >> 2) + (unsigned)((b & 3) * 4 + lane * 8192) : 0x7ffffff0u;
  if (w < 2) PIN_V(tso);
  float minf = g >= 2 ? -INFINITY : 0.f;
  if (w < 2) PIN_V(minf);
  int sv = NV - 1;
  bf16x8 vox[5];
  unsigned vma[3], need2;
  u32x4 slo = {0u, 0u, 0u, 0u}, shi = slo;
  float k1 = 0.f;
  const u16* xsrc;
  float invv = 1.0f;
  auto prep0 = [&](int s) {
    xsrc = x_src(a, s + 3, b);
    if (w == 2) { o3p = ts_slot(ck, (s + 1 - LAG3) & 3); o3 = ts_slot(ck, imax(s, 0)); o4 = pt_slot(ck, imax(s - LAG3, 0)); PIN_S(o3); }
    if (w == 3) { o3p = pt_slot(ck, (s - LAG4) & 3); o4 = ts_slot(ck, s + 1); }
    PIN_S(xsrc); PIN_S(o3p); PIN_S(o4);
    __builtin_amdgcn_sched_barrier(0);
  };
  auto prep = [&](int s) {
    o2 = (unsigned)OFF_ACC + (unsigned)(s % SR) * (unsigned)ACC_STEP;
    sv = sv == NV - 1 ? 0 : sv + 1;
    PIN_S(o2); PIN_S(sv);
    if (w < 2) {
      PIN_S(tc0);
      o1 = sct_slot(ck, s % SR); o1m = scm_slot(ck, s % SR);
      need2 = 4u * (unsigned)(s + 1) + 2u * ((unsigned)(s + 2) / NSEG);
      PIN_S(o1); PIN_S(o1m); PIN_S(need2);
      __builtin_amdgcn_sched_barrier(0);
      kq_get(s);
    }
    invv = misc[4 + (s & 1)];
  };
  auto prep2 = [&](int s) {
#pragma unroll
    for (int k = 0; k < 3; ++k) {
      const int t = k == 0 ? 2 : (k == 1 ? far : 1 - far);
      const char* p = lds + L_VOX + sv * 5120 + t * 1024 + (plane & 47) * 16 + (g & 1) * 8;
      auto rd = [&](const char* q) { const u32x2 a0 = *(const u32x2*)q, a1 = *(const u32x2*)(q + 256); return mk8(a0[0], a0[1], a1[0], a1[1]); };
      vox[k == 0 ? 0 : 2 * k - 1] = rd(p);
      if (k) vox[2 * k] = rd(p + 3072);
      vma[k] = lds0 + (unsigned)(L_VOM + sv * 1280 + t * 256 + (plane & 31) * 8);
      PIN_V(vma[k]);
    }
    __builtin_amdgcn_sched_barrier(0);
    if (F0A) { xsrc = x_src(a, s + 3, b); PIN_S(xsrc); }
    const unsigned iu = __builtin_amdgcn_readfirstlane(f2u(invv));
    if (__builtin_expect(iu != f2u(inv), 0)) {
      inv = u2f(iu);
#pragma unroll
      for (int i = 0; i < 8; ++i) {
        u32x4& m = i < 4 ? slo : shi;
        m[i & 3] = (c >> 1) == i ? ((c & 1) ? iu & 0xffff0000u : iu >> 16) : 0u;
      }
      k1 = KEXP * inv;
    }
    PIN_V(k1); PIN_V(slo); PIN_V(shi);
    asm volatile("" : "+v"(vox[0]), "+v"(vox[1]), "+v"(vox[2]), "+v"(vox[3]), "+v"(vox[4]));
    __builtin_amdgcn_sched_barrier(0);
  };
  auto rows = [&](int s) {
    const int dl = spin_until(tw);
    if (w < 2) tc0 = cyc32();
    __builtin_amdgcn_sched_barrier(0);
    STAMP(1);
    unsigned spins = 0;
    auto pass = [&](bool first) {
      u32x4 x[2 * NC];
#pragma unroll
      for (int f = 0; f < NC; ++f) {
        if (f && w < 2 && first) { prep(s); asm volatile(".rept %0\n\ts_nop 3\n\t.endr" :: "n"(P2_G1)); }
        else if (f) asm volatile(".rept %0\n\ts_nop 3\n\t.endr" :: "n"(w < 2 ? P2_G0 : (f == 1 ? P2_G2 : P2_G3)));
        __builtin_amdgcn_sched_barrier(0);
        x[2 * f] = SC1_LOADS(l2off[2 * f], o2p); x[2 * f + 1] = SC1_LOADS(l2off[2 * f + 1], o2p);
        __builtin_amdgcn_sched_barrier(0);
      }
      if (first) lds_inc<L_MISC + 92>(lds0);
      if (first && w < 2 && PR_P != 3) __builtin_amdgcn_s_setprio(PR_P);
      if (first) { if (w >= 2) prep(s); else prep2(s); }
      if (w >= 2 && first && P2_W) lds_spin<L_MISC + 92, 0>(lds0, 4u * (unsigned)s);
      if (w == 2 && first) tk.store(rs, o4, s > LAG3 ? 0u : 0x7ff00000u);
      if (w == 3 && first) t2.store(rs, t2o, o4);
      if (first && w < 2) {
        p1cur = p1v; offs = (offs - (unsigned)dl) & ~3u;
        PIN_V(p1cur); PIN_S(offs);
        tstar = tc0 + (unsigned)(e1 + D1_M + (w == 1 ? D1_W1 : 0)) - offs * 20u;
        dmin = offs < 40u ? D1_MIN : REC_D1;
        dmin = s < EARLY_N ? REC_D1 + EARLY_D1 : dmin;
        dmax = REC_D1;
        PIN_S(tstar); PIN_S(dmin); PIN_S(dmax);
      }
      __builtin_amdgcn_sched_barrier(0);
      unsigned ok = 32u;
#pragma unroll
      for (int t = 0; t < 3; ++t) { acc[t] = Z4; PIN_V(acc[t]); }
      if (first && w < 2 && PR_P != 3) __builtin_amdgcn_s_setprio(3);
      __builtin_amdgcn_sched_barrier(0);
      bool r = true;
#pragma unroll
      for (int f = 0; f < NC; ++f) {
        unsigned p[4];
#pragma unroll
        for (int k = 0; k < 2; ++k) {
          const u32x4 v = x[2 * f + k];
          p[2 * k] = pack2((float)(int)v[0], (float)(int)v[1]); p[2 * k + 1] = pack2((float)(int)v[2], (float)(int)v[3]);
          asm volatile("" : "+v"(p[2 * k]), "+v"(p[2 * k + 1]));
          __builtin_amdgcn_sched_barrier(0);
        }
        ok = (ok & x[2 * f][0] & x[2 * f][2]) & (x[2 * f + 1][0] & x[2 * f + 1][2]);
        PIN_V(ok);
        __builtin_amdgcn_sched_barrier(0);
        bm[f] = mk8(p[0], p[1], p[2], p[3]);
        acc[0] = smfma64(bm[f], Wqk[0][f], acc[0], sidx);
        __builtin_amdgcn_sched_barrier(0);
        bm_put(f);
        acc[1] = smfma64(bm[f], Wqk[1][f], acc[1], sidx);
        __builtin_amdgcn_sched_barrier(0);
        if (f == NC - 1) { u64 bad = __builtin_amdgcn_ballot_w64(ok == 0u); PIN_S(bad); r = bad == 0ull; }
        __builtin_amdgcn_sched_barrier(0);
        acc[2] = smfma64(bm[f], Wqk[2][f], acc[2], sidx);
        __builtin_amdgcn_sched_barrier(0);
      }
      red_put();
      return r;
    };
    if (__builtin_expect(!pass(true), 0)) {
      FC(0);
      do {
        if (dead || ++spins > SPIN_CAP) { dead = true; break; }
        asm volatile("" ::: "memory");
      } while (!pass(false));
    }
    STAMPV(12, spins); LOGF(1, spins);
    TSTAMP(2, now32()); TSTAMP(3, spins);
  };
  if (w >= 2) prep0(-1);
  prep(0);
  if (w < 2) prep2(0);
  VM_DRAIN();
  CODE_PAD((PADR >> (4 * w)) & 15);
#pragma unroll 1
  for (int s = 0;; ++s) {
    if (__builtin_expect(o2p != 0u, 1)) rows(s);
    if (w < 2 && __builtin_expect(s == NSEG - 1, 0)) break;
    STAMP(2);
    BAR();
    STAMP(3);
    if (w < 2) pub1(p1cur, w == 0 ? o1m : o1);
    if (w == 2) tk.load(rs, o3p);
    if (w == 3) pv2.load(rs, o3p);
    const unsigned tp1 = cyc32();
    __builtin_amdgcn_sched_barrier(0);
    if (w == 2) {
      __builtin_amdgcn_s_setprio(W2_P);
      prep0(s);
      if (W2_D) spin_cyc(tp1 + W2_D);
      fr = qk_frag(lds + qo4, lds + qo2, lds + qo2b);
      t2_mem(fr, qx[0], qx[1], rs, o3, h, j, invv * KEXP, lane);
      __builtin_amdgcn_sched_barrier(0);
      asm volatile("s_waitcnt vmcnt(1)" ::: "memory");
      lds_inc<L_MISC + 28>(lds0);
      kq_get(s + 1);
    }
    if (w == 3) {
      __builtin_amdgcn_s_setprio(T4_P);
      prep0(s);
      t2.get(lds, s + 1, lane); t2.run(0); t2.run(1);
      __builtin_amdgcn_sched_barrier(0);
    }
    if (w >= 2 && __builtin_expect(s == NSEG - 2, 0)) { VM_DRAIN(); lds_inc<L_MISC + 28>(lds0); }
    if (w >= 2) __builtin_amdgcn_s_setprio(UN_P);
    TSTAMP(0, now32());
    STAMP(4);
    __builtin_amdgcn_sched_barrier(0);
    unsigned tpub = 0;
    u32x2 sw;
    if (w == 2) {
      STAMP(5);
      __builtin_amdgcn_sched_barrier(0);
      STAMP(6);
      ARRIVE(0);
      tk.run(j, lane, SelLo);
      if (__builtin_expect(s == NSEG - 1, 0)) { tlast = tp1; break; }
      if (F2B) { lds_spin<L_MISC + 48, 1>(lds0, 2u * (unsigned)(s + 1)); x_fetch_n<F2B>(xsrc, lds0, s + 3, P2B, lane * 16); }
      __builtin_amdgcn_s_setprio(SP_P);
    }
    if (w == 3) {
      pv2.run<false>(lds, sv < 1 + LAG4 ? sv + NV - 1 - LAG4 : sv - 1 - LAG4, lane, rs, 0u);
      if (__builtin_expect(s == NSEG - 1, 0)) { tlast = tp1; break; }
      lds_inc<L_MISC + 112>(lds0);
      STAMP(11);
      __builtin_amdgcn_s_setprio(SP_P);
      STAMP(5);
      ARRIVE(0);
      if (F3) { lds_spin<L_MISC + 48, 1>(lds0, 2u * (unsigned)(s + 1)); x_fetch_n<F3>(xsrc, lds0, s + 3, 40 - F3C - F3, lane * 16); }
      STAMP(6);
    }
    if (w < 2) {
      __builtin_amdgcn_s_setprio(D1_P);
      x_fetch_n<F0A>(xsrc, lds0, s + 3, PA + w * F0A, lane * 16);
      int dq = imin((int)(tstar - tp1), dmax);
      dq = dq < -D1_LATE ? dmax : dq;
      PIN_S(dq);
      spin_exact(tp1 + (unsigned)imax(dq, dmin));
      STAMP(5);
      u32x32 S;
      auto sc_load = [&]() {
        asm volatile("buffer_load_dwordx4 v[224:227], %2, %4, %5 offen sc1\n\t.rept %7\n\ts_nop 3\n\t.endr\n\t"
                     "buffer_load_dwordx4 v[228:231], %3, %4, %6 offen sc1\n\tbuffer_load_dwordx4 v[232:235], %3, %4, %6 offen offset:2048 sc1\n\t"
                     "buffer_load_dwordx4 v[236:239], %3, %4, %6 offen offset:256 sc1\n\tbuffer_load_dwordx4 v[240:243], %3, %4, %6 offen offset:2304 sc1\n\t"
                     "v_mov_b32 v220, 0x42000000\n\tv_mov_b32 v221, 0\n\tv_mov_b32 v222, 0x42000000\n\tv_mov_b32 v223, 0"
                     : "=&{v[224:255]}"(S), "+{v[208:223]}"(P) : "v"(l1offm), "v"(l1off), "s"(rs), "s"(o1m), "s"(o1), "n"(P1_G) : "memory");
      };
      sc_load();
      __builtin_amdgcn_sched_barrier(0);
      lds_inc<L_MISC + 48>(lds0);
      STAMP(6);
      ARRIVE(0);
      lds_spin<L_MISC + 28, 0>(lds0, need2);
      __builtin_amdgcn_s_setprio(3);
      asm volatile("ds_read_b64 v[192:193], %3\n\tds_read_b64 v[196:197], %3 offset:768\n\tds_read_b64 v[200:201], %4\n\tds_read_b64 v[204:205], %4 offset:768\n\tds_read_b64 v[184:185], %2"
                   : "+{v[184:191]}"(V8), "+{v[192:207]}"(V) : "v"(vma[0]), "v"(vma[1]), "v"(vma[2]) : "memory");
      __builtin_amdgcn_sched_barrier(0);
      {
        const int de = imin((int)(tp1 - tc0) - e1, D1_C);
        e1 += s == 0 ? 0 : de >> (s < EARLY_N && de < -D1_C ? 0 : D1_G);
        PIN_S(e1);
      }
      unsigned sn = (unsigned)(s + 1);
      PIN_V(sn);
      STAMP(11);
      __builtin_amdgcn_sched_barrier(0);
      {
        unsigned spins = 0;
        u64 miss, bad;
#define MM(d, a, b, c) "v_mfma_f32_16x16x32_bf16 " d ", " a ", " b ", " c "\n\t"
#define FM(d, x, r, ctrl) "v_fmac_f32_dpp " d ", " x ", " r " " ctrl "\n\t"
#define ID_ "quad_perm:[0,1,2,3] row_mask:0xf bank_mask:"
#define HM_ "row_half_mirror row_mask:0xf bank_mask:"
#define R8_ "row_ror:8 row_mask:0xf bank_mask:"
#define MI_ "row_mirror row_mask:0xf bank_mask:"
        auto soft = [&]() {
          asm volatile(
            "s_waitcnt vmcnt(4)\n\t" MM("v[244:247]", "%6", "v[224:227]", "0") "v_min3_u32 v214, v224, v225, v226\n\t"
            "s_waitcnt vmcnt(3)\n\t" MM("v[248:251]", "%6", "v[228:231]", "0") "v_min3_u32 v214, v214, v227, v228\n\tv_min3_u32 v214, v214, v229, v230\n\t"
            "s_waitcnt vmcnt(2)\n\t" MM("v[248:251]", "%6", "v[232:235]", "v[248:251]") "v_min3_u32 v214, v214, v231, v232\n\t"
            "v_add_f32_dpp v212, v244, v244 " R8_ "0x3\n\tv_add_f32_dpp v213, v245, v245 " R8_ "0x3\n\t"
            "s_waitcnt vmcnt(1)\n\t" MM("v[248:251]", "%7", "v[236:239]", "v[248:251]")
            "v_add_f32_dpp v212, v246, v246 " R8_ "0xc\n\tv_add_f32_dpp v213, v247, v247 " R8_ "0xc\n\t"
            "s_waitcnt vmcnt(0) lgkmcnt(0)\n\t" MM("v[248:251]", "%7", "v[240:243]", "v[248:251]")
            "v_fma_f32 v212, v212, %14, %15\n\tv_fma_f32 v213, v213, %14, %15\n\tv_exp_f32 v216, v212\n\tv_exp_f32 v217, v213\n\t"
            "v_min3_u32 v214, v214, v233, v234\n\tv_cvt_pk_bf16_f32 v208, v216, v217\n\tv_min3_u32 v214, v214, v235, v236\n\tds_read_b96 v[188:190], %16 offset:%18\n\t"
            "v_exp_f32 v216, v248\n\tv_mov_b32_dpp v209, v208 " R8_ "0xf\n\tv_exp_f32 v217, v249\n\tv_exp_f32 v218, v250\n\t"
            MM("v[252:255]", "%8", "v[208:211]", "0") "v_exp_f32 v219, v251\n\tv_cvt_pk_bf16_f32 v232, v216, v217\n\t"
            MM("v[224:227]", "v[192:195]", "v[208:211]", "0") "v_cvt_pk_bf16_f32 v233, v218, v219\n\tv_min3_u32 v214, v214, v237, v238\n\tv_mov_b32_dpp v234, v232 " R8_ "0xf\n\t"
            MM("v[228:231]", "v[196:199]", "v[208:211]", "0") "v_mov_b32_dpp v235, v233 " R8_ "0xf\n\t"
            MM("v[244:247]", "v[204:207]", "v[208:211]", "0") "v_min3_u32 v214, v214, v239, v240\n\t"
            MM("v[252:255]", "%8", "v[232:235]", "v[252:255]") MM("v[224:227]", "%10", "v[232:235]", "v[224:227]") "v_min3_u32 v214, v214, v241, v242\n\t"
            MM("v[228:231]", "%11", "v[232:235]", "v[228:231]") "s_waitcnt lgkmcnt(0)\n\tv_sub_u32 v215, v190, %17 clamp\n\t"
            MM("v[236:239]", "v[200:203]", "v[208:211]", "0") "v_min3_u32 v214, v214, v243, v215\n\tv_cmp_eq_u32 %4, 0, v214\n\tv_rcp_f32 v216, v252\n\t"
            MM("v[240:243]", "v[184:187]", "v[208:211]", "0") "v_mul_f32 v216, v216, v188\n\tv_subrev_u32 v215, 0x2f800000, v252\n\t"
            MM("v[236:239]", "%12", "v[232:235]", "v[236:239]") FM("v220", "v224", "v216", ID_ "0x1") FM("v221", "v225", "v216", ID_ "0x1")
            "v_mov_b32_dpp v217, v216 " HM_ "0xf\n\t"
            MM("v[244:247]", "%13", "v[232:235]", "v[244:247]") FM("v220", "v230", "v216", ID_ "0x2") FM("v221", "v231", "v216", ID_ "0x2")
            MM("v[240:243]", "%9", "v[232:235]", "v[240:243]") FM("v220", "v228", "v217", HM_ "0x1") FM("v221", "v229", "v217", HM_ "0x1")
            "v_cmp_ge_u32 vcc, v215, v189\n\t" FM("v220", "v226", "v217", HM_ "0x2") FM("v221", "v227", "v217", HM_ "0x2")
            "s_or_b64 %5, %4, vcc"
            : "+{v[224:255]}"(S), "+{v[208:223]}"(P), "+{v[184:191]}"(V8), "+{v[192:207]}"(V), "=&s"(miss), "=&s"(bad)
            : "v"(slo), "v"(shi), "v"(ONES), "v"(vox[0]), "v"(vox[1]), "v"(vox[2]), "v"(vox[3]), "v"(vox[4]), "v"(k1), "v"(minf), "v"(lds0), "s"(s), "n"(L_MISC + 32)
            : "vcc", "scc", "memory");
        };
        soft();
        if (__builtin_expect(bad != 0ull, 0)) {
          if (miss) FC(1); else FC(2);
          while (miss != 0ull && !dead) {
            if (++spins > SPIN_CAP) dead = true;
            asm volatile("s_nop 7\n\ts_nop 7" ::: "memory");
            sc_load(); soft();
          }
          if (bad != 0ull) {
            u32x4 y[5];
#pragma unroll
            for (int k = 0; k < 4; ++k) y[k] = SC1_LOADS(l1off + (k & 1) * 2048 + (k >> 1) * 256, o1);
            y[4] = SC1_LOADS(l1offm, o1m);
            const f32x4 d0 = mfma32(as8(slo), as8(y[4]), Z4);
            f32x4 d1 = mfma32(as8(shi), as8(y[3]), mfma32(as8(shi), as8(y[2]), mfma32(as8(slo), as8(y[1]), mfma32(as8(slo), as8(y[0]), Z4))));
            const bool lo = c < 8;
            const float m0 = __builtin_fmaf((lo ? d0[0] : d0[2]) + dppf<DPP_ROR8>(lo ? d0[2] : d0[0]), k1, minf);
            const float m1 = __builtin_fmaf((lo ? d0[1] : d0[3]) + dppf<DPP_ROR8>(lo ? d0[3] : d0[1]), k1, minf);
            float mx = fmx(fmx(fmx(d1[0], d1[1]), d1[2]), fmx(fmx(d1[3], m0), m1));
            mx = fmx(mx, dppf<DPP_ROR8>(mx));
            mx = x32mx(x16mx(mx));
            d1 -= mx;
            const unsigned mm = pack2(__builtin_amdgcn_exp2f(m0 - mx), __builtin_amdgcn_exp2f(m1 - mx));
            const unsigned t01 = pack2(__builtin_amdgcn_exp2f(d1[0]), __builtin_amdgcn_exp2f(d1[1])), t23 = pack2(__builtin_amdgcn_exp2f(d1[2]), __builtin_amdgcn_exp2f(d1[3]));
            const bf16x8 pm = mk8(mm, dpp_ror8(mm), 0u, 0u), pt = mk8(t01, t23, dpp_ror8(t01), dpp_ror8(t23));
            const float r = __builtin_amdgcn_rcpf(mfma32(ONES, pt, mfma32(ONES, pm, Z4))[0]) * u2f(V8[4]), rm = dppf<0x141>(r);
            f32x4 o[5];
#pragma unroll
            for (int k = 0; k < 5; ++k) o[k] = mfma32(vox[k], pt, mfma32(k ? mk8(V[4 * k - 4], V[4 * k - 3], 0u, 0u) : mk8(V8[0], V8[1], 0u, 0u), pm, Z4));
#pragma unroll
            for (int i = 0; i < 2; ++i) {
              const float oa = dppf<0x141>(o[2][i]), ob = dppf<0x141>(o[1][2 + i]);
              P[12 + i] = f2u(__builtin_fmaf(hd ? ob : oa, rm, __builtin_fmaf(hd ? o[2][2 + i] : o[1][i], r, i ? 0.f : 32.0f)));
            }
            P[8] = f2u(r); P[9] = f2u(rm);
#pragma unroll
            for (int i = 0; i < 4; ++i) { S[12 + i] = f2u(o[3][i]); S[16 + i] = f2u(o[0][i]); S[20 + i] = f2u(o[4][i]); }
            asm volatile("s_nop 7\n\ts_nop 7" : "+{v[224:255]}"(S), "+{v[208:223]}"(P));
          }
        }
        STAMP(7); STAMPV(13, spins); LOGF(0, spins);
        {
          asm volatile(".if %11 == 0\n\tds_write_b32 %8, %12 offset:%10\n\t.endif\n\t"
                       "v_cvt_i32_f32 v218, v220\n\tv_cvt_i32_f32 v219, v221\n\tv_and_or_b32 v218, v218, %7, 2\n\tbuffer_atomic_add_x2 v[218:219], %3, %5, %6 offen\n\t"
                       ".if %11 == 1\n\tds_write_b32 %8, %12 offset:%10\n\t.endif\n\t"
                       FM("v222", "v236", "v216", R8_ "0x4") FM("v223", "v237", "v216", R8_ "0x4") "ds_read_b64 %2, %8 offset:%9\n\t"
                       FM("v222", "v238", "v217", MI_ "0x8") FM("v223", "v239", "v217", MI_ "0x8") "v_permlane32_swap_b32 v240, v242\n\t"
                       FM("v222", "v244", "v217", MI_ "0x4") FM("v223", "v245", "v217", MI_ "0x4") "v_permlane32_swap_b32 v241, v243\n\t"
                       FM("v222", "v246", "v216", R8_ "0x8") FM("v223", "v247", "v216", R8_ "0x8") ".if %11 == 2\n\tds_write_b32 %8, %12 offset:%10\n\t.endif\n\t"
                       FM("v222", "v240", "v216", ID_ "0x1") FM("v223", "v241", "v216", ID_ "0x1") FM("v222", "v242", "v217", HM_ "0x1") FM("v223", "v243", "v217", HM_ "0x1")
                       "v_cvt_i32_f32 v212, v222\n\tv_cvt_i32_f32 v213, v223\n\tv_and_or_b32 v212, v212, %7, 2\n\tbuffer_atomic_add_x2 v[212:213], %4, %5, %6 offen\n\t"
                       ".if %11 == 3\n\tds_write_b32 %8, %12 offset:%10\n\ts_waitcnt lgkmcnt(1)\n\t.else\n\ts_waitcnt lgkmcnt(0)\n\t.endif"
                       : "+{v[224:255]}"(S), "+{v[208:223]}"(P), "=&v"(sw) : "v"(acco), "v"(acco2), "s"(rs), "s"(o2), "s"(~63u), "v"(lds0), "n"(L_MISC + 96),
                         "n"(L_MISC + 104 + 4 * (w & 1)), "n"(REL_AT), "v"(sn) : "memory");
          __builtin_amdgcn_sched_barrier(0);
          tpub = now32();
          asm volatile("buffer_atomic_umax %0, %1, %2, %3 offen" :: "v"(tpub | 1u), "v"(tso), "s"(rs), "s"(o2) : "memory");
          STAMP(10);
        }
      }
    }
    STAMP(8);
    ARRIVE(1);
    o2p = o2;
    PIN_S(o2p);
    if (w < 2) {
      if (__builtin_expect(__builtin_amdgcn_readfirstlane(sw[1]) <= (unsigned)s, 0)) {
        lds_spin<L_MISC + 100, 0>(lds0, (unsigned)s + 1u);
        sw[0] = __hip_atomic_load((unsigned*)(misc + 24), __ATOMIC_RELAXED, __HIP_MEMORY_SCOPE_WORKGROUP);
      }
    } else {
      u32x4 q;
      asm volatile("1:\n\tds_read_b128 %0, %1 offset:%3\n\t.if %4\n\ts_sleep 1\n\t.endif\n\ts_waitcnt lgkmcnt(0)\n\tv_min3_u32 v201, v201, v202, v203\n\tv_cmp_ge_u32 vcc, %2, v201\n\ts_cbranch_vccnz 1b"
                   : "=&{v[200:203]}"(q) : "v"(lds0), "s"(s), "n"(L_MISC + 96), "n"(SP_SL) : "vcc", "memory");
      __builtin_amdgcn_s_setprio(3);
      tpub = now32();
      sw[0] = q[0];
    }
    STAMP(9);
    static_assert(F3C == 0 && F2C == 0 && F0C == 0, "nothing behind the atomics");
    LOGT(0, tpub); LOGT(1, tb2_);
    TSTAMP(1, tpub);
    ARRIVE_OUT();
    const unsigned an = __builtin_amdgcn_readfirstlane(sw[0]);
    const int ahead = (int)(an - tpub);
    tw = tpub + (unsigned)(an == 0u || ahead > 1000 ? SCH_FIRST : imax(ahead, w < 2 ? SCH_DMIN : 0));
    offs = tw - an;
  }
  STAMP_OUT();
  LOGP(4);
  __builtin_amdgcn_s_setprio(3);
  if (w < 2) { BAR(); tlast = cyc32(); }
  if (w == 2) tk.store(rs, o4);
  if (w == 3) {
    const unsigned oo = op_slot(NSEG - 2 - LAG4) + (unsigned)((m * 32 + gp) * 1280);
    const char* sp = lds + L_STG + 2048;
    const u32x4 v0 = *(const u32x4*)(sp + lane * 16), v1 = *(const u32x4*)(sp + 1280 + lane * 16), v2 = *(const u32x4*)(sp + (g & 1) * 1280 + 1024 + c * 16);
    SC1_STORE(v0, oo + lane * 16); SC1_STORE(v1, oo + 20480 + lane * 16); SC1_STORE(v2, lane < 32 ? oo + g * 20480 + 1024 + c * 16 : 0x7ffffff0u);
  }
  if (dead) tlast |= 1u; else tlast &= ~1u;
}

__device__ __forceinline__ void drain(const RecArgs& a, char* lds, const __amdgpu_buffer_rsrc_t rs, const int w, const unsigned tp1) {
  static_assert(LAG3 == 2 && LAG4 == 3 && LAG5 == 6, "drain schedule");
  const int lane = __builtin_amdgcn_workitem_id_x() & 63, b = __builtin_amdgcn_workgroup_id_x(), c = lane & 15;
  const int gp = (b & 7) * 2 + (b >> 7), m = (b >> 3) & 15, j = m & 7;
  unsigned* const misc = (unsigned*)(lds + L_MISC);
  const unsigned lds0 = (unsigned)(uintptr_t)(LAS char*)lds, ck = __builtin_amdgcn_readfirstlane(misc[11]);
  bool dead = tp1 & 1u;
  auto poll = [&](auto load, auto ready) {
    for (unsigned spins = 0;;) {
      asm volatile("" ::: "memory");
      load();
      if (ready() || dead) break;
      if (++spins > SPIN_CAP) dead = true;
      if (END_SL) __builtin_amdgcn_s_sleep(END_SL);
    }
  };
  LOGD(0);
#define TASK(ty, sg, tm) ((u64)((ty) | ((sg) - 56) << 2 | ((tm) / 100) << 5))
  u64 todo = w == 0 ? TASK(1, 63, END_E1) | TASK(2, 63, END_E2) << 12 | TASK(3, 62, END_E3) << 24
           : w == 1 ? TASK(1, 62, 0) | TASK(2, 62, END_E62) << 12 | TASK(3, 60, END_E3) << 24
           : w == 2 ? TASK(2, 60, 0) | TASK(3, 56, 0) << 12 : TASK(2, 61, END_E61) | TASK(3, 58, END_E59) << 12;
  TokTask tk;
  PvTask2 pv;
  OutTask ot;
  {
    const u32x4* p = (const u32x4*)(lds + L_DRN) + lane;
    const u32x4 q0 = p[0], q1 = p[64], q2 = p[128];
    tk.l0 = q0[0]; tk.l1 = q0[0] + 16384u; tk.lm = q0[1]; tk.s0 = q0[2]; tk.s1 = q0[3];
    pv.lo = q1[0]; pv.lm = q1[1]; pv.vr = q1[2]; pv.ma = q1[3]; pv.mz = q2[0]; pv.so = q2[1];
    pv.pa[0] = pv.pa[1] = pv.pb[0] = pv.pb[1] = (u32x4){0u, 0u, 0u, 0u};
    ot.t5f = q2[2]; ot.t5o = q2[3]; ot.sel = as8(p[192]);
  }
#pragma unroll 1
  for (int ti = 0; todo; todo >>= 12, ++ti) {
    const int ty = (int)todo & 3, sl = 56 + (((int)todo >> 2) & 7);
    const unsigned tm = tp1 + (((unsigned)todo >> 5) & 0x7fu) * 100u;
    if (ty == 1) {
      wait_cyc(tm);
      LOGD(3 * ti + 1);
      poll([&]() { tk.load(rs, ts_slot(ck, sl)); }, [&]() { return tk.ready(); });
      LOGD(3 * ti + 2);
      tk.run(j, lane, ot.sel); tk.store(rs, pt_slot(ck, sl));
    } else if (ty == 2) {
      const unsigned t0 = cyc32();
      if (sl >= NSEG - 2) { lds_wait(misc + 7, 4u * NSEG + 3u); wait_cyc(t0 + END_G2); }
      wait_cyc(tm);
      LOGD(3 * ti + 1);
      poll([&]() { pv.load(rs, pt_slot(ck, sl)); }, [&]() { return pv.ready(); });
      LOGD(3 * ti + 2);
      pv.run<true>(lds, sl % NV, lane, rs, op_slot(sl) + (unsigned)((m * 32 + gp) * 1280));
      if (sl == NSEG - 1) misc[14] = cyc32() | 1u;
    } else {
      if (sl >= NSEG - 4) { lds_wait(misc + 14, 1u); wait_cyc(__builtin_amdgcn_readfirstlane(misc[14]) + END_G3); }
      wait_cyc(tm);
      LOGD(3 * ti + 1);
      OutTask o2 = ot;
      poll([&]() { o2.load(a, rs, sl + 1, b); ot.load(a, rs, sl, b); }, [&]() { return ot.ready(b) & o2.ready(b); });
      LOGD(3 * ti + 2);
      o2.run(lane); o2.store(a, sl + 1, b, lane);
      ot.run(lane); ot.store(a, sl, b, lane);
    }
    LOGD(3 * ti + 3);
  }
  VM_DRAIN();
  if (w != 0) lds_inc<L_MISC + 64>(lds0);
  else {
    constexpr unsigned o2 = (unsigned)OFF_ACC + (unsigned)((NSEG - 1) % SR) * (unsigned)ACC_STEP;
    lds_wait(misc + 16, 3u);
    LOGD(10);
    if (lane == 0) __hip_atomic_fetch_add((gu64*)((GAS char*)(a.ws + o2) + acc_off(m * 4, 0)), (u64)1, RLX_AGENT);
    wait_until(now32() + SCH_DRAIN);
    wait_cyc(tp1 + END_E4);
    unsigned x;
    if (qkv_row(b) + 48 >= 55 && qkv_col(b) < 15) poll([&]() { x = __builtin_amdgcn_raw_buffer_load_b32(rs, acc_off(c * 4, 0), o2, 16); }, [&]() { return wave_all((x & 16u) != 0u); });
    lds_inc<L_MISC + 68>(lds0);
    LOGD(11);
  }
  if (qkv_row(b) + 48 >= 55 && qkv_col(b) < 15) {
    lds_wait(misc + 18, 8u);
    lds_spin<L_MISC + 68, 1>(lds0, 1u);
    x_fetch_n<10>(x_src(a, NSEG + 3, b), lds0, 3, 10 * w, lane * 16);
    VM_DRAIN();
    lds_inc<L_MISC + 68>(lds0);
  }
  LOGP(8);
}

__device__ __forceinline__ void proj_front(const RecArgs& a, char* lds, const __amdgpu_buffer_rsrc_t rs, const int v) {
  const int lane = __builtin_amdgcn_workitem_id_x() & 63, b = __builtin_amdgcn_workgroup_id_x(), c = lane & 15, g = lane >> 4;
  const int rb = (b & 7) + 8 * (b >> 5), cq = (b >> 3) & 3;
  const unsigned lds0 = (unsigned)(uintptr_t)(LAS char*)lds;
  unsigned* const cnt = (unsigned*)(lds + L_MISC) + 20;
  LOGF2(0);
  float* const red = (float*)(lds + L_RED1);
  float* const fin = red + 640;
  const f32x4 Z4 = {0.f, 0.f, 0.f, 0.f};
  x_fetch_n<8>((const u16*)(a.ws + OFF_OB) + (size_t)rb * 32 * DM, lds0, 0, 8 * v, lane * 16);
  bf16x8 W[2][20];
  const __amdgpu_buffer_rsrc_t rw = __builtin_amdgcn_make_buffer_rsrc(a.ws + OFF_WOBT + (size_t)(cq * 10 + 2 * v) * 20480, 0, 40960, 0x00020000);
  auto w_get = [&](int ks) {
#pragma unroll
    for (int i = 0; i < 2; ++i) W[i][ks] = as8(__builtin_amdgcn_raw_buffer_load_b128(rw, lane * 16 + (ks & 3) * 1024, (i * 20 + (ks & ~3)) * 1024, 0));
    __builtin_amdgcn_sched_barrier(0);
  };
#pragma unroll
  for (int ks = 0; ks < FW; ++ks) w_get(ks);
  const unsigned fo = (unsigned)(rb * 40960 + (cq * 5 + v) * 1024 + ((g >> 1) * 16 + c) * 16), f8 = fo + (g & 1) * 8, f16 = fo + (g & 1) * 20480;
  u32x2 xo[2][2];
#pragma unroll
  for (int k = 0; k < 4; ++k)
    xo[k >> 1][k & 1] = __builtin_bit_cast(u32x2, __hip_atomic_load((const gu64*)((const char*)a.xnew + f8 + (k >> 1) * 512 + (k & 1) * 20480), RLX_AGENT));
  asm volatile("s_waitcnt vmcnt(%0)" :: "n"(2 * FW + 4) : "memory");
  lds_inc<L_MISC + 80>(lds0);
  lds_wait(cnt, 5u);
  LOGF2(1);
  f32x4 acc[2][2] = {{Z4, Z4}, {Z4, Z4}};
  bf16x8 x[20][2];
#pragma unroll
  for (int ks = -3; ks < 20; ++ks) {
    if (ks >= 0 && ks + FW < 20) w_get(ks + FW);
#pragma unroll
    for (int t = 0; t < 2; ++t) {
      if (ks + 3 < 20) x[ks + 3][t] = *(const bf16x8*)(lds + L_XN + (t * 20 + ks + 3) * 1024 + lane * 16);
#pragma unroll
      for (int i = 0; i < 2 && ks >= 0; ++i) acc[i][t] = mfma32(W[i][ks < 0 ? 0 : ks], x[ks < 0 ? 0 : ks][t], acc[i][t]);
    }
    __builtin_amdgcn_sched_barrier(0);
  }
  u32x2 xp[2][2];
  PIN_V(acc[1][1]);
  LOGF2(2);
#pragma unroll
  for (int t = 0; t < 2; ++t) {
#pragma unroll
    for (int k = 0; k < 4; ++k) {
      const int i = k >> 1, q = k & 1;
      const unsigned r = pack2(acc[i][t][2 * q], acc[i][t][2 * q + 1]), x = xo[i][t][q];
      xp[i][t][q] = pack2(u2f(x << 16) + u2f(r << 16), u2f(x & 0xffff0000u) + u2f(r & 0xffff0000u));
      acc[i][t][2 * q] = u2f(xp[i][t][q] << 16); acc[i][t][2 * q + 1] = u2f(xp[i][t][q] & 0xffff0000u);
    }
    const f32x4 sm = acc[0][t] + acc[1][t];
    const float ml = x32sum(x16sum((sm[0] + sm[1]) + (sm[2] + sm[3]))) * (1.0f / 32.0f);
    float q = 0.f;
#pragma unroll
    for (int r = 0; r < 8; ++r) { const float d = acc[r >> 2][t][r & 3] - ml; q += d * d; }
    q = x32sum(x16sum(q));
    *(f32x2*)(red + (v * 32 + t * 16 + c) * 2) = (f32x2){ml, q};
  }
  lds_inc<L_MISC + 80>(lds0);
  if (v == 0) {
    lds_wait(cnt, 10u);
    LOGF2(3);
    float mw[5], ml = 0.f, m2 = 0.f;
#pragma unroll
    for (int i = 0; i < 5; ++i) { const f32x2 u = *(const f32x2*)(red + (i * 32 + (lane & 31)) * 2); mw[i] = u[0]; ml += u[0]; m2 += u[1]; }
    ml *= 0.2f;
#pragma unroll
    for (int i = 0; i < 5; ++i) m2 += 32.0f * (mw[i] - ml) * (mw[i] - ml);
    const unsigned xs = (unsigned)OFF_LNX + rb * 1024;
    if (lane < 32) SC1_STORE8S(((u32x2){f2u(ml), nz(f2u(m2))}), (cq * 32 + lane) * 8, xs);
    wait_cyc(cyc32() + FR_D);
    u32x2 x0, x1;
    for (unsigned spins = 0; spins < SPIN_CAP; ++spins) {
      x0 = __builtin_amdgcn_raw_buffer_load_b64(rs, lane * 8, xs, 16); x1 = __builtin_amdgcn_raw_buffer_load_b64(rs, 512 + lane * 8, xs, 16);
      if (wave_all(x0[1] != 0u && x1[1] != 0u)) break;
      asm volatile("" ::: "memory");
    }
    const float m0 = u2f(x0[0]), m1 = u2f(x1[0]);
    const float mean = x32sum(m0 + m1) * 0.25f;
    const float M2 = x32sum((u2f(x0[1]) + u2f(x1[1])) + 160.0f * ((m0 - mean) * (m0 - mean) + (m1 - mean) * (m1 - mean)));
    const float mb = rbf(mean), den = rbf(__builtin_sqrtf(rbf(rbf(M2 * (1.0f / DM)) + 1e-5f)));
    if (lane < 32) {
      fin[lane] = mb; fin[32 + lane] = 1.0f / den;
      if (cq == 0) __hip_atomic_store((GAS unsigned*)(a.ws + OFF_XNRM) + (rb * 32 + lane) * 4, f2u(1.02f * (M2 + DM * (mean - mb) * (mean - mb)) / (den * den)), RLX_AGENT);
    }
    lds_inc<L_MISC + 84>(lds0);
  }
  lds_wait(cnt + 1, 1u);
  LOGF2(4);
  const __amdgpu_buffer_rsrc_t rx = __builtin_amdgcn_make_buffer_rsrc(a.ws + OFF_XN, 0, (int)(OFF_QB - OFF_XN), 0x00020000);
  auto put = [&](const u32x2 p0, const u32x2 p1, unsigned so) {
    const auto r0 = __builtin_amdgcn_permlane16_swap(p0[0], p1[0], false, false), r1 = __builtin_amdgcn_permlane16_swap(p0[1], p1[1], false, false);
    __builtin_amdgcn_raw_buffer_store_b128((u32x4){r0[0], r1[0], r0[1], r1[1]}, rx, f16, so, 16);
  };
#pragma unroll
  for (int i = 0; i < 2; ++i) {
    u32x2 y[2];
#pragma unroll
    for (int t = 0; t < 2; ++t) {
      const float mb = fin[t * 16 + c], rden = fin[32 + t * 16 + c];
      y[t] = (u32x2){pack2(rbf(acc[i][t][0] - mb) * rden, rbf(acc[i][t][1] - mb) * rden), pack2(rbf(acc[i][t][2] - mb) * rden, rbf(acc[i][t][3] - mb) * rden)};
    }
    put(y[0], y[1], i * 512);
  }
  VM_DRAIN();
  lds_inc<L_MISC + 80>(lds0);
#pragma unroll
  for (int i = 0; i < 2; ++i) put(xp[i][0], xp[i][1], (unsigned)(OFF_XB - OFF_XN) + i * 512);
  if (v == 0) {
    lds_wait(cnt, 15u);
    if (rb < 4 && lane < 8) __hip_atomic_fetch_add((GAS unsigned*)(a.ws + OFF_RDY) + lane * 64, 1u, RLX_AGENT);
  }
  LOGF2(5);
}

__global__ __launch_bounds__(512, 2) void rec_kernel(RecArgs a) {
  __shared__ __attribute__((aligned(16))) char lds[L_BYTES];
  const int tid = __builtin_amdgcn_workitem_id_x(), wid = __builtin_amdgcn_readfirstlane(tid >> 6), w = wid & 3;
  const __amdgpu_buffer_rsrc_t rs = __builtin_amdgcn_make_buffer_rsrc(a.ws, 0, (int)COMM_BYTES, 0x00020000);
  if (tid < 32) ((float*)(lds + L_MISC))[tid] = (tid == 4 || tid == 5) ? 1.0f : 0.0f;
  BAR();
  if (a.layer && wid < 5) proj_front(a, lds, rs, wid);
  if (wid < 4) {
    unsigned tlast;
    if (w == 0) rec_waves<0>(a, lds, rs, tlast);
    else if (w == 1) rec_waves<1>(a, lds, rs, tlast);
    else if (w == 2) rec_waves<2>(a, lds, rs, tlast);
    else rec_waves<3>(a, lds, rs, tlast);
    drain(a, lds, rs, w, tlast);
  } else {
    unsigned vo[2][2];
    if (w == 0) token_waves<0>(a, lds, rs, vo);
    else if (w == 1) token_waves<1>(a, lds, rs, vo);
    else if (w == 2) token_waves<2>(a, lds, rs, vo);
    else token_waves<3>(a, lds, rs, vo);
    qkv_tail(a, lds, w, vo);
  }
}

#if defined(__HIP_DEVICE_COMPILE__)
#pragma clang attribute pop
#endif

#define ATT_P1 6
#define ATT_P2 10
#define ATT_P3 14
#define ATT_THR 6.0f
#define ATT_NT 4
constexpr int NT = ATT_NT, ATT_NQ = 8 / NT, KQ = S_ / NT;
#define ATT_LMAX 1.0e12f
#if defined(__HIP_DEVICE_COMPILE__)
#pragma clang attribute push(__attribute__((target("no-packed-fp32-ops"))), apply_to = function)
#endif
__global__ __launch_bounds__(512, 2) void attb_kernel(char* __restrict__ ws, u16* __restrict__ out) {
  const u16* const Qp = (const u16*)(ws + OFF_QB);
  const u16* const KV = (const u16*)(ws + OFF_KV);
  u16* const O = (u16*)(ws + OFF_OB);
  __shared__ __attribute__((aligned(16))) char wl[40960], ob[40960];
  __shared__ f32x4 mo_[8][NT - 1][3][64];
  const int tid = __builtin_amdgcn_workitem_id_x(), bid = __builtin_amdgcn_workgroup_id_x();
  __shared__ unsigned sink[64];
  own_code<ATT_CODE>(tid, sink);
  const int lane = tid & 63, wid = __builtin_amdgcn_readfirstlane(tid >> 6), kh = wid / ATT_NQ, r = lane & 31, hh = lane >> 5;
  const int hd = (bid & 7) * 4 + ((bid >> 3) & 3), qb = (bid >> 5) * ATT_NQ + wid % ATT_NQ;
  const int q0 = qb * 32 * NT;
  bf16x8 qa[NT]; u32x2 qb2[NT];
#pragma unroll
  for (int qt = 0; qt < NT; ++qt) {
    const u16* qr = Qp + ((size_t)hd * S_ + q0 + (qt + kh) % NT * 32 + r) * QST;
    { const u32x2* qp = (const u32x2*)(qr + 8 * hh); const u32x2 q0_ = qp[0], q1_ = qp[1]; qa[qt] = mk8(q0_[0], q0_[1], q1_[0], q1_[1]); }
    const u32x2 t = *(const u32x2*)(qr + 16);
    qb2[qt] = hh ? (u32x2){0u, 0u} : t;
  }
  const __amdgpu_buffer_rsrc_t rk = __builtin_amdgcn_make_buffer_rsrc((void*)(KV + ((size_t)hd * 64 + kh * (KQ / 32)) * (KVT / 2)), 0, KQ / 32 * KVT, 0x00020000);
  unsigned ok1 = lane * 16, ok2 = hh ? 1600 : 1024 + r * 8, ov = r > 20 ? 1u << 30 : 1280 + (hh * 21 + r) * 16;
  const __amdgpu_buffer_rsrc_t ro = __builtin_amdgcn_make_buffer_rsrc(O, 0, S_ * DM * 2, 0x00020000);
  unsigned oo[3];
#pragma unroll
  for (int i = 0; i < 3; ++i) {
    const int row = q0 + kh * 32 + r, k = hd * BD + 4 * hh + 8 * i;
    oo[i] = i == 2 && hh ? 1u << 30 : (unsigned)(((((row >> 4) * 20 + (k >> 5)) * 64 + ((k & 31) >> 3) * 16 + (row & 15)) * 8 + (k & 7)) * 2);
  }
  f32x16 o[NT];
  float m[NT];
  const f32x16 Z16 = {0.f, 0.f, 0.f, 0.f, 0.f, 0.f, 0.f, 0.f, 0.f, 0.f, 0.f, 0.f, 0.f, 0.f, 0.f, 0.f};
  int odd = 0xDDDD;
  asm volatile("" : "+v"(odd));
  struct KvT { u32x4 k1, v0, v1; u32x2 k2; };
  auto fetch_k = [&](KvT& x, int t) {
    x.k1 = __builtin_amdgcn_raw_buffer_load_b128(rk, ok1, imin(t, KQ / 32 - 1) * KVT, 0); x.k2 = __builtin_amdgcn_raw_buffer_load_b64(rk, ok2, imin(t, KQ / 32 - 1) * KVT, 0);
    __builtin_amdgcn_sched_barrier(0);
  };
  auto fetch_v = [&](KvT& x, int t) {
    x.v0 = __builtin_amdgcn_raw_buffer_load_b128(rk, ov, imin(t, KQ / 32 - 1) * KVT, 0); x.v1 = __builtin_amdgcn_raw_buffer_load_b128(rk, ov + 672, imin(t, KQ / 32 - 1) * KVT, 0);
    __builtin_amdgcn_sched_barrier(0);
  };
  auto fetch = [&](int t) { KvT x; __builtin_amdgcn_sched_barrier(0); fetch_k(x, t); fetch_v(x, t); return x; };
  auto qk1 = [&](const KvT& x, int qt) { return __builtin_amdgcn_mfma_f32_32x32x16_bf16(as8(x.k1), qa[qt], Z16, 0, 0, 0); };
  auto qk2 = [&](const KvT& x, int qt, f32x16 c) { return __builtin_amdgcn_mfma_f32_32x32x8bf16_1k(__builtin_bit_cast(v4i16_t, x.k2), __builtin_bit_cast(v4i16_t, qb2[qt]), c, 0, 0, 0); };
  auto pv = [&](const u32x4 v, const f32x16 p, int hi, f32x16 c) {
    const f32x8 ph = hi ? __builtin_shufflevector(p, p, 8, 9, 10, 11, 12, 13, 14, 15) : __builtin_shufflevector(p, p, 0, 1, 2, 3, 4, 5, 6, 7);
    return __builtin_amdgcn_smfmac_f32_32x32x32_bf16(__builtin_bit_cast(hbf16x8, v), __builtin_bit_cast(hbf16x16, ph), c, odd, 0, 0);
  };
  auto slow = [&]() {
    asm volatile("" : "+v"(ok1), "+v"(ok2), "+v"(ov) :: "memory");
#pragma unroll
    for (int qt = 0; qt < NT; ++qt) o[qt] = Z16;
#pragma unroll 1
    for (int t = 0; t < KQ / 32; ++t) {
      const KvT x = fetch(t);
#pragma unroll
      for (int qt = 0; qt < NT; ++qt) {
        f32x16 sa = qk2(x, qt, qk1(x, qt));
        float mx = fmx(fmx(sa[0], sa[1]), sa[2]);
#pragma unroll
        for (int i = 3; i < 15; i += 2) mx = fmx(fmx(mx, sa[i]), sa[i + 1]);
        mx = fmx(mx, sa[15]);
        if (t == 0 || !wave_all(mx <= ATT_THR)) {
          mx = x32mx(mx);
          if (t) mx = fmx(mx, 0.f);
          const float mn = m[qt] + mx;
          const unsigned h1 = f2bf(mn); const float r1 = mn - bf2f(h1);
          const unsigned h2 = f2bf(r1); const float r2 = r1 - bf2f(h2);
          const unsigned h3 = f2bf(r2);
          const float me = (bf2f(h1) + bf2f(h2)) + bf2f(h3);
          const float dm = me - m[qt];
          m[qt] = me;
          if (hh) qb2[qt] = (u32x2){(h1 | (h2 << 16)) ^ 0x80008000u, h3 ^ 0x8000u};
          const float alpha = t ? __builtin_amdgcn_exp2f(-dm) : 0.f;
#pragma unroll
          for (int i = 0; i < 16; ++i) { o[qt][i] *= alpha; sa[i] -= dm; }
        }
#pragma unroll
        for (int i = 0; i < 16; ++i) sa[i] = __builtin_amdgcn_exp2f(sa[i]);
        asm volatile("" : "+v"(sa));
        o[qt] = pv(x.v1, sa, 1, pv(x.v0, sa, 0, o[qt]));
        __builtin_amdgcn_sched_barrier(0);
      }
    }
  };
  f32x16 sc[2];
  auto phase = [&](int qt, const u32x4 vp, const KvT& xk, const u32x4 v0, auto mid) {
    const int u = qt & 1;
    auto ex = [&](int i0, int i1) {
#pragma unroll
      for (int i = i0; i < i1; ++i) sc[u][i] = __builtin_amdgcn_exp2f(sc[u][i]);
      asm volatile("" : "+v"(sc[u]));
      __builtin_amdgcn_sched_barrier(0);
    };
    o[(qt + NT - 1) % NT] = pv(vp, sc[1 - u], 1, o[(qt + NT - 1) % NT]);
    __builtin_amdgcn_sched_barrier(0);
    ex(0, 3);
    sc[1 - u] = qk1(xk, (qt + 1) % NT);
    __builtin_amdgcn_sched_barrier(0);
    ex(3, 6);
    sc[1 - u] = qk2(xk, (qt + 1) % NT, sc[1 - u]);
    __builtin_amdgcn_sched_barrier(0);
    ex(6, 10);
    o[qt] = pv(v0, sc[u], 0, o[qt]);
    __builtin_amdgcn_sched_barrier(0);
    mid();
    ex(10, 16);
  };
  static_assert(NT == 4, "projection");
  const int rb = (bid >> 5) * 8 + (bid & 7), cb = (bid >> 3) & 3, g = lane >> 4, c = lane & 15;
  const __amdgpu_buffer_rsrc_t rw = __builtin_amdgcn_make_buffer_rsrc(ws + OFF_WOBT + (size_t)cb * 204800, 0, out ? 204800 : 0, 0x00020000);
  const __amdgpu_buffer_rsrc_t rx = __builtin_amdgcn_make_buffer_rsrc(ws + OFF_XA + (size_t)rb * 40960, 0, out ? 40960 : 0, 0x00020000);
  bf16x8 W[20];
  u32x2 xo[3];
  auto w_get = [&](int t) {
    if (t >= 2 && t < 12) {
#pragma unroll
      for (int i = 0; i < 2; ++i) W[t < 2 || t >= 12 ? 0 : 2 * t - 4 + i] = as8(__builtin_amdgcn_raw_buffer_load_b128(rw, ok1 + i * 1024, (wid * 20 + 2 * t - 4) * 1024, 0));
    }
    if (t >= 4 && t < 9) {
      unsigned keep;
      asm volatile("s_mov_b32 %0, m0\n\ts_mov_b32 m0, %4\n\ts_nop 0\n\tbuffer_load_dwordx4 %1, %2, %3 offen lds\n\ts_mov_b32 m0, %0"
                   : "=&s"(keep) : "v"(ok1), "s"(rw), "s"((160 + wid * 5 + t - 4) * 1024), "s"((unsigned)(uintptr_t)(LAS char*)wl + (wid * 5 + t - 4) * 1024) : "memory");
    }
    if (t == 12) {
#pragma unroll
      for (int i = 0; i < 3; ++i) {
        const int col = cb * 160 + (i < 2 ? wid : 8 + ((wid >> 1) & 1)) * 16 + 4 * g, tt = (i ^ wid) & 1;
        xo[i] = __builtin_amdgcn_raw_buffer_load_b64(rx, (((tt * 20 + (col >> 5)) * 64 + ((col & 31) >> 3) * 16 + c) * 8 + (col & 7)) * 2, 0, 0);
      }
    }
    __builtin_amdgcn_sched_barrier(0);
  };
  if (ATT_P1) __builtin_amdgcn_s_setprio(3);
  {
    KvT xa = fetch(0), xb;
    fetch_k(xb, 1);
    xb.v1 = (u32x4){0u, 0u, 0u, 0u};
    sc[1] = Z16;
    asm volatile("" : "+v"(sc[1]), "+v"(xb.v1), "+v"(oo[0]), "+v"(oo[1]), "+v"(oo[2]));
#pragma unroll
    for (int qt = 0; qt < NT; ++qt) { m[qt] = 0.f; o[qt] = Z16; asm volatile("" : "+v"(o[qt])); }
    sc[0] = qk2(xa, 0, qk1(xa, 0));
    auto step = [&](KvT& x, KvT& y, int t) {
#pragma unroll
      for (int qt = 0; qt < NT; ++qt)
        phase(qt, qt ? x.v1 : y.v1, qt < NT - 1 ? x : y, x.v0, [&]() {
          if (qt == 0 && t + 1 < KQ / 32) fetch_v(y, t + 1);
          if (qt == NT - 2 && t + 2 < KQ / 32) fetch_k(x, t + 2);
          if (qt == NT - 1) w_get(t);
        });
    };
#pragma unroll
    for (int t = 0; t < KQ / 32; t += 2) {
      if (t == ATT_P1 * 4 / NT) __builtin_amdgcn_s_setprio(2);
      if (t == ATT_P2 * 4 / NT) __builtin_amdgcn_s_setprio(1);
      if (t == ATT_P3 * 4 / NT) __builtin_amdgcn_s_setprio(0);
      step(xa, xb, t);
      step(xb, xa, t + 1);
    }
    o[NT - 1] = pv(xb.v1, sc[1], 1, o[NT - 1]);
    float big = 0.f;
#pragma unroll
    for (int qt = 0; qt < NT; ++qt) {
      big = fmx(big, hh ? (o[qt][8] >= 1.0f / ATT_LMAX ? o[qt][8] * (3.0e38f / ATT_LMAX) : INFINITY) : __builtin_fabsf(o[qt][8]));
#pragma unroll
      for (int i = 0; i < 12; ++i) if (i != 8) big = fmx(big, __builtin_fabsf(o[qt][i]));
    }
    if (__builtin_expect(!wave_all(big <= 3.0e38f), 0)) slow();
  }
#pragma unroll
  for (int qt = 1; qt < NT; ++qt) {
    f32x4 (*mo)[64] = mo_[(qt + kh) % NT * ATT_NQ + wid % ATT_NQ][qt - 1];
#pragma unroll
    for (int k = 0; k < 3; ++k) mo[k][lane] = (f32x4){o[qt][4 * k], k == 2 && hh ? m[qt] : o[qt][4 * k + 1], o[qt][4 * k + 2], o[qt][4 * k + 3]};
  }
  asm volatile("s_waitcnt lgkmcnt(0)\n\ts_barrier" ::: "memory");
  {
    f32x2 lm[NT - 1];
    f32x4 u[NT - 1][3];
#pragma unroll
    for (int i = 0; i < NT - 1; ++i) {
      lm[i] = *(const f32x2*)&mo_[wid][i][2][lane | 32];
#pragma unroll
      for (int k = 0; k < 3; ++k) u[i][k] = mo_[wid][i][k][lane];
    }
    float mn = m[0];
#pragma unroll
    for (int i = 0; i < NT - 1; ++i) mn = fmx(mn, lm[i][1]);
    float a0 = __builtin_amdgcn_exp2f(m[0] - mn), a[NT - 1];
    float l = u2f(__builtin_amdgcn_permlane32_swap(f2u(o[0][8]), f2u(o[0][8]), false, false)[1]) * a0;
#pragma unroll
    for (int i = 0; i < NT - 1; ++i) { a[i] = __builtin_amdgcn_exp2f(lm[i][1] - mn); l += lm[i][0] * a[i]; }
    const float inv = __builtin_amdgcn_rcpf(l);
    a0 *= inv;
#pragma unroll
    for (int i = 0; i < NT - 1; ++i) a[i] *= inv;
#pragma unroll
    for (int k = 0; k < 3; ++k) {
      f32x4 v = (f32x4){o[0][4 * k], o[0][4 * k + 1], o[0][4 * k + 2], o[0][4 * k + 3]} * a0;
#pragma unroll
      for (int i = 0; i < NT - 1; ++i) v += u[i][k] * a[i];
      __builtin_amdgcn_raw_buffer_store_b64((u32x2){pack2(v[0], v[1]), pack2(v[2], v[3])}, ro, oo[k], 0, 16);
    }
  }
  if (!out) return;
  asm volatile("s_waitcnt vmcnt(0)\n\ts_barrier" ::: "memory");
  GAS unsigned* const cnt = (GAS unsigned*)(ws + OFF_FIN) + (bid >> 5) * 512;
  if (wid == 0) {
    if (lane < 8) __hip_atomic_fetch_add(cnt + lane * 64, 1u, RLX_AGENT);
  }
  bf16x8 W2[20];
  const __amdgpu_buffer_rsrc_t rf = __builtin_amdgcn_make_buffer_rsrc(out + (size_t)rb * 32 * DM, 0, 32 * DM * 2, 0x00020000);
  unsigned xa0 = (wid & 1) * 20480 + lane * 16, xa1 = 20480 - (wid & 1) * 20480 + lane * 16;
#pragma unroll
  for (int i = 0; i < 3; ++i) oo[i] = (unsigned)(((((i ^ wid) & 1) * 16 + c) * DM + cb * 160 + (i < 2 ? wid : 8 + ((wid >> 1) & 1)) * 16 + 4 * g) * 2);
  asm volatile("" : "+v"(xa0), "+v"(xa1), "+v"(oo[0]), "+v"(oo[1]), "+v"(oo[2]));
#pragma unroll
  for (int ks = 0; ks < 20; ++ks) W2[ks] = *(const bf16x8*)(wl + (((wid >> 1) & 1) * 20 + ks) * 1024 + lane * 16);
  __builtin_amdgcn_sched_barrier(0);
#pragma unroll
  for (int ks = 0; ks < 20; ks += 4) asm volatile("" : "+v"(W2[ks]), "+v"(W2[ks + 1]), "+v"(W2[ks + 2]), "+v"(W2[ks + 3]));
  if (wid == 0) {
    __builtin_amdgcn_s_sleep(FIN_D);
    for (unsigned spins = 0; __hip_atomic_load(cnt + (bid & 7) * 64, RLX_AGENT) < 32u && spins < SPIN_CAP; ++spins) __builtin_amdgcn_s_sleep(FIN_SL);
  }
  asm volatile("s_barrier" ::: "memory");
  x_fetch_n<5>(O + (size_t)rb * 32 * DM, (unsigned)(uintptr_t)(LAS char*)ob - L_XN, 0, wid * 5, ok1);
  asm volatile("s_waitcnt vmcnt(0)\n\ts_barrier" ::: "memory");
  auto fin = [&](auto N) {
    f32x4 acc[3] = {{0.f, 0.f, 0.f, 0.f}, {0.f, 0.f, 0.f, 0.f}, {0.f, 0.f, 0.f, 0.f}};
    bf16x8 x[20][2];
#pragma unroll
    for (int ks = -4; ks < 20; ++ks) {
      if (ks + 4 < 20) { x[ks + 4][0] = *(const bf16x8*)(ob + xa0 + (ks + 4) * 1024); x[ks + 4][1] = *(const bf16x8*)(ob + xa1 + (ks + 4) * 1024); }
      if (ks >= 0) {
        acc[0] = mfma32(W[ks < 0 ? 0 : ks], x[ks < 0 ? 0 : ks][0], acc[0]); acc[1] = mfma32(W[ks < 0 ? 0 : ks], x[ks < 0 ? 0 : ks][1], acc[1]);
        if (N == 3) acc[2] = mfma32(W2[ks < 0 ? 0 : ks], x[ks < 0 ? 0 : ks][0], acc[2]);
      }
      __builtin_amdgcn_sched_barrier(0);
    }
#pragma unroll
    for (int i = 0; i < N; ++i) {
      const unsigned r01 = pack2(acc[i][0], acc[i][1]), r23 = pack2(acc[i][2], acc[i][3]);
      const unsigned p01 = pack2(u2f(xo[i][0] << 16) + u2f(r01 << 16), u2f(xo[i][0] & 0xffff0000u) + u2f(r01 & 0xffff0000u));
      const unsigned p23 = pack2(u2f(xo[i][1] << 16) + u2f(r23 << 16), u2f(xo[i][1] & 0xffff0000u) + u2f(r23 & 0xffff0000u));
      __builtin_amdgcn_raw_buffer_store_b64((u32x2){p01, p23}, rf, oo[i], 0, 0);
    }
  };
  if (wid >= 4) fin(std::integral_constant<int, 3>{}); else fin(std::integral_constant<int, 2>{});
}
#if defined(__HIP_DEVICE_COMPILE__)
#pragma clang attribute pop
#endif

extern "C" void kernel_launch(void* const* d_in, const int* in_sizes, int n_in, void* d_out, int out_size, void* d_ws,
                              size_t ws_size, hipStream_t stream) {
  const u16* A_x = (const u16*)d_in[0];
  const u16* A_mem0 = (const u16*)d_in[1];
  const u16* A_Wq = (const u16*)d_in[2];
  const u16* A_Wk = (const u16*)d_in[3];
  const u16* A_Wv = (const u16*)d_in[4];
  const u16* A_Wo = (const u16*)d_in[5];
  const u16* B_wqkv = (const u16*)d_in[8];
  const u16* B_wo = (const u16*)d_in[9];
  char* ws = (char*)(((uintptr_t)d_ws + ((size_t)WS_OFF << 20) + WS_AL - 1) & ~(uintptr_t)(WS_AL - 1));
  u16* xa = (u16*)(ws + OFF_XA);

  prologue_kernel<<<256, 640, 0, stream>>>(ProArgs{A_x, A_Wq, A_Wk, A_Wv, A_Wo, B_wqkv, B_wo, ws});
  for (int layer = 0; layer < 2; ++layer) {
    rec_kernel<<<256, 512, 0, stream>>>(RecArgs{A_mem0, A_Wq, A_Wk, (const u16*)(ws + OFF_XB), xa, ws, (unsigned)layer, 0u});
    attb_kernel<<<BH * 8, 512, 0, stream>>>(ws, layer ? (u16*)d_out : nullptr);
  }
  spin_kernel<<<1, 1, 0, stream>>>(1, 0, 13, 63);
}
```

```cpp
#include <hip/hip_runtime.h>
#include <hip/hip_bf16.h>
#include <stdint.h>
#include <type_traits>

typedef unsigned short u16;
typedef unsigned long long u64;
typedef __attribute__((ext_vector_type(8))) short bf16x8;
typedef __attribute__((ext_vector_type(16))) short bf16x16;
typedef __attribute__((ext_vector_type(4))) float f32x4;
typedef __attribute__((ext_vector_type(2))) float f32x2;
typedef __attribute__((ext_vector_type(16))) float f32x16;
typedef __attribute__((ext_vector_type(8))) float f32x8;
typedef __attribute__((ext_vector_type(4))) unsigned u32x4;
typedef __attribute__((ext_vector_type(2))) unsigned u32x2;
typedef __attribute__((ext_vector_type(3))) unsigned u32x3;
typedef __attribute__((ext_vector_type(8))) unsigned u32x8;
typedef __attribute__((ext_vector_type(16))) unsigned u32x16;
typedef __attribute__((ext_vector_type(32))) unsigned u32x32;
#define GAS __attribute__((address_space(1)))
#define LAS __attribute__((address_space(3)))
typedef GAS unsigned long long gu64;
#define BAR() do { __builtin_amdgcn_s_waitcnt(0xC07F); asm volatile("s_waitcnt lgkmcnt(0)\n\ts_barrier" ::: "memory"); } while (0)

constexpr int S_ = 2048, DM = 640;
constexpr int NH = 32, DH = 192, HD = NH * DH;
constexpr int MEM = 8, SEG = 32, NSEG = S_ / SEG;
constexpr int BH = 32, BD = 20;

constexpr size_t al256(size_t x) { return (x + 255) & ~(size_t)255; }
constexpr int LAG3 = 2, LAG4 = 3, LAG5 = 6;
#define SR_ 8
constexpr int SR = SR_;
constexpr unsigned SCT_STEP = 131072, OFF_SCM = SR * SCT_STEP, SCM_STEP = 32768;
constexpr int ACC_NB = 192;
constexpr size_t ACC_STEP = 65536;
constexpr unsigned ACC_TS = ACC_NB * 256;
constexpr size_t ACC_LAYER = SR * ACC_STEP;
constexpr size_t OFF_ACC = 3 * 524288;
static_assert(SR == 8, "regions");
__device__ __forceinline__ unsigned ts_piece(int i) { return ACC_TS + (unsigned)(i < 128 ? (i >> 1) * 256 + (i & 1) * 16 : 128 + (i & 1) * 8192); }
constexpr size_t OFF_LNX = OFF_SCM + SR * SCM_STEP;
constexpr size_t OFF_RDY = OFF_LNX + 65536;
constexpr size_t OFF_FIN = OFF_RDY + 2048;
constexpr size_t PG = 2097152;
#define RING_ 4
constexpr int RING = RING_;
constexpr size_t TS_SLOT = (size_t)NH * 8 * 2560, PT_SLOT = (size_t)NH * 2560, OP_SLOT = (size_t)256 * 2560;
__device__ __forceinline__ unsigned ts_tok(int h, int j) { return (unsigned)((h & 15) * 32768 + (j >> 2) * 16384 + (j & 3) * 2048); }
__device__ __forceinline__ unsigned ts_mem(int h, int j) { return (unsigned)(524288 + ((h & 15) >> 1) * 16384 + (h & 1) * 4096 + j * 512); }
__device__ __forceinline__ unsigned pt_tok(int h) { return (unsigned)(((h & 15) >> 2) * 16384 + (h & 3) * 2048); }
__device__ __forceinline__ unsigned pt_mem(int h) { return (unsigned)(65536 + (h & 15) * 512); }
constexpr size_t OFF_X = 2 * PG + TS_SLOT + 3 * PT_SLOT, X_BYTES = 7 * OP_SLOT + 4 * PT_SLOT;
constexpr size_t OFF_OP = OFF_X + TS_SLOT + 4 * PT_SLOT;
constexpr size_t COMM_BYTES = OFF_X + X_BYTES + RING * OP_SLOT;
__device__ __forceinline__ unsigned near_half(unsigned ck, unsigned o) { return o + ((ck >> (o >= 2 * PG ? 8 : 4)) & 8192u); }
__device__ __forceinline__ unsigned ts_slot(unsigned ck, int sl) {
  return near_half(ck, sl == NSEG - 1 ? (unsigned)OFF_X : ((sl & 3) == 3 ? (unsigned)(2 * PG) : (unsigned)PG + (unsigned)(sl & 3) * (unsigned)TS_SLOT));
}
__device__ __forceinline__ unsigned pt_slot(unsigned ck, int sl) {
  return near_half(ck, sl >= NSEG - 4 ? (unsigned)(OFF_X + TS_SLOT) + (unsigned)(sl - (NSEG - 4)) * (unsigned)PT_SLOT
                                      : ((sl & 3) == 0 ? (unsigned)(PG + 3 * TS_SLOT) : (unsigned)(2 * PG + TS_SLOT - PT_SLOT) + (unsigned)(sl & 3) * (unsigned)PT_SLOT));
}
__device__ __forceinline__ unsigned op_slot(int sl) { return (unsigned)OFF_OP + (unsigned)(sl >= NSEG - 6 ? sl - (NSEG - 6) : 6 + sl % RING) * (unsigned)OP_SLOT; }
static_assert(RING == 4 && OFF_OP <= 3 * PG && OFF_FIN + 16384 <= OFF_ACC && OFF_ACC + ACC_LAYER == PG && TS_SLOT % 16384 == 0 && PT_SLOT % 16384 == 0, "pages");
static_assert(TS_SLOT == OP_SLOT && RING >= 4 && NSEG % SR == 0 && SR >= 5 && X_BYTES % 4096 == 0 && X_BYTES <= 256 * 20 * 1024, "slots");
constexpr size_t OFF_WB = (COMM_BYTES + 16383) / 16384 * 16384, WB_BYTES = (size_t)256 * 200 * 1024;
__device__ __forceinline__ unsigned wb_off(int F) { const unsigned o = (unsigned)((F >> 3) * 16384 + (F & 7) * 1024); return o | ((((unsigned)OFF_WB + o) >> 8) & 8192u); }
constexpr size_t OFF_PHI = OFF_WB + WB_BYTES;
constexpr size_t OFF_BNRM = OFF_PHI + 1024;
constexpr size_t OFF_LAT = OFF_BNRM + 1024;
constexpr size_t OFF_XNRM = OFF_LAT + 2048;
constexpr size_t OFF_WQKVBT = OFF_XNRM + al256(S_ * 16);
constexpr size_t OFF_WOBT = OFF_WQKVBT + al256((size_t)3 * DM * DM * 2);
constexpr size_t OFF_XN = OFF_WOBT + al256((size_t)DM * DM * 2);
constexpr int QST = BD;
constexpr size_t OFF_XA = OFF_XN + al256((size_t)S_ * DM * 2);
constexpr size_t OFF_XB = OFF_XA + al256((size_t)S_ * DM * 2);
constexpr size_t OFF_QB = OFF_XB + al256((size_t)S_ * DM * 2);
constexpr int KVT = 2624;
constexpr size_t OFF_KV = OFF_QB + al256((size_t)BH * S_ * QST * 2);
constexpr size_t OFF_OB = OFF_KV + al256((size_t)BH * 64 * KVT);
constexpr size_t OFF_DBG = OFF_OB + al256((size_t)S_ * DM * 2);
constexpr size_t WS_TOTAL = OFF_DBG + (size_t)2 * 64 * 8 * DM * 2;
#define WS_AL 2097152
#define WS_OFF 0
static_assert(WS_TOTAL + WS_AL + ((size_t)WS_OFF << 20) <= ((size_t)256 << 20), "workspace budget");

#if defined(__HIP_DEVICE_COMPILE__)
#pragma clang attribute push(__attribute__((target("no-packed-fp32-ops"))), apply_to = function)
#endif
__device__ __forceinline__ float u2f(unsigned v) { return __builtin_bit_cast(float, v); }
__device__ __forceinline__ unsigned f2u(float v) { return __builtin_bit_cast(unsigned, v); }
__device__ __forceinline__ float bf2f(unsigned v) { return __builtin_bit_cast(float, v << 16); }
__device__ __forceinline__ unsigned f2bf(float f) { __bf16 b = (__bf16)f; return (unsigned)__builtin_bit_cast(u16, b); }
__device__ __forceinline__ float rbf(float f) { return bf2f(f2bf(f)); }
typedef __attribute__((ext_vector_type(2))) __bf16 hbf16x2;
__device__ __forceinline__ unsigned pack2(float lo, float hi) { f32x2 v = {lo, hi}; hbf16x2 b = __builtin_convertvector(v, hbf16x2); return __builtin_bit_cast(unsigned, b); }
__device__ __forceinline__ f32x4 mfma32(bf16x8 a, bf16x8 b, f32x4 c) { return __builtin_amdgcn_mfma_f32_16x16x32_bf16(a, b, c, 0, 0, 0); }
typedef __attribute__((ext_vector_type(8))) __bf16 hbf16x8;
typedef __attribute__((ext_vector_type(16))) __bf16 hbf16x16;
__device__ __forceinline__ f32x4 smfma64(bf16x8 a, bf16x16 b, f32x4 c, int idx) {
  return __builtin_amdgcn_smfmac_f32_16x16x64_bf16(__builtin_bit_cast(hbf16x8, a), __builtin_bit_cast(hbf16x16, b), c, idx, 0, 0);
}
__device__ __forceinline__ bf16x16 cat16(bf16x8 a, bf16x8 b) { return __builtin_shufflevector(a, b, 0, 1, 2, 3, 4, 5, 6, 7, 8, 9, 10, 11, 12, 13, 14, 15); }
__device__ __forceinline__ bf16x8 half8(bf16x16 v, int hi) { return hi ? __builtin_shufflevector(v, v, 8, 9, 10, 11, 12, 13, 14, 15) : __builtin_shufflevector(v, v, 0, 1, 2, 3, 4, 5, 6, 7); }
__device__ __forceinline__ bf16x8 mk8(unsigned a, unsigned b, unsigned c, unsigned d) { u32x4 v = {a, b, c, d}; return __builtin_bit_cast(bf16x8, v); }
__device__ __forceinline__ unsigned dpp_ror8(unsigned v) { return (unsigned)__builtin_amdgcn_mov_dpp((int)v, 0x128, 0xf, 0xf, true); }
__device__ __forceinline__ float fmx(float a, float b) { return __builtin_elementwise_maximum(a, b); }

__device__ __forceinline__ int imax(int a, int b) { return a > b ? a : b; }
__device__ __forceinline__ int imin(int a, int b) { return a < b ? a : b; }
__device__ __forceinline__ unsigned umin(unsigned a, unsigned b) { return a < b ? a : b; }
__device__ __forceinline__ unsigned umax(unsigned a, unsigned b) { return a > b ? a : b; }
__device__ __forceinline__ bool wave_all(bool p) { return __builtin_amdgcn_ballot_w64(!p) == 0ull; }
__device__ __forceinline__ float x16mx(float x) { unsigned u = __builtin_bit_cast(unsigned, x); auto r = __builtin_amdgcn_permlane16_swap(u, u, false, false); return fmx(u2f(r[0]), u2f(r[1])); }
__device__ __forceinline__ float x32mx(float x) { unsigned u = __builtin_bit_cast(unsigned, x); auto r = __builtin_amdgcn_permlane32_swap(u, u, false, false); return fmx(u2f(r[0]), u2f(r[1])); }
__device__ __forceinline__ float x16sum(float x) { unsigned u = __builtin_bit_cast(unsigned, x); auto r = __builtin_amdgcn_permlane16_swap(u, u, false, false); return u2f(r[0]) + u2f(r[1]); }
__device__ __forceinline__ float x32sum(float x) { unsigned u = __builtin_bit_cast(unsigned, x); auto r = __builtin_amdgcn_permlane32_swap(u, u, false, false); return u2f(r[0]) + u2f(r[1]); }
template <int CTRL> __device__ __forceinline__ float dppf(float v) { return u2f((unsigned)__builtin_amdgcn_mov_dpp((int)f2u(v), CTRL, 0xf, 0xf, true)); }
#define OWN_PF 1
template <int BYTES> __device__ __forceinline__ void own_code(int tid, unsigned* sink) {
  if (!OWN_PF || __builtin_amdgcn_readfirstlane(tid >> 6) * 4096 >= BYTES) return;
  const char* pc; unsigned keep;
  asm volatile("s_getpc_b64 %0" : "=s"(pc));
  asm volatile("s_mov_b32 %0, m0\n\ts_mov_b32 m0, %3\n\ts_nop 0\n\tglobal_load_lds_dword %1, %2\n\ts_mov_b32 m0, %0"
               : "=&s"(keep) : "v"(tid * 64 < BYTES ? tid * 64 : 0), "s"(pc), "s"((unsigned)(uintptr_t)(LAS unsigned*)sink) : "memory");
}
constexpr int DPP_XOR1 = 0xB1, DPP_XOR2 = 0x4E, DPP_ROR4 = 0x124, DPP_ROR8 = 0x128;
#if defined(__HIP_DEVICE_COMPILE__)
#pragma clang attribute pop
#endif

typedef short v4i16_t __attribute__((ext_vector_type(4)));
__device__ __forceinline__ bf16x8 tr_frag(const char* lo_, const char* hi_) {
  const v4i16_t lo = __builtin_amdgcn_ds_read_tr16_b64_v4i16((LAS v4i16_t*)lo_), hi = __builtin_amdgcn_ds_read_tr16_b64_v4i16((LAS v4i16_t*)hi_);
  return (bf16x8){lo[0], lo[1], lo[2], lo[3], hi[0], hi[1], hi[2], hi[3]};
}
__device__ __forceinline__ int rec_wg(int gp, int m) { return (((gp & 1) * 16 + m) << 3) | (gp >> 1); }
__device__ __forceinline__ float wsum(float v) {
  v += dppf<DPP_XOR1>(v); v += dppf<DPP_XOR2>(v); v += dppf<DPP_ROR4>(v); v += dppf<DPP_ROR8>(v);
  return x32sum(x16sum(v));
}
__device__ __forceinline__ float ln_row(int lane, u32x4 (&xo)[2]) {
  const bool two = lane < 16;
  float v[16];
#pragma unroll
  for (int hf = 0; hf < 2; ++hf)
#pragma unroll
    for (int i = 0; i < 4; ++i) { const unsigned u = xo[hf][i]; v[hf * 8 + 2 * i] = bf2f(u & 0xffff); v[hf * 8 + 2 * i + 1] = bf2f(u >> 16); }
  float s0 = 0.f, s1 = 0.f;
#pragma unroll
  for (int i = 0; i < 8; ++i) { s0 += v[i]; s1 += v[8 + i]; }
  const float mean = wsum(two ? s0 + s1 : s0) * (1.0f / DM);
  s0 = s1 = 0.f;
#pragma unroll
  for (int i = 0; i < 8; ++i) { const float d0 = v[i] - mean, d1 = v[8 + i] - mean; s0 += d0 * d0; s1 += d1 * d1; }
  const float var = rbf(wsum(two ? s0 + s1 : s0) * (1.0f / DM));
  const float mb = rbf(mean);
  const float rden = 1.0f / rbf(sqrtf(rbf(var + 1e-5f)));
  float n2[2] = {0.f, 0.f};
#pragma unroll
  for (int hf = 0; hf < 2; ++hf) {
    float y[8];
#pragma unroll
    for (int i = 0; i < 8; ++i) { y[i] = rbf(rbf(v[hf * 8 + i] - mb) * rden); n2[hf] += y[i] * y[i]; }
    xo[hf] = (u32x4){pack2(y[0], y[1]), pack2(y[2], y[3]), pack2(y[4], y[5]), pack2(y[6], y[7])};
  }
  return wsum(two ? n2[0] + n2[1] : n2[0]);
}

#define PROT(k)
#define WARM_CODE 1
#define B_AUX 16
#define X_AUX 16
#define T_AUX 16
#define ATT_CODE 15000
#define FIN_D 4
#define FIN_SL 2
#define WB_NEAR 1
#define PF_POS 0
#define PRB_N0 3
#define PRB_N1 0
#define PRB_LOG(i, v)
#define PLD(p) __builtin_nontemporal_load((const u32x4*)(p))
struct ProArgs { const u16* x; const u16* wq; const u16* wk; const u16* wv; const u16* wo; const u16* wqkvb; const u16* wob; char* ws; };
__global__ __launch_bounds__(640, 3) void prologue_kernel(ProArgs a) {
  __shared__ __attribute__((aligned(16))) char img[192 * 320];
  __shared__ __attribute__((aligned(16))) char slots[10 * 6144];
  __shared__ float red[10][160];
  __shared__ unsigned latw[10];
  __shared__ u32x4 xst[2][640];
  const int b = blockIdx.x, tid = threadIdx.x, lane = tid & 63, v = __builtin_amdgcn_readfirstlane(tid >> 6), c = lane & 15, g = lane >> 4;
  const int h = (b & 7) * 4 + ((b >> 3) & 3), dh = (b >> 5) & 1, cq = b >> 6, ks = dh * 10 + v;
  char* const slot = slots + v * 6144;
  PROT(0);
  __shared__ unsigned sink[64];
  own_code<9600>(tid, sink);
  const unsigned zo = v < 8 ? (unsigned)(((b >> 6) * 512 + ((b & 7) * 2 + (v >> 2)) * 32 + ((b >> 3) & 7) * 4 + (v & 3)) * 1024) : (unsigned)(((v - 7) * 2048 + (b & 7) * 32 + ((b >> 3) & 7) * 4 + (b >> 6)) * 1024);
  __builtin_amdgcn_raw_buffer_store_b128((u32x4){0u, 0u, 0u, 0u}, __builtin_amdgcn_make_buffer_rsrc(a.ws, 0, (int)(3 * PG), 0x00020000), zo + lane * 16, 0, 16);
  __builtin_amdgcn_sched_barrier(0);
  u32x4 wa[2][6], wb[6], tr[4], xo[2];
  const int xrow = b * 8 + (v & 7);
  xo[0] = *(const u32x4*)(a.x + (size_t)xrow * DM + lane * 8);
  xo[1] = *(const u32x4*)(a.x + (size_t)xrow * DM + 512 + c * 8);
  const int job = imin(b + 256 * (9 - v), 800);
  const int jn = job < 600 ? job / 20 : (job % 200) / 20, jk = job % 20;
  {
    const u16* src = job < 600 ? a.wqkvb + (size_t)(32 * jk) * 3 * DM : a.wob + (size_t)(32 * jk) * DM;
#pragma unroll
    for (int i = 0; i < 4; ++i) tr[i] = *(const u32x4*)(src + (size_t)(i * 8 + (lane >> 3)) * (job < 600 ? 3 * DM : DM) + jn * 64 + (lane & 7) * 8);
  }
#pragma unroll
  for (int i = 0; i < 6; ++i) {
    const int idx = (v * 6 + i) * 64 + lane, row = idx / 20;
    wb[i] = PLD(a.wo + (size_t)(h * DH + row) * DM + cq * 160 + (idx - row * 20) * 8);
  }
#pragma unroll
  for (int i = 0; i < 6; ++i) {
    const int idx = i * 64 + lane, r = idx / 24;
#pragma unroll
    for (int u = 0; u < 2; ++u) wa[u][i] = PLD(a.wv + (size_t)(32 * ks + 8 * (r >> 2) + (r & 3) + 4 * u) * HD + h * DH + (idx - r * 24) * 8);
  }
  unsigned pf[2] = {0u, 0u};
  auto prefetch = [&]() {
#pragma unroll
    for (int i = 0; i < 2; ++i) pf[i] = *(const unsigned*)((i ? a.wq : a.wk) + ((size_t)(b * 10 + v) * 48 + imin(lane, 47)) * 32);
  };
  if (PF_POS == 0) prefetch();
  __builtin_amdgcn_sched_barrier(0);
  unsigned lat = ~0u;
  auto probe = [&](int n0, int n) {
    const char* p = a.ws + zo;
#pragma unroll 1
    for (int i = n0; i < n0 + n; ++i) {
      u64 t0, t1; unsigned d;
      asm volatile("s_memtime %0\n\ts_waitcnt lgkmcnt(0)\n\ts_load_dword %2, %3, %4\n\ts_waitcnt lgkmcnt(0)\n\ts_memtime %1\n\ts_waitcnt lgkmcnt(0)"
                   : "=&s"(t0), "=&s"(t1), "=&s"(d) : "s"(p), "s"((i & 3) * 256 + (i >> 2) * 128) : "memory");
      PRB_LOG(i, (unsigned)t1 - (unsigned)t0);
      lat = umin(lat, (unsigned)t1 - (unsigned)t0);
    }
  };
  asm volatile("s_waitcnt vmcnt(26)" ::: "memory");
  probe(0, PRB_N0);
  latw[v] = lat;
  __builtin_amdgcn_sched_barrier(0);
  auto xput = [&](int k) {
    if (v < 8) { xst[k][lane * 8 + (v ^ (lane & 7))] = xo[0]; if (lane < 16) xst[k][(64 + lane) * 8 + (v ^ (lane & 7))] = xo[1]; }
  };
  xput(0);
  const float xn2 = u2f(__builtin_amdgcn_readfirstlane(f2u(ln_row(lane, xo))));
  xput(1);
#pragma unroll
  for (int i = 0; i < 4; ++i) *(u32x4*)(slot + (i * 8 + (lane >> 3)) * 144 + (lane & 7) * 16) = tr[i];
  __builtin_amdgcn_fence(__ATOMIC_RELEASE, "wavefront"); __builtin_amdgcn_wave_barrier();
  bf16x8 tf[4];
#pragma unroll
  for (int t = 0; t < 4; ++t) {
    const char* bp = slot + (8 * g + (c >> 2)) * 144 + t * 32 + (c & 3) * 8;
    tf[t] = tr_frag(bp, bp + 4 * 144);
  }
  __builtin_amdgcn_fence(__ATOMIC_RELEASE, "wavefront"); __builtin_amdgcn_wave_barrier();
  __builtin_amdgcn_sched_barrier(0);
#pragma unroll
  for (int i = 0; i < 6; ++i) {
    const int idx = (v * 6 + i) * 64 + lane, row = idx / 20;
    *(u32x4*)(img + row * 320 + (((idx - row * 20) ^ ((row >> 2) & 2)) * 16)) = wb[i];
  }
  bf16x8 A[2][6];
#pragma unroll
  for (int u = 0; u < 2; ++u) {
#pragma unroll
    for (int i = 0; i < 6; ++i) {
      const int idx = i * 64 + lane, r = idx / 24;
      *(u32x4*)(slot + r * 384 + (((idx - r * 24) ^ (r >> 1)) * 16)) = wa[u][i];
    }
    __builtin_amdgcn_fence(__ATOMIC_RELEASE, "wavefront"); __builtin_amdgcn_wave_barrier();
#pragma unroll
    for (int e = 0; e < 6; ++e) A[u][e] = *(const bf16x8*)(slot + c * 384 + (((4 * e + g) ^ (c >> 1)) * 16));
    __builtin_amdgcn_fence(__ATOMIC_RELEASE, "wavefront"); __builtin_amdgcn_wave_barrier();
  }
  BAR();
  PROT(1);
  if (PF_POS == 0) asm volatile("" :: "v"(pf[0]), "v"(pf[1]));
  if (PF_POS == 1) prefetch();
  {
    const unsigned xf = (unsigned)((b >> 1) * 20480 + (tid >> 3) * 256 + (b & 1) * 128 + (tid & 7) * 16);
    const __amdgpu_buffer_rsrc_t rx = __builtin_amdgcn_make_buffer_rsrc(a.ws + OFF_XN, 0, (int)(OFF_QB - OFF_XN), 0x00020000);
    __builtin_amdgcn_raw_buffer_store_b128(xst[1][tid ^ ((tid >> 3) & 7)], rx, xf, 0, X_AUX);
    __builtin_amdgcn_raw_buffer_store_b128(xst[0][tid ^ ((tid >> 3) & 7)], rx, xf + (unsigned)(OFF_XB - OFF_XN), 0, X_AUX);
    if (v < 8 && lane < 4) ((float*)(a.ws + OFF_XNRM))[xrow * 4 + lane] = lane == 0 ? xn2 : 0.f;
  }
  if (job < 800) {
    const __amdgpu_buffer_rsrc_t rt = __builtin_amdgcn_make_buffer_rsrc(a.ws + (job < 600 ? OFF_WQKVBT : OFF_WOBT) + ((size_t)jn * 80 + jk) * 1024, 0, 81920, 0x00020000);
#pragma unroll
    for (int t = 0; t < 4; ++t) __builtin_amdgcn_raw_buffer_store_b128(__builtin_bit_cast(u32x4, tf[t]), rt, lane * 16 + t * 20480, 0, T_AUX);
  }
  const int gp = h >> 1, hh = h & 1;
  const __amdgpu_buffer_rsrc_t rwb = __builtin_amdgcn_make_buffer_rsrc(a.ws + OFF_WB, 0, (int)WB_BYTES, 0x00020000);
  const unsigned phi = WB_NEAR ? ((((unsigned)b >> 4) & 1u) ^ (unsigned)(latw[8] < latw[9]) ^ (WB_NEAR - 1)) << 13 : (unsigned)(b & 32) << 8;
  if (tid == 0) ((unsigned*)(a.ws + OFF_PHI))[b] = phi;
#pragma unroll
  for (int n = 0; n < 10; ++n) {
    const int off = n * 32 + 8 * (c & 3), T = cq * 10 + n;
    const char* bp = img + (8 * g + (c >> 2)) * 320 + (off ^ ((g & 1) << 5));
    f32x4 ac[2] = {{0.f, 0.f, 0.f, 0.f}, {0.f, 0.f, 0.f, 0.f}};
#pragma unroll
    for (int e = 0; e < 6; ++e) {
      const bf16x8 bf = tr_frag(bp + e * 32 * 320, bp + (e * 32 + 4) * 320);
      ac[0] = mfma32(A[0][e], bf, ac[0]); ac[1] = mfma32(A[1][e], bf, ac[1]);
    }
    const u32x4 o = {pack2(ac[0][0], ac[0][1]), pack2(ac[0][2], ac[0][3]), pack2(ac[1][0], ac[1][1]), pack2(ac[1][2], ac[1][3])};
    const unsigned oo = (T < 32 ? wb_off(rec_wg(gp, T & 15) * 100 + (hh * 3 + (T >> 4)) * 20 + ks) + (unsigned)(lane * 16)
                                : wb_off(rec_wg(gp, 2 * (T - 32) + (c >> 3)) * 100 + 40 + ks) + (unsigned)((g * 16 + hh * 8 + (c & 7)) * 16)) ^ phi;
    __builtin_amdgcn_raw_buffer_store_b128(o, rwb, oo, 0, B_AUX);
    float n2 = 0.f;
#pragma unroll
    for (int r = 0; r < 4; ++r) n2 += ac[0][r] * ac[0][r] + ac[1][r] * ac[1][r];
    n2 = x32sum(x16sum(n2));
    if (g == 0) red[v][n * 16 + c] = n2;
  }
  PROT(2);
  if (PF_POS == 1) asm volatile("" :: "v"(pf[0]), "v"(pf[1]));
  if (v >= 8 && b < 64 && lane == 0) ((unsigned*)(a.ws + OFF_LAT))[(v - 4) * 64 + b] = umin(lat, 60000u);
  if (WARM_CODE && v == 7) {
    const char* pc; int d0, d1;
    asm("s_getpc_b64 %0\n\ts_mov_b32 %1, _Z10rec_kernel7RecArgs@rel32@lo+4\n\ts_mov_b32 %2, _Z11attb_kernelPcPt@rel32@lo+12" : "=s"(pc), "=s"(d0), "=s"(d1));
    const char *c0 = pc + d0, *c1 = pc + d1 + ATT_CODE;
    const long o = ((long)b * 64 + lane) * 64;
    if (o < c1 - c0) asm volatile("" :: "v"(*(const volatile GAS unsigned*)(c0 + o)));
  }
  if (v == 9 && lane < 32) {
    const int q = b * 32 + lane;
    *(u32x4*)(a.ws + OFF_KV + (size_t)(q >> 2) * KVT + 1280 + (q & 3) * 336 + 320) = (u32x4){0x3f803f80u, 0x3f803f80u, 0x3f803f80u, 0x3f803f80u};
  }
  BAR();
  if (v == 1 && lane == 0) ((unsigned*)(a.ws + OFF_LAT))[b] = umin(umin(umin(latw[0], latw[4]) + umin(latw[1], latw[5]), 30000u) + umin(umin(latw[2], latw[6]) + umin(latw[3], latw[7]), 30000u), 60000u);
  if (v == 0) {
    float mx = 0.f;
#pragma unroll
    for (int k = 0; k < 3; ++k) {
      const int col = k * 64 + lane;
      float sm = 0.f;
#pragma unroll
      for (int i = 0; i < 10; ++i) sm += red[i][col < 160 ? col : 0];
      mx = fmx(mx, sm);
    }
    mx = fmx(mx, dppf<DPP_XOR1>(mx)); mx = fmx(mx, dppf<DPP_XOR2>(mx)); mx = fmx(mx, dppf<DPP_ROR4>(mx)); mx = fmx(mx, dppf<DPP_ROR8>(mx));
    mx = x32mx(x16mx(mx));
    if (lane == 0) ((float*)(a.ws + OFF_BNRM))[(cq * 32 + h) * 2 + dh] = mx;
  }
  PROT(3);
}

struct RecArgs {
  const u16* mem0; const u16* wq; const u16* wk; const u16* xold; u16* xnew; char* ws;
  unsigned layer; unsigned pad;
};
#define RLX_AGENT __ATOMIC_RELAXED, __HIP_MEMORY_SCOPE_AGENT
constexpr unsigned SPIN_CAP = 1u << 20;
#define TSTAMP(k, v)
#define ARRIVE(k)
#define ARRIVE_OUT()
#define ARRIVE_DECL()
#define LOGV(k, v)
#define LOGF(k, v)
#define LOGP(k)
#define LOGQ(k)
#define LOGF2(k)
#define LOGD(k)
#define LOGT(k, v)
#define STAMP_DECL()
#define STAMP(k)
#define STAMPV(k, v)
#define STAMP_OUT()
#define REC_D1 520
#define SCH_V 10
#define SCH_VMIN 0
#define SCH_DMIN 4
#define SCH_PEN 170
#define SCH_VMIN2 (-4)
#define SCH_PEN2 40
#define SCH_GU 1
#define SCH_GD 2
#define SCH_UP 6
#define SCH_D0 196
#define SCH_FIRST 120
#define EXP_NODMA 0
#define BG_HOLD 220
#define QBATCH 0
#define WPF_N 5
#define WPF_W 0
#define WPF0 (63 - WPF_N)
#define BG_HOLD4 250
#define D1_W1 (-10)
#define F3_D 1200
#define T2W 2
#define AT_ASM 1
#define PM_Z 1
#define P2_X 1
#define ST_KS 14
#define ST_KS0 ST_KS
#define ST_KS1 ST_KS
#define PADR 0x6666
#define PADT 0x000
#define CODE_PAD(n) asm volatile(".p2align 6\n.rept %0\ns_nop 0\n.endr" :: "n"(n))
#define ST_X 270
#define ST_ALL 4
#define CPF_S 61
#define FR_D 700
#define FR_PACE 6
#define END_E60 600
#define END_E1 1300
#define END_E2 3400
#define END_E62 2800
#define END_E3 6800
#define END_E4 11400
#define EARLY_N 6
#define EARLY_D1 80
#define EARLY_V 10
#define EARLY_V1 18
#define F3 0
#define F3A 0
#define F3A_D 400
#define T4_D 300
#define T4_P 1
#define P2_W 1
#define PS_SL 1
#define F2B 4
#define F0C 0
#define F2C 0
#define W2_P 1
#define SCH_W 1
#define SP_SL 0
#define D1_M 320
#define D1_MIN 290
#define D1_C 96
#define D1_G 2
#define BG_HOLD6 0
#define W2_D 0
#define END_E61 2600
#define D1_LATE 200
#define P2_G0 10
#define P2_G2 6
#define P2_G3 6
#define SCH_RL 0
#define SCH_P 3
#define MMA_P 1
#define SP_P 0
#define F6 8
#define F7 10
#define XPF_W 2
#define SCH_LATE 0
#define LATE_V 8
#define QW 3
#define FB_SL 0
#define P2_G1 1
#define P1_G 0
#define F4Z 0
#define F5Z 0
#define T5_Z 2
#define F0A 3
#define FB_E 0
#define EXACT 1
#define D1_P 3
#define VO_A0 9
#define VO_A2 10
#define VO_B0 3
#define VO_B1 0
#define VO_N2 7
#define VO_N3 10
#define VO_W -1
#define UN_P 2
#define VP_E 1
#define TS_B 0
#define ST_E 1
#define W1_SL 0
#define VO_A1 4
#define LAST_V 22
#define END_E59 3200
#define END_G2 600
#define END_G3 600
#define QR 4
#define WPF_S 128
#define END_SL 4
#define SCH_DR 182
#define SCH_DK 3
#define FR_HOLD 0
#define FW 8
#define PR_P 0
#define REL_AT 1
#define XO_T 4
#define Q_AUX 0
#define O_WT 0
#define P_WT 0
#define HOLD67 0
#define H6_SL 0
#define H7_SL 0
#define PK_ADD 1
#define KP0 0
#define KP1 0
#define XPF 3
#define W1_D 0
#define F4 12
#define F5 0
#define F67 0
#define KP2 0
#define REC_RNG 70u
#define SCH_DRAIN 40
#define PL_X 0
#define FC(k)
#define PIN_V(x) asm volatile("" : "+v"(x))
#define PIN_S(x) asm volatile("" : "+s"(x))
#define VM_DRAIN() do { asm volatile("s_waitcnt vmcnt(0)" ::: "memory"); __builtin_amdgcn_s_waitcnt(0x0F70); } while (0)
#define SC1_LOAD(off) __builtin_amdgcn_raw_buffer_load_b128(rs, (off), 0, 16)
#define SC1_LOADS(off, soff) __builtin_amdgcn_raw_buffer_load_b128(rs, (off), (soff), 16)
#define SC1_STORE8S(v, off, soff) __builtin_amdgcn_raw_buffer_store_b64((v), rs, (off), (soff), 16)
#define SC1_LOAD8(off) __builtin_amdgcn_raw_buffer_load_b64(rs, (off), 0, 16)
#define SC1_STORE(v, off) __builtin_amdgcn_raw_buffer_store_b128((v), rs, (off), 0, 16)
#define SC1_STORE8(v, off) __builtin_amdgcn_raw_buffer_store_b64((v), rs, (off), 0, 16)
#if defined(__HIP_DEVICE_COMPILE__)
#pragma clang attribute push(__attribute__((target("no-packed-fp32-ops"))), apply_to = function)
#endif
__device__ __forceinline__ unsigned now32() { return (unsigned)__builtin_amdgcn_s_memrealtime(); }
__device__ __forceinline__ void wait_until(unsigned t) { for (;;) { const int d = (int)(t - now32()); if (d <= 0) break; if (d > 8) __builtin_amdgcn_s_sleep(1); } }
__device__ __forceinline__ unsigned cyc32() { return (unsigned)__builtin_amdgcn_s_memtime(); }
__device__ __forceinline__ void wait_cyc(unsigned t) { for (;;) { const int d = (int)(t - cyc32()); if (d <= 0) break; if (d > 200) __builtin_amdgcn_s_sleep(1); } }
#define WAIT_ASM(clk, far_) "1:\n\t" clk " vcc\n\ts_waitcnt lgkmcnt(0)\n\ts_sub_i32 %0, %1, vcc_lo\n\ts_cmp_lt_i32 %0, " far_ "\n\ts_cbranch_scc1 3f\n\ts_sleep 1\n\ts_branch 1b\n" \
                            "2:\n\t" clk " vcc\n\ts_waitcnt lgkmcnt(0)\n\ts_sub_i32 %0, %1, vcc_lo\n3:\n\ts_cmp_gt_i32 %0, 0\n\ts_cbranch_scc1 2b"
__device__ __forceinline__ int spin_until(unsigned t) { int d; asm volatile(WAIT_ASM("s_memrealtime", "9") : "=&s"(d) : "s"(t) : "vcc", "scc", "memory"); return d; }
__device__ __forceinline__ int spin_cyc(unsigned t) { int d; asm volatile(WAIT_ASM("s_memtime", "200") : "=&s"(d) : "s"(t) : "vcc", "scc", "memory"); return d; }
__device__ __forceinline__ void spin_exact(unsigned t) {
  if (!EXACT) { spin_cyc(t); return; }
  int d;
  asm volatile("1:\n\ts_memtime vcc\n\ts_waitcnt lgkmcnt(0)\n\ts_sub_i32 %0, %1, vcc_lo\n\ts_cmp_gt_i32 %0, 95\n\ts_cbranch_scc1 1b\n\t"
               "s_max_i32 %0, %0, 0\n\ts_andn2_b32 %0, %0, 3\n\ts_sub_i32 %0, 3f-2f, %0\n\ts_getpc_b64 vcc\n"
               "2:\n\ts_add_u32 vcc_lo, vcc_lo, %0\n\ts_addc_u32 vcc_hi, vcc_hi, 0\n\ts_setpc_b64 vcc\n\t.rept 24\n\ts_nop 0\n\t.endr\n3:"
               : "=&s"(d) : "s"(t) : "scc", "vcc", "memory");
}
template <int OFF, int SL> __device__ __forceinline__ void lds_spin(unsigned lds0, unsigned want) {
  unsigned v;
  asm volatile("1:\n\tds_read_b32 %0, %1 offset:%3\n\t.if %4\n\ts_sleep %4\n\t.endif\n\ts_waitcnt lgkmcnt(0)\n\tv_cmp_gt_u32 vcc, %2, %0\n\ts_cbranch_vccnz 1b"
               : "=&v"(v) : "v"(lds0), "s"(want), "n"(OFF), "n"(SL) : "vcc", "memory");
}
template <int CTRL> __device__ __forceinline__ int dppi(int v) { return __builtin_amdgcn_mov_dpp(v, CTRL, 0xf, 0xf, true); }
__device__ __forceinline__ int wave_imax(int l) {
  l = imax(l, dppi<DPP_XOR1>(l)); l = imax(l, dppi<DPP_XOR2>(l)); l = imax(l, dppi<DPP_ROR4>(l)); l = imax(l, dppi<DPP_ROR8>(l));
  int a = imax(__builtin_amdgcn_readlane(l, 0), __builtin_amdgcn_readlane(l, 16)), b = imax(__builtin_amdgcn_readlane(l, 32), __builtin_amdgcn_readlane(l, 48));
  PIN_S(a); PIN_S(b);
  return imax(a, b);
}
__device__ __forceinline__ void glds16(const void* gsrc, unsigned lds_dst) {
  unsigned keep;
  asm volatile("s_mov_b32 %0, m0\n\ts_mov_b32 m0, %2\n\ts_nop 0\n\tglobal_load_lds_dwordx4 %1, off\n\ts_mov_b32 m0, %0" : "=&s"(keep) : "v"(gsrc), "s"(lds_dst) : "memory");
}
__device__ __forceinline__ void glds16_sc1(const void* gsrc, unsigned lds_dst) {
  unsigned keep;
  asm volatile("s_mov_b32 %0, m0\n\ts_mov_b32 m0, %2\n\ts_nop 0\n\tglobal_load_lds_dwordx4 %1, off sc1\n\ts_mov_b32 m0, %0" : "=&s"(keep) : "v"(gsrc), "s"(lds_dst) : "memory");
}
__device__ __forceinline__ unsigned acc_off(int G, int row) {
  return (unsigned)(G < 128 ? ((G & 31) | ((G & 32) << 1) | ((G & 64) >> 1)) * 256 + row * 16 : (2 * G - 128 + (row >> 2)) * 256 + (row & 3) * 16);
}
__device__ __forceinline__ unsigned nz(unsigned v) { return v > 1u ? v : 1u; }
__device__ __forceinline__ unsigned pack2n(float lo, float hi) { return nz(pack2(lo, hi)); }
__device__ __forceinline__ unsigned min4(u32x4 v) { return umin(umin(v[0], v[1]), umin(v[2], v[3])); }
__device__ __forceinline__ bf16x8 as8(u32x4 v) { return __builtin_bit_cast(bf16x8, v); }

constexpr int NV = 7, NKQ = 2;
constexpr int L_RED1 = 0;
constexpr int L_BM = L_RED1 + 8 * 1536;
constexpr int L_STG = L_BM + 10240;
constexpr int L_ST = L_STG + 2048 + 2560;
constexpr int L_MISC = L_ST + 512;
constexpr int L_KX = L_MISC + 128;
constexpr int L_QX = L_KX + NKQ * 2048;
constexpr int L_XN = L_QX + NKQ * 2048;
constexpr int L_VOX = L_XN + 81920;
constexpr int L_VOM = L_VOX + NV * 5120;
constexpr int L_BYTES = L_VOM + NV * 1280;
constexpr int S_IMG = L_XN + 40960;
static_assert(S_IMG + 61440 <= L_VOM, "start-up image");
static_assert(L_BYTES <= 160 * 1024, "LDS budget");

__device__ __forceinline__ bf16x8 wqk_frag(const char* lds, int t, int ks, int lane) {
  const int c = lane & 15, g = lane >> 4, p = c & 3;
  const char* q = lds + S_IMG + (32 * ks + 8 * g + (c >> 2)) * 96 + 8 * p + (t == 0 ? 0 : (t == 1 ? 48 : (p < 2 ? 32 : 64)));
  const v4i16_t lo = __builtin_amdgcn_ds_read_tr16_b64_v4i16((LAS v4i16_t*)q), hi = __builtin_amdgcn_ds_read_tr16_b64_v4i16((LAS v4i16_t*)(q + 4 * 96));
  return (bf16x8){lo[0], lo[1], lo[2], lo[3], hi[0], hi[1], hi[2], hi[3]};
}
__device__ __forceinline__ bf16x8 sel_frag(int c, int base) {
  bf16x8 r;
#pragma unroll
  for (int i = 0; i < 8; ++i) r[i] = (c == base + i) ? (short)0x3f80 : (short)0;
  return r;
}
__device__ __forceinline__ bf16x8 qk_frag(const char* p4, const char* p2, const char* p2b) {
  f32x4 a0 = *(const f32x4*)(p4), a1 = *(const f32x4*)(p4 + 1792), a2 = *(const f32x4*)(p4 + 3584), a3 = *(const f32x4*)(p4 + 5376);
  f32x2 b0 = *(const f32x2*)(p2), b1 = *(const f32x2*)(p2 + 1792), b2 = *(const f32x2*)(p2b), b3 = *(const f32x2*)(p2b + 1792);
  f32x4 va = (a0 + a1) + (a2 + a3);
  f32x2 vb = (b0 + b1) + (b2 + b3);
  return mk8(pack2(va[0], va[1]), pack2(va[2], va[3]), pack2(vb[0], vb[1]), 0u);
}
__device__ __forceinline__ void x_fetch(const u16* src, unsigned lds0, int seg, int p, int lane) {
  if (EXP_NODMA) return;
  glds16_sc1(src + (size_t)p * 512 + lane * 8, lds0 + L_XN + (seg & 1) * 40960 + p * 1024);
}
template <int N> __device__ __forceinline__ void x_fetch_n(const u16* src, unsigned lds0, int seg, int p0, unsigned voff) {
  if (EXP_NODMA) return;
#pragma unroll
  for (int i = 0; i < N; i += 4) {
    unsigned keep;
    asm volatile("s_mov_b32 %0, m0\n\ts_mov_b32 m0, %3\n\ts_nop 0\n\tglobal_load_lds_dwordx4 %1, %2 sc1\n\t"
                 ".if %4 > 1\n\tglobal_load_lds_dwordx4 %1, %2 offset:1024 sc1\n\t.endif\n\t.if %4 > 2\n\tglobal_load_lds_dwordx4 %1, %2 offset:2048 sc1\n\t.endif\n\t"
                 ".if %4 > 3\n\tglobal_load_lds_dwordx4 %1, %2 offset:3072 sc1\n\t.endif\n\ts_mov_b32 m0, %0"
                 : "=&s"(keep) : "v"(voff), "s"(src + (size_t)(p0 + i) * 512), "s"(lds0 + L_XN + (seg & 1) * 40960 + (p0 + i) * 1024), "n"(N - i < 4 ? N - i : 4) : "memory");
  }
}
__device__ __forceinline__ int qkv_row(int b) { return (b & 7) / QR * (2 * QR) + (b >> 3) / (16 / QR); }
__device__ __forceinline__ int qkv_col(int b) { return (b & 7) % QR * (16 / QR) + (b >> 3) % (16 / QR); }
__device__ __forceinline__ const u16* x_src(const RecArgs& a, int seg, int b) {
  return seg < NSEG ? (const u16*)(a.ws + OFF_XN) + (size_t)seg * 32 * DM : a.xnew + (size_t)(qkv_row(b) + 16 * (seg - NSEG)) * 32 * DM;
}
template <int N>
__device__ __forceinline__ void img_fetch(const RecArgs& a, unsigned lds0, int h, int j, int part, int lane) {
#pragma unroll
  for (int i = 0; i < N; ++i) {
    const int n = part * N + i, idx = n * 64 + lane, row = idx / 6, pc = idx - row * 6;
    if (FR_PACE && N == 20) asm volatile("s_waitcnt vmcnt(%0)" :: "n"(FR_PACE) : "memory");
    glds16((pc < 3 ? a.wq : a.wk - 24) + (size_t)row * HD + h * DH + j * 24 + pc * 8, lds0 + S_IMG + n * 1024);
  }
}
constexpr int P2B = F4 + F5 + 2 * F67 + F6 + F7, PZ = P2B + F2B, PA = PZ, P0C = PA + 2 * F0A, F3C = 40 - F3A - F3 - P0C - 2 * F0C - F2C;
static_assert(F3C >= 0, "fetch plan");
template <int OFF> __device__ __forceinline__ void lds_inc(unsigned lds0) {
  asm volatile("s_mov_b64 exec, 1\n\tds_add_u32 %0, %1 offset:%2\n\ts_mov_b64 exec, -1" :: "v"(lds0), "v"(1u), "n"(OFF) : "memory");
}
__device__ __forceinline__ void lds_arrive(unsigned* p, int lane) {
  asm volatile("s_waitcnt lgkmcnt(0)" ::: "memory");
  if (lane == 0) __hip_atomic_fetch_add(p, 1u, __ATOMIC_RELAXED, __HIP_MEMORY_SCOPE_WORKGROUP);
}
__device__ __forceinline__ void lds_wait(unsigned* p, unsigned want) {
  while (__hip_atomic_load(p, __ATOMIC_RELAXED, __HIP_MEMORY_SCOPE_WORKGROUP) < want) __builtin_amdgcn_s_sleep(1);
  asm volatile("" ::: "memory");
}
constexpr float KEXP = 0.07216878364870322f * 1.4426950408889634f;
#define FRESH_LANE() int lane = lane0; asm volatile("" : "+v"(lane)); const int c = lane & 15, g = lane >> 4; (void)c; (void)g

struct OutTask {
  u32x4 y[4]; u32x2 xo, out;
  bf16x8 sel;
  unsigned t5f, t5o;
  __device__ __forceinline__ void init(int b, int lane) {
    const int um = b / 10, uq = (b / 5) & 1, up = b % 5, tok = lane & 15, k = up < 4 ? (up >> 1) * 256 + um * 16 + (up & 1) * 8 : 512 + um * 8, q4 = ((lane >> 4) & 1) * 4;
    t5f = (unsigned)(((uq * 20 + (k >> 5)) * 64 + ((k & 31) >> 3) * 16 + tok) * 8 + q4);
    t5o = (unsigned)(((um * 2 + uq) * 16 + (lane >> 4)) * 1280 + up * 256 + tok * 16);
    if (b >= 160) { t5f = 0u; t5o = 0x7ffffff0u; }
    sel = sel_frag(tok, 0);
    PIN_V(sel);
  }
  static __device__ __forceinline__ bool ok(int sl, int b) { return sl >= 0 && b < 160; }
  __device__ __forceinline__ void load(const RecArgs& a, const __amdgpu_buffer_rsrc_t rs, int sl, int b, int part = 3) {
    const unsigned o = t5o | (sl < 0 ? 0x7ff00000u : 0u);
    sl = imax(sl, 0);
#pragma unroll
    for (int k = 0; k < 4; ++k) if (part & 1) y[k] = SC1_LOADS(o, op_slot(sl) + k * 5120);
    if (part & 2) xo = __builtin_bit_cast(u32x2, __hip_atomic_load((const gu64*)(a.xold + (size_t)sl * 32 * DM + t5f), RLX_AGENT));
  }
  __device__ __forceinline__ bool ready(int b) { return b >= 160 || wave_all(umin(umin(min4(y[0]), min4(y[1])), umin(min4(y[2]), min4(y[3]))) != 0u); }
  __device__ __forceinline__ void run(int lane) {
    f32x4 d = {0.f, 0.f, 0.f, 0.f};
#pragma unroll
    for (int k = 0; k < 4; ++k) d = mfma32(sel, as8(y[k]), d);
#pragma unroll
    for (int i = 0; i < 2; ++i) {
      const unsigned r = pack2(d[2 * i], d[2 * i + 1]), x = xo[i];
      out[i] = pack2(u2f(x << 16) + u2f(r << 16), u2f(x & 0xffff0000u) + u2f(r & 0xffff0000u));
    }
    PIN_V(out);
  }
  __device__ __forceinline__ void store(const RecArgs& a, int sl, int b, int lane) {
    __builtin_amdgcn_raw_buffer_store_b64(out, __builtin_amdgcn_make_buffer_rsrc(a.xnew, 0, ok(sl, b) ? S_ * DM * 2 : 0, 0x00020000), (t5f + (unsigned)sl * 32 * DM) * 2u | (unsigned)(lane & 32) << 25, 0, 16);
  }
};

struct PvTask2 {
  u32x4 y[2][2], pa[2], pb[2];
  unsigned lo, lm, vr, ma, mz, so, sz;
  __device__ __forceinline__ void init(int gp, int lane) {
    const int hi = lane >> 5;
    lo = pt_tok(2 * gp) + (unsigned)(lane * 16); lm = pt_mem(2 * gp + hi) + (unsigned)((lane & 31) * 8);
    vr = (unsigned)(L_VOX + 2048 + (lane ^ 8) * 16);
    ma = (unsigned)(L_VOM + hi * 768 + (lane & 31) * 8); mz = (unsigned)(L_VOM + 512 + ((lane & 31) ^ (hi * 8)) * 8);
    so = (unsigned)(L_STG + 2048 + (hi * 16 + (lane & 15)) * 16 + ((lane >> 4) & 1) * 8);
    sz = hi ? (unsigned)(L_RED1 + 8704 + lane * 8) : so + 1024;
    PIN_V(lo); PIN_V(lm); PIN_V(vr); PIN_V(ma); PIN_V(mz); PIN_V(so); PIN_V(sz);
#pragma unroll
    for (int q = 0; q < 2; ++q) { pa[q] = pb[q] = y[q][0] = y[q][1] = (u32x4){0u, 0u, 0u, 0u}; PIN_V(pa[q]); PIN_V(pb[q]); }
  }
  __device__ __forceinline__ void load(const __amdgpu_buffer_rsrc_t rs, unsigned o) {
#pragma unroll
    for (int q = 0; q < 2; ++q) {
      y[q][0] = SC1_LOADS(lo + q * 1024, o); y[q][1] = SC1_LOADS(lo + 2048 + q * 1024, o);
      const u32x2 p = __builtin_amdgcn_raw_buffer_load_b64(rs, lm + q * 256, o, 16);
      pa[q][0] = p[0]; pa[q][1] = p[1];
    }
  }
  __device__ __forceinline__ bool ready() {
    return wave_all(umin(umin(umin(min4(y[0][0]), min4(y[0][1])), umin(min4(y[1][0]), min4(y[1][1]))), umin(umin(pa[0][0], pa[0][1]), umin(pa[1][0], pa[1][1]))) != 0u);
  }
  template <bool DIRECT> __device__ __forceinline__ void run(char* lds, int sv, int lane, const __amdgpu_buffer_rsrc_t rs, unsigned og) {
    const f32x4 Z4 = {0.f, 0.f, 0.f, 0.f};
    const char* vp = lds + L_VOX + sv * 5120 + lane * 16;
    bf16x8 vx[6];
#pragma unroll
    for (int t = 0; t < 5; ++t) vx[t] = *(const bf16x8*)(vp + t * 1024);
    vx[5] = *(const bf16x8*)(lds + vr + sv * 5120);
    const u32x2 a0 = *(const u32x2*)(lds + ma + sv * 1280), a1 = *(const u32x2*)(lds + ma + sv * 1280 + 256);
    const u32x2 z0 = *(const u32x2*)(lds + mz + sv * 1280), z1 = *(const u32x2*)(lds + mz + sv * 1280 + 256);
    const bf16x8 vm = mk8(a0[0], a0[1], a1[0], a1[1]), vz = mk8(z0[0], z0[1], z1[0], z1[1]);
#pragma unroll
    for (int q = 0; q < 2; ++q) { PIN_V(pa[q]); pb[q][2] = pa[q][0]; pb[q][3] = pa[q][1]; PIN_V(pb[q]); }
    __builtin_amdgcn_sched_barrier(0);
    f32x4 o[2][3];
#pragma unroll
    for (int q = 0; q < 2; ++q) {
#pragma unroll
      for (int t = 0; t < 3; ++t) o[q][t] = mfma32(vx[t], as8(y[q][0]), Z4);
#pragma unroll
      for (int t = 0; t < 3; ++t) o[q][t] = mfma32(vx[3 + t], as8(y[q][1]), o[q][t]);
      o[q][0] = mfma32(vm, as8(pa[q]), o[q][0]); o[q][1] = mfma32(vm, as8(pb[q]), o[q][1]); o[q][2] = mfma32(vz, as8(pa[q]), o[q][2]);
    }
#pragma unroll
    for (int q = 0; q < 2; ++q) {
#pragma unroll
      for (int t = 0; t < 3; ++t) {
        const u32x2 v = {pack2n(o[q][t][0], o[q][t][1]), pack2n(o[q][t][2], o[q][t][3])};
        if (DIRECT) SC1_STORE8S(v, (so - (L_STG + 2048) + t * 512) | (t == 2 ? (unsigned)(lane & 32) << 25 : 0u), og + q * 20480);
        else *(u32x2*)(lds + (t < 2 ? so + t * 512 : sz) + q * 1280) = v;
      }
    }
  }
};

struct TokScores {
  bf16x8 k[2], q[2]; u32x4 v[2];
  __device__ __forceinline__ void get(const char* lds, int seg, int lane) {
#pragma unroll
    for (int i = 0; i < 2; ++i) { k[i] = *(const bf16x8*)(lds + L_KX + (seg & 1) * 2048 + i * 1024 + lane * 16); q[i] = *(const bf16x8*)(lds + L_QX + (seg & 1) * 2048 + i * 1024 + lane * 16); }
  }
  __device__ __forceinline__ void run(int qt) {
    const f32x4 Z4 = {0.f, 0.f, 0.f, 0.f};
    const f32x4 d1 = mfma32(k[0], q[qt], Z4), d2 = mfma32(k[1], q[qt], Z4);
    v[qt] = (u32x4){pack2n(d1[0], d1[1]), pack2n(d1[2], d1[3]), pack2n(d2[0], d2[1]), pack2n(d2[2], d2[3])};
    PIN_V(v[qt]);
  }
  __device__ __forceinline__ void store(const __amdgpu_buffer_rsrc_t rs, unsigned off, unsigned o) {
    __builtin_amdgcn_raw_buffer_store_b128(v[0], rs, off, o, 16); __builtin_amdgcn_raw_buffer_store_b128(v[1], rs, off + 1024, o, 16);
    asm volatile("s_nop 1" ::: "memory");
  }
};

__device__ __forceinline__ void touch(const char* base, unsigned off, unsigned lds0) {
  unsigned keep;
  asm volatile("s_mov_b32 %0, m0\n\ts_mov_b32 m0, %3\n\ts_nop 0\n\tglobal_load_lds_dword %1, %2 sc1\n\ts_mov_b32 m0, %0" : "=&s"(keep) : "v"(off), "s"(base), "s"(lds0 + L_RED1 + 8192) : "memory");
}
template <int N> __device__ __forceinline__ void stouch(const char* p, unsigned& d) {
  asm volatile(".set pf_o, 0\n\t.rept %2\n\ts_load_dword %0, %1, pf_o\n\t.set pf_o, pf_o + 128\n\t.endr" : "+s"(d) : "s"(p), "n"(N) : "memory");
}
__device__ __forceinline__ void code_touch(int b, int lane, unsigned lds0) {
  const char* pc; int d0, d1;
  asm volatile("s_getpc_b64 %0\n\ts_mov_b32 %1, _Z10rec_kernel7RecArgs@rel32@lo+4\n\ts_mov_b32 %2, _Z11attb_kernelPcPt@rel32@lo+12" : "=s"(pc), "=s"(d0), "=s"(d1));
  const int o = (lane * 32 + (b >> 3)) * 128;
  touch(pc + d0, (unsigned)(o < d1 - d0 ? o : 0), lds0);
}
__device__ __forceinline__ unsigned zero_base(int b, int h, int j, int ln) {
  return ln < 32 ? (unsigned)((h & 3) * 32768 + j * 512 + ln * 16) : ln < 40 ? (unsigned)((h & 2) * 1024 + j * 256 + (ln & 4) * 32 + (h & 1) * 64 + (ln & 3) * 16)
                 : (unsigned)OFF_ACC + (b < ACC_NB ? (unsigned)(b * 256 + (ln - 40) * 16) : ts_piece((b - ACC_NB) * 8 + ln - 40));
}
__device__ __forceinline__ unsigned sct_slot(unsigned ck, unsigned sl) { return sl * SCT_STEP + ((ck >> (sl & 4u)) & 7u) * 4096u; }
__device__ __forceinline__ unsigned scm_slot(unsigned ck, unsigned sl) { return OFF_SCM + sl * SCM_STEP + ((ck >> 8) & 7u) * 4096u; }
__device__ __forceinline__ int ck_rot(int x) { return ((x & 1) << 2) | (x >> 1); }
struct Place {
  unsigned t[8];
  __device__ __forceinline__ void load(const char* ws, int lane) {
#pragma unroll
    for (int x = 0; x < 8; ++x) t[x] = *(const unsigned*)(ws + OFF_LAT + ((imin(lane >> 3, 5) * 8 + (ck_rot(x) ^ (lane & 7))) * 8 + x) * 4);
  }
  __device__ __forceinline__ unsigned get(int b, int lane) {
    int c = (int)((((t[0] + t[1]) + (t[2] + t[3])) + ((t[4] + t[5]) + (t[6] + t[7]))) * 8u) + (lane & 7);
    c = imin(c, dppi<DPP_XOR1>(c)); c = imin(c, dppi<DPP_XOR2>(c)); c = imin(c, dppi<0x141>(c));
    unsigned k = 0u;
#pragma unroll
    for (int r = 0; r < 6; ++r) k |= (unsigned)(__builtin_amdgcn_readlane(c, 8 * r) & 7) << (4 * r);
    return (k ^ PL_X) ^ (unsigned)ck_rot(b & 7) * 0x111111u;
  }
};

struct Sched {
  u32x4 tsv, rec;
  unsigned side, o;
  __device__ __forceinline__ void pre(int s) { o = (unsigned)OFF_ACC + (unsigned)((s + SR - 1) % SR) * (unsigned)ACC_STEP + ACC_TS + side; PIN_S(o); }
  __device__ __forceinline__ void load(const __amdgpu_buffer_rsrc_t rs, int lane) {
    tsv = SC1_LOADS(ts_piece(lane & 63) - ACC_TS, o); rec = SC1_LOADS(128, o);
  }
  __device__ __forceinline__ unsigned last(unsigned ref) {
    const unsigned o = 0x40000000u - ref;
    int l = (int)umax(umax(tsv[0] ? tsv[0] + o : 0u, tsv[1] ? tsv[1] + o : 0u), umax(tsv[2] ? tsv[2] + o : 0u, tsv[3] ? tsv[3] + o : 0u));
    return (unsigned)wave_imax(l) - o;
  }
  __device__ __forceinline__ unsigned run(const RecArgs& a, const __amdgpu_buffer_rsrc_t rs, int s, int b, int lane) {
    constexpr int w = 3; (void)w;
    unsigned acur = __builtin_amdgcn_readfirstlane(rec[0]), aprev = __builtin_amdgcn_readfirstlane(rec[1]), hist = __builtin_amdgcn_readfirstlane(rec[3]);
    int D8 = (int)__builtin_amdgcn_readfirstlane(rec[2]);
    const int d = wave_imax((int)(umax(umax(tsv[0], tsv[1]), umax(tsv[2], tsv[3])) - aprev)), dd = 8 * d - D8;
    int Dn = D8 + (dd >> (dd > 0 ? SCH_GU : SCH_GD));
    unsigned anext = (acur + (unsigned)((Dn + 4 + imax(Dn - 8 * SCH_DR, 0) * SCH_DK / 8) >> 3) + SCH_V + (s < EARLY_N ? EARLY_V : (s >= WPF0 ? (s == NSEG - 2 ? LAST_V : LATE_V) : 0))) | 1u;
    if (__builtin_expect(hist < 2u || aprev == 0u || d < 1 || d + SCH_VMIN > (int)(acur - aprev), 0)) {
      if (s == 0) {
        for (int it = 0; it < 40 && !wave_all(min4(tsv) != 0u); ++it) {
          __builtin_amdgcn_s_sleep(ST_ALL);
          asm volatile("" ::: "memory");
          load(rs, lane);
        }
      }
      const unsigned now = now32(), L = last(now);
      if (acur == 0u) { D8 = 8 * SCH_D0; hist = 0u; }
      const bool late = acur == 0u || (int)(L + SCH_VMIN - acur) > 0;
      if (late) { if (b == 0) FC(3); if (acur) D8 += 8 * (hist ? SCH_UP : 3 * SCH_UP); }
      else if (aprev) { if (hist < 2u) D8 = 8 * imin(imax((int)(L - aprev), 1), 4000); hist = umin(hist + 1u, 2u); }
      const unsigned base = acur == 0u ? L + SCH_FIRST : (late ? L + SCH_V + ((int)(L + SCH_VMIN2 - acur) <= 0 ? SCH_PEN2 : SCH_PEN) : acur);
      anext = (base + (unsigned)((D8 + 4) >> 3) + SCH_V + (s == 1 ? EARLY_V1 : (s < EARLY_N ? EARLY_V : 0))) | 1u;
      if (s == 0) anext = (L + ST_X) | 1u;
      if ((int)(now - L) > 50000) anext = 0u;
      if (late) acur = 0u;
      Dn = D8;
    }
    if (__builtin_expect(b == 0, 0))
      __builtin_amdgcn_raw_buffer_store_b128((u32x4){anext, acur, (unsigned)Dn, hist}, rs, lane < 2 ? lane * 8192 : 0x7ffffff0, (unsigned)OFF_ACC + (unsigned)(s % SR) * (unsigned)ACC_STEP + ACC_TS + 128u, 16);
    if (XO_T && anext) anext += (b & 1) * XO_T;
    LOGV(0, anext); LOGV(1, acur == 0u); LOGV(2, (Dn + 4) >> 3); LOGV(3, d); LOGV(4, acur - aprev - d); LOGV(5, aprev);
    return anext;
  }
};

struct TokTask {
  u32x4 y[2]; u32x2 ym; u32x4 res;
  unsigned l0, l1, lm, s0, s1;
  static __device__ __forceinline__ bool ok(int sl) { return sl >= 0 && sl < NSEG; }
  __device__ __forceinline__ void init(int h, int j, int lane) {
    const int c = lane & 15, g = lane >> 4;
    l0 = ts_tok(h, g) + (unsigned)(j * 256 + c * 16); l1 = l0 + 16384u;
    lm = ts_mem(h, g + 4 * (c >> 3)) + (unsigned)(j * 64 + (c & 7) * 8);
    res = (u32x4){0u, 0u, 0u, 0u};
    const int qi = 4 * j + (c & 3);
    s0 = g < 2 ? pt_tok(h) + (unsigned)((qi >> 4) * 1024 + ((c >> 2) * 16 + (qi & 15)) * 16 + g * 8) : 0x7ffffff0u;
    s1 = lane < 8 ? pt_mem(h) + (unsigned)((qi >> 4) * 256 + ((c >> 2) * 16 + (qi & 15)) * 8) : 0x7ffffff0u;
    PIN_V(l0); PIN_V(l1); PIN_V(lm); PIN_V(s0); PIN_V(s1);
  }
  __device__ __forceinline__ void load(const __amdgpu_buffer_rsrc_t rs, unsigned o) {
    y[0] = SC1_LOADS(l0, o); y[1] = SC1_LOADS(l1, o);
    ym = __builtin_amdgcn_raw_buffer_load_b64(rs, lm, o, 16);
  }
  __device__ __forceinline__ bool ready() { return wave_all(umin(umin(min4(y[0]), min4(y[1])), umin(ym[0], ym[1])) != 0u); }
  __device__ __forceinline__ void run(int j, int lane, const bf16x8 SelLo) {
    const int c = lane & 15, g = lane >> 4;
    const f32x4 Z4 = {0.f, 0.f, 0.f, 0.f};
    const f32x4 d1 = mfma32(SelLo, as8(y[1]), mfma32(SelLo, as8(y[0]), Z4));
    const f32x4 d0 = mfma32(SelLo, mk8(ym[0], ym[1], 0u, 0u), Z4);
    const int qi = 4 * j + (c & 3), k0 = 16 * g + 4 * (c >> 2);
    float t[4], mv[4];
    float mx = -INFINITY;
#pragma unroll
    for (int r = 0; r < 4; ++r) {
      t[r] = (g < 2 && k0 + r <= qi) ? d1[r] : -INFINITY;
      const float mt = d0[r] + dppf<DPP_ROR8>(d0[r]);
      mv[r] = lane < 8 ? mt : -INFINITY;
      mx = fmx(mx, fmx(t[r], mv[r]));
    }
    mx = fmx(mx, dppf<DPP_ROR8>(mx)); mx = fmx(mx, dppf<DPP_ROR4>(mx));
    mx = x16mx(mx);
    float sum = 0.f;
#pragma unroll
    for (int r = 0; r < 4; ++r) {
      t[r] = __builtin_amdgcn_exp2f(t[r] - mx); mv[r] = __builtin_amdgcn_exp2f(mv[r] - mx);
      sum += t[r] + mv[r];
    }
    sum += dppf<DPP_ROR8>(sum); sum += dppf<DPP_ROR4>(sum);
    sum = x16sum(sum);
    const float rs_ = __builtin_amdgcn_rcpf(sum);
    res = (u32x4){pack2n(t[0] * rs_, t[1] * rs_), pack2n(t[2] * rs_, t[3] * rs_), pack2n(mv[0] * rs_, mv[1] * rs_), pack2n(mv[2] * rs_, mv[3] * rs_)};
    PIN_V(res);
  }
  __device__ __forceinline__ void store(const __amdgpu_buffer_rsrc_t rs, unsigned o, unsigned no = 0u) {
    SC1_STORE8S(((u32x2){res[0], res[1]}), s0 | no, o);
    SC1_STORE8S(((u32x2){res[2], res[3]}), s1 | no, o);
  }
};

constexpr int L_DRN = L_VOX + (NSEG % NV) * 5120;
__device__ __forceinline__ void drain_pre(char* lds, int b, int lane) {
  const int gp = (b & 7) * 2 + (b >> 7), m = (b >> 3) & 15;
  TokTask tk; PvTask2 pv; OutTask ot;
  tk.init(2 * gp + (m >> 3), m & 7, lane); pv.init(gp, lane); ot.init(b, lane);
  u32x4* p = (u32x4*)(lds + L_DRN) + lane;
  p[0] = (u32x4){tk.l0, tk.lm, tk.s0, tk.s1}; p[64] = (u32x4){pv.lo, pv.lm, pv.vr, pv.ma}; p[128] = (u32x4){pv.mz, pv.so, ot.t5f, ot.t5o};
  p[192] = __builtin_bit_cast(u32x4, ot.sel);
}
__device__ __forceinline__ void qkv_pre(unsigned (&vo)[2][2], int b, int w, int lane) {
  const int cb = qkv_col(b), c = lane & 15, g = lane >> 4, t0 = w & 1, col0 = (cb % 5) * 128 + w * 32;
  const bool SW = cb < 10;
#pragma unroll
  for (int i = 0; i < 2; ++i)
#pragma unroll
    for (int tt = 0; tt < 2; ++tt) {
      const int tq = tt ^ t0, tk = tq * 16 + c, cc = col0 + i * 16 + (SW ? 4 * g : c), hd = cc / BD, e = cc - hd * BD;
      const int kv = (int)(OFF_KV - OFF_QB) + hd * 64 * KVT;
      vo[i][tt] = (unsigned)(!SW ? kv + 1280 + tq * 672 + ((g >> 1) * 21 + e) * 16 + (g & 1) * 8
                                 : (cb < 5 ? (hd * S_ + tk) * 2 * QST + e * 2 : kv + (e < 16 ? (tk + 32 * (e >> 3)) * 16 + (e & 7) * 2 : 1024 + tk * 8)));
      PIN_V(vo[i][tt]);
    }
}

template <int w>
__device__ __forceinline__ void token_waves(const RecArgs& a, char* lds, const __amdgpu_buffer_rsrc_t rs, unsigned (&vo)[2][2]) {
  const int lane0 = __builtin_amdgcn_workitem_id_x() & 63, b = __builtin_amdgcn_workgroup_id_x(), wid = w + 4; (void)wid;
  const int gp = (b & 7) * 2 + (b >> 7), m = (b >> 3) & 15, h0 = 2 * gp, h = h0 + (m >> 3), j = m & 7;
  float* const misc = (float*)(lds + L_MISC);
  const f32x4 Z4 = {0.f, 0.f, 0.f, 0.f};
  const unsigned lds0 = (unsigned)(uintptr_t)(LAS char*)lds;
  unsigned* const cnt = (unsigned*)(misc + 6);
  const float* const xnrm = (const float*)(a.ws + OFF_XNRM);
  bf16x16 W[2][10];
  auto btile = [&](int i) { return w == 1 ? 0 : (w == 2 ? 1 + i : 3 + i); };
  if (w == 1) code_touch(b, lane0, lds0);
  Place pl;
  unsigned phi[2][2], ph[2][2][2];
#pragma unroll
  for (int k = 0; k < 8; ++k) {
    const int bt = btile(k >> 2), hw = h0 + (bt == 2 ? k & 1 : bt / 3), T = bt == 2 ? 32 + (m >> 1) : (bt % 3) * 16 + m;
    ph[k >> 2][(k >> 1) & 1][k & 1] = ((const __attribute__((address_space(4))) unsigned*)(a.ws + OFF_PHI))[(hw >> 2) | (hw & 3) << 3 | (k & 2) << 4 | (T / 10) << 6];
  }
  __builtin_amdgcn_sched_barrier(0);
  if (w == 3) pl.load(a.ws, lane0);
  if (a.layer) {
    if (w != 0 && FR_HOLD) wait_until(now32() + FR_HOLD);
    if (w != 0) img_fetch<20>(a, lds0, h, j, w - 1, lane0);
  } else {
    if (w == 0) img_fetch<12>(a, lds0, h, j, 4, lane0);
#pragma unroll
    for (int i = 0; i < 10; ++i) x_fetch(x_src(a, 0, b), lds0, 0, 10 * w + i, lane0);
  }
  const __amdgpu_buffer_rsrc_t rwb = __builtin_amdgcn_make_buffer_rsrc(a.ws + OFF_WB, 0, (int)WB_BYTES, 0x00020000);
#pragma unroll
  for (int k = 0; k < 4; ++k) phi[k >> 1][k & 1] = (((lane0 & 8) ? ph[k >> 1][k & 1][1] : ph[k >> 1][k & 1][0]) & 8192u) | (unsigned)(lane0 * 16);
#pragma unroll
  for (int i = 0; i < 2; ++i)
#pragma unroll
    for (int p = 0; p < 10; ++p)
      if (w >= 2 || (w == 1 && i == 1)) {
        const unsigned wo = wb_off((b * 5 + btile(i)) * 20 + 2 * p), wv = phi[i][p / 5] ^ (wo & 8192u);
        if (FR_PACE && a.layer) asm volatile("s_waitcnt vmcnt(%0)" :: "n"(FR_PACE) : "memory");
        W[i][p] = cat16(as8(__builtin_amdgcn_raw_buffer_load_b128(rwb, wv, wo & ~8192u, 0)), as8(__builtin_amdgcn_raw_buffer_load_b128(rwb, wv + 1024, wo & ~8192u, 0)));
      }
  f32x4 bn = Z4;
  if (w == 0) {
    bn = *(const f32x4*)(a.ws + OFF_BNRM + lane0 * 16);
    if (!a.layer) asm volatile("s_waitcnt vmcnt(0)" ::: "memory");
  } else asm volatile("s_waitcnt vmcnt(%0)" :: "n"(w == 1 ? 20 : 40) : "memory");
  unsigned ck = 0u;
  if (w == 3) { ck = pl.get(b, lane0); *(unsigned*)(misc + 11) = ck; }
  BAR();
#pragma unroll
  for (int p = 0; p < 10; ++p) {
    if (w < 2) W[0][p] = cat16(wqk_frag(lds, 2 * w, 2 * p, lane0), wqk_frag(lds, 2 * w, 2 * p + 1, lane0));
    if (w == 0) W[1][p] = cat16(wqk_frag(lds, 1, 2 * p, lane0), wqk_frag(lds, 1, 2 * p + 1, lane0));
  }
  float cb2 = 0.f;
  if (w == 0) {
    float n = __builtin_amdgcn_sqrtf(bn[0] + bn[1]) + __builtin_amdgcn_sqrtf(bn[2] + bn[3]);
    n += dppf<DPP_XOR1>(n); n += dppf<DPP_XOR2>(n); n += dppf<DPP_ROR4>(n); n += dppf<DPP_ROR8>(n);
    n = x32mx(x16mx(n));
    cb2 = u2f(__builtin_amdgcn_readfirstlane(f2u(1.15f * n * n)));
  }
  const float cm2 = u2f((f2u(cb2) & 0x7fffffu) | 0x40000000u);
  const int ek = imax((int)((f2u(cb2) >> 23) & 0xffu), 1) - 252;
  if (w >= 2) {
#pragma unroll
    for (int i = 0; i < 4; ++i) *(unsigned*)(lds + L_KX + (w - 2) * 4096 + i * 1024 + lane0 * 16 + 12) = 0u;
  }
  OutTask ot;
  const int zl = lane0 - 16;
  const bool zon = w == 3 && zl >= 0 && (zl < 40 || b < ACC_NB + 17);
  unsigned zo = zon ? zero_base(b, h, j, zl) + (zl < 32 ? sct_slot(ck, 0u) : (zl < 40 ? scm_slot(ck, 0u) : 0u)) : 0x7ffffff0u;
  unsigned zm = zon ? (zl < 32 ? SCT_STEP + 1u : (zl < 40 ? SCM_STEP : (unsigned)ACC_STEP)) : 0u;
  const unsigned zx = ((ck ^ (ck >> 4)) & 7u) * 4096u;
  if (w == 3) { PIN_V(zo); PIN_V(zm); }
  const __amdgpu_buffer_rsrc_t rx = __builtin_amdgcn_make_buffer_rsrc(a.ws + OFF_XNRM, 0, (int)(OFF_QB - OFF_XNRM), 0x00020000);
  u32x4 v0, v1;
  f32x4 d5;
  unsigned oo, za, xa, ya, n4, so_op, so_y, so_x;
  float xn4 = 0.f;
  auto w1_part = [&](int k, int s, int lane) {
    if (w < 2) return;
    if (w == 2) {
      if (k >= 1 && k <= 4) d5 = mfma32(ot.sel, as8(ot.y[k < 1 || k > 4 ? 0 : k - 1]), k == 1 ? Z4 : d5);
      if (k == 6 || k == 7) {
        const unsigned r = pack2(d5[2 * (k & 1)], d5[2 * (k & 1) + 1]), x = ot.xo[k & 1];
        ot.out[k & 1] = pack2(u2f(x << 16) + u2f(r << 16), u2f(x & 0xffff0000u) + u2f(r & 0xffff0000u));
        PIN_V(ot.out);
      }
      if (k == 8) xn4 = fmx(xn4, dppf<DPP_XOR1>(xn4));
      if (k == 9) xn4 = fmx(xn4, dppf<DPP_XOR2>(xn4));
      if (k == 10) xn4 = fmx(xn4, dppf<DPP_ROR4>(xn4));
      if (k == 11) xn4 = fmx(xn4, dppf<DPP_ROR8>(xn4));
      if (k >= 8 && k <= 11) PIN_V(xn4);
      if (k == 12) misc[s & 3] = x16mx(xn4);
      if (k == 5) { so_y = op_slot(imax(s - 1 - LAG5, 0)); so_x = (unsigned)imax(s - 1 - LAG5, 0) * 40960u; PIN_S(so_y); PIN_S(so_x); }
      if (k == 14) {
        ya = ot.t5o | (s <= LAG5 ? 0x7ff00000u : 0u);
        xa = (ot.t5f * 2u | (unsigned)(lane & 32) << 25) | (OutTask::ok(s - 2 - LAG5, b) ? 0u : 0x7ff00000u);
        PIN_V(ya); PIN_V(xa);
      }
    } else if (k == 8) {
      const unsigned zs = (unsigned)(s + 2) % SR;
      za = (zo + (s < 0 ? 1024u : zs) * (zm & ~1u)) ^ ((zm & 1u) * (zs & 4u ? zx : 0u));
      PIN_V(za);
    }
    if (k == 13) {
      n4 = __hip_atomic_load((unsigned*)(misc + 28), __ATOMIC_RELAXED, __HIP_MEMORY_SCOPE_WORKGROUP);
      so_op = s < 2 + LAG4 ? 0x7ff00000u : op_slot(s - 2 - LAG4) + (unsigned)((m * 32 + (w & 1) * 16 + gp) * 1280);
      PIN_S(so_op);
    }
    if (k == 15 && __builtin_expect((int)__builtin_amdgcn_readfirstlane(n4) < s, 0)) lds_spin<L_MISC + 112, 1>(lds0, (unsigned)s);
    if (k == 16) {
      asm volatile("" ::: "memory");
      const char* sp = lds + L_STG + 2048 + (w & 1) * 1280;
      v0 = *(const u32x4*)(sp + lane * 16); v1 = *(const u32x4*)(sp + 1024 + (lane & 15) * 16);
      oo = so_op + (unsigned)(lane * 16);
      za = lane < 16 ? oo + 1024 : (w == 3 ? za : 0x7ffffff0u);
      PIN_V(oo); PIN_V(za);
    }
    if (k == 19 && w == 3) {
#pragma unroll
      for (int i = 0; i < 4; ++i) { v1[i] = lane < 16 ? v1[i] : 0u; PIN_V(v1[i]); }
    }
    __builtin_amdgcn_sched_barrier(0);
  };
  auto w1_fire = [&](int s, int lane, auto last) {
    if (w < 2) return;
    lds_spin<L_MISC + 92, W1_SL>(lds0, 4u * (unsigned)imax(s, 0));
    SC1_STORE(v0, oo);
    SC1_STORE(v1, za);
    __builtin_amdgcn_sched_barrier(0);
    if (w == 2) {
      __builtin_amdgcn_raw_buffer_store_b64(ot.out, rx, xa, so_x + (unsigned)(OFF_XA - OFF_XNRM - 40960), 16);
      if (last == 0) {
#pragma unroll
        for (int k = 0; k < 4; ++k) ot.y[k] = SC1_LOADS(ya, so_y + k * 5120);
        ot.xo = __builtin_amdgcn_raw_buffer_load_b64(rx, ot.t5f * 2u, so_x + (unsigned)(OFF_XB - OFF_XNRM), 16);
        xn4 = u2f(__builtin_amdgcn_raw_buffer_load_b32(rx, (lane & 31) * 16, (s + 1) * 512, 16));
      }
    } else {
      const unsigned xo_ = (unsigned)((s - 8) * 1024 + lane * 16);
      if (__builtin_expect((unsigned)(s - 8) < 20u, 0)) SC1_STORE(((u32x4){0u, 0u, 0u, 0u}), xo_ < X_BYTES / 256 ? (unsigned)(OFF_X + b * (X_BYTES / 256)) + xo_ : 0x7ffffff0u);
    }
    __builtin_amdgcn_sched_barrier(0);
  };
  STAMP_DECL(); ARRIVE_DECL();
  auto mma_seg = [&](int seg, int sv, int lane, auto pause) {
    const int c = lane & 15, g = lane >> 4;
    const char* xp = lds + L_XN + (seg & 1) * 40960 + lane * 16;
    f32x4 acc[2][2] = {{Z4, Z4}, {Z4, Z4}};
    bf16x8 x[4][2];
#pragma unroll
    for (int p = 0; p < 3; ++p) { x[p][0] = *(const bf16x8*)(xp + p * 1024); x[p][1] = *(const bf16x8*)(xp + (20 + p) * 1024); }
    unsigned eo[2];
#pragma unroll
    for (int ks = 0; ks < 20; ++ks) {
      if (ks == (w == 0 ? KP0 : (w == 1 ? KP1 : KP2))) { pause(); __builtin_amdgcn_sched_barrier(0); }
      if (ks == 17) {
#pragma unroll
        for (int i = 0; i < 2; ++i) {
          eo[i] = (unsigned)(w == 0 ? (i ? L_KX : L_QX) + (seg % NKQ) * 2048 + lane * 16
                                    : (w == 1 && i == 0 ? ((g >> 1) ? L_KX : L_QX) + (seg % NKQ) * 2048 + (2 * (g & 1) * 16 + c) * 16 + 8 : L_VOX + sv * 5120 + btile(i) * 1024 + lane * 16));
          if (i == 0 || w == 1) PIN_V(eo[i]);
        }
        if (w == 0) eo[1] = eo[0] + (L_KX - L_QX);
        if (w >= 2) eo[1] = eo[0] + 1024;
      }
      __builtin_amdgcn_sched_barrier(0);
#pragma unroll
      for (int q = 0; q < 4; ++q) {
        const int tt = 1 - (q >> 1), i = q & 1;
        const bool isqk = w == 0 || (w == 1 && i == 0);
        const bf16x8 wf = half8(W[i][ks >> 1], ks & 1);
        acc[i][tt] = isqk ? mfma32(wf, x[ks & 3][tt], acc[i][tt]) : mfma32(x[ks & 3][tt], wf, acc[i][tt]);
        __builtin_amdgcn_sched_barrier(0);
        if (q < 2 && ks + 3 < 20) x[(ks + 3) & 3][q] = *(const bf16x8*)(xp + (q * 20 + ks + 3) * 1024);
        if (q == 2) w1_part(ks, seg - 1, lane);
        __builtin_amdgcn_sched_barrier(0);
      }
    }
#pragma unroll
    for (int i = 0; i < 2; ++i) {
      if (w + i == 1) { const float ks_ = (w == 0 || (g >> 1)) ? KEXP : 1.0f; acc[i][0] *= ks_; acc[i][1] *= ks_; }
      const u32x4 v = {pack2(acc[i][0][0], acc[i][0][1]), pack2(acc[i][0][2], acc[i][0][3]), pack2(acc[i][1][0], acc[i][1][1]), pack2(acc[i][1][2], acc[i][1][3])};
      char* p = lds + eo[i];
      if (w == 0) { *(u32x2*)(p) = (u32x2){v[0], v[1]}; *(u32x2*)(p + 1024) = (u32x2){v[2], v[3]}; }
      else if (w == 1 && i == 0) { *(unsigned*)(p) = v[0]; *(unsigned*)(p + 256) = v[1]; *(unsigned*)(p + 1024) = v[2]; *(unsigned*)(p + 1280) = v[3]; }
      else *(u32x4*)p = v;
    }
  };
  unsigned vp, mp, ip;
  int sidx, ei = 127;
  float inv;
  f32x4 acc[2];
  auto vo_pre = [&](int s, int sv, int lane) {
    vp = (unsigned)(w == 0 ? L_MISC + (s & 3) * 4 : L_VOM + sv * 1280 + (btile(0) * 32 + (w == 1 ? lane & 31 : lane)) * 8);
    mp = (unsigned)(L_BM + lane * 16);
    PIN_V(vp); PIN_V(mp);
    if (w == 0) { ip = (unsigned)(L_MISC + 16 + ((s + 1) & 1) * 4); PIN_V(ip); }
    else {
      inv = misc[4 + (s & 1)];
      sidx = (lane & 8) ? 0xEEEE : 0x4444;
      acc[0] = acc[1] = Z4;
      PIN_V(sidx); PIN_V(acc[1]);
      if (w >= 2) { PIN_V(acc[0]); PIN_V(inv); }
    }
    __builtin_amdgcn_sched_barrier(0);
  };
  auto vo_mem = [&](int s, auto last) {
    __builtin_amdgcn_s_setprio(2);
    asm volatile(".rept %0\n\ts_nop 3\n\t.endr" :: "n"(w == 0 ? VO_A0 : (w == 1 ? VO_A1 : VO_A2)) : "memory");
    float xmax = 0.f;
    if (w == 0) xmax = *(const float*)(lds + vp);
    constexpr int NX = w == 2 ? VO_N2 : (w == 3 ? VO_N3 : 10);
    bf16x8 x[10];
#pragma unroll
    for (int p = 0; p < NX; ++p) x[p] = *(const bf16x8*)(lds + mp + p * 1024);
    __builtin_amdgcn_sched_barrier(0);
    if (w < 2) asm volatile(".rept %0\n\ts_nop 3\n\t.endr" :: "n"(w == 0 ? VO_B0 : VO_B1) : "memory");
    if (VO_W >= 0) asm volatile("s_waitcnt lgkmcnt(%0)" :: "n"(VO_W < 0 ? 0 : VO_W) : "memory");
    STAMP(5);
    __builtin_amdgcn_sched_barrier(0);
    int ex = 0;
#pragma unroll
    for (int p = 0; p < 10; ++p) {
      if (w == 0) acc[0] = mfma32(x[p], x[p], p ? acc[0] : Z4);
      if (w == 0 && p == 9) { ex = imax((int)((__builtin_amdgcn_readfirstlane(f2u(__builtin_amdgcn_sqrtf(cb2 * xmax))) >> 23) & 0xffu) + 1, 32); PIN_S(ex); }
#pragma unroll
      for (int i = (w == 1); i < 2 && w != 0; ++i) acc[i] = smfma64(x[p], W[i][p], acc[i], sidx);
      __builtin_amdgcn_sched_barrier(0);
      if (p + NX < 10) x[p + NX] = *(const bf16x8*)(lds + mp + (p + NX) * 1024);
      __builtin_amdgcn_sched_barrier(0);
    }
    STAMP(7);
    if (w != 0) {
      float v[4];
#pragma unroll
      for (int r = 0; r < 4; ++r) {
        const auto sw = __builtin_amdgcn_permlane32_swap(f2u(acc[w == 1][r]), f2u(acc[1][r]), false, false);
        v[r] = (u2f(sw[0]) + u2f(sw[1])) * inv;
      }
      *(u32x2*)(lds + vp) = (u32x2){pack2(v[0], v[1]), pack2(v[2], v[3])};
    } else {
      const int t = (int)f2u(fmx(fmx(acc[0][0], acc[0][1]), fmx(acc[0][2], acc[0][3])) * cm2);
      const int et = wave_imax(t) >> 23, em = ((et + ek) >> 1) + ei;
      int e = imax(et == 255 ? 256 : em, ex);
      PIN_S(e);
      e = imin(e, 256);
      ei = e - 31;
      *(unsigned*)(lds + ip) = (unsigned)ei << 23;
      const u32x3 sr = {(unsigned)(285 - e) << 23, (unsigned)(e - 87) < 79u ? REC_RNG << 23 : 0u, (unsigned)(s + 1)};
      asm volatile("ds_write_b96 %0, %1 offset:%2" :: "v"(lds0), "v"(sr), "n"(L_MISC + 32) : "memory");
    }
    if (last == 0 && w >= 2) asm volatile("s_waitcnt vmcnt(%0)" :: "n"(w == SCH_W ? 2 : 0) : "memory");
    if (w != 0) lds_inc<L_MISC + 28>(lds0);
    STAMP(8);
    if (last) __builtin_amdgcn_s_setprio(0);
    if (last && w == 2) { VM_DRAIN(); lds_inc<L_MISC + 28>(lds0); }
  };
  unsigned* const cntq = (unsigned*)(misc + 12);
  unsigned* const cntt = (unsigned*)(misc + 26 + (w & 1));
  unsigned* const cntp = (unsigned*)(misc + 23);
  unsigned* const cntx2 = (unsigned*)(misc + 28);
  auto wpf = [&](int s, int lane) {
    constexpr int PER = 81920 / QR / WPF_N;
    static_assert(PER % WPF_S == 0 && PER / WPF_S <= 64, "prefetch");
    touch(a.ws + OFF_WQKVBT + (size_t)imin(qkv_col(b), 14) * 163840 + (qkv_row(b) % (2 * QR)) * (81920 / QR), (s - WPF0) * PER + imin(lane, PER / WPF_S - 1) * WPF_S, lds0);
  };
  Sched sch;
  auto late = [&](int s, int lane) {
    if (__builtin_expect(s < 0, 0)) return;
    if (w == SCH_W && SCH_LATE && __builtin_expect(s >= 0, 1)) *(u32x2*)(misc + 24) = (u32x2){sch.run(a, rs, s, b, lane), (unsigned)(s + 1)};
    if ((w == 2 && F6) || (w == 0 && F4) || (w == 1 && F5) || (w == 3 && F7)) { lds_spin<L_MISC + 48, 1>(lds0, 2u * (unsigned)imax(s + 1, 0)); if (FB_SL && w != 2) __builtin_amdgcn_s_sleep(FB_SL); }
    if (w >= 2) { x_fetch_n<w == 2 ? F6 : F7>(x_src(a, s + 3, b), lds0, s + 3, F4 + F5 + 2 * F67 + (w == 3 ? F6 : 0), lane * 16); if (HOLD67 == 1 || (HOLD67 == 2 && w == 3)) lds_spin<L_MISC + 104 + 4 * (w & 1), 1>(lds0, (unsigned)(s + 1)); if (w == 2 ? H6_SL : H7_SL) __builtin_amdgcn_s_sleep(w == 2 ? H6_SL : H7_SL); return; }
    unsigned sd = 0u;
    if ((w == 0 && XPF == 3) || XPF == 4 || (w == 1 && XPF == 5)) stouch<10>(a.ws + OFF_XN + (size_t)imin(s + 4, NSEG - 1) * (s < 1 ? 0 : 40960) + (b >> 3) * 1280 + w * 64, sd);
    x_fetch_n<w == 0 ? F4 : F5>(x_src(a, s + 3, b), lds0, s + 3, w == 0 ? 0 : F4, lane * 16);
    if (WPF_W == 0 && w == 0 && __builtin_expect(s >= WPF0 && s < WPF0 + WPF_N, 0)) wpf(s, lane);
    if (w == 1 && __builtin_expect(s == CPF_S, 0)) code_touch(b, lane, lds0);
    lds_spin<L_MISC + 104 + 4 * (w & 1), PS_SL>(lds0, (unsigned)imax(s + 1, 0));
    asm volatile("" :: "s"(sd));
  };
  if (w == 2) { ot.init(b, lane0); ot.out = (u32x2){0u, 0u}; ot.xo = ot.out; ot.y[0] = ot.y[1] = ot.y[2] = ot.y[3] = (u32x4){0u, 0u, 0u, 0u}; }
  VM_DRAIN();
  lds_arrive(cnt, lane0);
  lds_wait(cnt, 8u);
  sch.tsv = sch.rec = (u32x4){0u, 0u, 0u, 0u};
  sch.side = __builtin_amdgcn_readfirstlane(*(const unsigned*)(misc + 11)) & 8192u;
  sch.o = 0u;
  int sv = NV - 1;
  CODE_PAD((PADT >> (4 * w)) & 15);
#pragma unroll 1
  for (int s = -1; s < NSEG - 1; ++s) {
    const int sv1 = sv == NV - 1 ? 0 : sv + 1;
    FRESH_LANE();
    STAMP(0);
    mma_seg(s + 1, sv1, lane, [&]() { STAMP(1); late(s - 1, lane); if (w == SCH_W) sch.pre(s); STAMP(9); });
    STAMP(2);
    __builtin_amdgcn_sched_barrier(0);
    VM_DRAIN();
    STAMP(10);
    w1_fire(s, lane, std::integral_constant<int, 0>{});
    if (w >= 2) vo_pre(s, sv, lane);
    STAMP(3);
    BAR();
    STAMP(4);
    if (__builtin_expect(s >= 0, 1)) {
      if (w == SCH_W) { sch.load(rs, lane0); __builtin_amdgcn_sched_barrier(0); }
      if (w < 2) vo_pre(s, sv, lane);
      vo_mem(s, std::integral_constant<int, 0>{});
      STAMP(6);
      if (w == SCH_W && !SCH_LATE) {
        __builtin_amdgcn_s_setprio(SCH_P);
        *(u32x2*)(misc + 24) = (u32x2){sch.run(a, rs, s, b, lane0), (unsigned)(s + 1)};
      }
      STAMP(11);
    }
    __builtin_amdgcn_s_setprio(MMA_P);
    if (w == 2) VM_DRAIN();
    __builtin_amdgcn_sched_barrier(0);
    sv = sv1;
  }
  __builtin_amdgcn_s_setprio(0);
  STAMP_OUT();
  late(NSEG - 2, lane0);
  { FRESH_LANE(); if (w == 0) drain_pre(lds, b, lane); qkv_pre(vo, b, w, lane); }
#pragma unroll
  for (int k = 0; k < 20; ++k) w1_part(k, NSEG - 1, lane0);
  w1_fire(NSEG - 1, lane0, std::integral_constant<int, 1>{});
  if (w != 0) vo_pre(NSEG - 1, (NSEG - 1) % NV, lane0);
  BAR();
  if (w != 0) vo_mem(NSEG - 1, std::integral_constant<int, 1>{});
}

__device__ __forceinline__ void qkv_tail(const RecArgs& a, char* lds, const int w, const unsigned (&vo)[2][2]) {
  const int lane0 = __builtin_amdgcn_workitem_id_x() & 63, b = __builtin_amdgcn_workgroup_id_x();
  const int r = qkv_row(b), cb = qkv_col(b);
  if (cb >= 15) return;
  const unsigned lds0 = (unsigned)(uintptr_t)(LAS char*)lds;
  unsigned* const stage = (unsigned*)(lds + L_MISC) + 17;
  unsigned* const cnty = stage + 1;
  const f32x4 Z4 = {0.f, 0.f, 0.f, 0.f};
  auto fetch = [&](int i) {
    x_fetch_n<10>(x_src(a, NSEG + i, b), lds0, i, 10 * w, lane0 * 16);
  };
  unsigned want = 0;
  auto sync = [&]() { VM_DRAIN(); want += 4u; lds_inc<L_MISC + 72>(lds0); while (__hip_atomic_load(cnty, __ATOMIC_RELAXED, __HIP_MEMORY_SCOPE_WORKGROUP) < want) { } asm volatile("" ::: "memory"); };
  LOGQ(0);
  bf16x8 W[2][20];
  const __amdgpu_buffer_rsrc_t rw = __builtin_amdgcn_make_buffer_rsrc(a.ws + OFF_WQKVBT + (size_t)(cb * 8 + 2 * w) * 20480, 0, 40960, 0x00020000);
  auto w_get = [&](int ks) {
#pragma unroll
    for (int i = 0; i < 2; ++i) W[i][ks] = as8(__builtin_amdgcn_raw_buffer_load_b128(rw, lane0 * 16 + (ks & 3) * 1024, (i * 20 + (ks & ~3)) * 1024, 0));
    __builtin_amdgcn_sched_barrier(0);
  };
#pragma unroll
  for (int ks = 0; ks < QW; ++ks) w_get(ks);
  bf16x8 ones = mk8(0x3f803f80u, 0x3f803f80u, 0x3f803f80u, 0x3f803f80u);
  PIN_V(ones);
  const __amdgpu_buffer_rsrc_t ro = __builtin_amdgcn_make_buffer_rsrc(a.ws + OFF_QB, 0, (int)(OFF_OB - OFF_QB), 0x00020000);
  const int t0 = w & 1;
  auto body = [&](auto SW, auto SQ) {
    const float qs = cb < 5 ? 0.22360679774997896f * 1.4426950408889634f : 1.0f;
    f32x4 cs[2] = {Z4, Z4};
    LOGQ(1);
    f32x4 acc[2][2], st;
    auto mma = [&](int it, auto FIRST) {
      const char* xp0 = lds + L_XN + (it & 1) * 40960 + t0 * 20480 + lane0 * 16;
      const char* xp1 = lds + L_XN + (it & 1) * 40960 + (1 - t0) * 20480 + lane0 * 16;
      acc[0][0] = acc[0][1] = acc[1][0] = acc[1][1] = st = Z4;
      bf16x8 x[4][2];
#pragma unroll
      for (int p = 0; p < 3; ++p) { x[p][0] = *(const bf16x8*)(xp0 + p * 1024); x[p][1] = *(const bf16x8*)(xp1 + p * 1024); }
      __builtin_amdgcn_sched_barrier(0);
#pragma unroll
      for (int ks = 0; ks < 20; ++ks) {
        if (FIRST && ks + QW < 20) w_get(ks + QW);
#pragma unroll
        for (int q = 0; q < 4; ++q) {
          const int tt = 1 - (q >> 1), i = 1 - (q & 1);
          acc[i][tt] = SW ? mfma32(W[i][ks], x[ks & 3][tt], acc[i][tt]) : mfma32(x[ks & 3][tt], W[i][ks], acc[i][tt]);
          __builtin_amdgcn_sched_barrier(0);
          if (q < 2 && ks + 3 < 20) x[(ks + 3) & 3][q] = *(const bf16x8*)((q ? xp1 : xp0) + (ks + 3) * 1024);
          __builtin_amdgcn_sched_barrier(0);
        }
        st = mfma32(SQ ? x[ks & 3][0] : ones, x[ks & 3][0], st);
        if (FIRST) {
#pragma unroll
          for (int i = 0; i < 2; ++i) cs[i] = SW ? mfma32(W[i][ks], ones, cs[i]) : mfma32(ones, W[i][ks], cs[i]);
        }
        __builtin_amdgcn_sched_barrier(0);
      }
    };
    mma(0, std::integral_constant<bool, true>{});
#pragma unroll 1
    for (int it = 0; it < 4; ++it) {
      if (it) mma(it, std::integral_constant<bool, false>{});
      LOGQ(2 + 3 * it);
      FRESH_LANE();
      float* const sp = (float*)(lds + L_ST) + (it & 1) * 64;
      if (SQ) { if ((c >> 2) == g) sp[32 + t0 * 16 + c] = (c & 3) == 0 ? st[0] : ((c & 3) == 1 ? st[1] : ((c & 3) == 2 ? st[2] : st[3])); }
      else if (g == 0) sp[t0 * 16 + c] = st[0];
      sync();
      LOGQ(3 + 3 * it);
      if (it == 0) fetch(2);
      const bool late = r + 48 >= 55;
      if (it == 1 && !late) fetch(3);
      const unsigned so = (unsigned)((r + 16 * it) * (cb < 5 ? 64 * QST : KVT));
      auto stat = [&](float sm, float sq, float& mean, float& rstd) {
        mean = sm * (1.0f / DM);
        rstd = __builtin_amdgcn_rsqf(__builtin_fmaxf(sq * (1.0f / DM) - mean * mean, 0.f) + 1e-5f);
      };
#pragma unroll
      for (int tt = 0; tt < 2; ++tt) {
        const int tq = tt ^ t0;
        float mean[4], rstd[4];
        if (SW) {
          stat(sp[tq * 16 + c], sp[32 + tq * 16 + c], mean[0], rstd[0]);
          rstd[0] *= qs;
        } else {
          const f32x4 sm = *(const f32x4*)(sp + tq * 16 + 4 * g), sq = *(const f32x4*)(sp + 32 + tq * 16 + 4 * g);
#pragma unroll
          for (int k = 0; k < 4; ++k) stat(sm[k], sq[k], mean[k], rstd[k]);
        }
#pragma unroll
        for (int i = 0; i < 2; ++i) {
          float v[4];
#pragma unroll
          for (int k = 0; k < 4; ++k) v[k] = (acc[i][tt][k] - mean[SW ? 0 : k] * cs[i][SW ? k : 0]) * rstd[SW ? 0 : k];
          __builtin_amdgcn_raw_buffer_store_b64((u32x2){pack2(v[0], v[1]), pack2(v[2], v[3])}, ro, vo[i][tt], so, Q_AUX);
        }
      }
      LOGQ(4 + 3 * it);
      if (it == 2 && late) lds_spin<L_MISC + 68, 0>(lds0, 5u);
    }
  };
  typedef std::integral_constant<bool, false> F_; typedef std::integral_constant<bool, true> T_;
  if (cb < 10) { if (w >> 1) body(T_{}, T_{}); else body(T_{}, F_{}); }
  else { if (w >> 1) body(F_{}, T_{}); else body(F_{}, F_{}); }
}

__device__ __forceinline__ void t2_mem(const bf16x8 fr, const bf16x8 q0, const bf16x8 q1, const __amdgpu_buffer_rsrc_t rs, unsigned o, int h, int j, float inv, int lane) {
  const f32x4 Z4 = {0.f, 0.f, 0.f, 0.f};
  const f32x4 d0 = mfma32(fr, q0, Z4) * inv, d1 = mfma32(fr, q1, Z4) * inv;
  const unsigned a0 = pack2n(d0[0], d0[1]), a1 = pack2n(d0[2], d0[3]), b0 = pack2n(d1[0], d1[1]), b1 = pack2n(d1[2], d1[3]);
  const auto r0 = __builtin_amdgcn_permlane32_swap(a0, b0, false, false), r1 = __builtin_amdgcn_permlane32_swap(a1, b1, false, false);
  SC1_STORE8S(((u32x2){r0[1], r1[1]}), ts_mem(h, j) + (lane >> 5) * 256 + (lane & 12) * 16 + (lane & 16) * 2 + (lane & 3) * 8, o);
}

template <int w>
__device__ __forceinline__ void rec_waves(const RecArgs& a, char* lds, __amdgpu_buffer_rsrc_t rs, unsigned& tlast) {
  PIN_S(rs);
  const int lane = __builtin_amdgcn_workitem_id_x() & 63, b = __builtin_amdgcn_workgroup_id_x(), c = lane & 15, g = lane >> 4, wid = w; (void)wid;
  const int gp = (b & 7) * 2 + (b >> 7), m = (b >> 3) & 15, h0 = 2 * gp, h = h0 + (m >> 3), j = m & 7;
  float* const misc = (float*)(lds + L_MISC);
  const f32x4 Z4 = {0.f, 0.f, 0.f, 0.f};
  LOGP(0);
  __builtin_amdgcn_s_setprio(3);
  const unsigned lds0 = (unsigned)(uintptr_t)(LAS char*)lds;
  if (!a.layer) img_fetch<12>(a, lds0, h, j, w, lane);
  constexpr int NC = w < 2 ? 2 : 3;
  const int far = ((b >> 2) & 1) ^ 1;
  auto chunk = [&](int f) { return f == 0 ? 4 * far + w : (w == 2 && f == 1 ? 8 + far : (w == 3 && f == 2 ? 9 - far : 4 - 4 * far + w)); };
  bf16x16 Wqk[3][NC];
  u32x4 bm0[NC][2];
#pragma unroll
  for (int f = 0; f < 2 * NC; ++f) bm0[f >> 1][f & 1] = *(const u32x4*)(a.mem0 + (size_t)(c & 7) * DM + 64 * chunk(f >> 1) + 16 * g + 8 * (f & 1));
  if (!a.layer) asm volatile("s_waitcnt vmcnt(0)" ::: "memory");
  BAR();
  LOGP(9);
  const unsigned ck = __builtin_amdgcn_readfirstlane(*(const unsigned*)(misc + 11));
  const unsigned flip = (ck ^ (unsigned)(b << 11)) & 8192u;
#pragma unroll
  for (int t = 0; t < 3; ++t)
#pragma unroll
    for (int f = 0; f < NC; ++f) Wqk[t][f] = cat16(wqk_frag(lds, t, 2 * chunk(f), lane), wqk_frag(lds, t, 2 * chunk(f) + 1, lane));
  if (a.layer) {
    if (w == 0) {
      for (unsigned spins = 0; spins < SPIN_CAP; ++spins) {
        if (__builtin_amdgcn_raw_buffer_load_b32(rs, (b & 7) * 256, (unsigned)OFF_RDY, 16) >= 16u) break;
        __builtin_amdgcn_s_sleep(4);
        asm volatile("" ::: "memory");
      }
      lds_inc<L_MISC + 84>(lds0);
    }
    lds_wait((unsigned*)(misc + 21), 2u);
#pragma unroll
    for (int f = 0; f < 2 * NC; ++f) PIN_V(bm0[f >> 1][f & 1]);
#pragma unroll
    for (int i = 0; i < 10; ++i) x_fetch(x_src(a, 0, b), lds0, 0, 10 * w + i, lane);
  }
  const bf16x8 SelLo = sel_frag(c, 0);
  bf16x8 ONES = mk8(0x3f803f80u, 0x3f803f80u, 0x3f803f80u, 0x3f803f80u);
  if (w < 2) PIN_V(ONES);
  bool dead = false;
  float inv = 0.f;
  unsigned* const cnt2 = (unsigned*)(misc + 7);
  unsigned l2off[2 * NC];
#pragma unroll
  for (int f = 0; f < 2 * NC; ++f) { l2off[f] = acc_off(16 * chunk(f >> 1) + 4 * g + 2 * (f & 1) + (c >> 3), c & 7) ^ flip; PIN_V(l2off[f]); }
  int sidx = (c >> 3) ? 0xEEEE : 0x4444;
  PIN_V(sidx);
  const unsigned p1off = (unsigned)((w == 0 ? (h & 2) * 1024 + j * 256 + (h & 1) * 64 + (g & 1) * 8 : (h & 3) * 32768 + j * 512 + (g >> 1) * 256 + (g & 1) * 64) + (c & 4) * 32 + (c & 3) * 16);
  unsigned p1v = (c < 8) && (w != 0 || g >= 2) ? p1off : 0x7ffffff0u, p1cur = 0x7ffffff0u;
  PIN_V(p1v); PIN_V(p1cur);
  const int hd = (c >> 2) & 1, mr = hd ? 3 - (c & 3) : (c & 3);
  const unsigned l1off = (unsigned)(((h0 + hd) & 3) * 32768 + g * 512 + (w & 1) * 128 + (c >> 3) * 64 + mr * 16);
  const unsigned l1offm = (unsigned)((h0 & 2) * 1024 + (g + 4 * (c >> 3)) * 256 + (w & 1) * 128 + hd * 64 + mr * 16);
  const int plane = (lane & ~6) | ((lane & 2) << 1) | ((lane & 4) >> 1);
  unsigned acco = c < 8 ? (acc_off(64 * far + m * 4 + g, 4 * (w & 1) + mr) + (unsigned)(hd * 8)) ^ flip : 0x7ffffff0u;
  unsigned acco2 = c >= 8 ? (acc_off(64 - 64 * far + m * 4 + g, 4 * (w & 1) + mr) + (unsigned)(hd * 8)) ^ flip
                          : (c < 4 ? (acc_off(128 + m * 2 + (g & 1), 4 * (w & 1) + mr) + (unsigned)((g >> 1) * 8)) ^ flip : 0x7ffffff0u);
  if (w < 2) { PIN_V(acco); PIN_V(acco2); }
  unsigned bmo[NC];
#pragma unroll
  for (int f = 0; f < NC; ++f) { bmo[f] = (unsigned)(chunk(f) * 1024 + lane * 16); PIN_V(bmo[f]); }
  bf16x8 bm[NC];
  auto bm_put = [&](int f) { *(bf16x8*)(lds + L_BM + bmo[f]) = bm[f]; __builtin_amdgcn_sched_barrier(0); };
  f32x4 acc[3] = {Z4, Z4, Z4};
  const int rt2 = ((c >> 3) * 8 + 4 * (g & 1)) * 28 + 16 + (c & 7);
  unsigned redo[3] = {(unsigned)(4 * g * 28 + c), (unsigned)(g < 2 ? (4 * g + 2) * 28 + c : rt2), (unsigned)(g < 2 ? (4 * g + 10) * 28 + c : rt2 + 56)};
#pragma unroll
  for (int k = 0; k < 3; ++k) { redo[k] = L_RED1 + w * 1792 + redo[k] * 4; PIN_V(redo[k]); }
  auto red_put = [&]() {
    auto hs = [&](float x, float y) { const auto sw = __builtin_amdgcn_permlane32_swap(f2u(x), f2u(y), false, false); return u2f(sw[0]) + u2f(sw[1]); };
#pragma unroll
    for (int k = 0; k < 3; ++k) {
#pragma unroll
      for (int i = 0; i < 2; ++i) *(float*)(lds + redo[k] + i * 112) = k == 0 ? hs(acc[0][i], acc[1][i]) : hs(acc[k - 1][2 + i], acc[2][2 * k - 2 + i]);
      __builtin_amdgcn_sched_barrier(0);
    }
  };
#pragma unroll
  for (int f = 0; f < NC; ++f) {
    const u32x4 u = bm0[f][0], v = bm0[f][1];
    bm[f] = c < 8 ? mk8(u[0], u[2], v[0], v[2]) : mk8(u[1], u[3], v[1], v[3]);
    bm_put(f);
#pragma unroll
    for (int t = 0; t < 3; ++t) acc[t] = smfma64(bm[f], Wqk[t][f], acc[t], sidx);
  }
  red_put();
  VM_DRAIN();
  lds_arrive((unsigned*)(misc + 6), lane);
  lds_wait((unsigned*)(misc + 6), 8u);
  x_fetch_n<10>(x_src(a, 1, b), lds0, 1, 10 * w, lane * 16);
  LOGP(1);
  STAMP_DECL(); ARRIVE_DECL();
  bf16x8 fr, qx[2];
  auto kq_get = [&](int seg) {
    const char* p = lds + (w == 1 ? L_KX : L_QX) + (seg % NKQ) * 2048 + lane * 16;
    if (w == 1 || w == 2) { qx[0] = *(const bf16x8*)p; qx[1] = *(const bf16x8*)(p + 1024); }
  };
  unsigned qo4 = (unsigned)(L_RED1 + (w == 1 ? (c & 7) * 112 + (c >> 3) * 3584 : c * 112) + g * 16), qo2 = qo4 + 64u - (unsigned)(g * 8), qo2b = qo2 + 3584u;
  if (w < 3) { PIN_V(qo4); PIN_V(qo2); if (w != 1) PIN_V(qo2b); }
  auto pub1 = [&](unsigned off, unsigned o1) {
    if (w == 0 && PK_ADD) {
#define PKA(d, b) ".long 0xd3b24000 + " #d ", 0x18000000 + 256 + " #d " + ((256 + " #b ") << 9)\n\t"
      u32x32 T;
      asm volatile("ds_read_b128 v[224:227], %1\n\tds_read_b128 v[228:231], %1 offset:1792\n\tds_read_b128 v[232:235], %1 offset:3584\n\tds_read_b128 v[236:239], %1 offset:5376\n\t"
                   "ds_read2_b64 v[240:243], %2 offset1:224\n\tds_read2_b64 v[244:247], %3 offset1:224\n\tv_mov_b32 v251, 0\n\t"
                   "s_waitcnt lgkmcnt(4)\n\t" PKA(224, 228) PKA(226, 230) "s_waitcnt lgkmcnt(2)\n\t" PKA(232, 236) PKA(234, 238) PKA(224, 232) PKA(226, 234)
                   "s_waitcnt lgkmcnt(0)\n\t" PKA(240, 242) PKA(244, 246) "v_cvt_pk_bf16_f32 v248, v224, v225\n\t" PKA(240, 244) "v_cvt_pk_bf16_f32 v249, v226, v227\n\t"
                   "v_cvt_pk_bf16_f32 v250, v240, v241\n\ts_nop 1\n\tv_mfma_f32_16x16x32_bf16 v[224:227], v[248:251], v[248:251], 0\n\ts_nop 7\n\t"
                   "v_cvt_pk_bf16_f32 v228, v224, v225\n\tv_cvt_pk_bf16_f32 v229, v226, v227\n\tv_max_u32 v228, 1, v228\n\tv_max_u32 v229, 1, v229\n\t"
                   "buffer_store_dwordx2 v[228:229], %4, %5, %6 offen sc1"
                   : "=&{v[224:255]}"(T) : "v"(lds0 + qo4), "v"(lds0 + qo2), "v"(lds0 + qo2b), "v"(off), "s"(rs), "s"(o1) : "memory");
      return;
    }
    if (w == 0) fr = qk_frag(lds + qo4, lds + qo2, lds + qo2b);
    else {
      const f32x4 a0 = *(const f32x4*)(lds + qo4), a1 = *(const f32x4*)(lds + qo4 + 1792);
      const f32x2 b0 = *(const f32x2*)(lds + qo2), b1 = *(const f32x2*)(lds + qo2 + 1792);
      float v[6];
#pragma unroll
      for (int i = 0; i < 6; ++i) v[i] = i < 4 ? a0[i] + a1[i] : b0[i - 4] + b1[i - 4];
#pragma unroll
      for (int i = 0; i < 6; ++i) v[i] += dppf<DPP_ROR8>(v[i]);
      fr = mk8(pack2(v[0], v[1]), pack2(v[2], v[3]), pack2(v[4], v[5]), 0u);
    }
    const f32x4 sc = mfma32(w == 0 ? fr : qx[0], fr, Z4);
    if (w == 0) SC1_STORE8S(((u32x2){pack2n(sc[0], sc[1]), pack2n(sc[2], sc[3])}), off, o1);
    else {
      const f32x4 s2 = mfma32(qx[1], fr, Z4);
      __builtin_amdgcn_raw_buffer_store_b128((u32x4){pack2n(sc[0], sc[1]), pack2n(sc[2], sc[3]), pack2n(s2[0], s2[1]), pack2n(s2[2], s2[3])}, rs, off, o1, 16);
    }
  };
  unsigned o1 = sct_slot(ck, 0u), o1m = scm_slot(ck, 0u);
  unsigned t2o = ts_tok(h, j) + (unsigned)((c >> 2) * 256 + g * 64 + (c & 3) * 16);
  TokScores t2;
  TokTask tk;
  PvTask2 pv2;
  if (w == 2) tk.init(h, j, lane);
  if (w == 3) { PIN_V(t2o); pv2.init(gp, lane); }
  if (w == 0) pub1(p1v, o1m);
  VM_DRAIN();
  BAR();
  const unsigned tst = now32() | 1u;
  if (w == 3 && ST_X) __builtin_amdgcn_raw_buffer_store_b32(tst, rs, lane < 2 ? ts_piece(b >> 2) - ACC_TS + (b & 3) * 4 + lane * 8192 : 0x7ffffff0, (unsigned)(OFF_ACC + (SR - 1) * ACC_STEP) + ACC_TS, 16);
  LOGP(2);
  kq_get(0);
  if (w == 1) pub1(p1v, o1);
  x_fetch_n<10>(x_src(a, 2, b), lds0, 2, 10 * w, lane * 16);
  if (w == 3) { t2.get(lds, 0, lane); t2.run(0); t2.run(1); t2.store(rs, t2o, ts_slot(ck, 0)); }
  LOGP(3);
  unsigned o2 = (unsigned)OFF_ACC, o2p = 0u, tw = 0u;
  asm volatile("" : "+s"(o2p));
  unsigned tc0 = 0u, tstar = 0u;
  int dmin = 0, dmax = 0;
  int e1 = 100000;
  unsigned offs = 100u;
  unsigned o4 = 0u;
  unsigned o3, o3p;
  u32x16 P = {0u, 0u, 0u, 0u, 0u, 0u, 0u, 0u, 0u, 0u, 0u, 0u, 0u, 0u, 0u, 0u}, V = P;
  u32x8 V8 = {0u, 0u, 0u, 0u, 0u, 0u, 0u, 0u};
  if (w < 2) asm volatile("" : "+{v[208:223]}"(P), "+{v[184:191]}"(V8), "+{v[192:207]}"(V));
  unsigned tso = lane < 2 ? ts_piece(b >> 2) + (unsigned)((b & 3) * 4 + lane * 8192) : 0x7ffffff0u;
  if (w < 2) PIN_V(tso);
  float minf = g >= 2 ? -INFINITY : 0.f;
  if (w < 2) PIN_V(minf);
  int sv = NV - 1;
  bf16x8 vox[5];
  unsigned vma[3], need2;
  u32x4 slo = {0u, 0u, 0u, 0u}, shi = slo;
  float k1 = 0.f;
  const u16* xsrc;
  float invv = 1.0f;
  auto prep0 = [&](int s) {
    xsrc = x_src(a, s + 3, b);
    if (w == 2) { o3p = ts_slot(ck, (s + 1 - LAG3) & 3); o3 = ts_slot(ck, imax(s, 0)); o4 = pt_slot(ck, imax(s - LAG3, 0)); PIN_S(o3); }
    if (w == 3) { o3p = pt_slot(ck, (s - LAG4) & 3); o4 = ts_slot(ck, s + 1); }
    PIN_S(xsrc); PIN_S(o3p); PIN_S(o4);
    __builtin_amdgcn_sched_barrier(0);
  };
  auto prep = [&](int s) {
    o2 = (unsigned)OFF_ACC + (unsigned)(s % SR) * (unsigned)ACC_STEP;
    sv = sv == NV - 1 ? 0 : sv + 1;
    PIN_S(o2); PIN_S(sv);
    if (w < 2) {
      PIN_S(tc0);
      o1 = sct_slot(ck, s % SR); o1m = scm_slot(ck, s % SR);
      need2 = 4u * (unsigned)(s + 1) + 2u * ((unsigned)(s + 2) / NSEG);
      PIN_S(o1); PIN_S(o1m); PIN_S(need2);
      __builtin_amdgcn_sched_barrier(0);
      kq_get(s);
    }
    invv = misc[4 + (s & 1)];
  };
  auto prep2 = [&](int s) {
#pragma unroll
    for (int k = 0; k < 3; ++k) {
      const int t = k == 0 ? 2 : (k == 1 ? far : 1 - far);
      const char* p = lds + L_VOX + sv * 5120 + t * 1024 + (plane & 47) * 16 + (g & 1) * 8;
      auto rd = [&](const char* q) { const u32x2 a0 = *(const u32x2*)q, a1 = *(const u32x2*)(q + 256); return mk8(a0[0], a0[1], a1[0], a1[1]); };
      vox[k == 0 ? 0 : 2 * k - 1] = rd(p);
      if (k) vox[2 * k] = rd(p + 3072);
      vma[k] = lds0 + (unsigned)(L_VOM + sv * 1280 + t * 256 + (plane & 31) * 8);
      PIN_V(vma[k]);
    }
    __builtin_amdgcn_sched_barrier(0);
    if (F0A) { xsrc = x_src(a, s + 3, b); PIN_S(xsrc); }
    const unsigned iu = __builtin_amdgcn_readfirstlane(f2u(invv));
    if (__builtin_expect(iu != f2u(inv), 0)) {
      inv = u2f(iu);
#pragma unroll
      for (int i = 0; i < 8; ++i) {
        u32x4& m = i < 4 ? slo : shi;
        m[i & 3] = (c >> 1) == i ? ((c & 1) ? iu & 0xffff0000u : iu >> 16) : 0u;
      }
      k1 = KEXP * inv;
    }
    PIN_V(k1); PIN_V(slo); PIN_V(shi);
    asm volatile("" : "+v"(vox[0]), "+v"(vox[1]), "+v"(vox[2]), "+v"(vox[3]), "+v"(vox[4]));
    __builtin_amdgcn_sched_barrier(0);
  };
  auto rows = [&](int s) {
    const int dl = spin_until(tw);
    if (w < 2) tc0 = cyc32();
    __builtin_amdgcn_sched_barrier(0);
    STAMP(1);
    unsigned spins = 0;
    auto pass = [&](bool first) {
      u32x4 x[2 * NC];
#pragma unroll
      for (int f = 0; f < NC; ++f) {
        if (f && w < 2 && first) { prep(s); asm volatile(".rept %0\n\ts_nop 3\n\t.endr" :: "n"(P2_G1)); }
        else if (f) asm volatile(".rept %0\n\ts_nop 3\n\t.endr" :: "n"(w < 2 ? P2_G0 : (f == 1 ? P2_G2 : P2_G3)));
        __builtin_amdgcn_sched_barrier(0);
        x[2 * f] = SC1_LOADS(l2off[2 * f], o2p); x[2 * f + 1] = SC1_LOADS(l2off[2 * f + 1], o2p);
        __builtin_amdgcn_sched_barrier(0);
      }
      if (first) lds_inc<L_MISC + 92>(lds0);
      if (first && w < 2 && PR_P != 3) __builtin_amdgcn_s_setprio(PR_P);
      if (first) { if (w >= 2) prep(s); else prep2(s); }
      if (w >= 2 && first && P2_W) lds_spin<L_MISC + 92, 0>(lds0, 4u * (unsigned)s);
      if (w == 2 && first) tk.store(rs, o4, s > LAG3 ? 0u : 0x7ff00000u);
      if (w == 3 && first) t2.store(rs, t2o, o4);
      if (first && w < 2) {
        p1cur = p1v; offs = (offs - (unsigned)dl) & ~3u;
        PIN_V(p1cur); PIN_S(offs);
        tstar = tc0 + (unsigned)(e1 + D1_M + (w == 1 ? D1_W1 : 0)) - offs * 20u;
        dmin = offs < 40u ? D1_MIN : REC_D1;
        dmin = s < EARLY_N ? REC_D1 + EARLY_D1 : dmin;
        dmax = REC_D1;
        PIN_S(tstar); PIN_S(dmin); PIN_S(dmax);
      }
      __builtin_amdgcn_sched_barrier(0);
      unsigned ok = 32u;
#pragma unroll
      for (int t = 0; t < 3; ++t) { acc[t] = Z4; PIN_V(acc[t]); }
      if (first && w < 2 && PR_P != 3) __builtin_amdgcn_s_setprio(3);
      __builtin_amdgcn_sched_barrier(0);
      bool r = true;
#pragma unroll
      for (int f = 0; f < NC; ++f) {
        unsigned p[4];
#pragma unroll
        for (int k = 0; k < 2; ++k) {
          const u32x4 v = x[2 * f + k];
          p[2 * k] = pack2((float)(int)v[0], (float)(int)v[1]); p[2 * k + 1] = pack2((float)(int)v[2], (float)(int)v[3]);
          asm volatile("" : "+v"(p[2 * k]), "+v"(p[2 * k + 1]));
          __builtin_amdgcn_sched_barrier(0);
        }
        ok = (ok & x[2 * f][0] & x[2 * f][2]) & (x[2 * f + 1][0] & x[2 * f + 1][2]);
        PIN_V(ok);
        __builtin_amdgcn_sched_barrier(0);
        bm[f] = mk8(p[0], p[1], p[2], p[3]);
        acc[0] = smfma64(bm[f], Wqk[0][f], acc[0], sidx);
        __builtin_amdgcn_sched_barrier(0);
        bm_put(f);
        acc[1] = smfma64(bm[f], Wqk[1][f], acc[1], sidx);
        __builtin_amdgcn_sched_barrier(0);
        if (f == NC - 1) { u64 bad = __builtin_amdgcn_ballot_w64(ok == 0u); PIN_S(bad); r = bad == 0ull; }
        __builtin_amdgcn_sched_barrier(0);
        acc[2] = smfma64(bm[f], Wqk[2][f], acc[2], sidx);
        __builtin_amdgcn_sched_barrier(0);
      }
      red_put();
      return r;
    };
    if (__builtin_expect(!pass(true), 0)) {
      FC(0);
      do {
        if (dead || ++spins > SPIN_CAP) { dead = true; break; }
        asm volatile("" ::: "memory");
      } while (!pass(false));
    }
    STAMPV(12, spins); LOGF(1, spins);
    TSTAMP(2, now32()); TSTAMP(3, spins);
  };
  if (w >= 2) prep0(-1);
  prep(0);
  if (w < 2) prep2(0);
  VM_DRAIN();
  CODE_PAD((PADR >> (4 * w)) & 15);
#pragma unroll 1
  for (int s = 0;; ++s) {
    if (__builtin_expect(o2p != 0u, 1)) rows(s);
    if (w < 2 && __builtin_expect(s == NSEG - 1, 0)) break;
    STAMP(2);
    BAR();
    STAMP(3);
    if (w < 2) pub1(p1cur, w == 0 ? o1m : o1);
    if (w == 2) tk.load(rs, o3p);
    if (w == 3) pv2.load(rs, o3p);
    const unsigned tp1 = cyc32();
    __builtin_amdgcn_sched_barrier(0);
    if (w == 2) {
      __builtin_amdgcn_s_setprio(W2_P);
      prep0(s);
      if (W2_D) spin_cyc(tp1 + W2_D);
      fr = qk_frag(lds + qo4, lds + qo2, lds + qo2b);
      t2_mem(fr, qx[0], qx[1], rs, o3, h, j, invv * KEXP, lane);
      __builtin_amdgcn_sched_barrier(0);
      asm volatile("s_waitcnt vmcnt(1)" ::: "memory");
      lds_inc<L_MISC + 28>(lds0);
      kq_get(s + 1);
    }
    if (w == 3) {
      __builtin_amdgcn_s_setprio(T4_P);
      prep0(s);
      t2.get(lds, s + 1, lane); t2.run(0); t2.run(1);
      __builtin_amdgcn_sched_barrier(0);
    }
    if (w >= 2 && __builtin_expect(s == NSEG - 2, 0)) { VM_DRAIN(); lds_inc<L_MISC + 28>(lds0); }
    if (w >= 2) __builtin_amdgcn_s_setprio(UN_P);
    TSTAMP(0, now32());
    STAMP(4);
    __builtin_amdgcn_sched_barrier(0);
    unsigned tpub = 0;
    u32x2 sw;
    if (w == 2) {
      STAMP(5);
      __builtin_amdgcn_sched_barrier(0);
      STAMP(6);
      ARRIVE(0);
      tk.run(j, lane, SelLo);
      if (__builtin_expect(s == NSEG - 1, 0)) { tlast = tp1; break; }
      if (F2B) { lds_spin<L_MISC + 48, 1>(lds0, 2u * (unsigned)(s + 1)); x_fetch_n<F2B>(xsrc, lds0, s + 3, P2B, lane * 16); }
      __builtin_amdgcn_s_setprio(SP_P);
    }
    if (w == 3) {
      pv2.run<false>(lds, sv < 1 + LAG4 ? sv + NV - 1 - LAG4 : sv - 1 - LAG4, lane, rs, 0u);
      if (__builtin_expect(s == NSEG - 1, 0)) { tlast = tp1; break; }
      lds_inc<L_MISC + 112>(lds0);
      STAMP(11);
      __builtin_amdgcn_s_setprio(SP_P);
      STAMP(5);
      ARRIVE(0);
      if (F3) { lds_spin<L_MISC + 48, 1>(lds0, 2u * (unsigned)(s + 1)); x_fetch_n<F3>(xsrc, lds0, s + 3, 40 - F3C - F3, lane * 16); }
      STAMP(6);
    }
    if (w < 2) {
      __builtin_amdgcn_s_setprio(D1_P);
      x_fetch_n<F0A>(xsrc, lds0, s + 3, PA + w * F0A, lane * 16);
      int dq = imin((int)(tstar - tp1), dmax);
      dq = dq < -D1_LATE ? dmax : dq;
      PIN_S(dq);
      spin_exact(tp1 + (unsigned)imax(dq, dmin));
      STAMP(5);
      u32x32 S;
      auto sc_load = [&]() {
        asm volatile("buffer_load_dwordx4 v[224:227], %2, %4, %5 offen sc1\n\t.rept %7\n\ts_nop 3\n\t.endr\n\t"
                     "buffer_load_dwordx4 v[228:231], %3, %4, %6 offen sc1\n\tbuffer_load_dwordx4 v[232:235], %3, %4, %6 offen offset:2048 sc1\n\t"
                     "buffer_load_dwordx4 v[236:239], %3, %4, %6 offen offset:256 sc1\n\tbuffer_load_dwordx4 v[240:243], %3, %4, %6 offen offset:2304 sc1\n\t"
                     "v_mov_b32 v220, 0x42000000\n\tv_mov_b32 v221, 0\n\tv_mov_b32 v222, 0x42000000\n\tv_mov_b32 v223, 0"
                     : "=&{v[224:255]}"(S), "+{v[208:223]}"(P) : "v"(l1offm), "v"(l1off), "s"(rs), "s"(o1m), "s"(o1), "n"(P1_G) : "memory");
      };
      sc_load();
      __builtin_amdgcn_sched_barrier(0);
      lds_inc<L_MISC + 48>(lds0);
      STAMP(6);
      ARRIVE(0);
      lds_spin<L_MISC + 28, 0>(lds0, need2);
      __builtin_amdgcn_s_setprio(3);
      asm volatile("ds_read_b64 v[192:193], %3\n\tds_read_b64 v[196:197], %3 offset:768\n\tds_read_b64 v[200:201], %4\n\tds_read_b64 v[204:205], %4 offset:768\n\tds_read_b64 v[184:185], %2"
                   : "+{v[184:191]}"(V8), "+{v[192:207]}"(V) : "v"(vma[0]), "v"(vma[1]), "v"(vma[2]) : "memory");
      __builtin_amdgcn_sched_barrier(0);
      {
        const int de = imin((int)(tp1 - tc0) - e1, D1_C);
        e1 += s == 0 ? 0 : de >> (s < EARLY_N && de < -D1_C ? 0 : D1_G);
        PIN_S(e1);
      }
      unsigned sn = (unsigned)(s + 1);
      PIN_V(sn);
      STAMP(11);
      __builtin_amdgcn_sched_barrier(0);
      {
        unsigned spins = 0;
        u64 miss, bad;
#define MM(d, a, b, c) "v_mfma_f32_16x16x32_bf16 " d ", " a ", " b ", " c "\n\t"
#define FM(d, x, r, ctrl) "v_fmac_f32_dpp " d ", " x ", " r " " ctrl "\n\t"
#define ID_ "quad_perm:[0,1,2,3] row_mask:0xf bank_mask:"
#define HM_ "row_half_mirror row_mask:0xf bank_mask:"
#define R8_ "row_ror:8 row_mask:0xf bank_mask:"
#define MI_ "row_mirror row_mask:0xf bank_mask:"
        auto soft = [&]() {
          asm volatile(
            "s_waitcnt vmcnt(4)\n\t" MM("v[244:247]", "%6", "v[224:227]", "0") "v_min3_u32 v214, v224, v225, v226\n\t"
            "s_waitcnt vmcnt(3)\n\t" MM("v[248:251]", "%6", "v[228:231]", "0") "v_min3_u32 v214, v214, v227, v228\n\tv_min3_u32 v214, v214, v229, v230\n\t"
            "s_waitcnt vmcnt(2)\n\t" MM("v[248:251]", "%6", "v[232:235]", "v[248:251]") "v_min3_u32 v214, v214, v231, v232\n\t"
            "v_add_f32_dpp v212, v244, v244 " R8_ "0x3\n\tv_add_f32_dpp v213, v245, v245 " R8_ "0x3\n\t"
            "s_waitcnt vmcnt(1)\n\t" MM("v[248:251]", "%7", "v[236:239]", "v[248:251]")
            "v_add_f32_dpp v212, v246, v246 " R8_ "0xc\n\tv_add_f32_dpp v213, v247, v247 " R8_ "0xc\n\t"
            "s_waitcnt vmcnt(0) lgkmcnt(0)\n\t" MM("v[248:251]", "%7", "v[240:243]", "v[248:251]")
            "v_fma_f32 v212, v212, %14, %15\n\tv_fma_f32 v213, v213, %14, %15\n\tv_exp_f32 v216, v212\n\tv_exp_f32 v217, v213\n\t"
            "v_min3_u32 v214, v214, v233, v234\n\tv_cvt_pk_bf16_f32 v208, v216, v217\n\tv_min3_u32 v214, v214, v235, v236\n\tds_read_b96 v[188:190], %16 offset:%18\n\t"
            "v_exp_f32 v216, v248\n\tv_mov_b32_dpp v209, v208 " R8_ "0xf\n\tv_exp_f32 v217, v249\n\tv_exp_f32 v218, v250\n\t"
            MM("v[252:255]", "%8", "v[208:211]", "0") "v_exp_f32 v219, v251\n\tv_cvt_pk_bf16_f32 v232, v216, v217\n\t"
            MM("v[224:227]", "v[192:195]", "v[208:211]", "0") "v_cvt_pk_bf16_f32 v233, v218, v219\n\tv_min3_u32 v214, v214, v237, v238\n\tv_mov_b32_dpp v234, v232 " R8_ "0xf\n\t"
            MM("v[228:231]", "v[196:199]", "v[208:211]", "0") "v_mov_b32_dpp v235, v233 " R8_ "0xf\n\t"
            MM("v[244:247]", "v[204:207]", "v[208:211]", "0") "v_min3_u32 v214, v214, v239, v240\n\t"
            MM("v[252:255]", "%8", "v[232:235]", "v[252:255]") MM("v[224:227]", "%10", "v[232:235]", "v[224:227]") "v_min3_u32 v214, v214, v241, v242\n\t"
            MM("v[228:231]", "%11", "v[232:235]", "v[228:231]") "s_waitcnt lgkmcnt(0)\n\tv_sub_u32 v215, v190, %17 clamp\n\t"
            MM("v[236:239]", "v[200:203]", "v[208:211]", "0") "v_min3_u32 v214, v214, v243, v215\n\tv_cmp_eq_u32 %4, 0, v214\n\tv_rcp_f32 v216, v252\n\t"
            MM("v[240:243]", "v[184:187]", "v[208:211]", "0") "v_mul_f32 v216, v216, v188\n\tv_subrev_u32 v215, 0x2f800000, v252\n\t"
            MM("v[236:239]", "%12", "v[232:235]", "v[236:239]") FM("v220", "v224", "v216", ID_ "0x1") FM("v221", "v225", "v216", ID_ "0x1")
            "v_mov_b32_dpp v217, v216 " HM_ "0xf\n\t"
            MM("v[244:247]", "%13", "v[232:235]", "v[244:247]") FM("v220", "v230", "v216", ID_ "0x2") FM("v221", "v231", "v216", ID_ "0x2")
            MM("v[240:243]", "%9", "v[232:235]", "v[240:243]") FM("v220", "v228", "v217", HM_ "0x1") FM("v221", "v229", "v217", HM_ "0x1")
            "v_cmp_ge_u32 vcc, v215, v189\n\t" FM("v220", "v226", "v217", HM_ "0x2") FM("v221", "v227", "v217", HM_ "0x2")
            "s_or_b64 %5, %4, vcc"
            : "+{v[224:255]}"(S), "+{v[208:223]}"(P), "+{v[184:191]}"(V8), "+{v[192:207]}"(V), "=&s"(miss), "=&s"(bad)
            : "v"(slo), "v"(shi), "v"(ONES), "v"(vox[0]), "v"(vox[1]), "v"(vox[2]), "v"(vox[3]), "v"(vox[4]), "v"(k1), "v"(minf), "v"(lds0), "s"(s), "n"(L_MISC + 32)
            : "vcc", "scc", "memory");
        };
        soft();
        if (__builtin_expect(bad != 0ull, 0)) {
          if (miss) FC(1); else FC(2);
          while (miss != 0ull && !dead) {
            if (++spins > SPIN_CAP) dead = true;
            asm volatile("s_nop 7\n\ts_nop 7" ::: "memory");
            sc_load(); soft();
          }
          if (bad != 0ull) {
            u32x4 y[5];
#pragma unroll
            for (int k = 0; k < 4; ++k) y[k] = SC1_LOADS(l1off + (k & 1) * 2048 + (k >> 1) * 256, o1);
            y[4] = SC1_LOADS(l1offm, o1m);
            const f32x4 d0 = mfma32(as8(slo), as8(y[4]), Z4);
            f32x4 d1 = mfma32(as8(shi), as8(y[3]), mfma32(as8(shi), as8(y[2]), mfma32(as8(slo), as8(y[1]), mfma32(as8(slo), as8(y[0]), Z4))));
            const bool lo = c < 8;
            const float m0 = __builtin_fmaf((lo ? d0[0] : d0[2]) + dppf<DPP_ROR8>(lo ? d0[2] : d0[0]), k1, minf);
            const float m1 = __builtin_fmaf((lo ? d0[1] : d0[3]) + dppf<DPP_ROR8>(lo ? d0[3] : d0[1]), k1, minf);
            float mx = fmx(fmx(fmx(d1[0], d1[1]), d1[2]), fmx(fmx(d1[3], m0), m1));
            mx = fmx(mx, dppf<DPP_ROR8>(mx));
            mx = x32mx(x16mx(mx));
            d1 -= mx;
            const unsigned mm = pack2(__builtin_amdgcn_exp2f(m0 - mx), __builtin_amdgcn_exp2f(m1 - mx));
            const unsigned t01 = pack2(__builtin_amdgcn_exp2f(d1[0]), __builtin_amdgcn_exp2f(d1[1])), t23 = pack2(__builtin_amdgcn_exp2f(d1[2]), __builtin_amdgcn_exp2f(d1[3]));
            const bf16x8 pm = mk8(mm, dpp_ror8(mm), 0u, 0u), pt = mk8(t01, t23, dpp_ror8(t01), dpp_ror8(t23));
            const float r = __builtin_amdgcn_rcpf(mfma32(ONES, pt, mfma32(ONES, pm, Z4))[0]) * u2f(V8[4]), rm = dppf<0x141>(r);
            f32x4 o[5];
#pragma unroll
            for (int k = 0; k < 5; ++k) o[k] = mfma32(vox[k], pt, mfma32(k ? mk8(V[4 * k - 4], V[4 * k - 3], 0u, 0u) : mk8(V8[0], V8[1], 0u, 0u), pm, Z4));
#pragma unroll
            for (int i = 0; i < 2; ++i) {
              const float oa = dppf<0x141>(o[2][i]), ob = dppf<0x141>(o[1][2 + i]);
              P[12 + i] = f2u(__builtin_fmaf(hd ? ob : oa, rm, __builtin_fmaf(hd ? o[2][2 + i] : o[1][i], r, i ? 0.f : 32.0f)));
            }
            P[8] = f2u(r); P[9] = f2u(rm);
#pragma unroll
            for (int i = 0; i < 4; ++i) { S[12 + i] = f2u(o[3][i]); S[16 + i] = f2u(o[0][i]); S[20 + i] = f2u(o[4][i]); }
            asm volatile("s_nop 7\n\ts_nop 7" : "+{v[224:255]}"(S), "+{v[208:223]}"(P));
          }
        }
        STAMP(7); STAMPV(13, spins); LOGF(0, spins);
        {
          asm volatile(".if %11 == 0\n\tds_write_b32 %8, %12 offset:%10\n\t.endif\n\t"
                       "v_cvt_i32_f32 v218, v220\n\tv_cvt_i32_f32 v219, v221\n\tv_and_or_b32 v218, v218, %7, 2\n\tbuffer_atomic_add_x2 v[218:219], %3, %5, %6 offen\n\t"
                       ".if %11 == 1\n\tds_write_b32 %8, %12 offset:%10\n\t.endif\n\t"
                       FM("v222", "v236", "v216", R8_ "0x4") FM("v223", "v237", "v216", R8_ "0x4") "ds_read_b64 %2, %8 offset:%9\n\t"
                       FM("v222", "v238", "v217", MI_ "0x8") FM("v223", "v239", "v217", MI_ "0x8") "v_permlane32_swap_b32 v240, v242\n\t"
                       FM("v222", "v244", "v217", MI_ "0x4") FM("v223", "v245", "v217", MI_ "0x4") "v_permlane32_swap_b32 v241, v243\n\t"
                       FM("v222", "v246", "v216", R8_ "0x8") FM("v223", "v247", "v216", R8_ "0x8") ".if %11 == 2\n\tds_write_b32 %8, %12 offset:%10\n\t.endif\n\t"
                       FM("v222", "v240", "v216", ID_ "0x1") FM("v223", "v241", "v216", ID_ "0x1") FM("v222", "v242", "v217", HM_ "0x1") FM("v223", "v243", "v217", HM_ "0x1")
                       "v_cvt_i32_f32 v212, v222\n\tv_cvt_i32_f32 v213, v223\n\tv_and_or_b32 v212, v212, %7, 2\n\tbuffer_atomic_add_x2 v[212:213], %4, %5, %6 offen\n\t"
                       ".if %11 == 3\n\tds_write_b32 %8, %12 offset:%10\n\ts_waitcnt lgkmcnt(1)\n\t.else\n\ts_waitcnt lgkmcnt(0)\n\t.endif"
                       : "+{v[224:255]}"(S), "+{v[208:223]}"(P), "=&v"(sw) : "v"(acco), "v"(acco2), "s"(rs), "s"(o2), "s"(~63u), "v"(lds0), "n"(L_MISC + 96),
                         "n"(L_MISC + 104 + 4 * (w & 1)), "n"(REL_AT), "v"(sn) : "memory");
          __builtin_amdgcn_sched_barrier(0);
          tpub = now32();
          asm volatile("buffer_atomic_umax %0, %1, %2, %3 offen" :: "v"(tpub | 1u), "v"(tso), "s"(rs), "s"(o2) : "memory");
          STAMP(10);
        }
      }
    }
    STAMP(8);
    ARRIVE(1);
    o2p = o2;
    PIN_S(o2p);
    if (w < 2) {
      if (__builtin_expect(__builtin_amdgcn_readfirstlane(sw[1]) <= (unsigned)s, 0)) {
        lds_spin<L_MISC + 100, 0>(lds0, (unsigned)s + 1u);
        sw[0] = __hip_atomic_load((unsigned*)(misc + 24), __ATOMIC_RELAXED, __HIP_MEMORY_SCOPE_WORKGROUP);
      }
    } else {
      u32x4 q;
      asm volatile("1:\n\tds_read_b128 %0, %1 offset:%3\n\t.if %4\n\ts_sleep 1\n\t.endif\n\ts_waitcnt lgkmcnt(0)\n\tv_min3_u32 v201, v201, v202, v203\n\tv_cmp_ge_u32 vcc, %2, v201\n\ts_cbranch_vccnz 1b"
                   : "=&{v[200:203]}"(q) : "v"(lds0), "s"(s), "n"(L_MISC + 96), "n"(SP_SL) : "vcc", "memory");
      __builtin_amdgcn_s_setprio(3);
      tpub = now32();
      sw[0] = q[0];
    }
    STAMP(9);
    static_assert(F3C == 0 && F2C == 0 && F0C == 0, "nothing behind the atomics");
    LOGT(0, tpub); LOGT(1, tb2_);
    TSTAMP(1, tpub);
    ARRIVE_OUT();
    const unsigned an = __builtin_amdgcn_readfirstlane(sw[0]);
    const int ahead = (int)(an - tpub);
    tw = tpub + (unsigned)(an == 0u || ahead > 1000 ? SCH_FIRST : imax(ahead, w < 2 ? SCH_DMIN : 0));
    offs = tw - an;
  }
  STAMP_OUT();
  LOGP(4);
  __builtin_amdgcn_s_setprio(3);
  if (w < 2) { BAR(); tlast = cyc32(); }
  if (w == 2) tk.store(rs, o4);
  if (w == 3) {
    const unsigned oo = op_slot(NSEG - 2 - LAG4) + (unsigned)((m * 32 + gp) * 1280);
    const char* sp = lds + L_STG + 2048;
    const u32x4 v0 = *(const u32x4*)(sp + lane * 16), v1 = *(const u32x4*)(sp + 1280 + lane * 16), v2 = *(const u32x4*)(sp + (g & 1) * 1280 + 1024 + c * 16);
    SC1_STORE(v0, oo + lane * 16); SC1_STORE(v1, oo + 20480 + lane * 16); SC1_STORE(v2, lane < 32 ? oo + g * 20480 + 1024 + c * 16 : 0x7ffffff0u);
  }
  if (dead) tlast |= 1u; else tlast &= ~1u;
}

__device__ __forceinline__ void drain(const RecArgs& a, char* lds, const __amdgpu_buffer_rsrc_t rs, const int w, const unsigned tp1) {
  static_assert(LAG3 == 2 && LAG4 == 3 && LAG5 == 6, "drain schedule");
  const int lane = __builtin_amdgcn_workitem_id_x() & 63, b = __builtin_amdgcn_workgroup_id_x(), c = lane & 15;
  const int gp = (b & 7) * 2 + (b >> 7), m = (b >> 3) & 15, j = m & 7;
  unsigned* const misc = (unsigned*)(lds + L_MISC);
  const unsigned lds0 = (unsigned)(uintptr_t)(LAS char*)lds, ck = __builtin_amdgcn_readfirstlane(misc[11]);
  bool dead = tp1 & 1u;
  auto poll = [&](auto load, auto ready) {
    for (unsigned spins = 0;;) {
      asm volatile("" ::: "memory");
      load();
      if (ready() || dead) break;
      if (++spins > SPIN_CAP) dead = true;
      if (END_SL) __builtin_amdgcn_s_sleep(END_SL);
    }
  };
  LOGD(0);
#define TASK(ty, sg, tm) ((u64)((ty) | ((sg) - 56) << 2 | ((tm) / 100) << 5))
  u64 todo = w == 0 ? TASK(1, 63, END_E1) | TASK(2, 63, END_E2) << 12 | TASK(3, 62, END_E3) << 24
           : w == 1 ? TASK(1, 62, 0) | TASK(2, 62, END_E62) << 12 | TASK(3, 60, END_E3) << 24
           : w == 2 ? TASK(2, 60, 0) | TASK(3, 56, 0) << 12 : TASK(2, 61, END_E61) | TASK(3, 58, END_E59) << 12;
  TokTask tk;
  PvTask2 pv;
  OutTask ot;
  {
    const u32x4* p = (const u32x4*)(lds + L_DRN) + lane;
    const u32x4 q0 = p[0], q1 = p[64], q2 = p[128];
    tk.l0 = q0[0]; tk.l1 = q0[0] + 16384u; tk.lm = q0[1]; tk.s0 = q0[2]; tk.s1 = q0[3];
    pv.lo = q1[0]; pv.lm = q1[1]; pv.vr = q1[2]; pv.ma = q1[3]; pv.mz = q2[0]; pv.so = q2[1];
    pv.pa[0] = pv.pa[1] = pv.pb[0] = pv.pb[1] = (u32x4){0u, 0u, 0u, 0u};
    ot.t5f = q2[2]; ot.t5o = q2[3]; ot.sel = as8(p[192]);
  }
#pragma unroll 1
  for (int ti = 0; todo; todo >>= 12, ++ti) {
    const int ty = (int)todo & 3, sl = 56 + (((int)todo >> 2) & 7);
    const unsigned tm = tp1 + (((unsigned)todo >> 5) & 0x7fu) * 100u;
    if (ty == 1) {
      wait_cyc(tm);
      LOGD(3 * ti + 1);
      poll([&]() { tk.load(rs, ts_slot(ck, sl)); }, [&]() { return tk.ready(); });
      LOGD(3 * ti + 2);
      tk.run(j, lane, ot.sel); tk.store(rs, pt_slot(ck, sl));
    } else if (ty == 2) {
      const unsigned t0 = cyc32();
      if (sl >= NSEG - 2) { lds_wait(misc + 7, 4u * NSEG + 3u); wait_cyc(t0 + END_G2); }
      wait_cyc(tm);
      LOGD(3 * ti + 1);
      poll([&]() { pv.load(rs, pt_slot(ck, sl)); }, [&]() { return pv.ready(); });
      LOGD(3 * ti + 2);
      pv.run<true>(lds, sl % NV, lane, rs, op_slot(sl) + (unsigned)((m * 32 + gp) * 1280));
      if (sl == NSEG - 1) misc[14] = cyc32() | 1u;
    } else {
      if (sl >= NSEG - 4) { lds_wait(misc + 14, 1u); wait_cyc(__builtin_amdgcn_readfirstlane(misc[14]) + END_G3); }
      wait_cyc(tm);
      LOGD(3 * ti + 1);
      OutTask o2 = ot;
      poll([&]() { o2.load(a, rs, sl + 1, b); ot.load(a, rs, sl, b); }, [&]() { return ot.ready(b) & o2.ready(b); });
      LOGD(3 * ti + 2);
      o2.run(lane); o2.store(a, sl + 1, b, lane);
      ot.run(lane); ot.store(a, sl, b, lane);
    }
    LOGD(3 * ti + 3);
  }
  VM_DRAIN();
  if (w != 0) lds_inc<L_MISC + 64>(lds0);
  else {
    constexpr unsigned o2 = (unsigned)OFF_ACC + (unsigned)((NSEG - 1) % SR) * (unsigned)ACC_STEP;
    lds_wait(misc + 16, 3u);
    LOGD(10);
    if (lane == 0) __hip_atomic_fetch_add((gu64*)((GAS char*)(a.ws + o2) + acc_off(m * 4, 0)), (u64)1, RLX_AGENT);
    wait_until(now32() + SCH_DRAIN);
    wait_cyc(tp1 + END_E4);
    unsigned x;
    if (qkv_row(b) + 48 >= 55 && qkv_col(b) < 15) poll([&]() { x = __builtin_amdgcn_raw_buffer_load_b32(rs, acc_off(c * 4, 0), o2, 16); }, [&]() { return wave_all((x & 16u) != 0u); });
    lds_inc<L_MISC + 68>(lds0);
    LOGD(11);
  }
  if (qkv_row(b) + 48 >= 55 && qkv_col(b) < 15) {
    lds_wait(misc + 18, 8u);
    lds_spin<L_MISC + 68, 1>(lds0, 1u);
    x_fetch_n<10>(x_src(a, NSEG + 3, b), lds0, 3, 10 * w, lane * 16);
    VM_DRAIN();
    lds_inc<L_MISC + 68>(lds0);
  }
  LOGP(8);
}

__device__ __forceinline__ void proj_front(const RecArgs& a, char* lds, const __amdgpu_buffer_rsrc_t rs, const int v) {
  const int lane = __builtin_amdgcn_workitem_id_x() & 63, b = __builtin_amdgcn_workgroup_id_x(), c = lane & 15, g = lane >> 4;
  const int rb = (b & 7) + 8 * (b >> 5), cq = (b >> 3) & 3;
  const unsigned lds0 = (unsigned)(uintptr_t)(LAS char*)lds;
  unsigned* const cnt = (unsigned*)(lds + L_MISC) + 20;
  LOGF2(0);
  float* const red = (float*)(lds + L_RED1);
  float* const fin = red + 640;
  const f32x4 Z4 = {0.f, 0.f, 0.f, 0.f};
  x_fetch_n<8>((const u16*)(a.ws + OFF_OB) + (size_t)rb * 32 * DM, lds0, 0, 8 * v, lane * 16);
  bf16x8 W[2][20];
  const __amdgpu_buffer_rsrc_t rw = __builtin_amdgcn_make_buffer_rsrc(a.ws + OFF_WOBT + (size_t)(cq * 10 + 2 * v) * 20480, 0, 40960, 0x00020000);
  auto w_get = [&](int ks) {
#pragma unroll
    for (int i = 0; i < 2; ++i) W[i][ks] = as8(__builtin_amdgcn_raw_buffer_load_b128(rw, lane * 16 + (ks & 3) * 1024, (i * 20 + (ks & ~3)) * 1024, 0));
    __builtin_amdgcn_sched_barrier(0);
  };
#pragma unroll
  for (int ks = 0; ks < FW; ++ks) w_get(ks);
  const unsigned fo = (unsigned)(rb * 40960 + (cq * 5 + v) * 1024 + ((g >> 1) * 16 + c) * 16), f8 = fo + (g & 1) * 8, f16 = fo + (g & 1) * 20480;
  u32x2 xo[2][2];
#pragma unroll
  for (int k = 0; k < 4; ++k)
    xo[k >> 1][k & 1] = __builtin_bit_cast(u32x2, __hip_atomic_load((const gu64*)((const char*)a.xnew + f8 + (k >> 1) * 512 + (k & 1) * 20480), RLX_AGENT));
  asm volatile("s_waitcnt vmcnt(%0)" :: "n"(2 * FW + 4) : "memory");
  lds_inc<L_MISC + 80>(lds0);
  lds_wait(cnt, 5u);
  LOGF2(1);
  f32x4 acc[2][2] = {{Z4, Z4}, {Z4, Z4}};
  bf16x8 x[20][2];
#pragma unroll
  for (int ks = -3; ks < 20; ++ks) {
    if (ks >= 0 && ks + FW < 20) w_get(ks + FW);
#pragma unroll
    for (int t = 0; t < 2; ++t) {
      if (ks + 3 < 20) x[ks + 3][t] = *(const bf16x8*)(lds + L_XN + (t * 20 + ks + 3) * 1024 + lane * 16);
#pragma unroll
      for (int i = 0; i < 2 && ks >= 0; ++i) acc[i][t] = mfma32(W[i][ks < 0 ? 0 : ks], x[ks < 0 ? 0 : ks][t], acc[i][t]);
    }
    __builtin_amdgcn_sched_barrier(0);
  }
  u32x2 xp[2][2];
  PIN_V(acc[1][1]);
  LOGF2(2);
#pragma unroll
  for (int t = 0; t < 2; ++t) {
#pragma unroll
    for (int k = 0; k < 4; ++k) {
      const int i = k >> 1, q = k & 1;
      const unsigned r = pack2(acc[i][t][2 * q], acc[i][t][2 * q + 1]), x = xo[i][t][q];
      xp[i][t][q] = pack2(u2f(x << 16) + u2f(r << 16), u2f(x & 0xffff0000u) + u2f(r & 0xffff0000u));
      acc[i][t][2 * q] = u2f(xp[i][t][q] << 16); acc[i][t][2 * q + 1] = u2f(xp[i][t][q] & 0xffff0000u);
    }
    const f32x4 sm = acc[0][t] + acc[1][t];
    const float ml = x32sum(x16sum((sm[0] + sm[1]) + (sm[2] + sm[3]))) * (1.0f / 32.0f);
    float q = 0.f;
#pragma unroll
    for (int r = 0; r < 8; ++r) { const float d = acc[r >> 2][t][r & 3] - ml; q += d * d; }
    q = x32sum(x16sum(q));
    *(f32x2*)(red + (v * 32 + t * 16 + c) * 2) = (f32x2){ml, q};
  }
  lds_inc<L_MISC + 80>(lds0);
  if (v == 0) {
    lds_wait(cnt, 10u);
    LOGF2(3);
    float mw[5], ml = 0.f, m2 = 0.f;
#pragma unroll
    for (int i = 0; i < 5; ++i) { const f32x2 u = *(const f32x2*)(red + (i * 32 + (lane & 31)) * 2); mw[i] = u[0]; ml += u[0]; m2 += u[1]; }
    ml *= 0.2f;
#pragma unroll
    for (int i = 0; i < 5; ++i) m2 += 32.0f * (mw[i] - ml) * (mw[i] - ml);
    const unsigned xs = (unsigned)OFF_LNX + rb * 1024;
    if (lane < 32) SC1_STORE8S(((u32x2){f2u(ml), nz(f2u(m2))}), (cq * 32 + lane) * 8, xs);
    wait_cyc(cyc32() + FR_D);
    u32x2 x0, x1;
    for (unsigned spins = 0; spins < SPIN_CAP; ++spins) {
      x0 = __builtin_amdgcn_raw_buffer_load_b64(rs, lane * 8, xs, 16); x1 = __builtin_amdgcn_raw_buffer_load_b64(rs, 512 + lane * 8, xs, 16);
      if (wave_all(x0[1] != 0u && x1[1] != 0u)) break;
      asm volatile("" ::: "memory");
    }
    const float m0 = u2f(x0[0]), m1 = u2f(x1[0]);
    const float mean = x32sum(m0 + m1) * 0.25f;
    const float M2 = x32sum((u2f(x0[1]) + u2f(x1[1])) + 160.0f * ((m0 - mean) * (m0 - mean) + (m1 - mean) * (m1 - mean)));
    const float mb = rbf(mean), den = rbf(__builtin_sqrtf(rbf(rbf(M2 * (1.0f / DM)) + 1e-5f)));
    if (lane < 32) {
      fin[lane] = mb; fin[32 + lane] = 1.0f / den;
      if (cq == 0) __hip_atomic_store((GAS unsigned*)(a.ws + OFF_XNRM) + (rb * 32 + lane) * 4, f2u(1.02f * (M2 + DM * (mean - mb) * (mean - mb)) / (den * den)), RLX_AGENT);
    }
    lds_inc<L_MISC + 84>(lds0);
  }
  lds_wait(cnt + 1, 1u);
  LOGF2(4);
  const __amdgpu_buffer_rsrc_t rx = __builtin_amdgcn_make_buffer_rsrc(a.ws + OFF_XN, 0, (int)(OFF_QB - OFF_XN), 0x00020000);
  auto put = [&](const u32x2 p0, const u32x2 p1, unsigned so) {
    const auto r0 = __builtin_amdgcn_permlane16_swap(p0[0], p1[0], false, false), r1 = __builtin_amdgcn_permlane16_swap(p0[1], p1[1], false, false);
    __builtin_amdgcn_raw_buffer_store_b128((u32x4){r0[0], r1[0], r0[1], r1[1]}, rx, f16, so, 16);
  };
#pragma unroll
  for (int i = 0; i < 2; ++i) {
    u32x2 y[2];
#pragma unroll
    for (int t = 0; t < 2; ++t) {
      const float mb = fin[t * 16 + c], rden = fin[32 + t * 16 + c];
      y[t] = (u32x2){pack2(rbf(acc[i][t][0] - mb) * rden, rbf(acc[i][t][1] - mb) * rden), pack2(rbf(acc[i][t][2] - mb) * rden, rbf(acc[i][t][3] - mb) * rden)};
    }
    put(y[0], y[1], i * 512);
  }
  VM_DRAIN();
  lds_inc<L_MISC + 80>(lds0);
#pragma unroll
  for (int i = 0; i < 2; ++i) put(xp[i][0], xp[i][1], (unsigned)(OFF_XB - OFF_XN) + i * 512);
  if (v == 0) {
    lds_wait(cnt, 15u);
    if (rb < 4 && lane < 8) __hip_atomic_fetch_add((GAS unsigned*)(a.ws + OFF_RDY) + lane * 64, 1u, RLX_AGENT);
  }
  LOGF2(5);
}

__global__ __launch_bounds__(512, 2) void rec_kernel(RecArgs a) {
  __shared__ __attribute__((aligned(16))) char lds[L_BYTES];
  const int tid = __builtin_amdgcn_workitem_id_x(), wid = __builtin_amdgcn_readfirstlane(tid >> 6), w = wid & 3;
  const __amdgpu_buffer_rsrc_t rs = __builtin_amdgcn_make_buffer_rsrc(a.ws, 0, (int)COMM_BYTES, 0x00020000);
  if (tid < 32) ((float*)(lds + L_MISC))[tid] = (tid == 4 || tid == 5) ? 1.0f : 0.0f;
  BAR();
  if (a.layer && wid < 5) proj_front(a, lds, rs, wid);
  if (wid < 4) {
    unsigned tlast;
    if (w == 0) rec_waves<0>(a, lds, rs, tlast);
    else if (w == 1) rec_waves<1>(a, lds, rs, tlast);
    else if (w == 2) rec_waves<2>(a, lds, rs, tlast);
    else rec_waves<3>(a, lds, rs, tlast);
    drain(a, lds, rs, w, tlast);
  } else {
    unsigned vo[2][2];
    if (w == 0) token_waves<0>(a, lds, rs, vo);
    else if (w == 1) token_waves<1>(a, lds, rs, vo);
    else if (w == 2) token_waves<2>(a, lds, rs, vo);
    else token_waves<3>(a, lds, rs, vo);
    qkv_tail(a, lds, w, vo);
  }
}

#if defined(__HIP_DEVICE_COMPILE__)
#pragma clang attribute pop
#endif

#define ATT_P1 6
#define ATT_P2 10
#define ATT_P3 14
#define ATT_THR 6.0f
#define ATT_NT 4
constexpr int NT = ATT_NT, ATT_NQ = 8 / NT, KQ = S_ / NT;
#define ATT_LMAX 1.0e12f
#if defined(__HIP_DEVICE_COMPILE__)
#pragma clang attribute push(__attribute__((target("no-packed-fp32-ops"))), apply_to = function)
#endif
__global__ __launch_bounds__(512, 2) void attb_kernel(char* __restrict__ ws, u16* __restrict__ out) {
  const u16* const Qp = (const u16*)(ws + OFF_QB);
  const u16* const KV = (const u16*)(ws + OFF_KV);
  u16* const O = (u16*)(ws + OFF_OB);
  __shared__ __attribute__((aligned(16))) char wl[40960], ob[40960];
  __shared__ f32x4 mo_[8][NT - 1][3][64];
  const int tid = __builtin_amdgcn_workitem_id_x(), bid = __builtin_amdgcn_workgroup_id_x();
  __shared__ unsigned sink[64];
  own_code<ATT_CODE>(tid, sink);
  const int lane = tid & 63, wid = __builtin_amdgcn_readfirstlane(tid >> 6), kh = wid / ATT_NQ, r = lane & 31, hh = lane >> 5;
  const int hd = (bid & 7) * 4 + ((bid >> 3) & 3), qb = (bid >> 5) * ATT_NQ + wid % ATT_NQ;
  const int q0 = qb * 32 * NT;
  bf16x8 qa[NT]; u32x2 qb2[NT];
#pragma unroll
  for (int qt = 0; qt < NT; ++qt) {
    const u16* qr = Qp + ((size_t)hd * S_ + q0 + (qt + kh) % NT * 32 + r) * QST;
    { const u32x2* qp = (const u32x2*)(qr + 8 * hh); const u32x2 q0_ = qp[0], q1_ = qp[1]; qa[qt] = mk8(q0_[0], q0_[1], q1_[0], q1_[1]); }
    const u32x2 t = *(const u32x2*)(qr + 16);
    qb2[qt] = hh ? (u32x2){0u, 0u} : t;
  }
  const __amdgpu_buffer_rsrc_t rk = __builtin_amdgcn_make_buffer_rsrc((void*)(KV + ((size_t)hd * 64 + kh * (KQ / 32)) * (KVT / 2)), 0, KQ / 32 * KVT, 0x00020000);
  unsigned ok1 = lane * 16, ok2 = hh ? 1600 : 1024 + r * 8, ov = r > 20 ? 1u << 30 : 1280 + (hh * 21 + r) * 16;
  const __amdgpu_buffer_rsrc_t ro = __builtin_amdgcn_make_buffer_rsrc(O, 0, S_ * DM * 2, 0x00020000);
  unsigned oo[3];
#pragma unroll
  for (int i = 0; i < 3; ++i) {
    const int row = q0 + kh * 32 + r, k = hd * BD + 4 * hh + 8 * i;
    oo[i] = i == 2 && hh ? 1u << 30 : (unsigned)(((((row >> 4) * 20 + (k >> 5)) * 64 + ((k & 31) >> 3) * 16 + (row & 15)) * 8 + (k & 7)) * 2);
  }
  f32x16 o[NT];
  float m[NT];
  const f32x16 Z16 = {0.f, 0.f, 0.f, 0.f, 0.f, 0.f, 0.f, 0.f, 0.f, 0.f, 0.f, 0.f, 0.f, 0.f, 0.f, 0.f};
  int odd = 0xDDDD;
  asm volatile("" : "+v"(odd));
  struct KvT { u32x4 k1, v0, v1; u32x2 k2; };
  auto fetch_k = [&](KvT& x, int t) {
    x.k1 = __builtin_amdgcn_raw_buffer_load_b128(rk, ok1, imin(t, KQ / 32 - 1) * KVT, 0); x.k2 = __builtin_amdgcn_raw_buffer_load_b64(rk, ok2, imin(t, KQ / 32 - 1) * KVT, 0);
    __builtin_amdgcn_sched_barrier(0);
  };
  auto fetch_v = [&](KvT& x, int t) {
    x.v0 = __builtin_amdgcn_raw_buffer_load_b128(rk, ov, imin(t, KQ / 32 - 1) * KVT, 0); x.v1 = __builtin_amdgcn_raw_buffer_load_b128(rk, ov + 672, imin(t, KQ / 32 - 1) * KVT, 0);
    __builtin_amdgcn_sched_barrier(0);
  };
  auto fetch = [&](int t) { KvT x; __builtin_amdgcn_sched_barrier(0); fetch_k(x, t); fetch_v(x, t); return x; };
  auto qk1 = [&](const KvT& x, int qt) { return __builtin_amdgcn_mfma_f32_32x32x16_bf16(as8(x.k1), qa[qt], Z16, 0, 0, 0); };
  auto qk2 = [&](const KvT& x, int qt, f32x16 c) { return __builtin_amdgcn_mfma_f32_32x32x8bf16_1k(__builtin_bit_cast(v4i16_t, x.k2), __builtin_bit_cast(v4i16_t, qb2[qt]), c, 0, 0, 0); };
  auto pv = [&](const u32x4 v, const f32x16 p, int hi, f32x16 c) {
    const f32x8 ph = hi ? __builtin_shufflevector(p, p, 8, 9, 10, 11, 12, 13, 14, 15) : __builtin_shufflevector(p, p, 0, 1, 2, 3, 4, 5, 6, 7);
    return __builtin_amdgcn_smfmac_f32_32x32x32_bf16(__builtin_bit_cast(hbf16x8, v), __builtin_bit_cast(hbf16x16, ph), c, odd, 0, 0);
  };
  auto slow = [&]() {
    asm volatile("" : "+v"(ok1), "+v"(ok2), "+v"(ov) :: "memory");
#pragma unroll
    for (int qt = 0; qt < NT; ++qt) o[qt] = Z16;
#pragma unroll 1
    for (int t = 0; t < KQ / 32; ++t) {
      const KvT x = fetch(t);
#pragma unroll
      for (int qt = 0; qt < NT; ++qt) {
        f32x16 sa = qk2(x, qt, qk1(x, qt));
        float mx = fmx(fmx(sa[0], sa[1]), sa[2]);
#pragma unroll
        for (int i = 3; i < 15; i += 2) mx = fmx(fmx(mx, sa[i]), sa[i + 1]);
        mx = fmx(mx, sa[15]);
        if (t == 0 || !wave_all(mx <= ATT_THR)) {
          mx = x32mx(mx);
          if (t) mx = fmx(mx, 0.f);
          const float mn = m[qt] + mx;
          const unsigned h1 = f2bf(mn); const float r1 = mn - bf2f(h1);
          const unsigned h2 = f2bf(r1); const float r2 = r1 - bf2f(h2);
          const unsigned h3 = f2bf(r2);
          const float me = (bf2f(h1) + bf2f(h2)) + bf2f(h3);
          const float dm = me - m[qt];
          m[qt] = me;
          if (hh) qb2[qt] = (u32x2){(h1 | (h2 << 16)) ^ 0x80008000u, h3 ^ 0x8000u};
          const float alpha = t ? __builtin_amdgcn_exp2f(-dm) : 0.f;
#pragma unroll
          for (int i = 0; i < 16; ++i) { o[qt][i] *= alpha; sa[i] -= dm; }
        }
#pragma unroll
        for (int i = 0; i < 16; ++i) sa[i] = __builtin_amdgcn_exp2f(sa[i]);
        asm volatile("" : "+v"(sa));
        o[qt] = pv(x.v1, sa, 1, pv(x.v0, sa, 0, o[qt]));
        __builtin_amdgcn_sched_barrier(0);
      }
    }
  };
  f32x16 sc[2];
  auto phase = [&](int qt, const u32x4 vp, const KvT& xk, const u32x4 v0, auto mid) {
    const int u = qt & 1;
    auto ex = [&](int i0, int i1) {
#pragma unroll
      for (int i = i0; i < i1; ++i) sc[u][i] = __builtin_amdgcn_exp2f(sc[u][i]);
      asm volatile("" : "+v"(sc[u]));
      __builtin_amdgcn_sched_barrier(0);
    };
    o[(qt + NT - 1) % NT] = pv(vp, sc[1 - u], 1, o[(qt + NT - 1) % NT]);
    __builtin_amdgcn_sched_barrier(0);
    ex(0, 3);
    sc[1 - u] = qk1(xk, (qt + 1) % NT);
    __builtin_amdgcn_sched_barrier(0);
    ex(3, 6);
    sc[1 - u] = qk2(xk, (qt + 1) % NT, sc[1 - u]);
    __builtin_amdgcn_sched_barrier(0);
    ex(6, 10);
    o[qt] = pv(v0, sc[u], 0, o[qt]);
    __builtin_amdgcn_sched_barrier(0);
    mid();
    ex(10, 16);
  };
  static_assert(NT == 4, "projection");
  const int rb = (bid >> 5) * 8 + (bid & 7), cb = (bid >> 3) & 3, g = lane >> 4, c = lane & 15;
  const __amdgpu_buffer_rsrc_t rw = __builtin_amdgcn_make_buffer_rsrc(ws + OFF_WOBT + (size_t)cb * 204800, 0, out ? 204800 : 0, 0x00020000);
  const __amdgpu_buffer_rsrc_t rx = __builtin_amdgcn_make_buffer_rsrc(ws + OFF_XA + (size_t)rb * 40960, 0, out ? 40960 : 0, 0x00020000);
  bf16x8 W[20];
  u32x2 xo[3];
  auto w_get = [&](int t) {
    if (t >= 2 && t < 12) {
#pragma unroll
      for (int i = 0; i < 2; ++i) W[t < 2 || t >= 12 ? 0 : 2 * t - 4 + i] = as8(__builtin_amdgcn_raw_buffer_load_b128(rw, ok1 + i * 1024, (wid * 20 + 2 * t - 4) * 1024, 0));
    }
    if (t >= 4 && t < 9) {
      unsigned keep;
      asm volatile("s_mov_b32 %0, m0\n\ts_mov_b32 m0, %4\n\ts_nop 0\n\tbuffer_load_dwordx4 %1, %2, %3 offen lds\n\ts_mov_b32 m0, %0"
                   : "=&s"(keep) : "v"(ok1), "s"(rw), "s"((160 + wid * 5 + t - 4) * 1024), "s"((unsigned)(uintptr_t)(LAS char*)wl + (wid * 5 + t - 4) * 1024) : "memory");
    }
    if (t == 12) {
#pragma unroll
      for (int i = 0; i < 3; ++i) {
        const int col = cb * 160 + (i < 2 ? wid : 8 + ((wid >> 1) & 1)) * 16 + 4 * g, tt = (i ^ wid) & 1;
        xo[i] = __builtin_amdgcn_raw_buffer_load_b64(rx, (((tt * 20 + (col >> 5)) * 64 + ((col & 31) >> 3) * 16 + c) * 8 + (col & 7)) * 2, 0, 0);
      }
    }
    __builtin_amdgcn_sched_barrier(0);
  };
  if (ATT_P1) __builtin_amdgcn_s_setprio(3);
  {
    KvT xa = fetch(0), xb;
    fetch_k(xb, 1);
    xb.v1 = (u32x4){0u, 0u, 0u, 0u};
    sc[1] = Z16;
    asm volatile("" : "+v"(sc[1]), "+v"(xb.v1), "+v"(oo[0]), "+v"(oo[1]), "+v"(oo[2]));
#pragma unroll
    for (int qt = 0; qt < NT; ++qt) { m[qt] = 0.f; o[qt] = Z16; asm volatile("" : "+v"(o[qt])); }
    sc[0] = qk2(xa, 0, qk1(xa, 0));
    auto step = [&](KvT& x, KvT& y, int t) {
#pragma unroll
      for (int qt = 0; qt < NT; ++qt)
        phase(qt, qt ? x.v1 : y.v1, qt < NT - 1 ? x : y, x.v0, [&]() {
          if (qt == 0 && t + 1 < KQ / 32) fetch_v(y, t + 1);
          if (qt == NT - 2 && t + 2 < KQ / 32) fetch_k(x, t + 2);
          if (qt == NT - 1) w_get(t);
        });
    };
#pragma unroll
    for (int t = 0; t < KQ / 32; t += 2) {
      if (t == ATT_P1 * 4 / NT) __builtin_amdgcn_s_setprio(2);
      if (t == ATT_P2 * 4 / NT) __builtin_amdgcn_s_setprio(1);
      if (t == ATT_P3 * 4 / NT) __builtin_amdgcn_s_setprio(0);
      step(xa, xb, t);
      step(xb, xa, t + 1);
    }
    o[NT - 1] = pv(xb.v1, sc[1], 1, o[NT - 1]);
    float big = 0.f;
#pragma unroll
    for (int qt = 0; qt < NT; ++qt) {
      big = fmx(big, hh ? (o[qt][8] >= 1.0f / ATT_LMAX ? o[qt][8] * (3.0e38f / ATT_LMAX) : INFINITY) : __builtin_fabsf(o[qt][8]));
#pragma unroll
      for (int i = 0; i < 12; ++i) if (i != 8) big = fmx(big, __builtin_fabsf(o[qt][i]));
    }
    if (__builtin_expect(!wave_all(big <= 3.0e38f), 0)) slow();
  }
#pragma unroll
  for (int qt = 1; qt < NT; ++qt) {
    f32x4 (*mo)[64] = mo_[(qt + kh) % NT * ATT_NQ + wid % ATT_NQ][qt - 1];
#pragma unroll
    for (int k = 0; k < 3; ++k) mo[k][lane] = (f32x4){o[qt][4 * k], k == 2 && hh ? m[qt] : o[qt][4 * k + 1], o[qt][4 * k + 2], o[qt][4 * k + 3]};
  }
  asm volatile("s_waitcnt lgkmcnt(0)\n\ts_barrier" ::: "memory");
  {
    f32x2 lm[NT - 1];
    f32x4 u[NT - 1][3];
#pragma unroll
    for (int i = 0; i < NT - 1; ++i) {
      lm[i] = *(const f32x2*)&mo_[wid][i][2][lane | 32];
#pragma unroll
      for (int k = 0; k < 3; ++k) u[i][k] = mo_[wid][i][k][lane];
    }
    float mn = m[0];
#pragma unroll
    for (int i = 0; i < NT - 1; ++i) mn = fmx(mn, lm[i][1]);
    float a0 = __builtin_amdgcn_exp2f(m[0] - mn), a[NT - 1];
    float l = u2f(__builtin_amdgcn_permlane32_swap(f2u(o[0][8]), f2u(o[0][8]), false, false)[1]) * a0;
#pragma unroll
    for (int i = 0; i < NT - 1; ++i) { a[i] = __builtin_amdgcn_exp2f(lm[i][1] - mn); l += lm[i][0] * a[i]; }
    const float inv = __builtin_amdgcn_rcpf(l);
    a0 *= inv;
#pragma unroll
    for (int i = 0; i < NT - 1; ++i) a[i] *= inv;
#pragma unroll
    for (int k = 0; k < 3; ++k) {
      f32x4 v = (f32x4){o[0][4 * k], o[0][4 * k + 1], o[0][4 * k + 2], o[0][4 * k + 3]} * a0;
#pragma unroll
      for (int i = 0; i < NT - 1; ++i) v += u[i][k] * a[i];
      __builtin_amdgcn_raw_buffer_store_b64((u32x2){pack2(v[0], v[1]), pack2(v[2], v[3])}, ro, oo[k], 0, 16);
    }
  }
  if (!out) return;
  asm volatile("s_waitcnt vmcnt(0)\n\ts_barrier" ::: "memory");
  GAS unsigned* const cnt = (GAS unsigned*)(ws + OFF_FIN) + (bid >> 5) * 512;
  if (wid == 0) {
    if (lane < 8) __hip_atomic_fetch_add(cnt + lane * 64, 1u, RLX_AGENT);
  }
  bf16x8 W2[20];
  const __amdgpu_buffer_rsrc_t rf = __builtin_amdgcn_make_buffer_rsrc(out + (size_t)rb * 32 * DM, 0, 32 * DM * 2, 0x00020000);
  unsigned xa0 = (wid & 1) * 20480 + lane * 16, xa1 = 20480 - (wid & 1) * 20480 + lane * 16;
#pragma unroll
  for (int i = 0; i < 3; ++i) oo[i] = (unsigned)(((((i ^ wid) & 1) * 16 + c) * DM + cb * 160 + (i < 2 ? wid : 8 + ((wid >> 1) & 1)) * 16 + 4 * g) * 2);
  asm volatile("" : "+v"(xa0), "+v"(xa1), "+v"(oo[0]), "+v"(oo[1]), "+v"(oo[2]));
#pragma unroll
  for (int ks = 0; ks < 20; ++ks) W2[ks] = *(const bf16x8*)(wl + (((wid >> 1) & 1) * 20 + ks) * 1024 + lane * 16);
  __builtin_amdgcn_sched_barrier(0);
#pragma unroll
  for (int ks = 0; ks < 20; ks += 4) asm volatile("" : "+v"(W2[ks]), "+v"(W2[ks + 1]), "+v"(W2[ks + 2]), "+v"(W2[ks + 3]));
  if (wid == 0) {
    __builtin_amdgcn_s_sleep(FIN_D);
    for (unsigned spins = 0; __hip_atomic_load(cnt + (bid & 7) * 64, RLX_AGENT) < 32u && spins < SPIN_CAP; ++spins) __builtin_amdgcn_s_sleep(FIN_SL);
  }
  asm volatile("s_barrier" ::: "memory");
  x_fetch_n<5>(O + (size_t)rb * 32 * DM, (unsigned)(uintptr_t)(LAS char*)ob - L_XN, 0, wid * 5, ok1);
  asm volatile("s_waitcnt vmcnt(0)\n\ts_barrier" ::: "memory");
  auto fin = [&](auto N) {
    f32x4 acc[3] = {{0.f, 0.f, 0.f, 0.f}, {0.f, 0.f, 0.f, 0.f}, {0.f, 0.f, 0.f, 0.f}};
    bf16x8 x[20][2];
#pragma unroll
    for (int ks = -4; ks < 20; ++ks) {
      if (ks + 4 < 20) { x[ks + 4][0] = *(const bf16x8*)(ob + xa0 + (ks + 4) * 1024); x[ks + 4][1] = *(const bf16x8*)(ob + xa1 + (ks + 4) * 1024); }
      if (ks >= 0) {
        acc[0] = mfma32(W[ks < 0 ? 0 : ks], x[ks < 0 ? 0 : ks][0], acc[0]); acc[1] = mfma32(W[ks < 0 ? 0 : ks], x[ks < 0 ? 0 : ks][1], acc[1]);
        if (N == 3) acc[2] = mfma32(W2[ks < 0 ? 0 : ks], x[ks < 0 ? 0 : ks][0], acc[2]);
      }
      __builtin_amdgcn_sched_barrier(0);
    }
#pragma unroll
    for (int i = 0; i < N; ++i) {
      const unsigned r01 = pack2(acc[i][0], acc[i][1]), r23 = pack2(acc[i][2], acc[i][3]);
      const unsigned p01 = pack2(u2f(xo[i][0] << 16) + u2f(r01 << 16), u2f(xo[i][0] & 0xffff0000u) + u2f(r01 & 0xffff0000u));
      const unsigned p23 = pack2(u2f(xo[i][1] << 16) + u2f(r23 << 16), u2f(xo[i][1] & 0xffff0000u) + u2f(r23 & 0xffff0000u));
      __builtin_amdgcn_raw_buffer_store_b64((u32x2){p01, p23}, rf, oo[i], 0, 0);
    }
  };
  if (wid >= 4) fin(std::integral_constant<int, 3>{}); else fin(std::integral_constant<int, 2>{});
}
#if defined(__HIP_DEVICE_COMPILE__)
#pragma clang attribute pop
#endif

extern "C" void kernel_launch(void* const* d_in, const int* in_sizes, int n_in, void* d_out, int out_size, void* d_ws,
                              size_t ws_size, hipStream_t stream) {
  const u16* A_x = (const u16*)d_in[0];
  const u16* A_mem0 = (const u16*)d_in[1];
  const u16* A_Wq = (const u16*)d_in[2];
  const u16* A_Wk = (const u16*)d_in[3];
  const u16* A_Wv = (const u16*)d_in[4];
  const u16* A_Wo = (const u16*)d_in[5];
  const u16* B_wqkv = (const u16*)d_in[8];
  const u16* B_wo = (const u16*)d_in[9];
  char* ws = (char*)(((uintptr_t)d_ws + ((size_t)WS_OFF << 20) + WS_AL - 1) & ~(uintptr_t)(WS_AL - 1));
  u16* xa = (u16*)(ws + OFF_XA);

  prologue_kernel<<<256, 640, 0, stream>>>(ProArgs{A_x, A_Wq, A_Wk, A_Wv, A_Wo, B_wqkv, B_wo, ws});
  for (int layer = 0; layer < 2; ++layer) {
    rec_kernel<<<256, 512, 0, stream>>>(RecArgs{A_mem0, A_Wq, A_Wk, (const u16*)(ws + OFF_XB), xa, ws, (unsigned)layer, 0u});
    attb_kernel<<<BH * 8, 512, 0, stream>>>(ws, layer ? (u16*)d_out : nullptr);
  }
}
```

```cpp
#include <hip/hip_runtime.h>
#include <hip/hip_bf16.h>
#include <stdint.h>
#include <type_traits>

typedef unsigned short u16;
typedef unsigned long long u64;
typedef __attribute__((ext_vector_type(8))) short bf16x8;
typedef __attribute__((ext_vector_type(16))) short bf16x16;
typedef __attribute__((ext_vector_type(4))) float f32x4;
typedef __attribute__((ext_vector_type(2))) float f32x2;
typedef __attribute__((ext_vector_type(16))) float f32x16;
typedef __attribute__((ext_vector_type(8))) float f32x8;
typedef __attribute__((ext_vector_type(4))) unsigned u32x4;
typedef __attribute__((ext_vector_type(2))) unsigned u32x2;
typedef __attribute__((ext_vector_type(3))) unsigned u32x3;
typedef __attribute__((ext_vector_type(8))) unsigned u32x8;
typedef __attribute__((ext_vector_type(16))) unsigned u32x16;
typedef __attribute__((ext_vector_type(32))) unsigned u32x32;
#define GAS __attribute__((address_space(1)))
#define LAS __attribute__((address_space(3)))
typedef GAS unsigned long long gu64;
#define BAR() do { __builtin_amdgcn_s_waitcnt(0xC07F); asm volatile("s_waitcnt lgkmcnt(0)\n\ts_barrier" ::: "memory"); } while (0)

constexpr int S_ = 2048, DM = 640;
constexpr int NH = 32, DH = 192, HD = NH * DH;
constexpr int MEM = 8, SEG = 32, NSEG = S_ / SEG;
constexpr int BH = 32, BD = 20;

constexpr size_t al256(size_t x) { return (x + 255) & ~(size_t)255; }
constexpr int LAG3 = 2, LAG4 = 3, LAG5 = 6;
#define SR_ 8
constexpr int SR = SR_;
constexpr unsigned SCT_STEP = 131072, OFF_SCM = SR * SCT_STEP, SCM_STEP = 32768;
constexpr int ACC_NB = 192;
constexpr size_t ACC_STEP = 65536;
constexpr unsigned ACC_TS = ACC_NB * 256;
constexpr size_t ACC_LAYER = SR * ACC_STEP;
constexpr size_t OFF_ACC = 3 * 524288;
static_assert(SR == 8, "regions");
__device__ __forceinline__ unsigned ts_piece(int i) { return ACC_TS + (unsigned)(i < 128 ? (i >> 1) * 256 + (i & 1) * 16 : 128 + (i & 1) * 8192); }
constexpr size_t OFF_LNX = OFF_SCM + SR * SCM_STEP;
constexpr size_t OFF_RDY = OFF_LNX + 65536;
constexpr size_t OFF_FIN = OFF_RDY + 2048;
constexpr size_t PG = 2097152;
#define RING_ 4
constexpr int RING = RING_;
constexpr size_t TS_SLOT = (size_t)NH * 8 * 2560, PT_SLOT = (size_t)NH * 2560, OP_SLOT = (size_t)256 * 2560;
__device__ __forceinline__ unsigned ts_tok(int h, int j) { return (unsigned)((h & 15) * 32768 + (j >> 2) * 16384 + (j & 3) * 2048); }
__device__ __forceinline__ unsigned ts_mem(int h, int j) { return (unsigned)(524288 + ((h & 15) >> 1) * 16384 + (h & 1) * 4096 + j * 512); }
__device__ __forceinline__ unsigned pt_tok(int h) { return (unsigned)(((h & 15) >> 2) * 16384 + (h & 3) * 2048); }
__device__ __forceinline__ unsigned pt_mem(int h) { return (unsigned)(65536 + (h & 15) * 512); }
constexpr size_t OFF_X = 2 * PG + TS_SLOT + 3 * PT_SLOT, X_BYTES = 7 * OP_SLOT + 4 * PT_SLOT;
constexpr size_t OFF_OP = OFF_X + TS_SLOT + 4 * PT_SLOT;
constexpr size_t COMM_BYTES = OFF_X + X_BYTES + RING * OP_SLOT;
__device__ __forceinline__ unsigned near_half(unsigned ck, unsigned o) { return o + ((ck >> (o >= 2 * PG ? 8 : 4)) & 8192u); }
__device__ __forceinline__ unsigned ts_slot(unsigned ck, int sl) {
  return near_half(ck, sl == NSEG - 1 ? (unsigned)OFF_X : ((sl & 3) == 3 ? (unsigned)(2 * PG) : (unsigned)PG + (unsigned)(sl & 3) * (unsigned)TS_SLOT));
}
__device__ __forceinline__ unsigned pt_slot(unsigned ck, int sl) {
  return near_half(ck, sl >= NSEG - 4 ? (unsigned)(OFF_X + TS_SLOT) + (unsigned)(sl - (NSEG - 4)) * (unsigned)PT_SLOT
                                      : ((sl & 3) == 0 ? (unsigned)(PG + 3 * TS_SLOT) : (unsigned)(2 * PG + TS_SLOT - PT_SLOT) + (unsigned)(sl & 3) * (unsigned)PT_SLOT));
}
__device__ __forceinline__ unsigned op_slot(int sl) { return (unsigned)OFF_OP + (unsigned)(sl >= NSEG - 6 ? sl - (NSEG - 6) : 6 + sl % RING) * (unsigned)OP_SLOT; }
static_assert(RING == 4 && OFF_OP <= 3 * PG && OFF_FIN + 16384 <= OFF_ACC && OFF_ACC + ACC_LAYER == PG && TS_SLOT % 16384 == 0 && PT_SLOT % 16384 == 0, "pages");
static_assert(TS_SLOT == OP_SLOT && RING >= 4 && NSEG % SR == 0 && SR >= 5 && X_BYTES % 4096 == 0 && X_BYTES <= 256 * 20 * 1024, "slots");
constexpr size_t OFF_WB = (COMM_BYTES + 16383) / 16384 * 16384, WB_BYTES = (size_t)256 * 200 * 1024;
__device__ __forceinline__ unsigned wb_off(int F) { const unsigned o = (unsigned)((F >> 3) * 16384 + (F & 7) * 1024); return o | ((((unsigned)OFF_WB + o) >> 8) & 8192u); }
constexpr size_t OFF_PHI = OFF_WB + WB_BYTES;
constexpr size_t OFF_BNRM = OFF_PHI + 1024;
constexpr size_t OFF_LAT = OFF_BNRM + 1024;
constexpr size_t OFF_XNRM = OFF_LAT + 2048;
constexpr size_t OFF_WQKVBT = OFF_XNRM + al256(S_ * 16);
constexpr size_t OFF_WOBT = OFF_WQKVBT + al256((size_t)3 * DM * DM * 2);
constexpr size_t OFF_XN = OFF_WOBT + al256((size_t)DM * DM * 2);
constexpr int QST = BD;
constexpr size_t OFF_XA = OFF_XN + al256((size_t)S_ * DM * 2);
constexpr size_t OFF_XB = OFF_XA + al256((size_t)S_ * DM * 2);
constexpr size_t OFF_QB = OFF_XB + al256((size_t)S_ * DM * 2);
constexpr int KVT = 2624;
constexpr size_t OFF_KV = OFF_QB + al256((size_t)BH * S_ * QST * 2);
constexpr size_t OFF_OB = OFF_KV + al256((size_t)BH * 64 * KVT);
constexpr size_t OFF_DBG = OFF_OB + al256((size_t)S_ * DM * 2);
constexpr size_t WS_TOTAL = OFF_DBG + (size_t)2 * 64 * 8 * DM * 2;
#define WS_AL 2097152
#define WS_OFF 0
static_assert(WS_TOTAL + WS_AL + ((size_t)WS_OFF << 20) <= ((size_t)256 << 20), "workspace budget");

#if defined(__HIP_DEVICE_COMPILE__)
#pragma clang attribute push(__attribute__((target("no-packed-fp32-ops"))), apply_to = function)
#endif
__device__ __forceinline__ float u2f(unsigned v) { return __builtin_bit_cast(float, v); }
__device__ __forceinline__ unsigned f2u(float v) { return __builtin_bit_cast(unsigned, v); }
__device__ __forceinline__ float bf2f(unsigned v) { return __builtin_bit_cast(float, v << 16); }
__device__ __forceinline__ unsigned f2bf(float f) { __bf16 b = (__bf16)f; return (unsigned)__builtin_bit_cast(u16, b); }
__device__ __forceinline__ float rbf(float f) { return bf2f(f2bf(f)); }
typedef __attribute__((ext_vector_type(2))) __bf16 hbf16x2;
__device__ __forceinline__ unsigned pack2(float lo, float hi) { f32x2 v = {lo, hi}; hbf16x2 b = __builtin_convertvector(v, hbf16x2); return __builtin_bit_cast(unsigned, b); }
__device__ __forceinline__ f32x4 mfma32(bf16x8 a, bf16x8 b, f32x4 c) { return __builtin_amdgcn_mfma_f32_16x16x32_bf16(a, b, c, 0, 0, 0); }
typedef __attribute__((ext_vector_type(8))) __bf16 hbf16x8;
typedef __attribute__((ext_vector_type(16))) __bf16 hbf16x16;
__device__ __forceinline__ f32x4 smfma64(bf16x8 a, bf16x16 b, f32x4 c, int idx) {
  return __builtin_amdgcn_smfmac_f32_16x16x64_bf16(__builtin_bit_cast(hbf16x8, a), __builtin_bit_cast(hbf16x16, b), c, idx, 0, 0);
}
__device__ __forceinline__ bf16x16 cat16(bf16x8 a, bf16x8 b) { return __builtin_shufflevector(a, b, 0, 1, 2, 3, 4, 5, 6, 7, 8, 9, 10, 11, 12, 13, 14, 15); }
__device__ __forceinline__ bf16x8 half8(bf16x16 v, int hi) { return hi ? __builtin_shufflevector(v, v, 8, 9, 10, 11, 12, 13, 14, 15) : __builtin_shufflevector(v, v, 0, 1, 2, 3, 4, 5, 6, 7); }
__device__ __forceinline__ bf16x8 mk8(unsigned a, unsigned b, unsigned c, unsigned d) { u32x4 v = {a, b, c, d}; return __builtin_bit_cast(bf16x8, v); }
__device__ __forceinline__ unsigned dpp_ror8(unsigned v) { return (unsigned)__builtin_amdgcn_mov_dpp((int)v, 0x128, 0xf, 0xf, true); }
__device__ __forceinline__ float fmx(float a, float b) { return __builtin_elementwise_maximum(a, b); }

__device__ __forceinline__ int imax(int a, int b) { return a > b ? a : b; }
__device__ __forceinline__ int imin(int a, int b) { return a < b ? a : b; }
__device__ __forceinline__ unsigned umin(unsigned a, unsigned b) { return a < b ? a : b; }
__device__ __forceinline__ unsigned umax(unsigned a, unsigned b) { return a > b ? a : b; }
__device__ __forceinline__ bool wave_all(bool p) { return __builtin_amdgcn_ballot_w64(!p) == 0ull; }
__device__ __forceinline__ float x16mx(float x) { unsigned u = __builtin_bit_cast(unsigned, x); auto r = __builtin_amdgcn_permlane16_swap(u, u, false, false); return fmx(u2f(r[0]), u2f(r[1])); }
__device__ __forceinline__ float x32mx(float x) { unsigned u = __builtin_bit_cast(unsigned, x); auto r = __builtin_amdgcn_permlane32_swap(u, u, false, false); return fmx(u2f(r[0]), u2f(r[1])); }
__device__ __forceinline__ float x16sum(float x) { unsigned u = __builtin_bit_cast(unsigned, x); auto r = __builtin_amdgcn_permlane16_swap(u, u, false, false); return u2f(r[0]) + u2f(r[1]); }
__device__ __forceinline__ float x32sum(float x) { unsigned u = __builtin_bit_cast(unsigned, x); auto r = __builtin_amdgcn_permlane32_swap(u, u, false, false); return u2f(r[0]) + u2f(r[1]); }
template <int CTRL> __device__ __forceinline__ float dppf(float v) { return u2f((unsigned)__builtin_amdgcn_mov_dpp((int)f2u(v), CTRL, 0xf, 0xf, true)); }
#define OWN_PF 1
template <int BYTES> __device__ __forceinline__ void own_code(int tid, unsigned* sink) {
  if (!OWN_PF || __builtin_amdgcn_readfirstlane(tid >> 6) * 4096 >= BYTES) return;
  const char* pc; unsigned keep;
  asm volatile("s_getpc_b64 %0" : "=s"(pc));
  asm volatile("s_mov_b32 %0, m0\n\ts_mov_b32 m0, %3\n\ts_nop 0\n\tglobal_load_lds_dword %1, %2\n\ts_mov_b32 m0, %0"
               : "=&s"(keep) : "v"(tid * 64 < BYTES ? tid * 64 : 0), "s"(pc), "s"((unsigned)(uintptr_t)(LAS unsigned*)sink) : "memory");
}
constexpr int DPP_XOR1 = 0xB1, DPP_XOR2 = 0x4E, DPP_ROR4 = 0x124, DPP_ROR8 = 0x128;
#if defined(__HIP_DEVICE_COMPILE__)
#pragma clang attribute pop
#endif

typedef short v4i16_t __attribute__((ext_vector_type(4)));
__device__ __forceinline__ bf16x8 tr_frag(const char* lo_, const char* hi_) {
  const v4i16_t lo = __builtin_amdgcn_ds_read_tr16_b64_v4i16((LAS v4i16_t*)lo_), hi = __builtin_amdgcn_ds_read_tr16_b64_v4i16((LAS v4i16_t*)hi_);
  return (bf16x8){lo[0], lo[1], lo[2], lo[3], hi[0], hi[1], hi[2], hi[3]};
}
__device__ __forceinline__ int rec_wg(int gp, int m) { return (((gp & 1) * 16 + m) << 3) | (gp >> 1); }
__device__ __forceinline__ float wsum(float v) {
  v += dppf<DPP_XOR1>(v); v += dppf<DPP_XOR2>(v); v += dppf<DPP_ROR4>(v); v += dppf<DPP_ROR8>(v);
  return x32sum(x16sum(v));
}
__device__ __forceinline__ float ln_row(int lane, u32x4 (&xo)[2]) {
  const bool two = lane < 16;
  float v[16];
#pragma unroll
  for (int hf = 0; hf < 2; ++hf)
#pragma unroll
    for (int i = 0; i < 4; ++i) { const unsigned u = xo[hf][i]; v[hf * 8 + 2 * i] = bf2f(u & 0xffff); v[hf * 8 + 2 * i + 1] = bf2f(u >> 16); }
  float s0 = 0.f, s1 = 0.f;
#pragma unroll
  for (int i = 0; i < 8; ++i) { s0 += v[i]; s1 += v[8 + i]; }
  const float mean = wsum(two ? s0 + s1 : s0) * (1.0f / DM);
  s0 = s1 = 0.f;
#pragma unroll
  for (int i = 0; i < 8; ++i) { const float d0 = v[i] - mean, d1 = v[8 + i] - mean; s0 += d0 * d0; s1 += d1 * d1; }
  const float var = rbf(wsum(two ? s0 + s1 : s0) * (1.0f / DM));
  const float mb = rbf(mean);
  const float rden = 1.0f / rbf(sqrtf(rbf(var + 1e-5f)));
  float n2[2] = {0.f, 0.f};
#pragma unroll
  for (int hf = 0; hf < 2; ++hf) {
    float y[8];
#pragma unroll
    for (int i = 0; i < 8; ++i) { y[i] = rbf(rbf(v[hf * 8 + i] - mb) * rden); n2[hf] += y[i] * y[i]; }
    xo[hf] = (u32x4){pack2(y[0], y[1]), pack2(y[2], y[3]), pack2(y[4], y[5]), pack2(y[6], y[7])};
  }
  return wsum(two ? n2[0] + n2[1] : n2[0]);
}

#define PROT(k)
#define WARM_CODE 1
#define B_AUX 16
#define X_AUX 16
#define T_AUX 16
#define ATT_CODE 15000
#define FIN_D 4
#define FIN_SL 2
#define WB_NEAR 1
#define PRB_N0 3
#define PRB_LOG(i, v)
#define PLD(p) __builtin_nontemporal_load((const u32x4*)(p))
struct ProArgs { const u16* x; const u16* wq; const u16* wk; const u16* wv; const u16* wo; const u16* wqkvb; const u16* wob; char* ws; };
__global__ __launch_bounds__(640, 3) void prologue_kernel(ProArgs a) {
  __shared__ __attribute__((aligned(16))) char img[192 * 320];
  __shared__ __attribute__((aligned(16))) char slots[10 * 6144];
  __shared__ float red[10][160];
  __shared__ unsigned latw[10];
  __shared__ u32x4 xst[2][640];
  const int b = blockIdx.x, tid = threadIdx.x, lane = tid & 63, v = __builtin_amdgcn_readfirstlane(tid >> 6), c = lane & 15, g = lane >> 4;
  const int h = (b & 7) * 4 + ((b >> 3) & 3), dh = (b >> 5) & 1, cq = b >> 6, ks = dh * 10 + v;
  char* const slot = slots + v * 6144;
  PROT(0);
  __shared__ unsigned sink[64];
  own_code<9600>(tid, sink);
  const unsigned zo = v < 8 ? (unsigned)(((b >> 6) * 512 + ((b & 7) * 2 + (v >> 2)) * 32 + ((b >> 3) & 7) * 4 + (v & 3)) * 1024) : (unsigned)(((v - 7) * 2048 + (b & 7) * 32 + ((b >> 3) & 7) * 4 + (b >> 6)) * 1024);
  __builtin_amdgcn_raw_buffer_store_b128((u32x4){0u, 0u, 0u, 0u}, __builtin_amdgcn_make_buffer_rsrc(a.ws, 0, (int)(3 * PG), 0x00020000), zo + lane * 16, 0, 16);
  __builtin_amdgcn_sched_barrier(0);
  u32x4 wa[2][6], wb[6], tr[4], xo[2];
  const int xrow = b * 8 + (v & 7);
  xo[0] = *(const u32x4*)(a.x + (size_t)xrow * DM + lane * 8);
  xo[1] = *(const u32x4*)(a.x + (size_t)xrow * DM + 512 + c * 8);
  const int job = imin(b + 256 * (9 - v), 800);
  const int jn = job < 600 ? job / 20 : (job % 200) / 20, jk = job % 20;
  {
    const u16* src = job < 600 ? a.wqkvb + (size_t)(32 * jk) * 3 * DM : a.wob + (size_t)(32 * jk) * DM;
#pragma unroll
    for (int i = 0; i < 4; ++i) tr[i] = *(const u32x4*)(src + (size_t)(i * 8 + (lane >> 3)) * (job < 600 ? 3 * DM : DM) + jn * 64 + (lane & 7) * 8);
  }
#pragma unroll
  for (int i = 0; i < 6; ++i) {
    const int idx = (v * 6 + i) * 64 + lane, row = idx / 20;
    wb[i] = PLD(a.wo + (size_t)(h * DH + row) * DM + cq * 160 + (idx - row * 20) * 8);
  }
#pragma unroll
  for (int i = 0; i < 6; ++i) {
    const int idx = i * 64 + lane, r = idx / 24;
#pragma unroll
    for (int u = 0; u < 2; ++u) wa[u][i] = PLD(a.wv + (size_t)(32 * ks + 8 * (r >> 2) + (r & 3) + 4 * u) * HD + h * DH + (idx - r * 24) * 8);
  }
  unsigned pf[2] = {0u, 0u};
  auto prefetch = [&]() {
#pragma unroll
    for (int i = 0; i < 2; ++i) pf[i] = *(const unsigned*)((i ? a.wq : a.wk) + ((size_t)(b * 10 + v) * 48 + imin(lane, 47)) * 32);
  };
  prefetch();
  __builtin_amdgcn_sched_barrier(0);
  unsigned lat = ~0u;
  auto probe = [&](int n0, int n) {
    const char* p = a.ws + zo;
#pragma unroll 1
    for (int i = n0; i < n0 + n; ++i) {
      u64 t0, t1; unsigned d;
      asm volatile("s_memtime %0\n\ts_waitcnt lgkmcnt(0)\n\ts_load_dword %2, %3, %4\n\ts_waitcnt lgkmcnt(0)\n\ts_memtime %1\n\ts_waitcnt lgkmcnt(0)"
                   : "=&s"(t0), "=&s"(t1), "=&s"(d) : "s"(p), "s"((i & 3) * 256 + (i >> 2) * 128) : "memory");
      PRB_LOG(i, (unsigned)t1 - (unsigned)t0);
      lat = umin(lat, (unsigned)t1 - (unsigned)t0);
    }
  };
  asm volatile("s_waitcnt vmcnt(26)" ::: "memory");
  probe(0, PRB_N0);
  latw[v] = lat;
  __builtin_amdgcn_sched_barrier(0);
  auto xput = [&](int k) {
    if (v < 8) { xst[k][lane * 8 + (v ^ (lane & 7))] = xo[0]; if (lane < 16) xst[k][(64 + lane) * 8 + (v ^ (lane & 7))] = xo[1]; }
  };
  xput(0);
  const float xn2 = u2f(__builtin_amdgcn_readfirstlane(f2u(ln_row(lane, xo))));
  xput(1);
#pragma unroll
  for (int i = 0; i < 4; ++i) *(u32x4*)(slot + (i * 8 + (lane >> 3)) * 144 + (lane & 7) * 16) = tr[i];
  __builtin_amdgcn_fence(__ATOMIC_RELEASE, "wavefront"); __builtin_amdgcn_wave_barrier();
  bf16x8 tf[4];
#pragma unroll
  for (int t = 0; t < 4; ++t) {
    const char* bp = slot + (8 * g + (c >> 2)) * 144 + t * 32 + (c & 3) * 8;
    tf[t] = tr_frag(bp, bp + 4 * 144);
  }
  __builtin_amdgcn_fence(__ATOMIC_RELEASE, "wavefront"); __builtin_amdgcn_wave_barrier();
  __builtin_amdgcn_sched_barrier(0);
#pragma unroll
  for (int i = 0; i < 6; ++i) {
    const int idx = (v * 6 + i) * 64 + lane, row = idx / 20;
    *(u32x4*)(img + row * 320 + (((idx - row * 20) ^ ((row >> 2) & 2)) * 16)) = wb[i];
  }
  bf16x8 A[2][6];
#pragma unroll
  for (int u = 0; u < 2; ++u) {
#pragma unroll
    for (int i = 0; i < 6; ++i) {
      const int idx = i * 64 + lane, r = idx / 24;
      *(u32x4*)(slot + r * 384 + (((idx - r * 24) ^ (r >> 1)) * 16)) = wa[u][i];
    }
    __builtin_amdgcn_fence(__ATOMIC_RELEASE, "wavefront"); __builtin_amdgcn_wave_barrier();
#pragma unroll
    for (int e = 0; e < 6; ++e) A[u][e] = *(const bf16x8*)(slot + c * 384 + (((4 * e + g) ^ (c >> 1)) * 16));
    __builtin_amdgcn_fence(__ATOMIC_RELEASE, "wavefront"); __builtin_amdgcn_wave_barrier();
  }
  BAR();
  PROT(1);
  asm volatile("" :: "v"(pf[0]), "v"(pf[1]));
  {
    const unsigned xf = (unsigned)((b >> 1) * 20480 + (tid >> 3) * 256 + (b & 1) * 128 + (tid & 7) * 16);
    const __amdgpu_buffer_rsrc_t rx = __builtin_amdgcn_make_buffer_rsrc(a.ws + OFF_XN, 0, (int)(OFF_QB - OFF_XN), 0x00020000);
    __builtin_amdgcn_raw_buffer_store_b128(xst[1][tid ^ ((tid >> 3) & 7)], rx, xf, 0, X_AUX);
    __builtin_amdgcn_raw_buffer_store_b128(xst[0][tid ^ ((tid >> 3) & 7)], rx, xf + (unsigned)(OFF_XB - OFF_XN), 0, X_AUX);
    if (v < 8 && lane < 4) ((float*)(a.ws + OFF_XNRM))[xrow * 4 + lane] = lane == 0 ? xn2 : 0.f;
  }
  if (job < 800) {
    const __amdgpu_buffer_rsrc_t rt = __builtin_amdgcn_make_buffer_rsrc(a.ws + (job < 600 ? OFF_WQKVBT : OFF_WOBT) + ((size_t)jn * 80 + jk) * 1024, 0, 81920, 0x00020000);
#pragma unroll
    for (int t = 0; t < 4; ++t) __builtin_amdgcn_raw_buffer_store_b128(__builtin_bit_cast(u32x4, tf[t]), rt, lane * 16 + t * 20480, 0, T_AUX);
  }
  const int gp = h >> 1, hh = h & 1;
  const __amdgpu_buffer_rsrc_t rwb = __builtin_amdgcn_make_buffer_rsrc(a.ws + OFF_WB, 0, (int)WB_BYTES, 0x00020000);
  const unsigned phi = WB_NEAR ? ((((unsigned)b >> 4) & 1u) ^ (unsigned)(latw[8] < latw[9]) ^ (WB_NEAR - 1)) << 13 : (unsigned)(b & 32) << 8;
  if (tid == 0) ((unsigned*)(a.ws + OFF_PHI))[b] = phi;
#pragma unroll
  for (int n = 0; n < 10; ++n) {
    const int off = n * 32 + 8 * (c & 3), T = cq * 10 + n;
    const char* bp = img + (8 * g + (c >> 2)) * 320 + (off ^ ((g & 1) << 5));
    f32x4 ac[2] = {{0.f, 0.f, 0.f, 0.f}, {0.f, 0.f, 0.f, 0.f}};
#pragma unroll
    for (int e = 0; e < 6; ++e) {
      const bf16x8 bf = tr_frag(bp + e * 32 * 320, bp + (e * 32 + 4) * 320);
      ac[0] = mfma32(A[0][e], bf, ac[0]); ac[1] = mfma32(A[1][e], bf, ac[1]);
    }
    const u32x4 o = {pack2(ac[0][0], ac[0][1]), pack2(ac[0][2], ac[0][3]), pack2(ac[1][0], ac[1][1]), pack2(ac[1][2], ac[1][3])};
    const unsigned oo = (T < 32 ? wb_off(rec_wg(gp, T & 15) * 100 + (hh * 3 + (T >> 4)) * 20 + ks) + (unsigned)(lane * 16)
                                : wb_off(rec_wg(gp, 2 * (T - 32) + (c >> 3)) * 100 + 40 + ks) + (unsigned)((g * 16 + hh * 8 + (c & 7)) * 16)) ^ phi;
    __builtin_amdgcn_raw_buffer_store_b128(o, rwb, oo, 0, B_AUX);
    float n2 = 0.f;
#pragma unroll
    for (int r = 0; r < 4; ++r) n2 += ac[0][r] * ac[0][r] + ac[1][r] * ac[1][r];
    n2 = x32sum(x16sum(n2));
    if (g == 0) red[v][n * 16 + c] = n2;
  }
  PROT(2);
  if (v >= 8 && b < 64 && lane == 0) ((unsigned*)(a.ws + OFF_LAT))[(v - 4) * 64 + b] = umin(lat, 60000u);
  if (WARM_CODE && v == 7) {
    const char* pc; int d0, d1;
    asm("s_getpc_b64 %0\n\ts_mov_b32 %1, _Z10rec_kernel7RecArgs@rel32@lo+4\n\ts_mov_b32 %2, _Z11attb_kernelPcPt@rel32@lo+12" : "=s"(pc), "=s"(d0), "=s"(d1));
    const char *c0 = pc + d0, *c1 = pc + d1 + ATT_CODE;
    const long o = ((long)b * 64 + lane) * 64;
    if (o < c1 - c0) asm volatile("" :: "v"(*(const volatile GAS unsigned*)(c0 + o)));
  }
  if (v == 9 && lane < 32) {
    const int q = b * 32 + lane;
    *(u32x4*)(a.ws + OFF_KV + (size_t)(q >> 2) * KVT + 1280 + (q & 3) * 336 + 320) = (u32x4){0x3f803f80u, 0x3f803f80u, 0x3f803f80u, 0x3f803f80u};
  }
  BAR();
  if (v == 1 && lane == 0) ((unsigned*)(a.ws + OFF_LAT))[b] = umin(umin(umin(latw[0], latw[4]) + umin(latw[1], latw[5]), 30000u) + umin(umin(latw[2], latw[6]) + umin(latw[3], latw[7]), 30000u), 60000u);
  if (v == 0) {
    float mx = 0.f;
#pragma unroll
    for (int k = 0; k < 3; ++k) {
      const int col = k * 64 + lane;
      float sm = 0.f;
#pragma unroll
      for (int i = 0; i < 10; ++i) sm += red[i][col < 160 ? col : 0];
      mx = fmx(mx, sm);
    }
    mx = fmx(mx, dppf<DPP_XOR1>(mx)); mx = fmx(mx, dppf<DPP_XOR2>(mx)); mx = fmx(mx, dppf<DPP_ROR4>(mx)); mx = fmx(mx, dppf<DPP_ROR8>(mx));
    mx = x32mx(x16mx(mx));
    if (lane == 0) ((float*)(a.ws + OFF_BNRM))[(cq * 32 + h) * 2 + dh] = mx;
  }
  PROT(3);
}

struct RecArgs {
  const u16* mem0; const u16* wq; const u16* wk; const u16* xold; u16* xnew; char* ws;
  unsigned layer; unsigned pad;
};
#define RLX_AGENT __ATOMIC_RELAXED, __HIP_MEMORY_SCOPE_AGENT
constexpr unsigned SPIN_CAP = 1u << 20;
#define TSTAMP(k, v)
#define ARRIVE(k)
#define ARRIVE_OUT()
#define ARRIVE_DECL()
#define LOGV(k, v)
#define LOGF(k, v)
#define LOGP(k)
#define LOGQ(k)
#define LOGF2(k)
#define LOGD(k)
#define LOGT(k, v)
#define STAMP_DECL()
#define STAMP(k)
#define STAMPV(k, v)
#define STAMP_OUT()
#define REC_D1 520
#define SCH_V 10
#define SCH_VMIN 0
#define SCH_DMIN 4
#define SCH_PEN 170
#define SCH_VMIN2 (-4)
#define SCH_PEN2 40
#define SCH_GU 1
#define SCH_GD 2
#define SCH_UP 6
#define SCH_D0 196
#define SCH_FIRST 120
#define EXP_NODMA 0
#define WPF_N 5
#define WPF0 (63 - WPF_N)
#define D1_W1 (-10)
#define PADR 0x6666
#define PADT 0x000
#define CODE_PAD(n) asm volatile(".p2align 6\n.rept %0\ns_nop 0\n.endr" :: "n"(n))
#define ST_X 270
#define ST_ALL 4
#define CPF_S 61
#define FR_D 700
#define FR_PACE 6
#define END_E1 1300
#define END_E2 3400
#define END_E62 2800
#define END_E3 6800
#define END_E4 11400
#define EARLY_N 6
#define EARLY_D1 80
#define EARLY_V 10
#define EARLY_V1 18
#define F3 0
#define T4_P 1
#define P2_W 1
#define PS_SL 1
#define F2B 4
#define W2_P 1
#define SCH_W 1
#define SP_SL 0
#define D1_M 320
#define D1_MIN 290
#define D1_C 96
#define D1_G 2
#define END_E61 2600
#define D1_LATE 200
#define P2_G0 10
#define P2_G2 6
#define P2_G3 6
#define SCH_P 3
#define MMA_P 1
#define SP_P 0
#define F6 8
#define F7 10
#define LATE_V 8
#define QW 3
#define P2_G1 1
#define P1_G 0
#define F0A 3
#define EXACT 1
#define D1_P 3
#define VO_A0 9
#define VO_A2 10
#define VO_B0 3
#define VO_B1 0
#define VO_N2 7
#define VO_N3 10
#define UN_P 2
#define W1_SL 0
#define VO_A1 4
#define LAST_V 22
#define END_E59 3200
#define END_G2 600
#define END_G3 600
#define QR 4
#define WPF_S 128
#define END_SL 4
#define SCH_DR 182
#define SCH_DK 3
#define FW 8
#define PR_P 0
#define REL_AT 1
#define XO_T 4
#define PK_ADD 1
#define KP0 0
#define KP1 0
#define F4 12
#define F5 0
#define KP2 0
#define REC_RNG 70u
#define SCH_DRAIN 40
#define PL_X 0
#define FC(k)
#define PIN_V(x) asm volatile("" : "+v"(x))
#define PIN_S(x) asm volatile("" : "+s"(x))
#define VM_DRAIN() do { asm volatile("s_waitcnt vmcnt(0)" ::: "memory"); __builtin_amdgcn_s_waitcnt(0x0F70); } while (0)
#define SC1_LOAD(off) __builtin_amdgcn_raw_buffer_load_b128(rs, (off), 0, 16)
#define SC1_LOADS(off, soff) __builtin_amdgcn_raw_buffer_load_b128(rs, (off), (soff), 16)
#define SC1_STORE8S(v, off, soff) __builtin_amdgcn_raw_buffer_store_b64((v), rs, (off), (soff), 16)
#define SC1_LOAD8(off) __builtin_amdgcn_raw_buffer_load_b64(rs, (off), 0, 16)
#define SC1_STORE(v, off) __builtin_amdgcn_raw_buffer_store_b128((v), rs, (off), 0, 16)
#define SC1_STORE8(v, off) __builtin_amdgcn_raw_buffer_store_b64((v), rs, (off), 0, 16)
#if defined(__HIP_DEVICE_COMPILE__)
#pragma clang attribute push(__attribute__((target("no-packed-fp32-ops"))), apply_to = function)
#endif
__device__ __forceinline__ unsigned now32() { return (unsigned)__builtin_amdgcn_s_memrealtime(); }
__device__ __forceinline__ void wait_until(unsigned t) { for (;;) { const int d = (int)(t - now32()); if (d <= 0) break; if (d > 8) __builtin_amdgcn_s_sleep(1); } }
__device__ __forceinline__ unsigned cyc32() { return (unsigned)__builtin_amdgcn_s_memtime(); }
__device__ __forceinline__ void wait_cyc(unsigned t) { for (;;) { const int d = (int)(t - cyc32()); if (d <= 0) break; if (d > 200) __builtin_amdgcn_s_sleep(1); } }
#define WAIT_ASM(clk, far_) "1:\n\t" clk " vcc\n\ts_waitcnt lgkmcnt(0)\n\ts_sub_i32 %0, %1, vcc_lo\n\ts_cmp_lt_i32 %0, " far_ "\n\ts_cbranch_scc1 3f\n\ts_sleep 1\n\ts_branch 1b\n" \
                            "2:\n\t" clk " vcc\n\ts_waitcnt lgkmcnt(0)\n\ts_sub_i32 %0, %1, vcc_lo\n3:\n\ts_cmp_gt_i32 %0, 0\n\ts_cbranch_scc1 2b"
__device__ __forceinline__ int spin_until(unsigned t) { int d; asm volatile(WAIT_ASM("s_memrealtime", "9") : "=&s"(d) : "s"(t) : "vcc", "scc", "memory"); return d; }
__device__ __forceinline__ int spin_cyc(unsigned t) { int d; asm volatile(WAIT_ASM("s_memtime", "200") : "=&s"(d) : "s"(t) : "vcc", "scc", "memory"); return d; }
__device__ __forceinline__ void spin_exact(unsigned t) {
  if (!EXACT) { spin_cyc(t); return; }
  int d;
  asm volatile("1:\n\ts_memtime vcc\n\ts_waitcnt lgkmcnt(0)\n\ts_sub_i32 %0, %1, vcc_lo\n\ts_cmp_gt_i32 %0, 95\n\ts_cbranch_scc1 1b\n\t"
               "s_max_i32 %0, %0, 0\n\ts_andn2_b32 %0, %0, 3\n\ts_sub_i32 %0, 3f-2f, %0\n\ts_getpc_b64 vcc\n"
               "2:\n\ts_add_u32 vcc_lo, vcc_lo, %0\n\ts_addc_u32 vcc_hi, vcc_hi, 0\n\ts_setpc_b64 vcc\n\t.rept 24\n\ts_nop 0\n\t.endr\n3:"
               : "=&s"(d) : "s"(t) : "scc", "vcc", "memory");
}
template <int OFF, int SL> __device__ __forceinline__ void lds_spin(unsigned lds0, unsigned want) {
  unsigned v;
  asm volatile("1:\n\tds_read_b32 %0, %1 offset:%3\n\t.if %4\n\ts_sleep %4\n\t.endif\n\ts_waitcnt lgkmcnt(0)\n\tv_cmp_gt_u32 vcc, %2, %0\n\ts_cbranch_vccnz 1b"
               : "=&v"(v) : "v"(lds0), "s"(want), "n"(OFF), "n"(SL) : "vcc", "memory");
}
template <int CTRL> __device__ __forceinline__ int dppi(int v) { return __builtin_amdgcn_mov_dpp(v, CTRL, 0xf, 0xf, true); }
__device__ __forceinline__ int wave_imax(int l) {
  l = imax(l, dppi<DPP_XOR1>(l)); l = imax(l, dppi<DPP_XOR2>(l)); l = imax(l, dppi<DPP_ROR4>(l)); l = imax(l, dppi<DPP_ROR8>(l));
  int a = imax(__builtin_amdgcn_readlane(l, 0), __builtin_amdgcn_readlane(l, 16)), b = imax(__builtin_amdgcn_readlane(l, 32), __builtin_amdgcn_readlane(l, 48));
  PIN_S(a); PIN_S(b);
  return imax(a, b);
}
__device__ __forceinline__ void glds16(const void* gsrc, unsigned lds_dst) {
  unsigned keep;
  asm volatile("s_mov_b32 %0, m0\n\ts_mov_b32 m0, %2\n\ts_nop 0\n\tglobal_load_lds_dwordx4 %1, off\n\ts_mov_b32 m0, %0" : "=&s"(keep) : "v"(gsrc), "s"(lds_dst) : "memory");
}
__device__ __forceinline__ void glds16_sc1(const void* gsrc, unsigned lds_dst) {
  unsigned keep;
  asm volatile("s_mov_b32 %0, m0\n\ts_mov_b32 m0, %2\n\ts_nop 0\n\tglobal_load_lds_dwordx4 %1, off sc1\n\ts_mov_b32 m0, %0" : "=&s"(keep) : "v"(gsrc), "s"(lds_dst) : "memory");
}
__device__ __forceinline__ unsigned acc_off(int G, int row) {
  return (unsigned)(G < 128 ? ((G & 31) | ((G & 32) << 1) | ((G & 64) >> 1)) * 256 + row * 16 : (2 * G - 128 + (row >> 2)) * 256 + (row & 3) * 16);
}
__device__ __forceinline__ unsigned nz(unsigned v) { return v > 1u ? v : 1u; }
__device__ __forceinline__ unsigned pack2n(float lo, float hi) { return nz(pack2(lo, hi)); }
__device__ __forceinline__ unsigned min4(u32x4 v) { return umin(umin(v[0], v[1]), umin(v[2], v[3])); }
__device__ __forceinline__ bf16x8 as8(u32x4 v) { return __builtin_bit_cast(bf16x8, v); }

constexpr int NV = 7, NKQ = 2;
constexpr int L_RED1 = 0;
constexpr int L_BM = L_RED1 + 8 * 1536;
constexpr int L_STG = L_BM + 10240;
constexpr int L_ST = L_STG + 2048 + 2560;
constexpr int L_MISC = L_ST + 512;
constexpr int L_KX = L_MISC + 128;
constexpr int L_QX = L_KX + NKQ * 2048;
constexpr int L_XN = L_QX + NKQ * 2048;
constexpr int L_VOX = L_XN + 81920;
constexpr int L_VOM = L_VOX + NV * 5120;
constexpr int L_BYTES = L_VOM + NV * 1280;
constexpr int S_IMG = L_XN + 40960;
static_assert(S_IMG + 61440 <= L_VOM, "start-up image");
static_assert(L_BYTES <= 160 * 1024, "LDS budget");

__device__ __forceinline__ bf16x8 wqk_frag(const char* lds, int t, int ks, int lane) {
  const int c = lane & 15, g = lane >> 4, p = c & 3;
  const char* q = lds + S_IMG + (32 * ks + 8 * g + (c >> 2)) * 96 + 8 * p + (t == 0 ? 0 : (t == 1 ? 48 : (p < 2 ? 32 : 64)));
  const v4i16_t lo = __builtin_amdgcn_ds_read_tr16_b64_v4i16((LAS v4i16_t*)q), hi = __builtin_amdgcn_ds_read_tr16_b64_v4i16((LAS v4i16_t*)(q + 4 * 96));
  return (bf16x8){lo[0], lo[1], lo[2], lo[3], hi[0], hi[1], hi[2], hi[3]};
}
__device__ __forceinline__ bf16x8 sel_frag(int c, int base) {
  bf16x8 r;
#pragma unroll
  for (int i = 0; i < 8; ++i) r[i] = (c == base + i) ? (short)0x3f80 : (short)0;
  return r;
}
__device__ __forceinline__ bf16x8 qk_frag(const char* p4, const char* p2, const char* p2b) {
  f32x4 a0 = *(const f32x4*)(p4), a1 = *(const f32x4*)(p4 + 1792), a2 = *(const f32x4*)(p4 + 3584), a3 = *(const f32x4*)(p4 + 5376);
  f32x2 b0 = *(const f32x2*)(p2), b1 = *(const f32x2*)(p2 + 1792), b2 = *(const f32x2*)(p2b), b3 = *(const f32x2*)(p2b + 1792);
  f32x4 va = (a0 + a1) + (a2 + a3);
  f32x2 vb = (b0 + b1) + (b2 + b3);
  return mk8(pack2(va[0], va[1]), pack2(va[2], va[3]), pack2(vb[0], vb[1]), 0u);
}
__device__ __forceinline__ void x_fetch(const u16* src, unsigned lds0, int seg, int p, int lane) {
  if (EXP_NODMA) return;
  glds16_sc1(src + (size_t)p * 512 + lane * 8, lds0 + L_XN + (seg & 1) * 40960 + p * 1024);
}
template <int N> __device__ __forceinline__ void x_fetch_n(const u16* src, unsigned lds0, int seg, int p0, unsigned voff) {
  if (EXP_NODMA) return;
#pragma unroll
  for (int i = 0; i < N; i += 4) {
    unsigned keep;
    asm volatile("s_mov_b32 %0, m0\n\ts_mov_b32 m0, %3\n\ts_nop 0\n\tglobal_load_lds_dwordx4 %1, %2 sc1\n\t"
                 ".if %4 > 1\n\tglobal_load_lds_dwordx4 %1, %2 offset:1024 sc1\n\t.endif\n\t.if %4 > 2\n\tglobal_load_lds_dwordx4 %1, %2 offset:2048 sc1\n\t.endif\n\t"
                 ".if %4 > 3\n\tglobal_load_lds_dwordx4 %1, %2 offset:3072 sc1\n\t.endif\n\ts_mov_b32 m0, %0"
                 : "=&s"(keep) : "v"(voff), "s"(src + (size_t)(p0 + i) * 512), "s"(lds0 + L_XN + (seg & 1) * 40960 + (p0 + i) * 1024), "n"(N - i < 4 ? N - i : 4) : "memory");
  }
}
__device__ __forceinline__ int qkv_row(int b) { return (b & 7) / QR * (2 * QR) + (b >> 3) / (16 / QR); }
__device__ __forceinline__ int qkv_col(int b) { return (b & 7) % QR * (16 / QR) + (b >> 3) % (16 / QR); }
__device__ __forceinline__ const u16* x_src(const RecArgs& a, int seg, int b) {
  return seg < NSEG ? (const u16*)(a.ws + OFF_XN) + (size_t)seg * 32 * DM : a.xnew + (size_t)(qkv_row(b) + 16 * (seg - NSEG)) * 32 * DM;
}
template <int N>
__device__ __forceinline__ void img_fetch(const RecArgs& a, unsigned lds0, int h, int j, int part, int lane) {
#pragma unroll
  for (int i = 0; i < N; ++i) {
    const int n = part * N + i, idx = n * 64 + lane, row = idx / 6, pc = idx - row * 6;
    if (FR_PACE && N == 20) asm volatile("s_waitcnt vmcnt(%0)" :: "n"(FR_PACE) : "memory");
    glds16((pc < 3 ? a.wq : a.wk - 24) + (size_t)row * HD + h * DH + j * 24 + pc * 8, lds0 + S_IMG + n * 1024);
  }
}
constexpr int P2B = F4 + F5 + F6 + F7, PA = P2B + F2B;
static_assert(PA + 2 * F0A + F3 == 40, "fetch plan");
template <int OFF> __device__ __forceinline__ void lds_inc(unsigned lds0) {
  asm volatile("s_mov_b64 exec, 1\n\tds_add_u32 %0, %1 offset:%2\n\ts_mov_b64 exec, -1" :: "v"(lds0), "v"(1u), "n"(OFF) : "memory");
}
__device__ __forceinline__ void lds_arrive(unsigned* p, int lane) {
  asm volatile("s_waitcnt lgkmcnt(0)" ::: "memory");
  if (lane == 0) __hip_atomic_fetch_add(p, 1u, __ATOMIC_RELAXED, __HIP_MEMORY_SCOPE_WORKGROUP);
}
__device__ __forceinline__ void lds_wait(unsigned* p, unsigned want) {
  while (__hip_atomic_load(p, __ATOMIC_RELAXED, __HIP_MEMORY_SCOPE_WORKGROUP) < want) __builtin_amdgcn_s_sleep(1);
  asm volatile("" ::: "memory");
}
constexpr float KEXP = 0.07216878364870322f * 1.4426950408889634f;
#define FRESH_LANE() int lane = lane0; asm volatile("" : "+v"(lane)); const int c = lane & 15, g = lane >> 4; (void)c; (void)g

struct OutTask {
  u32x4 y[4]; u32x2 xo, out;
  bf16x8 sel;
  unsigned t5f, t5o;
  __device__ __forceinline__ void init(int b, int lane) {
    const int um = b / 10, uq = (b / 5) & 1, up = b % 5, tok = lane & 15, k = up < 4 ? (up >> 1) * 256 + um * 16 + (up & 1) * 8 : 512 + um * 8, q4 = ((lane >> 4) & 1) * 4;
    t5f = (unsigned)(((uq * 20 + (k >> 5)) * 64 + ((k & 31) >> 3) * 16 + tok) * 8 + q4);
    t5o = (unsigned)(((um * 2 + uq) * 16 + (lane >> 4)) * 1280 + up * 256 + tok * 16);
    if (b >= 160) { t5f = 0u; t5o = 0x7ffffff0u; }
    sel = sel_frag(tok, 0);
    PIN_V(sel);
  }
  static __device__ __forceinline__ bool ok(int sl, int b) { return sl >= 0 && b < 160; }
  __device__ __forceinline__ void load(const RecArgs& a, const __amdgpu_buffer_rsrc_t rs, int sl, int b, int part = 3) {
    const unsigned o = t5o | (sl < 0 ? 0x7ff00000u : 0u);
    sl = imax(sl, 0);
#pragma unroll
    for (int k = 0; k < 4; ++k) if (part & 1) y[k] = SC1_LOADS(o, op_slot(sl) + k * 5120);
    if (part & 2) xo = __builtin_bit_cast(u32x2, __hip_atomic_load((const gu64*)(a.xold + (size_t)sl * 32 * DM + t5f), RLX_AGENT));
  }
  __device__ __forceinline__ bool ready(int b) { return b >= 160 || wave_all(umin(umin(min4(y[0]), min4(y[1])), umin(min4(y[2]), min4(y[3]))) != 0u); }
  __device__ __forceinline__ void run(int lane) {
    f32x4 d = {0.f, 0.f, 0.f, 0.f};
#pragma unroll
    for (int k = 0; k < 4; ++k) d = mfma32(sel, as8(y[k]), d);
#pragma unroll
    for (int i = 0; i < 2; ++i) {
      const unsigned r = pack2(d[2 * i], d[2 * i + 1]), x = xo[i];
      out[i] = pack2(u2f(x << 16) + u2f(r << 16), u2f(x & 0xffff0000u) + u2f(r & 0xffff0000u));
    }
    PIN_V(out);
  }
  __device__ __forceinline__ void store(const RecArgs& a, int sl, int b, int lane) {
    __builtin_amdgcn_raw_buffer_store_b64(out, __builtin_amdgcn_make_buffer_rsrc(a.xnew, 0, ok(sl, b) ? S_ * DM * 2 : 0, 0x00020000), (t5f + (unsigned)sl * 32 * DM) * 2u | (unsigned)(lane & 32) << 25, 0, 16);
  }
};

struct PvTask2 {
  u32x4 y[2][2], pa[2], pb[2];
  unsigned lo, lm, vr, ma, mz, so, sz;
  __device__ __forceinline__ void init(int gp, int lane) {
    const int hi = lane >> 5;
    lo = pt_tok(2 * gp) + (unsigned)(lane * 16); lm = pt_mem(2 * gp + hi) + (unsigned)((lane & 31) * 8);
    vr = (unsigned)(L_VOX + 2048 + (lane ^ 8) * 16);
    ma = (unsigned)(L_VOM + hi * 768 + (lane & 31) * 8); mz = (unsigned)(L_VOM + 512 + ((lane & 31) ^ (hi * 8)) * 8);
    so = (unsigned)(L_STG + 2048 + (hi * 16 + (lane & 15)) * 16 + ((lane >> 4) & 1) * 8);
    sz = hi ? (unsigned)(L_RED1 + 8704 + lane * 8) : so + 1024;
    PIN_V(lo); PIN_V(lm); PIN_V(vr); PIN_V(ma); PIN_V(mz); PIN_V(so); PIN_V(sz);
#pragma unroll
    for (int q = 0; q < 2; ++q) { pa[q] = pb[q] = y[q][0] = y[q][1] = (u32x4){0u, 0u, 0u, 0u}; PIN_V(pa[q]); PIN_V(pb[q]); }
  }
  __device__ __forceinline__ void load(const __amdgpu_buffer_rsrc_t rs, unsigned o) {
#pragma unroll
    for (int q = 0; q < 2; ++q) {
      y[q][0] = SC1_LOADS(lo + q * 1024, o); y[q][1] = SC1_LOADS(lo + 2048 + q * 1024, o);
      const u32x2 p = __builtin_amdgcn_raw_buffer_load_b64(rs, lm + q * 256, o, 16);
      pa[q][0] = p[0]; pa[q][1] = p[1];
    }
  }
  __device__ __forceinline__ bool ready() {
    return wave_all(umin(umin(umin(min4(y[0][0]), min4(y[0][1])), umin(min4(y[1][0]), min4(y[1][1]))), umin(umin(pa[0][0], pa[0][1]), umin(pa[1][0], pa[1][1]))) != 0u);
  }
  template <bool DIRECT> __device__ __forceinline__ void run(char* lds, int sv, int lane, const __amdgpu_buffer_rsrc_t rs, unsigned og) {
    const f32x4 Z4 = {0.f, 0.f, 0.f, 0.f};
    const char* vp = lds + L_VOX + sv * 5120 + lane * 16;
    bf16x8 vx[6];
#pragma unroll
    for (int t = 0; t < 5; ++t) vx[t] = *(const bf16x8*)(vp + t * 1024);
    vx[5] = *(const bf16x8*)(lds + vr + sv * 5120);
    const u32x2 a0 = *(const u32x2*)(lds + ma + sv * 1280), a1 = *(const u32x2*)(lds + ma + sv * 1280 + 256);
    const u32x2 z0 = *(const u32x2*)(lds + mz + sv * 1280), z1 = *(const u32x2*)(lds + mz + sv * 1280 + 256);
    const bf16x8 vm = mk8(a0[0], a0[1], a1[0], a1[1]), vz = mk8(z0[0], z0[1], z1[0], z1[1]);
#pragma unroll
    for (int q = 0; q < 2; ++q) { PIN_V(pa[q]); pb[q][2] = pa[q][0]; pb[q][3] = pa[q][1]; PIN_V(pb[q]); }
    __builtin_amdgcn_sched_barrier(0);
    f32x4 o[2][3];
#pragma unroll
    for (int q = 0; q < 2; ++q) {
#pragma unroll
      for (int t = 0; t < 3; ++t) o[q][t] = mfma32(vx[t], as8(y[q][0]), Z4);
#pragma unroll
      for (int t = 0; t < 3; ++t) o[q][t] = mfma32(vx[3 + t], as8(y[q][1]), o[q][t]);
      o[q][0] = mfma32(vm, as8(pa[q]), o[q][0]); o[q][1] = mfma32(vm, as8(pb[q]), o[q][1]); o[q][2] = mfma32(vz, as8(pa[q]), o[q][2]);
    }
#pragma unroll
    for (int q = 0; q < 2; ++q) {
#pragma unroll
      for (int t = 0; t < 3; ++t) {
        const u32x2 v = {pack2n(o[q][t][0], o[q][t][1]), pack2n(o[q][t][2], o[q][t][3])};
        if (DIRECT) SC1_STORE8S(v, (so - (L_STG + 2048) + t * 512) | (t == 2 ? (unsigned)(lane & 32) << 25 : 0u), og + q * 20480);
        else *(u32x2*)(lds + (t < 2 ? so + t * 512 : sz) + q * 1280) = v;
      }
    }
  }
};

struct TokScores {
  bf16x8 k[2], q[2]; u32x4 v[2];
  __device__ __forceinline__ void get(const char* lds, int seg, int lane) {
#pragma unroll
    for (int i = 0; i < 2; ++i) { k[i] = *(const bf16x8*)(lds + L_KX + (seg & 1) * 2048 + i * 1024 + lane * 16); q[i] = *(const bf16x8*)(lds + L_QX + (seg & 1) * 2048 + i * 1024 + lane * 16); }
  }
  __device__ __forceinline__ void run(int qt) {
    const f32x4 Z4 = {0.f, 0.f, 0.f, 0.f};
    const f32x4 d1 = mfma32(k[0], q[qt], Z4), d2 = mfma32(k[1], q[qt], Z4);
    v[qt] = (u32x4){pack2n(d1[0], d1[1]), pack2n(d1[2], d1[3]), pack2n(d2[0], d2[1]), pack2n(d2[2], d2[3])};
    PIN_V(v[qt]);
  }
  __device__ __forceinline__ void store(const __amdgpu_buffer_rsrc_t rs, unsigned off, unsigned o) {
    __builtin_amdgcn_raw_buffer_store_b128(v[0], rs, off, o, 16); __builtin_amdgcn_raw_buffer_store_b128(v[1], rs, off + 1024, o, 16);
    asm volatile("s_nop 1" ::: "memory");
  }
};

__device__ __forceinline__ void touch(const char* base, unsigned off, unsigned lds0) {
  unsigned keep;
  asm volatile("s_mov_b32 %0, m0\n\ts_mov_b32 m0, %3\n\ts_nop 0\n\tglobal_load_lds_dword %1, %2 sc1\n\ts_mov_b32 m0, %0" : "=&s"(keep) : "v"(off), "s"(base), "s"(lds0 + L_RED1 + 8192) : "memory");
}
template <int N> __device__ __forceinline__ void stouch(const char* p, unsigned& d) {
  asm volatile(".set pf_o, 0\n\t.rept %2\n\ts_load_dword %0, %1, pf_o\n\t.set pf_o, pf_o + 128\n\t.endr" : "+s"(d) : "s"(p), "n"(N) : "memory");
}
__device__ __forceinline__ void code_touch(int b, int lane, unsigned lds0) {
  const char* pc; int d0, d1;
  asm volatile("s_getpc_b64 %0\n\ts_mov_b32 %1, _Z10rec_kernel7RecArgs@rel32@lo+4\n\ts_mov_b32 %2, _Z11attb_kernelPcPt@rel32@lo+12" : "=s"(pc), "=s"(d0), "=s"(d1));
  const int o = (lane * 32 + (b >> 3)) * 128;
  touch(pc + d0, (unsigned)(o < d1 - d0 ? o : 0), lds0);
}
__device__ __forceinline__ unsigned zero_base(int b, int h, int j, int ln) {
  return ln < 32 ? (unsigned)((h & 3) * 32768 + j * 512 + ln * 16) : ln < 40 ? (unsigned)((h & 2) * 1024 + j * 256 + (ln & 4) * 32 + (h & 1) * 64 + (ln & 3) * 16)
                 : (unsigned)OFF_ACC + (b < ACC_NB ? (unsigned)(b * 256 + (ln - 40) * 16) : ts_piece((b - ACC_NB) * 8 + ln - 40));
}
__device__ __forceinline__ unsigned sct_slot(unsigned ck, unsigned sl) { return sl * SCT_STEP + ((ck >> (sl & 4u)) & 7u) * 4096u; }
__device__ __forceinline__ unsigned scm_slot(unsigned ck, unsigned sl) { return OFF_SCM + sl * SCM_STEP + ((ck >> 8) & 7u) * 4096u; }
__device__ __forceinline__ int ck_rot(int x) { return ((x & 1) << 2) | (x >> 1); }
struct Place {
  unsigned t[8];
  __device__ __forceinline__ void load(const char* ws, int lane) {
#pragma unroll
    for (int x = 0; x < 8; ++x) t[x] = *(const unsigned*)(ws + OFF_LAT + ((imin(lane >> 3, 5) * 8 + (ck_rot(x) ^ (lane & 7))) * 8 + x) * 4);
  }
  __device__ __forceinline__ unsigned get(int b, int lane) {
    int c = (int)((((t[0] + t[1]) + (t[2] + t[3])) + ((t[4] + t[5]) + (t[6] + t[7]))) * 8u) + (lane & 7);
    c = imin(c, dppi<DPP_XOR1>(c)); c = imin(c, dppi<DPP_XOR2>(c)); c = imin(c, dppi<0x141>(c));
    unsigned k = 0u;
#pragma unroll
    for (int r = 0; r < 6; ++r) k |= (unsigned)(__builtin_amdgcn_readlane(c, 8 * r) & 7) << (4 * r);
    return (k ^ PL_X) ^ (unsigned)ck_rot(b & 7) * 0x111111u;
  }
};

struct Sched {
  u32x4 tsv, rec;
  unsigned side, o;
  __device__ __forceinline__ void pre(int s) { o = (unsigned)OFF_ACC + (unsigned)((s + SR - 1) % SR) * (unsigned)ACC_STEP + ACC_TS + side; PIN_S(o); }
  __device__ __forceinline__ void load(const __amdgpu_buffer_rsrc_t rs, int lane) {
    tsv = SC1_LOADS(ts_piece(lane & 63) - ACC_TS, o); rec = SC1_LOADS(128, o);
  }
  __device__ __forceinline__ unsigned last(unsigned ref) {
    const unsigned o = 0x40000000u - ref;
    int l = (int)umax(umax(tsv[0] ? tsv[0] + o : 0u, tsv[1] ? tsv[1] + o : 0u), umax(tsv[2] ? tsv[2] + o : 0u, tsv[3] ? tsv[3] + o : 0u));
    return (unsigned)wave_imax(l) - o;
  }
  __device__ __forceinline__ unsigned run(const RecArgs& a, const __amdgpu_buffer_rsrc_t rs, int s, int b, int lane) {
    constexpr int w = 3; (void)w;
    unsigned acur = __builtin_amdgcn_readfirstlane(rec[0]), aprev = __builtin_amdgcn_readfirstlane(rec[1]), hist = __builtin_amdgcn_readfirstlane(rec[3]);
    int D8 = (int)__builtin_amdgcn_readfirstlane(rec[2]);
    const int d = wave_imax((int)(umax(umax(tsv[0], tsv[1]), umax(tsv[2], tsv[3])) - aprev)), dd = 8 * d - D8;
    int Dn = D8 + (dd >> (dd > 0 ? SCH_GU : SCH_GD));
    unsigned anext = (acur + (unsigned)((Dn + 4 + imax(Dn - 8 * SCH_DR, 0) * SCH_DK / 8) >> 3) + SCH_V + (s < EARLY_N ? EARLY_V : (s >= WPF0 ? (s == NSEG - 2 ? LAST_V : LATE_V) : 0))) | 1u;
    if (__builtin_expect(hist < 2u || aprev == 0u || d < 1 || d + SCH_VMIN > (int)(acur - aprev), 0)) {
      if (s == 0) {
        for (int it = 0; it < 40 && !wave_all(min4(tsv) != 0u); ++it) {
          __builtin_amdgcn_s_sleep(ST_ALL);
          asm volatile("" ::: "memory");
          load(rs, lane);
        }
      }
      const unsigned now = now32(), L = last(now);
      if (acur == 0u) { D8 = 8 * SCH_D0; hist = 0u; }
      const bool late = acur == 0u || (int)(L + SCH_VMIN - acur) > 0;
      if (late) { if (b == 0) FC(3); if (acur) D8 += 8 * (hist ? SCH_UP : 3 * SCH_UP); }
      else if (aprev) { if (hist < 2u) D8 = 8 * imin(imax((int)(L - aprev), 1), 4000); hist = umin(hist + 1u, 2u); }
      const unsigned base = acur == 0u ? L + SCH_FIRST : (late ? L + SCH_V + ((int)(L + SCH_VMIN2 - acur) <= 0 ? SCH_PEN2 : SCH_PEN) : acur);
      anext = (base + (unsigned)((D8 + 4) >> 3) + SCH_V + (s == 1 ? EARLY_V1 : (s < EARLY_N ? EARLY_V : 0))) | 1u;
      if (s == 0) anext = (L + ST_X) | 1u;
      if ((int)(now - L) > 50000) anext = 0u;
      if (late) acur = 0u;
      Dn = D8;
    }
    if (__builtin_expect(b == 0, 0))
      __builtin_amdgcn_raw_buffer_store_b128((u32x4){anext, acur, (unsigned)Dn, hist}, rs, lane < 2 ? lane * 8192 : 0x7ffffff0, (unsigned)OFF_ACC + (unsigned)(s % SR) * (unsigned)ACC_STEP + ACC_TS + 128u, 16);
    if (XO_T && anext) anext += (b & 1) * XO_T;
    LOGV(0, anext); LOGV(1, acur == 0u); LOGV(2, (Dn + 4) >> 3); LOGV(3, d); LOGV(4, acur - aprev - d); LOGV(5, aprev);
    return anext;
  }
};

struct TokTask {
  u32x4 y[2]; u32x2 ym; u32x4 res;
  unsigned l0, l1, lm, s0, s1;
  static __device__ __forceinline__ bool ok(int sl) { return sl >= 0 && sl < NSEG; }
  __device__ __forceinline__ void init(int h, int j, int lane) {
    const int c = lane & 15, g = lane >> 4;
    l0 = ts_tok(h, g) + (unsigned)(j * 256 + c * 16); l1 = l0 + 16384u;
    lm = ts_mem(h, g + 4 * (c >> 3)) + (unsigned)(j * 64 + (c & 7) * 8);
    res = (u32x4){0u, 0u, 0u, 0u};
    const int qi = 4 * j + (c & 3);
    s0 = g < 2 ? pt_tok(h) + (unsigned)((qi >> 4) * 1024 + ((c >> 2) * 16 + (qi & 15)) * 16 + g * 8) : 0x7ffffff0u;
    s1 = lane < 8 ? pt_mem(h) + (unsigned)((qi >> 4) * 256 + ((c >> 2) * 16 + (qi & 15)) * 8) : 0x7ffffff0u;
    PIN_V(l0); PIN_V(l1); PIN_V(lm); PIN_V(s0); PIN_V(s1);
  }
  __device__ __forceinline__ void load(const __amdgpu_buffer_rsrc_t rs, unsigned o) {
    y[0] = SC1_LOADS(l0, o); y[1] = SC1_LOADS(l1, o);
    ym = __builtin_amdgcn_raw_buffer_load_b64(rs, lm, o, 16);
  }
  __device__ __forceinline__ bool ready() { return wave_all(umin(umin(min4(y[0]), min4(y[1])), umin(ym[0], ym[1])) != 0u); }
  __device__ __forceinline__ void run(int j, int lane, const bf16x8 SelLo) {
    const int c = lane & 15, g = lane >> 4;
    const f32x4 Z4 = {0.f, 0.f, 0.f, 0.f};
    const f32x4 d1 = mfma32(SelLo, as8(y[1]), mfma32(SelLo, as8(y[0]), Z4));
    const f32x4 d0 = mfma32(SelLo, mk8(ym[0], ym[1], 0u, 0u), Z4);
    const int qi = 4 * j + (c & 3), k0 = 16 * g + 4 * (c >> 2);
    float t[4], mv[4];
    float mx = -INFINITY;
#pragma unroll
    for (int r = 0; r < 4; ++r) {
      t[r] = (g < 2 && k0 + r <= qi) ? d1[r] : -INFINITY;
      const float mt = d0[r] + dppf<DPP_ROR8>(d0[r]);
      mv[r] = lane < 8 ? mt : -INFINITY;
      mx = fmx(mx, fmx(t[r], mv[r]));
    }
    mx = fmx(mx, dppf<DPP_ROR8>(mx)); mx = fmx(mx, dppf<DPP_ROR4>(mx));
    mx = x16mx(mx);
    float sum = 0.f;
#pragma unroll
    for (int r = 0; r < 4; ++r) {
      t[r] = __builtin_amdgcn_exp2f(t[r] - mx); mv[r] = __builtin_amdgcn_exp2f(mv[r] - mx);
      sum += t[r] + mv[r];
    }
    sum += dppf<DPP_ROR8>(sum); sum += dppf<DPP_ROR4>(sum);
    sum = x16sum(sum);
    const float rs_ = __builtin_amdgcn_rcpf(sum);
    res = (u32x4){pack2n(t[0] * rs_, t[1] * rs_), pack2n(t[2] * rs_, t[3] * rs_), pack2n(mv[0] * rs_, mv[1] * rs_), pack2n(mv[2] * rs_, mv[3] * rs_)};
    PIN_V(res);
  }
  __device__ __forceinline__ void store(const __amdgpu_buffer_rsrc_t rs, unsigned o, unsigned no = 0u) {
    SC1_STORE8S(((u32x2){res[0], res[1]}), s0 | no, o);
    SC1_STORE8S(((u32x2){res[2], res[3]}), s1 | no, o);
  }
};

constexpr int L_DRN = L_VOX + (NSEG % NV) * 5120;
__device__ __forceinline__ void drain_pre(char* lds, int b, int lane) {
  const int gp = (b & 7) * 2 + (b >> 7), m = (b >> 3) & 15;
  TokTask tk; PvTask2 pv; OutTask ot;
  tk.init(2 * gp + (m >> 3), m & 7, lane); pv.init(gp, lane); ot.init(b, lane);
  u32x4* p = (u32x4*)(lds + L_DRN) + lane;
  p[0] = (u32x4){tk.l0, tk.lm, tk.s0, tk.s1}; p[64] = (u32x4){pv.lo, pv.lm, pv.vr, pv.ma}; p[128] = (u32x4){pv.mz, pv.so, ot.t5f, ot.t5o};
  p[192] = __builtin_bit_cast(u32x4, ot.sel);
}
__device__ __forceinline__ void qkv_pre(unsigned (&vo)[2][2], int b, int w, int lane) {
  const int cb = qkv_col(b), c = lane & 15, g = lane >> 4, t0 = w & 1, col0 = (cb % 5) * 128 + w * 32;
  const bool SW = cb < 10;
#pragma unroll
  for (int i = 0; i < 2; ++i)
#pragma unroll
    for (int tt = 0; tt < 2; ++tt) {
      const int tq = tt ^ t0, tk = tq * 16 + c, cc = col0 + i * 16 + (SW ? 4 * g : c), hd = cc / BD, e = cc - hd * BD;
      const int kv = (int)(OFF_KV - OFF_QB) + hd * 64 * KVT;
      vo[i][tt] = (unsigned)(!SW ? kv + 1280 + tq * 672 + ((g >> 1) * 21 + e) * 16 + (g & 1) * 8
                                 : (cb < 5 ? (hd * S_ + tk) * 2 * QST + e * 2 : kv + (e < 16 ? (tk + 32 * (e >> 3)) * 16 + (e & 7) * 2 : 1024 + tk * 8)));
      PIN_V(vo[i][tt]);
    }
}

template <int w>
__device__ __forceinline__ void token_waves(const RecArgs& a, char* lds, const __amdgpu_buffer_rsrc_t rs, unsigned (&vo)[2][2]) {
  const int lane0 = __builtin_amdgcn_workitem_id_x() & 63, b = __builtin_amdgcn_workgroup_id_x(), wid = w + 4; (void)wid;
  const int gp = (b & 7) * 2 + (b >> 7), m = (b >> 3) & 15, h0 = 2 * gp, h = h0 + (m >> 3), j = m & 7;
  float* const misc = (float*)(lds + L_MISC);
  const f32x4 Z4 = {0.f, 0.f, 0.f, 0.f};
  const unsigned lds0 = (unsigned)(uintptr_t)(LAS char*)lds;
  unsigned* const cnt = (unsigned*)(misc + 6);
  bf16x16 W[2][10];
  auto btile = [&](int i) { return w == 1 ? 0 : (w == 2 ? 1 + i : 3 + i); };
  if (w == 1) code_touch(b, lane0, lds0);
  Place pl;
  unsigned phi[2][2], ph[2][2][2];
#pragma unroll
  for (int k = 0; k < 8; ++k) {
    const int bt = btile(k >> 2), hw = h0 + (bt == 2 ? k & 1 : bt / 3), T = bt == 2 ? 32 + (m >> 1) : (bt % 3) * 16 + m;
    ph[k >> 2][(k >> 1) & 1][k & 1] = ((const __attribute__((address_space(4))) unsigned*)(a.ws + OFF_PHI))[(hw >> 2) | (hw & 3) << 3 | (k & 2) << 4 | (T / 10) << 6];
  }
  __builtin_amdgcn_sched_barrier(0);
  if (w == 3) pl.load(a.ws, lane0);
  if (a.layer) {
    if (w != 0) img_fetch<20>(a, lds0, h, j, w - 1, lane0);
  } else {
    if (w == 0) img_fetch<12>(a, lds0, h, j, 4, lane0);
#pragma unroll
    for (int i = 0; i < 10; ++i) x_fetch(x_src(a, 0, b), lds0, 0, 10 * w + i, lane0);
  }
  const __amdgpu_buffer_rsrc_t rwb = __builtin_amdgcn_make_buffer_rsrc(a.ws + OFF_WB, 0, (int)WB_BYTES, 0x00020000);
#pragma unroll
  for (int k = 0; k < 4; ++k) phi[k >> 1][k & 1] = (((lane0 & 8) ? ph[k >> 1][k & 1][1] : ph[k >> 1][k & 1][0]) & 8192u) | (unsigned)(lane0 * 16);
#pragma unroll
  for (int i = 0; i < 2; ++i)
#pragma unroll
    for (int p = 0; p < 10; ++p)
      if (w >= 2 || (w == 1 && i == 1)) {
        const unsigned wo = wb_off((b * 5 + btile(i)) * 20 + 2 * p), wv = phi[i][p / 5] ^ (wo & 8192u);
        if (FR_PACE && a.layer) asm volatile("s_waitcnt vmcnt(%0)" :: "n"(FR_PACE) : "memory");
        W[i][p] = cat16(as8(__builtin_amdgcn_raw_buffer_load_b128(rwb, wv, wo & ~8192u, 0)), as8(__builtin_amdgcn_raw_buffer_load_b128(rwb, wv + 1024, wo & ~8192u, 0)));
      }
  f32x4 bn = Z4;
  if (w == 0) {
    bn = *(const f32x4*)(a.ws + OFF_BNRM + lane0 * 16);
    if (!a.layer) asm volatile("s_waitcnt vmcnt(0)" ::: "memory");
  } else asm volatile("s_waitcnt vmcnt(%0)" :: "n"(w == 1 ? 20 : 40) : "memory");
  unsigned ck = 0u;
  if (w == 3) { ck = pl.get(b, lane0); *(unsigned*)(misc + 11) = ck; }
  BAR();
#pragma unroll
  for (int p = 0; p < 10; ++p) {
    if (w < 2) W[0][p] = cat16(wqk_frag(lds, 2 * w, 2 * p, lane0), wqk_frag(lds, 2 * w, 2 * p + 1, lane0));
    if (w == 0) W[1][p] = cat16(wqk_frag(lds, 1, 2 * p, lane0), wqk_frag(lds, 1, 2 * p + 1, lane0));
  }
  float cb2 = 0.f;
  if (w == 0) {
    float n = __builtin_amdgcn_sqrtf(bn[0] + bn[1]) + __builtin_amdgcn_sqrtf(bn[2] + bn[3]);
    n += dppf<DPP_XOR1>(n); n += dppf<DPP_XOR2>(n); n += dppf<DPP_ROR4>(n); n += dppf<DPP_ROR8>(n);
    n = x32mx(x16mx(n));
    cb2 = u2f(__builtin_amdgcn_readfirstlane(f2u(1.15f * n * n)));
  }
  const float cm2 = u2f((f2u(cb2) & 0x7fffffu) | 0x40000000u);
  const int ek = imax((int)((f2u(cb2) >> 23) & 0xffu), 1) - 252;
  if (w >= 2) {
#pragma unroll
    for (int i = 0; i < 4; ++i) *(unsigned*)(lds + L_KX + (w - 2) * 4096 + i * 1024 + lane0 * 16 + 12) = 0u;
  }
  OutTask ot;
  const int zl = lane0 - 16;
  const bool zon = w == 3 && zl >= 0 && (zl < 40 || b < ACC_NB + 17);
  unsigned zo = zon ? zero_base(b, h, j, zl) + (zl < 32 ? sct_slot(ck, 0u) : (zl < 40 ? scm_slot(ck, 0u) : 0u)) : 0x7ffffff0u;
  unsigned zm = zon ? (zl < 32 ? SCT_STEP + 1u : (zl < 40 ? SCM_STEP : (unsigned)ACC_STEP)) : 0u;
  const unsigned zx = ((ck ^ (ck >> 4)) & 7u) * 4096u;
  if (w == 3) { PIN_V(zo); PIN_V(zm); }
  const __amdgpu_buffer_rsrc_t rx = __builtin_amdgcn_make_buffer_rsrc(a.ws + OFF_XNRM, 0, (int)(OFF_QB - OFF_XNRM), 0x00020000);
  u32x4 v0, v1;
  f32x4 d5;
  unsigned oo, za, xa, ya, n4, so_op, so_y, so_x;
  float xn4 = 0.f;
  auto w1_part = [&](int k, int s, int lane) {
    if (w < 2) return;
    if (w == 2) {
      if (k >= 1 && k <= 4) d5 = mfma32(ot.sel, as8(ot.y[k < 1 || k > 4 ? 0 : k - 1]), k == 1 ? Z4 : d5);
      if (k == 6 || k == 7) {
        const unsigned r = pack2(d5[2 * (k & 1)], d5[2 * (k & 1) + 1]), x = ot.xo[k & 1];
        ot.out[k & 1] = pack2(u2f(x << 16) + u2f(r << 16), u2f(x & 0xffff0000u) + u2f(r & 0xffff0000u));
        PIN_V(ot.out);
      }
      if (k == 8) xn4 = fmx(xn4, dppf<DPP_XOR1>(xn4));
      if (k == 9) xn4 = fmx(xn4, dppf<DPP_XOR2>(xn4));
      if (k == 10) xn4 = fmx(xn4, dppf<DPP_ROR4>(xn4));
      if (k == 11) xn4 = fmx(xn4, dppf<DPP_ROR8>(xn4));
      if (k >= 8 && k <= 11) PIN_V(xn4);
      if (k == 12) misc[s & 3] = x16mx(xn4);
      if (k == 5) { so_y = op_slot(imax(s - 1 - LAG5, 0)); so_x = (unsigned)imax(s - 1 - LAG5, 0) * 40960u; PIN_S(so_y); PIN_S(so_x); }
      if (k == 14) {
        ya = ot.t5o | (s <= LAG5 ? 0x7ff00000u : 0u);
        xa = (ot.t5f * 2u | (unsigned)(lane & 32) << 25) | (OutTask::ok(s - 2 - LAG5, b) ? 0u : 0x7ff00000u);
        PIN_V(ya); PIN_V(xa);
      }
    } else if (k == 8) {
      const unsigned zs = (unsigned)(s + 2) % SR;
      za = (zo + (s < 0 ? 1024u : zs) * (zm & ~1u)) ^ ((zm & 1u) * (zs & 4u ? zx : 0u));
      PIN_V(za);
    }
    if (k == 13) {
      n4 = __hip_atomic_load((unsigned*)(misc + 28), __ATOMIC_RELAXED, __HIP_MEMORY_SCOPE_WORKGROUP);
      so_op = s < 2 + LAG4 ? 0x7ff00000u : op_slot(s - 2 - LAG4) + (unsigned)((m * 32 + (w & 1) * 16 + gp) * 1280);
      PIN_S(so_op);
    }
    if (k == 15 && __builtin_expect((int)__builtin_amdgcn_readfirstlane(n4) < s, 0)) lds_spin<L_MISC + 112, 1>(lds0, (unsigned)s);
    if (k == 16) {
      asm volatile("" ::: "memory");
      const char* sp = lds + L_STG + 2048 + (w & 1) * 1280;
      v0 = *(const u32x4*)(sp + lane * 16); v1 = *(const u32x4*)(sp + 1024 + (lane & 15) * 16);
      oo = so_op + (unsigned)(lane * 16);
      za = lane < 16 ? oo + 1024 : (w == 3 ? za : 0x7ffffff0u);
      PIN_V(oo); PIN_V(za);
    }
    if (k == 19 && w == 3) {
#pragma unroll
      for (int i = 0; i < 4; ++i) { v1[i] = lane < 16 ? v1[i] : 0u; PIN_V(v1[i]); }
    }
    __builtin_amdgcn_sched_barrier(0);
  };
  auto w1_fire = [&](int s, int lane, auto last) {
    if (w < 2) return;
    lds_spin<L_MISC + 92, W1_SL>(lds0, 4u * (unsigned)imax(s, 0));
    SC1_STORE(v0, oo);
    SC1_STORE(v1, za);
    __builtin_amdgcn_sched_barrier(0);
    if (w == 2) {
      __builtin_amdgcn_raw_buffer_store_b64(ot.out, rx, xa, so_x + (unsigned)(OFF_XA - OFF_XNRM - 40960), 16);
      if (last == 0) {
#pragma unroll
        for (int k = 0; k < 4; ++k) ot.y[k] = SC1_LOADS(ya, so_y + k * 5120);
        ot.xo = __builtin_amdgcn_raw_buffer_load_b64(rx, ot.t5f * 2u, so_x + (unsigned)(OFF_XB - OFF_XNRM), 16);
        xn4 = u2f(__builtin_amdgcn_raw_buffer_load_b32(rx, (lane & 31) * 16, (s + 1) * 512, 16));
      }
    } else {
      const unsigned xo_ = (unsigned)((s - 8) * 1024 + lane * 16);
      if (__builtin_expect((unsigned)(s - 8) < 20u, 0)) SC1_STORE(((u32x4){0u, 0u, 0u, 0u}), xo_ < X_BYTES / 256 ? (unsigned)(OFF_X + b * (X_BYTES / 256)) + xo_ : 0x7ffffff0u);
    }
    __builtin_amdgcn_sched_barrier(0);
  };
  STAMP_DECL(); ARRIVE_DECL();
  auto mma_seg = [&](int seg, int sv, int lane, auto pause) {
    const int c = lane & 15, g = lane >> 4;
    const char* xp = lds + L_XN + (seg & 1) * 40960 + lane * 16;
    f32x4 acc[2][2] = {{Z4, Z4}, {Z4, Z4}};
    bf16x8 x[4][2];
#pragma unroll
    for (int p = 0; p < 3; ++p) { x[p][0] = *(const bf16x8*)(xp + p * 1024); x[p][1] = *(const bf16x8*)(xp + (20 + p) * 1024); }
    unsigned eo[2];
#pragma unroll
    for (int ks = 0; ks < 20; ++ks) {
      if (ks == (w == 0 ? KP0 : (w == 1 ? KP1 : KP2))) { pause(); __builtin_amdgcn_sched_barrier(0); }
      if (ks == 17) {
#pragma unroll
        for (int i = 0; i < 2; ++i) {
          eo[i] = (unsigned)(w == 0 ? (i ? L_KX : L_QX) + (seg % NKQ) * 2048 + lane * 16
                                    : (w == 1 && i == 0 ? ((g >> 1) ? L_KX : L_QX) + (seg % NKQ) * 2048 + (2 * (g & 1) * 16 + c) * 16 + 8 : L_VOX + sv * 5120 + btile(i) * 1024 + lane * 16));
          if (i == 0 || w == 1) PIN_V(eo[i]);
        }
        if (w == 0) eo[1] = eo[0] + (L_KX - L_QX);
        if (w >= 2) eo[1] = eo[0] + 1024;
      }
      __builtin_amdgcn_sched_barrier(0);
#pragma unroll
      for (int q = 0; q < 4; ++q) {
        const int tt = 1 - (q >> 1), i = q & 1;
        const bool isqk = w == 0 || (w == 1 && i == 0);
        const bf16x8 wf = half8(W[i][ks >> 1], ks & 1);
        acc[i][tt] = isqk ? mfma32(wf, x[ks & 3][tt], acc[i][tt]) : mfma32(x[ks & 3][tt], wf, acc[i][tt]);
        __builtin_amdgcn_sched_barrier(0);
        if (q < 2 && ks + 3 < 20) x[(ks + 3) & 3][q] = *(const bf16x8*)(xp + (q * 20 + ks + 3) * 1024);
        if (q == 2) w1_part(ks, seg - 1, lane);
        __builtin_amdgcn_sched_barrier(0);
      }
    }
#pragma unroll
    for (int i = 0; i < 2; ++i) {
      if (w + i == 1) { const float ks_ = (w == 0 || (g >> 1)) ? KEXP : 1.0f; acc[i][0] *= ks_; acc[i][1] *= ks_; }
      const u32x4 v = {pack2(acc[i][0][0], acc[i][0][1]), pack2(acc[i][0][2], acc[i][0][3]), pack2(acc[i][1][0], acc[i][1][1]), pack2(acc[i][1][2], acc[i][1][3])};
      char* p = lds + eo[i];
      if (w == 0) { *(u32x2*)(p) = (u32x2){v[0], v[1]}; *(u32x2*)(p + 1024) = (u32x2){v[2], v[3]}; }
      else if (w == 1 && i == 0) { *(unsigned*)(p) = v[0]; *(unsigned*)(p + 256) = v[1]; *(unsigned*)(p + 1024) = v[2]; *(unsigned*)(p + 1280) = v[3]; }
      else *(u32x4*)p = v;
    }
  };
  unsigned vp, mp, ip;
  int sidx, ei = 127;
  float inv;
  f32x4 acc[2];
  auto vo_pre = [&](int s, int sv, int lane) {
    vp = (unsigned)(w == 0 ? L_MISC + (s & 3) * 4 : L_VOM + sv * 1280 + (btile(0) * 32 + (w == 1 ? lane & 31 : lane)) * 8);
    mp = (unsigned)(L_BM + lane * 16);
    PIN_V(vp); PIN_V(mp);
    if (w == 0) { ip = (unsigned)(L_MISC + 16 + ((s + 1) & 1) * 4); PIN_V(ip); }
    else {
      inv = misc[4 + (s & 1)];
      sidx = (lane & 8) ? 0xEEEE : 0x4444;
      acc[0] = acc[1] = Z4;
      PIN_V(sidx); PIN_V(acc[1]);
      if (w >= 2) { PIN_V(acc[0]); PIN_V(inv); }
    }
    __builtin_amdgcn_sched_barrier(0);
  };
  auto vo_mem = [&](int s, auto last) {
    __builtin_amdgcn_s_setprio(2);
    asm volatile(".rept %0\n\ts_nop 3\n\t.endr" :: "n"(w == 0 ? VO_A0 : (w == 1 ? VO_A1 : VO_A2)) : "memory");
    float xmax = 0.f;
    if (w == 0) xmax = *(const float*)(lds + vp);
    constexpr int NX = w == 2 ? VO_N2 : (w == 3 ? VO_N3 : 10);
    bf16x8 x[10];
#pragma unroll
    for (int p = 0; p < NX; ++p) x[p] = *(const bf16x8*)(lds + mp + p * 1024);
    __builtin_amdgcn_sched_barrier(0);
    if (w < 2) asm volatile(".rept %0\n\ts_nop 3\n\t.endr" :: "n"(w == 0 ? VO_B0 : VO_B1) : "memory");
    STAMP(5);
    __builtin_amdgcn_sched_barrier(0);
    int ex = 0;
#pragma unroll
    for (int p = 0; p < 10; ++p) {
      if (w == 0) acc[0] = mfma32(x[p], x[p], p ? acc[0] : Z4);
      if (w == 0 && p == 9) { ex = imax((int)((__builtin_amdgcn_readfirstlane(f2u(__builtin_amdgcn_sqrtf(cb2 * xmax))) >> 23) & 0xffu) + 1, 32); PIN_S(ex); }
#pragma unroll
      for (int i = (w == 1); i < 2 && w != 0; ++i) acc[i] = smfma64(x[p], W[i][p], acc[i], sidx);
      __builtin_amdgcn_sched_barrier(0);
      if (p + NX < 10) x[p + NX] = *(const bf16x8*)(lds + mp + (p + NX) * 1024);
      __builtin_amdgcn_sched_barrier(0);
    }
    STAMP(7);
    if (w != 0) {
      float v[4];
#pragma unroll
      for (int r = 0; r < 4; ++r) {
        const auto sw = __builtin_amdgcn_permlane32_swap(f2u(acc[w == 1][r]), f2u(acc[1][r]), false, false);
        v[r] = (u2f(sw[0]) + u2f(sw[1])) * inv;
      }
      *(u32x2*)(lds + vp) = (u32x2){pack2(v[0], v[1]), pack2(v[2], v[3])};
    } else {
      const int t = (int)f2u(fmx(fmx(acc[0][0], acc[0][1]), fmx(acc[0][2], acc[0][3])) * cm2);
      const int et = wave_imax(t) >> 23, em = ((et + ek) >> 1) + ei;
      int e = imax(et == 255 ? 256 : em, ex);
      PIN_S(e);
      e = imin(e, 256);
      ei = e - 31;
      *(unsigned*)(lds + ip) = (unsigned)ei << 23;
      const u32x3 sr = {(unsigned)(285 - e) << 23, (unsigned)(e - 87) < 79u ? REC_RNG << 23 : 0u, (unsigned)(s + 1)};
      asm volatile("ds_write_b96 %0, %1 offset:%2" :: "v"(lds0), "v"(sr), "n"(L_MISC + 32) : "memory");
    }
    if (last == 0 && w >= 2) asm volatile("s_waitcnt vmcnt(%0)" :: "n"(w == SCH_W ? 2 : 0) : "memory");
    if (w != 0) lds_inc<L_MISC + 28>(lds0);
    STAMP(8);
    if (last) __builtin_amdgcn_s_setprio(0);
    if (last && w == 2) { VM_DRAIN(); lds_inc<L_MISC + 28>(lds0); }
  };
  auto wpf = [&](int s, int lane) {
    constexpr int PER = 81920 / QR / WPF_N;
    static_assert(PER % WPF_S == 0 && PER / WPF_S <= 64, "prefetch");
    touch(a.ws + OFF_WQKVBT + (size_t)imin(qkv_col(b), 14) * 163840 + (qkv_row(b) % (2 * QR)) * (81920 / QR), (s - WPF0) * PER + imin(lane, PER / WPF_S - 1) * WPF_S, lds0);
  };
  Sched sch;
  auto late = [&](int s, int lane) {
    if (__builtin_expect(s < 0, 0)) return;
    if ((w == 2 && F6) || (w == 0 && F4) || (w == 1 && F5) || (w == 3 && F7)) lds_spin<L_MISC + 48, 1>(lds0, 2u * (unsigned)(s + 1));
    if (w >= 2) { x_fetch_n<w == 2 ? F6 : F7>(x_src(a, s + 3, b), lds0, s + 3, F4 + F5 + (w == 3 ? F6 : 0), lane * 16); return; }
    unsigned sd = 0u;
    if (w == 0) stouch<10>(a.ws + OFF_XN + (size_t)imin(s + 4, NSEG - 1) * (s < 1 ? 0 : 40960) + (b >> 3) * 1280, sd);
    x_fetch_n<w == 0 ? F4 : F5>(x_src(a, s + 3, b), lds0, s + 3, w == 0 ? 0 : F4, lane * 16);
    if (w == 0 && __builtin_expect(s >= WPF0 && s < WPF0 + WPF_N, 0)) wpf(s, lane);
    if (w == 1 && __builtin_expect(s == CPF_S, 0)) code_touch(b, lane, lds0);
    lds_spin<L_MISC + 104 + 4 * (w & 1), PS_SL>(lds0, (unsigned)imax(s + 1, 0));
    asm volatile("" :: "s"(sd));
  };
  if (w == 2) { ot.init(b, lane0); ot.out = (u32x2){0u, 0u}; ot.xo = ot.out; ot.y[0] = ot.y[1] = ot.y[2] = ot.y[3] = (u32x4){0u, 0u, 0u, 0u}; }
  VM_DRAIN();
  lds_arrive(cnt, lane0);
  lds_wait(cnt, 8u);
  sch.tsv = sch.rec = (u32x4){0u, 0u, 0u, 0u};
  sch.side = __builtin_amdgcn_readfirstlane(*(const unsigned*)(misc + 11)) & 8192u;
  sch.o = 0u;
  int sv = NV - 1;
  CODE_PAD((PADT >> (4 * w)) & 15);
#pragma unroll 1
  for (int s = -1; s < NSEG - 1; ++s) {
    const int sv1 = sv == NV - 1 ? 0 : sv + 1;
    FRESH_LANE();
    STAMP(0);
    mma_seg(s + 1, sv1, lane, [&]() { STAMP(1); late(s - 1, lane); if (w == SCH_W) sch.pre(s); STAMP(9); });
    STAMP(2);
    __builtin_amdgcn_sched_barrier(0);
    VM_DRAIN();
    STAMP(10);
    w1_fire(s, lane, std::integral_constant<int, 0>{});
    if (w >= 2) vo_pre(s, sv, lane);
    STAMP(3);
    BAR();
    STAMP(4);
    if (__builtin_expect(s >= 0, 1)) {
      if (w == SCH_W) { sch.load(rs, lane0); __builtin_amdgcn_sched_barrier(0); }
      if (w < 2) vo_pre(s, sv, lane);
      vo_mem(s, std::integral_constant<int, 0>{});
      STAMP(6);
      if (w == SCH_W) {
        __builtin_amdgcn_s_setprio(SCH_P);
        *(u32x2*)(misc + 24) = (u32x2){sch.run(a, rs, s, b, lane0), (unsigned)(s + 1)};
      }
      STAMP(11);
    }
    __builtin_amdgcn_s_setprio(MMA_P);
    if (w == 2) VM_DRAIN();
    __builtin_amdgcn_sched_barrier(0);
    sv = sv1;
  }
  __builtin_amdgcn_s_setprio(0);
  STAMP_OUT();
  late(NSEG - 2, lane0);
  { FRESH_LANE(); if (w == 0) drain_pre(lds, b, lane); qkv_pre(vo, b, w, lane); }
#pragma unroll
  for (int k = 0; k < 20; ++k) w1_part(k, NSEG - 1, lane0);
  w1_fire(NSEG - 1, lane0, std::integral_constant<int, 1>{});
  if (w != 0) vo_pre(NSEG - 1, (NSEG - 1) % NV, lane0);
  BAR();
  if (w != 0) vo_mem(NSEG - 1, std::integral_constant<int, 1>{});
}

__device__ __forceinline__ void qkv_tail(const RecArgs& a, char* lds, const int w, const unsigned (&vo)[2][2]) {
  const int lane0 = __builtin_amdgcn_workitem_id_x() & 63, b = __builtin_amdgcn_workgroup_id_x();
  const int r = qkv_row(b), cb = qkv_col(b);
  if (cb >= 15) return;
  const unsigned lds0 = (unsigned)(uintptr_t)(LAS char*)lds;
  unsigned* const stage = (unsigned*)(lds + L_MISC) + 17;
  unsigned* const cnty = stage + 1;
  const f32x4 Z4 = {0.f, 0.f, 0.f, 0.f};
  auto fetch = [&](int i) {
    x_fetch_n<10>(x_src(a, NSEG + i, b), lds0, i, 10 * w, lane0 * 16);
  };
  unsigned want = 0;
  auto sync = [&]() { VM_DRAIN(); want += 4u; lds_inc<L_MISC + 72>(lds0); while (__hip_atomic_load(cnty, __ATOMIC_RELAXED, __HIP_MEMORY_SCOPE_WORKGROUP) < want) { } asm volatile("" ::: "memory"); };
  LOGQ(0);
  bf16x8 W[2][20];
  const __amdgpu_buffer_rsrc_t rw = __builtin_amdgcn_make_buffer_rsrc(a.ws + OFF_WQKVBT + (size_t)(cb * 8 + 2 * w) * 20480, 0, 40960, 0x00020000);
  auto w_get = [&](int ks) {
#pragma unroll
    for (int i = 0; i < 2; ++i) W[i][ks] = as8(__builtin_amdgcn_raw_buffer_load_b128(rw, lane0 * 16 + (ks & 3) * 1024, (i * 20 + (ks & ~3)) * 1024, 0));
    __builtin_amdgcn_sched_barrier(0);
  };
#pragma unroll
  for (int ks = 0; ks < QW; ++ks) w_get(ks);
  bf16x8 ones = mk8(0x3f803f80u, 0x3f803f80u, 0x3f803f80u, 0x3f803f80u);
  PIN_V(ones);
  const __amdgpu_buffer_rsrc_t ro = __builtin_amdgcn_make_buffer_rsrc(a.ws + OFF_QB, 0, (int)(OFF_OB - OFF_QB), 0x00020000);
  const int t0 = w & 1;
  auto body = [&](auto SW, auto SQ) {
    const float qs = cb < 5 ? 0.22360679774997896f * 1.4426950408889634f : 1.0f;
    f32x4 cs[2] = {Z4, Z4};
    LOGQ(1);
    f32x4 acc[2][2], st;
    auto mma = [&](int it, auto FIRST) {
      const char* xp0 = lds + L_XN + (it & 1) * 40960 + t0 * 20480 + lane0 * 16;
      const char* xp1 = lds + L_XN + (it & 1) * 40960 + (1 - t0) * 20480 + lane0 * 16;
      acc[0][0] = acc[0][1] = acc[1][0] = acc[1][1] = st = Z4;
      bf16x8 x[4][2];
#pragma unroll
      for (int p = 0; p < 3; ++p) { x[p][0] = *(const bf16x8*)(xp0 + p * 1024); x[p][1] = *(const bf16x8*)(xp1 + p * 1024); }
      __builtin_amdgcn_sched_barrier(0);
#pragma unroll
      for (int ks = 0; ks < 20; ++ks) {
        if (FIRST && ks + QW < 20) w_get(ks + QW);
#pragma unroll
        for (int q = 0; q < 4; ++q) {
          const int tt = 1 - (q >> 1), i = 1 - (q & 1);
          acc[i][tt] = SW ? mfma32(W[i][ks], x[ks & 3][tt], acc[i][tt]) : mfma32(x[ks & 3][tt], W[i][ks], acc[i][tt]);
          __builtin_amdgcn_sched_barrier(0);
          if (q < 2 && ks + 3 < 20) x[(ks + 3) & 3][q] = *(const bf16x8*)((q ? xp1 : xp0) + (ks + 3) * 1024);
          __builtin_amdgcn_sched_barrier(0);
        }
        st = mfma32(SQ ? x[ks & 3][0] : ones, x[ks & 3][0], st);
        if (FIRST) {
#pragma unroll
          for (int i = 0; i < 2; ++i) cs[i] = SW ? mfma32(W[i][ks], ones, cs[i]) : mfma32(ones, W[i][ks], cs[i]);
        }
        __builtin_amdgcn_sched_barrier(0);
      }
    };
    mma(0, std::integral_constant<bool, true>{});
#pragma unroll 1
    for (int it = 0; it < 4; ++it) {
      if (it) mma(it, std::integral_constant<bool, false>{});
      LOGQ(2 + 3 * it);
      FRESH_LANE();
      float* const sp = (float*)(lds + L_ST) + (it & 1) * 64;
      if (SQ) { if ((c >> 2) == g) sp[32 + t0 * 16 + c] = (c & 3) == 0 ? st[0] : ((c & 3) == 1 ? st[1] : ((c & 3) == 2 ? st[2] : st[3])); }
      else if (g == 0) sp[t0 * 16 + c] = st[0];
      sync();
      LOGQ(3 + 3 * it);
      if (it == 0) fetch(2);
      const bool late = r + 48 >= 55;
      if (it == 1 && !late) fetch(3);
      const unsigned so = (unsigned)((r + 16 * it) * (cb < 5 ? 64 * QST : KVT));
      auto stat = [&](float sm, float sq, float& mean, float& rstd) {
        mean = sm * (1.0f / DM);
        rstd = __builtin_amdgcn_rsqf(__builtin_fmaxf(sq * (1.0f / DM) - mean * mean, 0.f) + 1e-5f);
      };
#pragma unroll
      for (int tt = 0; tt < 2; ++tt) {
        const int tq = tt ^ t0;
        float mean[4], rstd[4];
        if (SW) {
          stat(sp[tq * 16 + c], sp[32 + tq * 16 + c], mean[0], rstd[0]);
          rstd[0] *= qs;
        } else {
          const f32x4 sm = *(const f32x4*)(sp + tq * 16 + 4 * g), sq = *(const f32x4*)(sp + 32 + tq * 16 + 4 * g);
#pragma unroll
          for (int k = 0; k < 4; ++k) stat(sm[k], sq[k], mean[k], rstd[k]);
        }
#pragma unroll
        for (int i = 0; i < 2; ++i) {
          float v[4];
#pragma unroll
          for (int k = 0; k < 4; ++k) v[k] = (acc[i][tt][k] - mean[SW ? 0 : k] * cs[i][SW ? k : 0]) * rstd[SW ? 0 : k];
          __builtin_amdgcn_raw_buffer_store_b64((u32x2){pack2(v[0], v[1]), pack2(v[2], v[3])}, ro, vo[i][tt], so, 0);
        }
      }
      LOGQ(4 + 3 * it);
      if (it == 2 && late) lds_spin<L_MISC + 68, 0>(lds0, 5u);
    }
  };
  typedef std::integral_constant<bool, false> F_; typedef std::integral_constant<bool, true> T_;
  if (cb < 10) { if (w >> 1) body(T_{}, T_{}); else body(T_{}, F_{}); }
  else { if (w >> 1) body(F_{}, T_{}); else body(F_{}, F_{}); }
}

__device__ __forceinline__ void t2_mem(const bf16x8 fr, const bf16x8 q0, const bf16x8 q1, const __amdgpu_buffer_rsrc_t rs, unsigned o, int h, int j, float inv, int lane) {
  const f32x4 Z4 = {0.f, 0.f, 0.f, 0.f};
  const f32x4 d0 = mfma32(fr, q0, Z4) * inv, d1 = mfma32(fr, q1, Z4) * inv;
  const unsigned a0 = pack2n(d0[0], d0[1]), a1 = pack2n(d0[2], d0[3]), b0 = pack2n(d1[0], d1[1]), b1 = pack2n(d1[2], d1[3]);
  const auto r0 = __builtin_amdgcn_permlane32_swap(a0, b0, false, false), r1 = __builtin_amdgcn_permlane32_swap(a1, b1, false, false);
  SC1_STORE8S(((u32x2){r0[1], r1[1]}), ts_mem(h, j) + (lane >> 5) * 256 + (lane & 12) * 16 + (lane & 16) * 2 + (lane & 3) * 8, o);
}

template <int w>
__device__ __forceinline__ void rec_waves(const RecArgs& a, char* lds, __amdgpu_buffer_rsrc_t rs, unsigned& tlast) {
  PIN_S(rs);
  const int lane = __builtin_amdgcn_workitem_id_x() & 63, b = __builtin_amdgcn_workgroup_id_x(), c = lane & 15, g = lane >> 4, wid = w; (void)wid;
  const int gp = (b & 7) * 2 + (b >> 7), m = (b >> 3) & 15, h0 = 2 * gp, h = h0 + (m >> 3), j = m & 7;
  float* const misc = (float*)(lds + L_MISC);
  const f32x4 Z4 = {0.f, 0.f, 0.f, 0.f};
  LOGP(0);
  __builtin_amdgcn_s_setprio(3);
  const unsigned lds0 = (unsigned)(uintptr_t)(LAS char*)lds;
  if (!a.layer) img_fetch<12>(a, lds0, h, j, w, lane);
  constexpr int NC = w < 2 ? 2 : 3;
  const int far = ((b >> 2) & 1) ^ 1;
  auto chunk = [&](int f) { return f == 0 ? 4 * far + w : (w == 2 && f == 1 ? 8 + far : (w == 3 && f == 2 ? 9 - far : 4 - 4 * far + w)); };
  bf16x16 Wqk[3][NC];
  u32x4 bm0[NC][2];
#pragma unroll
  for (int f = 0; f < 2 * NC; ++f) bm0[f >> 1][f & 1] = *(const u32x4*)(a.mem0 + (size_t)(c & 7) * DM + 64 * chunk(f >> 1) + 16 * g + 8 * (f & 1));
  if (!a.layer) asm volatile("s_waitcnt vmcnt(0)" ::: "memory");
  BAR();
  LOGP(9);
  const unsigned ck = __builtin_amdgcn_readfirstlane(*(const unsigned*)(misc + 11));
  const unsigned flip = (ck ^ (unsigned)(b << 11)) & 8192u;
#pragma unroll
  for (int t = 0; t < 3; ++t)
#pragma unroll
    for (int f = 0; f < NC; ++f) Wqk[t][f] = cat16(wqk_frag(lds, t, 2 * chunk(f), lane), wqk_frag(lds, t, 2 * chunk(f) + 1, lane));
  if (a.layer) {
    if (w == 0) {
      for (unsigned spins = 0; spins < SPIN_CAP; ++spins) {
        if (__builtin_amdgcn_raw_buffer_load_b32(rs, (b & 7) * 256, (unsigned)OFF_RDY, 16) >= 16u) break;
        __builtin_amdgcn_s_sleep(4);
        asm volatile("" ::: "memory");
      }
      lds_inc<L_MISC + 84>(lds0);
    }
    lds_wait((unsigned*)(misc + 21), 2u);
#pragma unroll
    for (int f = 0; f < 2 * NC; ++f) PIN_V(bm0[f >> 1][f & 1]);
#pragma unroll
    for (int i = 0; i < 10; ++i) x_fetch(x_src(a, 0, b), lds0, 0, 10 * w + i, lane);
  }
  const bf16x8 SelLo = sel_frag(c, 0);
  bf16x8 ONES = mk8(0x3f803f80u, 0x3f803f80u, 0x3f803f80u, 0x3f803f80u);
  if (w < 2) PIN_V(ONES);
  bool dead = false;
  float inv = 0.f;
  unsigned l2off[2 * NC];
#pragma unroll
  for (int f = 0; f < 2 * NC; ++f) { l2off[f] = acc_off(16 * chunk(f >> 1) + 4 * g + 2 * (f & 1) + (c >> 3), c & 7) ^ flip; PIN_V(l2off[f]); }
  int sidx = (c >> 3) ? 0xEEEE : 0x4444;
  PIN_V(sidx);
  const unsigned p1off = (unsigned)((w == 0 ? (h & 2) * 1024 + j * 256 + (h & 1) * 64 + (g & 1) * 8 : (h & 3) * 32768 + j * 512 + (g >> 1) * 256 + (g & 1) * 64) + (c & 4) * 32 + (c & 3) * 16);
  unsigned p1v = (c < 8) && (w != 0 || g >= 2) ? p1off : 0x7ffffff0u, p1cur = 0x7ffffff0u;
  PIN_V(p1v); PIN_V(p1cur);
  const int hd = (c >> 2) & 1, mr = hd ? 3 - (c & 3) : (c & 3);
  const unsigned l1off = (unsigned)(((h0 + hd) & 3) * 32768 + g * 512 + (w & 1) * 128 + (c >> 3) * 64 + mr * 16);
  const unsigned l1offm = (unsigned)((h0 & 2) * 1024 + (g + 4 * (c >> 3)) * 256 + (w & 1) * 128 + hd * 64 + mr * 16);
  const int plane = (lane & ~6) | ((lane & 2) << 1) | ((lane & 4) >> 1);
  unsigned acco = c < 8 ? (acc_off(64 * far + m * 4 + g, 4 * (w & 1) + mr) + (unsigned)(hd * 8)) ^ flip : 0x7ffffff0u;
  unsigned acco2 = c >= 8 ? (acc_off(64 - 64 * far + m * 4 + g, 4 * (w & 1) + mr) + (unsigned)(hd * 8)) ^ flip
                          : (c < 4 ? (acc_off(128 + m * 2 + (g & 1), 4 * (w & 1) + mr) + (unsigned)((g >> 1) * 8)) ^ flip : 0x7ffffff0u);
  if (w < 2) { PIN_V(acco); PIN_V(acco2); }
  unsigned bmo[NC];
#pragma unroll
  for (int f = 0; f < NC; ++f) { bmo[f] = (unsigned)(chunk(f) * 1024 + lane * 16); PIN_V(bmo[f]); }
  bf16x8 bm[NC];
  auto bm_put = [&](int f) { *(bf16x8*)(lds + L_BM + bmo[f]) = bm[f]; __builtin_amdgcn_sched_barrier(0); };
  f32x4 acc[3] = {Z4, Z4, Z4};
  const int rt2 = ((c >> 3) * 8 + 4 * (g & 1)) * 28 + 16 + (c & 7);
  unsigned redo[3] = {(unsigned)(4 * g * 28 + c), (unsigned)(g < 2 ? (4 * g + 2) * 28 + c : rt2), (unsigned)(g < 2 ? (4 * g + 10) * 28 + c : rt2 + 56)};
#pragma unroll
  for (int k = 0; k < 3; ++k) { redo[k] = L_RED1 + w * 1792 + redo[k] * 4; PIN_V(redo[k]); }
  auto red_put = [&]() {
    auto hs = [&](float x, float y) { const auto sw = __builtin_amdgcn_permlane32_swap(f2u(x), f2u(y), false, false); return u2f(sw[0]) + u2f(sw[1]); };
#pragma unroll
    for (int k = 0; k < 3; ++k) {
#pragma unroll
      for (int i = 0; i < 2; ++i) *(float*)(lds + redo[k] + i * 112) = k == 0 ? hs(acc[0][i], acc[1][i]) : hs(acc[k - 1][2 + i], acc[2][2 * k - 2 + i]);
      __builtin_amdgcn_sched_barrier(0);
    }
  };
#pragma unroll
  for (int f = 0; f < NC; ++f) {
    const u32x4 u = bm0[f][0], v = bm0[f][1];
    bm[f] = c < 8 ? mk8(u[0], u[2], v[0], v[2]) : mk8(u[1], u[3], v[1], v[3]);
    bm_put(f);
#pragma unroll
    for (int t = 0; t < 3; ++t) acc[t] = smfma64(bm[f], Wqk[t][f], acc[t], sidx);
  }
  red_put();
  VM_DRAIN();
  lds_arrive((unsigned*)(misc + 6), lane);
  lds_wait((unsigned*)(misc + 6), 8u);
  x_fetch_n<10>(x_src(a, 1, b), lds0, 1, 10 * w, lane * 16);
  LOGP(1);
  STAMP_DECL(); ARRIVE_DECL();
  bf16x8 fr, qx[2];
  auto kq_get = [&](int seg) {
    const char* p = lds + (w == 1 ? L_KX : L_QX) + (seg % NKQ) * 2048 + lane * 16;
    if (w == 1 || w == 2) { qx[0] = *(const bf16x8*)p; qx[1] = *(const bf16x8*)(p + 1024); }
  };
  unsigned qo4 = (unsigned)(L_RED1 + (w == 1 ? (c & 7) * 112 + (c >> 3) * 3584 : c * 112) + g * 16), qo2 = qo4 + 64u - (unsigned)(g * 8), qo2b = qo2 + 3584u;
  if (w < 3) { PIN_V(qo4); PIN_V(qo2); if (w != 1) PIN_V(qo2b); }
  auto pub1 = [&](unsigned off, unsigned o1) {
    if (w == 0 && PK_ADD) {
#define PKA(d, b) ".long 0xd3b24000 + " #d ", 0x18000000 + 256 + " #d " + ((256 + " #b ") << 9)\n\t"
      u32x32 T;
      asm volatile("ds_read_b128 v[224:227], %1\n\tds_read_b128 v[228:231], %1 offset:1792\n\tds_read_b128 v[232:235], %1 offset:3584\n\tds_read_b128 v[236:239], %1 offset:5376\n\t"
                   "ds_read2_b64 v[240:243], %2 offset1:224\n\tds_read2_b64 v[244:247], %3 offset1:224\n\tv_mov_b32 v251, 0\n\t"
                   "s_waitcnt lgkmcnt(4)\n\t" PKA(224, 228) PKA(226, 230) "s_waitcnt lgkmcnt(2)\n\t" PKA(232, 236) PKA(234, 238) PKA(224, 232) PKA(226, 234)
                   "s_waitcnt lgkmcnt(0)\n\t" PKA(240, 242) PKA(244, 246) "v_cvt_pk_bf16_f32 v248, v224, v225\n\t" PKA(240, 244) "v_cvt_pk_bf16_f32 v249, v226, v227\n\t"
                   "v_cvt_pk_bf16_f32 v250, v240, v241\n\ts_nop 1\n\tv_mfma_f32_16x16x32_bf16 v[224:227], v[248:251], v[248:251], 0\n\ts_nop 7\n\t"
                   "v_cvt_pk_bf16_f32 v228, v224, v225\n\tv_cvt_pk_bf16_f32 v229, v226, v227\n\tv_max_u32 v228, 1, v228\n\tv_max_u32 v229, 1, v229\n\t"
                   "buffer_store_dwordx2 v[228:229], %4, %5, %6 offen sc1"
                   : "=&{v[224:255]}"(T) : "v"(lds0 + qo4), "v"(lds0 + qo2), "v"(lds0 + qo2b), "v"(off), "s"(rs), "s"(o1) : "memory");
      return;
    }
    if (w == 0) fr = qk_frag(lds + qo4, lds + qo2, lds + qo2b);
    else {
      const f32x4 a0 = *(const f32x4*)(lds + qo4), a1 = *(const f32x4*)(lds + qo4 + 1792);
      const f32x2 b0 = *(const f32x2*)(lds + qo2), b1 = *(const f32x2*)(lds + qo2 + 1792);
      float v[6];
#pragma unroll
      for (int i = 0; i < 6; ++i) v[i] = i < 4 ? a0[i] + a1[i] : b0[i - 4] + b1[i - 4];
#pragma unroll
      for (int i = 0; i < 6; ++i) v[i] += dppf<DPP_ROR8>(v[i]);
      fr = mk8(pack2(v[0], v[1]), pack2(v[2], v[3]), pack2(v[4], v[5]), 0u);
    }
    const f32x4 sc = mfma32(w == 0 ? fr : qx[0], fr, Z4);
    if (w == 0) SC1_STORE8S(((u32x2){pack2n(sc[0], sc[1]), pack2n(sc[2], sc[3])}), off, o1);
    else {
      const f32x4 s2 = mfma32(qx[1], fr, Z4);
      __builtin_amdgcn_raw_buffer_store_b128((u32x4){pack2n(sc[0], sc[1]), pack2n(sc[2], sc[3]), pack2n(s2[0], s2[1]), pack2n(s2[2], s2[3])}, rs, off, o1, 16);
    }
  };
  unsigned o1 = sct_slot(ck, 0u), o1m = scm_slot(ck, 0u);
  unsigned t2o = ts_tok(h, j) + (unsigned)((c >> 2) * 256 + g * 64 + (c & 3) * 16);
  TokScores t2;
  TokTask tk;
  PvTask2 pv2;
  if (w == 2) tk.init(h, j, lane);
  if (w == 3) { PIN_V(t2o); pv2.init(gp, lane); }
  if (w == 0) pub1(p1v, o1m);
  VM_DRAIN();
  BAR();
  const unsigned tst = now32() | 1u;
  if (w == 3 && ST_X) __builtin_amdgcn_raw_buffer_store_b32(tst, rs, lane < 2 ? ts_piece(b >> 2) - ACC_TS + (b & 3) * 4 + lane * 8192 : 0x7ffffff0, (unsigned)(OFF_ACC + (SR - 1) * ACC_STEP) + ACC_TS, 16);
  LOGP(2);
  kq_get(0);
  if (w == 1) pub1(p1v, o1);
  x_fetch_n<10>(x_src(a, 2, b), lds0, 2, 10 * w, lane * 16);
  if (w == 3) { t2.get(lds, 0, lane); t2.run(0); t2.run(1); t2.store(rs, t2o, ts_slot(ck, 0)); }
  LOGP(3);
  unsigned o2 = (unsigned)OFF_ACC, o2p = 0u, tw = 0u;
  asm volatile("" : "+s"(o2p));
  unsigned tc0 = 0u, tstar = 0u;
  int dmin = 0, dmax = 0;
  int e1 = 100000;
  unsigned offs = 100u;
  unsigned o4 = 0u;
  unsigned o3, o3p;
  u32x16 P = {0u, 0u, 0u, 0u, 0u, 0u, 0u, 0u, 0u, 0u, 0u, 0u, 0u, 0u, 0u, 0u}, V = P;
  u32x8 V8 = {0u, 0u, 0u, 0u, 0u, 0u, 0u, 0u};
  if (w < 2) asm volatile("" : "+{v[208:223]}"(P), "+{v[184:191]}"(V8), "+{v[192:207]}"(V));
  unsigned tso = lane < 2 ? ts_piece(b >> 2) + (unsigned)((b & 3) * 4 + lane * 8192) : 0x7ffffff0u;
  if (w < 2) PIN_V(tso);
  float minf = g >= 2 ? -INFINITY : 0.f;
  if (w < 2) PIN_V(minf);
  int sv = NV - 1;
  bf16x8 vox[5];
  unsigned vma[3], need2;
  u32x4 slo = {0u, 0u, 0u, 0u}, shi = slo;
  float k1 = 0.f;
  const u16* xsrc;
  float invv = 1.0f;
  auto prep0 = [&](int s) {
    xsrc = x_src(a, s + 3, b);
    if (w == 2) { o3p = ts_slot(ck, (s + 1 - LAG3) & 3); o3 = ts_slot(ck, imax(s, 0)); o4 = pt_slot(ck, imax(s - LAG3, 0)); PIN_S(o3); }
    if (w == 3) { o3p = pt_slot(ck, (s - LAG4) & 3); o4 = ts_slot(ck, s + 1); }
    PIN_S(xsrc); PIN_S(o3p); PIN_S(o4);
    __builtin_amdgcn_sched_barrier(0);
  };
  auto prep = [&](int s) {
    o2 = (unsigned)OFF_ACC + (unsigned)(s % SR) * (unsigned)ACC_STEP;
    sv = sv == NV - 1 ? 0 : sv + 1;
    PIN_S(o2); PIN_S(sv);
    if (w < 2) {
      PIN_S(tc0);
      o1 = sct_slot(ck, s % SR); o1m = scm_slot(ck, s % SR);
      need2 = 4u * (unsigned)(s + 1) + 2u * ((unsigned)(s + 2) / NSEG);
      PIN_S(o1); PIN_S(o1m); PIN_S(need2);
      __builtin_amdgcn_sched_barrier(0);
      kq_get(s);
    }
    invv = misc[4 + (s & 1)];
  };
  auto prep2 = [&](int s) {
#pragma unroll
    for (int k = 0; k < 3; ++k) {
      const int t = k == 0 ? 2 : (k == 1 ? far : 1 - far);
      const char* p = lds + L_VOX + sv * 5120 + t * 1024 + (plane & 47) * 16 + (g & 1) * 8;
      auto rd = [&](const char* q) { const u32x2 a0 = *(const u32x2*)q, a1 = *(const u32x2*)(q + 256); return mk8(a0[0], a0[1], a1[0], a1[1]); };
      vox[k == 0 ? 0 : 2 * k - 1] = rd(p);
      if (k) vox[2 * k] = rd(p + 3072);
      vma[k] = lds0 + (unsigned)(L_VOM + sv * 1280 + t * 256 + (plane & 31) * 8);
      PIN_V(vma[k]);
    }
    __builtin_amdgcn_sched_barrier(0);
    if (F0A) { xsrc = x_src(a, s + 3, b); PIN_S(xsrc); }
    const unsigned iu = __builtin_amdgcn_readfirstlane(f2u(invv));
    if (__builtin_expect(iu != f2u(inv), 0)) {
      inv = u2f(iu);
#pragma unroll
      for (int i = 0; i < 8; ++i) {
        u32x4& m = i < 4 ? slo : shi;
        m[i & 3] = (c >> 1) == i ? ((c & 1) ? iu & 0xffff0000u : iu >> 16) : 0u;
      }
      k1 = KEXP * inv;
    }
    PIN_V(k1); PIN_V(slo); PIN_V(shi);
    asm volatile("" : "+v"(vox[0]), "+v"(vox[1]), "+v"(vox[2]), "+v"(vox[3]), "+v"(vox[4]));
    __builtin_amdgcn_sched_barrier(0);
  };
  auto rows = [&](int s) {
    const int dl = spin_until(tw);
    if (w < 2) tc0 = cyc32();
    __builtin_amdgcn_sched_barrier(0);
    STAMP(1);
    unsigned spins = 0;
    auto pass = [&](bool first) {
      u32x4 x[2 * NC];
#pragma unroll
      for (int f = 0; f < NC; ++f) {
        if (f && w < 2 && first) { prep(s); asm volatile(".rept %0\n\ts_nop 3\n\t.endr" :: "n"(P2_G1)); }
        else if (f) asm volatile(".rept %0\n\ts_nop 3\n\t.endr" :: "n"(w < 2 ? P2_G0 : (f == 1 ? P2_G2 : P2_G3)));
        __builtin_amdgcn_sched_barrier(0);
        x[2 * f] = SC1_LOADS(l2off[2 * f], o2p); x[2 * f + 1] = SC1_LOADS(l2off[2 * f + 1], o2p);
        __builtin_amdgcn_sched_barrier(0);
      }
      if (first) lds_inc<L_MISC + 92>(lds0);
      if (first && w < 2 && PR_P != 3) __builtin_amdgcn_s_setprio(PR_P);
      if (first) { if (w >= 2) prep(s); else prep2(s); }
      if (w >= 2 && first && P2_W) lds_spin<L_MISC + 92, 0>(lds0, 4u * (unsigned)s);
      if (w == 2 && first) tk.store(rs, o4, s > LAG3 ? 0u : 0x7ff00000u);
      if (w == 3 && first) t2.store(rs, t2o, o4);
      if (first && w < 2) {
        p1cur = p1v; offs = (offs - (unsigned)dl) & ~3u;
        PIN_V(p1cur); PIN_S(offs);
        tstar = tc0 + (unsigned)(e1 + D1_M + (w == 1 ? D1_W1 : 0)) - offs * 20u;
        dmin = offs < 40u ? D1_MIN : REC_D1;
        dmin = s < EARLY_N ? REC_D1 + EARLY_D1 : dmin;
        dmax = REC_D1;
        PIN_S(tstar); PIN_S(dmin); PIN_S(dmax);
      }
      __builtin_amdgcn_sched_barrier(0);
      unsigned ok = 32u;
#pragma unroll
      for (int t = 0; t < 3; ++t) { acc[t] = Z4; PIN_V(acc[t]); }
      if (first && w < 2 && PR_P != 3) __builtin_amdgcn_s_setprio(3);
      __builtin_amdgcn_sched_barrier(0);
      bool r = true;
#pragma unroll
      for (int f = 0; f < NC; ++f) {
        unsigned p[4];
#pragma unroll
        for (int k = 0; k < 2; ++k) {
          const u32x4 v = x[2 * f + k];
          p[2 * k] = pack2((float)(int)v[0], (float)(int)v[1]); p[2 * k + 1] = pack2((float)(int)v[2], (float)(int)v[3]);
          asm volatile("" : "+v"(p[2 * k]), "+v"(p[2 * k + 1]));
          __builtin_amdgcn_sched_barrier(0);
        }
        ok = (ok & x[2 * f][0] & x[2 * f][2]) & (x[2 * f + 1][0] & x[2 * f + 1][2]);
        PIN_V(ok);
        __builtin_amdgcn_sched_barrier(0);
        bm[f] = mk8(p[0], p[1], p[2], p[3]);
        acc[0] = smfma64(bm[f], Wqk[0][f], acc[0], sidx);
        __builtin_amdgcn_sched_barrier(0);
        bm_put(f);
        acc[1] = smfma64(bm[f], Wqk[1][f], acc[1], sidx);
        __builtin_amdgcn_sched_barrier(0);
        if (f == NC - 1) { u64 bad = __builtin_amdgcn_ballot_w64(ok == 0u); PIN_S(bad); r = bad == 0ull; }
        __builtin_amdgcn_sched_barrier(0);
        acc[2] = smfma64(bm[f], Wqk[2][f], acc[2], sidx);
        __builtin_amdgcn_sched_barrier(0);
      }
      red_put();
      return r;
    };
    if (__builtin_expect(!pass(true), 0)) {
      FC(0);
      do {
        if (dead || ++spins > SPIN_CAP) { dead = true; break; }
        asm volatile("" ::: "memory");
      } while (!pass(false));
    }
    STAMPV(12, spins); LOGF(1, spins);
    TSTAMP(2, now32()); TSTAMP(3, spins);
  };
  if (w >= 2) prep0(-1);
  prep(0);
  if (w < 2) prep2(0);
  VM_DRAIN();
  CODE_PAD((PADR >> (4 * w)) & 15);
#pragma unroll 1
  for (int s = 0;; ++s) {
    if (__builtin_expect(o2p != 0u, 1)) rows(s);
    if (w < 2 && __builtin_expect(s == NSEG - 1, 0)) break;
    STAMP(2);
    BAR();
    STAMP(3);
    if (w < 2) pub1(p1cur, w == 0 ? o1m : o1);
    if (w == 2) tk.load(rs, o3p);
    if (w == 3) pv2.load(rs, o3p);
    const unsigned tp1 = cyc32();
    __builtin_amdgcn_sched_barrier(0);
    if (w == 2) {
      __builtin_amdgcn_s_setprio(W2_P);
      prep0(s);
      fr = qk_frag(lds + qo4, lds + qo2, lds + qo2b);
      t2_mem(fr, qx[0], qx[1], rs, o3, h, j, invv * KEXP, lane);
      __builtin_amdgcn_sched_barrier(0);
      asm volatile("s_waitcnt vmcnt(1)" ::: "memory");
      lds_inc<L_MISC + 28>(lds0);
      kq_get(s + 1);
    }
    if (w == 3) {
      __builtin_amdgcn_s_setprio(T4_P);
      prep0(s);
      t2.get(lds, s + 1, lane); t2.run(0); t2.run(1);
      __builtin_amdgcn_sched_barrier(0);
    }
    if (w >= 2 && __builtin_expect(s == NSEG - 2, 0)) { VM_DRAIN(); lds_inc<L_MISC + 28>(lds0); }
    if (w >= 2) __builtin_amdgcn_s_setprio(UN_P);
    TSTAMP(0, now32());
    STAMP(4);
    __builtin_amdgcn_sched_barrier(0);
    unsigned tpub = 0;
    u32x2 sw;
    if (w == 2) {
      STAMP(5);
      __builtin_amdgcn_sched_barrier(0);
      STAMP(6);
      ARRIVE(0);
      tk.run(j, lane, SelLo);
      if (__builtin_expect(s == NSEG - 1, 0)) { tlast = tp1; break; }
      if (F2B) { lds_spin<L_MISC + 48, 1>(lds0, 2u * (unsigned)(s + 1)); x_fetch_n<F2B>(xsrc, lds0, s + 3, P2B, lane * 16); }
      __builtin_amdgcn_s_setprio(SP_P);
    }
    if (w == 3) {
      pv2.run<false>(lds, sv < 1 + LAG4 ? sv + NV - 1 - LAG4 : sv - 1 - LAG4, lane, rs, 0u);
      if (__builtin_expect(s == NSEG - 1, 0)) { tlast = tp1; break; }
      lds_inc<L_MISC + 112>(lds0);
      STAMP(11);
      __builtin_amdgcn_s_setprio(SP_P);
      STAMP(5);
      ARRIVE(0);
      if (F3) { lds_spin<L_MISC + 48, 1>(lds0, 2u * (unsigned)(s + 1)); x_fetch_n<F3>(xsrc, lds0, s + 3, 40 - F3, lane * 16); }
      STAMP(6);
    }
    if (w < 2) {
      __builtin_amdgcn_s_setprio(D1_P);
      x_fetch_n<F0A>(xsrc, lds0, s + 3, PA + w * F0A, lane * 16);
      int dq = imin((int)(tstar - tp1), dmax);
      dq = dq < -D1_LATE ? dmax : dq;
      PIN_S(dq);
      spin_exact(tp1 + (unsigned)imax(dq, dmin));
      STAMP(5);
      u32x32 S;
      auto sc_load = [&]() {
        asm volatile("buffer_load_dwordx4 v[224:227], %2, %4, %5 offen sc1\n\t.rept %7\n\ts_nop 3\n\t.endr\n\t"
                     "buffer_load_dwordx4 v[228:231], %3, %4, %6 offen sc1\n\tbuffer_load_dwordx4 v[232:235], %3, %4, %6 offen offset:2048 sc1\n\t"
                     "buffer_load_dwordx4 v[236:239], %3, %4, %6 offen offset:256 sc1\n\tbuffer_load_dwordx4 v[240:243], %3, %4, %6 offen offset:2304 sc1\n\t"
                     "v_mov_b32 v220, 0x42000000\n\tv_mov_b32 v221, 0\n\tv_mov_b32 v222, 0x42000000\n\tv_mov_b32 v223, 0"
                     : "=&{v[224:255]}"(S), "+{v[208:223]}"(P) : "v"(l1offm), "v"(l1off), "s"(rs), "s"(o1m), "s"(o1), "n"(P1_G) : "memory");
      };
      sc_load();
      __builtin_amdgcn_sched_barrier(0);
      lds_inc<L_MISC + 48>(lds0);
      STAMP(6);
      ARRIVE(0);
      lds_spin<L_MISC + 28, 0>(lds0, need2);
      __builtin_amdgcn_s_setprio(3);
      asm volatile("ds_read_b64 v[192:193], %3\n\tds_read_b64 v[196:197], %3 offset:768\n\tds_read_b64 v[200:201], %4\n\tds_read_b64 v[204:205], %4 offset:768\n\tds_read_b64 v[184:185], %2"
                   : "+{v[184:191]}"(V8), "+{v[192:207]}"(V) : "v"(vma[0]), "v"(vma[1]), "v"(vma[2]) : "memory");
      __builtin_amdgcn_sched_barrier(0);
      {
        const int de = imin((int)(tp1 - tc0) - e1, D1_C);
        e1 += s == 0 ? 0 : de >> (s < EARLY_N && de < -D1_C ? 0 : D1_G);
        PIN_S(e1);
      }
      unsigned sn = (unsigned)(s + 1);
      PIN_V(sn);
      STAMP(11);
      __builtin_amdgcn_sched_barrier(0);
      {
        unsigned spins = 0;
        u64 miss, bad;
#define MM(d, a, b, c) "v_mfma_f32_16x16x32_bf16 " d ", " a ", " b ", " c "\n\t"
#define FM(d, x, r, ctrl) "v_fmac_f32_dpp " d ", " x ", " r " " ctrl "\n\t"
#define ID_ "quad_perm:[0,1,2,3] row_mask:0xf bank_mask:"
#define HM_ "row_half_mirror row_mask:0xf bank_mask:"
#define R8_ "row_ror:8 row_mask:0xf bank_mask:"
#define MI_ "row_mirror row_mask:0xf bank_mask:"
        auto soft = [&]() {
          asm volatile(
            "s_waitcnt vmcnt(4)\n\t" MM("v[244:247]", "%6", "v[224:227]", "0") "v_min3_u32 v214, v224, v225, v226\n\t"
            "s_waitcnt vmcnt(3)\n\t" MM("v[248:251]", "%6", "v[228:231]", "0") "v_min3_u32 v214, v214, v227, v228\n\tv_min3_u32 v214, v214, v229, v230\n\t"
            "s_waitcnt vmcnt(2)\n\t" MM("v[248:251]", "%6", "v[232:235]", "v[248:251]") "v_min3_u32 v214, v214, v231, v232\n\t"
            "v_add_f32_dpp v212, v244, v244 " R8_ "0x3\n\tv_add_f32_dpp v213, v245, v245 " R8_ "0x3\n\t"
            "s_waitcnt vmcnt(1)\n\t" MM("v[248:251]", "%7", "v[236:239]", "v[248:251]")
            "v_add_f32_dpp v212, v246, v246 " R8_ "0xc\n\tv_add_f32_dpp v213, v247, v247 " R8_ "0xc\n\t"
            "s_waitcnt vmcnt(0) lgkmcnt(0)\n\t" MM("v[248:251]", "%7", "v[240:243]", "v[248:251]")
            "v_fma_f32 v212, v212, %14, %15\n\tv_fma_f32 v213, v213, %14, %15\n\tv_exp_f32 v216, v212\n\tv_exp_f32 v217, v213\n\t"
            "v_min3_u32 v214, v214, v233, v234\n\tv_cvt_pk_bf16_f32 v208, v216, v217\n\tv_min3_u32 v214, v214, v235, v236\n\tds_read_b96 v[188:190], %16 offset:%18\n\t"
            "v_exp_f32 v216, v248\n\tv_mov_b32_dpp v209, v208 " R8_ "0xf\n\tv_exp_f32 v217, v249\n\tv_exp_f32 v218, v250\n\t"
            MM("v[252:255]", "%8", "v[208:211]", "0") "v_exp_f32 v219, v251\n\tv_cvt_pk_bf16_f32 v232, v216, v217\n\t"
            MM("v[224:227]", "v[192:195]", "v[208:211]", "0") "v_cvt_pk_bf16_f32 v233, v218, v219\n\tv_min3_u32 v214, v214, v237, v238\n\tv_mov_b32_dpp v234, v232 " R8_ "0xf\n\t"
            MM("v[228:231]", "v[196:199]", "v[208:211]", "0") "v_mov_b32_dpp v235, v233 " R8_ "0xf\n\t"
            MM("v[244:247]", "v[204:207]", "v[208:211]", "0") "v_min3_u32 v214, v214, v239, v240\n\t"
            MM("v[252:255]", "%8", "v[232:235]", "v[252:255]") MM("v[224:227]", "%10", "v[232:235]", "v[224:227]") "v_min3_u32 v214, v214, v241, v242\n\t"
            MM("v[228:231]", "%11", "v[232:235]", "v[228:231]") "s_waitcnt lgkmcnt(0)\n\tv_sub_u32 v215, v190, %17 clamp\n\t"
            MM("v[236:239]", "v[200:203]", "v[208:211]", "0") "v_min3_u32 v214, v214, v243, v215\n\tv_cmp_eq_u32 %4, 0, v214\n\tv_rcp_f32 v216, v252\n\t"
            MM("v[240:243]", "v[184:187]", "v[208:211]", "0") "v_mul_f32 v216, v216, v188\n\tv_subrev_u32 v215, 0x2f800000, v252\n\t"
            MM("v[236:239]", "%12", "v[232:235]", "v[236:239]") FM("v220", "v224", "v216", ID_ "0x1") FM("v221", "v225", "v216", ID_ "0x1")
            "v_mov_b32_dpp v217, v216 " HM_ "0xf\n\t"
            MM("v[244:247]", "%13", "v[232:235]", "v[244:247]") FM("v220", "v230", "v216", ID_ "0x2") FM("v221", "v231", "v216", ID_ "0x2")
            MM("v[240:243]", "%9", "v[232:235]", "v[240:243]") FM("v220", "v228", "v217", HM_ "0x1") FM("v221", "v229", "v217", HM_ "0x1")
            "v_cmp_ge_u32 vcc, v215, v189\n\t" FM("v220", "v226", "v217", HM_ "0x2") FM("v221", "v227", "v217", HM_ "0x2")
            "s_or_b64 %5, %4, vcc"
            : "+{v[224:255]}"(S), "+{v[208:223]}"(P), "+{v[184:191]}"(V8), "+{v[192:207]}"(V), "=&s"(miss), "=&s"(bad)
            : "v"(slo), "v"(shi), "v"(ONES), "v"(vox[0]), "v"(vox[1]), "v"(vox[2]), "v"(vox[3]), "v"(vox[4]), "v"(k1), "v"(minf), "v"(lds0), "s"(s), "n"(L_MISC + 32)
            : "vcc", "scc", "memory");
        };
        soft();
        if (__builtin_expect(bad != 0ull, 0)) {
          if (miss) FC(1); else FC(2);
          while (miss != 0ull && !dead) {
            if (++spins > SPIN_CAP) dead = true;
            asm volatile("s_nop 7\n\ts_nop 7" ::: "memory");
            sc_load(); soft();
          }
          if (bad != 0ull) {
            u32x4 y[5];
#pragma unroll
            for (int k = 0; k < 4; ++k) y[k] = SC1_LOADS(l1off + (k & 1) * 2048 + (k >> 1) * 256, o1);
            y[4] = SC1_LOADS(l1offm, o1m);
            const f32x4 d0 = mfma32(as8(slo), as8(y[4]), Z4);
            f32x4 d1 = mfma32(as8(shi), as8(y[3]), mfma32(as8(shi), as8(y[2]), mfma32(as8(slo), as8(y[1]), mfma32(as8(slo), as8(y[0]), Z4))));
            const bool lo = c < 8;
            const float m0 = __builtin_fmaf((lo ? d0[0] : d0[2]) + dppf<DPP_ROR8>(lo ? d0[2] : d0[0]), k1, minf);
            const float m1 = __builtin_fmaf((lo ? d0[1] : d0[3]) + dppf<DPP_ROR8>(lo ? d0[3] : d0[1]), k1, minf);
            float mx = fmx(fmx(fmx(d1[0], d1[1]), d1[2]), fmx(fmx(d1[3], m0), m1));
            mx = fmx(mx, dppf<DPP_ROR8>(mx));
            mx = x32mx(x16mx(mx));
            d1 -= mx;
            const unsigned mm = pack2(__builtin_amdgcn_exp2f(m0 - mx), __builtin_amdgcn_exp2f(m1 - mx));
            const unsigned t01 = pack2(__builtin_amdgcn_exp2f(d1[0]), __builtin_amdgcn_exp2f(d1[1])), t23 = pack2(__builtin_amdgcn_exp2f(d1[2]), __builtin_amdgcn_exp2f(d1[3]));
            const bf16x8 pm = mk8(mm, dpp_ror8(mm), 0u, 0u), pt = mk8(t01, t23, dpp_ror8(t01), dpp_ror8(t23));
            const float r = __builtin_amdgcn_rcpf(mfma32(ONES, pt, mfma32(ONES, pm, Z4))[0]) * u2f(V8[4]), rm = dppf<0x141>(r);
            f32x4 o[5];
#pragma unroll
            for (int k = 0; k < 5; ++k) o[k] = mfma32(vox[k], pt, mfma32(k ? mk8(V[4 * k - 4], V[4 * k - 3], 0u, 0u) : mk8(V8[0], V8[1], 0u, 0u), pm, Z4));
#pragma unroll
            for (int i = 0; i < 2; ++i) {
              const float oa = dppf<0x141>(o[2][i]), ob = dppf<0x141>(o[1][2 + i]);
              P[12 + i] = f2u(__builtin_fmaf(hd ? ob : oa, rm, __builtin_fmaf(hd ? o[2][2 + i] : o[1][i], r, i ? 0.f : 32.0f)));
            }
            P[8] = f2u(r); P[9] = f2u(rm);
#pragma unroll
            for (int i = 0; i < 4; ++i) { S[12 + i] = f2u(o[3][i]); S[16 + i] = f2u(o[0][i]); S[20 + i] = f2u(o[4][i]); }
            asm volatile("s_nop 7\n\ts_nop 7" : "+{v[224:255]}"(S), "+{v[208:223]}"(P));
          }
        }
        STAMP(7); STAMPV(13, spins); LOGF(0, spins);
        {
          asm volatile(".if %11 == 0\n\tds_write_b32 %8, %12 offset:%10\n\t.endif\n\t"
                       "v_cvt_i32_f32 v218, v220\n\tv_cvt_i32_f32 v219, v221\n\tv_and_or_b32 v218, v218, %7, 2\n\tbuffer_atomic_add_x2 v[218:219], %3, %5, %6 offen\n\t"
                       ".if %11 == 1\n\tds_write_b32 %8, %12 offset:%10\n\t.endif\n\t"
                       FM("v222", "v236", "v216", R8_ "0x4") FM("v223", "v237", "v216", R8_ "0x4") "ds_read_b64 %2, %8 offset:%9\n\t"
                       FM("v222", "v238", "v217", MI_ "0x8") FM("v223", "v239", "v217", MI_ "0x8") "v_permlane32_swap_b32 v240, v242\n\t"
                       FM("v222", "v244", "v217", MI_ "0x4") FM("v223", "v245", "v217", MI_ "0x4") "v_permlane32_swap_b32 v241, v243\n\t"
                       FM("v222", "v246", "v216", R8_ "0x8") FM("v223", "v247", "v216", R8_ "0x8") ".if %11 == 2\n\tds_write_b32 %8, %12 offset:%10\n\t.endif\n\t"
                       FM("v222", "v240", "v216", ID_ "0x1") FM("v223", "v241", "v216", ID_ "0x1") FM("v222", "v242", "v217", HM_ "0x1") FM("v223", "v243", "v217", HM_ "0x1")
                       "v_cvt_i32_f32 v212, v222\n\tv_cvt_i32_f32 v213, v223\n\tv_and_or_b32 v212, v212, %7, 2\n\tbuffer_atomic_add_x2 v[212:213], %4, %5, %6 offen\n\t"
                       ".if %11 == 3\n\tds_write_b32 %8, %12 offset:%10\n\ts_waitcnt lgkmcnt(1)\n\t.else\n\ts_waitcnt lgkmcnt(0)\n\t.endif"
                       : "+{v[224:255]}"(S), "+{v[208:223]}"(P), "=&v"(sw) : "v"(acco), "v"(acco2), "s"(rs), "s"(o2), "s"(~63u), "v"(lds0), "n"(L_MISC + 96),
                         "n"(L_MISC + 104 + 4 * (w & 1)), "n"(REL_AT), "v"(sn) : "memory");
          __builtin_amdgcn_sched_barrier(0);
          tpub = now32();
          asm volatile("buffer_atomic_umax %0, %1, %2, %3 offen" :: "v"(tpub | 1u), "v"(tso), "s"(rs), "s"(o2) : "memory");
          STAMP(10);
        }
      }
    }
    STAMP(8);
    ARRIVE(1);
    o2p = o2;
    PIN_S(o2p);
    if (w < 2) {
      if (__builtin_expect(__builtin_amdgcn_readfirstlane(sw[1]) <= (unsigned)s, 0)) {
        lds_spin<L_MISC + 100, 0>(lds0, (unsigned)s + 1u);
        sw[0] = __hip_atomic_load((unsigned*)(misc + 24), __ATOMIC_RELAXED, __HIP_MEMORY_SCOPE_WORKGROUP);
      }
    } else {
      u32x4 q;
      asm volatile("1:\n\tds_read_b128 %0, %1 offset:%3\n\t.if %4\n\ts_sleep 1\n\t.endif\n\ts_waitcnt lgkmcnt(0)\n\tv_min3_u32 v201, v201, v202, v203\n\tv_cmp_ge_u32 vcc, %2, v201\n\ts_cbranch_vccnz 1b"
                   : "=&{v[200:203]}"(q) : "v"(lds0), "s"(s), "n"(L_MISC + 96), "n"(SP_SL) : "vcc", "memory");
      __builtin_amdgcn_s_setprio(3);
      tpub = now32();
      sw[0] = q[0];
    }
    STAMP(9);
    LOGT(0, tpub); LOGT(1, tb2_);
    TSTAMP(1, tpub);
    ARRIVE_OUT();
    const unsigned an = __builtin_amdgcn_readfirstlane(sw[0]);
    const int ahead = (int)(an - tpub);
    tw = tpub + (unsigned)(an == 0u || ahead > 1000 ? SCH_FIRST : imax(ahead, w < 2 ? SCH_DMIN : 0));
    offs = tw - an;
  }
  STAMP_OUT();
  LOGP(4);
  __builtin_amdgcn_s_setprio(3);
  if (w < 2) { BAR(); tlast = cyc32(); }
  if (w == 2) tk.store(rs, o4);
  if (w == 3) {
    const unsigned oo = op_slot(NSEG - 2 - LAG4) + (unsigned)((m * 32 + gp) * 1280);
    const char* sp = lds + L_STG + 2048;
    const u32x4 v0 = *(const u32x4*)(sp + lane * 16), v1 = *(const u32x4*)(sp + 1280 + lane * 16), v2 = *(const u32x4*)(sp + (g & 1) * 1280 + 1024 + c * 16);
    SC1_STORE(v0, oo + lane * 16); SC1_STORE(v1, oo + 20480 + lane * 16); SC1_STORE(v2, lane < 32 ? oo + g * 20480 + 1024 + c * 16 : 0x7ffffff0u);
  }
  if (dead) tlast |= 1u; else tlast &= ~1u;
}

__device__ __forceinline__ void drain(const RecArgs& a, char* lds, const __amdgpu_buffer_rsrc_t rs, const int w, const unsigned tp1) {
  static_assert(LAG3 == 2 && LAG4 == 3 && LAG5 == 6, "drain schedule");
  const int lane = __builtin_amdgcn_workitem_id_x() & 63, b = __builtin_amdgcn_workgroup_id_x(), c = lane & 15;
  const int gp = (b & 7) * 2 + (b >> 7), m = (b >> 3) & 15, j = m & 7;
  unsigned* const misc = (unsigned*)(lds + L_MISC);
  const unsigned lds0 = (unsigned)(uintptr_t)(LAS char*)lds, ck = __builtin_amdgcn_readfirstlane(misc[11]);
  bool dead = tp1 & 1u;
  auto poll = [&](auto load, auto ready) {
    for (unsigned spins = 0;;) {
      asm volatile("" ::: "memory");
      load();
      if (ready() || dead) break;
      if (++spins > SPIN_CAP) dead = true;
      if (END_SL) __builtin_amdgcn_s_sleep(END_SL);
    }
  };
  LOGD(0);
#define TASK(ty, sg, tm) ((u64)((ty) | ((sg) - 56) << 2 | ((tm) / 100) << 5))
  u64 todo = w == 0 ? TASK(1, 63, END_E1) | TASK(2, 63, END_E2) << 12 | TASK(3, 62, END_E3) << 24
           : w == 1 ? TASK(1, 62, 0) | TASK(2, 62, END_E62) << 12 | TASK(3, 60, END_E3) << 24
           : w == 2 ? TASK(2, 60, 0) | TASK(3, 56, 0) << 12 : TASK(2, 61, END_E61) | TASK(3, 58, END_E59) << 12;
  TokTask tk;
  PvTask2 pv;
  OutTask ot;
  {
    const u32x4* p = (const u32x4*)(lds + L_DRN) + lane;
    const u32x4 q0 = p[0], q1 = p[64], q2 = p[128];
    tk.l0 = q0[0]; tk.l1 = q0[0] + 16384u; tk.lm = q0[1]; tk.s0 = q0[2]; tk.s1 = q0[3];
    pv.lo = q1[0]; pv.lm = q1[1]; pv.vr = q1[2]; pv.ma = q1[3]; pv.mz = q2[0]; pv.so = q2[1];
    pv.pa[0] = pv.pa[1] = pv.pb[0] = pv.pb[1] = (u32x4){0u, 0u, 0u, 0u};
    ot.t5f = q2[2]; ot.t5o = q2[3]; ot.sel = as8(p[192]);
  }
#pragma unroll 1
  for (int ti = 0; todo; todo >>= 12, ++ti) {
    const int ty = (int)todo & 3, sl = 56 + (((int)todo >> 2) & 7);
    const unsigned tm = tp1 + (((unsigned)todo >> 5) & 0x7fu) * 100u;
    if (ty == 1) {
      wait_cyc(tm);
      LOGD(3 * ti + 1);
      poll([&]() { tk.load(rs, ts_slot(ck, sl)); }, [&]() { return tk.ready(); });
      LOGD(3 * ti + 2);
      tk.run(j, lane, ot.sel); tk.store(rs, pt_slot(ck, sl));
    } else if (ty == 2) {
      const unsigned t0 = cyc32();
      if (sl >= NSEG - 2) { lds_wait(misc + 7, 4u * NSEG + 3u); wait_cyc(t0 + END_G2); }
      wait_cyc(tm);
      LOGD(3 * ti + 1);
      poll([&]() { pv.load(rs, pt_slot(ck, sl)); }, [&]() { return pv.ready(); });
      LOGD(3 * ti + 2);
      pv.run<true>(lds, sl % NV, lane, rs, op_slot(sl) + (unsigned)((m * 32 + gp) * 1280));
      if (sl == NSEG - 1) misc[14] = cyc32() | 1u;
    } else {
      if (sl >= NSEG - 4) { lds_wait(misc + 14, 1u); wait_cyc(__builtin_amdgcn_readfirstlane(misc[14]) + END_G3); }
      wait_cyc(tm);
      LOGD(3 * ti + 1);
      OutTask o2 = ot;
      poll([&]() { o2.load(a, rs, sl + 1, b); ot.load(a, rs, sl, b); }, [&]() { return ot.ready(b) & o2.ready(b); });
      LOGD(3 * ti + 2);
      o2.run(lane); o2.store(a, sl + 1, b, lane);
      ot.run(lane); ot.store(a, sl, b, lane);
    }
    LOGD(3 * ti + 3);
  }
  VM_DRAIN();
  if (w != 0) lds_inc<L_MISC + 64>(lds0);
  else {
    constexpr unsigned o2 = (unsigned)OFF_ACC + (unsigned)((NSEG - 1) % SR) * (unsigned)ACC_STEP;
    lds_wait(misc + 16, 3u);
    LOGD(10);
    if (lane == 0) __hip_atomic_fetch_add((gu64*)((GAS char*)(a.ws + o2) + acc_off(m * 4, 0)), (u64)1, RLX_AGENT);
    wait_until(now32() + SCH_DRAIN);
    wait_cyc(tp1 + END_E4);
    unsigned x;
    if (qkv_row(b) + 48 >= 55 && qkv_col(b) < 15) poll([&]() { x = __builtin_amdgcn_raw_buffer_load_b32(rs, acc_off(c * 4, 0), o2, 16); }, [&]() { return wave_all((x & 16u) != 0u); });
    lds_inc<L_MISC + 68>(lds0);
    LOGD(11);
  }
  if (qkv_row(b) + 48 >= 55 && qkv_col(b) < 15) {
    lds_wait(misc + 18, 8u);
    lds_spin<L_MISC + 68, 1>(lds0, 1u);
    x_fetch_n<10>(x_src(a, NSEG + 3, b), lds0, 3, 10 * w, lane * 16);
    VM_DRAIN();
    lds_inc<L_MISC + 68>(lds0);
  }
  LOGP(8);
}

__device__ __forceinline__ void proj_front(const RecArgs& a, char* lds, const __amdgpu_buffer_rsrc_t rs, const int v) {
  const int lane = __builtin_amdgcn_workitem_id_x() & 63, b = __builtin_amdgcn_workgroup_id_x(), c = lane & 15, g = lane >> 4;
  const int rb = (b & 7) + 8 * (b >> 5), cq = (b >> 3) & 3;
  const unsigned lds0 = (unsigned)(uintptr_t)(LAS char*)lds;
  unsigned* const cnt = (unsigned*)(lds + L_MISC) + 20;
  LOGF2(0);
  float* const red = (float*)(lds + L_RED1);
  float* const fin = red + 640;
  const f32x4 Z4 = {0.f, 0.f, 0.f, 0.f};
  x_fetch_n<8>((const u16*)(a.ws + OFF_OB) + (size_t)rb * 32 * DM, lds0, 0, 8 * v, lane * 16);
  bf16x8 W[2][20];
  const __amdgpu_buffer_rsrc_t rw = __builtin_amdgcn_make_buffer_rsrc(a.ws + OFF_WOBT + (size_t)(cq * 10 + 2 * v) * 20480, 0, 40960, 0x00020000);
  auto w_get = [&](int ks) {
#pragma unroll
    for (int i = 0; i < 2; ++i) W[i][ks] = as8(__builtin_amdgcn_raw_buffer_load_b128(rw, lane * 16 + (ks & 3) * 1024, (i * 20 + (ks & ~3)) * 1024, 0));
    __builtin_amdgcn_sched_barrier(0);
  };
#pragma unroll
  for (int ks = 0; ks < FW; ++ks) w_get(ks);
  const unsigned fo = (unsigned)(rb * 40960 + (cq * 5 + v) * 1024 + ((g >> 1) * 16 + c) * 16), f8 = fo + (g & 1) * 8, f16 = fo + (g & 1) * 20480;
  u32x2 xo[2][2];
#pragma unroll
  for (int k = 0; k < 4; ++k)
    xo[k >> 1][k & 1] = __builtin_bit_cast(u32x2, __hip_atomic_load((const gu64*)((const char*)a.xnew + f8 + (k >> 1) * 512 + (k & 1) * 20480), RLX_AGENT));
  asm volatile("s_waitcnt vmcnt(%0)" :: "n"(2 * FW + 4) : "memory");
  lds_inc<L_MISC + 80>(lds0);
  lds_wait(cnt, 5u);
  LOGF2(1);
  f32x4 acc[2][2] = {{Z4, Z4}, {Z4, Z4}};
  bf16x8 x[20][2];
#pragma unroll
  for (int ks = -3; ks < 20; ++ks) {
    if (ks >= 0 && ks + FW < 20) w_get(ks + FW);
#pragma unroll
    for (int t = 0; t < 2; ++t) {
      if (ks + 3 < 20) x[ks + 3][t] = *(const bf16x8*)(lds + L_XN + (t * 20 + ks + 3) * 1024 + lane * 16);
#pragma unroll
      for (int i = 0; i < 2 && ks >= 0; ++i) acc[i][t] = mfma32(W[i][ks < 0 ? 0 : ks], x[ks < 0 ? 0 : ks][t], acc[i][t]);
    }
    __builtin_amdgcn_sched_barrier(0);
  }
  u32x2 xp[2][2];
  PIN_V(acc[1][1]);
  LOGF2(2);
#pragma unroll
  for (int t = 0; t < 2; ++t) {
#pragma unroll
    for (int k = 0; k < 4; ++k) {
      const int i = k >> 1, q = k & 1;
      const unsigned r = pack2(acc[i][t][2 * q], acc[i][t][2 * q + 1]), x = xo[i][t][q];
      xp[i][t][q] = pack2(u2f(x << 16) + u2f(r << 16), u2f(x & 0xffff0000u) + u2f(r & 0xffff0000u));
      acc[i][t][2 * q] = u2f(xp[i][t][q] << 16); acc[i][t][2 * q + 1] = u2f(xp[i][t][q] & 0xffff0000u);
    }
    const f32x4 sm = acc[0][t] + acc[1][t];
    const float ml = x32sum(x16sum((sm[0] + sm[1]) + (sm[2] + sm[3]))) * (1.0f / 32.0f);
    float q = 0.f;
#pragma unroll
    for (int r = 0; r < 8; ++r) { const float d = acc[r >> 2][t][r & 3] - ml; q += d * d; }
    q = x32sum(x16sum(q));
    *(f32x2*)(red + (v * 32 + t * 16 + c) * 2) = (f32x2){ml, q};
  }
  lds_inc<L_MISC + 80>(lds0);
  if (v == 0) {
    lds_wait(cnt, 10u);
    LOGF2(3);
    float mw[5], ml = 0.f, m2 = 0.f;
#pragma unroll
    for (int i = 0; i < 5; ++i) { const f32x2 u = *(const f32x2*)(red + (i * 32 + (lane & 31)) * 2); mw[i] = u[0]; ml += u[0]; m2 += u[1]; }
    ml *= 0.2f;
#pragma unroll
    for (int i = 0; i < 5; ++i) m2 += 32.0f * (mw[i] - ml) * (mw[i] - ml);
    const unsigned xs = (unsigned)OFF_LNX + rb * 1024;
    if (lane < 32) SC1_STORE8S(((u32x2){f2u(ml), nz(f2u(m2))}), (cq * 32 + lane) * 8, xs);
    wait_cyc(cyc32() + FR_D);
    u32x2 x0, x1;
    for (unsigned spins = 0; spins < SPIN_CAP; ++spins) {
      x0 = __builtin_amdgcn_raw_buffer_load_b64(rs, lane * 8, xs, 16); x1 = __builtin_amdgcn_raw_buffer_load_b64(rs, 512 + lane * 8, xs, 16);
      if (wave_all(x0[1] != 0u && x1[1] != 0u)) break;
      asm volatile("" ::: "memory");
    }
    const float m0 = u2f(x0[0]), m1 = u2f(x1[0]);
    const float mean = x32sum(m0 + m1) * 0.25f;
    const float M2 = x32sum((u2f(x0[1]) + u2f(x1[1])) + 160.0f * ((m0 - mean) * (m0 - mean) + (m1 - mean) * (m1 - mean)));
    const float mb = rbf(mean), den = rbf(__builtin_sqrtf(rbf(rbf(M2 * (1.0f / DM)) + 1e-5f)));
    if (lane < 32) {
      fin[lane] = mb; fin[32 + lane] = 1.0f / den;
      if (cq == 0) __hip_atomic_store((GAS unsigned*)(a.ws + OFF_XNRM) + (rb * 32 + lane) * 4, f2u(1.02f * (M2 + DM * (mean - mb) * (mean - mb)) / (den * den)), RLX_AGENT);
    }
    lds_inc<L_MISC + 84>(lds0);
  }
  lds_wait(cnt + 1, 1u);
  LOGF2(4);
  const __amdgpu_buffer_rsrc_t rx = __builtin_amdgcn_make_buffer_rsrc(a.ws + OFF_XN, 0, (int)(OFF_QB - OFF_XN), 0x00020000);
  auto put = [&](const u32x2 p0, const u32x2 p1, unsigned so) {
    const auto r0 = __builtin_amdgcn_permlane16_swap(p0[0], p1[0], false, false), r1 = __builtin_amdgcn_permlane16_swap(p0[1], p1[1], false, false);
    __builtin_amdgcn_raw_buffer_store_b128((u32x4){r0[0], r1[0], r0[1], r1[1]}, rx, f16, so, 16);
  };
#pragma unroll
  for (int i = 0; i < 2; ++i) {
    u32x2 y[2];
#pragma unroll
    for (int t = 0; t < 2; ++t) {
      const float mb = fin[t * 16 + c], rden = fin[32 + t * 16 + c];
      y[t] = (u32x2){pack2(rbf(acc[i][t][0] - mb) * rden, rbf(acc[i][t][1] - mb) * rden), pack2(rbf(acc[i][t][2] - mb) * rden, rbf(acc[i][t][3] - mb) * rden)};
    }
    put(y[0], y[1], i * 512);
  }
  VM_DRAIN();
  lds_inc<L_MISC + 80>(lds0);
#pragma unroll
  for (int i = 0; i < 2; ++i) put(xp[i][0], xp[i][1], (unsigned)(OFF_XB - OFF_XN) + i * 512);
  if (v == 0) {
    lds_wait(cnt, 15u);
    if (rb < 4 && lane < 8) __hip_atomic_fetch_add((GAS unsigned*)(a.ws + OFF_RDY) + lane * 64, 1u, RLX_AGENT);
  }
  LOGF2(5);
}

__global__ __launch_bounds__(512, 2) void rec_kernel(RecArgs a) {
  __shared__ __attribute__((aligned(16))) char lds[L_BYTES];
  const int tid = __builtin_amdgcn_workitem_id_x(), wid = __builtin_amdgcn_readfirstlane(tid >> 6), w = wid & 3;
  const __amdgpu_buffer_rsrc_t rs = __builtin_amdgcn_make_buffer_rsrc(a.ws, 0, (int)COMM_BYTES, 0x00020000);
  if (tid < 32) ((float*)(lds + L_MISC))[tid] = (tid == 4 || tid == 5) ? 1.0f : 0.0f;
  BAR();
  if (a.layer && wid < 5) proj_front(a, lds, rs, wid);
  if (wid < 4) {
    unsigned tlast;
    if (w == 0) rec_waves<0>(a, lds, rs, tlast);
    else if (w == 1) rec_waves<1>(a, lds, rs, tlast);
    else if (w == 2) rec_waves<2>(a, lds, rs, tlast);
    else rec_waves<3>(a, lds, rs, tlast);
    drain(a, lds, rs, w, tlast);
  } else {
    unsigned vo[2][2];
    if (w == 0) token_waves<0>(a, lds, rs, vo);
    else if (w == 1) token_waves<1>(a, lds, rs, vo);
    else if (w == 2) token_waves<2>(a, lds, rs, vo);
    else token_waves<3>(a, lds, rs, vo);
    qkv_tail(a, lds, w, vo);
  }
}

#if defined(__HIP_DEVICE_COMPILE__)
#pragma clang attribute pop
#endif

#define ATT_P1 6
#define ATT_P2 10
#define ATT_P3 14
#define ATT_THR 6.0f
#define ATT_NT 4
constexpr int NT = ATT_NT, ATT_NQ = 8 / NT, KQ = S_ / NT;
#define ATT_LMAX 1.0e12f
#if defined(__HIP_DEVICE_COMPILE__)
#pragma clang attribute push(__attribute__((target("no-packed-fp32-ops"))), apply_to = function)
#endif
__global__ __launch_bounds__(512, 2) void attb_kernel(char* __restrict__ ws, u16* __restrict__ out) {
  const u16* const Qp = (const u16*)(ws + OFF_QB);
  const u16* const KV = (const u16*)(ws + OFF_KV);
  u16* const O = (u16*)(ws + OFF_OB);
  __shared__ __attribute__((aligned(16))) char wl[40960], ob[40960];
  __shared__ f32x4 mo_[8][NT - 1][3][64];
  const int tid = __builtin_amdgcn_workitem_id_x(), bid = __builtin_amdgcn_workgroup_id_x();
  __shared__ unsigned sink[64];
  own_code<ATT_CODE>(tid, sink);
  const int lane = tid & 63, wid = __builtin_amdgcn_readfirstlane(tid >> 6), kh = wid / ATT_NQ, r = lane & 31, hh = lane >> 5;
  const int hd = (bid & 7) * 4 + ((bid >> 3) & 3), qb = (bid >> 5) * ATT_NQ + wid % ATT_NQ;
  const int q0 = qb * 32 * NT;
  bf16x8 qa[NT]; u32x2 qb2[NT];
#pragma unroll
  for (int qt = 0; qt < NT; ++qt) {
    const u16* qr = Qp + ((size_t)hd * S_ + q0 + (qt + kh) % NT * 32 + r) * QST;
    { const u32x2* qp = (const u32x2*)(qr + 8 * hh); const u32x2 q0_ = qp[0], q1_ = qp[1]; qa[qt] = mk8(q0_[0], q0_[1], q1_[0], q1_[1]); }
    const u32x2 t = *(const u32x2*)(qr + 16);
    qb2[qt] = hh ? (u32x2){0u, 0u} : t;
  }
  const __amdgpu_buffer_rsrc_t rk = __builtin_amdgcn_make_buffer_rsrc((void*)(KV + ((size_t)hd * 64 + kh * (KQ / 32)) * (KVT / 2)), 0, KQ / 32 * KVT, 0x00020000);
  unsigned ok1 = lane * 16, ok2 = hh ? 1600 : 1024 + r * 8, ov = r > 20 ? 1u << 30 : 1280 + (hh * 21 + r) * 16;
  const __amdgpu_buffer_rsrc_t ro = __builtin_amdgcn_make_buffer_rsrc(O, 0, S_ * DM * 2, 0x00020000);
  unsigned oo[3];
#pragma unroll
  for (int i = 0; i < 3; ++i) {
    const int row = q0 + kh * 32 + r, k = hd * BD + 4 * hh + 8 * i;
    oo[i] = i == 2 && hh ? 1u << 30 : (unsigned)(((((row >> 4) * 20 + (k >> 5)) * 64 + ((k & 31) >> 3) * 16 + (row & 15)) * 8 + (k & 7)) * 2);
  }
  f32x16 o[NT];
  float m[NT];
  const f32x16 Z16 = {0.f, 0.f, 0.f, 0.f, 0.f, 0.f, 0.f, 0.f, 0.f, 0.f, 0.f, 0.f, 0.f, 0.f, 0.f, 0.f};
  int odd = 0xDDDD;
  asm volatile("" : "+v"(odd));
  struct KvT { u32x4 k1, v0, v1; u32x2 k2; };
  auto fetch_k = [&](KvT& x, int t) {
    x.k1 = __builtin_amdgcn_raw_buffer_load_b128(rk, ok1, imin(t, KQ / 32 - 1) * KVT, 0); x.k2 = __builtin_amdgcn_raw_buffer_load_b64(rk, ok2, imin(t, KQ / 32 - 1) * KVT, 0);
    __builtin_amdgcn_sched_barrier(0);
  };
  auto fetch_v = [&](KvT& x, int t) {
    x.v0 = __builtin_amdgcn_raw_buffer_load_b128(rk, ov, imin(t, KQ / 32 - 1) * KVT, 0); x.v1 = __builtin_amdgcn_raw_buffer_load_b128(rk, ov + 672, imin(t, KQ / 32 - 1) * KVT, 0);
    __builtin_amdgcn_sched_barrier(0);
  };
  auto fetch = [&](int t) { KvT x; __builtin_amdgcn_sched_barrier(0); fetch_k(x, t); fetch_v(x, t); return x; };
  auto qk1 = [&](const KvT& x, int qt) { return __builtin_amdgcn_mfma_f32_32x32x16_bf16(as8(x.k1), qa[qt], Z16, 0, 0, 0); };
  auto qk2 = [&](const KvT& x, int qt, f32x16 c) { return __builtin_amdgcn_mfma_f32_32x32x8bf16_1k(__builtin_bit_cast(v4i16_t, x.k2), __builtin_bit_cast(v4i16_t, qb2[qt]), c, 0, 0, 0); };
  auto pv = [&](const u32x4 v, const f32x16 p, int hi, f32x16 c) {
    const f32x8 ph = hi ? __builtin_shufflevector(p, p, 8, 9, 10, 11, 12, 13, 14, 15) : __builtin_shufflevector(p, p, 0, 1, 2, 3, 4, 5, 6, 7);
    return __builtin_amdgcn_smfmac_f32_32x32x32_bf16(__builtin_bit_cast(hbf16x8, v), __builtin_bit_cast(hbf16x16, ph), c, odd, 0, 0);
  };
  auto slow = [&]() {
    asm volatile("" : "+v"(ok1), "+v"(ok2), "+v"(ov) :: "memory");
#pragma unroll
    for (int qt = 0; qt < NT; ++qt) o[qt] = Z16;
#pragma unroll 1
    for (int t = 0; t < KQ / 32; ++t) {
      const KvT x = fetch(t);
#pragma unroll
      for (int qt = 0; qt < NT; ++qt) {
        f32x16 sa = qk2(x, qt, qk1(x, qt));
        float mx = fmx(fmx(sa[0], sa[1]), sa[2]);
#pragma unroll
        for (int i = 3; i < 15; i += 2) mx = fmx(fmx(mx, sa[i]), sa[i + 1]);
        mx = fmx(mx, sa[15]);
        if (t == 0 || !wave_all(mx <= ATT_THR)) {
          mx = x32mx(mx);
          if (t) mx = fmx(mx, 0.f);
          const float mn = m[qt] + mx;
          const unsigned h1 = f2bf(mn); const float r1 = mn - bf2f(h1);
          const unsigned h2 = f2bf(r1); const float r2 = r1 - bf2f(h2);
          const unsigned h3 = f2bf(r2);
          const float me = (bf2f(h1) + bf2f(h2)) + bf2f(h3);
          const float dm = me - m[qt];
          m[qt] = me;
          if (hh) qb2[qt] = (u32x2){(h1 | (h2 << 16)) ^ 0x80008000u, h3 ^ 0x8000u};
          const float alpha = t ? __builtin_amdgcn_exp2f(-dm) : 0.f;
#pragma unroll
          for (int i = 0; i < 16; ++i) { o[qt][i] *= alpha; sa[i] -= dm; }
        }
#pragma unroll
        for (int i = 0; i < 16; ++i) sa[i] = __builtin_amdgcn_exp2f(sa[i]);
        asm volatile("" : "+v"(sa));
        o[qt] = pv(x.v1, sa, 1, pv(x.v0, sa, 0, o[qt]));
        __builtin_amdgcn_sched_barrier(0);
      }
    }
  };
  f32x16 sc[2];
  auto phase = [&](int qt, const u32x4 vp, const KvT& xk, const u32x4 v0, auto mid) {
    const int u = qt & 1;
    auto ex = [&](int i0, int i1) {
#pragma unroll
      for (int i = i0; i < i1; ++i) sc[u][i] = __builtin_amdgcn_exp2f(sc[u][i]);
      asm volatile("" : "+v"(sc[u]));
      __builtin_amdgcn_sched_barrier(0);
    };
    o[(qt + NT - 1) % NT] = pv(vp, sc[1 - u], 1, o[(qt + NT - 1) % NT]);
    __builtin_amdgcn_sched_barrier(0);
    ex(0, 3);
    sc[1 - u] = qk1(xk, (qt + 1) % NT);
    __builtin_amdgcn_sched_barrier(0);
    ex(3, 6);
    sc[1 - u] = qk2(xk, (qt + 1) % NT, sc[1 - u]);
    __builtin_amdgcn_sched_barrier(0);
    ex(6, 10);
    o[qt] = pv(v0, sc[u], 0, o[qt]);
    __builtin_amdgcn_sched_barrier(0);
    mid();
    ex(10, 16);
  };
  static_assert(NT == 4, "projection");
  const int rb = (bid >> 5) * 8 + (bid & 7), cb = (bid >> 3) & 3, g = lane >> 4, c = lane & 15;
  const __amdgpu_buffer_rsrc_t rw = __builtin_amdgcn_make_buffer_rsrc(ws + OFF_WOBT + (size_t)cb * 204800, 0, out ? 204800 : 0, 0x00020000);
  const __amdgpu_buffer_rsrc_t rx = __builtin_amdgcn_make_buffer_rsrc(ws + OFF_XA + (size_t)rb * 40960, 0, out ? 40960 : 0, 0x00020000);
  bf16x8 W[20];
  u32x2 xo[3];
  auto w_get = [&](int t) {
    if (t >= 2 && t < 12) {
#pragma unroll
      for (int i = 0; i < 2; ++i) W[t < 2 || t >= 12 ? 0 : 2 * t - 4 + i] = as8(__builtin_amdgcn_raw_buffer_load_b128(rw, ok1 + i * 1024, (wid * 20 + 2 * t - 4) * 1024, 0));
    }
    if (t >= 4 && t < 9) {
      unsigned keep;
      asm volatile("s_mov_b32 %0, m0\n\ts_mov_b32 m0, %4\n\ts_nop 0\n\tbuffer_load_dwordx4 %1, %2, %3 offen lds\n\ts_mov_b32 m0, %0"
                   : "=&s"(keep) : "v"(ok1), "s"(rw), "s"((160 + wid * 5 + t - 4) * 1024), "s"((unsigned)(uintptr_t)(LAS char*)wl + (wid * 5 + t - 4) * 1024) : "memory");
    }
    if (t == 12) {
#pragma unroll
      for (int i = 0; i < 3; ++i) {
        const int col = cb * 160 + (i < 2 ? wid : 8 + ((wid >> 1) & 1)) * 16 + 4 * g, tt = (i ^ wid) & 1;
        xo[i] = __builtin_amdgcn_raw_buffer_load_b64(rx, (((tt * 20 + (col >> 5)) * 64 + ((col & 31) >> 3) * 16 + c) * 8 + (col & 7)) * 2, 0, 0);
      }
    }
    __builtin_amdgcn_sched_barrier(0);
  };
  if (ATT_P1) __builtin_amdgcn_s_setprio(3);
  {
    KvT xa = fetch(0), xb;
    fetch_k(xb, 1);
    xb.v1 = (u32x4){0u, 0u, 0u, 0u};
    sc[1] = Z16;
    asm volatile("" : "+v"(sc[1]), "+v"(xb.v1), "+v"(oo[0]), "+v"(oo[1]), "+v"(oo[2]));
#pragma unroll
    for (int qt = 0; qt < NT; ++qt) { m[qt] = 0.f; o[qt] = Z16; asm volatile("" : "+v"(o[qt])); }
    sc[0] = qk2(xa, 0, qk1(xa, 0));
    auto step = [&](KvT& x, KvT& y, int t) {
#pragma unroll
      for (int qt = 0; qt < NT; ++qt)
        phase(qt, qt ? x.v1 : y.v1, qt < NT - 1 ? x : y, x.v0, [&]() {
          if (qt == 0 && t + 1 < KQ / 32) fetch_v(y, t + 1);
          if (qt == NT - 2 && t + 2 < KQ / 32) fetch_k(x, t + 2);
          if (qt == NT - 1) w_get(t);
        });
    };
#pragma unroll
    for (int t = 0; t < KQ / 32; t += 2) {
      if (t == ATT_P1 * 4 / NT) __builtin_amdgcn_s_setprio(2);
      if (t == ATT_P2 * 4 / NT) __builtin_amdgcn_s_setprio(1);
      if (t == ATT_P3 * 4 / NT) __builtin_amdgcn_s_setprio(0);
      step(xa, xb, t);
      step(xb, xa, t + 1);
    }
    o[NT - 1] = pv(xb.v1, sc[1], 1, o[NT - 1]);
    float big = 0.f;
#pragma unroll
    for (int qt = 0; qt < NT; ++qt) {
      big = fmx(big, hh ? (o[qt][8] >= 1.0f / ATT_LMAX ? o[qt][8] * (3.0e38f / ATT_LMAX) : INFINITY) : __builtin_fabsf(o[qt][8]));
#pragma unroll
      for (int i = 0; i < 12; ++i) if (i != 8) big = fmx(big, __builtin_fabsf(o[qt][i]));
    }
    if (__builtin_expect(!wave_all(big <= 3.0e38f), 0)) slow();
  }
#pragma unroll
  for (int qt = 1; qt < NT; ++qt) {
    f32x4 (*mo)[64] = mo_[(qt + kh) % NT * ATT_NQ + wid % ATT_NQ][qt - 1];
#pragma unroll
    for (int k = 0; k < 3; ++k) mo[k][lane] = (f32x4){o[qt][4 * k], k == 2 && hh ? m[qt] : o[qt][4 * k + 1], o[qt][4 * k + 2], o[qt][4 * k + 3]};
  }
  asm volatile("s_waitcnt lgkmcnt(0)\n\ts_barrier" ::: "memory");
  {
    f32x2 lm[NT - 1];
    f32x4 u[NT - 1][3];
#pragma unroll
    for (int i = 0; i < NT - 1; ++i) {
      lm[i] = *(const f32x2*)&mo_[wid][i][2][lane | 32];
#pragma unroll
      for (int k = 0; k < 3; ++k) u[i][k] = mo_[wid][i][k][lane];
    }
    float mn = m[0];
#pragma unroll
    for (int i = 0; i < NT - 1; ++i) mn = fmx(mn, lm[i][1]);
    float a0 = __builtin_amdgcn_exp2f(m[0] - mn), a[NT - 1];
    float l = u2f(__builtin_amdgcn_permlane32_swap(f2u(o[0][8]), f2u(o[0][8]), false, false)[1]) * a0;
#pragma unroll
    for (int i = 0; i < NT - 1; ++i) { a[i] = __builtin_amdgcn_exp2f(lm[i][1] - mn); l += lm[i][0] * a[i]; }
    const float inv = __builtin_amdgcn_rcpf(l);
    a0 *= inv;
#pragma unroll
    for (int i = 0; i < NT - 1; ++i) a[i] *= inv;
#pragma unroll
    for (int k = 0; k < 3; ++k) {
      f32x4 v = (f32x4){o[0][4 * k], o[0][4 * k + 1], o[0][4 * k + 2], o[0][4 * k + 3]} * a0;
#pragma unroll
      for (int i = 0; i < NT - 1; ++i) v += u[i][k] * a[i];
      __builtin_amdgcn_raw_buffer_store_b64((u32x2){pack2(v[0], v[1]), pack2(v[2], v[3])}, ro, oo[k], 0, 16);
    }
  }
  if (!out) return;
  asm volatile("s_waitcnt vmcnt(0)\n\ts_barrier" ::: "memory");
  GAS unsigned* const cnt = (GAS unsigned*)(ws + OFF_FIN) + (bid >> 5) * 512;
  if (wid == 0) {
    if (lane < 8) __hip_atomic_fetch_add(cnt + lane * 64, 1u, RLX_AGENT);
  }
  bf16x8 W2[20];
  const __amdgpu_buffer_rsrc_t rf = __builtin_amdgcn_make_buffer_rsrc(out + (size_t)rb * 32 * DM, 0, 32 * DM * 2, 0x00020000);
  unsigned xa0 = (wid & 1) * 20480 + lane * 16, xa1 = 20480 - (wid & 1) * 20480 + lane * 16;
#pragma unroll
  for (int i = 0; i < 3; ++i) oo[i] = (unsigned)(((((i ^ wid) & 1) * 16 + c) * DM + cb * 160 + (i < 2 ? wid : 8 + ((wid >> 1) & 1)) * 16 + 4 * g) * 2);
  asm volatile("" : "+v"(xa0), "+v"(xa1), "+v"(oo[0]), "+v"(oo[1]), "+v"(oo[2]));
#pragma unroll
  for (int ks = 0; ks < 20; ++ks) W2[ks] = *(const bf16x8*)(wl + (((wid >> 1) & 1) * 20 + ks) * 1024 + lane * 16);
  __builtin_amdgcn_sched_barrier(0);
#pragma unroll
  for (int ks = 0; ks < 20; ks += 4) asm volatile("" : "+v"(W2[ks]), "+v"(W2[ks + 1]), "+v"(W2[ks + 2]), "+v"(W2[ks + 3]));
  if (wid == 0) {
    __builtin_amdgcn_s_sleep(FIN_D);
    for (unsigned spins = 0; __hip_atomic_load(cnt + (bid & 7) * 64, RLX_AGENT) < 32u && spins < SPIN_CAP; ++spins) __builtin_amdgcn_s_sleep(FIN_SL);
  }
  asm volatile("s_barrier" ::: "memory");
  x_fetch_n<5>(O + (size_t)rb * 32 * DM, (unsigned)(uintptr_t)(LAS char*)ob - L_XN, 0, wid * 5, ok1);
  asm volatile("s_waitcnt vmcnt(0)\n\ts_barrier" ::: "memory");
  auto fin = [&](auto N) {
    f32x4 acc[3] = {{0.f, 0.f, 0.f, 0.f}, {0.f, 0.f, 0.f, 0.f}, {0.f, 0.f, 0.f, 0.f}};
    bf16x8 x[20][2];
#pragma unroll
    for (int ks = -4; ks < 20; ++ks) {
      if (ks + 4 < 20) { x[ks + 4][0] = *(const bf16x8*)(ob + xa0 + (ks + 4) * 1024); x[ks + 4][1] = *(const bf16x8*)(ob + xa1 + (ks + 4) * 1024); }
      if (ks >= 0) {
        acc[0] = mfma32(W[ks < 0 ? 0 : ks], x[ks < 0 ? 0 : ks][0], acc[0]); acc[1] = mfma32(W[ks < 0 ? 0 : ks], x[ks < 0 ? 0 : ks][1], acc[1]);
        if (N == 3) acc[2] = mfma32(W2[ks < 0 ? 0 : ks], x[ks < 0 ? 0 : ks][0], acc[2]);
      }
      __builtin_amdgcn_sched_barrier(0);
    }
#pragma unroll
    for (int i = 0; i < N; ++i) {
      const unsigned r01 = pack2(acc[i][0], acc[i][1]), r23 = pack2(acc[i][2], acc[i][3]);
      const unsigned p01 = pack2(u2f(xo[i][0] << 16) + u2f(r01 << 16), u2f(xo[i][0] & 0xffff0000u) + u2f(r01 & 0xffff0000u));
      const unsigned p23 = pack2(u2f(xo[i][1] << 16) + u2f(r23 << 16), u2f(xo[i][1] & 0xffff0000u) + u2f(r23 & 0xffff0000u));
      __builtin_amdgcn_raw_buffer_store_b64((u32x2){p01, p23}, rf, oo[i], 0, 0);
    }
  };
  if (wid >= 4) fin(std::integral_constant<int, 3>{}); else fin(std::integral_constant<int, 2>{});
}
#if defined(__HIP_DEVICE_COMPILE__)
#pragma clang attribute pop
#endif

extern "C" void kernel_launch(void* const* d_in, const int* in_sizes, int n_in, void* d_out, int out_size, void* d_ws,
                              size_t ws_size, hipStream_t stream) {
  const u16* A_x = (const u16*)d_in[0];
  const u16* A_mem0 = (const u16*)d_in[1];
  const u16* A_Wq = (const u16*)d_in[2];
  const u16* A_Wk = (const u16*)d_in[3];
  const u16* A_Wv = (const u16*)d_in[4];
  const u16* A_Wo = (const u16*)d_in[5];
  const u16* B_wqkv = (const u16*)d_in[8];
  const u16* B_wo = (const u16*)d_in[9];
  char* ws = (char*)(((uintptr_t)d_ws + ((size_t)WS_OFF << 20) + WS_AL - 1) & ~(uintptr_t)(WS_AL - 1));
  u16* xa = (u16*)(ws + OFF_XA);

  prologue_kernel<<<256, 640, 0, stream>>>(ProArgs{A_x, A_Wq, A_Wk, A_Wv, A_Wo, B_wqkv, B_wo, ws});
  for (int layer = 0; layer < 2; ++layer) {
    rec_kernel<<<256, 512, 0, stream>>>(RecArgs{A_mem0, A_Wq, A_Wk, (const u16*)(ws + OFF_XB), xa, ws, (unsigned)layer, 0u});
    attb_kernel<<<BH * 8, 512, 0, stream>>>(ws, layer ? (u16*)d_out : nullptr);
  }
}
```
